```python
import torch

def _softmax(x, axis=-1):
    return torch.softmax(x, dim=axis)
def _gelu(x):
    return torch.nn.functional.gelu(x, approximate="tanh")
def _silu(x):
    return torch.nn.functional.silu(x)
def _sigmoid(x):
    return torch.sigmoid(x)
def _layernorm(x, axis=-1, eps=1e-5):
    m = x.mean(dim=axis, keepdim=True)
    v = x.var(dim=axis, keepdim=True, unbiased=False)
    return (x - m) / torch.sqrt(v + eps)
def _scatter_add(tgt, idx, src, axis=0):
    return tgt.clone().index_add_(axis, idx, src)
def _segment_sum(src, seg, n):
    out = torch.zeros((n,) + src.shape[1:], dtype=src.dtype)
    return out.index_add_(0, seg, src)
def _pad(x, pw):
    flat = tuple(v for pair in reversed(pw) for v in pair)
    return torch.nn.functional.pad(x, flat)

kernel_name = 'standalone__sequential_behavior_din__recsys'

def setup_inputs():

    _w = (lambda a: (lambda t: t.to(torch.bfloat16) if t.is_floating_point() else t)(torch.as_tensor(a)))
    B, L, D = 16384, 200, 64
    H = D
    return {
        "target": _w((lambda *s: torch.randn(*s, dtype=torch.bfloat16) * 1.6)(B, D)),
        "hist": _w((lambda *s: torch.randn(*s, dtype=torch.bfloat16) * 1.6)(B, L, D)),
        "mask": _w((lambda lo, hi, *s: torch.randint(lo, hi, s))(0, 2, B, L).to(float)),
        "w1": _w((lambda *s: torch.randn(*s, dtype=torch.bfloat16) * 1.6)(4 * D, H) * (4 * D) ** -0.5),
        "b1": _w((lambda *s: torch.randn(*s, dtype=torch.bfloat16) * 1.6)(H) * 0.1),
        "w2": _w((lambda *s: torch.randn(*s, dtype=torch.bfloat16) * 0.05)(H, 1) * H ** -0.5),
        "b2": _w((lambda *s: torch.randn(*s, dtype=torch.bfloat16) * 0.05)(1) * 0.1),
    }

def reference(target, hist, mask, w1, b1, w2, b2):
    L = hist.shape[1]
    tgt = (lambda x, n, axis: torch.repeat_interleave(x, n, dim=axis))(target[:, None, :], L, axis=1)
    feat = torch.concatenate([hist, tgt, hist - tgt, hist * tgt], axis=-1)
    h = _sigmoid(feat @ w1 + b1)
    scores = (h @ w2 + b2)[..., 0] * mask
    return torch.einsum('bl,bld->bd', scores, hist)
```

```cpp
#include <hip/hip_runtime.h>
#include <stdint.h>

constexpr int SMEM_B = 163712;
constexpr uint32_t NO_BATCH = 0x7fffffc0u;

__device__ __forceinline__ float bf2f(unsigned short u) { return __uint_as_float(((uint32_t)u) << 16); }

__global__ __launch_bounds__(512, 2) __attribute__((aligned(4096))) void din_kernel(
    const unsigned short* hist, uint32_t rows, uint32_t f0, const unsigned short* mask, uint32_t mask_bytes, uint32_t f1,
    const unsigned short* target, uint32_t target_bytes, uint32_t f2, unsigned short* out, uint32_t out_bytes, uint32_t f3,
    const unsigned short* w1, const unsigned short* w2, const unsigned short* b1, const unsigned short* b2,
    uint32_t L, uint32_t no_batch, uint32_t B, uint32_t round_rows, uint32_t row_bytes, uint32_t round_bytes, uint32_t period, uint32_t extra) {
  __shared__ __attribute__((aligned(128))) char smem[SMEM_B];
  unsigned long long kp = (unsigned long long)(uintptr_t)__builtin_amdgcn_kernarg_segment_ptr();
  uint32_t tid = threadIdx.x;
  asm volatile(
    "s_load_dwordx16 s[40:55], s[0:1], 0x0\n"
    "s_load_dwordx16 s[4:19], s[0:1], 0x40\n"
    "s_memrealtime s[76:77]\n"
    "v_readfirstlane_b32 s3, v0\n"
    "v_and_b32 v251, 63, v0\n"
    "s_lshr_b32 s3, s3, 6\n"
    "s_lshl3_add_u32 s61, s2, s3\n"
    "s_cmp_lt_u32 s3, 4\n"
    "s_cbranch_scc0 .Ldin_young\n"
    "v_mul_u32_u24 v32, 0x9e3779, v251\n"
    "s_mov_b32 s90, 0x807f807f\n"
    "v_mov_b32 v47, 0x3f003f00\n"
    "v_add_u32 v32, 0x7f4a7c15, v32\n"
    "v_mul_lo_u32 v33, v32, v32\n"
    "v_mul_lo_u32 v34, v33, v32\n"
    "v_mul_lo_u32 v35, v34, v32\n"
    "v_mul_lo_u32 v36, v35, v32\n"
    "v_mul_lo_u32 v37, v36, v32\n"
    "v_mul_lo_u32 v38, v37, v32\n"
    "v_mul_lo_u32 v39, v38, v32\n"
    "v_and_or_b32 v32, v32, s90, v47\n"
    "v_and_or_b32 v33, v33, s90, v47\n"
    "v_and_or_b32 v34, v34, s90, v47\n"
    "v_and_or_b32 v35, v35, s90, v47\n"
    "v_and_or_b32 v36, v36, s90, v47\n"
    "v_and_or_b32 v37, v37, s90, v47\n"
    "v_and_or_b32 v38, v38, s90, v47\n"
    "v_and_or_b32 v39, v39, s90, v47\n"
    "s_nop 1\n"
    "v_lshlrev_b32 v105, 3, v251\n"
    "v_mov_b32 v8, 0\n"
    "v_mov_b32 v9, 0\n"
    "v_mov_b32 v10, 0\n"
    "v_lshlrev_b32 v249, 1, v251\n"
    "s_mul_i32 s68, s3, 11392\n"
    "v_lshlrev_b32_e64 v248, 4, v251\n"
    "s_add_u32 s68, s68, 72576\n"
    "v_and_b32 v24, 0x60, v249\n"
    "s_add_u32 s67, s68, 8192\n"
    "v_mfma_f32_32x32x16_bf16 v[64:79], v[36:39], v[32:35], 0\n"
    "v_or_b32 v250, 0x8000, v24\n"
    "s_add_u32 s65, s68, 9728\n"
    "v_lshrrev_b32 v20, 4, v251\n"
    "s_xor_b32 s64, s67, s65\n"
    "v_and_b32 v244, 56, v251\n"
    "s_add_u32 s96, s68, 11264\n"
    "v_bitop3_b32 v21, v20, v251, 7 bitop3:0x78\n"
    "v_mfma_f32_32x32x16_bf16 v[48:63], v[32:35], v[36:39], 0\n"
    "s_lshl_b32 s66, s3, 7\n"
    "v_lshlrev_b32 v21, 4, v21\n"
    "s_add_u32 s66, s66, 68512\n"
    "v_or_b32 v237, 0x400, v21\n"
    "s_lshr_b32 s95, s67, 2\n"
    "v_xor_b32 v239, 64, v21\n"
    "s_mov_b32 s38, 0xffff\n"
    "v_mfma_f32_32x32x16_bf16 v[64:79], v[36:39], v[32:35], 0\n"
    "v_add_u32 v253, s67, v244\n"
    "v_lshlrev_b32 v105, 3, v251\n"
    "v_mov_b32 v201, v237\n"
    "v_mov_b32 v203, v239\n"
    "v_mov_b32 v205, v237\n"
    "v_mov_b32 v207, v239\n"
    "v_mov_b32 v209, v237\n"
    "v_mfma_f32_32x32x16_bf16 v[48:63], v[32:35], v[36:39], 0\n"
    "v_mov_b32 v211, v239\n"
    "v_mov_b32 v213, v237\n"
    "v_mov_b32 v215, v239\n"
    "v_mov_b32 v220, s95\n"
    "v_mov_b32 v224, 0\n"
    "v_mov_b32 v225, 0\n"
    "v_mov_b32 v226, 0\n"
    "v_mfma_f32_32x32x16_bf16 v[64:79], v[36:39], v[32:35], 0\n"
    "v_mov_b32 v227, 0\n"
    "v_bfe_u32 v21, v251, 1, 3\n"
    "v_xor_b32 v21, v20, v21\n"
    "v_and_b32 v22, 0xf0, v248\n"
    "s_mov_b32 s70, 0x47000000\n"
    "v_lshl_add_u32 v22, v22, 3, s68\n"
    "s_mov_b32 s71, s70\n"
    "v_mfma_f32_32x32x16_bf16 v[48:63], v[32:35], v[36:39], 0\n"
    "v_lshl_add_u32 v234, v21, 4, v22\n"
    "s_add_u32 s63, s66, 16\n"
    "v_xor_b32 v235, 64, v234\n"
    "s_mov_b32 s81, s67\n"
    "v_and_b32 v20, 3, v251\n"
    "s_mov_b64 s[92:93], 0xffff\n"
    "v_lshlrev_b64 v[254:255], v248, s[92:93]\n"
    "v_mfma_f32_32x32x16_bf16 v[64:79], v[36:39], v[32:35], 0\n"
    "s_add_u32 s82, s65, 256\n"
    "v_lshl_or_b32 v21, v20, 3, v24\n"
    "s_mul_i32 s62, s3, 272\n"
    "v_and_b32 v22, 8, v251\n"
    "v_mul_u32_u24 v23, 136, v22\n"
    "v_xor_b32 v21, v21, v23\n"
    "v_and_b32 v23, 4, v251\n"
    "v_mfma_f32_32x32x16_bf16 v[48:63], v[32:35], v[36:39], 0\n"
    "v_lshl_or_b32 v21, v23, 5, v21\n"
    "v_add_u32 v240, s68, v21\n"
    "v_xor_b32 v241, 16, v240\n"
    "v_xor_b32 v242, 32, v240\n"
    "v_xor_b32 v243, 48, v240\n"
    "v_add_u32 v252, s62, v250\n"
    "v_and_b32 v21, 24, v249\n"
    "v_mfma_f32_32x32x16_bf16 v[64:79], v[36:39], v[32:35], 0\n"
    "v_add_u32 v21, s96, v21\n"
    "v_lshl_add_u32 v246, v20, 5, v21\n"
    "v_and_b32 v21, 6, v251\n"
    "v_lshl_or_b32 v21, v21, 2, v24\n"
    "v_and_b32 v23, 2, v249\n"
    "v_lshrrev_b32 v22, 1, v22\n"
    "v_or3_b32 v21, v21, v22, v23\n"
    "v_mfma_f32_32x32x16_bf16 v[48:63], v[32:35], v[36:39], 0\n"
    "v_add_u32 v245, s96, v21\n"
    "s_setprio 2\n"
    "s_waitcnt lgkmcnt(0)\n"
    "s_mul_i32 s36, s61, s16\n"
    "s_add_u32 s94, s36, s16\n"
    "s_cmp_lt_u32 s61, s14\n"
    "s_cselect_b32 s46, s94, 0\n"
    "buffer_load_dwordx2 v[228:229], v105, s[44:47], s36 offen nt\n"
    "s_load_dword s10, s[10:11], 0x0\n"
    "global_load_short_d16_hi v8, v249, s[6:7]\n"
    "global_load_short_d16_hi v9, v249, s[8:9]\n"
    "s_setprio 0\n"
    "s_branch .Ldin_old2\n"
    ".Ldin_young:\n"
    "s_setprio 1\n"
    "v_and_b32 v16, 7, v251\n"
    "s_lshl_b32 s94, s3, 11\n"
    "v_lshlrev_b32 v2, 4, v16\n"
    "v_and_b32 v3, 56, v251\n"
    "v_lshl_or_b32 v3, v3, 5, v2\n"
    "v_add_u32 v6, s94, v3\n"
    "v_add_u32 v3, 0x2000, v6\n"
    "v_add_u32 v4, 0xffffe000, v6\n"
    "v_add_u32 v5, 0x4000, v6\n"
    "v_and_b32 v17, 8, v251\n"
    "s_and_b32 s88, s3, 1\n"
    "v_and_b32 v18, 32, v251\n"
    "s_and_b32 s89, s3, 2\n"
    "v_lshl_or_b32 v18, v16, 2, v18\n"
    "s_lshl_b32 s84, s88, 9\n"
    "v_lshl_add_u32 v18, v18, 3, s84\n"
    "s_lshl_b32 s84, s89, 10\n"
    "v_and_b32 v19, 16, v251\n"
    "s_lshl_b32 s85, s89, 9\n"
    "v_lshl_or_b32 v19, v19, 6, v17\n"
    "s_add_u32 s85, s85, 0x4000\n"
    "v_add3_u32 v12, v18, v19, s84\n"
    "s_lshl2_add_u32 s86, s89, s88\n"
    "v_add_u32 v13, 16, v12\n"
    "s_lshl_b32 s86, s86, 4\n"
    "v_and_b32 v19, 24, v251\n"
    "s_add_u32 s87, s94, 0x4000\n"
    "v_lshrrev_b32 v19, 1, v19\n"
    "v_add3_u32 v14, v18, v19, s85\n"
    "v_add_u32 v15, 16, v14\n"
    "v_bfrev_b32 v7, 1\n"
    "v_lshlrev_b32 v26, 4, v251\n"
    "v_and_b32 v26, 0x1f0, v26\n"
    "v_lshrrev_b32 v27, 5, v251\n"
    "s_add_u32 s39, s61, s3\n"
    "s_sub_u32 s39, s39, 8\n"
    "v_lshlrev_b32 v249, 1, v251\n"
    "s_mul_i32 s68, s3, 11392\n"
    "v_lshlrev_b32_e64 v248, 4, v251\n"
    "s_add_u32 s68, s68, 72576\n"
    "v_and_b32 v24, 0x60, v249\n"
    "s_add_u32 s67, s68, 8192\n"
    "v_or_b32 v250, 0x8000, v24\n"
    "s_add_u32 s65, s68, 9728\n"
    "v_lshrrev_b32 v20, 4, v251\n"
    "s_xor_b32 s64, s67, s65\n"
    "v_and_b32 v244, 56, v251\n"
    "s_add_u32 s96, s68, 11264\n"
    "v_bitop3_b32 v21, v20, v251, 7 bitop3:0x78\n"
    "s_lshl_b32 s66, s3, 7\n"
    "v_lshlrev_b32 v21, 4, v21\n"
    "s_add_u32 s66, s66, 68512\n"
    "v_or_b32 v237, 0x400, v21\n"
    "s_lshr_b32 s95, s67, 2\n"
    "v_xor_b32 v239, 64, v21\n"
    "s_mov_b32 s38, 0xffff\n"
    "v_add_u32 v253, s67, v244\n"
    "v_lshlrev_b32 v105, 3, v251\n"
    "v_mov_b32 v201, v237\n"
    "v_mov_b32 v203, v239\n"
    "v_mov_b32 v205, v237\n"
    "v_mov_b32 v207, v239\n"
    "v_mov_b32 v209, v237\n"
    "v_mov_b32 v211, v239\n"
    "v_mov_b32 v213, v237\n"
    "v_mov_b32 v215, v239\n"
    "v_mov_b32 v220, s95\n"
    "v_mov_b32 v224, 0\n"
    "v_mov_b32 v225, 0\n"
    "s_waitcnt lgkmcnt(0)\n"
    "global_load_dwordx4 v[8:11], v2, s[6:7]\n"
    "global_load_dwordx4 v[128:131], v3, s[4:5] offset:0\n"
    "global_load_dwordx4 v[132:135], v3, s[4:5] offset:128\n"
    "v_mov_b32 v226, 0\n"
    "v_mov_b32 v227, 0\n"
    "v_bfe_u32 v21, v251, 1, 3\n"
    "v_xor_b32 v21, v20, v21\n"
    "v_and_b32 v22, 0xf0, v248\n"
    "s_mov_b32 s70, 0x47000000\n"
    "v_lshl_add_u32 v22, v22, 3, s68\n"
    "s_mov_b32 s71, s70\n"
    "v_lshl_add_u32 v234, v21, 4, v22\n"
    "s_add_u32 s63, s66, 16\n"
    "v_xor_b32 v235, 64, v234\n"
    "s_mov_b32 s81, s67\n"
    "global_load_dwordx4 v[136:139], v4, s[4:5] offset:0\n"
    "v_and_b32_e64 v20, 3, v251\n"
    "s_mov_b64 s[92:93], 0xffff\n"
    "v_lshlrev_b64 v[254:255], v248, s[92:93]\n"
    "s_add_u32 s82, s65, 256\n"
    "v_lshl_or_b32 v21, v20, 3, v24\n"
    "s_mul_i32 s62, s3, 272\n"
    "v_and_b32 v22, 8, v251\n"
    "v_mul_u32_u24 v23, 136, v22\n"
    "v_xor_b32 v21, v21, v23\n"
    "v_and_b32 v23, 4, v251\n"
    "v_lshl_or_b32 v21, v23, 5, v21\n"
    "v_add_u32 v240, s68, v21\n"
    "global_load_dwordx4 v[140:143], v4, s[4:5] offset:128\n"
    "v_xor_b32 v241, 16, v240\n"
    "v_xor_b32 v242, 32, v240\n"
    "v_xor_b32 v243, 48, v240\n"
    "v_add_u32 v252, s62, v250\n"
    "v_and_b32 v21, 24, v249\n"
    "v_add_u32 v21, s96, v21\n"
    "v_lshl_add_u32 v246, v20, 5, v21\n"
    "v_and_b32 v21, 6, v251\n"
    "v_lshl_or_b32 v21, v21, 2, v24\n"
    "v_and_b32 v23, 2, v249\n"
    "v_lshrrev_b32 v22, 1, v22\n"
    "v_or3_b32 v21, v21, v22, v23\n"
    "global_load_dwordx4 v[144:147], v5, s[4:5] offset:0\n"
    "v_add_u32 v245, s96, v21\n"
    "v_cmp_gt_u32 vcc, s16, v26\n"
    "v_mad_u32_u24 v27, v27, s16, v26\n"
    "s_mul_i32 s39, s39, s16\n"
    "s_lshl1_add_u32 s39, s17, s39\n"
    "v_cndmask_b32 v27, v7, v27, vcc\n"
    "s_add_u32 s40, s40, 0xfffffc00\n"
    "s_addc_u32 s41, s41, -1\n"
    "s_mul_i32 s94, s61, s12\n"
    "s_bitset1_b32 s41, 23\n"
    "v_lshl_add_u32 v216, v251, 2, s94\n"
    "s_mov_b64 s[56:57], s[12:13]\n"
    "global_load_dwordx4 v[148:151], v5, s[4:5] offset:128\n"
    "v_add_u32 v217, 1, v216\n"
    "v_add_u32 v218, 2, v216\n"
    "v_add_u32 v219, 3, v216\n"
    "s_mov_b64 s[72:73], s[18:19]\n"
    "s_cmp_lg_u32 s72, 0\n"
    "s_cselect_b32 s94, 6, 0\n"
    "s_cselect_b32 s95, 4, 0\n"
    "s_add_u32 s74, s72, s94\n"
    "s_sub_u32 s75, s72, s95\n"
    "v_mov_b32 v20, s13\n"
    "s_mov_b64 exec, 0xffffff\n"
    "v_mov_b32 v20, -1\n"
    "s_mov_b64 exec, 0xff\n"
    "v_mov_b32 v20, 0\n"
    "s_mov_b32 exec_lo, -1\n"
    "v_lshl_add_u32 v21, v249, 1, s66\n"
    "v_add_u32 v21, -32, v21\n"
    "ds_write_b32 v21, v20\n"
    "s_mov_b32 exec_hi, -1\n"
    "global_load_dwordx4 v[152:155], v6, s[4:5] offset:0\n"
    "s_mov_b32 s37, 0xffff0000\n"
    "v_and_b32 v19, 15, v251\n"
    "v_mul_u32_u24 v19, 272, v19\n"
    "v_add_u32 v97, v19, v250\n"
    "v_add_u32 v98, s87, v248\n"
    "v_add_u32 v99, s86, v250\n"
    "v_add_u32 v100, s86, v97\n"
    "s_mov_b32 s84, 0xbfb8aa3b\n"
    "global_load_dwordx4 v[156:159], v6, s[4:5] offset:128\n"
    "s_mov_b64 exec, 0\n"
    "s_bitcmp1_b32 s3, 1\n"
    "s_cbranch_scc1 .Ldin_t1_hi\n"
    "s_bitcmp1_b32 s3, 0\n"
    "s_cbranch_scc0 .Ldin_stub_4\n"
    "s_branch .Ldin_stub_5\n"
    ".Ldin_t1_hi:\n"
    "s_bitcmp1_b32 s3, 0\n"
    "s_cbranch_scc0 .Ldin_stub_6\n"
    "s_branch .Ldin_stub_7\n"
    ".Ldin_old2:\n"
    "v_and_b32 v2, 48, v251\n"
    "v_and_b32 v3, 1, v251\n"
    "v_lshl_or_b32 v2, v3, 6, v2\n"
    "s_lshl_b32 s39, s61, 7\n"
    "s_add_u32 s40, s40, 0xfffffc00\n"
    "s_addc_u32 s41, s41, -1\n"
    "s_mul_i32 s94, s61, s12\n"
    "s_bitset1_b32 s41, 23\n"
    "v_lshl_add_u32 v216, v251, 2, s94\n"
    "s_mov_b64 s[56:57], s[12:13]\n"
    "v_add_u32 v217, 1, v216\n"
    "v_add_u32 v218, 2, v216\n"
    "v_add_u32 v219, 3, v216\n"
    "s_mov_b64 s[72:73], s[18:19]\n"
    "s_cmp_lg_u32 s72, 0\n"
    "s_cselect_b32 s94, 6, 0\n"
    "s_cselect_b32 s95, 4, 0\n"
    "s_add_u32 s74, s72, s94\n"
    "s_sub_u32 s75, s72, s95\n"
    "v_mov_b32 v20, s13\n"
    "s_mov_b64 exec, 0xffffff\n"
    "v_mov_b32 v20, -1\n"
    "s_mov_b64 exec, 0xff\n"
    "v_mov_b32 v20, 0\n"
    "s_mov_b32 exec_lo, -1\n"
    "v_lshl_add_u32 v21, v249, 1, s66\n"
    "v_add_u32 v21, -32, v21\n"
    "ds_write_b32 v21, v20\n"
    "s_mov_b32 exec_hi, -1\n"
    "v_and_b32 v4, 2, v251\n"
    "v_lshl_add_u32 v4, v4, 6, v250\n"
    "v_and_b32 v5, 16, v248\n"
    "v_and_b32 v6, 12, v251\n"
    "v_or3_b32 v104, v4, v5, v6\n"
    "s_mul_i32 s84, s3, s15\n"
    "v_mul_u32_u24 v4, 34, v244\n"
    "s_lshl3_add_u32 s84, s2, s84\n"
    "v_and_b32 v5, 3, v251\n"
    "s_lshl_b32 s84, s84, 7\n"
    "v_lshl_add_u32 v4, v5, 5, v4\n"
    "v_and_b32 v5, 4, v251\n"
    "v_lshl_add_u32 v4, v5, 2, v4\n"
    "s_lshl_b32 s85, s15, 9\n"
    "s_lshl_b32 s86, s62, 3\n"
    "s_add_u32 s85, s85, s84\n"
    "v_add_u32 v18, s86, v4\n"
    "s_lshl_b32 s94, s61, 6\n"
    "s_add_u32 s94, s3, s94\n"
    "s_cmp_lt_u32 s61, s14\n"
    "s_cselect_b32 s58, s94, s13\n"
    "s_add_u32 s95, s61, s15\n"
    "s_add_u32 s36, s36, s17\n"
    "s_lshl_b32 s94, s95, 6\n"
    "s_add_u32 s94, s3, s94\n"
    "s_add_u32 s96, s36, s16\n"
    "s_add_u32 s94, s94, 8\n"
    "s_cmp_lt_u32 s95, s14\n"
    "s_cselect_b32 s46, s96, 0\n"
    "s_cselect_b32 s59, s94, s13\n"
    "s_setprio 2\n"
    "buffer_load_dwordx4 v[160:163], v2, s[48:51], s39 offen nt\n"
    "buffer_load_dwordx4 v[128:131], v248, s[48:51], s84 offen nt\n"
    "buffer_load_dwordx4 v[132:135], v248, s[48:51], s85 offen nt\n"
    "buffer_load_short_d16_hi v224, v249, s[44:47], s36 offen offset:0 nt\n"
    "buffer_load_short_d16_hi v225, v249, s[44:47], s36 offen offset:128 nt\n"
    "buffer_load_short_d16_hi v226, v249, s[44:47], s36 offen offset:256 nt\n"
    "buffer_load_short_d16_hi v227, v249, s[44:47], s36 offen offset:384 nt\n"
    "s_mul_i32 s46, s14, s16\n"
    "s_mov_b64 exec, 0\n"
    "s_bitcmp1_b32 s3, 1\n"
    "s_cbranch_scc1 .Ldin_t0_hi\n"
    "s_bitcmp1_b32 s3, 0\n"
    "s_cbranch_scc0 .Ldin_stub_0\n"
    "s_branch .Ldin_stub_1\n"
    ".Ldin_t0_hi:\n"
    "s_bitcmp1_b32 s3, 0\n"
    "s_cbranch_scc0 .Ldin_stub_2\n"
    "s_branch .Ldin_stub_3\n"
    ".Ldin_t0_ret:\n"
    "s_mov_b64 exec, -1\n"
    "s_setprio 2\n"
    "s_waitcnt vmcnt(9)\n"
    "v_cmp_ne_u16_e64 s[84:85], 0, v228\n"
    "v_cmp_lt_u32_e64 s[86:87], s38, v228\n"
    "v_cmp_ne_u16_e64 s[88:89], 0, v229\n"
    "v_cmp_lt_u32_e64 s[90:91], s38, v229\n"
    "v_mbcnt_lo_u32_b32 v220, s84, v220\n"
    "s_bcnt1_i32_b64 s92, s[84:85]\n"
    "v_mbcnt_hi_u32_b32 v220, s85, v220\n"
    "s_lshl2_add_u32 s92, s92, s67\n"
    "v_mbcnt_lo_u32_b32 v220, s86, v220\n"
    "s_bcnt1_i32_b64 s93, s[86:87]\n"
    "v_mbcnt_hi_u32_b32 v220, s87, v220\n"
    "s_lshl2_add_u32 s93, s93, s92\n"
    "v_mbcnt_lo_u32_b32 v220, s88, v220\n"
    "s_bcnt1_i32_b64 s94, s[88:89]\n"
    "v_mbcnt_hi_u32_b32 v220, s89, v220\n"
    "s_lshl2_add_u32 s94, s94, s93\n"
    "v_mbcnt_lo_u32_b32 v220, s90, v220\n"
    "s_bcnt1_i32_b64 s95, s[90:91]\n"
    "v_mbcnt_hi_u32_b32 v220, s91, v220\n"
    "s_lshl2_add_u32 s95, s95, s94\n"
    "v_mov_b32 v21, -1\n"
    "v_lshl_add_u32 v20, v249, 1, s95\n"
    "v_addc_co_u32_e64 v221, vcc, v220, 0, s[84:85]\n"
    "v_addc_co_u32_e64 v222, vcc, v221, 0, s[86:87]\n"
    "v_addc_co_u32_e64 v223, vcc, v222, 0, s[88:89]\n"
    "ds_write2_b32 v20, v21, v21 offset1:64\n"
    "v_lshlrev_b32 v220, 2, v220\n"
    "v_lshlrev_b32 v221, 2, v221\n"
    "v_lshlrev_b32 v222, 2, v222\n"
    "v_lshlrev_b32_e64 v223, 2, v223\n"
    "s_mov_b64 exec, s[84:85]\n"
    "ds_write_b32 v220, v216\n"
    "s_mov_b64 exec, s[86:87]\n"
    "ds_write_b32 v221, v217\n"
    "s_mov_b64 exec, s[88:89]\n"
    "ds_write_b32 v222, v218\n"
    "s_mov_b64 exec, s[90:91]\n"
    "ds_write_b32 v223, v219\n"
    "s_mov_b64 exec, -1\n"
    "ds_read_b32 v200, v253 offset:0\n"
    "ds_read_b32 v202, v253 offset:4\n"
    "ds_read_b32 v204, v253 offset:64\n"
    "ds_read_b32 v206, v253 offset:68\n"
    "ds_read_b32 v208, v253 offset:128\n"
    "ds_read_b32 v210, v253 offset:132\n"
    "ds_read_b32 v212, v253 offset:192\n"
    "ds_read_b32 v214, v253 offset:196\n"
    "s_add_u32 m0, s68, 0\n"
    "s_waitcnt lgkmcnt(6)\n"
    "buffer_load_dwordx4 v[200:201], s[40:43], 0 idxen offen offset:0 nt lds\n"
    "buffer_load_dwordx4 v[202:203], s[40:43], 0 idxen offen offset:1024 nt lds\n"
    "s_add_u32 s95, s95, 60\n"
    "s_add_u32 s94, s67, 384\n"
    "s_andn2_b32 s95, s95, 63\n"
    "s_max_u32 s80, s95, s94\n"
    "s_add_u32 m0, s68, 2048\n"
    "s_waitcnt lgkmcnt(0)\n"
    "buffer_load_dwordx4 v[204:205], s[40:43], 0 idxen offen offset:0 nt lds\n"
    "s_lshl_b32 s10, s10, 16\n"
    "v_writelane_b32 v10, s10, 0\n"
    "s_waitcnt vmcnt(10)\n"
    "v_and_b32 v11, 0x7fffffff, v8\n"
    "v_and_b32 v12, 0x7fffffff, v10\n"
    "v_min_f32 v14, 0, v8\n"
    "v_add_u32 v12, 0xf6000000, v12\n"
    "v_add_f32 v14, v14, v10\n"
    "v_max_i32 v12, v11, v12\n"
    "v_rcp_f32 v11, v11\n"
    "v_add_f32_dpp v14, v14, v14 row_ror:8 row_mask:0xf bank_mask:0xf\n"
    "v_max_u32_dpp v12, v12, v12 row_ror:8 row_mask:0xf bank_mask:0xf\n"
    "s_mov_b32 s84, 0xbfb8aa3b\n"
    "v_add_f32_dpp v14, v14, v14 row_ror:4 row_mask:0xf bank_mask:0xf\n"
    "v_max_u32_dpp v12, v12, v12 row_ror:4 row_mask:0xf bank_mask:0xf\n"
    "v_bfrev_b32 v13, 1\n"
    "v_add_f32_dpp v14, v14, v14 row_ror:2 row_mask:0xf bank_mask:0xf\n"
    "v_max_u32_dpp v12, v12, v12 row_ror:2 row_mask:0xf bank_mask:0xf\n"
    "v_bitop3_b32 v8, v8, s84, v13 bitop3:0x6c\n"
    "v_add_f32_dpp v14, v14, v14 row_ror:1 row_mask:0xf bank_mask:0xf\n"
    "v_max_u32_dpp v12, v12, v12 row_ror:1 row_mask:0xf bank_mask:0xf\n"
    "s_nop 0\n"
    ".Ldin_stub_6:\n"
    "s_cbranch_execz .Ldin_t1_ret\n"
    "s_nop 0\n"
    "v_readlane_b32 s84, v12, 0\n"
    "v_readlane_b32 s85, v12, 16\n"
    "v_readlane_b32 s86, v12, 32\n"
    "v_readlane_b32 s87, v12, 48\n"
    "s_max_u32 s84, s84, s85\n"
    "s_max_u32 s86, s86, s87\n"
    "s_max_u32 s84, s84, s86\n"
    "s_lshr_b32 s84, s84, 23\n"
    "buffer_load_dwordx4 v[206:207], s[40:43], 0 idxen offen offset:1024 nt lds\n"
    "s_max_u32 s84, s84, 15\n"
    "s_min_u32 s84, s84, 253\n"
    "s_lshl_b32 s84, s84, 23\n"
    "s_add_u32 s85, s84, 0xf9000000\n"
    "s_sub_u32 s86, 0x86000000, s84\n"
    "v_mul_f32 v11, s85, v11\n"
    "v_min_f32 v11, 0x46800000, v11\n"
    "v_log_f32 v13, v11\n"
    "v_mul_f32 v14, s86, v14\n"
    "v_mov_b32 v15, s85\n"
    "v_add_f32 v13, 0xc1700000, v13\n"
    "v_fmac_f32 v13, v9, v8\n"
    "ds_write_b32 v104, v13 offset:35200\n"
    "ds_write_b32 v104, v11 offset:34816\n"
    "v_mov_b32_e64 v247, v14\n"
    "s_mov_b32 s69, s85\n"
    "ds_write_b64 v250, v[14:15] offset:35072\n"
    "s_mov_b32 s94, 0x10001\n"
    "s_mov_b32 s95, 0xc080c080\n"
    "v_pk_min_u16 v22, v228, s94\n"
    "v_pk_min_u16 v23, v229, s94\n"
    "v_pk_mad_u16 v228, v22, s95, v228\n"
    "v_pk_mad_u16 v229, v23, s95, v229\n"
    "s_sub_u32 s87, s80, s81\n"
    "v_or_b32 v228, v228, v229\n"
    "s_lshr_b32 s87, s87, 6\n"
    "s_lshl2_add_u32 s86, s3, 68224\n"
    "s_waitcnt vmcnt(8)\n"
    "ds_write_b128 v18, v[128:131] offset:32768\n"
    "ds_write_b128 v18, v[132:135] offset:41472\n"
    "v_and_b32_dpp v184, v160, v254 quad_perm:[0,0,0,0] row_mask:0xf bank_mask:0xf\n"
    "v_and_b32_dpp v185, v161, v255 quad_perm:[0,0,0,0] row_mask:0xf bank_mask:0xf\n"
    "v_and_b32_dpp v186, v162, v254 quad_perm:[0,0,0,0] row_mask:0xf bank_mask:0xf\n"
    "v_and_b32_dpp v187, v163, v255 quad_perm:[0,0,0,0] row_mask:0xf bank_mask:0xf\n"
    "v_and_b32_dpp v188, v160, v254 quad_perm:[1,1,1,1] row_mask:0xf bank_mask:0xf\n"
    "v_and_b32_dpp v189, v161, v255 quad_perm:[1,1,1,1] row_mask:0xf bank_mask:0xf\n"
    "v_and_b32_dpp v190, v162, v254 quad_perm:[1,1,1,1] row_mask:0xf bank_mask:0xf\n"
    "v_and_b32_dpp v191, v163, v255 quad_perm:[1,1,1,1] row_mask:0xf bank_mask:0xf\n"
    "v_mov_b32 v106, s86\n"
    "v_mov_b32 v108, s66\n"
    "v_mov_b32 v109, s58\n"
    "v_mov_b32_e64 v110, s59\n"
    "s_waitcnt vmcnt(4)\n"
    "v_cmp_neq_f32_e64 s[20:21], 0, v224\n"
    "v_cmp_neq_f32_e64 s[22:23], 0, v225\n"
    "v_cmp_neq_f32_e64 s[24:25], 0, v226\n"
    "v_cmp_neq_f32_e64 s[26:27], 0, v227\n"
    "v_fma_f32 v224, v224, v224, -v224\n"
    "v_fma_f32 v225, v225, v225, -v225\n"
    "v_fma_f32 v226, v226, v226, -v226\n"
    "v_fma_f32 v227, v227, v227, -v227\n"
    "s_bcnt1_i32_b64 s88, s[20:21]\n"
    "s_bcnt1_i32_b64 s89, s[22:23]\n"
    "s_bcnt1_i32_b64 s90, s[24:25]\n"
    "s_bcnt1_i32_b64 s91, s[26:27]\n"
    "v_or3_b32 v228, v228, v224, v225\n"
    "s_add_u32 s88, s88, s89\n"
    "v_or3_b32 v228, v228, v226, v227\n"
    "s_add_u32 s90, s90, s91\n"
    "v_cmp_ne_u32_e64 s[84:85], 0, v228\n"
    "s_add_u32 s88, s88, s90\n"
    "s_pack_ll_b32_b16 s88, s87, s88\n"
    "s_mov_b64 exec, 1\n"
    "v_mov_b32_e64 v107, s88\n"
    "ds_write2_b32 v108, v109, v110 offset1:2\n"
    "s_cmp_lg_u64 s[84:85], 0\n"
    "ds_write_b32 v106, v107\n"
    "s_cselect_b32 s91, 2.0, 0\n"
    "s_mov_b64 exec, -1\n"
    "s_or_b32 s58, s58, s91\n"
    "s_or_b32 s59, s59, s91\n"
    "s_mov_b64 vcc, 0\n"
    "s_waitcnt lgkmcnt(0)\n"
    "s_barrier\n"
    "s_setprio 0\n"
    ".Ldin_entry:\n"
    "ds_read_b128 v[32:35], v248 offset:16384\n"
    "ds_read_b128 v[64:67], v248 offset:0\n"
    "ds_read_b128 v[68:71], v248 offset:1024\n"
    "ds_read_b128 v[36:39], v248 offset:17408\n"
    "ds_read_b128 v[72:75], v248 offset:2048\n"
    "ds_read_b128 v[76:79], v248 offset:3072\n"
    "ds_read_b128 v[40:43], v248 offset:18432\n"
    "ds_read_b128 v[80:83], v248 offset:4096\n"
    "ds_read_b128 v[84:87], v248 offset:5120\n"
    "ds_read_b128 v[44:47], v248 offset:19456\n"
    "ds_read_b128 v[88:91], v248 offset:6144\n"
    "ds_read_b128 v[92:95], v248 offset:7168\n"
    "s_add_u32 s67, s67, 320\n"
    "s_waitcnt lgkmcnt(9)\n"
    "ds_read_b128 v[48:51], v248 offset:20480\n"
    "ds_read_b128 v[192:195], v248 offset:8192\n"
    "ds_read_b128 v[196:199], v248 offset:9216\n"
    "v_mfma_f32_4x4x4_16b_bf16 v[144:147], v[184:185], v[32:33], v[64:67]\n"
    "s_nop 0\n"
    "v_mfma_f32_4x4x4_16b_bf16 v[148:151], v[186:187], v[34:35], v[68:71]\n"
    "s_add_u32 m0, s68, 4096\n"
    "s_cbranch_vccnz .Ldin_e1\n"
    "buffer_load_dwordx4 v[208:209], s[40:43], 0 idxen offen offset:0 nt lds\n"
    "buffer_load_dwordx4 v[210:211], s[40:43], 0 idxen offen offset:1024 nt lds\n"
    ".Ldin_e1:\n"
    "s_waitcnt lgkmcnt(9)\n"
    "ds_read_b128 v[52:55], v248 offset:21504\n"
    "ds_read_b128 v[200:203], v248 offset:10240\n"
    "ds_read_b128 v[204:207], v248 offset:11264\n"
    "v_mfma_f32_4x4x4_16b_bf16 v[152:155], v[188:189], v[36:37], v[72:75]\n"
    "s_nop 1\n"
    "v_cvt_pk_bf16_f32 v0, v144, v145\n"
    "v_cvt_pk_bf16_f32 v1, v146, v147\n"
    "v_mfma_f32_4x4x4_16b_bf16 v[156:159], v[190:191], v[38:39], v[76:79]\n"
    "s_add_u32 m0, s68, 6144\n"
    "s_cbranch_vccnz .Ldin_e2\n"
    "buffer_load_dwordx4 v[212:213], s[40:43], 0 idxen offen offset:0 nt lds\n"
    "buffer_load_dwordx4 v[214:215], s[40:43], 0 idxen offen offset:1024 nt lds\n"
    ".Ldin_e2:\n"
    "s_waitcnt lgkmcnt(9)\n"
    "s_nop 0\n"
    "ds_read_b128 v[56:59], v248 offset:22528\n"
    "ds_read_b128 v[208:211], v248 offset:12288\n"
    "ds_read_b128 v[212:215], v248 offset:13312\n"
    "v_cvt_pk_bf16_f32 v2, v148, v149\n"
    "v_cvt_pk_bf16_f32 v3, v150, v151\n"
    "v_mfma_f32_4x4x4_16b_bf16 v[144:147], v[184:185], v[40:41], v[80:83]\n"
    "v_cvt_pk_bf16_f32 v4, v152, v153\n"
    "v_cvt_pk_bf16_f32 v5, v154, v155\n"
    "v_mfma_f32_4x4x4_16b_bf16 v[148:151], v[186:187], v[42:43], v[84:87]\n"
    "s_cbranch_vccnz .Ldin_e3\n"
    "s_barrier\n"
    ".Ldin_e3:\n"
    "s_waitcnt lgkmcnt(9)\n"
    "s_nop 0\n"
    "ds_read_b128 v[60:63], v248 offset:23552\n"
    "ds_read_b128 v[216:219], v248 offset:14336\n"
    "ds_read_b128 v[220:223], v248 offset:15360\n"
    "v_cvt_pk_bf16_f32 v6, v156, v157\n"
    "v_cvt_pk_bf16_f32 v7, v158, v159\n"
    "v_mfma_f32_4x4x4_16b_bf16 v[152:155], v[188:189], v[44:45], v[88:91]\n"
    "v_cvt_pk_bf16_f32 v8, v144, v145\n"
    "v_cvt_pk_bf16_f32 v9, v146, v147\n"
    "v_mfma_f32_4x4x4_16b_bf16 v[156:159], v[190:191], v[46:47], v[92:95]\n"
    "s_waitcnt lgkmcnt(9)\n"
    "v_cvt_pk_bf16_f32 v10, v148, v149\n"
    "v_cvt_pk_bf16_f32 v11, v150, v151\n"
    "v_mfma_f32_4x4x4_16b_bf16 v[144:147], v[184:185], v[48:49], v[192:195]\n"
    "v_cvt_pk_bf16_f32 v12, v152, v153\n"
    "v_cvt_pk_bf16_f32 v13, v154, v155\n"
    "v_mfma_f32_4x4x4_16b_bf16 v[148:151], v[186:187], v[50:51], v[196:199]\n"
    "s_waitcnt lgkmcnt(6)\n"
    ".Ldin_stub_7:\n"
    "s_cbranch_execz .Ldin_t1_ret\n"
    "s_nop 0\n"
    "v_cvt_pk_bf16_f32 v14, v156, v157\n"
    "v_cvt_pk_bf16_f32 v15, v158, v159\n"
    "v_mfma_f32_4x4x4_16b_bf16 v[152:155], v[188:189], v[52:53], v[200:203]\n"
    "v_cvt_pk_bf16_f32 v16, v144, v145\n"
    "v_cvt_pk_bf16_f32 v17, v146, v147\n"
    "v_mfma_f32_4x4x4_16b_bf16 v[156:159], v[190:191], v[54:55], v[204:207]\n"
    "s_waitcnt lgkmcnt(3)\n"
    "s_waitcnt vmcnt(6)\n"
    "ds_read_b128 v[160:163], v234 offset:0\n"
    "ds_read_b128 v[164:167], v235 offset:0\n"
    "ds_read_b32 v236, v253 offset:256\n"
    "ds_read_b32 v238, v253 offset:260\n"
    "ds_read_b64_tr_b16 v[168:169], v240 offset:0\n"
    "ds_read_b64_tr_b16 v[170:171], v241 offset:256\n"
    "ds_read_b64_tr_b16 v[172:173], v242 offset:512\n"
    "ds_read_b64_tr_b16 v[174:175], v243 offset:768\n"
    "v_cvt_pk_bf16_f32 v18, v148, v149\n"
    "v_cvt_pk_bf16_f32 v19, v150, v151\n"
    "v_mfma_f32_4x4x4_16b_bf16 v[144:147], v[184:185], v[56:57], v[208:211]\n"
    "v_cvt_pk_bf16_f32 v20, v152, v153\n"
    "v_cvt_pk_bf16_f32 v21, v154, v155\n"
    "v_mfma_f32_4x4x4_16b_bf16 v[148:151], v[186:187], v[58:59], v[212:215]\n"
    "s_waitcnt lgkmcnt(8)\n"
    "v_cvt_pk_bf16_f32 v22, v156, v157\n"
    "v_cvt_pk_bf16_f32 v23, v158, v159\n"
    "v_mfma_f32_4x4x4_16b_bf16 v[152:155], v[188:189], v[60:61], v[216:219]\n"
    "v_cvt_pk_bf16_f32 v24, v144, v145\n"
    "v_cvt_pk_bf16_f32 v25, v146, v147\n"
    "v_mfma_f32_4x4x4_16b_bf16 v[156:159], v[190:191], v[62:63], v[220:223]\n"
    "s_cbranch_vccnz .Ldin_e8\n"
    "s_barrier\n"
    ".Ldin_e8:\n"
    "s_nop 0\n"
    "ds_read_b128 v[112:115], v252 offset:17408\n"
    "ds_read_b128 v[116:119], v252 offset:17424\n"
    "ds_read_b128 v[120:123], v252 offset:17536\n"
    "ds_read_b128 v[124:127], v252 offset:17552\n"
    "v_cvt_pk_bf16_f32 v26, v148, v149\n"
    "v_cvt_pk_bf16_f32 v27, v150, v151\n"
    "v_cvt_pk_bf16_f32 v28, v152, v153\n"
    "v_cvt_pk_bf16_f32 v29, v154, v155\n"
    "s_nop 0\n"
    "v_cvt_pk_bf16_f32 v30, v156, v157\n"
    "v_cvt_pk_bf16_f32 v31, v158, v159\n"
    "s_add_u32 m0, s68, 0\n"
    "s_waitcnt lgkmcnt(0)\n"
    "v_mfma_f32_16x16x32_bf16 v[128:131], v[0:3], v[160:163], v[112:115]\n"
    "ds_read_b128 v[96:99], v250 offset:34816\n"
    "v_mfma_f32_16x16x32_bf16 v[128:131], v[4:7], v[164:167], v[128:131]\n"
    "ds_read_b128 v[100:103], v250 offset:34832\n"
    "v_mfma_f32_16x16x32_bf16 v[132:135], v[8:11], v[160:163], v[116:119]\n"
    "ds_read_b128 v[104:107], v250 offset:34944\n"
    "v_mfma_f32_16x16x32_bf16 v[132:135], v[12:15], v[164:167], v[132:135]\n"
    "ds_read_b128 v[108:111], v250 offset:34960\n"
    "v_mfma_f32_16x16x32_bf16 v[136:139], v[16:19], v[160:163], v[120:123]\n"
    "v_mfma_f32_16x16x32_bf16 v[136:139], v[20:23], v[164:167], v[136:139]\n"
    "v_mfma_f32_16x16x32_bf16 v[140:143], v[24:27], v[160:163], v[124:127]\n"
    "v_mfma_f32_16x16x32_bf16 v[140:143], v[28:31], v[164:167], v[140:143]\n"
    "s_waitcnt vmcnt(4)\n"
    "ds_read_b128 v[160:163], v234 offset:2048\n"
    "ds_read_b128 v[164:167], v235 offset:2048\n"
    "buffer_load_dwordx4 v[236:237], s[40:43], 0 idxen offen offset:0 nt lds\n"
    "buffer_load_dwordx4 v[238:239], s[40:43], 0 idxen offen offset:1024 nt lds\n"
    "s_cmp_lt_i32 s59, s57\n"
    "s_cselect_b32 s76, s76, -1\n"
    "s_bitcmp1_b32 s58, 30\n"
    "s_waitcnt lgkmcnt(0)\n"
    "s_cbranch_scc1 .Ldin_odd\n"
    "s_nop 0\n"
    ".Ldin_A0:\n"
    "v_mfma_f32_16x16x32_bf16 v[144:147], v[0:3], v[160:163], v[112:115]\n"
    "v_exp_f32 v128, v128 clamp\n"
    "v_mfma_f32_16x16x32_bf16 v[144:147], v[4:7], v[164:167], v[144:147]\n"
    ".Ldin_stub_1:\n"
    "s_cbranch_execz .Ldin_t0_ret\n"
    "s_nop 0\n"
    "v_exp_f32 v129, v129 clamp\n"
    "v_mfma_f32_16x16x32_bf16 v[148:151], v[8:11], v[160:163], v[116:119]\n"
    "v_exp_f32 v130, v130 clamp\n"
    "v_mfma_f32_16x16x32_bf16 v[148:151], v[12:15], v[164:167], v[148:151]\n"
    "v_exp_f32 v131, v131 clamp\n"
    "v_mfma_f32_16x16x32_bf16 v[152:155], v[16:19], v[160:163], v[120:123]\n"
    "v_exp_f32 v132, v132 clamp\n"
    "v_mfma_f32_16x16x32_bf16 v[152:155], v[20:23], v[164:167], v[152:155]\n"
    "v_exp_f32 v133, v133 clamp\n"
    "v_mfma_f32_16x16x32_bf16 v[156:159], v[24:27], v[160:163], v[124:127]\n"
    "v_exp_f32 v134, v134 clamp\n"
    "v_mfma_f32_16x16x32_bf16 v[156:159], v[28:31], v[164:167], v[156:159]\n"
    "v_exp_f32 v135, v135 clamp\n"
    "v_exp_f32 v136, v136 clamp\n"
    "s_add_u32 m0, s68, 2048\n"
    "v_exp_f32 v137, v137 clamp\n"
    "v_add_u32_e64 v253, s67, v244\n"
    "s_waitcnt lgkmcnt(0)\n"
    "v_mfma_f32_4x4x4_16b_bf16 v[224:227], v[228:229], v[176:177], v[224:227] cbsz:4 abid:0\n"
    "v_exp_f32 v138, v138 clamp\n"
    "v_exp_f32 v139, v139 clamp\n"
    "v_mfma_f32_4x4x4_16b_bf16 v[224:227], v[228:229], v[178:179], v[224:227] cbsz:4 abid:1\n"
    "v_exp_f32 v140, v140 clamp\n"
    "v_exp_f32 v141, v141 clamp\n"
    "v_mfma_f32_4x4x4_16b_bf16 v[224:227], v[228:229], v[180:181], v[224:227] cbsz:4 abid:2\n"
    "v_exp_f32 v142, v142 clamp\n"
    "v_exp_f32 v143, v143 clamp\n"
    "v_mfma_f32_4x4x4_16b_bf16 v[224:227], v[228:229], v[182:183], v[224:227] cbsz:4 abid:3\n"
    "s_waitcnt vmcnt(4)\n"
    "ds_read_b64_tr_b16 v[176:177], v240 offset:2048\n"
    "ds_read_b64_tr_b16 v[178:179], v241 offset:2304\n"
    "ds_read_b64_tr_b16 v[180:181], v242 offset:2560\n"
    "ds_read_b64_tr_b16 v[182:183], v243 offset:2816\n"
    "ds_read_b32 v236, v253 offset:0\n"
    "ds_read_b32 v238, v253 offset:4\n"
    "ds_read_b128 v[160:163], v234 offset:4096\n"
    "ds_read_b128 v[164:167], v235 offset:4096\n"
    "s_cbranch_vccz .Ldin_A0_np\n"
    "s_setprio 1\n"
    ".Ldin_A0_np:\n"
    "v_pk_fma_f32 v[128:129], v[128:129], s[70:71], v[96:97]\n"
    "v_pk_fma_f32 v[130:131], v[130:131], s[70:71], v[98:99]\n"
    "v_add_f32 v191, v224, v225\n"
    "v_add_f32 v190, v226, v227\n"
    "v_add_f32 v191, v191, v190\n"
    "v_add_u32 v190, s62, v249\n"
    "v_mul_f32 v191, s69, v191\n"
    "v_cvt_pk_bf16_f32 v191, v191, v191\n"
    "ds_write_b16 v190, v191 offset:32768\n"
    "s_bfe_u32 s94, s59, 0x180006\n"
    "s_mul_i32 s86, s94, s56\n"
    "v_mbcnt_lo_u32_b32 v184, s20, 0\n"
    "v_mbcnt_lo_u32_b32 v185, s22, 0\n"
    "v_mbcnt_lo_u32_b32 v186, s24, 0\n"
    "v_mbcnt_lo_u32_b32 v187, s26, 0\n"
    "s_bcnt1_i32_b64 s90, s[20:21]\n"
    "s_bcnt1_i32_b64 s91, s[22:23]\n"
    "s_bcnt1_i32_b64 s92, s[24:25]\n"
    "s_bcnt1_i32_b64 s93, s[26:27]\n"
    "v_mbcnt_hi_u32_b32 v184, s21, v184\n"
    "v_mbcnt_hi_u32_b32 v185, s23, v185\n"
    "v_mbcnt_hi_u32_b32 v186, s25, v186\n"
    "v_mbcnt_hi_u32_b32 v187, s27, v187\n"
    "s_lshl2_add_u32 s90, s90, s65\n"
    "s_lshl2_add_u32 s91, s91, s90\n"
    "s_lshl2_add_u32 s92, s92, s91\n"
    "s_lshl2_add_u32 s93, s93, s92\n"
    "v_add_u32_e64 v188, s86, v251\n"
    "v_pk_add_f32 v[230:231], v[128:129], v[130:131]\n"
    "v_pk_mul_f32 v[232:233], v[128:129], v[130:131]\n"
    "v_pk_fma_f32 v[132:133], v[132:133], s[70:71], v[100:101]\n"
    "v_pk_fma_f32 v[230:231], v[230:231], v[132:133], v[232:233]\n"
    "v_pk_mul_f32 v[232:233], v[232:233], v[132:133]\n"
    "v_lshl_add_u32 v184, v184, 2, s65\n"
    "v_lshl_add_u32 v185, v185, 2, s90\n"
    "v_lshl_add_u32 v186, v186, 2, s91\n"
    "v_lshl_add_u32 v187, v187, 2, s92\n"
    "v_mov_b32 v191, -1\n"
    "v_lshl_add_u32 v190, v249, 1, s93\n"
    "ds_write2_b32 v190, v191, v191 offset1:64\n"
    "s_mov_b64 exec, s[20:21]\n"
    "ds_write_b32 v184, v188\n"
    "s_mov_b64 exec, s[22:23]\n"
    "v_add_u32 v189, 64, v188\n"
    "ds_write_b32 v185, v189\n"
    "s_mov_b64 exec, s[24:25]\n"
    "v_add_u32 v190, 128, v188\n"
    "ds_write_b32 v186, v190\n"
    "s_mov_b64 exec, s[26:27]\n"
    "v_add_u32 v189, 192, v188\n"
    "ds_write_b32 v187, v189\n"
    "s_mov_b64 exec, -1\n"
    "s_add_u32 s95, s93, 60\n"
    "s_add_u32 s94, s82, 128\n"
    "s_andn2_b32 s95, s95, 63\n"
    "s_max_u32 s83, s95, s94\n"
    "v_pk_fma_f32 v[134:135], v[134:135], s[70:71], v[102:103]\n"
    "v_pk_fma_f32 v[230:231], v[230:231], v[134:135], v[232:233]\n"
    "v_pk_mul_f32 v[232:233], v[232:233], v[134:135]\n"
    "v_pk_fma_f32 v[136:137], v[136:137], s[70:71], v[104:105]\n"
    "v_pk_fma_f32 v[230:231], v[230:231], v[136:137], v[232:233]\n"
    "v_pk_mul_f32 v[232:233], v[232:233], v[136:137]\n"
    "v_pk_fma_f32 v[138:139], v[138:139], s[70:71], v[106:107]\n"
    "v_pk_fma_f32 v[230:231], v[230:231], v[138:139], v[232:233]\n"
    "v_pk_mul_f32 v[232:233], v[232:233], v[138:139]\n"
    "v_pk_fma_f32 v[140:141], v[140:141], s[70:71], v[108:109]\n"
    "v_pk_fma_f32 v[230:231], v[230:231], v[140:141], v[232:233]\n"
    "v_pk_mul_f32 v[232:233], v[232:233], v[140:141]\n"
    "v_pk_fma_f32 v[142:143], v[142:143], s[70:71], v[110:111]\n"
    "v_pk_fma_f32 v[230:231], v[230:231], v[142:143], v[232:233]\n"
    "v_pk_mul_f32 v[232:233], v[232:233], v[142:143]\n"
    "v_rcp_f32 v232, v232\n"
    "v_rcp_f32 v233, v233\n"
    "v_fma_f32 v230, v230, v232, v247\n"
    "v_fmac_f32 v230, v231, v233\n"
    "v_cvt_pk_bf16_f32 v230, v230, v230\n"
    "s_add_u32 s67, s67, 64\n"
    "s_waitcnt lgkmcnt(0)\n"
    "ds_write_b16 v245, v230\n"
    "ds_read_b64 v[228:229], v246\n"
    "buffer_load_dwordx4 v[236:237], s[40:43], 0 idxen offen offset:0 nt lds\n"
    "buffer_load_dwordx4 v[238:239], s[40:43], 0 idxen offen offset:1024 nt lds\n"
    ".Ldin_B1:\n"
    "v_mfma_f32_16x16x32_bf16 v[128:131], v[0:3], v[160:163], v[112:115]\n"
    "v_exp_f32 v144, v144 clamp\n"
    "v_mfma_f32_16x16x32_bf16 v[128:131], v[4:7], v[164:167], v[128:131]\n"
    "v_exp_f32 v145, v145 clamp\n"
    "v_mfma_f32_16x16x32_bf16 v[132:135], v[8:11], v[160:163], v[116:119]\n"
    "v_exp_f32 v146, v146 clamp\n"
    "v_mfma_f32_16x16x32_bf16 v[132:135], v[12:15], v[164:167], v[132:135]\n"
    "v_exp_f32 v147, v147 clamp\n"
    "v_mfma_f32_16x16x32_bf16 v[136:139], v[16:19], v[160:163], v[120:123]\n"
    "v_exp_f32 v148, v148 clamp\n"
    "v_mfma_f32_16x16x32_bf16 v[136:139], v[20:23], v[164:167], v[136:139]\n"
    "v_exp_f32 v149, v149 clamp\n"
    "v_mfma_f32_16x16x32_bf16 v[140:143], v[24:27], v[160:163], v[124:127]\n"
    "v_exp_f32 v150, v150 clamp\n"
    "v_mfma_f32_16x16x32_bf16 v[140:143], v[28:31], v[164:167], v[140:143]\n"
    "v_exp_f32 v151, v151 clamp\n"
    "v_exp_f32 v152, v152 clamp\n"
    "s_cmp_eq_u32 s67, s80\n"
    "s_cselect_b32 s67, s65, s67\n"
    "s_and_b32 s94, s59, 63\n"
    "s_mul_i32 s94, s94, 272\n"
    "v_add_u32 v252, s94, v250\n"
    "s_add_u32 m0, s68, 4096\n"
    "v_exp_f32 v153, v153 clamp\n"
    "v_add_u32 v253, s67, v244\n"
    ".Ldin_stub_3:\n"
    "s_cbranch_execz .Ldin_t0_ret\n"
    "s_nop 0\n"
    "s_waitcnt lgkmcnt(0)\n"
    "v_mfma_f32_4x4x4_16b_bf16 v[224:227], v[228:229], v[168:169], 0 cbsz:4 abid:0\n"
    "v_exp_f32 v154, v154 clamp\n"
    "v_exp_f32 v155, v155 clamp\n"
    "v_mfma_f32_4x4x4_16b_bf16 v[224:227], v[228:229], v[170:171], v[224:227] cbsz:4 abid:1\n"
    "v_exp_f32 v156, v156 clamp\n"
    "v_exp_f32 v157, v157 clamp\n"
    "v_mfma_f32_4x4x4_16b_bf16 v[224:227], v[228:229], v[172:173], v[224:227] cbsz:4 abid:2\n"
    "v_exp_f32 v158, v158 clamp\n"
    "v_exp_f32 v159, v159 clamp\n"
    "v_mfma_f32_4x4x4_16b_bf16 v[224:227], v[228:229], v[174:175], v[224:227] cbsz:4 abid:3\n"
    "s_waitcnt vmcnt(4)\n"
    "s_memrealtime s[34:35]\n"
    "ds_read_b64_tr_b16 v[168:169], v240 offset:4096\n"
    "ds_read_b64_tr_b16 v[170:171], v241 offset:4352\n"
    "ds_read_b64_tr_b16 v[172:173], v242 offset:4608\n"
    "ds_read_b64_tr_b16 v[174:175], v243 offset:4864\n"
    "ds_read_b32 v236, v253 offset:0\n"
    "ds_read_b32 v238, v253 offset:4\n"
    "ds_read_b128 v[160:163], v234 offset:6144\n"
    "ds_read_b128 v[164:167], v235 offset:6144\n"
    "ds_read_b128 v[184:187], v252 offset:0\n"
    "ds_read_b128 v[188:191], v252 offset:16\n"
    "v_pk_fma_f32 v[144:145], v[144:145], s[70:71], v[96:97]\n"
    "v_pk_fma_f32 v[146:147], v[146:147], s[70:71], v[98:99]\n"
    "v_pk_add_f32 v[230:231], v[144:145], v[146:147]\n"
    "v_pk_mul_f32 v[232:233], v[144:145], v[146:147]\n"
    "v_pk_fma_f32 v[148:149], v[148:149], s[70:71], v[100:101]\n"
    "v_pk_fma_f32 v[230:231], v[230:231], v[148:149], v[232:233]\n"
    "v_pk_mul_f32 v[232:233], v[232:233], v[148:149]\n"
    "v_pk_fma_f32 v[150:151], v[150:151], s[70:71], v[102:103]\n"
    "v_pk_fma_f32 v[230:231], v[230:231], v[150:151], v[232:233]\n"
    "v_pk_mul_f32 v[232:233], v[232:233], v[150:151]\n"
    "v_pk_fma_f32 v[152:153], v[152:153], s[70:71], v[104:105]\n"
    "v_pk_fma_f32 v[230:231], v[230:231], v[152:153], v[232:233]\n"
    "v_pk_mul_f32 v[232:233], v[232:233], v[152:153]\n"
    "v_pk_fma_f32 v[154:155], v[154:155], s[70:71], v[106:107]\n"
    "v_pk_fma_f32 v[230:231], v[230:231], v[154:155], v[232:233]\n"
    "v_pk_mul_f32 v[232:233], v[232:233], v[154:155]\n"
    "v_pk_fma_f32 v[156:157], v[156:157], s[70:71], v[108:109]\n"
    "v_pk_fma_f32 v[230:231], v[230:231], v[156:157], v[232:233]\n"
    "v_pk_mul_f32 v[232:233], v[232:233], v[156:157]\n"
    "v_pk_fma_f32 v[158:159], v[158:159], s[70:71], v[110:111]\n"
    "v_pk_fma_f32 v[230:231], v[230:231], v[158:159], v[232:233]\n"
    "v_pk_mul_f32 v[232:233], v[232:233], v[158:159]\n"
    "v_rcp_f32 v232, v232\n"
    "v_rcp_f32 v233, v233\n"
    "v_fma_f32 v230, v230, v232, v247\n"
    "v_fmac_f32 v230, v231, v233\n"
    "v_cvt_pk_bf16_f32 v230, v230, v230\n"
    "s_add_u32 s67, s67, 64\n"
    "s_waitcnt lgkmcnt(0)\n"
    "ds_write_b16 v245, v230\n"
    "ds_read_b64 v[228:229], v246\n"
    "s_cmp_le_u32 s34, s76\n"
    "s_cbranch_scc1 .Ldin_B1_wait\n"
    ".Ldin_B1_go:\n"
    "s_sub_u32 s94, s34, 60\n"
    "s_max_u32 s76, s76, s94\n"
    "s_add_u32 s76, s76, s72\n"
    ".Ldin_B1_dma:\n"
    "buffer_load_dwordx4 v[236:237], s[40:43], 0 idxen offen offset:0 nt lds\n"
    "buffer_load_dwordx4 v[238:239], s[40:43], 0 idxen offen offset:1024 nt lds\n"
    ".Ldin_B1_nd:\n"
    ".Ldin_G2:\n"
    "v_mfma_f32_16x16x32_bf16 v[144:147], v[0:3], v[160:163], v[112:115]\n"
    "v_exp_f32 v128, v128 clamp\n"
    "v_mfma_f32_16x16x32_bf16 v[144:147], v[4:7], v[164:167], v[144:147]\n"
    "v_exp_f32 v129, v129 clamp\n"
    "v_mfma_f32_16x16x32_bf16 v[148:151], v[8:11], v[160:163], v[116:119]\n"
    "v_exp_f32 v130, v130 clamp\n"
    "v_mfma_f32_16x16x32_bf16 v[148:151], v[12:15], v[164:167], v[148:151]\n"
    "v_exp_f32 v131, v131 clamp\n"
    "v_mfma_f32_16x16x32_bf16 v[152:155], v[16:19], v[160:163], v[120:123]\n"
    "v_exp_f32 v132, v132 clamp\n"
    "v_mfma_f32_16x16x32_bf16 v[152:155], v[20:23], v[164:167], v[152:155]\n"
    "v_exp_f32 v133, v133 clamp\n"
    "v_mfma_f32_16x16x32_bf16 v[156:159], v[24:27], v[160:163], v[124:127]\n"
    "v_exp_f32 v134, v134 clamp\n"
    "v_mfma_f32_16x16x32_bf16 v[156:159], v[28:31], v[164:167], v[156:159]\n"
    "v_exp_f32 v135, v135 clamp\n"
    "v_exp_f32 v136, v136 clamp\n"
    "s_cmp_eq_u32 s67, s80\n"
    "s_cselect_b32 s67, s65, s67\n"
    "s_add_u32 m0, s68, 6144\n"
    "v_exp_f32 v137, v137 clamp\n"
    "v_add_u32_e64 v253, s67, v244\n"
    "s_waitcnt lgkmcnt(0)\n"
    "v_mfma_f32_4x4x4_16b_bf16 v[224:227], v[228:229], v[176:177], v[224:227] cbsz:4 abid:0\n"
    "v_exp_f32 v138, v138 clamp\n"
    "v_exp_f32 v139, v139 clamp\n"
    "v_mfma_f32_4x4x4_16b_bf16 v[224:227], v[228:229], v[178:179], v[224:227] cbsz:4 abid:1\n"
    "v_exp_f32 v140, v140 clamp\n"
    "v_exp_f32 v141, v141 clamp\n"
    "v_mfma_f32_4x4x4_16b_bf16 v[224:227], v[228:229], v[180:181], v[224:227] cbsz:4 abid:2\n"
    "v_exp_f32 v142, v142 clamp\n"
    "v_exp_f32 v143, v143 clamp\n"
    "v_mfma_f32_4x4x4_16b_bf16 v[224:227], v[228:229], v[182:183], v[224:227] cbsz:4 abid:3\n"
    "s_waitcnt vmcnt(4)\n"
    "s_memrealtime s[34:35]\n"
    "ds_read_b64_tr_b16 v[176:177], v240 offset:6144\n"
    "ds_read_b64_tr_b16 v[178:179], v241 offset:6400\n"
    "ds_read_b64_tr_b16 v[180:181], v242 offset:6656\n"
    "ds_read_b64_tr_b16 v[182:183], v243 offset:6912\n"
    "ds_read_b32 v236, v253 offset:0\n"
    "ds_read_b32 v238, v253 offset:4\n"
    "ds_read_b128 v[160:163], v234 offset:0\n"
    "ds_read_b128 v[164:167], v235 offset:0\n"
    "v_pk_fma_f32 v[128:129], v[128:129], s[70:71], v[96:97]\n"
    "v_pk_fma_f32 v[130:131], v[130:131], s[70:71], v[98:99]\n"
    "v_pk_add_f32 v[230:231], v[128:129], v[130:131]\n"
    "v_pk_mul_f32 v[232:233], v[128:129], v[130:131]\n"
    "v_pk_fma_f32 v[132:133], v[132:133], s[70:71], v[100:101]\n"
    "v_pk_fma_f32 v[230:231], v[230:231], v[132:133], v[232:233]\n"
    "v_pk_mul_f32 v[232:233], v[232:233], v[132:133]\n"
    "v_pk_fma_f32 v[134:135], v[134:135], s[70:71], v[102:103]\n"
    "v_pk_fma_f32 v[230:231], v[230:231], v[134:135], v[232:233]\n"
    "v_pk_mul_f32 v[232:233], v[232:233], v[134:135]\n"
    "v_pk_fma_f32 v[136:137], v[136:137], s[70:71], v[104:105]\n"
    "v_pk_fma_f32 v[230:231], v[230:231], v[136:137], v[232:233]\n"
    "v_pk_mul_f32 v[232:233], v[232:233], v[136:137]\n"
    "v_pk_fma_f32 v[138:139], v[138:139], s[70:71], v[106:107]\n"
    "v_pk_fma_f32 v[230:231], v[230:231], v[138:139], v[232:233]\n"
    "v_pk_mul_f32 v[232:233], v[232:233], v[138:139]\n"
    "v_pk_fma_f32 v[140:141], v[140:141], s[70:71], v[108:109]\n"
    "v_pk_fma_f32 v[230:231], v[230:231], v[140:141], v[232:233]\n"
    "v_pk_mul_f32 v[232:233], v[232:233], v[140:141]\n"
    "v_pk_fma_f32 v[142:143], v[142:143], s[70:71], v[110:111]\n"
    "v_pk_fma_f32 v[230:231], v[230:231], v[142:143], v[232:233]\n"
    "v_pk_mul_f32 v[232:233], v[232:233], v[142:143]\n"
    "v_rcp_f32 v232, v232\n"
    "v_rcp_f32 v233, v233\n"
    "v_fma_f32 v230, v230, v232, v247\n"
    "v_fmac_f32 v230, v231, v233\n"
    "v_cvt_pk_bf16_f32 v230, v230, v230\n"
    "s_add_u32 s67, s67, 64\n"
    "s_waitcnt lgkmcnt(0)\n"
    "ds_write_b16 v245, v230\n"
    "ds_read_b64 v[228:229], v246\n"
    "s_cmp_le_u32 s34, s76\n"
    "s_cbranch_scc1 .Ldin_G2_wait\n"
    ".Ldin_G2_go:\n"
    "s_sub_u32 s94, s34, 60\n"
    "s_max_u32 s76, s76, s94\n"
    "s_add_u32 s76, s76, s72\n"
    ".Ldin_G2_dma:\n"
    "buffer_load_dwordx4 v[236:237], s[40:43], 0 idxen offen offset:0 nt lds\n"
    "buffer_load_dwordx4 v[238:239], s[40:43], 0 idxen offen offset:1024 nt lds\n"
    ".Ldin_G2_nd:\n"
    "s_cmp_eq_u32 s67, s82\n"
    "s_cbranch_scc1 .Ldin_L3\n"
    ".Ldin_G3:\n"
    "v_mfma_f32_16x16x32_bf16 v[128:131], v[0:3], v[160:163], v[112:115]\n"
    "v_exp_f32 v144, v144 clamp\n"
    "v_mfma_f32_16x16x32_bf16 v[128:131], v[4:7], v[164:167], v[128:131]\n"
    "v_exp_f32 v145, v145 clamp\n"
    "v_mfma_f32_16x16x32_bf16 v[132:135], v[8:11], v[160:163], v[116:119]\n"
    "v_exp_f32 v146, v146 clamp\n"
    "v_mfma_f32_16x16x32_bf16 v[132:135], v[12:15], v[164:167], v[132:135]\n"
    "v_exp_f32 v147, v147 clamp\n"
    "v_mfma_f32_16x16x32_bf16 v[136:139], v[16:19], v[160:163], v[120:123]\n"
    "v_exp_f32 v148, v148 clamp\n"
    "v_mfma_f32_16x16x32_bf16 v[136:139], v[20:23], v[164:167], v[136:139]\n"
    "v_exp_f32 v149, v149 clamp\n"
    "v_mfma_f32_16x16x32_bf16 v[140:143], v[24:27], v[160:163], v[124:127]\n"
    "v_exp_f32 v150, v150 clamp\n"
    "v_mfma_f32_16x16x32_bf16 v[140:143], v[28:31], v[164:167], v[140:143]\n"
    "v_exp_f32 v151, v151 clamp\n"
    "v_exp_f32 v152, v152 clamp\n"
    "s_cmp_eq_u32 s67, s80\n"
    "s_cselect_b32 s67, s65, s67\n"
    "s_add_u32 m0, s68, 0\n"
    "v_exp_f32 v153, v153 clamp\n"
    "v_add_u32 v253, s67, v244\n"
    "s_waitcnt lgkmcnt(0)\n"
    "v_mfma_f32_4x4x4_16b_bf16 v[224:227], v[228:229], v[168:169], v[224:227] cbsz:4 abid:0\n"
    "v_exp_f32 v154, v154 clamp\n"
    "v_exp_f32 v155, v155 clamp\n"
    "v_mfma_f32_4x4x4_16b_bf16 v[224:227], v[228:229], v[170:171], v[224:227] cbsz:4 abid:1\n"
    "v_exp_f32 v156, v156 clamp\n"
    "v_exp_f32 v157, v157 clamp\n"
    "v_mfma_f32_4x4x4_16b_bf16 v[224:227], v[228:229], v[172:173], v[224:227] cbsz:4 abid:2\n"
    "v_exp_f32 v158, v158 clamp\n"
    "v_exp_f32 v159, v159 clamp\n"
    "v_mfma_f32_4x4x4_16b_bf16 v[224:227], v[228:229], v[174:175], v[224:227] cbsz:4 abid:3\n"
    "s_waitcnt vmcnt(4)\n"
    "s_memrealtime s[34:35]\n"
    "ds_read_b64_tr_b16 v[168:169], v240 offset:0\n"
    "ds_read_b64_tr_b16 v[170:171], v241 offset:256\n"
    "ds_read_b64_tr_b16 v[172:173], v242 offset:512\n"
    "ds_read_b64_tr_b16 v[174:175], v243 offset:768\n"
    "ds_read_b32 v236, v253 offset:0\n"
    "ds_read_b32 v238, v253 offset:4\n"
    "ds_read_b128 v[160:163], v234 offset:2048\n"
    "ds_read_b128 v[164:167], v235 offset:2048\n"
    "v_pk_fma_f32 v[144:145], v[144:145], s[70:71], v[96:97]\n"
    "v_pk_fma_f32 v[146:147], v[146:147], s[70:71], v[98:99]\n"
    "v_pk_add_f32 v[230:231], v[144:145], v[146:147]\n"
    "v_pk_mul_f32 v[232:233], v[144:145], v[146:147]\n"
    "v_pk_fma_f32 v[148:149], v[148:149], s[70:71], v[100:101]\n"
    "v_pk_fma_f32 v[230:231], v[230:231], v[148:149], v[232:233]\n"
    "v_pk_mul_f32 v[232:233], v[232:233], v[148:149]\n"
    "v_pk_fma_f32 v[150:151], v[150:151], s[70:71], v[102:103]\n"
    "v_pk_fma_f32 v[230:231], v[230:231], v[150:151], v[232:233]\n"
    "v_pk_mul_f32 v[232:233], v[232:233], v[150:151]\n"
    "v_pk_fma_f32 v[152:153], v[152:153], s[70:71], v[104:105]\n"
    "v_pk_fma_f32 v[230:231], v[230:231], v[152:153], v[232:233]\n"
    "v_pk_mul_f32 v[232:233], v[232:233], v[152:153]\n"
    "v_pk_fma_f32 v[154:155], v[154:155], s[70:71], v[106:107]\n"
    "v_pk_fma_f32 v[230:231], v[230:231], v[154:155], v[232:233]\n"
    "v_pk_mul_f32 v[232:233], v[232:233], v[154:155]\n"
    "v_pk_fma_f32 v[156:157], v[156:157], s[70:71], v[108:109]\n"
    "v_pk_fma_f32 v[230:231], v[230:231], v[156:157], v[232:233]\n"
    "v_pk_mul_f32 v[232:233], v[232:233], v[156:157]\n"
    "v_pk_fma_f32 v[158:159], v[158:159], s[70:71], v[110:111]\n"
    "v_pk_fma_f32 v[230:231], v[230:231], v[158:159], v[232:233]\n"
    "v_pk_mul_f32 v[232:233], v[232:233], v[158:159]\n"
    "v_rcp_f32 v232, v232\n"
    "v_rcp_f32 v233, v233\n"
    "v_fma_f32 v230, v230, v232, v247\n"
    "v_fmac_f32 v230, v231, v233\n"
    "v_cvt_pk_bf16_f32 v230, v230, v230\n"
    "s_add_u32 s67, s67, 64\n"
    "s_waitcnt lgkmcnt(0)\n"
    "ds_write_b16 v245, v230\n"
    "ds_read_b64 v[228:229], v246\n"
    "s_cmp_le_u32 s34, s76\n"
    "s_cbranch_scc1 .Ldin_G3_wait\n"
    ".Ldin_G3_go:\n"
    "s_add_u32 s76, s76, s72\n"
    ".Ldin_G3_dma:\n"
    "buffer_load_dwordx4 v[236:237], s[40:43], 0 idxen offen offset:0 nt lds\n"
    "buffer_load_dwordx4 v[238:239], s[40:43], 0 idxen offen offset:1024 nt lds\n"
    ".Ldin_G3_nd:\n"
    "s_cmp_eq_u32 s67, s82\n"
    "s_cbranch_scc1 .Ldin_L0\n"
    ".Ldin_G0:\n"
    "v_mfma_f32_16x16x32_bf16 v[144:147], v[0:3], v[160:163], v[112:115]\n"
    "v_exp_f32 v128, v128 clamp\n"
    "v_mfma_f32_16x16x32_bf16 v[144:147], v[4:7], v[164:167], v[144:147]\n"
    "v_exp_f32 v129, v129 clamp\n"
    "v_mfma_f32_16x16x32_bf16 v[148:151], v[8:11], v[160:163], v[116:119]\n"
    "v_exp_f32 v130, v130 clamp\n"
    "v_mfma_f32_16x16x32_bf16 v[148:151], v[12:15], v[164:167], v[148:151]\n"
    "v_exp_f32 v131, v131 clamp\n"
    "v_mfma_f32_16x16x32_bf16 v[152:155], v[16:19], v[160:163], v[120:123]\n"
    "v_exp_f32 v132, v132 clamp\n"
    "v_mfma_f32_16x16x32_bf16 v[152:155], v[20:23], v[164:167], v[152:155]\n"
    "v_exp_f32 v133, v133 clamp\n"
    "v_mfma_f32_16x16x32_bf16 v[156:159], v[24:27], v[160:163], v[124:127]\n"
    "v_exp_f32 v134, v134 clamp\n"
    "v_mfma_f32_16x16x32_bf16 v[156:159], v[28:31], v[164:167], v[156:159]\n"
    "v_exp_f32 v135, v135 clamp\n"
    "v_exp_f32 v136, v136 clamp\n"
    "s_cmp_eq_u32 s67, s80\n"
    "s_cselect_b32 s67, s65, s67\n"
    "s_add_u32 m0, s68, 2048\n"
    "v_exp_f32 v137, v137 clamp\n"
    "v_add_u32_e64 v253, s67, v244\n"
    "s_waitcnt lgkmcnt(0)\n"
    "v_mfma_f32_4x4x4_16b_bf16 v[224:227], v[228:229], v[176:177], v[224:227] cbsz:4 abid:0\n"
    "v_exp_f32 v138, v138 clamp\n"
    "v_exp_f32 v139, v139 clamp\n"
    "v_mfma_f32_4x4x4_16b_bf16 v[224:227], v[228:229], v[178:179], v[224:227] cbsz:4 abid:1\n"
    "v_exp_f32 v140, v140 clamp\n"
    "v_exp_f32 v141, v141 clamp\n"
    "v_mfma_f32_4x4x4_16b_bf16 v[224:227], v[228:229], v[180:181], v[224:227] cbsz:4 abid:2\n"
    "v_exp_f32 v142, v142 clamp\n"
    "v_exp_f32 v143, v143 clamp\n"
    "v_mfma_f32_4x4x4_16b_bf16 v[224:227], v[228:229], v[182:183], v[224:227] cbsz:4 abid:3\n"
    "s_waitcnt vmcnt(4)\n"
    "s_memrealtime s[34:35]\n"
    "ds_read_b64_tr_b16 v[176:177], v240 offset:2048\n"
    "ds_read_b64_tr_b16 v[178:179], v241 offset:2304\n"
    "ds_read_b64_tr_b16 v[180:181], v242 offset:2560\n"
    "ds_read_b64_tr_b16 v[182:183], v243 offset:2816\n"
    "ds_read_b32 v236, v253 offset:0\n"
    "ds_read_b32 v238, v253 offset:4\n"
    "ds_read_b128 v[160:163], v234 offset:4096\n"
    "ds_read_b128 v[164:167], v235 offset:4096\n"
    "v_pk_fma_f32 v[128:129], v[128:129], s[70:71], v[96:97]\n"
    "v_pk_fma_f32 v[130:131], v[130:131], s[70:71], v[98:99]\n"
    "v_pk_add_f32 v[230:231], v[128:129], v[130:131]\n"
    "v_pk_mul_f32 v[232:233], v[128:129], v[130:131]\n"
    "v_pk_fma_f32 v[132:133], v[132:133], s[70:71], v[100:101]\n"
    "v_pk_fma_f32 v[230:231], v[230:231], v[132:133], v[232:233]\n"
    "v_pk_mul_f32 v[232:233], v[232:233], v[132:133]\n"
    "v_pk_fma_f32 v[134:135], v[134:135], s[70:71], v[102:103]\n"
    "v_pk_fma_f32 v[230:231], v[230:231], v[134:135], v[232:233]\n"
    "v_pk_mul_f32 v[232:233], v[232:233], v[134:135]\n"
    "v_pk_fma_f32 v[136:137], v[136:137], s[70:71], v[104:105]\n"
    "v_pk_fma_f32 v[230:231], v[230:231], v[136:137], v[232:233]\n"
    "v_pk_mul_f32 v[232:233], v[232:233], v[136:137]\n"
    "v_pk_fma_f32 v[138:139], v[138:139], s[70:71], v[106:107]\n"
    "v_pk_fma_f32 v[230:231], v[230:231], v[138:139], v[232:233]\n"
    "v_pk_mul_f32 v[232:233], v[232:233], v[138:139]\n"
    "v_pk_fma_f32 v[140:141], v[140:141], s[70:71], v[108:109]\n"
    "v_pk_fma_f32 v[230:231], v[230:231], v[140:141], v[232:233]\n"
    "v_pk_mul_f32 v[232:233], v[232:233], v[140:141]\n"
    "v_pk_fma_f32 v[142:143], v[142:143], s[70:71], v[110:111]\n"
    "v_pk_fma_f32 v[230:231], v[230:231], v[142:143], v[232:233]\n"
    "v_pk_mul_f32 v[232:233], v[232:233], v[142:143]\n"
    "v_rcp_f32 v232, v232\n"
    "v_rcp_f32 v233, v233\n"
    "v_fma_f32 v230, v230, v232, v247\n"
    "v_fmac_f32 v230, v231, v233\n"
    "v_cvt_pk_bf16_f32 v230, v230, v230\n"
    "s_add_u32 s67, s67, 64\n"
    "s_waitcnt lgkmcnt(0)\n"
    "ds_write_b16 v245, v230\n"
    "ds_read_b64 v[228:229], v246\n"
    "s_cmp_le_u32 s34, s76\n"
    "s_cbranch_scc1 .Ldin_G0_wait\n"
    ".Ldin_G0_go:\n"
    "s_sub_u32 s94, s34, 60\n"
    "s_max_u32 s76, s76, s94\n"
    "s_add_u32 s76, s76, s72\n"
    ".Ldin_G0_dma:\n"
    "buffer_load_dwordx4 v[236:237], s[40:43], 0 idxen offen offset:0 nt lds\n"
    "buffer_load_dwordx4 v[238:239], s[40:43], 0 idxen offen offset:1024 nt lds\n"
    ".Ldin_G0_nd:\n"
    "s_cmp_eq_u32 s67, s82\n"
    "s_cbranch_scc1 .Ldin_L1\n"
    ".Ldin_G1:\n"
    "v_mfma_f32_16x16x32_bf16 v[128:131], v[0:3], v[160:163], v[112:115]\n"
    "v_exp_f32 v144, v144 clamp\n"
    "v_mfma_f32_16x16x32_bf16 v[128:131], v[4:7], v[164:167], v[128:131]\n"
    "v_exp_f32 v145, v145 clamp\n"
    "v_mfma_f32_16x16x32_bf16 v[132:135], v[8:11], v[160:163], v[116:119]\n"
    "v_exp_f32 v146, v146 clamp\n"
    "v_mfma_f32_16x16x32_bf16 v[132:135], v[12:15], v[164:167], v[132:135]\n"
    "v_exp_f32 v147, v147 clamp\n"
    "v_mfma_f32_16x16x32_bf16 v[136:139], v[16:19], v[160:163], v[120:123]\n"
    "v_exp_f32 v148, v148 clamp\n"
    "v_mfma_f32_16x16x32_bf16 v[136:139], v[20:23], v[164:167], v[136:139]\n"
    "v_exp_f32 v149, v149 clamp\n"
    "v_mfma_f32_16x16x32_bf16 v[140:143], v[24:27], v[160:163], v[124:127]\n"
    "v_exp_f32 v150, v150 clamp\n"
    "v_mfma_f32_16x16x32_bf16 v[140:143], v[28:31], v[164:167], v[140:143]\n"
    "v_exp_f32 v151, v151 clamp\n"
    "v_exp_f32 v152, v152 clamp\n"
    "s_cmp_eq_u32 s67, s80\n"
    "s_cselect_b32 s67, s65, s67\n"
    "s_add_u32 m0, s68, 4096\n"
    "v_exp_f32 v153, v153 clamp\n"
    "v_add_u32_e64 v253, s67, v244\n"
    "s_waitcnt lgkmcnt(0)\n"
    "v_mfma_f32_4x4x4_16b_bf16 v[224:227], v[228:229], v[168:169], v[224:227] cbsz:4 abid:0\n"
    "v_exp_f32 v154, v154 clamp\n"
    "v_exp_f32 v155, v155 clamp\n"
    "v_mfma_f32_4x4x4_16b_bf16 v[224:227], v[228:229], v[170:171], v[224:227] cbsz:4 abid:1\n"
    "v_exp_f32 v156, v156 clamp\n"
    "v_exp_f32 v157, v157 clamp\n"
    "v_mfma_f32_4x4x4_16b_bf16 v[224:227], v[228:229], v[172:173], v[224:227] cbsz:4 abid:2\n"
    "v_exp_f32 v158, v158 clamp\n"
    "v_exp_f32 v159, v159 clamp\n"
    "v_mfma_f32_4x4x4_16b_bf16 v[224:227], v[228:229], v[174:175], v[224:227] cbsz:4 abid:3\n"
    "s_waitcnt vmcnt(4)\n"
    "s_memrealtime s[34:35]\n"
    "ds_read_b64_tr_b16 v[168:169], v240 offset:4096\n"
    "ds_read_b64_tr_b16 v[170:171], v241 offset:4352\n"
    "ds_read_b64_tr_b16 v[172:173], v242 offset:4608\n"
    "ds_read_b64_tr_b16 v[174:175], v243 offset:4864\n"
    "ds_read_b32 v236, v253 offset:0\n"
    "ds_read_b32 v238, v253 offset:4\n"
    "ds_read_b128 v[160:163], v234 offset:6144\n"
    "ds_read_b128 v[164:167], v235 offset:6144\n"
    "v_pk_fma_f32 v[144:145], v[144:145], s[70:71], v[96:97]\n"
    "v_pk_fma_f32 v[146:147], v[146:147], s[70:71], v[98:99]\n"
    "v_pk_add_f32 v[230:231], v[144:145], v[146:147]\n"
    "v_pk_mul_f32 v[232:233], v[144:145], v[146:147]\n"
    "v_pk_fma_f32 v[148:149], v[148:149], s[70:71], v[100:101]\n"
    "v_pk_fma_f32 v[230:231], v[230:231], v[148:149], v[232:233]\n"
    "v_pk_mul_f32 v[232:233], v[232:233], v[148:149]\n"
    "v_pk_fma_f32 v[150:151], v[150:151], s[70:71], v[102:103]\n"
    "v_pk_fma_f32 v[230:231], v[230:231], v[150:151], v[232:233]\n"
    "v_pk_mul_f32 v[232:233], v[232:233], v[150:151]\n"
    "v_pk_fma_f32 v[152:153], v[152:153], s[70:71], v[104:105]\n"
    "v_pk_fma_f32 v[230:231], v[230:231], v[152:153], v[232:233]\n"
    "v_pk_mul_f32 v[232:233], v[232:233], v[152:153]\n"
    "v_pk_fma_f32 v[154:155], v[154:155], s[70:71], v[106:107]\n"
    "v_pk_fma_f32 v[230:231], v[230:231], v[154:155], v[232:233]\n"
    "v_pk_mul_f32 v[232:233], v[232:233], v[154:155]\n"
    "v_pk_fma_f32 v[156:157], v[156:157], s[70:71], v[108:109]\n"
    "v_pk_fma_f32 v[230:231], v[230:231], v[156:157], v[232:233]\n"
    "v_pk_mul_f32 v[232:233], v[232:233], v[156:157]\n"
    "v_pk_fma_f32 v[158:159], v[158:159], s[70:71], v[110:111]\n"
    "v_pk_fma_f32 v[230:231], v[230:231], v[158:159], v[232:233]\n"
    "v_pk_mul_f32 v[232:233], v[232:233], v[158:159]\n"
    "v_rcp_f32 v232, v232\n"
    "v_rcp_f32 v233, v233\n"
    "v_fma_f32 v230, v230, v232, v247\n"
    "v_fmac_f32 v230, v231, v233\n"
    "v_cvt_pk_bf16_f32 v230, v230, v230\n"
    "s_add_u32 s67, s67, 64\n"
    "s_waitcnt lgkmcnt(0)\n"
    "ds_write_b16 v245, v230\n"
    "ds_read_b64 v[228:229], v246\n"
    "s_cmp_le_u32 s34, s76\n"
    "s_cbranch_scc1 .Ldin_G1_wait\n"
    ".Ldin_G1_go:\n"
    "s_add_u32 s76, s76, s72\n"
    ".Ldin_G1_dma:\n"
    "buffer_load_dwordx4 v[236:237], s[40:43], 0 idxen offen offset:0 nt lds\n"
    "buffer_load_dwordx4 v[238:239], s[40:43], 0 idxen offen offset:1024 nt lds\n"
    ".Ldin_G1_nd:\n"
    "s_cmp_eq_u32 s67, s82\n"
    "s_cbranch_scc1 .Ldin_L2\n"
    "s_branch .Ldin_G2\n"
    ".Ldin_B1_wait:\n"
    "s_cmp_lt_i32 s59, s57\n"
    "s_cbranch_scc1 .Ldin_B1_w2\n"
    "s_sub_u32 s94, s67, s65\n"
    "s_cmp_gt_u32 s94, 256\n"
    "s_cbranch_scc1 .Ldin_B1_dma\n"
    "s_cmp_eq_u32 s94, 64\n"
    "s_waitcnt vmcnt(2)\n"
    "s_cbranch_scc1 .Ldin_B1_nd\n"
    "s_waitcnt vmcnt(0)\n"
    "s_branch .Ldin_B1_nd\n"
    ".Ldin_B1_slp:\n"
    "s_sleep 1\n"
    ".Ldin_B1_w2:\n"
    "s_memrealtime s[34:35]\n"
    "s_waitcnt lgkmcnt(0)\n"
    "s_sub_u32 s94, s34, s76\n"
    "s_cmp_lt_i32 s94, 0\n"
    "s_cbranch_scc1 .Ldin_B1_slp\n"
    "s_branch .Ldin_B1_go\n"
    ".Ldin_G2_wait:\n"
    "s_cmp_lt_i32 s59, s57\n"
    "s_cbranch_scc1 .Ldin_G2_w2\n"
    "s_sub_u32 s94, s67, s65\n"
    "s_cmp_gt_u32 s94, 256\n"
    "s_cbranch_scc1 .Ldin_G2_dma\n"
    "s_cmp_eq_u32 s94, 64\n"
    "s_waitcnt vmcnt(2)\n"
    "s_cbranch_scc1 .Ldin_G2_nd\n"
    "s_waitcnt vmcnt(0)\n"
    "s_branch .Ldin_G2_nd\n"
    ".Ldin_G2_slp:\n"
    "s_sleep 1\n"
    ".Ldin_G2_w2:\n"
    "s_memrealtime s[34:35]\n"
    "s_waitcnt lgkmcnt(0)\n"
    "s_sub_u32 s94, s34, s76\n"
    "s_cmp_lt_i32 s94, 0\n"
    "s_cbranch_scc1 .Ldin_G2_slp\n"
    "s_branch .Ldin_G2_go\n"
    ".Ldin_G3_wait:\n"
    "s_cmp_lt_i32 s59, s57\n"
    "s_cbranch_scc1 .Ldin_G3_w2\n"
    "s_sub_u32 s94, s67, s65\n"
    "s_cmp_gt_u32 s94, 256\n"
    "s_cbranch_scc1 .Ldin_G3_dma\n"
    "s_cmp_eq_u32 s94, 64\n"
    "s_waitcnt vmcnt(2)\n"
    "s_cbranch_scc1 .Ldin_G3_nd\n"
    "s_waitcnt vmcnt(0)\n"
    "s_branch .Ldin_G3_nd\n"
    ".Ldin_G3_slp:\n"
    "s_sleep 1\n"
    ".Ldin_G3_w2:\n"
    "s_memrealtime s[34:35]\n"
    "s_waitcnt lgkmcnt(0)\n"
    "s_sub_u32 s94, s34, s76\n"
    "s_cmp_lt_i32 s94, 0\n"
    "s_cbranch_scc1 .Ldin_G3_slp\n"
    "s_branch .Ldin_G3_go\n"
    ".Ldin_G0_wait:\n"
    "s_cmp_lt_i32 s59, s57\n"
    "s_cbranch_scc1 .Ldin_G0_w2\n"
    "s_sub_u32 s94, s67, s65\n"
    "s_cmp_gt_u32 s94, 256\n"
    "s_cbranch_scc1 .Ldin_G0_dma\n"
    "s_cmp_eq_u32 s94, 64\n"
    "s_waitcnt vmcnt(2)\n"
    "s_cbranch_scc1 .Ldin_G0_nd\n"
    "s_waitcnt vmcnt(0)\n"
    "s_branch .Ldin_G0_nd\n"
    ".Ldin_G0_slp:\n"
    "s_sleep 1\n"
    ".Ldin_G0_w2:\n"
    "s_memrealtime s[34:35]\n"
    "s_waitcnt lgkmcnt(0)\n"
    "s_sub_u32 s94, s34, s76\n"
    "s_cmp_lt_i32 s94, 0\n"
    "s_cbranch_scc1 .Ldin_G0_slp\n"
    "s_branch .Ldin_G0_go\n"
    ".Ldin_G1_wait:\n"
    "s_cmp_lt_i32 s59, s57\n"
    "s_cbranch_scc1 .Ldin_G1_w2\n"
    "s_sub_u32 s94, s67, s65\n"
    "s_cmp_gt_u32 s94, 256\n"
    "s_cbranch_scc1 .Ldin_G1_dma\n"
    "s_cmp_eq_u32 s94, 64\n"
    "s_waitcnt vmcnt(2)\n"
    "s_cbranch_scc1 .Ldin_G1_nd\n"
    "s_waitcnt vmcnt(0)\n"
    "s_branch .Ldin_G1_nd\n"
    ".Ldin_G1_slp:\n"
    "s_sleep 1\n"
    ".Ldin_G1_w2:\n"
    "s_memrealtime s[34:35]\n"
    "s_waitcnt lgkmcnt(0)\n"
    "s_sub_u32 s94, s34, s76\n"
    "s_cmp_lt_i32 s94, 0\n"
    "s_cbranch_scc1 .Ldin_G1_slp\n"
    "s_branch .Ldin_G1_go\n"
    ".p2align 5\n"
    ".Ldin_stub_4:\n"
    "s_cbranch_execz .Ldin_t1_ret\n"
    "s_nop 0\n"
    ".Ldin_y2:\n"
    ".Ldin_t1_ret:\n"
    "s_mov_b64 exec, -1\n"
    "s_waitcnt vmcnt(8)\n"
    "v_lshlrev_b32 v16, 16, v8\n"
    "v_lshlrev_b32 v18, 16, v9\n"
    "v_lshlrev_b32 v20, 16, v10\n"
    "v_lshlrev_b32 v22, 16, v11\n"
    "v_bitop3_b32 v16, v16, s84, v7 bitop3:0x6c\n"
    "v_bitop3_b32 v17, v8, s84, v7 bitop3:0x6c\n"
    "v_bitop3_b32 v18, v18, s84, v7 bitop3:0x6c\n"
    "v_bitop3_b32 v19, v9, s84, v7 bitop3:0x6c\n"
    "v_bitop3_b32 v20, v20, s84, v7 bitop3:0x6c\n"
    "v_bitop3_b32 v21, v10, s84, v7 bitop3:0x6c\n"
    "v_bitop3_b32 v22, v22, s84, v7 bitop3:0x6c\n"
    "v_bitop3_b32 v23, v11, s84, v7 bitop3:0x6c\n"
    "s_waitcnt vmcnt(6)\n"
    "buffer_load_dwordx4 v[32:35], v27, s[44:47], s39 offen nt\n"
    "s_add_u32 s39, s39, s17\n"
    "v_lshlrev_b32 v112, 16, v128\n"
    "v_and_b32 v114, s37, v128\n"
    "v_lshlrev_b32 v116, 16, v129\n"
    "v_and_b32 v118, s37, v129\n"
    "v_lshlrev_b32 v120, 16, v130\n"
    "v_and_b32 v122, s37, v130\n"
    "v_lshlrev_b32 v124, 16, v131\n"
    "v_and_b32 v126, s37, v131\n"
    "buffer_load_dwordx4 v[36:39], v27, s[44:47], s39 offen nt\n"
    "s_add_u32 s39, s39, s17\n"
    "v_lshlrev_b32 v113, 16, v132\n"
    "v_and_b32 v115, s37, v132\n"
    "v_lshlrev_b32 v117, 16, v133\n"
    "v_and_b32 v119, s37, v133\n"
    "v_lshlrev_b32 v121, 16, v134\n"
    "v_and_b32 v123, s37, v134\n"
    "v_lshlrev_b32 v125, 16, v135\n"
    "v_and_b32_e64 v127, s37, v135\n"
    "v_pk_mul_f32 v[112:113], v[112:113], v[16:17] op_sel_hi:[1,0]\n"
    "v_pk_mul_f32 v[114:115], v[114:115], v[16:17] op_sel:[0,1] op_sel_hi:[1,1]\n"
    "v_pk_mul_f32 v[116:117], v[116:117], v[18:19] op_sel_hi:[1,0]\n"
    "v_pk_mul_f32 v[118:119], v[118:119], v[18:19] op_sel:[0,1] op_sel_hi:[1,1]\n"
    "v_pk_mul_f32 v[120:121], v[120:121], v[20:21] op_sel_hi:[1,0]\n"
    "v_pk_mul_f32 v[122:123], v[122:123], v[20:21] op_sel:[0,1] op_sel_hi:[1,1]\n"
    "v_pk_mul_f32 v[124:125], v[124:125], v[22:23] op_sel_hi:[1,0]\n"
    "v_pk_mul_f32 v[126:127], v[126:127], v[22:23] op_sel:[0,1] op_sel_hi:[1,1]\n"
    "s_waitcnt vmcnt(6)\n"
    "buffer_load_dwordx4 v[40:43], v27, s[44:47], s39 offen nt\n"
    "s_add_u32 s39, s39, s17\n"
    "v_lshlrev_b32 v168, 16, v136\n"
    "v_and_b32 v170, s37, v136\n"
    "v_lshlrev_b32 v172, 16, v137\n"
    "v_and_b32 v174, s37, v137\n"
    "v_lshlrev_b32 v176, 16, v138\n"
    "v_and_b32 v178, s37, v138\n"
    "v_lshlrev_b32 v180, 16, v139\n"
    "v_and_b32 v182, s37, v139\n"
    "buffer_load_dwordx4 v[44:47], v27, s[44:47], s39 offen nt\n"
    "s_add_u32 s39, s39, s17\n"
    "v_lshlrev_b32 v169, 16, v140\n"
    "v_and_b32 v171, s37, v140\n"
    "v_lshlrev_b32 v173, 16, v141\n"
    "v_and_b32 v175, s37, v141\n"
    "v_lshlrev_b32 v177, 16, v142\n"
    "v_and_b32 v179, s37, v142\n"
    "v_lshlrev_b32 v181, 16, v143\n"
    "v_and_b32_e64 v183, s37, v143\n"
    "v_pk_fma_f32 v[168:169], v[168:169], v[16:17], v[112:113] op_sel_hi:[1,0,1]\n"
    "v_pk_fma_f32 v[170:171], v[170:171], v[16:17], v[114:115] op_sel:[0,1,0] op_sel_hi:[1,1,1]\n"
    "ds_write2st64_b64 v12, v[168:169], v[170:171] offset0:0 offset1:8\n"
    "v_pk_fma_f32 v[172:173], v[172:173], v[18:19], v[116:117] op_sel_hi:[1,0,1]\n"
    "v_pk_fma_f32 v[174:175], v[174:175], v[18:19], v[118:119] op_sel:[0,1,0] op_sel_hi:[1,1,1]\n"
    "ds_write2st64_b64 v12, v[172:173], v[174:175] offset0:16 offset1:24\n"
    "v_pk_fma_f32 v[176:177], v[176:177], v[20:21], v[120:121] op_sel_hi:[1,0,1]\n"
    "v_pk_fma_f32 v[178:179], v[178:179], v[20:21], v[122:123] op_sel:[0,1,0] op_sel_hi:[1,1,1]\n"
    "ds_write2st64_b64 v13, v[176:177], v[178:179] offset0:0 offset1:8\n"
    "v_pk_fma_f32 v[180:181], v[180:181], v[22:23], v[124:125] op_sel_hi:[1,0,1]\n"
    "v_pk_fma_f32 v[182:183], v[182:183], v[22:23], v[126:127] op_sel:[0,1,0] op_sel_hi:[1,1,1]\n"
    "ds_write2st64_b64 v13, v[180:181], v[182:183] offset0:16 offset1:24\n"
    "s_waitcnt vmcnt(6)\n"
    "buffer_load_dwordx4 v[48:51], v27, s[44:47], s39 offen nt\n"
    "s_add_u32 s39, s39, s17\n"
    "v_lshlrev_b32 v80, 16, v144\n"
    "v_and_b32 v82, s37, v144\n"
    "v_lshlrev_b32 v84, 16, v145\n"
    "v_and_b32 v86, s37, v145\n"
    "v_lshlrev_b32 v88, 16, v146\n"
    "v_and_b32 v90, s37, v146\n"
    "v_lshlrev_b32 v92, 16, v147\n"
    "v_and_b32 v94, s37, v147\n"
    "buffer_load_dwordx4 v[52:55], v27, s[44:47], s39 offen nt\n"
    "v_lshlrev_b32 v81, 16, v148\n"
    "v_and_b32 v83, s37, v148\n"
    "v_lshlrev_b32 v85, 16, v149\n"
    "v_and_b32 v87, s37, v149\n"
    "v_lshlrev_b32 v89, 16, v150\n"
    "v_and_b32 v91, s37, v150\n"
    "v_lshlrev_b32 v93, 16, v151\n"
    "v_and_b32 v95, s37, v151\n"
    "v_pk_mul_f32 v[80:81], v[80:81], v[16:17] op_sel_hi:[1,0]\n"
    "v_cvt_pk_bf16_f32 v80, v80, v81\n"
    "v_pk_mul_f32 v[82:83], v[82:83], v[16:17] op_sel:[0,1] op_sel_hi:[1,1]\n"
    "v_cvt_pk_bf16_f32 v82, v82, v83\n"
    "ds_write2st64_b32 v14, v80, v82 offset0:0 offset1:8\n"
    "v_pk_mul_f32 v[84:85], v[84:85], v[18:19] op_sel_hi:[1,0]\n"
    "v_cvt_pk_bf16_f32 v84, v84, v85\n"
    "v_pk_mul_f32 v[86:87], v[86:87], v[18:19] op_sel:[0,1] op_sel_hi:[1,1]\n"
    "v_cvt_pk_bf16_f32 v86, v86, v87\n"
    "ds_write2st64_b32 v14, v84, v86 offset0:16 offset1:24\n"
    "v_pk_mul_f32 v[88:89], v[88:89], v[20:21] op_sel_hi:[1,0]\n"
    "v_cvt_pk_bf16_f32 v88, v88, v89\n"
    "v_pk_mul_f32 v[90:91], v[90:91], v[20:21] op_sel:[0,1] op_sel_hi:[1,1]\n"
    "v_cvt_pk_bf16_f32 v90, v90, v91\n"
    "ds_write2st64_b32 v15, v88, v90 offset0:0 offset1:8\n"
    "v_pk_mul_f32 v[92:93], v[92:93], v[22:23] op_sel_hi:[1,0]\n"
    "v_cvt_pk_bf16_f32 v92, v92, v93\n"
    "v_pk_mul_f32 v[94:95], v[94:95], v[22:23] op_sel:[0,1] op_sel_hi:[1,1]\n"
    "v_cvt_pk_bf16_f32 v94, v94, v95\n"
    "ds_write2st64_b32 v15, v92, v94 offset0:16 offset1:24\n"
    "s_waitcnt vmcnt(6)\n"
    "v_lshlrev_b32 v184, 16, v152\n"
    "v_and_b32 v186, s37, v152\n"
    "v_lshlrev_b32 v188, 16, v153\n"
    "v_and_b32 v190, s37, v153\n"
    "v_lshlrev_b32 v192, 16, v154\n"
    "v_and_b32 v194, s37, v154\n"
    "v_lshlrev_b32 v196, 16, v155\n"
    "v_and_b32 v198, s37, v155\n"
    "v_lshlrev_b32 v185, 16, v156\n"
    "v_and_b32 v187, s37, v156\n"
    "v_lshlrev_b32 v189, 16, v157\n"
    "v_and_b32 v191, s37, v157\n"
    "v_lshlrev_b32 v193, 16, v158\n"
    "v_and_b32 v195, s37, v158\n"
    "v_lshlrev_b32 v197, 16, v159\n"
    "v_and_b32_e64 v199, s37, v159\n"
    "s_mul_i32 s36, s61, s16\n"
    "s_add_u32 s94, s36, s16\n"
    "s_cmp_lt_u32 s61, s14\n"
    "s_cselect_b32 s46, s94, 0\n"
    "buffer_load_dwordx2 v[228:229], v105, s[44:47], s36 offen nt\n"
    "s_lshl_b32 s94, s61, 6\n"
    "s_add_u32 s94, s3, s94\n"
    "s_cmp_lt_u32 s61, s14\n"
    "s_cselect_b32 s58, s94, s13\n"
    "s_add_u32 s95, s61, s15\n"
    "s_add_u32 s36, s36, s17\n"
    "s_lshl_b32 s94, s95, 6\n"
    "s_add_u32 s94, s3, s94\n"
    "s_add_u32 s96, s36, s16\n"
    "s_add_u32 s94, s94, 8\n"
    "s_cmp_lt_u32 s95, s14\n"
    "s_cselect_b32 s46, s96, 0\n"
    "s_cselect_b32 s59, s94, s13\n"
    "buffer_load_short_d16_hi v224, v249, s[44:47], s36 offen offset:0 nt\n"
    "buffer_load_short_d16_hi v225, v249, s[44:47], s36 offen offset:128 nt\n"
    "buffer_load_short_d16_hi v226, v249, s[44:47], s36 offen offset:256 nt\n"
    "buffer_load_short_d16_hi v227, v249, s[44:47], s36 offen offset:384 nt\n"
    "s_mul_i32 s46, s14, s16\n"
    "s_waitcnt lgkmcnt(0)\n"
    "s_barrier\n"
    "ds_read_b128 v[128:131], v97 offset:0\n"
    "ds_read_b128 v[132:135], v97 offset:16\n"
    "ds_read_b128 v[28:31], v99 offset:35200\n"
    "v_pk_fma_f32 v[184:185], v[184:185], v[16:17], v[112:113] op_sel_hi:[1,0,1] neg_lo:[0,0,1] neg_hi:[0,0,1]\n"
    "v_cvt_pk_bf16_f32 v184, v184, v185\n"
    "v_pk_fma_f32 v[186:187], v[186:187], v[16:17], v[114:115] op_sel:[0,1,0] op_sel_hi:[1,1,1] neg_lo:[0,0,1] neg_hi:[0,0,1]\n"
    "v_cvt_pk_bf16_f32 v186, v186, v187\n"
    "ds_write2st64_b32 v14, v184, v186 offset0:32 offset1:40\n"
    "v_pk_fma_f32 v[188:189], v[188:189], v[18:19], v[116:117] op_sel_hi:[1,0,1] neg_lo:[0,0,1] neg_hi:[0,0,1]\n"
    "v_cvt_pk_bf16_f32 v188, v188, v189\n"
    "v_pk_fma_f32 v[190:191], v[190:191], v[18:19], v[118:119] op_sel:[0,1,0] op_sel_hi:[1,1,1] neg_lo:[0,0,1] neg_hi:[0,0,1]\n"
    "v_cvt_pk_bf16_f32 v190, v190, v191\n"
    "ds_write2st64_b32 v14, v188, v190 offset0:48 offset1:56\n"
    "v_pk_fma_f32 v[192:193], v[192:193], v[20:21], v[120:121] op_sel_hi:[1,0,1] neg_lo:[0,0,1] neg_hi:[0,0,1]\n"
    "v_cvt_pk_bf16_f32 v192, v192, v193\n"
    "v_pk_fma_f32 v[194:195], v[194:195], v[20:21], v[122:123] op_sel:[0,1,0] op_sel_hi:[1,1,1] neg_lo:[0,0,1] neg_hi:[0,0,1]\n"
    "v_cvt_pk_bf16_f32 v194, v194, v195\n"
    "ds_write2st64_b32 v15, v192, v194 offset0:32 offset1:40\n"
    ".Ldin_stub_5:\n"
    "s_cbranch_execz .Ldin_t1_ret\n"
    "s_nop 0\n"
    "v_pk_fma_f32 v[196:197], v[196:197], v[22:23], v[124:125] op_sel_hi:[1,0,1] neg_lo:[0,0,1] neg_hi:[0,0,1]\n"
    "v_cvt_pk_bf16_f32 v196, v196, v197\n"
    "v_pk_fma_f32 v[198:199], v[198:199], v[22:23], v[126:127] op_sel:[0,1,0] op_sel_hi:[1,1,1] neg_lo:[0,0,1] neg_hi:[0,0,1]\n"
    "v_cvt_pk_bf16_f32 v198, v198, v199\n"
    "ds_write2st64_b32 v15, v196, v198 offset0:48 offset1:56\n"
    "s_waitcnt lgkmcnt(0)\n"
    "s_barrier\n"
    "ds_read_b128 v[8:11], v98 offset:0\n"
    "ds_read_b128 v[12:15], v98 offset:1024\n"
    "s_waitcnt lgkmcnt(1)\n"
    "v_mfma_f32_16x16x32_bf16 v[112:115], v[8:11], v[128:131], v[28:31]\n"
    "s_waitcnt lgkmcnt(0)\n"
    "v_mfma_f32_16x16x32_bf16 v[112:115], v[12:15], v[132:135], v[112:115]\n"
    "s_nop 7\n"
    "ds_write_b128 v100, v[112:115] offset:17408\n"
    "s_waitcnt lgkmcnt(0)\n"
    "s_barrier\n"
    "s_setprio 0\n"
    "ds_read_b128 v[184:187], v252 offset:0\n"
    "ds_read_b128 v[188:191], v252 offset:16\n"
    "ds_read_b32 v247, v250 offset:35072\n"
    "ds_read_b32 v232, v250 offset:35076\n"
    "s_and_b32 s84, s12, 7\n"
    "v_sub_u32 v4, s16, v26\n"
    "s_lshl_b32 s84, -1, s84\n"
    "v_cmp_gt_i32 vcc, 16, v4\n"
    "s_not_b32 s84, s84\n"
    "v_mov_b32 v6, s84\n"
    "s_add_u32 s85, s66, 480\n"
    "v_cndmask_b32 v6, -1, v6, vcc\n"
    "v_and_b32 v4, 32, v251\n"
    "v_add3_u32 v7, v4, v251, s85\n"
    "v_mov_b32 v5, 0\n"
    "s_mov_b32 s4, 0x10001\n"
    "s_mov_b32 s5, 0xc080c080\n"
    "s_waitcnt lgkmcnt(0)\n"
    "v_readfirstlane_b32 s69, v232\n"
    "v_and_b32 v184, v254, v184\n"
    "v_and_b32 v185, v255, v185\n"
    "v_and_b32 v186, v254, v186\n"
    "v_and_b32 v187, v255, v187\n"
    "v_and_b32 v188, v254, v188\n"
    "v_and_b32 v189, v255, v189\n"
    "v_and_b32 v190, v254, v190\n"
    "v_and_b32 v191, v255, v191\n"
    "s_waitcnt vmcnt(4)\n"
    "v_cmp_ne_u16_e64 s[84:85], 0, v228\n"
    "v_cmp_lt_u32_e64 s[86:87], s38, v228\n"
    "v_cmp_ne_u16_e64 s[88:89], 0, v229\n"
    "v_cmp_lt_u32_e64 s[90:91], s38, v229\n"
    "v_mbcnt_lo_u32_b32 v220, s84, v220\n"
    "s_bcnt1_i32_b64 s92, s[84:85]\n"
    "v_mbcnt_hi_u32_b32 v220, s85, v220\n"
    "s_lshl2_add_u32 s92, s92, s67\n"
    "v_mbcnt_lo_u32_b32 v220, s86, v220\n"
    "s_bcnt1_i32_b64 s93, s[86:87]\n"
    "v_mbcnt_hi_u32_b32 v220, s87, v220\n"
    "s_lshl2_add_u32 s93, s93, s92\n"
    "v_mbcnt_lo_u32_b32 v220, s88, v220\n"
    "s_bcnt1_i32_b64 s94, s[88:89]\n"
    "v_mbcnt_hi_u32_b32 v220, s89, v220\n"
    "s_lshl2_add_u32 s94, s94, s93\n"
    "v_mbcnt_lo_u32_b32 v220, s90, v220\n"
    "s_bcnt1_i32_b64 s95, s[90:91]\n"
    "v_mbcnt_hi_u32_b32 v220, s91, v220\n"
    "s_lshl2_add_u32 s95, s95, s94\n"
    "v_mov_b32_e64 v21, -1\n"
    "v_lshl_add_u32 v20, v249, 1, s95\n"
    "v_addc_co_u32_e64 v221, vcc, v220, 0, s[84:85]\n"
    "v_addc_co_u32_e64 v222, vcc, v221, 0, s[86:87]\n"
    "v_addc_co_u32_e64 v223, vcc, v222, 0, s[88:89]\n"
    "ds_write2_b32 v20, v21, v21 offset1:64\n"
    "v_lshlrev_b32 v220, 2, v220\n"
    "v_lshlrev_b32 v221, 2, v221\n"
    "v_lshlrev_b32 v222, 2, v222\n"
    "v_lshlrev_b32_e64 v223, 2, v223\n"
    "s_mov_b64 exec, s[84:85]\n"
    "ds_write_b32 v220, v216\n"
    "s_mov_b64 exec, s[86:87]\n"
    "ds_write_b32 v221, v217\n"
    "s_mov_b64 exec, s[88:89]\n"
    "ds_write_b32 v222, v218\n"
    "s_mov_b64 exec, s[90:91]\n"
    "ds_write_b32 v223, v219\n"
    "s_mov_b64 exec, -1\n"
    "ds_read_b32 v200, v253 offset:0\n"
    "ds_read_b32 v202, v253 offset:4\n"
    "ds_read_b32 v204, v253 offset:64\n"
    "ds_read_b32 v206, v253 offset:68\n"
    "ds_read_b32 v208, v253 offset:128\n"
    "ds_read_b32 v210, v253 offset:132\n"
    "ds_read_b32 v212, v253 offset:192\n"
    "ds_read_b32 v214, v253 offset:196\n"
    "s_add_u32 m0, s68, 0\n"
    "s_waitcnt lgkmcnt(6)\n"
    "buffer_load_dwordx4 v[200:201], s[40:43], 0 idxen offen offset:0 nt lds\n"
    "buffer_load_dwordx4 v[202:203], s[40:43], 0 idxen offen offset:1024 nt lds\n"
    "s_add_u32 s95, s95, 60\n"
    "s_add_u32 s94, s67, 384\n"
    "s_andn2_b32 s95, s95, 63\n"
    "s_max_u32 s80, s95, s94\n"
    "s_add_u32 m0, s68, 2048\n"
    "s_waitcnt lgkmcnt(0)\n"
    "buffer_load_dwordx4 v[204:205], s[40:43], 0 idxen offen offset:0 nt lds\n"
    "s_mov_b32 s94, 0x10001\n"
    "s_mov_b32 s95, 0xc080c080\n"
    "v_pk_min_u16 v22, v228, s94\n"
    "v_pk_min_u16 v23, v229, s94\n"
    "v_pk_mad_u16 v228, v22, s95, v228\n"
    "v_pk_mad_u16 v229, v23, s95, v229\n"
    "s_sub_u32 s87, s80, s81\n"
    "v_or_b32 v228, v228, v229\n"
    "s_lshr_b32 s87, s87, 6\n"
    "s_lshl2_add_u32 s86, s3, 68224\n"
    "v_mov_b32 v106, s86\n"
    "v_mov_b32 v108, s66\n"
    "v_mov_b32 v109, s58\n"
    "v_mov_b32 v110, s59\n"
    "s_waitcnt vmcnt(3)\n"
    "v_cmp_neq_f32_e64 s[20:21], 0, v224\n"
    "v_cmp_neq_f32_e64 s[22:23], 0, v225\n"
    "v_cmp_neq_f32_e64 s[24:25], 0, v226\n"
    "v_cmp_neq_f32_e64 s[26:27], 0, v227\n"
    "v_fma_f32 v224, v224, v224, -v224\n"
    "v_fma_f32 v225, v225, v225, -v225\n"
    "v_fma_f32 v226, v226, v226, -v226\n"
    "v_fma_f32 v227, v227, v227, -v227\n"
    "s_bcnt1_i32_b64 s88, s[20:21]\n"
    "s_bcnt1_i32_b64 s89, s[22:23]\n"
    "buffer_load_dwordx4 v[206:207], s[40:43], 0 idxen offen offset:1024 nt lds\n"
    "s_bcnt1_i32_b64 s90, s[24:25]\n"
    "s_bcnt1_i32_b64 s91, s[26:27]\n"
    "v_or3_b32 v228, v228, v224, v225\n"
    "s_add_u32 s88, s88, s89\n"
    "v_or3_b32 v228, v228, v226, v227\n"
    "s_add_u32 s90, s90, s91\n"
    "v_cmp_ne_u32_e64 s[84:85], 0, v228\n"
    "s_add_u32 s88, s88, s90\n"
    "s_pack_ll_b32_b16 s88, s87, s88\n"
    "s_mov_b64 exec, 1\n"
    "v_mov_b32_e64 v107, s88\n"
    "ds_write2_b32 v108, v109, v110 offset1:2\n"
    "s_cmp_lg_u64 s[84:85], 0\n"
    "ds_write_b32 v106, v107\n"
    "s_cselect_b32 s91, 2.0, 0\n"
    "s_mov_b64 exec, -1\n"
    "s_or_b32 s58, s58, s91\n"
    "s_or_b32 s59, s59, s91\n"
    "v_pk_min_u16 v0, v32, s4\n"
    "v_pk_min_u16 v1, v33, s4\n"
    "v_pk_min_u16 v2, v34, s4\n"
    "v_pk_min_u16 v3, v35, s4\n"
    "v_pk_mad_u16 v32, v0, s5, v32\n"
    "v_pk_mad_u16 v33, v1, s5, v33\n"
    "s_add_u32 m0, s68, 4096\n"
    "s_nop 0\n"
    "buffer_load_dwordx4 v[208:209], s[40:43], 0 idxen offen offset:0 nt lds\n"
    "v_pk_mad_u16 v34, v2, s5, v34\n"
    "v_pk_mad_u16 v35, v3, s5, v35\n"
    "v_lshl_or_b32 v0, v1, 2, v0\n"
    "v_lshl_or_b32 v0, v2, 4, v0\n"
    "v_lshl_or_b32 v0, v3, 6, v0\n"
    "v_or3_b32 v5, v5, v32, v33\n"
    "v_lshrrev_b32 v1, 15, v0\n"
    ".Ldin_stub_0:\n"
    "s_cbranch_execz .Ldin_t0_ret\n"
    "s_nop 0\n"
    "v_or3_b32 v5, v5, v34, v35\n"
    "v_bitop3_b32 v16, v1, v6, v0 bitop3:0xc8\n"
    "ds_write_b8 v7, v16 offset:0\n"
    "v_pk_min_u16 v0, v36, s4\n"
    "v_pk_min_u16 v1, v37, s4\n"
    "v_pk_min_u16 v2, v38, s4\n"
    "v_pk_min_u16 v3, v39, s4\n"
    "v_pk_mad_u16 v36, v0, s5, v36\n"
    "v_pk_mad_u16 v37, v1, s5, v37\n"
    "v_pk_mad_u16 v38, v2, s5, v38\n"
    "v_pk_mad_u16 v39, v3, s5, v39\n"
    "v_lshl_or_b32 v0, v1, 2, v0\n"
    "v_lshl_or_b32 v0, v2, 4, v0\n"
    "v_lshl_or_b32 v0, v3, 6, v0\n"
    "v_or3_b32 v5, v5, v36, v37\n"
    "v_lshrrev_b32_e64 v1, 15, v0\n"
    "v_or3_b32 v5, v5, v38, v39\n"
    "v_bitop3_b32 v17, v1, v6, v0 bitop3:0xc8\n"
    "buffer_load_dwordx4 v[210:211], s[40:43], 0 idxen offen offset:1024 nt lds\n"
    "ds_write_b8 v7, v17 offset:512\n"
    "v_pk_min_u16 v0, v40, s4\n"
    "v_pk_min_u16 v1, v41, s4\n"
    "v_pk_min_u16 v2, v42, s4\n"
    "v_pk_min_u16 v3, v43, s4\n"
    "v_pk_mad_u16 v40, v0, s5, v40\n"
    "v_pk_mad_u16 v41, v1, s5, v41\n"
    "v_pk_mad_u16 v42, v2, s5, v42\n"
    "v_pk_mad_u16 v43, v3, s5, v43\n"
    "v_lshl_or_b32 v0, v1, 2, v0\n"
    "v_lshl_or_b32 v0, v2, 4, v0\n"
    "v_lshl_or_b32 v0, v3, 6, v0\n"
    "v_or3_b32 v5, v5, v40, v41\n"
    "v_lshrrev_b32_e64 v1, 15, v0\n"
    "v_or3_b32 v5, v5, v42, v43\n"
    "v_bitop3_b32 v18, v1, v6, v0 bitop3:0xc8\n"
    "ds_write_b8 v7, v18 offset:1024\n"
    "v_pk_min_u16 v0, v44, s4\n"
    "v_pk_min_u16 v1, v45, s4\n"
    "v_pk_min_u16 v2, v46, s4\n"
    "v_pk_min_u16 v3, v47, s4\n"
    "v_pk_mad_u16 v44, v0, s5, v44\n"
    "v_pk_mad_u16 v45, v1, s5, v45\n"
    "v_pk_mad_u16 v46, v2, s5, v46\n"
    "v_pk_mad_u16 v47, v3, s5, v47\n"
    "s_add_u32 m0, s68, 6144\n"
    "s_nop 0\n"
    "buffer_load_dwordx4 v[212:213], s[40:43], 0 idxen offen offset:0 nt lds\n"
    "v_lshl_or_b32 v0, v1, 2, v0\n"
    "v_lshl_or_b32 v0, v2, 4, v0\n"
    "v_lshl_or_b32 v0, v3, 6, v0\n"
    "v_or3_b32 v5, v5, v44, v45\n"
    "v_lshrrev_b32 v1, 15, v0\n"
    "v_or3_b32 v5, v5, v46, v47\n"
    "v_bitop3_b32 v19, v1, v6, v0 bitop3:0xc8\n"
    "ds_write_b8 v7, v19 offset:1536\n"
    "v_pk_min_u16 v0, v48, s4\n"
    "v_pk_min_u16 v1, v49, s4\n"
    "v_pk_min_u16 v2, v50, s4\n"
    "v_pk_min_u16 v3, v51, s4\n"
    "v_pk_mad_u16 v48, v0, s5, v48\n"
    "v_pk_mad_u16 v49, v1, s5, v49\n"
    "v_pk_mad_u16 v50, v2, s5, v50\n"
    "v_pk_mad_u16 v51, v3, s5, v51\n"
    "v_lshl_or_b32 v0, v1, 2, v0\n"
    "v_lshl_or_b32 v0, v2, 4, v0\n"
    "v_lshl_or_b32 v0, v3, 6, v0\n"
    "v_or3_b32 v5, v5, v48, v49\n"
    "v_lshrrev_b32_e64 v1, 15, v0\n"
    "v_or3_b32 v5, v5, v50, v51\n"
    "v_bitop3_b32 v16, v1, v6, v0 bitop3:0xc8\n"
    "ds_write_b8 v7, v16 offset:2048\n"
    "v_pk_min_u16 v0, v52, s4\n"
    "buffer_load_dwordx4 v[214:215], s[40:43], 0 idxen offen offset:1024 nt lds\n"
    "v_pk_min_u16 v1, v53, s4\n"
    "v_pk_min_u16 v2, v54, s4\n"
    "v_pk_min_u16 v3, v55, s4\n"
    "v_pk_mad_u16 v52, v0, s5, v52\n"
    "v_pk_mad_u16 v53, v1, s5, v53\n"
    "v_pk_mad_u16 v54, v2, s5, v54\n"
    "v_pk_mad_u16 v55, v3, s5, v55\n"
    "v_lshl_or_b32 v0, v1, 2, v0\n"
    "v_lshl_or_b32 v0, v2, 4, v0\n"
    "v_lshl_or_b32 v0, v3, 6, v0\n"
    "v_or3_b32 v5, v5, v52, v53\n"
    "v_lshrrev_b32_e64 v1, 15, v0\n"
    "v_or3_b32 v5, v5, v54, v55\n"
    "v_bitop3_b32 v17, v1, v6, v0 bitop3:0xc8\n"
    "ds_write_b8 v7, v17 offset:2560\n"
    "ds_read_b128 v[128:131], v97 offset:4352\n"
    "ds_read_b128 v[132:135], v97 offset:4368\n"
    "ds_read_b128 v[144:147], v97 offset:8704\n"
    "ds_read_b128 v[148:151], v97 offset:8720\n"
    "ds_read_b128 v[160:163], v97 offset:13056\n"
    "ds_read_b128 v[164:167], v97 offset:13072\n"
    "s_waitcnt lgkmcnt(4)\n"
    "v_mfma_f32_16x16x32_bf16 v[116:119], v[8:11], v[128:131], v[28:31]\n"
    "v_mfma_f32_16x16x32_bf16 v[116:119], v[12:15], v[132:135], v[116:119]\n"
    "s_waitcnt lgkmcnt(2)\n"
    "v_mfma_f32_16x16x32_bf16 v[120:123], v[8:11], v[144:147], v[28:31]\n"
    "v_mfma_f32_16x16x32_bf16 v[120:123], v[12:15], v[148:151], v[120:123]\n"
    "s_waitcnt lgkmcnt(0)\n"
    "v_mfma_f32_16x16x32_bf16 v[124:127], v[8:11], v[160:163], v[28:31]\n"
    "v_mfma_f32_16x16x32_bf16 v[124:127], v[12:15], v[164:167], v[124:127]\n"
    "s_nop 7\n"
    "ds_write_b128 v100, v[116:119] offset:21760\n"
    "ds_write_b128 v100, v[120:123] offset:26112\n"
    "ds_write_b128 v100, v[124:127] offset:30464\n"
    "v_cmp_ne_u32 vcc, 0, v5\n"
    "s_cmp_lg_u64 vcc, 0\n"
    "s_cselect_b32 s94, 0x101, 1\n"
    "s_mov_b64 exec, 1\n"
    "v_mov_b32 v10, s94\n"
    "ds_add_rtn_u32 v10, v250, v10 offset:35840\n"
    "s_mov_b64 exec, -1\n"
    "s_waitcnt lgkmcnt(0)\n"
    "v_readfirstlane_b32 s95, v10\n"
    "s_add_u32 s96, s95, s94\n"
    "s_bitcmp1_b32 s96, 2\n"
    "s_cbranch_scc1 .Ldin_sched\n"
    ".Ldin_ygate:\n"
    ".Ldin_yentry:\n"
    "s_mov_b64 vcc, -1\n"
    "s_branch .Ldin_entry\n"
    ".Ldin_swait:\n"
    "s_sleep 1\n"
    "ds_read_b32 v10, v250 offset:35840\n"
    "s_waitcnt lgkmcnt(0)\n"
    "v_readfirstlane_b32 s96, v10\n"
    ".Ldin_sched:\n"
    "s_setprio 1\n"
    "v_lshrrev_b32 v2, 3, v251\n"
    "v_and_b32 v3, 7, v251\n"
    "v_lshlrev_b32 v6, 3, v3\n"
    "v_add_u32 v7, 16, v251\n"
    "s_mov_b32 s84, 0x10b80\n"
    "v_lshl_add_u32 v12, v7, 6, s84\n"
    "ds_read_b128 v[32:35], v12\n"
    "ds_read_b128 v[36:39], v12 offset:16\n"
    "s_mov_b32 s85, 0x10a80\n"
    "v_lshl_add_u32 v4, v2, 2, s85\n"
    "v_lshl_add_u32 v5, v3, 2, s85\n"
    "ds_read_u16 v17, v4 offset:2\n"
    "ds_read_u16 v4, v4\n"
    "ds_read_u16 v16, v5 offset:2\n"
    "ds_read_u16 v5, v5\n"
    "s_lshl_b32 s85, s2, 3\n"
    "s_lshl1_add_u32 s85, s15, s85\n"
    "v_mad_u32_u24 v8, v2, s15, v3\n"
    "v_add_u32 v8, s85, v8\n"
    "s_cmp_ge_u32 s96, 256\n"
    "v_cmp_gt_u32_e64 s[86:87], s14, v8\n"
    "s_cselect_b32 s85, 2.0, 0\n"
    "v_lshlrev_b32 v8, 6, v8\n"
    "s_and_b32 s87, s87, 0xffff\n"
    "v_or3_b32 v8, v8, v7, s85\n"
    "v_mov_b32 v12, s13\n"
    "v_cndmask_b32_e64 v8, v12, v8, s[86:87]\n"
    "v_cmp_eq_u32 vcc, s3, v2\n"
    "v_lshrrev_b32 v14, 2, v2\n"
    "v_mul_u32_u24 v14, 2, v14\n"
    "v_addc_co_u32_e64 v14, s[88:89], v14, 0, vcc\n"
    "v_cmp_eq_u32 vcc, s3, v3\n"
    "v_lshrrev_b32_e64 v15, 2, v3\n"
    "v_mul_u32_u24 v15, 2, v15\n"
    "v_addc_co_u32_e64 v15, s[88:89], v15, 0, vcc\n"
    "s_waitcnt lgkmcnt(4)\n"
    "v_bcnt_u32_b32 v9, v32, 15\n"
    "v_bcnt_u32_b32 v9, v33, v9\n"
    "v_bcnt_u32_b32 v9, v34, v9\n"
    "v_bcnt_u32_b32 v9, v35, v9\n"
    "v_bcnt_u32_b32 v9, v36, v9\n"
    "v_bcnt_u32_b32 v9, v37, v9\n"
    "v_bcnt_u32_b32 v9, v38, v9\n"
    "v_bcnt_u32_b32 v9, v39, v9\n"
    "v_lshrrev_b32 v9, 4, v9\n"
    "v_max_u32 v9, 6, v9\n"
    "v_lshlrev_b32 v9, 3, v9\n"
    "v_cndmask_b32_e64 v9, 0, v9, s[86:87]\n"
    "v_sub_u32 v10, v3, v9\n"
    "s_waitcnt lgkmcnt(0)\n"
    "v_add_u32 v16, 15, v16\n"
    "v_lshrrev_b32 v16, 4, v16\n"
    "v_max_u32 v16, 6, v16\n"
    "v_add_u32 v17, 15, v17\n"
    "v_lshrrev_b32_e64 v17, 4, v17\n"
    "v_max_u32 v17, 6, v17\n"
    "v_add3_u32 v4, v4, v14, v17\n"
    "v_add3_u32 v5, v5, v15, v16\n"
    "v_mov_b32_dpp v12, v10 row_half_mirror row_mask:0xf bank_mask:0xf\n"
    "v_lshl_or_b32 v4, v4, 3, v2\n"
    "v_lshl_or_b32 v5, v5, 3, v3\n"
    "v_sub_u32_dpp v32, v10, v10 quad_perm:[1,0,3,2] row_mask:0xf bank_mask:0xf\n"
    "v_sub_u32_dpp v33, v10, v10 quad_perm:[2,3,0,1] row_mask:0xf bank_mask:0xf\n"
    "v_sub_u32_dpp v34, v10, v10 quad_perm:[3,2,1,0] row_mask:0xf bank_mask:0xf\n"
    "v_sub_u32_dpp v35, v12, v10 quad_perm:[0,1,2,3] row_mask:0xf bank_mask:0xf\n"
    "v_sub_u32_dpp v36, v12, v10 quad_perm:[1,0,3,2] row_mask:0xf bank_mask:0xf\n"
    "v_sub_u32_dpp v37, v12, v10 quad_perm:[2,3,0,1] row_mask:0xf bank_mask:0xf\n"
    "v_sub_u32_dpp v38, v12, v10 quad_perm:[3,2,1,0] row_mask:0xf bank_mask:0xf\n"
    "v_lshrrev_b32 v32, 31, v32\n"
    "v_lshrrev_b32 v33, 31, v33\n"
    "v_lshrrev_b32 v34, 31, v34\n"
    "v_lshrrev_b32 v35, 31, v35\n"
    "v_lshrrev_b32 v36, 31, v36\n"
    "v_lshrrev_b32 v37, 31, v37\n"
    "v_lshrrev_b32 v38, 31, v38\n"
    "v_add3_u32 v11, v32, v33, v244\n"
    "v_add3_u32 v11, v34, v35, v11\n"
    "v_add3_u32 v11, v36, v37, v11\n"
    "v_add_u32 v11, v38, v11\n"
    "v_add_u32 v12, s65, v11\n"
    "v_lshl_add_u32 v14, v11, 3, s65\n"
    "ds_write_b8 v12, v9\n"
    "ds_write_b32 v14, v8 offset:64\n"
    "v_mov_b32 v12, s65\n"
    "ds_read_b128 v[40:43], v12 offset:0\n"
    "ds_read_b128 v[44:47], v12 offset:16\n"
    "ds_read_b128 v[48:51], v12 offset:32\n"
    "v_lshlrev_b32 v16, 3, v16\n"
    "v_sub_u32 v72, v5, v16\n"
    "v_cmp_lt_i32 vcc, v5, v4\n"
    "v_mov_b32_e64 v73, v5\n"
    "s_waitcnt lgkmcnt(0)\n"
    "v_lshrrev_b64 v[20:21], v6, vcc\n"
    "v_lshrrev_b64 v[18:19], v244, vcc\n"
    "v_and_b32 v20, 0xff, v20\n"
    "v_and_b32 v18, 0xff, v18\n"
    "v_bcnt_u32_b32 v20, v20, 0\n"
    "v_bcnt_u32_b32 v18, v18, 0\n"
    "v_lshl_add_u32 v20, v20, 3, s65\n"
    "v_lshlrev_b32_e64 v18, 3, v18\n"
    "ds_read_b32 v56, v20 offset:64\n"
    "v_lshrrev_b64 v[22:23], v20, v[40:41]\n"
    "v_lshrrev_b64 v[18:19], v18, v[40:41]\n"
    "v_and_b32 v22, 0xff, v22\n"
    "v_and_b32 v18, 0xff, v18\n"
    "v_add_u32 v5, v5, v22\n"
    "v_add_u32 v4, v4, v18\n"
    "v_cmp_lt_i32 vcc, v5, v4\n"
    "v_mov_b32_e64 v74, v5\n"
    "s_nop 0\n"
    "v_lshrrev_b64 v[20:21], v6, vcc\n"
    "v_lshrrev_b64 v[18:19], v244, vcc\n"
    "v_and_b32 v20, 0xff, v20\n"
    "v_and_b32 v18, 0xff, v18\n"
    "v_bcnt_u32_b32 v20, v20, 0\n"
    "v_bcnt_u32_b32 v18, v18, 0\n"
    "v_lshl_add_u32 v20, v20, 3, s65\n"
    "v_lshlrev_b32_e64 v18, 3, v18\n"
    "ds_read_b32 v58, v20 offset:128\n"
    "v_lshrrev_b64 v[22:23], v20, v[42:43]\n"
    "v_lshrrev_b64 v[18:19], v18, v[42:43]\n"
    "v_and_b32 v22, 0xff, v22\n"
    "v_and_b32 v18, 0xff, v18\n"
    "v_add_u32 v5, v5, v22\n"
    "v_add_u32 v4, v4, v18\n"
    "v_cmp_lt_i32 vcc, v5, v4\n"
    "v_mov_b32_e64 v75, v5\n"
    "s_nop 0\n"
    "v_lshrrev_b64 v[20:21], v6, vcc\n"
    "v_lshrrev_b64 v[18:19], v244, vcc\n"
    "v_and_b32 v20, 0xff, v20\n"
    "v_and_b32 v18, 0xff, v18\n"
    "v_bcnt_u32_b32 v20, v20, 0\n"
    "v_bcnt_u32_b32 v18, v18, 0\n"
    "v_lshl_add_u32 v20, v20, 3, s65\n"
    "v_lshlrev_b32_e64 v18, 3, v18\n"
    "ds_read_b32 v60, v20 offset:192\n"
    "v_lshrrev_b64 v[22:23], v20, v[44:45]\n"
    "v_lshrrev_b64 v[18:19], v18, v[44:45]\n"
    "v_and_b32 v22, 0xff, v22\n"
    "v_and_b32 v18, 0xff, v18\n"
    "v_add_u32 v5, v5, v22\n"
    "v_add_u32 v4, v4, v18\n"
    "v_cmp_lt_i32 vcc, v5, v4\n"
    "v_mov_b32_e64 v76, v5\n"
    ".Ldin_stub_2:\n"
    "s_cbranch_execz .Ldin_t0_ret\n"
    "s_nop 0\n"
    "s_nop 0\n"
    "v_lshrrev_b64 v[20:21], v6, vcc\n"
    "v_lshrrev_b64 v[18:19], v244, vcc\n"
    "v_and_b32 v20, 0xff, v20\n"
    "v_and_b32 v18, 0xff, v18\n"
    "v_bcnt_u32_b32 v20, v20, 0\n"
    "v_bcnt_u32_b32 v18, v18, 0\n"
    "v_lshl_add_u32 v20, v20, 3, s65\n"
    "v_lshlrev_b32_e64 v18, 3, v18\n"
    "ds_read_b32 v62, v20 offset:256\n"
    "v_lshrrev_b64 v[22:23], v20, v[46:47]\n"
    "v_lshrrev_b64 v[18:19], v18, v[46:47]\n"
    "v_and_b32 v22, 0xff, v22\n"
    "v_and_b32 v18, 0xff, v18\n"
    "v_add_u32 v5, v5, v22\n"
    "v_add_u32 v4, v4, v18\n"
    "v_cmp_lt_i32 vcc, v5, v4\n"
    "v_mov_b32_e64 v77, v5\n"
    "s_nop 0\n"
    "v_lshrrev_b64 v[20:21], v6, vcc\n"
    "v_lshrrev_b64 v[18:19], v244, vcc\n"
    "v_and_b32 v20, 0xff, v20\n"
    "v_and_b32 v18, 0xff, v18\n"
    "v_bcnt_u32_b32 v20, v20, 0\n"
    "v_bcnt_u32_b32 v18, v18, 0\n"
    "v_lshl_add_u32 v20, v20, 3, s65\n"
    "v_lshlrev_b32_e64 v18, 3, v18\n"
    "ds_read_b32 v64, v20 offset:320\n"
    "v_lshrrev_b64 v[22:23], v20, v[48:49]\n"
    "v_lshrrev_b64 v[18:19], v18, v[48:49]\n"
    "v_and_b32 v22, 0xff, v22\n"
    "v_and_b32 v18, 0xff, v18\n"
    "v_add_u32 v5, v5, v22\n"
    "v_add_u32 v4, v4, v18\n"
    "v_cmp_lt_i32 vcc, v5, v4\n"
    "v_mov_b32_e64 v78, v5\n"
    "s_nop 0\n"
    "v_lshrrev_b64 v[20:21], v6, vcc\n"
    "v_and_b32 v20, 0xff, v20\n"
    "v_bcnt_u32_b32 v20, v20, 0\n"
    "v_lshl_add_u32 v20, v20, 3, s65\n"
    "ds_read_b32 v66, v20 offset:384\n"
    "v_lshrrev_b64 v[22:23], v20, v[50:51]\n"
    "v_and_b32 v22, 0xff, v22\n"
    "v_add_u32 v5, v5, v22\n"
    "s_add_u32 s85, s76, 316\n"
    "s_lshl_b32 s86, s18, 4\n"
    "v_add_u32_dpp v12, v5, v5 quad_perm:[1,0,3,2] row_mask:0xf bank_mask:0xf\n"
    "s_add_u32 s85, s85, s86\n"
    "s_nop 0\n"
    "v_add_u32_dpp v12, v12, v12 quad_perm:[2,3,0,1] row_mask:0xf bank_mask:0xf\n"
    "s_nop 1\n"
    "v_add_u32_dpp v12, v12, v12 row_half_mirror row_mask:0xf bank_mask:0xf\n"
    "v_lshrrev_b32 v12, 3, v12\n"
    "v_sub_u32 v12, v12, v5\n"
    "v_add_u32 v72, v72, v12\n"
    "v_mul_u32_u24 v72, s18, v72\n"
    "v_lshrrev_b32 v72, 3, v72\n"
    "s_add_u32 s85, s85, s19\n"
    "v_add_u32 v57, s85, v72\n"
    "v_add_u32 v73, v73, v12\n"
    "v_mul_u32_u24 v73, s18, v73\n"
    "v_lshrrev_b32 v73, 3, v73\n"
    "s_add_u32 s85, s85, s19\n"
    "v_add_u32 v59, s85, v73\n"
    "v_add_u32 v74, v74, v12\n"
    "v_mul_u32_u24 v74, s18, v74\n"
    "v_lshrrev_b32 v74, 3, v74\n"
    "s_add_u32 s85, s85, s19\n"
    "v_add_u32 v61, s85, v74\n"
    "v_add_u32 v75, v75, v12\n"
    "v_mul_u32_u24 v75, s18, v75\n"
    "v_lshrrev_b32 v75, 3, v75\n"
    "s_add_u32 s85, s85, s19\n"
    "v_add_u32 v63, s85, v75\n"
    "v_add_u32 v76, v76, v12\n"
    "v_mul_u32_u24 v76, s18, v76\n"
    "v_lshrrev_b32 v76, 3, v76\n"
    "s_add_u32 s85, s85, s19\n"
    "v_add_u32 v65, s85, v76\n"
    "v_add_u32 v77, v77, v12\n"
    "v_mul_u32_u24 v77, s18, v77\n"
    "v_lshrrev_b32 v77, 3, v77\n"
    "s_add_u32 s85, s85, s19\n"
    "v_add_u32 v67, s85, v77\n"
    "v_lshl_add_u32 v12, v3, 7, s84\n"
    "s_mov_b64 exec, 0xff\n"
    "s_waitcnt lgkmcnt(0)\n"
    "ds_write_b128 v12, v[56:59] offset:48\n"
    "ds_write_b128 v12, v[60:63] offset:64\n"
    "ds_write_b128 v12, v[64:67] offset:80\n"
    "s_mov_b64 exec, -1\n"
    "s_branch .Ldin_ygate\n"
    ".Ldin_odd:\n"
    "s_cmp_lt_i32 s58, s57\n"
    "s_cbranch_scc1 .Ldin_A0s\n"
    "s_waitcnt vmcnt(0)\n"
    "s_branch .Ldin_exit\n"
    ".p2align 5\n"
    ".Ldin_L2:\n"
    "s_setprio 0\n"
    "s_cmp_ge_i32 s59, s57\n"
    "s_cbranch_scc1 .Ldin_E0\n"
    "s_add_u32 s76, s76, s73\n"
    "v_mov_b32 v253, s63\n"
    "v_and_b32 v184, v254, v184\n"
    "v_and_b32 v185, v255, v185\n"
    "v_and_b32 v186, v254, v186\n"
    "v_and_b32 v187, v255, v187\n"
    "v_and_b32 v188, v254, v188\n"
    "v_and_b32 v189, v255, v189\n"
    "v_and_b32 v190, v254, v190\n"
    "v_and_b32_e64 v191, v255, v191\n"
    "ds_read_b64 v[232:233], v253\n"
    "v_mfma_f32_4x4x4_16b_bf16 v[144:147], v[184:185], v[32:33], v[64:67]\n"
    "ds_read_b128 v[112:115], v252 offset:17408\n"
    "v_mfma_f32_4x4x4_16b_bf16 v[148:151], v[186:187], v[34:35], v[68:71]\n"
    "ds_read_b128 v[116:119], v252 offset:17424\n"
    "v_mfma_f32_4x4x4_16b_bf16 v[152:155], v[188:189], v[36:37], v[72:75]\n"
    "ds_read_b128 v[120:123], v252 offset:17536\n"
    "ds_read_b128 v[124:127], v252 offset:17552\n"
    "v_cvt_pk_bf16_f32 v0, v144, v145\n"
    "v_cvt_pk_bf16_f32 v1, v146, v147\n"
    "v_mfma_f32_4x4x4_16b_bf16 v[156:159], v[190:191], v[38:39], v[76:79]\n"
    "v_cvt_pk_bf16_f32 v2, v148, v149\n"
    "v_cvt_pk_bf16_f32 v3, v150, v151\n"
    "v_mfma_f32_4x4x4_16b_bf16 v[144:147], v[184:185], v[40:41], v[80:83]\n"
    "v_cvt_pk_bf16_f32 v4, v152, v153\n"
    "v_cvt_pk_bf16_f32 v5, v154, v155\n"
    "v_mfma_f32_4x4x4_16b_bf16 v[148:151], v[186:187], v[42:43], v[84:87]\n"
    "v_cvt_pk_bf16_f32 v6, v156, v157\n"
    "v_cvt_pk_bf16_f32 v7, v158, v159\n"
    "v_mfma_f32_4x4x4_16b_bf16 v[152:155], v[188:189], v[44:45], v[88:91]\n"
    "v_cvt_pk_bf16_f32 v8, v144, v145\n"
    "v_cvt_pk_bf16_f32 v9, v146, v147\n"
    "v_mfma_f32_4x4x4_16b_bf16 v[156:159], v[190:191], v[46:47], v[92:95]\n"
    "v_cvt_pk_bf16_f32 v10, v148, v149\n"
    "v_cvt_pk_bf16_f32 v11, v150, v151\n"
    "v_mfma_f32_4x4x4_16b_bf16 v[144:147], v[184:185], v[48:49], v[192:195]\n"
    "v_cvt_pk_bf16_f32 v12, v152, v153\n"
    "v_cvt_pk_bf16_f32 v13, v154, v155\n"
    "v_mfma_f32_4x4x4_16b_bf16 v[148:151], v[186:187], v[50:51], v[196:199]\n"
    "v_cvt_pk_bf16_f32 v14, v156, v157\n"
    "v_cvt_pk_bf16_f32 v15, v158, v159\n"
    "v_mfma_f32_4x4x4_16b_bf16 v[152:155], v[188:189], v[52:53], v[200:203]\n"
    "s_waitcnt lgkmcnt(0)\n"
    "v_readfirstlane_b32 s60, v232\n"
    "v_cvt_pk_bf16_f32 v16, v144, v145\n"
    "v_cvt_pk_bf16_f32 v17, v146, v147\n"
    "v_mfma_f32_4x4x4_16b_bf16 v[156:159], v[190:191], v[54:55], v[204:207]\n"
    "s_cmp_eq_u32 s60, -1\n"
    "s_cbranch_scc1 .Ldin_L2_poll\n"
    ".Ldin_L2_pr:\n"
    "v_readfirstlane_b32 s95, v233\n"
    "s_and_b32 s94, s60, 63\n"
    "s_lshl_b32 s94, s94, 6\n"
    "s_add_u32 s94, s94, 68480\n"
    "s_add_u32 s63, s63, 8\n"
    "v_lshl_add_u32 v252, v249, 1, s94\n"
    "ds_read_b32 v252, v252\n"
    "s_sub_u32 s95, s95, s76\n"
    "s_ashr_i32 s95, s95, 4\n"
    "s_min_i32 s95, s95, s74\n"
    "s_max_i32 s72, s95, s75\n"
    "v_cvt_pk_bf16_f32 v18, v148, v149\n"
    "v_cvt_pk_bf16_f32 v19, v150, v151\n"
    "v_mfma_f32_4x4x4_16b_bf16 v[144:147], v[184:185], v[56:57], v[208:211]\n"
    "v_cvt_pk_bf16_f32 v20, v152, v153\n"
    "v_cvt_pk_bf16_f32 v21, v154, v155\n"
    "v_mfma_f32_4x4x4_16b_bf16 v[148:151], v[186:187], v[58:59], v[212:215]\n"
    "v_cvt_pk_bf16_f32 v22, v156, v157\n"
    "v_cvt_pk_bf16_f32 v23, v158, v159\n"
    "v_mfma_f32_4x4x4_16b_bf16 v[152:155], v[188:189], v[60:61], v[216:219]\n"
    "v_cvt_pk_bf16_f32 v24, v144, v145\n"
    "v_cvt_pk_bf16_f32 v25, v146, v147\n"
    "v_mfma_f32_4x4x4_16b_bf16 v[156:159], v[190:191], v[62:63], v[220:223]\n"
    "v_cvt_pk_bf16_f32 v26, v148, v149\n"
    "v_cvt_pk_bf16_f32 v27, v150, v151\n"
    "v_cvt_pk_bf16_f32 v28, v152, v153\n"
    "v_cvt_pk_bf16_f32 v29, v154, v155\n"
    "v_exp_f32 v136, v136 clamp\n"
    "v_cvt_pk_bf16_f32 v30, v156, v157\n"
    "v_cvt_pk_bf16_f32 v31, v158, v159\n"
    "v_mfma_f32_16x16x32_bf16 v[144:147], v[0:3], v[160:163], v[112:115]\n"
    "v_exp_f32 v128, v128 clamp\n"
    "v_mfma_f32_16x16x32_bf16 v[144:147], v[4:7], v[164:167], v[144:147]\n"
    "v_exp_f32 v129, v129 clamp\n"
    "v_mfma_f32_16x16x32_bf16 v[148:151], v[8:11], v[160:163], v[116:119]\n"
    "v_exp_f32 v130, v130 clamp\n"
    "v_mfma_f32_16x16x32_bf16 v[148:151], v[12:15], v[164:167], v[148:151]\n"
    "v_exp_f32 v131, v131 clamp\n"
    "v_mfma_f32_16x16x32_bf16 v[152:155], v[16:19], v[160:163], v[120:123]\n"
    "v_exp_f32 v132, v132 clamp\n"
    "v_mfma_f32_16x16x32_bf16 v[152:155], v[20:23], v[164:167], v[152:155]\n"
    "v_exp_f32 v133, v133 clamp\n"
    "v_mfma_f32_16x16x32_bf16 v[156:159], v[24:27], v[160:163], v[124:127]\n"
    "v_exp_f32 v134, v134 clamp\n"
    "v_mfma_f32_16x16x32_bf16 v[156:159], v[28:31], v[164:167], v[156:159]\n"
    "v_exp_f32 v135, v135 clamp\n"
    "s_add_u32 m0, s68, 6144\n"
    "v_exp_f32 v137, v137 clamp\n"
    "v_add_u32 v253, s67, v244\n"
    "v_mfma_f32_4x4x4_16b_bf16 v[224:227], v[228:229], v[176:177], v[224:227] cbsz:4 abid:0\n"
    "v_exp_f32 v138, v138 clamp\n"
    "v_exp_f32 v139, v139 clamp\n"
    "v_mfma_f32_4x4x4_16b_bf16 v[224:227], v[228:229], v[178:179], v[224:227] cbsz:4 abid:1\n"
    "v_exp_f32 v140, v140 clamp\n"
    "v_exp_f32 v141, v141 clamp\n"
    "v_mfma_f32_4x4x4_16b_bf16 v[224:227], v[228:229], v[180:181], v[224:227] cbsz:4 abid:2\n"
    "v_exp_f32 v142, v142 clamp\n"
    "v_exp_f32 v143, v143 clamp\n"
    "v_mfma_f32_4x4x4_16b_bf16 v[224:227], v[228:229], v[182:183], v[224:227] cbsz:4 abid:3\n"
    "s_waitcnt vmcnt(4)\n"
    "ds_read_b64_tr_b16 v[176:177], v240 offset:6144\n"
    "ds_read_b64_tr_b16 v[178:179], v241 offset:6400\n"
    "ds_read_b64_tr_b16 v[180:181], v242 offset:6656\n"
    "ds_read_b64_tr_b16 v[182:183], v243 offset:6912\n"
    "ds_read_b32 v236, v253 offset:0\n"
    "ds_read_b32 v238, v253 offset:4\n"
    "ds_read_b128 v[160:163], v234 offset:0\n"
    "ds_read_b128 v[164:167], v235 offset:0\n"
    "s_waitcnt lgkmcnt(8)\n"
    "s_nop 0\n"
    "v_readlane_b32 s20, v252, 0\n"
    "v_readlane_b32 s21, v252, 1\n"
    "v_readlane_b32 s22, v252, 2\n"
    "v_readlane_b32 s23, v252, 3\n"
    "v_readlane_b32 s24, v252, 4\n"
    "v_readlane_b32 s25, v252, 5\n"
    "v_readlane_b32 s26, v252, 6\n"
    "v_readlane_b32 s27, v252, 7\n"
    "v_pk_fma_f32 v[128:129], v[128:129], s[70:71], v[96:97]\n"
    "v_pk_fma_f32 v[130:131], v[130:131], s[70:71], v[98:99]\n"
    "v_pk_add_f32 v[230:231], v[128:129], v[130:131]\n"
    "v_pk_mul_f32 v[232:233], v[128:129], v[130:131]\n"
    "v_pk_fma_f32 v[132:133], v[132:133], s[70:71], v[100:101]\n"
    "v_pk_fma_f32 v[230:231], v[230:231], v[132:133], v[232:233]\n"
    "v_pk_mul_f32 v[232:233], v[232:233], v[132:133]\n"
    "v_pk_fma_f32 v[134:135], v[134:135], s[70:71], v[102:103]\n"
    "v_pk_fma_f32 v[230:231], v[230:231], v[134:135], v[232:233]\n"
    "v_pk_mul_f32 v[232:233], v[232:233], v[134:135]\n"
    "v_pk_fma_f32 v[136:137], v[136:137], s[70:71], v[104:105]\n"
    "v_pk_fma_f32 v[230:231], v[230:231], v[136:137], v[232:233]\n"
    "v_pk_mul_f32 v[232:233], v[232:233], v[136:137]\n"
    "v_pk_fma_f32 v[138:139], v[138:139], s[70:71], v[106:107]\n"
    "v_pk_fma_f32 v[230:231], v[230:231], v[138:139], v[232:233]\n"
    "v_pk_mul_f32 v[232:233], v[232:233], v[138:139]\n"
    "v_pk_fma_f32 v[140:141], v[140:141], s[70:71], v[108:109]\n"
    "v_pk_fma_f32 v[230:231], v[230:231], v[140:141], v[232:233]\n"
    "v_pk_mul_f32 v[232:233], v[232:233], v[140:141]\n"
    "v_pk_fma_f32 v[142:143], v[142:143], s[70:71], v[110:111]\n"
    "v_pk_fma_f32 v[230:231], v[230:231], v[142:143], v[232:233]\n"
    "v_pk_mul_f32 v[232:233], v[232:233], v[142:143]\n"
    "v_rcp_f32 v232, v232\n"
    "v_rcp_f32 v233, v233\n"
    "v_fma_f32 v230, v230, v232, v247\n"
    "v_fmac_f32 v230, v231, v233\n"
    "v_cvt_pk_bf16_f32 v230, v230, v230\n"
    "s_add_u32 s67, s67, 64\n"
    "s_waitcnt lgkmcnt(0)\n"
    "ds_write_b16 v245, v230\n"
    "ds_read_b64 v[228:229], v246\n"
    "s_lshl1_add_u32 s76, s72, s76\n"
    "buffer_load_dwordx4 v[236:237], s[40:43], 0 idxen offen offset:0 nt lds\n"
    "buffer_load_dwordx4 v[238:239], s[40:43], 0 idxen offen offset:1024 nt lds\n"
    "s_and_b32 s62, s58, 63\n"
    "s_mul_i32 s62, s62, 272\n"
    "s_mov_b32 s58, s59\n"
    "s_mov_b32 s59, s60\n"
    "s_mov_b32 s81, s65\n"
    "s_xor_b32 s65, s65, s64\n"
    "s_mov_b32 s80, s83\n"
    "s_add_u32 s82, s65, 256\n"
    "s_cmp_lt_i32 s59, s57\n"
    "s_cselect_b32 s76, s76, -1\n"
    "s_bitcmp1_b32 s58, 30\n"
    "s_cbranch_scc1 .Ldin_A3s\n"
    ".Ldin_A3:\n"
    "v_mfma_f32_16x16x32_bf16 v[128:131], v[0:3], v[160:163], v[112:115]\n"
    "v_exp_f32 v144, v144 clamp\n"
    "v_mfma_f32_16x16x32_bf16 v[128:131], v[4:7], v[164:167], v[128:131]\n"
    "v_exp_f32 v145, v145 clamp\n"
    "v_mfma_f32_16x16x32_bf16 v[132:135], v[8:11], v[160:163], v[116:119]\n"
    "v_exp_f32 v146, v146 clamp\n"
    "v_mfma_f32_16x16x32_bf16 v[132:135], v[12:15], v[164:167], v[132:135]\n"
    "v_exp_f32 v147, v147 clamp\n"
    "v_mfma_f32_16x16x32_bf16 v[136:139], v[16:19], v[160:163], v[120:123]\n"
    "v_exp_f32 v148, v148 clamp\n"
    "v_mfma_f32_16x16x32_bf16 v[136:139], v[20:23], v[164:167], v[136:139]\n"
    "v_exp_f32 v149, v149 clamp\n"
    "v_mfma_f32_16x16x32_bf16 v[140:143], v[24:27], v[160:163], v[124:127]\n"
    "v_exp_f32 v150, v150 clamp\n"
    "v_mfma_f32_16x16x32_bf16 v[140:143], v[28:31], v[164:167], v[140:143]\n"
    "v_exp_f32 v151, v151 clamp\n"
    "v_exp_f32 v152, v152 clamp\n"
    "s_add_u32 m0, s68, 0\n"
    "v_exp_f32 v153, v153 clamp\n"
    "v_add_u32 v253, s67, v244\n"
    "s_waitcnt lgkmcnt(0)\n"
    "v_mfma_f32_4x4x4_16b_bf16 v[224:227], v[228:229], v[168:169], v[224:227] cbsz:4 abid:0\n"
    "v_exp_f32 v154, v154 clamp\n"
    "v_exp_f32 v155, v155 clamp\n"
    "v_mfma_f32_4x4x4_16b_bf16 v[224:227], v[228:229], v[170:171], v[224:227] cbsz:4 abid:1\n"
    "v_exp_f32 v156, v156 clamp\n"
    "v_exp_f32 v157, v157 clamp\n"
    "v_mfma_f32_4x4x4_16b_bf16 v[224:227], v[228:229], v[172:173], v[224:227] cbsz:4 abid:2\n"
    "v_exp_f32 v158, v158 clamp\n"
    "v_exp_f32 v159, v159 clamp\n"
    "v_mfma_f32_4x4x4_16b_bf16 v[224:227], v[228:229], v[174:175], v[224:227] cbsz:4 abid:3\n"
    "s_waitcnt vmcnt(4)\n"
    "ds_read_b64_tr_b16 v[168:169], v240 offset:0\n"
    "ds_read_b64_tr_b16 v[170:171], v241 offset:256\n"
    "ds_read_b64_tr_b16 v[172:173], v242 offset:512\n"
    "ds_read_b64_tr_b16 v[174:175], v243 offset:768\n"
    "ds_read_b32 v236, v253 offset:0\n"
    "ds_read_b32 v238, v253 offset:4\n"
    "ds_read_b128 v[160:163], v234 offset:2048\n"
    "ds_read_b128 v[164:167], v235 offset:2048\n"
    "s_cbranch_vccz .Ldin_A3_np\n"
    "s_setprio 1\n"
    ".Ldin_A3_np:\n"
    "v_pk_fma_f32 v[144:145], v[144:145], s[70:71], v[96:97]\n"
    "v_pk_fma_f32 v[146:147], v[146:147], s[70:71], v[98:99]\n"
    "v_add_f32 v191, v224, v225\n"
    "v_add_f32 v190, v226, v227\n"
    "v_add_f32 v191, v191, v190\n"
    "v_add_u32 v190, s62, v249\n"
    "v_mul_f32 v191, s69, v191\n"
    "v_cvt_pk_bf16_f32 v191, v191, v191\n"
    "ds_write_b16 v190, v191 offset:32768\n"
    "s_bfe_u32 s94, s59, 0x180006\n"
    "s_mul_i32 s86, s94, s56\n"
    "v_mbcnt_lo_u32_b32 v184, s20, 0\n"
    "v_mbcnt_lo_u32_b32 v185, s22, 0\n"
    "v_mbcnt_lo_u32_b32 v186, s24, 0\n"
    "v_mbcnt_lo_u32_b32 v187, s26, 0\n"
    "s_bcnt1_i32_b64 s90, s[20:21]\n"
    "s_bcnt1_i32_b64 s91, s[22:23]\n"
    "s_bcnt1_i32_b64 s92, s[24:25]\n"
    "s_bcnt1_i32_b64 s93, s[26:27]\n"
    "v_mbcnt_hi_u32_b32 v184, s21, v184\n"
    "v_mbcnt_hi_u32_b32 v185, s23, v185\n"
    "v_mbcnt_hi_u32_b32 v186, s25, v186\n"
    "v_mbcnt_hi_u32_b32 v187, s27, v187\n"
    "s_lshl2_add_u32 s90, s90, s65\n"
    "s_lshl2_add_u32 s91, s91, s90\n"
    "s_lshl2_add_u32 s92, s92, s91\n"
    "s_lshl2_add_u32 s93, s93, s92\n"
    "v_add_u32_e64 v188, s86, v251\n"
    "v_pk_add_f32 v[230:231], v[144:145], v[146:147]\n"
    "v_pk_mul_f32 v[232:233], v[144:145], v[146:147]\n"
    "v_pk_fma_f32 v[148:149], v[148:149], s[70:71], v[100:101]\n"
    "v_pk_fma_f32 v[230:231], v[230:231], v[148:149], v[232:233]\n"
    "v_pk_mul_f32 v[232:233], v[232:233], v[148:149]\n"
    "v_lshl_add_u32 v184, v184, 2, s65\n"
    "v_lshl_add_u32 v185, v185, 2, s90\n"
    "v_lshl_add_u32 v186, v186, 2, s91\n"
    "v_lshl_add_u32 v187, v187, 2, s92\n"
    "v_mov_b32 v191, -1\n"
    "v_lshl_add_u32 v190, v249, 1, s93\n"
    "ds_write2_b32 v190, v191, v191 offset1:64\n"
    "s_mov_b64 exec, s[20:21]\n"
    "ds_write_b32 v184, v188\n"
    "s_mov_b64 exec, s[22:23]\n"
    "v_add_u32 v189, 64, v188\n"
    "ds_write_b32 v185, v189\n"
    "s_mov_b64 exec, s[24:25]\n"
    "v_add_u32 v190, 128, v188\n"
    "ds_write_b32 v186, v190\n"
    "s_mov_b64 exec, s[26:27]\n"
    "v_add_u32 v189, 192, v188\n"
    "ds_write_b32 v187, v189\n"
    "s_mov_b64 exec, -1\n"
    "s_add_u32 s95, s93, 60\n"
    "s_add_u32 s94, s82, 128\n"
    "s_andn2_b32 s95, s95, 63\n"
    "s_max_u32 s83, s95, s94\n"
    "v_pk_fma_f32 v[150:151], v[150:151], s[70:71], v[102:103]\n"
    "v_pk_fma_f32 v[230:231], v[230:231], v[150:151], v[232:233]\n"
    "v_pk_mul_f32 v[232:233], v[232:233], v[150:151]\n"
    "v_pk_fma_f32 v[152:153], v[152:153], s[70:71], v[104:105]\n"
    "v_pk_fma_f32 v[230:231], v[230:231], v[152:153], v[232:233]\n"
    "v_pk_mul_f32 v[232:233], v[232:233], v[152:153]\n"
    "v_pk_fma_f32 v[154:155], v[154:155], s[70:71], v[106:107]\n"
    "v_pk_fma_f32 v[230:231], v[230:231], v[154:155], v[232:233]\n"
    "v_pk_mul_f32 v[232:233], v[232:233], v[154:155]\n"
    "v_pk_fma_f32 v[156:157], v[156:157], s[70:71], v[108:109]\n"
    "v_pk_fma_f32 v[230:231], v[230:231], v[156:157], v[232:233]\n"
    "v_pk_mul_f32 v[232:233], v[232:233], v[156:157]\n"
    "v_pk_fma_f32 v[158:159], v[158:159], s[70:71], v[110:111]\n"
    "v_pk_fma_f32 v[230:231], v[230:231], v[158:159], v[232:233]\n"
    "v_pk_mul_f32 v[232:233], v[232:233], v[158:159]\n"
    "v_rcp_f32 v232, v232\n"
    "v_rcp_f32 v233, v233\n"
    "v_fma_f32 v230, v230, v232, v247\n"
    "v_fmac_f32 v230, v231, v233\n"
    "v_cvt_pk_bf16_f32 v230, v230, v230\n"
    "s_add_u32 s67, s67, 64\n"
    "s_waitcnt lgkmcnt(0)\n"
    "ds_write_b16 v245, v230\n"
    "ds_read_b64 v[228:229], v246\n"
    "buffer_load_dwordx4 v[236:237], s[40:43], 0 idxen offen offset:0 nt lds\n"
    "buffer_load_dwordx4 v[238:239], s[40:43], 0 idxen offen offset:1024 nt lds\n"
    ".Ldin_B0:\n"
    "v_mfma_f32_16x16x32_bf16 v[144:147], v[0:3], v[160:163], v[112:115]\n"
    "v_exp_f32 v128, v128 clamp\n"
    "v_mfma_f32_16x16x32_bf16 v[144:147], v[4:7], v[164:167], v[144:147]\n"
    "v_exp_f32 v129, v129 clamp\n"
    "v_mfma_f32_16x16x32_bf16 v[148:151], v[8:11], v[160:163], v[116:119]\n"
    "v_exp_f32 v130, v130 clamp\n"
    "v_mfma_f32_16x16x32_bf16 v[148:151], v[12:15], v[164:167], v[148:151]\n"
    "v_exp_f32 v131, v131 clamp\n"
    "v_mfma_f32_16x16x32_bf16 v[152:155], v[16:19], v[160:163], v[120:123]\n"
    "v_exp_f32 v132, v132 clamp\n"
    "v_mfma_f32_16x16x32_bf16 v[152:155], v[20:23], v[164:167], v[152:155]\n"
    "v_exp_f32 v133, v133 clamp\n"
    "v_mfma_f32_16x16x32_bf16 v[156:159], v[24:27], v[160:163], v[124:127]\n"
    "v_exp_f32 v134, v134 clamp\n"
    "v_mfma_f32_16x16x32_bf16 v[156:159], v[28:31], v[164:167], v[156:159]\n"
    "v_exp_f32 v135, v135 clamp\n"
    "v_exp_f32 v136, v136 clamp\n"
    "s_cmp_eq_u32 s67, s80\n"
    "s_cselect_b32 s67, s65, s67\n"
    "s_and_b32 s94, s59, 63\n"
    "s_mul_i32 s94, s94, 272\n"
    "v_add_u32 v252, s94, v250\n"
    "s_add_u32 m0, s68, 2048\n"
    "v_exp_f32 v137, v137 clamp\n"
    "v_add_u32 v253, s67, v244\n"
    "s_waitcnt lgkmcnt(0)\n"
    "v_mfma_f32_4x4x4_16b_bf16 v[224:227], v[228:229], v[176:177], 0 cbsz:4 abid:0\n"
    "v_exp_f32 v138, v138 clamp\n"
    "v_exp_f32 v139, v139 clamp\n"
    "v_mfma_f32_4x4x4_16b_bf16 v[224:227], v[228:229], v[178:179], v[224:227] cbsz:4 abid:1\n"
    "v_exp_f32 v140, v140 clamp\n"
    "v_exp_f32 v141, v141 clamp\n"
    "v_mfma_f32_4x4x4_16b_bf16 v[224:227], v[228:229], v[180:181], v[224:227] cbsz:4 abid:2\n"
    "v_exp_f32 v142, v142 clamp\n"
    "v_exp_f32 v143, v143 clamp\n"
    "v_mfma_f32_4x4x4_16b_bf16 v[224:227], v[228:229], v[182:183], v[224:227] cbsz:4 abid:3\n"
    "s_waitcnt vmcnt(4)\n"
    "s_memrealtime s[34:35]\n"
    "ds_read_b64_tr_b16 v[176:177], v240 offset:2048\n"
    "ds_read_b64_tr_b16 v[178:179], v241 offset:2304\n"
    "ds_read_b64_tr_b16 v[180:181], v242 offset:2560\n"
    "ds_read_b64_tr_b16 v[182:183], v243 offset:2816\n"
    "ds_read_b32 v236, v253 offset:0\n"
    "ds_read_b32 v238, v253 offset:4\n"
    "ds_read_b128 v[160:163], v234 offset:4096\n"
    "ds_read_b128 v[164:167], v235 offset:4096\n"
    "ds_read_b128 v[184:187], v252 offset:0\n"
    "ds_read_b128 v[188:191], v252 offset:16\n"
    "v_pk_fma_f32 v[128:129], v[128:129], s[70:71], v[96:97]\n"
    "v_pk_fma_f32 v[130:131], v[130:131], s[70:71], v[98:99]\n"
    "v_pk_add_f32 v[230:231], v[128:129], v[130:131]\n"
    "v_pk_mul_f32 v[232:233], v[128:129], v[130:131]\n"
    "v_pk_fma_f32 v[132:133], v[132:133], s[70:71], v[100:101]\n"
    "v_pk_fma_f32 v[230:231], v[230:231], v[132:133], v[232:233]\n"
    "v_pk_mul_f32 v[232:233], v[232:233], v[132:133]\n"
    "v_pk_fma_f32 v[134:135], v[134:135], s[70:71], v[102:103]\n"
    "v_pk_fma_f32 v[230:231], v[230:231], v[134:135], v[232:233]\n"
    "v_pk_mul_f32 v[232:233], v[232:233], v[134:135]\n"
    "v_pk_fma_f32 v[136:137], v[136:137], s[70:71], v[104:105]\n"
    "v_pk_fma_f32 v[230:231], v[230:231], v[136:137], v[232:233]\n"
    "v_pk_mul_f32 v[232:233], v[232:233], v[136:137]\n"
    "v_pk_fma_f32 v[138:139], v[138:139], s[70:71], v[106:107]\n"
    "v_pk_fma_f32 v[230:231], v[230:231], v[138:139], v[232:233]\n"
    "v_pk_mul_f32 v[232:233], v[232:233], v[138:139]\n"
    "v_pk_fma_f32 v[140:141], v[140:141], s[70:71], v[108:109]\n"
    "v_pk_fma_f32 v[230:231], v[230:231], v[140:141], v[232:233]\n"
    "v_pk_mul_f32 v[232:233], v[232:233], v[140:141]\n"
    "v_pk_fma_f32 v[142:143], v[142:143], s[70:71], v[110:111]\n"
    "v_pk_fma_f32 v[230:231], v[230:231], v[142:143], v[232:233]\n"
    "v_pk_mul_f32 v[232:233], v[232:233], v[142:143]\n"
    "v_rcp_f32 v232, v232\n"
    "v_rcp_f32 v233, v233\n"
    "v_fma_f32 v230, v230, v232, v247\n"
    "v_fmac_f32 v230, v231, v233\n"
    "v_cvt_pk_bf16_f32 v230, v230, v230\n"
    "s_add_u32 s67, s67, 64\n"
    "s_waitcnt lgkmcnt(0)\n"
    "ds_write_b16 v245, v230\n"
    "ds_read_b64 v[228:229], v246\n"
    "s_cmp_le_u32 s34, s76\n"
    "s_cbranch_scc1 .Ldin_B0_wait\n"
    ".Ldin_B0_go:\n"
    "s_sub_u32 s94, s34, 60\n"
    "s_max_u32 s76, s76, s94\n"
    "s_add_u32 s76, s76, s72\n"
    ".Ldin_B0_dma:\n"
    "buffer_load_dwordx4 v[236:237], s[40:43], 0 idxen offen offset:0 nt lds\n"
    "buffer_load_dwordx4 v[238:239], s[40:43], 0 idxen offen offset:1024 nt lds\n"
    ".Ldin_B0_nd:\n"
    "s_branch .Ldin_G1\n"
    ".Ldin_L2_poll:\n"
    "s_sleep 1\n"
    "ds_read_b64 v[232:233], v253\n"
    "s_waitcnt lgkmcnt(0)\n"
    "v_readfirstlane_b32 s60, v232\n"
    "s_cmp_eq_u32 s60, -1\n"
    "s_cbranch_scc1 .Ldin_L2_poll\n"
    "s_branch .Ldin_L2_pr\n"
    ".Ldin_B0_wait:\n"
    "s_cmp_lt_i32 s59, s57\n"
    "s_cbranch_scc1 .Ldin_B0_w2\n"
    "s_sub_u32 s94, s67, s65\n"
    "s_cmp_gt_u32 s94, 256\n"
    "s_cbranch_scc1 .Ldin_B0_dma\n"
    "s_cmp_eq_u32 s94, 64\n"
    "s_waitcnt vmcnt(2)\n"
    "s_cbranch_scc1 .Ldin_B0_nd\n"
    "s_waitcnt vmcnt(0)\n"
    "s_branch .Ldin_B0_nd\n"
    ".Ldin_B0_slp:\n"
    "s_sleep 1\n"
    ".Ldin_B0_w2:\n"
    "s_memrealtime s[34:35]\n"
    "s_waitcnt lgkmcnt(0)\n"
    "s_sub_u32 s94, s34, s76\n"
    "s_cmp_lt_i32 s94, 0\n"
    "s_cbranch_scc1 .Ldin_B0_slp\n"
    "s_branch .Ldin_B0_go\n"
    ".p2align 5\n"
    ".Ldin_L1:\n"
    "s_setprio 0\n"
    "s_cmp_ge_i32 s59, s57\n"
    "s_cbranch_scc1 .Ldin_E1\n"
    "s_add_u32 s76, s76, s73\n"
    "v_mov_b32 v253, s63\n"
    "v_and_b32 v184, v254, v184\n"
    "v_and_b32 v185, v255, v185\n"
    "v_and_b32 v186, v254, v186\n"
    "v_and_b32 v187, v255, v187\n"
    "v_and_b32 v188, v254, v188\n"
    "v_and_b32 v189, v255, v189\n"
    "v_and_b32 v190, v254, v190\n"
    "v_and_b32_e64 v191, v255, v191\n"
    "ds_read_b64 v[232:233], v253\n"
    "v_mfma_f32_4x4x4_16b_bf16 v[128:131], v[184:185], v[32:33], v[64:67]\n"
    "ds_read_b128 v[112:115], v252 offset:17408\n"
    "v_mfma_f32_4x4x4_16b_bf16 v[132:135], v[186:187], v[34:35], v[68:71]\n"
    "ds_read_b128 v[116:119], v252 offset:17424\n"
    "v_mfma_f32_4x4x4_16b_bf16 v[136:139], v[188:189], v[36:37], v[72:75]\n"
    "ds_read_b128 v[120:123], v252 offset:17536\n"
    "ds_read_b128 v[124:127], v252 offset:17552\n"
    "v_cvt_pk_bf16_f32 v0, v128, v129\n"
    "v_cvt_pk_bf16_f32 v1, v130, v131\n"
    "v_mfma_f32_4x4x4_16b_bf16 v[140:143], v[190:191], v[38:39], v[76:79]\n"
    "v_cvt_pk_bf16_f32 v2, v132, v133\n"
    "v_cvt_pk_bf16_f32 v3, v134, v135\n"
    "v_mfma_f32_4x4x4_16b_bf16 v[128:131], v[184:185], v[40:41], v[80:83]\n"
    "v_cvt_pk_bf16_f32 v4, v136, v137\n"
    "v_cvt_pk_bf16_f32 v5, v138, v139\n"
    "v_mfma_f32_4x4x4_16b_bf16 v[132:135], v[186:187], v[42:43], v[84:87]\n"
    "v_cvt_pk_bf16_f32 v6, v140, v141\n"
    "v_cvt_pk_bf16_f32 v7, v142, v143\n"
    "v_mfma_f32_4x4x4_16b_bf16 v[136:139], v[188:189], v[44:45], v[88:91]\n"
    "v_cvt_pk_bf16_f32 v8, v128, v129\n"
    "v_cvt_pk_bf16_f32 v9, v130, v131\n"
    "v_mfma_f32_4x4x4_16b_bf16 v[140:143], v[190:191], v[46:47], v[92:95]\n"
    "v_cvt_pk_bf16_f32 v10, v132, v133\n"
    "v_cvt_pk_bf16_f32 v11, v134, v135\n"
    "v_mfma_f32_4x4x4_16b_bf16 v[128:131], v[184:185], v[48:49], v[192:195]\n"
    "v_cvt_pk_bf16_f32 v12, v136, v137\n"
    "v_cvt_pk_bf16_f32 v13, v138, v139\n"
    "v_mfma_f32_4x4x4_16b_bf16 v[132:135], v[186:187], v[50:51], v[196:199]\n"
    "v_cvt_pk_bf16_f32 v14, v140, v141\n"
    "v_cvt_pk_bf16_f32 v15, v142, v143\n"
    "v_mfma_f32_4x4x4_16b_bf16 v[136:139], v[188:189], v[52:53], v[200:203]\n"
    "s_waitcnt lgkmcnt(0)\n"
    "v_readfirstlane_b32 s60, v232\n"
    "v_cvt_pk_bf16_f32 v16, v128, v129\n"
    "v_cvt_pk_bf16_f32 v17, v130, v131\n"
    "v_mfma_f32_4x4x4_16b_bf16 v[140:143], v[190:191], v[54:55], v[204:207]\n"
    "s_cmp_eq_u32 s60, -1\n"
    "s_cbranch_scc1 .Ldin_L1_poll\n"
    ".Ldin_L1_pr:\n"
    "v_readfirstlane_b32 s95, v233\n"
    "s_and_b32 s94, s60, 63\n"
    "s_lshl_b32 s94, s94, 6\n"
    "s_add_u32 s94, s94, 68480\n"
    "s_add_u32 s63, s63, 8\n"
    "v_lshl_add_u32 v252, v249, 1, s94\n"
    "ds_read_b32 v252, v252\n"
    "s_sub_u32 s95, s95, s76\n"
    "s_ashr_i32 s95, s95, 4\n"
    "s_min_i32 s95, s95, s74\n"
    "s_max_i32 s72, s95, s75\n"
    "v_cvt_pk_bf16_f32 v18, v132, v133\n"
    "v_cvt_pk_bf16_f32 v19, v134, v135\n"
    "v_mfma_f32_4x4x4_16b_bf16 v[128:131], v[184:185], v[56:57], v[208:211]\n"
    "v_cvt_pk_bf16_f32 v20, v136, v137\n"
    "v_cvt_pk_bf16_f32 v21, v138, v139\n"
    "v_mfma_f32_4x4x4_16b_bf16 v[132:135], v[186:187], v[58:59], v[212:215]\n"
    "v_cvt_pk_bf16_f32 v22, v140, v141\n"
    "v_cvt_pk_bf16_f32 v23, v142, v143\n"
    "v_mfma_f32_4x4x4_16b_bf16 v[136:139], v[188:189], v[60:61], v[216:219]\n"
    "v_cvt_pk_bf16_f32 v24, v128, v129\n"
    "v_cvt_pk_bf16_f32 v25, v130, v131\n"
    "v_mfma_f32_4x4x4_16b_bf16 v[140:143], v[190:191], v[62:63], v[220:223]\n"
    "v_cvt_pk_bf16_f32 v26, v132, v133\n"
    "v_cvt_pk_bf16_f32 v27, v134, v135\n"
    "v_cvt_pk_bf16_f32 v28, v136, v137\n"
    "v_cvt_pk_bf16_f32 v29, v138, v139\n"
    "v_exp_f32 v152, v152 clamp\n"
    "v_cvt_pk_bf16_f32 v30, v140, v141\n"
    "v_cvt_pk_bf16_f32 v31, v142, v143\n"
    "v_mfma_f32_16x16x32_bf16 v[128:131], v[0:3], v[160:163], v[112:115]\n"
    "v_exp_f32 v144, v144 clamp\n"
    "v_mfma_f32_16x16x32_bf16 v[128:131], v[4:7], v[164:167], v[128:131]\n"
    "v_exp_f32 v145, v145 clamp\n"
    "v_mfma_f32_16x16x32_bf16 v[132:135], v[8:11], v[160:163], v[116:119]\n"
    "v_exp_f32 v146, v146 clamp\n"
    "v_mfma_f32_16x16x32_bf16 v[132:135], v[12:15], v[164:167], v[132:135]\n"
    "v_exp_f32 v147, v147 clamp\n"
    "v_mfma_f32_16x16x32_bf16 v[136:139], v[16:19], v[160:163], v[120:123]\n"
    "v_exp_f32 v148, v148 clamp\n"
    "v_mfma_f32_16x16x32_bf16 v[136:139], v[20:23], v[164:167], v[136:139]\n"
    "v_exp_f32 v149, v149 clamp\n"
    "v_mfma_f32_16x16x32_bf16 v[140:143], v[24:27], v[160:163], v[124:127]\n"
    "v_exp_f32 v150, v150 clamp\n"
    "v_mfma_f32_16x16x32_bf16 v[140:143], v[28:31], v[164:167], v[140:143]\n"
    "v_exp_f32 v151, v151 clamp\n"
    "s_add_u32 m0, s68, 4096\n"
    "v_exp_f32 v153, v153 clamp\n"
    "v_add_u32 v253, s67, v244\n"
    "v_mfma_f32_4x4x4_16b_bf16 v[224:227], v[228:229], v[168:169], v[224:227] cbsz:4 abid:0\n"
    "v_exp_f32 v154, v154 clamp\n"
    "v_exp_f32 v155, v155 clamp\n"
    "v_mfma_f32_4x4x4_16b_bf16 v[224:227], v[228:229], v[170:171], v[224:227] cbsz:4 abid:1\n"
    "v_exp_f32 v156, v156 clamp\n"
    "v_exp_f32 v157, v157 clamp\n"
    "v_mfma_f32_4x4x4_16b_bf16 v[224:227], v[228:229], v[172:173], v[224:227] cbsz:4 abid:2\n"
    "v_exp_f32 v158, v158 clamp\n"
    "v_exp_f32 v159, v159 clamp\n"
    "v_mfma_f32_4x4x4_16b_bf16 v[224:227], v[228:229], v[174:175], v[224:227] cbsz:4 abid:3\n"
    "s_waitcnt vmcnt(4)\n"
    "ds_read_b64_tr_b16 v[168:169], v240 offset:4096\n"
    "ds_read_b64_tr_b16 v[170:171], v241 offset:4352\n"
    "ds_read_b64_tr_b16 v[172:173], v242 offset:4608\n"
    "ds_read_b64_tr_b16 v[174:175], v243 offset:4864\n"
    "ds_read_b32 v236, v253 offset:0\n"
    "ds_read_b32 v238, v253 offset:4\n"
    "ds_read_b128 v[160:163], v234 offset:6144\n"
    "ds_read_b128 v[164:167], v235 offset:6144\n"
    "s_waitcnt lgkmcnt(8)\n"
    "s_nop 0\n"
    "v_readlane_b32 s20, v252, 0\n"
    "v_readlane_b32 s21, v252, 1\n"
    "v_readlane_b32 s22, v252, 2\n"
    "v_readlane_b32 s23, v252, 3\n"
    "v_readlane_b32 s24, v252, 4\n"
    "v_readlane_b32 s25, v252, 5\n"
    "v_readlane_b32 s26, v252, 6\n"
    "v_readlane_b32 s27, v252, 7\n"
    "v_pk_fma_f32 v[144:145], v[144:145], s[70:71], v[96:97]\n"
    "v_pk_fma_f32 v[146:147], v[146:147], s[70:71], v[98:99]\n"
    "v_pk_add_f32 v[230:231], v[144:145], v[146:147]\n"
    "v_pk_mul_f32 v[232:233], v[144:145], v[146:147]\n"
    "v_pk_fma_f32 v[148:149], v[148:149], s[70:71], v[100:101]\n"
    "v_pk_fma_f32 v[230:231], v[230:231], v[148:149], v[232:233]\n"
    "v_pk_mul_f32 v[232:233], v[232:233], v[148:149]\n"
    "v_pk_fma_f32 v[150:151], v[150:151], s[70:71], v[102:103]\n"
    "v_pk_fma_f32 v[230:231], v[230:231], v[150:151], v[232:233]\n"
    "v_pk_mul_f32 v[232:233], v[232:233], v[150:151]\n"
    "v_pk_fma_f32 v[152:153], v[152:153], s[70:71], v[104:105]\n"
    "v_pk_fma_f32 v[230:231], v[230:231], v[152:153], v[232:233]\n"
    "v_pk_mul_f32 v[232:233], v[232:233], v[152:153]\n"
    "v_pk_fma_f32 v[154:155], v[154:155], s[70:71], v[106:107]\n"
    "v_pk_fma_f32 v[230:231], v[230:231], v[154:155], v[232:233]\n"
    "v_pk_mul_f32 v[232:233], v[232:233], v[154:155]\n"
    "v_pk_fma_f32 v[156:157], v[156:157], s[70:71], v[108:109]\n"
    "v_pk_fma_f32 v[230:231], v[230:231], v[156:157], v[232:233]\n"
    "v_pk_mul_f32 v[232:233], v[232:233], v[156:157]\n"
    "v_pk_fma_f32 v[158:159], v[158:159], s[70:71], v[110:111]\n"
    "v_pk_fma_f32 v[230:231], v[230:231], v[158:159], v[232:233]\n"
    "v_pk_mul_f32 v[232:233], v[232:233], v[158:159]\n"
    "v_rcp_f32 v232, v232\n"
    "v_rcp_f32 v233, v233\n"
    "v_fma_f32 v230, v230, v232, v247\n"
    "v_fmac_f32 v230, v231, v233\n"
    "v_cvt_pk_bf16_f32 v230, v230, v230\n"
    "s_add_u32 s67, s67, 64\n"
    "s_waitcnt lgkmcnt(0)\n"
    "ds_write_b16 v245, v230\n"
    "ds_read_b64 v[228:229], v246\n"
    "s_lshl1_add_u32 s76, s72, s76\n"
    "buffer_load_dwordx4 v[236:237], s[40:43], 0 idxen offen offset:0 nt lds\n"
    "buffer_load_dwordx4 v[238:239], s[40:43], 0 idxen offen offset:1024 nt lds\n"
    "s_and_b32 s62, s58, 63\n"
    "s_mul_i32 s62, s62, 272\n"
    "s_mov_b32 s58, s59\n"
    "s_mov_b32 s59, s60\n"
    "s_mov_b32 s81, s65\n"
    "s_xor_b32 s65, s65, s64\n"
    "s_mov_b32 s80, s83\n"
    "s_add_u32 s82, s65, 256\n"
    "s_cmp_lt_i32 s59, s57\n"
    "s_cselect_b32 s76, s76, -1\n"
    "s_bitcmp1_b32 s58, 30\n"
    "s_cbranch_scc1 .Ldin_A2s\n"
    ".Ldin_A2:\n"
    "v_mfma_f32_16x16x32_bf16 v[144:147], v[0:3], v[160:163], v[112:115]\n"
    "v_exp_f32 v128, v128 clamp\n"
    "v_mfma_f32_16x16x32_bf16 v[144:147], v[4:7], v[164:167], v[144:147]\n"
    "v_exp_f32 v129, v129 clamp\n"
    "v_mfma_f32_16x16x32_bf16 v[148:151], v[8:11], v[160:163], v[116:119]\n"
    "v_exp_f32 v130, v130 clamp\n"
    "v_mfma_f32_16x16x32_bf16 v[148:151], v[12:15], v[164:167], v[148:151]\n"
    "v_exp_f32 v131, v131 clamp\n"
    "v_mfma_f32_16x16x32_bf16 v[152:155], v[16:19], v[160:163], v[120:123]\n"
    "v_exp_f32 v132, v132 clamp\n"
    "v_mfma_f32_16x16x32_bf16 v[152:155], v[20:23], v[164:167], v[152:155]\n"
    "v_exp_f32 v133, v133 clamp\n"
    "v_mfma_f32_16x16x32_bf16 v[156:159], v[24:27], v[160:163], v[124:127]\n"
    "v_exp_f32 v134, v134 clamp\n"
    "v_mfma_f32_16x16x32_bf16 v[156:159], v[28:31], v[164:167], v[156:159]\n"
    "v_exp_f32 v135, v135 clamp\n"
    "v_exp_f32 v136, v136 clamp\n"
    "s_add_u32 m0, s68, 6144\n"
    "v_exp_f32 v137, v137 clamp\n"
    "v_add_u32_e64 v253, s67, v244\n"
    "s_waitcnt lgkmcnt(0)\n"
    "v_mfma_f32_4x4x4_16b_bf16 v[224:227], v[228:229], v[176:177], v[224:227] cbsz:4 abid:0\n"
    "v_exp_f32 v138, v138 clamp\n"
    "v_exp_f32 v139, v139 clamp\n"
    "v_mfma_f32_4x4x4_16b_bf16 v[224:227], v[228:229], v[178:179], v[224:227] cbsz:4 abid:1\n"
    "v_exp_f32 v140, v140 clamp\n"
    "v_exp_f32 v141, v141 clamp\n"
    "v_mfma_f32_4x4x4_16b_bf16 v[224:227], v[228:229], v[180:181], v[224:227] cbsz:4 abid:2\n"
    "v_exp_f32 v142, v142 clamp\n"
    "v_exp_f32 v143, v143 clamp\n"
    "v_mfma_f32_4x4x4_16b_bf16 v[224:227], v[228:229], v[182:183], v[224:227] cbsz:4 abid:3\n"
    "s_waitcnt vmcnt(4)\n"
    "ds_read_b64_tr_b16 v[176:177], v240 offset:6144\n"
    "ds_read_b64_tr_b16 v[178:179], v241 offset:6400\n"
    "ds_read_b64_tr_b16 v[180:181], v242 offset:6656\n"
    "ds_read_b64_tr_b16 v[182:183], v243 offset:6912\n"
    "ds_read_b32 v236, v253 offset:0\n"
    "ds_read_b32 v238, v253 offset:4\n"
    "ds_read_b128 v[160:163], v234 offset:0\n"
    "ds_read_b128 v[164:167], v235 offset:0\n"
    "s_cbranch_vccz .Ldin_A2_np\n"
    "s_setprio 1\n"
    ".Ldin_A2_np:\n"
    "v_pk_fma_f32 v[128:129], v[128:129], s[70:71], v[96:97]\n"
    "v_pk_fma_f32 v[130:131], v[130:131], s[70:71], v[98:99]\n"
    "v_add_f32 v191, v224, v225\n"
    "v_add_f32 v190, v226, v227\n"
    "v_add_f32 v191, v191, v190\n"
    "v_add_u32 v190, s62, v249\n"
    "v_mul_f32 v191, s69, v191\n"
    "v_cvt_pk_bf16_f32 v191, v191, v191\n"
    "ds_write_b16 v190, v191 offset:32768\n"
    "s_bfe_u32 s94, s59, 0x180006\n"
    "s_mul_i32 s86, s94, s56\n"
    "v_mbcnt_lo_u32_b32 v184, s20, 0\n"
    "v_mbcnt_lo_u32_b32 v185, s22, 0\n"
    "v_mbcnt_lo_u32_b32 v186, s24, 0\n"
    "v_mbcnt_lo_u32_b32 v187, s26, 0\n"
    "s_bcnt1_i32_b64 s90, s[20:21]\n"
    "s_bcnt1_i32_b64 s91, s[22:23]\n"
    "s_bcnt1_i32_b64 s92, s[24:25]\n"
    "s_bcnt1_i32_b64 s93, s[26:27]\n"
    "v_mbcnt_hi_u32_b32 v184, s21, v184\n"
    "v_mbcnt_hi_u32_b32 v185, s23, v185\n"
    "v_mbcnt_hi_u32_b32 v186, s25, v186\n"
    "v_mbcnt_hi_u32_b32 v187, s27, v187\n"
    "s_lshl2_add_u32 s90, s90, s65\n"
    "s_lshl2_add_u32 s91, s91, s90\n"
    "s_lshl2_add_u32 s92, s92, s91\n"
    "s_lshl2_add_u32 s93, s93, s92\n"
    "v_add_u32_e64 v188, s86, v251\n"
    "v_pk_add_f32 v[230:231], v[128:129], v[130:131]\n"
    "v_pk_mul_f32 v[232:233], v[128:129], v[130:131]\n"
    "v_pk_fma_f32 v[132:133], v[132:133], s[70:71], v[100:101]\n"
    "v_pk_fma_f32 v[230:231], v[230:231], v[132:133], v[232:233]\n"
    "v_pk_mul_f32 v[232:233], v[232:233], v[132:133]\n"
    "v_lshl_add_u32 v184, v184, 2, s65\n"
    "v_lshl_add_u32 v185, v185, 2, s90\n"
    "v_lshl_add_u32 v186, v186, 2, s91\n"
    "v_lshl_add_u32 v187, v187, 2, s92\n"
    "v_mov_b32 v191, -1\n"
    "v_lshl_add_u32 v190, v249, 1, s93\n"
    "ds_write2_b32 v190, v191, v191 offset1:64\n"
    "s_mov_b64 exec, s[20:21]\n"
    "ds_write_b32 v184, v188\n"
    "s_mov_b64 exec, s[22:23]\n"
    "v_add_u32 v189, 64, v188\n"
    "ds_write_b32 v185, v189\n"
    "s_mov_b64 exec, s[24:25]\n"
    "v_add_u32 v190, 128, v188\n"
    "ds_write_b32 v186, v190\n"
    "s_mov_b64 exec, s[26:27]\n"
    "v_add_u32 v189, 192, v188\n"
    "ds_write_b32 v187, v189\n"
    "s_mov_b64 exec, -1\n"
    "s_add_u32 s95, s93, 60\n"
    "s_add_u32 s94, s82, 128\n"
    "s_andn2_b32 s95, s95, 63\n"
    "s_max_u32 s83, s95, s94\n"
    "v_pk_fma_f32 v[134:135], v[134:135], s[70:71], v[102:103]\n"
    "v_pk_fma_f32 v[230:231], v[230:231], v[134:135], v[232:233]\n"
    "v_pk_mul_f32 v[232:233], v[232:233], v[134:135]\n"
    "v_pk_fma_f32 v[136:137], v[136:137], s[70:71], v[104:105]\n"
    "v_pk_fma_f32 v[230:231], v[230:231], v[136:137], v[232:233]\n"
    "v_pk_mul_f32 v[232:233], v[232:233], v[136:137]\n"
    "v_pk_fma_f32 v[138:139], v[138:139], s[70:71], v[106:107]\n"
    "v_pk_fma_f32 v[230:231], v[230:231], v[138:139], v[232:233]\n"
    "v_pk_mul_f32 v[232:233], v[232:233], v[138:139]\n"
    "v_pk_fma_f32 v[140:141], v[140:141], s[70:71], v[108:109]\n"
    "v_pk_fma_f32 v[230:231], v[230:231], v[140:141], v[232:233]\n"
    "v_pk_mul_f32 v[232:233], v[232:233], v[140:141]\n"
    "v_pk_fma_f32 v[142:143], v[142:143], s[70:71], v[110:111]\n"
    "v_pk_fma_f32 v[230:231], v[230:231], v[142:143], v[232:233]\n"
    "v_pk_mul_f32 v[232:233], v[232:233], v[142:143]\n"
    "v_rcp_f32 v232, v232\n"
    "v_rcp_f32 v233, v233\n"
    "v_fma_f32 v230, v230, v232, v247\n"
    "v_fmac_f32 v230, v231, v233\n"
    "v_cvt_pk_bf16_f32 v230, v230, v230\n"
    "s_add_u32 s67, s67, 64\n"
    "s_waitcnt lgkmcnt(0)\n"
    "ds_write_b16 v245, v230\n"
    "ds_read_b64 v[228:229], v246\n"
    "buffer_load_dwordx4 v[236:237], s[40:43], 0 idxen offen offset:0 nt lds\n"
    "buffer_load_dwordx4 v[238:239], s[40:43], 0 idxen offen offset:1024 nt lds\n"
    ".Ldin_B3:\n"
    "v_mfma_f32_16x16x32_bf16 v[128:131], v[0:3], v[160:163], v[112:115]\n"
    "v_exp_f32 v144, v144 clamp\n"
    "v_mfma_f32_16x16x32_bf16 v[128:131], v[4:7], v[164:167], v[128:131]\n"
    "v_exp_f32 v145, v145 clamp\n"
    "v_mfma_f32_16x16x32_bf16 v[132:135], v[8:11], v[160:163], v[116:119]\n"
    "v_exp_f32 v146, v146 clamp\n"
    "v_mfma_f32_16x16x32_bf16 v[132:135], v[12:15], v[164:167], v[132:135]\n"
    "v_exp_f32 v147, v147 clamp\n"
    "v_mfma_f32_16x16x32_bf16 v[136:139], v[16:19], v[160:163], v[120:123]\n"
    "v_exp_f32 v148, v148 clamp\n"
    "v_mfma_f32_16x16x32_bf16 v[136:139], v[20:23], v[164:167], v[136:139]\n"
    "v_exp_f32 v149, v149 clamp\n"
    "v_mfma_f32_16x16x32_bf16 v[140:143], v[24:27], v[160:163], v[124:127]\n"
    "v_exp_f32 v150, v150 clamp\n"
    "v_mfma_f32_16x16x32_bf16 v[140:143], v[28:31], v[164:167], v[140:143]\n"
    "v_exp_f32 v151, v151 clamp\n"
    "v_exp_f32 v152, v152 clamp\n"
    "s_cmp_eq_u32 s67, s80\n"
    "s_cselect_b32 s67, s65, s67\n"
    "s_and_b32 s94, s59, 63\n"
    "s_mul_i32 s94, s94, 272\n"
    "v_add_u32 v252, s94, v250\n"
    "s_add_u32 m0, s68, 0\n"
    "v_exp_f32 v153, v153 clamp\n"
    "v_add_u32_e64 v253, s67, v244\n"
    "s_waitcnt lgkmcnt(0)\n"
    "v_mfma_f32_4x4x4_16b_bf16 v[224:227], v[228:229], v[168:169], 0 cbsz:4 abid:0\n"
    "v_exp_f32 v154, v154 clamp\n"
    "v_exp_f32 v155, v155 clamp\n"
    "v_mfma_f32_4x4x4_16b_bf16 v[224:227], v[228:229], v[170:171], v[224:227] cbsz:4 abid:1\n"
    "v_exp_f32 v156, v156 clamp\n"
    "v_exp_f32 v157, v157 clamp\n"
    "v_mfma_f32_4x4x4_16b_bf16 v[224:227], v[228:229], v[172:173], v[224:227] cbsz:4 abid:2\n"
    "v_exp_f32 v158, v158 clamp\n"
    "v_exp_f32 v159, v159 clamp\n"
    "v_mfma_f32_4x4x4_16b_bf16 v[224:227], v[228:229], v[174:175], v[224:227] cbsz:4 abid:3\n"
    "s_waitcnt vmcnt(4)\n"
    "s_memrealtime s[34:35]\n"
    "ds_read_b64_tr_b16 v[168:169], v240 offset:0\n"
    "ds_read_b64_tr_b16 v[170:171], v241 offset:256\n"
    "ds_read_b64_tr_b16 v[172:173], v242 offset:512\n"
    "ds_read_b64_tr_b16 v[174:175], v243 offset:768\n"
    "ds_read_b32 v236, v253 offset:0\n"
    "ds_read_b32 v238, v253 offset:4\n"
    "ds_read_b128 v[160:163], v234 offset:2048\n"
    "ds_read_b128 v[164:167], v235 offset:2048\n"
    "ds_read_b128 v[184:187], v252 offset:0\n"
    "ds_read_b128 v[188:191], v252 offset:16\n"
    "v_pk_fma_f32 v[144:145], v[144:145], s[70:71], v[96:97]\n"
    "v_pk_fma_f32 v[146:147], v[146:147], s[70:71], v[98:99]\n"
    "v_pk_add_f32 v[230:231], v[144:145], v[146:147]\n"
    "v_pk_mul_f32 v[232:233], v[144:145], v[146:147]\n"
    "v_pk_fma_f32 v[148:149], v[148:149], s[70:71], v[100:101]\n"
    "v_pk_fma_f32 v[230:231], v[230:231], v[148:149], v[232:233]\n"
    "v_pk_mul_f32 v[232:233], v[232:233], v[148:149]\n"
    "v_pk_fma_f32 v[150:151], v[150:151], s[70:71], v[102:103]\n"
    "v_pk_fma_f32 v[230:231], v[230:231], v[150:151], v[232:233]\n"
    "v_pk_mul_f32 v[232:233], v[232:233], v[150:151]\n"
    "v_pk_fma_f32 v[152:153], v[152:153], s[70:71], v[104:105]\n"
    "v_pk_fma_f32 v[230:231], v[230:231], v[152:153], v[232:233]\n"
    "v_pk_mul_f32 v[232:233], v[232:233], v[152:153]\n"
    "v_pk_fma_f32 v[154:155], v[154:155], s[70:71], v[106:107]\n"
    "v_pk_fma_f32 v[230:231], v[230:231], v[154:155], v[232:233]\n"
    "v_pk_mul_f32 v[232:233], v[232:233], v[154:155]\n"
    "v_pk_fma_f32 v[156:157], v[156:157], s[70:71], v[108:109]\n"
    "v_pk_fma_f32 v[230:231], v[230:231], v[156:157], v[232:233]\n"
    "v_pk_mul_f32 v[232:233], v[232:233], v[156:157]\n"
    "v_pk_fma_f32 v[158:159], v[158:159], s[70:71], v[110:111]\n"
    "v_pk_fma_f32 v[230:231], v[230:231], v[158:159], v[232:233]\n"
    "v_pk_mul_f32 v[232:233], v[232:233], v[158:159]\n"
    "v_rcp_f32 v232, v232\n"
    "v_rcp_f32 v233, v233\n"
    "v_fma_f32 v230, v230, v232, v247\n"
    "v_fmac_f32 v230, v231, v233\n"
    "v_cvt_pk_bf16_f32 v230, v230, v230\n"
    "s_add_u32 s67, s67, 64\n"
    "s_waitcnt lgkmcnt(0)\n"
    "ds_write_b16 v245, v230\n"
    "ds_read_b64 v[228:229], v246\n"
    "s_cmp_le_u32 s34, s76\n"
    "s_cbranch_scc1 .Ldin_B3_wait\n"
    ".Ldin_B3_go:\n"
    "s_sub_u32 s94, s34, 60\n"
    "s_max_u32 s76, s76, s94\n"
    "s_add_u32 s76, s76, s72\n"
    ".Ldin_B3_dma:\n"
    "buffer_load_dwordx4 v[236:237], s[40:43], 0 idxen offen offset:0 nt lds\n"
    "buffer_load_dwordx4 v[238:239], s[40:43], 0 idxen offen offset:1024 nt lds\n"
    ".Ldin_B3_nd:\n"
    "s_branch .Ldin_G0\n"
    ".Ldin_L1_poll:\n"
    "s_sleep 1\n"
    "ds_read_b64 v[232:233], v253\n"
    "s_waitcnt lgkmcnt(0)\n"
    "v_readfirstlane_b32 s60, v232\n"
    "s_cmp_eq_u32 s60, -1\n"
    "s_cbranch_scc1 .Ldin_L1_poll\n"
    "s_branch .Ldin_L1_pr\n"
    ".Ldin_B3_wait:\n"
    "s_cmp_lt_i32 s59, s57\n"
    "s_cbranch_scc1 .Ldin_B3_w2\n"
    "s_sub_u32 s94, s67, s65\n"
    "s_cmp_gt_u32 s94, 256\n"
    "s_cbranch_scc1 .Ldin_B3_dma\n"
    "s_cmp_eq_u32 s94, 64\n"
    "s_waitcnt vmcnt(2)\n"
    "s_cbranch_scc1 .Ldin_B3_nd\n"
    "s_waitcnt vmcnt(0)\n"
    "s_branch .Ldin_B3_nd\n"
    ".Ldin_B3_slp:\n"
    "s_sleep 1\n"
    ".Ldin_B3_w2:\n"
    "s_memrealtime s[34:35]\n"
    "s_waitcnt lgkmcnt(0)\n"
    "s_sub_u32 s94, s34, s76\n"
    "s_cmp_lt_i32 s94, 0\n"
    "s_cbranch_scc1 .Ldin_B3_slp\n"
    "s_branch .Ldin_B3_go\n"
    ".p2align 5\n"
    ".Ldin_L0:\n"
    "s_setprio 0\n"
    "s_cmp_ge_i32 s59, s57\n"
    "s_cbranch_scc1 .Ldin_E0\n"
    "s_add_u32 s76, s76, s73\n"
    "v_mov_b32 v253, s63\n"
    "v_and_b32 v184, v254, v184\n"
    "v_and_b32 v185, v255, v185\n"
    "v_and_b32 v186, v254, v186\n"
    "v_and_b32 v187, v255, v187\n"
    "v_and_b32 v188, v254, v188\n"
    "v_and_b32 v189, v255, v189\n"
    "v_and_b32 v190, v254, v190\n"
    "v_and_b32_e64 v191, v255, v191\n"
    "ds_read_b64 v[232:233], v253\n"
    "v_mfma_f32_4x4x4_16b_bf16 v[144:147], v[184:185], v[32:33], v[64:67]\n"
    "ds_read_b128 v[112:115], v252 offset:17408\n"
    "v_mfma_f32_4x4x4_16b_bf16 v[148:151], v[186:187], v[34:35], v[68:71]\n"
    "ds_read_b128 v[116:119], v252 offset:17424\n"
    "v_mfma_f32_4x4x4_16b_bf16 v[152:155], v[188:189], v[36:37], v[72:75]\n"
    "ds_read_b128 v[120:123], v252 offset:17536\n"
    "ds_read_b128 v[124:127], v252 offset:17552\n"
    "v_cvt_pk_bf16_f32 v0, v144, v145\n"
    "v_cvt_pk_bf16_f32 v1, v146, v147\n"
    "v_mfma_f32_4x4x4_16b_bf16 v[156:159], v[190:191], v[38:39], v[76:79]\n"
    "v_cvt_pk_bf16_f32 v2, v148, v149\n"
    "v_cvt_pk_bf16_f32 v3, v150, v151\n"
    "v_mfma_f32_4x4x4_16b_bf16 v[144:147], v[184:185], v[40:41], v[80:83]\n"
    "v_cvt_pk_bf16_f32 v4, v152, v153\n"
    "v_cvt_pk_bf16_f32 v5, v154, v155\n"
    "v_mfma_f32_4x4x4_16b_bf16 v[148:151], v[186:187], v[42:43], v[84:87]\n"
    "v_cvt_pk_bf16_f32 v6, v156, v157\n"
    "v_cvt_pk_bf16_f32 v7, v158, v159\n"
    "v_mfma_f32_4x4x4_16b_bf16 v[152:155], v[188:189], v[44:45], v[88:91]\n"
    "v_cvt_pk_bf16_f32 v8, v144, v145\n"
    "v_cvt_pk_bf16_f32 v9, v146, v147\n"
    "v_mfma_f32_4x4x4_16b_bf16 v[156:159], v[190:191], v[46:47], v[92:95]\n"
    "v_cvt_pk_bf16_f32 v10, v148, v149\n"
    "v_cvt_pk_bf16_f32 v11, v150, v151\n"
    "v_mfma_f32_4x4x4_16b_bf16 v[144:147], v[184:185], v[48:49], v[192:195]\n"
    "v_cvt_pk_bf16_f32 v12, v152, v153\n"
    "v_cvt_pk_bf16_f32 v13, v154, v155\n"
    "v_mfma_f32_4x4x4_16b_bf16 v[148:151], v[186:187], v[50:51], v[196:199]\n"
    "v_cvt_pk_bf16_f32 v14, v156, v157\n"
    "v_cvt_pk_bf16_f32 v15, v158, v159\n"
    "v_mfma_f32_4x4x4_16b_bf16 v[152:155], v[188:189], v[52:53], v[200:203]\n"
    "s_waitcnt lgkmcnt(0)\n"
    "v_readfirstlane_b32 s60, v232\n"
    "v_cvt_pk_bf16_f32 v16, v144, v145\n"
    "v_cvt_pk_bf16_f32 v17, v146, v147\n"
    "v_mfma_f32_4x4x4_16b_bf16 v[156:159], v[190:191], v[54:55], v[204:207]\n"
    "s_cmp_eq_u32 s60, -1\n"
    "s_cbranch_scc1 .Ldin_L0_poll\n"
    ".Ldin_L0_pr:\n"
    "v_readfirstlane_b32 s95, v233\n"
    "s_and_b32 s94, s60, 63\n"
    "s_lshl_b32 s94, s94, 6\n"
    "s_add_u32 s94, s94, 68480\n"
    "s_add_u32 s63, s63, 8\n"
    "v_lshl_add_u32 v252, v249, 1, s94\n"
    "ds_read_b32 v252, v252\n"
    "s_sub_u32 s95, s95, s76\n"
    "s_ashr_i32 s95, s95, 4\n"
    "s_min_i32 s95, s95, s74\n"
    "s_max_i32 s72, s95, s75\n"
    "v_cvt_pk_bf16_f32 v18, v148, v149\n"
    "v_cvt_pk_bf16_f32 v19, v150, v151\n"
    "v_mfma_f32_4x4x4_16b_bf16 v[144:147], v[184:185], v[56:57], v[208:211]\n"
    "v_cvt_pk_bf16_f32 v20, v152, v153\n"
    "v_cvt_pk_bf16_f32 v21, v154, v155\n"
    "v_mfma_f32_4x4x4_16b_bf16 v[148:151], v[186:187], v[58:59], v[212:215]\n"
    "v_cvt_pk_bf16_f32 v22, v156, v157\n"
    "v_cvt_pk_bf16_f32 v23, v158, v159\n"
    "v_mfma_f32_4x4x4_16b_bf16 v[152:155], v[188:189], v[60:61], v[216:219]\n"
    "v_cvt_pk_bf16_f32 v24, v144, v145\n"
    "v_cvt_pk_bf16_f32 v25, v146, v147\n"
    "v_mfma_f32_4x4x4_16b_bf16 v[156:159], v[190:191], v[62:63], v[220:223]\n"
    "v_cvt_pk_bf16_f32 v26, v148, v149\n"
    "v_cvt_pk_bf16_f32 v27, v150, v151\n"
    "v_cvt_pk_bf16_f32 v28, v152, v153\n"
    "v_cvt_pk_bf16_f32 v29, v154, v155\n"
    "v_exp_f32 v136, v136 clamp\n"
    "v_cvt_pk_bf16_f32 v30, v156, v157\n"
    "v_cvt_pk_bf16_f32 v31, v158, v159\n"
    "v_mfma_f32_16x16x32_bf16 v[144:147], v[0:3], v[160:163], v[112:115]\n"
    "v_exp_f32 v128, v128 clamp\n"
    "v_mfma_f32_16x16x32_bf16 v[144:147], v[4:7], v[164:167], v[144:147]\n"
    "v_exp_f32 v129, v129 clamp\n"
    "v_mfma_f32_16x16x32_bf16 v[148:151], v[8:11], v[160:163], v[116:119]\n"
    "v_exp_f32 v130, v130 clamp\n"
    "v_mfma_f32_16x16x32_bf16 v[148:151], v[12:15], v[164:167], v[148:151]\n"
    "v_exp_f32 v131, v131 clamp\n"
    "v_mfma_f32_16x16x32_bf16 v[152:155], v[16:19], v[160:163], v[120:123]\n"
    "v_exp_f32 v132, v132 clamp\n"
    "v_mfma_f32_16x16x32_bf16 v[152:155], v[20:23], v[164:167], v[152:155]\n"
    "v_exp_f32 v133, v133 clamp\n"
    "v_mfma_f32_16x16x32_bf16 v[156:159], v[24:27], v[160:163], v[124:127]\n"
    "v_exp_f32 v134, v134 clamp\n"
    "v_mfma_f32_16x16x32_bf16 v[156:159], v[28:31], v[164:167], v[156:159]\n"
    "v_exp_f32 v135, v135 clamp\n"
    "s_add_u32 m0, s68, 2048\n"
    "v_exp_f32 v137, v137 clamp\n"
    "v_add_u32 v253, s67, v244\n"
    "v_mfma_f32_4x4x4_16b_bf16 v[224:227], v[228:229], v[176:177], v[224:227] cbsz:4 abid:0\n"
    "v_exp_f32 v138, v138 clamp\n"
    "v_exp_f32 v139, v139 clamp\n"
    "v_mfma_f32_4x4x4_16b_bf16 v[224:227], v[228:229], v[178:179], v[224:227] cbsz:4 abid:1\n"
    "v_exp_f32 v140, v140 clamp\n"
    "v_exp_f32 v141, v141 clamp\n"
    "v_mfma_f32_4x4x4_16b_bf16 v[224:227], v[228:229], v[180:181], v[224:227] cbsz:4 abid:2\n"
    "v_exp_f32 v142, v142 clamp\n"
    "v_exp_f32 v143, v143 clamp\n"
    "v_mfma_f32_4x4x4_16b_bf16 v[224:227], v[228:229], v[182:183], v[224:227] cbsz:4 abid:3\n"
    "s_waitcnt vmcnt(4)\n"
    "ds_read_b64_tr_b16 v[176:177], v240 offset:2048\n"
    "ds_read_b64_tr_b16 v[178:179], v241 offset:2304\n"
    "ds_read_b64_tr_b16 v[180:181], v242 offset:2560\n"
    "ds_read_b64_tr_b16 v[182:183], v243 offset:2816\n"
    "ds_read_b32 v236, v253 offset:0\n"
    "ds_read_b32 v238, v253 offset:4\n"
    "ds_read_b128 v[160:163], v234 offset:4096\n"
    "ds_read_b128 v[164:167], v235 offset:4096\n"
    "s_waitcnt lgkmcnt(8)\n"
    "s_nop 0\n"
    "v_readlane_b32 s20, v252, 0\n"
    "v_readlane_b32 s21, v252, 1\n"
    "v_readlane_b32 s22, v252, 2\n"
    "v_readlane_b32 s23, v252, 3\n"
    "v_readlane_b32 s24, v252, 4\n"
    "v_readlane_b32 s25, v252, 5\n"
    "v_readlane_b32 s26, v252, 6\n"
    "v_readlane_b32 s27, v252, 7\n"
    "v_pk_fma_f32 v[128:129], v[128:129], s[70:71], v[96:97]\n"
    "v_pk_fma_f32 v[130:131], v[130:131], s[70:71], v[98:99]\n"
    "v_pk_add_f32 v[230:231], v[128:129], v[130:131]\n"
    "v_pk_mul_f32 v[232:233], v[128:129], v[130:131]\n"
    "v_pk_fma_f32 v[132:133], v[132:133], s[70:71], v[100:101]\n"
    "v_pk_fma_f32 v[230:231], v[230:231], v[132:133], v[232:233]\n"
    "v_pk_mul_f32 v[232:233], v[232:233], v[132:133]\n"
    "v_pk_fma_f32 v[134:135], v[134:135], s[70:71], v[102:103]\n"
    "v_pk_fma_f32 v[230:231], v[230:231], v[134:135], v[232:233]\n"
    "v_pk_mul_f32 v[232:233], v[232:233], v[134:135]\n"
    "v_pk_fma_f32 v[136:137], v[136:137], s[70:71], v[104:105]\n"
    "v_pk_fma_f32 v[230:231], v[230:231], v[136:137], v[232:233]\n"
    "v_pk_mul_f32 v[232:233], v[232:233], v[136:137]\n"
    "v_pk_fma_f32 v[138:139], v[138:139], s[70:71], v[106:107]\n"
    "v_pk_fma_f32 v[230:231], v[230:231], v[138:139], v[232:233]\n"
    "v_pk_mul_f32 v[232:233], v[232:233], v[138:139]\n"
    "v_pk_fma_f32 v[140:141], v[140:141], s[70:71], v[108:109]\n"
    "v_pk_fma_f32 v[230:231], v[230:231], v[140:141], v[232:233]\n"
    "v_pk_mul_f32 v[232:233], v[232:233], v[140:141]\n"
    "v_pk_fma_f32 v[142:143], v[142:143], s[70:71], v[110:111]\n"
    "v_pk_fma_f32 v[230:231], v[230:231], v[142:143], v[232:233]\n"
    "v_pk_mul_f32 v[232:233], v[232:233], v[142:143]\n"
    "v_rcp_f32 v232, v232\n"
    "v_rcp_f32 v233, v233\n"
    "v_fma_f32 v230, v230, v232, v247\n"
    "v_fmac_f32 v230, v231, v233\n"
    "v_cvt_pk_bf16_f32 v230, v230, v230\n"
    "s_add_u32 s67, s67, 64\n"
    "s_waitcnt lgkmcnt(0)\n"
    "ds_write_b16 v245, v230\n"
    "ds_read_b64 v[228:229], v246\n"
    "s_lshl1_add_u32 s76, s72, s76\n"
    "buffer_load_dwordx4 v[236:237], s[40:43], 0 idxen offen offset:0 nt lds\n"
    "buffer_load_dwordx4 v[238:239], s[40:43], 0 idxen offen offset:1024 nt lds\n"
    "s_and_b32 s62, s58, 63\n"
    "s_mul_i32 s62, s62, 272\n"
    "s_mov_b32 s58, s59\n"
    "s_mov_b32 s59, s60\n"
    "s_mov_b32 s81, s65\n"
    "s_xor_b32 s65, s65, s64\n"
    "s_mov_b32 s80, s83\n"
    "s_add_u32 s82, s65, 256\n"
    "s_cmp_lt_i32 s59, s57\n"
    "s_cselect_b32 s76, s76, -1\n"
    "s_bitcmp1_b32 s58, 30\n"
    "s_cbranch_scc1 .Ldin_A1s\n"
    ".Ldin_A1:\n"
    "v_mfma_f32_16x16x32_bf16 v[128:131], v[0:3], v[160:163], v[112:115]\n"
    "v_exp_f32 v144, v144 clamp\n"
    "v_mfma_f32_16x16x32_bf16 v[128:131], v[4:7], v[164:167], v[128:131]\n"
    "v_exp_f32 v145, v145 clamp\n"
    "v_mfma_f32_16x16x32_bf16 v[132:135], v[8:11], v[160:163], v[116:119]\n"
    "v_exp_f32 v146, v146 clamp\n"
    "v_mfma_f32_16x16x32_bf16 v[132:135], v[12:15], v[164:167], v[132:135]\n"
    "v_exp_f32 v147, v147 clamp\n"
    "v_mfma_f32_16x16x32_bf16 v[136:139], v[16:19], v[160:163], v[120:123]\n"
    "v_exp_f32 v148, v148 clamp\n"
    "v_mfma_f32_16x16x32_bf16 v[136:139], v[20:23], v[164:167], v[136:139]\n"
    "v_exp_f32 v149, v149 clamp\n"
    "v_mfma_f32_16x16x32_bf16 v[140:143], v[24:27], v[160:163], v[124:127]\n"
    "v_exp_f32 v150, v150 clamp\n"
    "v_mfma_f32_16x16x32_bf16 v[140:143], v[28:31], v[164:167], v[140:143]\n"
    "v_exp_f32 v151, v151 clamp\n"
    "v_exp_f32 v152, v152 clamp\n"
    "s_add_u32 m0, s68, 4096\n"
    "v_exp_f32 v153, v153 clamp\n"
    "v_add_u32_e64 v253, s67, v244\n"
    "s_waitcnt lgkmcnt(0)\n"
    "v_mfma_f32_4x4x4_16b_bf16 v[224:227], v[228:229], v[168:169], v[224:227] cbsz:4 abid:0\n"
    "v_exp_f32 v154, v154 clamp\n"
    "v_exp_f32 v155, v155 clamp\n"
    "v_mfma_f32_4x4x4_16b_bf16 v[224:227], v[228:229], v[170:171], v[224:227] cbsz:4 abid:1\n"
    "v_exp_f32 v156, v156 clamp\n"
    "v_exp_f32 v157, v157 clamp\n"
    "v_mfma_f32_4x4x4_16b_bf16 v[224:227], v[228:229], v[172:173], v[224:227] cbsz:4 abid:2\n"
    "v_exp_f32 v158, v158 clamp\n"
    "v_exp_f32 v159, v159 clamp\n"
    "v_mfma_f32_4x4x4_16b_bf16 v[224:227], v[228:229], v[174:175], v[224:227] cbsz:4 abid:3\n"
    "s_waitcnt vmcnt(4)\n"
    "ds_read_b64_tr_b16 v[168:169], v240 offset:4096\n"
    "ds_read_b64_tr_b16 v[170:171], v241 offset:4352\n"
    "ds_read_b64_tr_b16 v[172:173], v242 offset:4608\n"
    "ds_read_b64_tr_b16 v[174:175], v243 offset:4864\n"
    "ds_read_b32 v236, v253 offset:0\n"
    "ds_read_b32 v238, v253 offset:4\n"
    "ds_read_b128 v[160:163], v234 offset:6144\n"
    "ds_read_b128 v[164:167], v235 offset:6144\n"
    "s_cbranch_vccz .Ldin_A1_np\n"
    "s_setprio 1\n"
    ".Ldin_A1_np:\n"
    "v_pk_fma_f32 v[144:145], v[144:145], s[70:71], v[96:97]\n"
    "v_pk_fma_f32 v[146:147], v[146:147], s[70:71], v[98:99]\n"
    "v_add_f32 v191, v224, v225\n"
    "v_add_f32 v190, v226, v227\n"
    "v_add_f32 v191, v191, v190\n"
    "v_add_u32 v190, s62, v249\n"
    "v_mul_f32 v191, s69, v191\n"
    "v_cvt_pk_bf16_f32 v191, v191, v191\n"
    "ds_write_b16 v190, v191 offset:32768\n"
    "s_bfe_u32 s94, s59, 0x180006\n"
    "s_mul_i32 s86, s94, s56\n"
    "v_mbcnt_lo_u32_b32 v184, s20, 0\n"
    "v_mbcnt_lo_u32_b32 v185, s22, 0\n"
    "v_mbcnt_lo_u32_b32 v186, s24, 0\n"
    "v_mbcnt_lo_u32_b32 v187, s26, 0\n"
    "s_bcnt1_i32_b64 s90, s[20:21]\n"
    "s_bcnt1_i32_b64 s91, s[22:23]\n"
    "s_bcnt1_i32_b64 s92, s[24:25]\n"
    "s_bcnt1_i32_b64 s93, s[26:27]\n"
    "v_mbcnt_hi_u32_b32 v184, s21, v184\n"
    "v_mbcnt_hi_u32_b32 v185, s23, v185\n"
    "v_mbcnt_hi_u32_b32 v186, s25, v186\n"
    "v_mbcnt_hi_u32_b32 v187, s27, v187\n"
    "s_lshl2_add_u32 s90, s90, s65\n"
    "s_lshl2_add_u32 s91, s91, s90\n"
    "s_lshl2_add_u32 s92, s92, s91\n"
    "s_lshl2_add_u32 s93, s93, s92\n"
    "v_add_u32_e64 v188, s86, v251\n"
    "v_pk_add_f32 v[230:231], v[144:145], v[146:147]\n"
    "v_pk_mul_f32 v[232:233], v[144:145], v[146:147]\n"
    "v_pk_fma_f32 v[148:149], v[148:149], s[70:71], v[100:101]\n"
    "v_pk_fma_f32 v[230:231], v[230:231], v[148:149], v[232:233]\n"
    "v_pk_mul_f32 v[232:233], v[232:233], v[148:149]\n"
    "v_lshl_add_u32 v184, v184, 2, s65\n"
    "v_lshl_add_u32 v185, v185, 2, s90\n"
    "v_lshl_add_u32 v186, v186, 2, s91\n"
    "v_lshl_add_u32 v187, v187, 2, s92\n"
    "v_mov_b32 v191, -1\n"
    "v_lshl_add_u32 v190, v249, 1, s93\n"
    "ds_write2_b32 v190, v191, v191 offset1:64\n"
    "s_mov_b64 exec, s[20:21]\n"
    "ds_write_b32 v184, v188\n"
    "s_mov_b64 exec, s[22:23]\n"
    "v_add_u32 v189, 64, v188\n"
    "ds_write_b32 v185, v189\n"
    "s_mov_b64 exec, s[24:25]\n"
    "v_add_u32 v190, 128, v188\n"
    "ds_write_b32 v186, v190\n"
    "s_mov_b64 exec, s[26:27]\n"
    "v_add_u32 v189, 192, v188\n"
    "ds_write_b32 v187, v189\n"
    "s_mov_b64 exec, -1\n"
    "s_add_u32 s95, s93, 60\n"
    "s_add_u32 s94, s82, 128\n"
    "s_andn2_b32 s95, s95, 63\n"
    "s_max_u32 s83, s95, s94\n"
    "v_pk_fma_f32 v[150:151], v[150:151], s[70:71], v[102:103]\n"
    "v_pk_fma_f32 v[230:231], v[230:231], v[150:151], v[232:233]\n"
    "v_pk_mul_f32 v[232:233], v[232:233], v[150:151]\n"
    "v_pk_fma_f32 v[152:153], v[152:153], s[70:71], v[104:105]\n"
    "v_pk_fma_f32 v[230:231], v[230:231], v[152:153], v[232:233]\n"
    "v_pk_mul_f32 v[232:233], v[232:233], v[152:153]\n"
    "v_pk_fma_f32 v[154:155], v[154:155], s[70:71], v[106:107]\n"
    "v_pk_fma_f32 v[230:231], v[230:231], v[154:155], v[232:233]\n"
    "v_pk_mul_f32 v[232:233], v[232:233], v[154:155]\n"
    "v_pk_fma_f32 v[156:157], v[156:157], s[70:71], v[108:109]\n"
    "v_pk_fma_f32 v[230:231], v[230:231], v[156:157], v[232:233]\n"
    "v_pk_mul_f32 v[232:233], v[232:233], v[156:157]\n"
    "v_pk_fma_f32 v[158:159], v[158:159], s[70:71], v[110:111]\n"
    "v_pk_fma_f32 v[230:231], v[230:231], v[158:159], v[232:233]\n"
    "v_pk_mul_f32 v[232:233], v[232:233], v[158:159]\n"
    "v_rcp_f32 v232, v232\n"
    "v_rcp_f32 v233, v233\n"
    "v_fma_f32 v230, v230, v232, v247\n"
    "v_fmac_f32 v230, v231, v233\n"
    "v_cvt_pk_bf16_f32 v230, v230, v230\n"
    "s_add_u32 s67, s67, 64\n"
    "s_waitcnt lgkmcnt(0)\n"
    "ds_write_b16 v245, v230\n"
    "ds_read_b64 v[228:229], v246\n"
    "buffer_load_dwordx4 v[236:237], s[40:43], 0 idxen offen offset:0 nt lds\n"
    "buffer_load_dwordx4 v[238:239], s[40:43], 0 idxen offen offset:1024 nt lds\n"
    ".Ldin_B2:\n"
    "v_mfma_f32_16x16x32_bf16 v[144:147], v[0:3], v[160:163], v[112:115]\n"
    "v_exp_f32 v128, v128 clamp\n"
    "v_mfma_f32_16x16x32_bf16 v[144:147], v[4:7], v[164:167], v[144:147]\n"
    "v_exp_f32 v129, v129 clamp\n"
    "v_mfma_f32_16x16x32_bf16 v[148:151], v[8:11], v[160:163], v[116:119]\n"
    "v_exp_f32 v130, v130 clamp\n"
    "v_mfma_f32_16x16x32_bf16 v[148:151], v[12:15], v[164:167], v[148:151]\n"
    "v_exp_f32 v131, v131 clamp\n"
    "v_mfma_f32_16x16x32_bf16 v[152:155], v[16:19], v[160:163], v[120:123]\n"
    "v_exp_f32 v132, v132 clamp\n"
    "v_mfma_f32_16x16x32_bf16 v[152:155], v[20:23], v[164:167], v[152:155]\n"
    "v_exp_f32 v133, v133 clamp\n"
    "v_mfma_f32_16x16x32_bf16 v[156:159], v[24:27], v[160:163], v[124:127]\n"
    "v_exp_f32 v134, v134 clamp\n"
    "v_mfma_f32_16x16x32_bf16 v[156:159], v[28:31], v[164:167], v[156:159]\n"
    "v_exp_f32 v135, v135 clamp\n"
    "v_exp_f32 v136, v136 clamp\n"
    "s_cmp_eq_u32 s67, s80\n"
    "s_cselect_b32 s67, s65, s67\n"
    "s_and_b32 s94, s59, 63\n"
    "s_mul_i32 s94, s94, 272\n"
    "v_add_u32 v252, s94, v250\n"
    "s_add_u32 m0, s68, 6144\n"
    "v_exp_f32 v137, v137 clamp\n"
    "v_add_u32 v253, s67, v244\n"
    "s_waitcnt lgkmcnt(0)\n"
    "v_mfma_f32_4x4x4_16b_bf16 v[224:227], v[228:229], v[176:177], 0 cbsz:4 abid:0\n"
    "v_exp_f32 v138, v138 clamp\n"
    "v_exp_f32 v139, v139 clamp\n"
    "v_mfma_f32_4x4x4_16b_bf16 v[224:227], v[228:229], v[178:179], v[224:227] cbsz:4 abid:1\n"
    "v_exp_f32 v140, v140 clamp\n"
    "v_exp_f32 v141, v141 clamp\n"
    "v_mfma_f32_4x4x4_16b_bf16 v[224:227], v[228:229], v[180:181], v[224:227] cbsz:4 abid:2\n"
    "v_exp_f32 v142, v142 clamp\n"
    "v_exp_f32 v143, v143 clamp\n"
    "v_mfma_f32_4x4x4_16b_bf16 v[224:227], v[228:229], v[182:183], v[224:227] cbsz:4 abid:3\n"
    "s_waitcnt vmcnt(4)\n"
    "s_memrealtime s[34:35]\n"
    "ds_read_b64_tr_b16 v[176:177], v240 offset:6144\n"
    "ds_read_b64_tr_b16 v[178:179], v241 offset:6400\n"
    "ds_read_b64_tr_b16 v[180:181], v242 offset:6656\n"
    "ds_read_b64_tr_b16 v[182:183], v243 offset:6912\n"
    "ds_read_b32 v236, v253 offset:0\n"
    "ds_read_b32 v238, v253 offset:4\n"
    "ds_read_b128 v[160:163], v234 offset:0\n"
    "ds_read_b128 v[164:167], v235 offset:0\n"
    "ds_read_b128 v[184:187], v252 offset:0\n"
    "ds_read_b128 v[188:191], v252 offset:16\n"
    "v_pk_fma_f32 v[128:129], v[128:129], s[70:71], v[96:97]\n"
    "v_pk_fma_f32 v[130:131], v[130:131], s[70:71], v[98:99]\n"
    "v_pk_add_f32 v[230:231], v[128:129], v[130:131]\n"
    "v_pk_mul_f32 v[232:233], v[128:129], v[130:131]\n"
    "v_pk_fma_f32 v[132:133], v[132:133], s[70:71], v[100:101]\n"
    "v_pk_fma_f32 v[230:231], v[230:231], v[132:133], v[232:233]\n"
    "v_pk_mul_f32 v[232:233], v[232:233], v[132:133]\n"
    "v_pk_fma_f32 v[134:135], v[134:135], s[70:71], v[102:103]\n"
    "v_pk_fma_f32 v[230:231], v[230:231], v[134:135], v[232:233]\n"
    "v_pk_mul_f32 v[232:233], v[232:233], v[134:135]\n"
    "v_pk_fma_f32 v[136:137], v[136:137], s[70:71], v[104:105]\n"
    "v_pk_fma_f32 v[230:231], v[230:231], v[136:137], v[232:233]\n"
    "v_pk_mul_f32 v[232:233], v[232:233], v[136:137]\n"
    "v_pk_fma_f32 v[138:139], v[138:139], s[70:71], v[106:107]\n"
    "v_pk_fma_f32 v[230:231], v[230:231], v[138:139], v[232:233]\n"
    "v_pk_mul_f32 v[232:233], v[232:233], v[138:139]\n"
    "v_pk_fma_f32 v[140:141], v[140:141], s[70:71], v[108:109]\n"
    "v_pk_fma_f32 v[230:231], v[230:231], v[140:141], v[232:233]\n"
    "v_pk_mul_f32 v[232:233], v[232:233], v[140:141]\n"
    "v_pk_fma_f32 v[142:143], v[142:143], s[70:71], v[110:111]\n"
    "v_pk_fma_f32 v[230:231], v[230:231], v[142:143], v[232:233]\n"
    "v_pk_mul_f32 v[232:233], v[232:233], v[142:143]\n"
    "v_rcp_f32 v232, v232\n"
    "v_rcp_f32 v233, v233\n"
    "v_fma_f32 v230, v230, v232, v247\n"
    "v_fmac_f32 v230, v231, v233\n"
    "v_cvt_pk_bf16_f32 v230, v230, v230\n"
    "s_add_u32 s67, s67, 64\n"
    "s_waitcnt lgkmcnt(0)\n"
    "ds_write_b16 v245, v230\n"
    "ds_read_b64 v[228:229], v246\n"
    "s_cmp_le_u32 s34, s76\n"
    "s_cbranch_scc1 .Ldin_B2_wait\n"
    ".Ldin_B2_go:\n"
    "s_sub_u32 s94, s34, 60\n"
    "s_max_u32 s76, s76, s94\n"
    "s_add_u32 s76, s76, s72\n"
    ".Ldin_B2_dma:\n"
    "buffer_load_dwordx4 v[236:237], s[40:43], 0 idxen offen offset:0 nt lds\n"
    "buffer_load_dwordx4 v[238:239], s[40:43], 0 idxen offen offset:1024 nt lds\n"
    ".Ldin_B2_nd:\n"
    "s_branch .Ldin_G3\n"
    ".Ldin_L0_poll:\n"
    "s_sleep 1\n"
    "ds_read_b64 v[232:233], v253\n"
    "s_waitcnt lgkmcnt(0)\n"
    "v_readfirstlane_b32 s60, v232\n"
    "s_cmp_eq_u32 s60, -1\n"
    "s_cbranch_scc1 .Ldin_L0_poll\n"
    "s_branch .Ldin_L0_pr\n"
    ".Ldin_B2_wait:\n"
    "s_cmp_lt_i32 s59, s57\n"
    "s_cbranch_scc1 .Ldin_B2_w2\n"
    "s_sub_u32 s94, s67, s65\n"
    "s_cmp_gt_u32 s94, 256\n"
    "s_cbranch_scc1 .Ldin_B2_dma\n"
    "s_cmp_eq_u32 s94, 64\n"
    "s_waitcnt vmcnt(2)\n"
    "s_cbranch_scc1 .Ldin_B2_nd\n"
    "s_waitcnt vmcnt(0)\n"
    "s_branch .Ldin_B2_nd\n"
    ".Ldin_B2_slp:\n"
    "s_sleep 1\n"
    ".Ldin_B2_w2:\n"
    "s_memrealtime s[34:35]\n"
    "s_waitcnt lgkmcnt(0)\n"
    "s_sub_u32 s94, s34, s76\n"
    "s_cmp_lt_i32 s94, 0\n"
    "s_cbranch_scc1 .Ldin_B2_slp\n"
    "s_branch .Ldin_B2_go\n"
    ".p2align 5\n"
    ".Ldin_L3:\n"
    "s_setprio 0\n"
    "s_cmp_ge_i32 s59, s57\n"
    "s_cbranch_scc1 .Ldin_E1\n"
    "s_add_u32 s76, s76, s73\n"
    "v_mov_b32 v253, s63\n"
    "v_and_b32 v184, v254, v184\n"
    "v_and_b32 v185, v255, v185\n"
    "v_and_b32 v186, v254, v186\n"
    "v_and_b32 v187, v255, v187\n"
    "v_and_b32 v188, v254, v188\n"
    "v_and_b32 v189, v255, v189\n"
    "v_and_b32 v190, v254, v190\n"
    "v_and_b32_e64 v191, v255, v191\n"
    "ds_read_b64 v[232:233], v253\n"
    "v_mfma_f32_4x4x4_16b_bf16 v[128:131], v[184:185], v[32:33], v[64:67]\n"
    "ds_read_b128 v[112:115], v252 offset:17408\n"
    "v_mfma_f32_4x4x4_16b_bf16 v[132:135], v[186:187], v[34:35], v[68:71]\n"
    "ds_read_b128 v[116:119], v252 offset:17424\n"
    "v_mfma_f32_4x4x4_16b_bf16 v[136:139], v[188:189], v[36:37], v[72:75]\n"
    "ds_read_b128 v[120:123], v252 offset:17536\n"
    "ds_read_b128 v[124:127], v252 offset:17552\n"
    "v_cvt_pk_bf16_f32 v0, v128, v129\n"
    "v_cvt_pk_bf16_f32 v1, v130, v131\n"
    "v_mfma_f32_4x4x4_16b_bf16 v[140:143], v[190:191], v[38:39], v[76:79]\n"
    "v_cvt_pk_bf16_f32 v2, v132, v133\n"
    "v_cvt_pk_bf16_f32 v3, v134, v135\n"
    "v_mfma_f32_4x4x4_16b_bf16 v[128:131], v[184:185], v[40:41], v[80:83]\n"
    "v_cvt_pk_bf16_f32 v4, v136, v137\n"
    "v_cvt_pk_bf16_f32 v5, v138, v139\n"
    "v_mfma_f32_4x4x4_16b_bf16 v[132:135], v[186:187], v[42:43], v[84:87]\n"
    "v_cvt_pk_bf16_f32 v6, v140, v141\n"
    "v_cvt_pk_bf16_f32 v7, v142, v143\n"
    "v_mfma_f32_4x4x4_16b_bf16 v[136:139], v[188:189], v[44:45], v[88:91]\n"
    "v_cvt_pk_bf16_f32 v8, v128, v129\n"
    "v_cvt_pk_bf16_f32 v9, v130, v131\n"
    "v_mfma_f32_4x4x4_16b_bf16 v[140:143], v[190:191], v[46:47], v[92:95]\n"
    "v_cvt_pk_bf16_f32 v10, v132, v133\n"
    "v_cvt_pk_bf16_f32 v11, v134, v135\n"
    "v_mfma_f32_4x4x4_16b_bf16 v[128:131], v[184:185], v[48:49], v[192:195]\n"
    "v_cvt_pk_bf16_f32 v12, v136, v137\n"
    "v_cvt_pk_bf16_f32 v13, v138, v139\n"
    "v_mfma_f32_4x4x4_16b_bf16 v[132:135], v[186:187], v[50:51], v[196:199]\n"
    "v_cvt_pk_bf16_f32 v14, v140, v141\n"
    "v_cvt_pk_bf16_f32 v15, v142, v143\n"
    "v_mfma_f32_4x4x4_16b_bf16 v[136:139], v[188:189], v[52:53], v[200:203]\n"
    "s_waitcnt lgkmcnt(0)\n"
    "v_readfirstlane_b32 s60, v232\n"
    "v_cvt_pk_bf16_f32 v16, v128, v129\n"
    "v_cvt_pk_bf16_f32 v17, v130, v131\n"
    "v_mfma_f32_4x4x4_16b_bf16 v[140:143], v[190:191], v[54:55], v[204:207]\n"
    "s_cmp_eq_u32 s60, -1\n"
    "s_cbranch_scc1 .Ldin_L3_poll\n"
    ".Ldin_L3_pr:\n"
    "v_readfirstlane_b32 s95, v233\n"
    "s_and_b32 s94, s60, 63\n"
    "s_lshl_b32 s94, s94, 6\n"
    "s_add_u32 s94, s94, 68480\n"
    "s_add_u32 s63, s63, 8\n"
    "v_lshl_add_u32 v252, v249, 1, s94\n"
    "ds_read_b32 v252, v252\n"
    "s_sub_u32 s95, s95, s76\n"
    "s_ashr_i32 s95, s95, 4\n"
    "s_min_i32 s95, s95, s74\n"
    "s_max_i32 s72, s95, s75\n"
    "v_cvt_pk_bf16_f32 v18, v132, v133\n"
    "v_cvt_pk_bf16_f32 v19, v134, v135\n"
    "v_mfma_f32_4x4x4_16b_bf16 v[128:131], v[184:185], v[56:57], v[208:211]\n"
    "v_cvt_pk_bf16_f32 v20, v136, v137\n"
    "v_cvt_pk_bf16_f32 v21, v138, v139\n"
    "v_mfma_f32_4x4x4_16b_bf16 v[132:135], v[186:187], v[58:59], v[212:215]\n"
    "v_cvt_pk_bf16_f32 v22, v140, v141\n"
    "v_cvt_pk_bf16_f32 v23, v142, v143\n"
    "v_mfma_f32_4x4x4_16b_bf16 v[136:139], v[188:189], v[60:61], v[216:219]\n"
    "v_cvt_pk_bf16_f32 v24, v128, v129\n"
    "v_cvt_pk_bf16_f32 v25, v130, v131\n"
    "v_mfma_f32_4x4x4_16b_bf16 v[140:143], v[190:191], v[62:63], v[220:223]\n"
    "v_cvt_pk_bf16_f32 v26, v132, v133\n"
    "v_cvt_pk_bf16_f32 v27, v134, v135\n"
    "v_cvt_pk_bf16_f32 v28, v136, v137\n"
    "v_cvt_pk_bf16_f32 v29, v138, v139\n"
    "v_exp_f32 v152, v152 clamp\n"
    "v_cvt_pk_bf16_f32 v30, v140, v141\n"
    "v_cvt_pk_bf16_f32 v31, v142, v143\n"
    "v_mfma_f32_16x16x32_bf16 v[128:131], v[0:3], v[160:163], v[112:115]\n"
    "v_exp_f32 v144, v144 clamp\n"
    "v_mfma_f32_16x16x32_bf16 v[128:131], v[4:7], v[164:167], v[128:131]\n"
    "v_exp_f32 v145, v145 clamp\n"
    "v_mfma_f32_16x16x32_bf16 v[132:135], v[8:11], v[160:163], v[116:119]\n"
    "v_exp_f32 v146, v146 clamp\n"
    "v_mfma_f32_16x16x32_bf16 v[132:135], v[12:15], v[164:167], v[132:135]\n"
    "v_exp_f32 v147, v147 clamp\n"
    "v_mfma_f32_16x16x32_bf16 v[136:139], v[16:19], v[160:163], v[120:123]\n"
    "v_exp_f32 v148, v148 clamp\n"
    "v_mfma_f32_16x16x32_bf16 v[136:139], v[20:23], v[164:167], v[136:139]\n"
    "v_exp_f32 v149, v149 clamp\n"
    "v_mfma_f32_16x16x32_bf16 v[140:143], v[24:27], v[160:163], v[124:127]\n"
    "v_exp_f32 v150, v150 clamp\n"
    "v_mfma_f32_16x16x32_bf16 v[140:143], v[28:31], v[164:167], v[140:143]\n"
    "v_exp_f32 v151, v151 clamp\n"
    "s_add_u32 m0, s68, 0\n"
    "v_exp_f32 v153, v153 clamp\n"
    "v_add_u32_e64 v253, s67, v244\n"
    "v_mfma_f32_4x4x4_16b_bf16 v[224:227], v[228:229], v[168:169], v[224:227] cbsz:4 abid:0\n"
    "v_exp_f32 v154, v154 clamp\n"
    "v_exp_f32 v155, v155 clamp\n"
    "v_mfma_f32_4x4x4_16b_bf16 v[224:227], v[228:229], v[170:171], v[224:227] cbsz:4 abid:1\n"
    "v_exp_f32 v156, v156 clamp\n"
    "v_exp_f32 v157, v157 clamp\n"
    "v_mfma_f32_4x4x4_16b_bf16 v[224:227], v[228:229], v[172:173], v[224:227] cbsz:4 abid:2\n"
    "v_exp_f32 v158, v158 clamp\n"
    "v_exp_f32 v159, v159 clamp\n"
    "v_mfma_f32_4x4x4_16b_bf16 v[224:227], v[228:229], v[174:175], v[224:227] cbsz:4 abid:3\n"
    "s_waitcnt vmcnt(4)\n"
    "ds_read_b64_tr_b16 v[168:169], v240 offset:0\n"
    "ds_read_b64_tr_b16 v[170:171], v241 offset:256\n"
    "ds_read_b64_tr_b16 v[172:173], v242 offset:512\n"
    "ds_read_b64_tr_b16 v[174:175], v243 offset:768\n"
    "ds_read_b32 v236, v253 offset:0\n"
    "ds_read_b32 v238, v253 offset:4\n"
    "ds_read_b128 v[160:163], v234 offset:2048\n"
    "ds_read_b128 v[164:167], v235 offset:2048\n"
    "s_waitcnt lgkmcnt(8)\n"
    "s_nop 0\n"
    "v_readlane_b32 s20, v252, 0\n"
    "v_readlane_b32 s21, v252, 1\n"
    "v_readlane_b32 s22, v252, 2\n"
    "v_readlane_b32 s23, v252, 3\n"
    "v_readlane_b32 s24, v252, 4\n"
    "v_readlane_b32 s25, v252, 5\n"
    "v_readlane_b32 s26, v252, 6\n"
    "v_readlane_b32 s27, v252, 7\n"
    "v_pk_fma_f32 v[144:145], v[144:145], s[70:71], v[96:97]\n"
    "v_pk_fma_f32 v[146:147], v[146:147], s[70:71], v[98:99]\n"
    "v_pk_add_f32 v[230:231], v[144:145], v[146:147]\n"
    "v_pk_mul_f32 v[232:233], v[144:145], v[146:147]\n"
    "v_pk_fma_f32 v[148:149], v[148:149], s[70:71], v[100:101]\n"
    "v_pk_fma_f32 v[230:231], v[230:231], v[148:149], v[232:233]\n"
    "v_pk_mul_f32 v[232:233], v[232:233], v[148:149]\n"
    "v_pk_fma_f32 v[150:151], v[150:151], s[70:71], v[102:103]\n"
    "v_pk_fma_f32 v[230:231], v[230:231], v[150:151], v[232:233]\n"
    "v_pk_mul_f32 v[232:233], v[232:233], v[150:151]\n"
    "v_pk_fma_f32 v[152:153], v[152:153], s[70:71], v[104:105]\n"
    "v_pk_fma_f32 v[230:231], v[230:231], v[152:153], v[232:233]\n"
    "v_pk_mul_f32 v[232:233], v[232:233], v[152:153]\n"
    "v_pk_fma_f32 v[154:155], v[154:155], s[70:71], v[106:107]\n"
    "v_pk_fma_f32 v[230:231], v[230:231], v[154:155], v[232:233]\n"
    "v_pk_mul_f32 v[232:233], v[232:233], v[154:155]\n"
    "v_pk_fma_f32 v[156:157], v[156:157], s[70:71], v[108:109]\n"
    "v_pk_fma_f32 v[230:231], v[230:231], v[156:157], v[232:233]\n"
    "v_pk_mul_f32 v[232:233], v[232:233], v[156:157]\n"
    "v_pk_fma_f32 v[158:159], v[158:159], s[70:71], v[110:111]\n"
    "v_pk_fma_f32 v[230:231], v[230:231], v[158:159], v[232:233]\n"
    "v_pk_mul_f32 v[232:233], v[232:233], v[158:159]\n"
    "v_rcp_f32 v232, v232\n"
    "v_rcp_f32 v233, v233\n"
    "v_fma_f32 v230, v230, v232, v247\n"
    "v_fmac_f32 v230, v231, v233\n"
    "v_cvt_pk_bf16_f32 v230, v230, v230\n"
    "s_add_u32 s67, s67, 64\n"
    "s_waitcnt lgkmcnt(0)\n"
    "ds_write_b16 v245, v230\n"
    "ds_read_b64 v[228:229], v246\n"
    "s_lshl1_add_u32 s76, s72, s76\n"
    "buffer_load_dwordx4 v[236:237], s[40:43], 0 idxen offen offset:0 nt lds\n"
    "buffer_load_dwordx4 v[238:239], s[40:43], 0 idxen offen offset:1024 nt lds\n"
    "s_and_b32 s62, s58, 63\n"
    "s_mul_i32 s62, s62, 272\n"
    "s_mov_b32 s58, s59\n"
    "s_mov_b32 s59, s60\n"
    "s_mov_b32 s81, s65\n"
    "s_xor_b32 s65, s65, s64\n"
    "s_mov_b32 s80, s83\n"
    "s_add_u32 s82, s65, 256\n"
    "s_cmp_lt_i32 s59, s57\n"
    "s_cselect_b32 s76, s76, -1\n"
    "s_bitcmp1_b32 s58, 30\n"
    "s_cbranch_scc1 .Ldin_A0s\n"
    "s_branch .Ldin_A0\n"
    ".Ldin_L3_poll:\n"
    "s_sleep 1\n"
    "ds_read_b64 v[232:233], v253\n"
    "s_waitcnt lgkmcnt(0)\n"
    "v_readfirstlane_b32 s60, v232\n"
    "s_cmp_eq_u32 s60, -1\n"
    "s_cbranch_scc1 .Ldin_L3_poll\n"
    "s_branch .Ldin_L3_pr\n"
    ".p2align 5\n"
    ".Ldin_E0:\n"
    "v_exp_f32 v128, v128 clamp\n"
    "v_exp_f32 v129, v129 clamp\n"
    "v_exp_f32 v130, v130 clamp\n"
    "v_exp_f32 v131, v131 clamp\n"
    "v_exp_f32 v132, v132 clamp\n"
    "v_exp_f32 v133, v133 clamp\n"
    "v_exp_f32 v134, v134 clamp\n"
    "v_exp_f32 v135, v135 clamp\n"
    "s_and_b32 s62, s58, 63\n"
    "s_mul_i32 s62, s62, 272\n"
    "s_waitcnt lgkmcnt(0)\n"
    "v_mfma_f32_4x4x4_16b_bf16 v[224:227], v[228:229], v[176:177], v[224:227] cbsz:4 abid:0\n"
    "v_exp_f32 v136, v136 clamp\n"
    "v_exp_f32 v137, v137 clamp\n"
    "v_mfma_f32_4x4x4_16b_bf16 v[224:227], v[228:229], v[178:179], v[224:227] cbsz:4 abid:1\n"
    "v_exp_f32 v138, v138 clamp\n"
    "v_exp_f32 v139, v139 clamp\n"
    "v_mfma_f32_4x4x4_16b_bf16 v[224:227], v[228:229], v[180:181], v[224:227] cbsz:4 abid:2\n"
    "v_exp_f32 v140, v140 clamp\n"
    "v_exp_f32 v141, v141 clamp\n"
    "v_mfma_f32_4x4x4_16b_bf16 v[224:227], v[228:229], v[182:183], v[224:227] cbsz:4 abid:3\n"
    "v_exp_f32 v142, v142 clamp\n"
    "v_exp_f32 v143, v143 clamp\n"
    "v_pk_fma_f32 v[128:129], v[128:129], s[70:71], v[96:97]\n"
    "v_pk_fma_f32 v[130:131], v[130:131], s[70:71], v[98:99]\n"
    "v_pk_add_f32 v[230:231], v[128:129], v[130:131]\n"
    "v_pk_mul_f32 v[232:233], v[128:129], v[130:131]\n"
    "v_pk_fma_f32 v[132:133], v[132:133], s[70:71], v[100:101]\n"
    "v_pk_fma_f32 v[230:231], v[230:231], v[132:133], v[232:233]\n"
    "v_pk_mul_f32 v[232:233], v[232:233], v[132:133]\n"
    "v_pk_fma_f32 v[134:135], v[134:135], s[70:71], v[102:103]\n"
    "v_pk_fma_f32 v[230:231], v[230:231], v[134:135], v[232:233]\n"
    "v_pk_mul_f32 v[232:233], v[232:233], v[134:135]\n"
    "v_pk_fma_f32 v[136:137], v[136:137], s[70:71], v[104:105]\n"
    "v_pk_fma_f32 v[230:231], v[230:231], v[136:137], v[232:233]\n"
    "v_pk_mul_f32 v[232:233], v[232:233], v[136:137]\n"
    "v_pk_fma_f32 v[138:139], v[138:139], s[70:71], v[106:107]\n"
    "v_pk_fma_f32 v[230:231], v[230:231], v[138:139], v[232:233]\n"
    "v_pk_mul_f32 v[232:233], v[232:233], v[138:139]\n"
    "v_pk_fma_f32 v[140:141], v[140:141], s[70:71], v[108:109]\n"
    "v_pk_fma_f32 v[230:231], v[230:231], v[140:141], v[232:233]\n"
    "v_pk_mul_f32 v[232:233], v[232:233], v[140:141]\n"
    "v_pk_fma_f32 v[142:143], v[142:143], s[70:71], v[110:111]\n"
    "v_pk_fma_f32 v[230:231], v[230:231], v[142:143], v[232:233]\n"
    "v_pk_mul_f32 v[232:233], v[232:233], v[142:143]\n"
    "v_rcp_f32 v232, v232\n"
    "v_rcp_f32 v233, v233\n"
    "v_fma_f32 v230, v230, v232, v247\n"
    "v_fmac_f32 v230, v231, v233\n"
    "v_cvt_pk_bf16_f32 v230, v230, v230\n"
    "ds_write_b16 v245, v230\n"
    "ds_read_b64 v[228:229], v246\n"
    ".Ldin_drain0:\n"
    "v_add_u32 v253, s66, v244\n"
    "ds_read_b32 v253, v253\n"
    "v_lshlrev_b32 v202, 3, v249\n"
    "s_waitcnt lgkmcnt(0)\n"
    "v_mfma_f32_4x4x4_16b_bf16 v[224:227], v[228:229], v[168:169], v[224:227] cbsz:4 abid:0\n"
    "v_and_b32_e64 v201, 63, v253\n"
    "v_and_b32 v202, 0x70, v202\n"
    "v_mfma_f32_4x4x4_16b_bf16 v[224:227], v[228:229], v[170:171], v[224:227] cbsz:4 abid:1\n"
    "v_and_b32 v200, 0x3fffffc0, v253\n"
    "v_mul_u32_u24 v201, 272, v201\n"
    "v_mfma_f32_4x4x4_16b_bf16 v[224:227], v[228:229], v[172:173], v[224:227] cbsz:4 abid:2\n"
    "v_lshl_add_u32 v200, v200, 1, v202\n"
    "v_add_u32 v201, v201, v202\n"
    "v_mfma_f32_4x4x4_16b_bf16 v[224:227], v[228:229], v[174:175], v[224:227] cbsz:4 abid:3\n"
    "v_cmp_gt_u32 vcc, s57, v253\n"
    "v_bfrev_b32 v202, 1\n"
    "s_nop 0\n"
    "v_cndmask_b32 v200, v202, v200, vcc\n"
    "s_nop 0\n"
    "v_add_f32 v232, v224, v225\n"
    "v_add_f32 v233, v226, v227\n"
    "v_add_f32 v232, v232, v233\n"
    "v_add_u32 v233, s62, v249\n"
    "v_mul_f32 v232, s69, v232\n"
    "v_cvt_pk_bf16_f32 v232, v232, v232\n"
    "ds_write_b16 v233, v232 offset:32768\n"
    "ds_read_b128 v[216:219], v201 offset:32768\n"
    "s_waitcnt vmcnt(0) lgkmcnt(0)\n"
    "buffer_store_dwordx4 v[216:219], v200, s[52:55], 0 offen sc0 sc1 nt\n"
    "s_branch .Ldin_exit\n"
    ".p2align 5\n"
    ".Ldin_E1:\n"
    "v_exp_f32 v144, v144 clamp\n"
    "v_exp_f32 v145, v145 clamp\n"
    "v_exp_f32 v146, v146 clamp\n"
    "v_exp_f32 v147, v147 clamp\n"
    "v_exp_f32 v148, v148 clamp\n"
    "v_exp_f32 v149, v149 clamp\n"
    "v_exp_f32 v150, v150 clamp\n"
    "v_exp_f32 v151, v151 clamp\n"
    "s_and_b32 s62, s58, 63\n"
    "s_mul_i32 s62, s62, 272\n"
    "s_waitcnt lgkmcnt(0)\n"
    "v_mfma_f32_4x4x4_16b_bf16 v[224:227], v[228:229], v[168:169], v[224:227] cbsz:4 abid:0\n"
    "v_exp_f32 v152, v152 clamp\n"
    "v_exp_f32 v153, v153 clamp\n"
    "v_mfma_f32_4x4x4_16b_bf16 v[224:227], v[228:229], v[170:171], v[224:227] cbsz:4 abid:1\n"
    "v_exp_f32 v154, v154 clamp\n"
    "v_exp_f32 v155, v155 clamp\n"
    "v_mfma_f32_4x4x4_16b_bf16 v[224:227], v[228:229], v[172:173], v[224:227] cbsz:4 abid:2\n"
    "v_exp_f32 v156, v156 clamp\n"
    "v_exp_f32 v157, v157 clamp\n"
    "v_mfma_f32_4x4x4_16b_bf16 v[224:227], v[228:229], v[174:175], v[224:227] cbsz:4 abid:3\n"
    "v_exp_f32 v158, v158 clamp\n"
    "v_exp_f32 v159, v159 clamp\n"
    "v_pk_fma_f32 v[144:145], v[144:145], s[70:71], v[96:97]\n"
    "v_pk_fma_f32 v[146:147], v[146:147], s[70:71], v[98:99]\n"
    "v_pk_add_f32 v[230:231], v[144:145], v[146:147]\n"
    "v_pk_mul_f32 v[232:233], v[144:145], v[146:147]\n"
    "v_pk_fma_f32 v[148:149], v[148:149], s[70:71], v[100:101]\n"
    "v_pk_fma_f32 v[230:231], v[230:231], v[148:149], v[232:233]\n"
    "v_pk_mul_f32 v[232:233], v[232:233], v[148:149]\n"
    "v_pk_fma_f32 v[150:151], v[150:151], s[70:71], v[102:103]\n"
    "v_pk_fma_f32 v[230:231], v[230:231], v[150:151], v[232:233]\n"
    "v_pk_mul_f32 v[232:233], v[232:233], v[150:151]\n"
    "v_pk_fma_f32 v[152:153], v[152:153], s[70:71], v[104:105]\n"
    "v_pk_fma_f32 v[230:231], v[230:231], v[152:153], v[232:233]\n"
    "v_pk_mul_f32 v[232:233], v[232:233], v[152:153]\n"
    "v_pk_fma_f32 v[154:155], v[154:155], s[70:71], v[106:107]\n"
    "v_pk_fma_f32 v[230:231], v[230:231], v[154:155], v[232:233]\n"
    "v_pk_mul_f32 v[232:233], v[232:233], v[154:155]\n"
    "v_pk_fma_f32 v[156:157], v[156:157], s[70:71], v[108:109]\n"
    "v_pk_fma_f32 v[230:231], v[230:231], v[156:157], v[232:233]\n"
    "v_pk_mul_f32 v[232:233], v[232:233], v[156:157]\n"
    "v_pk_fma_f32 v[158:159], v[158:159], s[70:71], v[110:111]\n"
    "v_pk_fma_f32 v[230:231], v[230:231], v[158:159], v[232:233]\n"
    "v_pk_mul_f32 v[232:233], v[232:233], v[158:159]\n"
    "v_rcp_f32 v232, v232\n"
    "v_rcp_f32 v233, v233\n"
    "v_fma_f32 v230, v230, v232, v247\n"
    "v_fmac_f32 v230, v231, v233\n"
    "v_cvt_pk_bf16_f32 v230, v230, v230\n"
    "ds_write_b16 v245, v230\n"
    "ds_read_b64 v[228:229], v246\n"
    ".Ldin_drain1:\n"
    "v_add_u32 v253, s66, v244\n"
    "ds_read_b32 v253, v253\n"
    "v_lshlrev_b32 v202, 3, v249\n"
    "s_waitcnt lgkmcnt(0)\n"
    "v_mfma_f32_4x4x4_16b_bf16 v[224:227], v[228:229], v[176:177], v[224:227] cbsz:4 abid:0\n"
    "v_and_b32_e64 v201, 63, v253\n"
    "v_and_b32 v202, 0x70, v202\n"
    "v_mfma_f32_4x4x4_16b_bf16 v[224:227], v[228:229], v[178:179], v[224:227] cbsz:4 abid:1\n"
    "v_and_b32 v200, 0x3fffffc0, v253\n"
    "v_mul_u32_u24 v201, 272, v201\n"
    "v_mfma_f32_4x4x4_16b_bf16 v[224:227], v[228:229], v[180:181], v[224:227] cbsz:4 abid:2\n"
    "v_lshl_add_u32 v200, v200, 1, v202\n"
    "v_add_u32 v201, v201, v202\n"
    "v_mfma_f32_4x4x4_16b_bf16 v[224:227], v[228:229], v[182:183], v[224:227] cbsz:4 abid:3\n"
    "v_cmp_gt_u32 vcc, s57, v253\n"
    "v_bfrev_b32 v202, 1\n"
    "s_nop 0\n"
    "v_cndmask_b32 v200, v202, v200, vcc\n"
    "s_nop 0\n"
    "v_add_f32 v232, v224, v225\n"
    "v_add_f32 v233, v226, v227\n"
    "v_add_f32 v232, v232, v233\n"
    "v_add_u32 v233, s62, v249\n"
    "v_mul_f32 v232, s69, v232\n"
    "v_cvt_pk_bf16_f32 v232, v232, v232\n"
    "ds_write_b16 v233, v232 offset:32768\n"
    "ds_read_b128 v[216:219], v201 offset:32768\n"
    "s_waitcnt vmcnt(0) lgkmcnt(0)\n"
    "buffer_store_dwordx4 v[216:219], v200, s[52:55], 0 offen sc0 sc1 nt\n"
    "s_branch .Ldin_exit\n"
    ".Ldin_A0s:\n"
    "v_mfma_f32_16x16x32_bf16 v[144:147], v[0:3], v[160:163], v[112:115]\n"
    "v_exp_f32 v128, v128 clamp\n"
    "v_mfma_f32_16x16x32_bf16 v[144:147], v[4:7], v[164:167], v[144:147]\n"
    "v_exp_f32 v129, v129 clamp\n"
    "v_mfma_f32_16x16x32_bf16 v[148:151], v[8:11], v[160:163], v[116:119]\n"
    "v_exp_f32 v130, v130 clamp\n"
    "v_mfma_f32_16x16x32_bf16 v[148:151], v[12:15], v[164:167], v[148:151]\n"
    "v_exp_f32 v131, v131 clamp\n"
    "v_mfma_f32_16x16x32_bf16 v[152:155], v[16:19], v[160:163], v[120:123]\n"
    "v_exp_f32 v132, v132 clamp\n"
    "v_mfma_f32_16x16x32_bf16 v[152:155], v[20:23], v[164:167], v[152:155]\n"
    "v_exp_f32 v133, v133 clamp\n"
    "v_mfma_f32_16x16x32_bf16 v[156:159], v[24:27], v[160:163], v[124:127]\n"
    "v_exp_f32 v134, v134 clamp\n"
    "v_mfma_f32_16x16x32_bf16 v[156:159], v[28:31], v[164:167], v[156:159]\n"
    "v_exp_f32 v135, v135 clamp\n"
    "v_exp_f32 v136, v136 clamp\n"
    "s_add_u32 m0, s68, 2048\n"
    "v_exp_f32 v137, v137 clamp\n"
    "v_add_u32 v253, s67, v244\n"
    "s_waitcnt lgkmcnt(0)\n"
    "v_mfma_f32_4x4x4_16b_bf16 v[224:227], v[228:229], v[176:177], v[224:227] cbsz:4 abid:0\n"
    "v_exp_f32 v138, v138 clamp\n"
    "v_exp_f32 v139, v139 clamp\n"
    "v_mfma_f32_4x4x4_16b_bf16 v[224:227], v[228:229], v[178:179], v[224:227] cbsz:4 abid:1\n"
    "v_exp_f32 v140, v140 clamp\n"
    "v_exp_f32 v141, v141 clamp\n"
    "v_mfma_f32_4x4x4_16b_bf16 v[224:227], v[228:229], v[180:181], v[224:227] cbsz:4 abid:2\n"
    "v_exp_f32 v142, v142 clamp\n"
    "v_exp_f32 v143, v143 clamp\n"
    "v_mfma_f32_4x4x4_16b_bf16 v[224:227], v[228:229], v[182:183], v[224:227] cbsz:4 abid:3\n"
    "s_waitcnt vmcnt(4)\n"
    "ds_read_b64_tr_b16 v[176:177], v240 offset:2048\n"
    "ds_read_b64_tr_b16 v[178:179], v241 offset:2304\n"
    "ds_read_b64_tr_b16 v[180:181], v242 offset:2560\n"
    "ds_read_b64_tr_b16 v[182:183], v243 offset:2816\n"
    "ds_read_b32 v236, v253 offset:0\n"
    "ds_read_b32 v238, v253 offset:4\n"
    "ds_read_b128 v[160:163], v234 offset:4096\n"
    "ds_read_b128 v[164:167], v235 offset:4096\n"
    "s_cbranch_vccz .Ldin_A0s_np\n"
    "s_setprio 1\n"
    ".Ldin_A0s_np:\n"
    "v_pk_fma_f32 v[128:129], v[128:129], s[70:71], v[96:97]\n"
    "v_pk_fma_f32 v[130:131], v[130:131], s[70:71], v[98:99]\n"
    "v_add_f32 v191, v224, v225\n"
    "v_add_f32 v190, v226, v227\n"
    "v_add_f32 v191, v191, v190\n"
    "v_add_u32 v190, s62, v249\n"
    "v_mul_f32 v191, s69, v191\n"
    "v_cvt_pk_bf16_f32 v191, v191, v191\n"
    "ds_write_b16 v190, v191 offset:32768\n"
    "s_bfe_u32 s94, s59, 0x180006\n"
    "s_mul_i32 s86, s94, s56\n"
    "v_mbcnt_lo_u32_b32 v184, s20, 0\n"
    "v_mbcnt_lo_u32_b32 v185, s22, 0\n"
    "v_mbcnt_lo_u32_b32 v186, s24, 0\n"
    "v_mbcnt_lo_u32_b32 v187, s26, 0\n"
    "s_bcnt1_i32_b64 s90, s[20:21]\n"
    "s_bcnt1_i32_b64 s91, s[22:23]\n"
    "s_bcnt1_i32_b64 s92, s[24:25]\n"
    "s_bcnt1_i32_b64 s93, s[26:27]\n"
    "v_mbcnt_hi_u32_b32 v184, s21, v184\n"
    "v_mbcnt_hi_u32_b32 v185, s23, v185\n"
    "v_mbcnt_hi_u32_b32 v186, s25, v186\n"
    "v_mbcnt_hi_u32_b32 v187, s27, v187\n"
    "s_lshl2_add_u32 s90, s90, s65\n"
    "s_lshl2_add_u32 s91, s91, s90\n"
    "s_lshl2_add_u32 s92, s92, s91\n"
    "s_lshl2_add_u32 s93, s93, s92\n"
    "v_add_u32_e64 v188, s86, v251\n"
    "v_pk_add_f32 v[230:231], v[128:129], v[130:131]\n"
    "v_pk_mul_f32 v[232:233], v[128:129], v[130:131]\n"
    "v_pk_fma_f32 v[132:133], v[132:133], s[70:71], v[100:101]\n"
    "v_pk_fma_f32 v[230:231], v[230:231], v[132:133], v[232:233]\n"
    "v_pk_mul_f32 v[232:233], v[232:233], v[132:133]\n"
    "v_lshl_add_u32 v184, v184, 2, s65\n"
    "v_lshl_add_u32 v185, v185, 2, s90\n"
    "v_lshl_add_u32 v186, v186, 2, s91\n"
    "v_lshl_add_u32 v187, v187, 2, s92\n"
    "v_mov_b32 v191, -1\n"
    "v_lshl_add_u32 v190, v249, 1, s93\n"
    "ds_write2_b32 v190, v191, v191 offset1:64\n"
    "s_mov_b64 exec, s[20:21]\n"
    "ds_write_b32 v184, v188\n"
    "s_mov_b64 exec, s[22:23]\n"
    "v_add_u32 v189, 64, v188\n"
    "ds_write_b32 v185, v189\n"
    "s_mov_b64 exec, s[24:25]\n"
    "v_add_u32 v190, 128, v188\n"
    "ds_write_b32 v186, v190\n"
    "s_mov_b64 exec, s[26:27]\n"
    "v_add_u32 v189, 192, v188\n"
    "ds_write_b32 v187, v189\n"
    "s_mov_b64 exec, -1\n"
    "s_add_u32 s95, s93, 60\n"
    "s_add_u32 s94, s82, 128\n"
    "s_andn2_b32 s95, s95, 63\n"
    "s_max_u32 s83, s95, s94\n"
    "v_pk_fma_f32 v[134:135], v[134:135], s[70:71], v[102:103]\n"
    "v_pk_fma_f32 v[230:231], v[230:231], v[134:135], v[232:233]\n"
    "v_pk_mul_f32 v[232:233], v[232:233], v[134:135]\n"
    "v_pk_fma_f32 v[136:137], v[136:137], s[70:71], v[104:105]\n"
    "v_pk_fma_f32 v[230:231], v[230:231], v[136:137], v[232:233]\n"
    "v_pk_mul_f32 v[232:233], v[232:233], v[136:137]\n"
    "v_pk_fma_f32 v[138:139], v[138:139], s[70:71], v[106:107]\n"
    "v_pk_fma_f32 v[230:231], v[230:231], v[138:139], v[232:233]\n"
    "v_pk_mul_f32 v[232:233], v[232:233], v[138:139]\n"
    "v_pk_fma_f32 v[140:141], v[140:141], s[70:71], v[108:109]\n"
    "v_pk_fma_f32 v[230:231], v[230:231], v[140:141], v[232:233]\n"
    "v_pk_mul_f32 v[232:233], v[232:233], v[140:141]\n"
    "v_pk_fma_f32 v[142:143], v[142:143], s[70:71], v[110:111]\n"
    "v_pk_fma_f32 v[230:231], v[230:231], v[142:143], v[232:233]\n"
    "v_pk_mul_f32 v[232:233], v[232:233], v[142:143]\n"
    "v_rcp_f32 v232, v232\n"
    "v_rcp_f32 v233, v233\n"
    "v_fma_f32 v230, v230, v232, v247\n"
    "v_fmac_f32 v230, v231, v233\n"
    "v_lshlrev_b32 v232, 2, v249\n"
    "v_lshrrev_b32 v233, 2, v249\n"
    "v_and_b32 v232, 56, v232\n"
    "v_and_b32 v233, 4, v233\n"
    "v_add3_u32 v232, v232, v233, s81\n"
    "ds_read_b32 v232, v232\n"
    "s_add_u32 s81, s81, 64\n"
    "s_waitcnt lgkmcnt(0)\n"
    "v_lshlrev_b32 v232, 1, v232\n"
    "buffer_load_ushort v232, v232, s[44:47], 0 offen\n"
    "s_waitcnt vmcnt(0)\n"
    "v_lshlrev_b32 v232, 16, v232\n"
    "v_mul_f32 v230, v230, v232\n"
    "v_cvt_pk_bf16_f32 v230, v230, v230\n"
    "s_add_u32 s67, s67, 64\n"
    "s_waitcnt lgkmcnt(0)\n"
    "ds_write_b16 v245, v230\n"
    "ds_read_b64 v[228:229], v246\n"
    "buffer_load_dwordx4 v[236:237], s[40:43], 0 idxen offen offset:0 nt lds\n"
    "buffer_load_dwordx4 v[238:239], s[40:43], 0 idxen offen offset:1024 nt lds\n"
    ".Ldin_B1s:\n"
    "v_mfma_f32_16x16x32_bf16 v[128:131], v[0:3], v[160:163], v[112:115]\n"
    "v_exp_f32 v144, v144 clamp\n"
    "v_mfma_f32_16x16x32_bf16 v[128:131], v[4:7], v[164:167], v[128:131]\n"
    "v_exp_f32 v145, v145 clamp\n"
    "v_mfma_f32_16x16x32_bf16 v[132:135], v[8:11], v[160:163], v[116:119]\n"
    "v_exp_f32 v146, v146 clamp\n"
    "v_mfma_f32_16x16x32_bf16 v[132:135], v[12:15], v[164:167], v[132:135]\n"
    "v_exp_f32 v147, v147 clamp\n"
    "v_mfma_f32_16x16x32_bf16 v[136:139], v[16:19], v[160:163], v[120:123]\n"
    "v_exp_f32 v148, v148 clamp\n"
    "v_mfma_f32_16x16x32_bf16 v[136:139], v[20:23], v[164:167], v[136:139]\n"
    "v_exp_f32 v149, v149 clamp\n"
    "v_mfma_f32_16x16x32_bf16 v[140:143], v[24:27], v[160:163], v[124:127]\n"
    "v_exp_f32 v150, v150 clamp\n"
    "v_mfma_f32_16x16x32_bf16 v[140:143], v[28:31], v[164:167], v[140:143]\n"
    "v_exp_f32 v151, v151 clamp\n"
    "v_exp_f32 v152, v152 clamp\n"
    "s_cmp_eq_u32 s67, s80\n"
    "s_cselect_b32 s67, s65, s67\n"
    "s_and_b32 s94, s59, 63\n"
    "s_mul_i32 s94, s94, 272\n"
    "v_add_u32 v252, s94, v250\n"
    "s_add_u32 m0, s68, 4096\n"
    "v_exp_f32 v153, v153 clamp\n"
    "v_add_u32 v253, s67, v244\n"
    "s_waitcnt lgkmcnt(0)\n"
    "v_mfma_f32_4x4x4_16b_bf16 v[224:227], v[228:229], v[168:169], 0 cbsz:4 abid:0\n"
    "v_exp_f32 v154, v154 clamp\n"
    "v_exp_f32 v155, v155 clamp\n"
    "v_mfma_f32_4x4x4_16b_bf16 v[224:227], v[228:229], v[170:171], v[224:227] cbsz:4 abid:1\n"
    "v_exp_f32 v156, v156 clamp\n"
    "v_exp_f32 v157, v157 clamp\n"
    "v_mfma_f32_4x4x4_16b_bf16 v[224:227], v[228:229], v[172:173], v[224:227] cbsz:4 abid:2\n"
    "v_exp_f32 v158, v158 clamp\n"
    "v_exp_f32 v159, v159 clamp\n"
    "v_mfma_f32_4x4x4_16b_bf16 v[224:227], v[228:229], v[174:175], v[224:227] cbsz:4 abid:3\n"
    "s_waitcnt vmcnt(4)\n"
    "s_memrealtime s[34:35]\n"
    "ds_read_b64_tr_b16 v[168:169], v240 offset:4096\n"
    "ds_read_b64_tr_b16 v[170:171], v241 offset:4352\n"
    "ds_read_b64_tr_b16 v[172:173], v242 offset:4608\n"
    "ds_read_b64_tr_b16 v[174:175], v243 offset:4864\n"
    "ds_read_b32 v236, v253 offset:0\n"
    "ds_read_b32 v238, v253 offset:4\n"
    "ds_read_b128 v[160:163], v234 offset:6144\n"
    "ds_read_b128 v[164:167], v235 offset:6144\n"
    "ds_read_b128 v[184:187], v252 offset:0\n"
    "ds_read_b128 v[188:191], v252 offset:16\n"
    "v_pk_fma_f32 v[144:145], v[144:145], s[70:71], v[96:97]\n"
    "v_pk_fma_f32 v[146:147], v[146:147], s[70:71], v[98:99]\n"
    "v_pk_add_f32 v[230:231], v[144:145], v[146:147]\n"
    "v_pk_mul_f32 v[232:233], v[144:145], v[146:147]\n"
    "v_pk_fma_f32 v[148:149], v[148:149], s[70:71], v[100:101]\n"
    "v_pk_fma_f32 v[230:231], v[230:231], v[148:149], v[232:233]\n"
    "v_pk_mul_f32 v[232:233], v[232:233], v[148:149]\n"
    "v_pk_fma_f32 v[150:151], v[150:151], s[70:71], v[102:103]\n"
    "v_pk_fma_f32 v[230:231], v[230:231], v[150:151], v[232:233]\n"
    "v_pk_mul_f32 v[232:233], v[232:233], v[150:151]\n"
    "v_pk_fma_f32 v[152:153], v[152:153], s[70:71], v[104:105]\n"
    "v_pk_fma_f32 v[230:231], v[230:231], v[152:153], v[232:233]\n"
    "v_pk_mul_f32 v[232:233], v[232:233], v[152:153]\n"
    "v_pk_fma_f32 v[154:155], v[154:155], s[70:71], v[106:107]\n"
    "v_pk_fma_f32 v[230:231], v[230:231], v[154:155], v[232:233]\n"
    "v_pk_mul_f32 v[232:233], v[232:233], v[154:155]\n"
    "v_pk_fma_f32 v[156:157], v[156:157], s[70:71], v[108:109]\n"
    "v_pk_fma_f32 v[230:231], v[230:231], v[156:157], v[232:233]\n"
    "v_pk_mul_f32 v[232:233], v[232:233], v[156:157]\n"
    "v_pk_fma_f32 v[158:159], v[158:159], s[70:71], v[110:111]\n"
    "v_pk_fma_f32 v[230:231], v[230:231], v[158:159], v[232:233]\n"
    "v_pk_mul_f32 v[232:233], v[232:233], v[158:159]\n"
    "v_rcp_f32 v232, v232\n"
    "v_rcp_f32 v233, v233\n"
    "v_fma_f32 v230, v230, v232, v247\n"
    "v_fmac_f32 v230, v231, v233\n"
    "v_lshlrev_b32 v232, 2, v249\n"
    "v_lshrrev_b32 v233, 2, v249\n"
    "v_and_b32 v232, 56, v232\n"
    "v_and_b32 v233, 4, v233\n"
    "v_add3_u32 v232, v232, v233, s81\n"
    "ds_read_b32 v232, v232\n"
    "s_add_u32 s81, s81, 64\n"
    "s_waitcnt lgkmcnt(0)\n"
    "v_lshlrev_b32 v232, 1, v232\n"
    "buffer_load_ushort v232, v232, s[44:47], 0 offen\n"
    "s_waitcnt vmcnt(0)\n"
    "v_lshlrev_b32 v232, 16, v232\n"
    "v_mul_f32 v230, v230, v232\n"
    "v_cvt_pk_bf16_f32 v230, v230, v230\n"
    "s_add_u32 s67, s67, 64\n"
    "s_waitcnt lgkmcnt(0)\n"
    "ds_write_b16 v245, v230\n"
    "ds_read_b64 v[228:229], v246\n"
    "s_cmp_le_u32 s34, s76\n"
    "s_cbranch_scc1 .Ldin_B1s_wait\n"
    ".Ldin_B1s_go:\n"
    "s_sub_u32 s94, s34, 60\n"
    "s_max_u32 s76, s76, s94\n"
    "s_add_u32 s76, s76, s72\n"
    ".Ldin_B1s_dma:\n"
    "buffer_load_dwordx4 v[236:237], s[40:43], 0 idxen offen offset:0 nt lds\n"
    "buffer_load_dwordx4 v[238:239], s[40:43], 0 idxen offen offset:1024 nt lds\n"
    ".Ldin_B1s_nd:\n"
    ".Ldin_G2s:\n"
    "v_mfma_f32_16x16x32_bf16 v[144:147], v[0:3], v[160:163], v[112:115]\n"
    "v_exp_f32 v128, v128 clamp\n"
    "v_mfma_f32_16x16x32_bf16 v[144:147], v[4:7], v[164:167], v[144:147]\n"
    "v_exp_f32 v129, v129 clamp\n"
    "v_mfma_f32_16x16x32_bf16 v[148:151], v[8:11], v[160:163], v[116:119]\n"
    "v_exp_f32 v130, v130 clamp\n"
    "v_mfma_f32_16x16x32_bf16 v[148:151], v[12:15], v[164:167], v[148:151]\n"
    "v_exp_f32 v131, v131 clamp\n"
    "v_mfma_f32_16x16x32_bf16 v[152:155], v[16:19], v[160:163], v[120:123]\n"
    "v_exp_f32 v132, v132 clamp\n"
    "v_mfma_f32_16x16x32_bf16 v[152:155], v[20:23], v[164:167], v[152:155]\n"
    "v_exp_f32 v133, v133 clamp\n"
    "v_mfma_f32_16x16x32_bf16 v[156:159], v[24:27], v[160:163], v[124:127]\n"
    "v_exp_f32 v134, v134 clamp\n"
    "v_mfma_f32_16x16x32_bf16 v[156:159], v[28:31], v[164:167], v[156:159]\n"
    "v_exp_f32 v135, v135 clamp\n"
    "v_exp_f32 v136, v136 clamp\n"
    "s_cmp_eq_u32 s67, s80\n"
    "s_cselect_b32 s67, s65, s67\n"
    "s_add_u32 m0, s68, 6144\n"
    "v_exp_f32 v137, v137 clamp\n"
    "v_add_u32_e64 v253, s67, v244\n"
    "s_waitcnt lgkmcnt(0)\n"
    "v_mfma_f32_4x4x4_16b_bf16 v[224:227], v[228:229], v[176:177], v[224:227] cbsz:4 abid:0\n"
    "v_exp_f32 v138, v138 clamp\n"
    "v_exp_f32 v139, v139 clamp\n"
    "v_mfma_f32_4x4x4_16b_bf16 v[224:227], v[228:229], v[178:179], v[224:227] cbsz:4 abid:1\n"
    "v_exp_f32 v140, v140 clamp\n"
    "v_exp_f32 v141, v141 clamp\n"
    "v_mfma_f32_4x4x4_16b_bf16 v[224:227], v[228:229], v[180:181], v[224:227] cbsz:4 abid:2\n"
    "v_exp_f32 v142, v142 clamp\n"
    "v_exp_f32 v143, v143 clamp\n"
    "v_mfma_f32_4x4x4_16b_bf16 v[224:227], v[228:229], v[182:183], v[224:227] cbsz:4 abid:3\n"
    "s_waitcnt vmcnt(4)\n"
    "s_memrealtime s[34:35]\n"
    "ds_read_b64_tr_b16 v[176:177], v240 offset:6144\n"
    "ds_read_b64_tr_b16 v[178:179], v241 offset:6400\n"
    "ds_read_b64_tr_b16 v[180:181], v242 offset:6656\n"
    "ds_read_b64_tr_b16 v[182:183], v243 offset:6912\n"
    "ds_read_b32 v236, v253 offset:0\n"
    "ds_read_b32 v238, v253 offset:4\n"
    "ds_read_b128 v[160:163], v234 offset:0\n"
    "ds_read_b128 v[164:167], v235 offset:0\n"
    "v_pk_fma_f32 v[128:129], v[128:129], s[70:71], v[96:97]\n"
    "v_pk_fma_f32 v[130:131], v[130:131], s[70:71], v[98:99]\n"
    "v_pk_add_f32 v[230:231], v[128:129], v[130:131]\n"
    "v_pk_mul_f32 v[232:233], v[128:129], v[130:131]\n"
    "v_pk_fma_f32 v[132:133], v[132:133], s[70:71], v[100:101]\n"
    "v_pk_fma_f32 v[230:231], v[230:231], v[132:133], v[232:233]\n"
    "v_pk_mul_f32 v[232:233], v[232:233], v[132:133]\n"
    "v_pk_fma_f32 v[134:135], v[134:135], s[70:71], v[102:103]\n"
    "v_pk_fma_f32 v[230:231], v[230:231], v[134:135], v[232:233]\n"
    "v_pk_mul_f32 v[232:233], v[232:233], v[134:135]\n"
    "v_pk_fma_f32 v[136:137], v[136:137], s[70:71], v[104:105]\n"
    "v_pk_fma_f32 v[230:231], v[230:231], v[136:137], v[232:233]\n"
    "v_pk_mul_f32 v[232:233], v[232:233], v[136:137]\n"
    "v_pk_fma_f32 v[138:139], v[138:139], s[70:71], v[106:107]\n"
    "v_pk_fma_f32 v[230:231], v[230:231], v[138:139], v[232:233]\n"
    "v_pk_mul_f32 v[232:233], v[232:233], v[138:139]\n"
    "v_pk_fma_f32 v[140:141], v[140:141], s[70:71], v[108:109]\n"
    "v_pk_fma_f32 v[230:231], v[230:231], v[140:141], v[232:233]\n"
    "v_pk_mul_f32 v[232:233], v[232:233], v[140:141]\n"
    "v_pk_fma_f32 v[142:143], v[142:143], s[70:71], v[110:111]\n"
    "v_pk_fma_f32 v[230:231], v[230:231], v[142:143], v[232:233]\n"
    "v_pk_mul_f32 v[232:233], v[232:233], v[142:143]\n"
    "v_rcp_f32 v232, v232\n"
    "v_rcp_f32 v233, v233\n"
    "v_fma_f32 v230, v230, v232, v247\n"
    "v_fmac_f32 v230, v231, v233\n"
    "v_lshlrev_b32 v232, 2, v249\n"
    "v_lshrrev_b32 v233, 2, v249\n"
    "v_and_b32 v232, 56, v232\n"
    "v_and_b32 v233, 4, v233\n"
    "v_add3_u32 v232, v232, v233, s81\n"
    "ds_read_b32 v232, v232\n"
    "s_add_u32 s81, s81, 64\n"
    "s_waitcnt lgkmcnt(0)\n"
    "v_lshlrev_b32 v232, 1, v232\n"
    "buffer_load_ushort v232, v232, s[44:47], 0 offen\n"
    "s_waitcnt vmcnt(0)\n"
    "v_lshlrev_b32 v232, 16, v232\n"
    "v_mul_f32 v230, v230, v232\n"
    "v_cvt_pk_bf16_f32 v230, v230, v230\n"
    "s_add_u32 s67, s67, 64\n"
    "s_waitcnt lgkmcnt(0)\n"
    "ds_write_b16 v245, v230\n"
    "ds_read_b64 v[228:229], v246\n"
    "s_cmp_le_u32 s34, s76\n"
    "s_cbranch_scc1 .Ldin_G2s_wait\n"
    ".Ldin_G2s_go:\n"
    "s_sub_u32 s94, s34, 60\n"
    "s_max_u32 s76, s76, s94\n"
    "s_add_u32 s76, s76, s72\n"
    ".Ldin_G2s_dma:\n"
    "buffer_load_dwordx4 v[236:237], s[40:43], 0 idxen offen offset:0 nt lds\n"
    "buffer_load_dwordx4 v[238:239], s[40:43], 0 idxen offen offset:1024 nt lds\n"
    ".Ldin_G2s_nd:\n"
    "s_cmp_eq_u32 s67, s82\n"
    "s_cbranch_scc1 .Ldin_L3s\n"
    ".Ldin_G3s:\n"
    "v_mfma_f32_16x16x32_bf16 v[128:131], v[0:3], v[160:163], v[112:115]\n"
    "v_exp_f32 v144, v144 clamp\n"
    "v_mfma_f32_16x16x32_bf16 v[128:131], v[4:7], v[164:167], v[128:131]\n"
    "v_exp_f32 v145, v145 clamp\n"
    "v_mfma_f32_16x16x32_bf16 v[132:135], v[8:11], v[160:163], v[116:119]\n"
    "v_exp_f32 v146, v146 clamp\n"
    "v_mfma_f32_16x16x32_bf16 v[132:135], v[12:15], v[164:167], v[132:135]\n"
    "v_exp_f32 v147, v147 clamp\n"
    "v_mfma_f32_16x16x32_bf16 v[136:139], v[16:19], v[160:163], v[120:123]\n"
    "v_exp_f32 v148, v148 clamp\n"
    "v_mfma_f32_16x16x32_bf16 v[136:139], v[20:23], v[164:167], v[136:139]\n"
    "v_exp_f32 v149, v149 clamp\n"
    "v_mfma_f32_16x16x32_bf16 v[140:143], v[24:27], v[160:163], v[124:127]\n"
    "v_exp_f32 v150, v150 clamp\n"
    "v_mfma_f32_16x16x32_bf16 v[140:143], v[28:31], v[164:167], v[140:143]\n"
    "v_exp_f32 v151, v151 clamp\n"
    "v_exp_f32 v152, v152 clamp\n"
    "s_cmp_eq_u32 s67, s80\n"
    "s_cselect_b32 s67, s65, s67\n"
    "s_add_u32 m0, s68, 0\n"
    "v_exp_f32 v153, v153 clamp\n"
    "v_add_u32 v253, s67, v244\n"
    "s_waitcnt lgkmcnt(0)\n"
    "v_mfma_f32_4x4x4_16b_bf16 v[224:227], v[228:229], v[168:169], v[224:227] cbsz:4 abid:0\n"
    "v_exp_f32 v154, v154 clamp\n"
    "v_exp_f32 v155, v155 clamp\n"
    "v_mfma_f32_4x4x4_16b_bf16 v[224:227], v[228:229], v[170:171], v[224:227] cbsz:4 abid:1\n"
    "v_exp_f32 v156, v156 clamp\n"
    "v_exp_f32 v157, v157 clamp\n"
    "v_mfma_f32_4x4x4_16b_bf16 v[224:227], v[228:229], v[172:173], v[224:227] cbsz:4 abid:2\n"
    "v_exp_f32 v158, v158 clamp\n"
    "v_exp_f32 v159, v159 clamp\n"
    "v_mfma_f32_4x4x4_16b_bf16 v[224:227], v[228:229], v[174:175], v[224:227] cbsz:4 abid:3\n"
    "s_waitcnt vmcnt(4)\n"
    "s_memrealtime s[34:35]\n"
    "ds_read_b64_tr_b16 v[168:169], v240 offset:0\n"
    "ds_read_b64_tr_b16 v[170:171], v241 offset:256\n"
    "ds_read_b64_tr_b16 v[172:173], v242 offset:512\n"
    "ds_read_b64_tr_b16 v[174:175], v243 offset:768\n"
    "ds_read_b32 v236, v253 offset:0\n"
    "ds_read_b32 v238, v253 offset:4\n"
    "ds_read_b128 v[160:163], v234 offset:2048\n"
    "ds_read_b128 v[164:167], v235 offset:2048\n"
    "v_pk_fma_f32 v[144:145], v[144:145], s[70:71], v[96:97]\n"
    "v_pk_fma_f32 v[146:147], v[146:147], s[70:71], v[98:99]\n"
    "v_pk_add_f32 v[230:231], v[144:145], v[146:147]\n"
    "v_pk_mul_f32 v[232:233], v[144:145], v[146:147]\n"
    "v_pk_fma_f32 v[148:149], v[148:149], s[70:71], v[100:101]\n"
    "v_pk_fma_f32 v[230:231], v[230:231], v[148:149], v[232:233]\n"
    "v_pk_mul_f32 v[232:233], v[232:233], v[148:149]\n"
    "v_pk_fma_f32 v[150:151], v[150:151], s[70:71], v[102:103]\n"
    "v_pk_fma_f32 v[230:231], v[230:231], v[150:151], v[232:233]\n"
    "v_pk_mul_f32 v[232:233], v[232:233], v[150:151]\n"
    "v_pk_fma_f32 v[152:153], v[152:153], s[70:71], v[104:105]\n"
    "v_pk_fma_f32 v[230:231], v[230:231], v[152:153], v[232:233]\n"
    "v_pk_mul_f32 v[232:233], v[232:233], v[152:153]\n"
    "v_pk_fma_f32 v[154:155], v[154:155], s[70:71], v[106:107]\n"
    "v_pk_fma_f32 v[230:231], v[230:231], v[154:155], v[232:233]\n"
    "v_pk_mul_f32 v[232:233], v[232:233], v[154:155]\n"
    "v_pk_fma_f32 v[156:157], v[156:157], s[70:71], v[108:109]\n"
    "v_pk_fma_f32 v[230:231], v[230:231], v[156:157], v[232:233]\n"
    "v_pk_mul_f32 v[232:233], v[232:233], v[156:157]\n"
    "v_pk_fma_f32 v[158:159], v[158:159], s[70:71], v[110:111]\n"
    "v_pk_fma_f32 v[230:231], v[230:231], v[158:159], v[232:233]\n"
    "v_pk_mul_f32 v[232:233], v[232:233], v[158:159]\n"
    "v_rcp_f32 v232, v232\n"
    "v_rcp_f32 v233, v233\n"
    "v_fma_f32 v230, v230, v232, v247\n"
    "v_fmac_f32 v230, v231, v233\n"
    "v_lshlrev_b32 v232, 2, v249\n"
    "v_lshrrev_b32 v233, 2, v249\n"
    "v_and_b32 v232, 56, v232\n"
    "v_and_b32 v233, 4, v233\n"
    "v_add3_u32 v232, v232, v233, s81\n"
    "ds_read_b32 v232, v232\n"
    "s_add_u32 s81, s81, 64\n"
    "s_waitcnt lgkmcnt(0)\n"
    "v_lshlrev_b32 v232, 1, v232\n"
    "buffer_load_ushort v232, v232, s[44:47], 0 offen\n"
    "s_waitcnt vmcnt(0)\n"
    "v_lshlrev_b32 v232, 16, v232\n"
    "v_mul_f32 v230, v230, v232\n"
    "v_cvt_pk_bf16_f32 v230, v230, v230\n"
    "s_add_u32 s67, s67, 64\n"
    "s_waitcnt lgkmcnt(0)\n"
    "ds_write_b16 v245, v230\n"
    "ds_read_b64 v[228:229], v246\n"
    "s_cmp_le_u32 s34, s76\n"
    "s_cbranch_scc1 .Ldin_G3s_wait\n"
    ".Ldin_G3s_go:\n"
    "s_add_u32 s76, s76, s72\n"
    ".Ldin_G3s_dma:\n"
    "buffer_load_dwordx4 v[236:237], s[40:43], 0 idxen offen offset:0 nt lds\n"
    "buffer_load_dwordx4 v[238:239], s[40:43], 0 idxen offen offset:1024 nt lds\n"
    ".Ldin_G3s_nd:\n"
    "s_cmp_eq_u32 s67, s82\n"
    "s_cbranch_scc1 .Ldin_L0s\n"
    ".Ldin_G0s:\n"
    "v_mfma_f32_16x16x32_bf16 v[144:147], v[0:3], v[160:163], v[112:115]\n"
    "v_exp_f32 v128, v128 clamp\n"
    "v_mfma_f32_16x16x32_bf16 v[144:147], v[4:7], v[164:167], v[144:147]\n"
    "v_exp_f32 v129, v129 clamp\n"
    "v_mfma_f32_16x16x32_bf16 v[148:151], v[8:11], v[160:163], v[116:119]\n"
    "v_exp_f32 v130, v130 clamp\n"
    "v_mfma_f32_16x16x32_bf16 v[148:151], v[12:15], v[164:167], v[148:151]\n"
    "v_exp_f32 v131, v131 clamp\n"
    "v_mfma_f32_16x16x32_bf16 v[152:155], v[16:19], v[160:163], v[120:123]\n"
    "v_exp_f32 v132, v132 clamp\n"
    "v_mfma_f32_16x16x32_bf16 v[152:155], v[20:23], v[164:167], v[152:155]\n"
    "v_exp_f32 v133, v133 clamp\n"
    "v_mfma_f32_16x16x32_bf16 v[156:159], v[24:27], v[160:163], v[124:127]\n"
    "v_exp_f32 v134, v134 clamp\n"
    "v_mfma_f32_16x16x32_bf16 v[156:159], v[28:31], v[164:167], v[156:159]\n"
    "v_exp_f32 v135, v135 clamp\n"
    "v_exp_f32 v136, v136 clamp\n"
    "s_cmp_eq_u32 s67, s80\n"
    "s_cselect_b32 s67, s65, s67\n"
    "s_add_u32 m0, s68, 2048\n"
    "v_exp_f32 v137, v137 clamp\n"
    "v_add_u32_e64 v253, s67, v244\n"
    "s_waitcnt lgkmcnt(0)\n"
    "v_mfma_f32_4x4x4_16b_bf16 v[224:227], v[228:229], v[176:177], v[224:227] cbsz:4 abid:0\n"
    "v_exp_f32 v138, v138 clamp\n"
    "v_exp_f32 v139, v139 clamp\n"
    "v_mfma_f32_4x4x4_16b_bf16 v[224:227], v[228:229], v[178:179], v[224:227] cbsz:4 abid:1\n"
    "v_exp_f32 v140, v140 clamp\n"
    "v_exp_f32 v141, v141 clamp\n"
    "v_mfma_f32_4x4x4_16b_bf16 v[224:227], v[228:229], v[180:181], v[224:227] cbsz:4 abid:2\n"
    "v_exp_f32 v142, v142 clamp\n"
    "v_exp_f32 v143, v143 clamp\n"
    "v_mfma_f32_4x4x4_16b_bf16 v[224:227], v[228:229], v[182:183], v[224:227] cbsz:4 abid:3\n"
    "s_waitcnt vmcnt(4)\n"
    "s_memrealtime s[34:35]\n"
    "ds_read_b64_tr_b16 v[176:177], v240 offset:2048\n"
    "ds_read_b64_tr_b16 v[178:179], v241 offset:2304\n"
    "ds_read_b64_tr_b16 v[180:181], v242 offset:2560\n"
    "ds_read_b64_tr_b16 v[182:183], v243 offset:2816\n"
    "ds_read_b32 v236, v253 offset:0\n"
    "ds_read_b32 v238, v253 offset:4\n"
    "ds_read_b128 v[160:163], v234 offset:4096\n"
    "ds_read_b128 v[164:167], v235 offset:4096\n"
    "v_pk_fma_f32 v[128:129], v[128:129], s[70:71], v[96:97]\n"
    "v_pk_fma_f32 v[130:131], v[130:131], s[70:71], v[98:99]\n"
    "v_pk_add_f32 v[230:231], v[128:129], v[130:131]\n"
    "v_pk_mul_f32 v[232:233], v[128:129], v[130:131]\n"
    "v_pk_fma_f32 v[132:133], v[132:133], s[70:71], v[100:101]\n"
    "v_pk_fma_f32 v[230:231], v[230:231], v[132:133], v[232:233]\n"
    "v_pk_mul_f32 v[232:233], v[232:233], v[132:133]\n"
    "v_pk_fma_f32 v[134:135], v[134:135], s[70:71], v[102:103]\n"
    "v_pk_fma_f32 v[230:231], v[230:231], v[134:135], v[232:233]\n"
    "v_pk_mul_f32 v[232:233], v[232:233], v[134:135]\n"
    "v_pk_fma_f32 v[136:137], v[136:137], s[70:71], v[104:105]\n"
    "v_pk_fma_f32 v[230:231], v[230:231], v[136:137], v[232:233]\n"
    "v_pk_mul_f32 v[232:233], v[232:233], v[136:137]\n"
    "v_pk_fma_f32 v[138:139], v[138:139], s[70:71], v[106:107]\n"
    "v_pk_fma_f32 v[230:231], v[230:231], v[138:139], v[232:233]\n"
    "v_pk_mul_f32 v[232:233], v[232:233], v[138:139]\n"
    "v_pk_fma_f32 v[140:141], v[140:141], s[70:71], v[108:109]\n"
    "v_pk_fma_f32 v[230:231], v[230:231], v[140:141], v[232:233]\n"
    "v_pk_mul_f32 v[232:233], v[232:233], v[140:141]\n"
    "v_pk_fma_f32 v[142:143], v[142:143], s[70:71], v[110:111]\n"
    "v_pk_fma_f32 v[230:231], v[230:231], v[142:143], v[232:233]\n"
    "v_pk_mul_f32 v[232:233], v[232:233], v[142:143]\n"
    "v_rcp_f32 v232, v232\n"
    "v_rcp_f32 v233, v233\n"
    "v_fma_f32 v230, v230, v232, v247\n"
    "v_fmac_f32 v230, v231, v233\n"
    "v_lshlrev_b32 v232, 2, v249\n"
    "v_lshrrev_b32 v233, 2, v249\n"
    "v_and_b32 v232, 56, v232\n"
    "v_and_b32 v233, 4, v233\n"
    "v_add3_u32 v232, v232, v233, s81\n"
    "ds_read_b32 v232, v232\n"
    "s_add_u32 s81, s81, 64\n"
    "s_waitcnt lgkmcnt(0)\n"
    "v_lshlrev_b32 v232, 1, v232\n"
    "buffer_load_ushort v232, v232, s[44:47], 0 offen\n"
    "s_waitcnt vmcnt(0)\n"
    "v_lshlrev_b32 v232, 16, v232\n"
    "v_mul_f32 v230, v230, v232\n"
    "v_cvt_pk_bf16_f32 v230, v230, v230\n"
    "s_add_u32 s67, s67, 64\n"
    "s_waitcnt lgkmcnt(0)\n"
    "ds_write_b16 v245, v230\n"
    "ds_read_b64 v[228:229], v246\n"
    "s_cmp_le_u32 s34, s76\n"
    "s_cbranch_scc1 .Ldin_G0s_wait\n"
    ".Ldin_G0s_go:\n"
    "s_sub_u32 s94, s34, 60\n"
    "s_max_u32 s76, s76, s94\n"
    "s_add_u32 s76, s76, s72\n"
    ".Ldin_G0s_dma:\n"
    "buffer_load_dwordx4 v[236:237], s[40:43], 0 idxen offen offset:0 nt lds\n"
    "buffer_load_dwordx4 v[238:239], s[40:43], 0 idxen offen offset:1024 nt lds\n"
    ".Ldin_G0s_nd:\n"
    "s_cmp_eq_u32 s67, s82\n"
    "s_cbranch_scc1 .Ldin_L1s\n"
    ".Ldin_G1s:\n"
    "v_mfma_f32_16x16x32_bf16 v[128:131], v[0:3], v[160:163], v[112:115]\n"
    "v_exp_f32 v144, v144 clamp\n"
    "v_mfma_f32_16x16x32_bf16 v[128:131], v[4:7], v[164:167], v[128:131]\n"
    "v_exp_f32 v145, v145 clamp\n"
    "v_mfma_f32_16x16x32_bf16 v[132:135], v[8:11], v[160:163], v[116:119]\n"
    "v_exp_f32 v146, v146 clamp\n"
    "v_mfma_f32_16x16x32_bf16 v[132:135], v[12:15], v[164:167], v[132:135]\n"
    "v_exp_f32 v147, v147 clamp\n"
    "v_mfma_f32_16x16x32_bf16 v[136:139], v[16:19], v[160:163], v[120:123]\n"
    "v_exp_f32 v148, v148 clamp\n"
    "v_mfma_f32_16x16x32_bf16 v[136:139], v[20:23], v[164:167], v[136:139]\n"
    "v_exp_f32 v149, v149 clamp\n"
    "v_mfma_f32_16x16x32_bf16 v[140:143], v[24:27], v[160:163], v[124:127]\n"
    "v_exp_f32 v150, v150 clamp\n"
    "v_mfma_f32_16x16x32_bf16 v[140:143], v[28:31], v[164:167], v[140:143]\n"
    "v_exp_f32 v151, v151 clamp\n"
    "v_exp_f32 v152, v152 clamp\n"
    "s_cmp_eq_u32 s67, s80\n"
    "s_cselect_b32 s67, s65, s67\n"
    "s_add_u32 m0, s68, 4096\n"
    "v_exp_f32 v153, v153 clamp\n"
    "v_add_u32_e64 v253, s67, v244\n"
    "s_waitcnt lgkmcnt(0)\n"
    "v_mfma_f32_4x4x4_16b_bf16 v[224:227], v[228:229], v[168:169], v[224:227] cbsz:4 abid:0\n"
    "v_exp_f32 v154, v154 clamp\n"
    "v_exp_f32 v155, v155 clamp\n"
    "v_mfma_f32_4x4x4_16b_bf16 v[224:227], v[228:229], v[170:171], v[224:227] cbsz:4 abid:1\n"
    "v_exp_f32 v156, v156 clamp\n"
    "v_exp_f32 v157, v157 clamp\n"
    "v_mfma_f32_4x4x4_16b_bf16 v[224:227], v[228:229], v[172:173], v[224:227] cbsz:4 abid:2\n"
    "v_exp_f32 v158, v158 clamp\n"
    "v_exp_f32 v159, v159 clamp\n"
    "v_mfma_f32_4x4x4_16b_bf16 v[224:227], v[228:229], v[174:175], v[224:227] cbsz:4 abid:3\n"
    "s_waitcnt vmcnt(4)\n"
    "s_memrealtime s[34:35]\n"
    "ds_read_b64_tr_b16 v[168:169], v240 offset:4096\n"
    "ds_read_b64_tr_b16 v[170:171], v241 offset:4352\n"
    "ds_read_b64_tr_b16 v[172:173], v242 offset:4608\n"
    "ds_read_b64_tr_b16 v[174:175], v243 offset:4864\n"
    "ds_read_b32 v236, v253 offset:0\n"
    "ds_read_b32 v238, v253 offset:4\n"
    "ds_read_b128 v[160:163], v234 offset:6144\n"
    "ds_read_b128 v[164:167], v235 offset:6144\n"
    "v_pk_fma_f32 v[144:145], v[144:145], s[70:71], v[96:97]\n"
    "v_pk_fma_f32 v[146:147], v[146:147], s[70:71], v[98:99]\n"
    "v_pk_add_f32 v[230:231], v[144:145], v[146:147]\n"
    "v_pk_mul_f32 v[232:233], v[144:145], v[146:147]\n"
    "v_pk_fma_f32 v[148:149], v[148:149], s[70:71], v[100:101]\n"
    "v_pk_fma_f32 v[230:231], v[230:231], v[148:149], v[232:233]\n"
    "v_pk_mul_f32 v[232:233], v[232:233], v[148:149]\n"
    "v_pk_fma_f32 v[150:151], v[150:151], s[70:71], v[102:103]\n"
    "v_pk_fma_f32 v[230:231], v[230:231], v[150:151], v[232:233]\n"
    "v_pk_mul_f32 v[232:233], v[232:233], v[150:151]\n"
    "v_pk_fma_f32 v[152:153], v[152:153], s[70:71], v[104:105]\n"
    "v_pk_fma_f32 v[230:231], v[230:231], v[152:153], v[232:233]\n"
    "v_pk_mul_f32 v[232:233], v[232:233], v[152:153]\n"
    "v_pk_fma_f32 v[154:155], v[154:155], s[70:71], v[106:107]\n"
    "v_pk_fma_f32 v[230:231], v[230:231], v[154:155], v[232:233]\n"
    "v_pk_mul_f32 v[232:233], v[232:233], v[154:155]\n"
    "v_pk_fma_f32 v[156:157], v[156:157], s[70:71], v[108:109]\n"
    "v_pk_fma_f32 v[230:231], v[230:231], v[156:157], v[232:233]\n"
    "v_pk_mul_f32 v[232:233], v[232:233], v[156:157]\n"
    "v_pk_fma_f32 v[158:159], v[158:159], s[70:71], v[110:111]\n"
    "v_pk_fma_f32 v[230:231], v[230:231], v[158:159], v[232:233]\n"
    "v_pk_mul_f32 v[232:233], v[232:233], v[158:159]\n"
    "v_rcp_f32 v232, v232\n"
    "v_rcp_f32 v233, v233\n"
    "v_fma_f32 v230, v230, v232, v247\n"
    "v_fmac_f32 v230, v231, v233\n"
    "v_lshlrev_b32 v232, 2, v249\n"
    "v_lshrrev_b32 v233, 2, v249\n"
    "v_and_b32 v232, 56, v232\n"
    "v_and_b32 v233, 4, v233\n"
    "v_add3_u32 v232, v232, v233, s81\n"
    "ds_read_b32 v232, v232\n"
    "s_add_u32 s81, s81, 64\n"
    "s_waitcnt lgkmcnt(0)\n"
    "v_lshlrev_b32 v232, 1, v232\n"
    "buffer_load_ushort v232, v232, s[44:47], 0 offen\n"
    "s_waitcnt vmcnt(0)\n"
    "v_lshlrev_b32 v232, 16, v232\n"
    "v_mul_f32 v230, v230, v232\n"
    "v_cvt_pk_bf16_f32 v230, v230, v230\n"
    "s_add_u32 s67, s67, 64\n"
    "s_waitcnt lgkmcnt(0)\n"
    "ds_write_b16 v245, v230\n"
    "ds_read_b64 v[228:229], v246\n"
    "s_cmp_le_u32 s34, s76\n"
    "s_cbranch_scc1 .Ldin_G1s_wait\n"
    ".Ldin_G1s_go:\n"
    "s_add_u32 s76, s76, s72\n"
    ".Ldin_G1s_dma:\n"
    "buffer_load_dwordx4 v[236:237], s[40:43], 0 idxen offen offset:0 nt lds\n"
    "buffer_load_dwordx4 v[238:239], s[40:43], 0 idxen offen offset:1024 nt lds\n"
    ".Ldin_G1s_nd:\n"
    "s_cmp_eq_u32 s67, s82\n"
    "s_cbranch_scc1 .Ldin_L2s\n"
    "s_branch .Ldin_G2s\n"
    ".Ldin_B1s_wait:\n"
    "s_cmp_lt_i32 s59, s57\n"
    "s_cbranch_scc1 .Ldin_B1s_w2\n"
    "s_sub_u32 s94, s67, s65\n"
    "s_cmp_gt_u32 s94, 256\n"
    "s_cbranch_scc1 .Ldin_B1s_dma\n"
    "s_cmp_eq_u32 s94, 64\n"
    "s_waitcnt vmcnt(2)\n"
    "s_cbranch_scc1 .Ldin_B1s_nd\n"
    "s_waitcnt vmcnt(0)\n"
    "s_branch .Ldin_B1s_nd\n"
    ".Ldin_B1s_slp:\n"
    "s_sleep 1\n"
    ".Ldin_B1s_w2:\n"
    "s_memrealtime s[34:35]\n"
    "s_waitcnt lgkmcnt(0)\n"
    "s_sub_u32 s94, s34, s76\n"
    "s_cmp_lt_i32 s94, 0\n"
    "s_cbranch_scc1 .Ldin_B1s_slp\n"
    "s_branch .Ldin_B1s_go\n"
    ".Ldin_G2s_wait:\n"
    "s_cmp_lt_i32 s59, s57\n"
    "s_cbranch_scc1 .Ldin_G2s_w2\n"
    "s_sub_u32 s94, s67, s65\n"
    "s_cmp_gt_u32 s94, 256\n"
    "s_cbranch_scc1 .Ldin_G2s_dma\n"
    "s_cmp_eq_u32 s94, 64\n"
    "s_waitcnt vmcnt(2)\n"
    "s_cbranch_scc1 .Ldin_G2s_nd\n"
    "s_waitcnt vmcnt(0)\n"
    "s_branch .Ldin_G2s_nd\n"
    ".Ldin_G2s_slp:\n"
    "s_sleep 1\n"
    ".Ldin_G2s_w2:\n"
    "s_memrealtime s[34:35]\n"
    "s_waitcnt lgkmcnt(0)\n"
    "s_sub_u32 s94, s34, s76\n"
    "s_cmp_lt_i32 s94, 0\n"
    "s_cbranch_scc1 .Ldin_G2s_slp\n"
    "s_branch .Ldin_G2s_go\n"
    ".Ldin_G3s_wait:\n"
    "s_cmp_lt_i32 s59, s57\n"
    "s_cbranch_scc1 .Ldin_G3s_w2\n"
    "s_sub_u32 s94, s67, s65\n"
    "s_cmp_gt_u32 s94, 256\n"
    "s_cbranch_scc1 .Ldin_G3s_dma\n"
    "s_cmp_eq_u32 s94, 64\n"
    "s_waitcnt vmcnt(2)\n"
    "s_cbranch_scc1 .Ldin_G3s_nd\n"
    "s_waitcnt vmcnt(0)\n"
    "s_branch .Ldin_G3s_nd\n"
    ".Ldin_G3s_slp:\n"
    "s_sleep 1\n"
    ".Ldin_G3s_w2:\n"
    "s_memrealtime s[34:35]\n"
    "s_waitcnt lgkmcnt(0)\n"
    "s_sub_u32 s94, s34, s76\n"
    "s_cmp_lt_i32 s94, 0\n"
    "s_cbranch_scc1 .Ldin_G3s_slp\n"
    "s_branch .Ldin_G3s_go\n"
    ".Ldin_G0s_wait:\n"
    "s_cmp_lt_i32 s59, s57\n"
    "s_cbranch_scc1 .Ldin_G0s_w2\n"
    "s_sub_u32 s94, s67, s65\n"
    "s_cmp_gt_u32 s94, 256\n"
    "s_cbranch_scc1 .Ldin_G0s_dma\n"
    "s_cmp_eq_u32 s94, 64\n"
    "s_waitcnt vmcnt(2)\n"
    "s_cbranch_scc1 .Ldin_G0s_nd\n"
    "s_waitcnt vmcnt(0)\n"
    "s_branch .Ldin_G0s_nd\n"
    ".Ldin_G0s_slp:\n"
    "s_sleep 1\n"
    ".Ldin_G0s_w2:\n"
    "s_memrealtime s[34:35]\n"
    "s_waitcnt lgkmcnt(0)\n"
    "s_sub_u32 s94, s34, s76\n"
    "s_cmp_lt_i32 s94, 0\n"
    "s_cbranch_scc1 .Ldin_G0s_slp\n"
    "s_branch .Ldin_G0s_go\n"
    ".Ldin_G1s_wait:\n"
    "s_cmp_lt_i32 s59, s57\n"
    "s_cbranch_scc1 .Ldin_G1s_w2\n"
    "s_sub_u32 s94, s67, s65\n"
    "s_cmp_gt_u32 s94, 256\n"
    "s_cbranch_scc1 .Ldin_G1s_dma\n"
    "s_cmp_eq_u32 s94, 64\n"
    "s_waitcnt vmcnt(2)\n"
    "s_cbranch_scc1 .Ldin_G1s_nd\n"
    "s_waitcnt vmcnt(0)\n"
    "s_branch .Ldin_G1s_nd\n"
    ".Ldin_G1s_slp:\n"
    "s_sleep 1\n"
    ".Ldin_G1s_w2:\n"
    "s_memrealtime s[34:35]\n"
    "s_waitcnt lgkmcnt(0)\n"
    "s_sub_u32 s94, s34, s76\n"
    "s_cmp_lt_i32 s94, 0\n"
    "s_cbranch_scc1 .Ldin_G1s_slp\n"
    "s_branch .Ldin_G1s_go\n"
    ".p2align 5\n"
    ".Ldin_L2s:\n"
    "s_setprio 0\n"
    "s_cmp_ge_i32 s59, s57\n"
    "s_cbranch_scc1 .Ldin_E0s\n"
    "s_add_u32 s76, s76, s73\n"
    "v_mov_b32 v253, s63\n"
    "v_and_b32 v184, v254, v184\n"
    "v_and_b32 v185, v255, v185\n"
    "v_and_b32 v186, v254, v186\n"
    "v_and_b32 v187, v255, v187\n"
    "v_and_b32 v188, v254, v188\n"
    "v_and_b32 v189, v255, v189\n"
    "v_and_b32 v190, v254, v190\n"
    "v_and_b32_e64 v191, v255, v191\n"
    "ds_read_b64 v[232:233], v253\n"
    "v_mfma_f32_4x4x4_16b_bf16 v[144:147], v[184:185], v[32:33], v[64:67]\n"
    "ds_read_b128 v[112:115], v252 offset:17408\n"
    "v_mfma_f32_4x4x4_16b_bf16 v[148:151], v[186:187], v[34:35], v[68:71]\n"
    "ds_read_b128 v[116:119], v252 offset:17424\n"
    "v_mfma_f32_4x4x4_16b_bf16 v[152:155], v[188:189], v[36:37], v[72:75]\n"
    "ds_read_b128 v[120:123], v252 offset:17536\n"
    "ds_read_b128 v[124:127], v252 offset:17552\n"
    "v_cvt_pk_bf16_f32 v0, v144, v145\n"
    "v_cvt_pk_bf16_f32 v1, v146, v147\n"
    "v_mfma_f32_4x4x4_16b_bf16 v[156:159], v[190:191], v[38:39], v[76:79]\n"
    "v_cvt_pk_bf16_f32 v2, v148, v149\n"
    "v_cvt_pk_bf16_f32 v3, v150, v151\n"
    "v_mfma_f32_4x4x4_16b_bf16 v[144:147], v[184:185], v[40:41], v[80:83]\n"
    "v_cvt_pk_bf16_f32 v4, v152, v153\n"
    "v_cvt_pk_bf16_f32 v5, v154, v155\n"
    "v_mfma_f32_4x4x4_16b_bf16 v[148:151], v[186:187], v[42:43], v[84:87]\n"
    "v_cvt_pk_bf16_f32 v6, v156, v157\n"
    "v_cvt_pk_bf16_f32 v7, v158, v159\n"
    "v_mfma_f32_4x4x4_16b_bf16 v[152:155], v[188:189], v[44:45], v[88:91]\n"
    "v_cvt_pk_bf16_f32 v8, v144, v145\n"
    "v_cvt_pk_bf16_f32 v9, v146, v147\n"
    "v_mfma_f32_4x4x4_16b_bf16 v[156:159], v[190:191], v[46:47], v[92:95]\n"
    "v_cvt_pk_bf16_f32 v10, v148, v149\n"
    "v_cvt_pk_bf16_f32 v11, v150, v151\n"
    "v_mfma_f32_4x4x4_16b_bf16 v[144:147], v[184:185], v[48:49], v[192:195]\n"
    "v_cvt_pk_bf16_f32 v12, v152, v153\n"
    "v_cvt_pk_bf16_f32 v13, v154, v155\n"
    "v_mfma_f32_4x4x4_16b_bf16 v[148:151], v[186:187], v[50:51], v[196:199]\n"
    "v_cvt_pk_bf16_f32 v14, v156, v157\n"
    "v_cvt_pk_bf16_f32 v15, v158, v159\n"
    "v_mfma_f32_4x4x4_16b_bf16 v[152:155], v[188:189], v[52:53], v[200:203]\n"
    "s_waitcnt lgkmcnt(0)\n"
    "v_readfirstlane_b32 s60, v232\n"
    "v_cvt_pk_bf16_f32 v16, v144, v145\n"
    "v_cvt_pk_bf16_f32 v17, v146, v147\n"
    "v_mfma_f32_4x4x4_16b_bf16 v[156:159], v[190:191], v[54:55], v[204:207]\n"
    "s_cmp_eq_u32 s60, -1\n"
    "s_cbranch_scc1 .Ldin_L2s_poll\n"
    ".Ldin_L2s_pr:\n"
    "v_readfirstlane_b32 s95, v233\n"
    "s_and_b32 s94, s60, 63\n"
    "s_lshl_b32 s94, s94, 6\n"
    "s_add_u32 s94, s94, 68480\n"
    "s_add_u32 s63, s63, 8\n"
    "v_lshl_add_u32 v252, v249, 1, s94\n"
    "ds_read_b32 v252, v252\n"
    "s_sub_u32 s95, s95, s76\n"
    "s_ashr_i32 s95, s95, 4\n"
    "s_min_i32 s95, s95, s74\n"
    "s_max_i32 s72, s95, s75\n"
    "v_cvt_pk_bf16_f32 v18, v148, v149\n"
    "v_cvt_pk_bf16_f32 v19, v150, v151\n"
    "v_mfma_f32_4x4x4_16b_bf16 v[144:147], v[184:185], v[56:57], v[208:211]\n"
    "v_cvt_pk_bf16_f32 v20, v152, v153\n"
    "v_cvt_pk_bf16_f32 v21, v154, v155\n"
    "v_mfma_f32_4x4x4_16b_bf16 v[148:151], v[186:187], v[58:59], v[212:215]\n"
    "v_cvt_pk_bf16_f32 v22, v156, v157\n"
    "v_cvt_pk_bf16_f32 v23, v158, v159\n"
    "v_mfma_f32_4x4x4_16b_bf16 v[152:155], v[188:189], v[60:61], v[216:219]\n"
    "v_cvt_pk_bf16_f32 v24, v144, v145\n"
    "v_cvt_pk_bf16_f32 v25, v146, v147\n"
    "v_mfma_f32_4x4x4_16b_bf16 v[156:159], v[190:191], v[62:63], v[220:223]\n"
    "v_cvt_pk_bf16_f32 v26, v148, v149\n"
    "v_cvt_pk_bf16_f32 v27, v150, v151\n"
    "v_cvt_pk_bf16_f32 v28, v152, v153\n"
    "v_cvt_pk_bf16_f32 v29, v154, v155\n"
    "v_exp_f32 v136, v136 clamp\n"
    "v_cvt_pk_bf16_f32 v30, v156, v157\n"
    "v_cvt_pk_bf16_f32 v31, v158, v159\n"
    "v_mfma_f32_16x16x32_bf16 v[144:147], v[0:3], v[160:163], v[112:115]\n"
    "v_exp_f32 v128, v128 clamp\n"
    "v_mfma_f32_16x16x32_bf16 v[144:147], v[4:7], v[164:167], v[144:147]\n"
    "v_exp_f32 v129, v129 clamp\n"
    "v_mfma_f32_16x16x32_bf16 v[148:151], v[8:11], v[160:163], v[116:119]\n"
    "v_exp_f32 v130, v130 clamp\n"
    "v_mfma_f32_16x16x32_bf16 v[148:151], v[12:15], v[164:167], v[148:151]\n"
    "v_exp_f32 v131, v131 clamp\n"
    "v_mfma_f32_16x16x32_bf16 v[152:155], v[16:19], v[160:163], v[120:123]\n"
    "v_exp_f32 v132, v132 clamp\n"
    "v_mfma_f32_16x16x32_bf16 v[152:155], v[20:23], v[164:167], v[152:155]\n"
    "v_exp_f32 v133, v133 clamp\n"
    "v_mfma_f32_16x16x32_bf16 v[156:159], v[24:27], v[160:163], v[124:127]\n"
    "v_exp_f32 v134, v134 clamp\n"
    "v_mfma_f32_16x16x32_bf16 v[156:159], v[28:31], v[164:167], v[156:159]\n"
    "v_exp_f32 v135, v135 clamp\n"
    "s_add_u32 m0, s68, 6144\n"
    "v_exp_f32 v137, v137 clamp\n"
    "v_add_u32 v253, s67, v244\n"
    "v_mfma_f32_4x4x4_16b_bf16 v[224:227], v[228:229], v[176:177], v[224:227] cbsz:4 abid:0\n"
    "v_exp_f32 v138, v138 clamp\n"
    "v_exp_f32 v139, v139 clamp\n"
    "v_mfma_f32_4x4x4_16b_bf16 v[224:227], v[228:229], v[178:179], v[224:227] cbsz:4 abid:1\n"
    "v_exp_f32 v140, v140 clamp\n"
    "v_exp_f32 v141, v141 clamp\n"
    "v_mfma_f32_4x4x4_16b_bf16 v[224:227], v[228:229], v[180:181], v[224:227] cbsz:4 abid:2\n"
    "v_exp_f32 v142, v142 clamp\n"
    "v_exp_f32 v143, v143 clamp\n"
    "v_mfma_f32_4x4x4_16b_bf16 v[224:227], v[228:229], v[182:183], v[224:227] cbsz:4 abid:3\n"
    "s_waitcnt vmcnt(4)\n"
    "ds_read_b64_tr_b16 v[176:177], v240 offset:6144\n"
    "ds_read_b64_tr_b16 v[178:179], v241 offset:6400\n"
    "ds_read_b64_tr_b16 v[180:181], v242 offset:6656\n"
    "ds_read_b64_tr_b16 v[182:183], v243 offset:6912\n"
    "ds_read_b32 v236, v253 offset:0\n"
    "ds_read_b32 v238, v253 offset:4\n"
    "ds_read_b128 v[160:163], v234 offset:0\n"
    "ds_read_b128 v[164:167], v235 offset:0\n"
    "s_waitcnt lgkmcnt(8)\n"
    "s_nop 0\n"
    "v_readlane_b32 s20, v252, 0\n"
    "v_readlane_b32 s21, v252, 1\n"
    "v_readlane_b32 s22, v252, 2\n"
    "v_readlane_b32 s23, v252, 3\n"
    "v_readlane_b32 s24, v252, 4\n"
    "v_readlane_b32 s25, v252, 5\n"
    "v_readlane_b32 s26, v252, 6\n"
    "v_readlane_b32 s27, v252, 7\n"
    "v_pk_fma_f32 v[128:129], v[128:129], s[70:71], v[96:97]\n"
    "v_pk_fma_f32 v[130:131], v[130:131], s[70:71], v[98:99]\n"
    "v_pk_add_f32 v[230:231], v[128:129], v[130:131]\n"
    "v_pk_mul_f32 v[232:233], v[128:129], v[130:131]\n"
    "v_pk_fma_f32 v[132:133], v[132:133], s[70:71], v[100:101]\n"
    "v_pk_fma_f32 v[230:231], v[230:231], v[132:133], v[232:233]\n"
    "v_pk_mul_f32 v[232:233], v[232:233], v[132:133]\n"
    "v_pk_fma_f32 v[134:135], v[134:135], s[70:71], v[102:103]\n"
    "v_pk_fma_f32 v[230:231], v[230:231], v[134:135], v[232:233]\n"
    "v_pk_mul_f32 v[232:233], v[232:233], v[134:135]\n"
    "v_pk_fma_f32 v[136:137], v[136:137], s[70:71], v[104:105]\n"
    "v_pk_fma_f32 v[230:231], v[230:231], v[136:137], v[232:233]\n"
    "v_pk_mul_f32 v[232:233], v[232:233], v[136:137]\n"
    "v_pk_fma_f32 v[138:139], v[138:139], s[70:71], v[106:107]\n"
    "v_pk_fma_f32 v[230:231], v[230:231], v[138:139], v[232:233]\n"
    "v_pk_mul_f32 v[232:233], v[232:233], v[138:139]\n"
    "v_pk_fma_f32 v[140:141], v[140:141], s[70:71], v[108:109]\n"
    "v_pk_fma_f32 v[230:231], v[230:231], v[140:141], v[232:233]\n"
    "v_pk_mul_f32 v[232:233], v[232:233], v[140:141]\n"
    "v_pk_fma_f32 v[142:143], v[142:143], s[70:71], v[110:111]\n"
    "v_pk_fma_f32 v[230:231], v[230:231], v[142:143], v[232:233]\n"
    "v_pk_mul_f32 v[232:233], v[232:233], v[142:143]\n"
    "v_rcp_f32 v232, v232\n"
    "v_rcp_f32 v233, v233\n"
    "v_fma_f32 v230, v230, v232, v247\n"
    "v_fmac_f32 v230, v231, v233\n"
    "v_lshlrev_b32 v232, 2, v249\n"
    "v_lshrrev_b32 v233, 2, v249\n"
    "v_and_b32 v232, 56, v232\n"
    "v_and_b32 v233, 4, v233\n"
    "v_add3_u32 v232, v232, v233, s81\n"
    "ds_read_b32 v232, v232\n"
    "s_add_u32 s81, s81, 64\n"
    "s_waitcnt lgkmcnt(0)\n"
    "v_lshlrev_b32 v232, 1, v232\n"
    "buffer_load_ushort v232, v232, s[44:47], 0 offen\n"
    "s_waitcnt vmcnt(0)\n"
    "v_lshlrev_b32 v232, 16, v232\n"
    "v_mul_f32 v230, v230, v232\n"
    "v_cvt_pk_bf16_f32 v230, v230, v230\n"
    "s_add_u32 s67, s67, 64\n"
    "s_waitcnt lgkmcnt(0)\n"
    "ds_write_b16 v245, v230\n"
    "ds_read_b64 v[228:229], v246\n"
    "s_lshl1_add_u32 s76, s72, s76\n"
    "buffer_load_dwordx4 v[236:237], s[40:43], 0 idxen offen offset:0 nt lds\n"
    "buffer_load_dwordx4 v[238:239], s[40:43], 0 idxen offen offset:1024 nt lds\n"
    "s_and_b32 s62, s58, 63\n"
    "s_mul_i32 s62, s62, 272\n"
    "s_mov_b32 s58, s59\n"
    "s_mov_b32 s59, s60\n"
    "s_mov_b32 s81, s65\n"
    "s_xor_b32 s65, s65, s64\n"
    "s_mov_b32 s80, s83\n"
    "s_add_u32 s82, s65, 256\n"
    "s_cmp_lt_i32 s59, s57\n"
    "s_cselect_b32 s76, s76, -1\n"
    "s_bitcmp1_b32 s58, 30\n"
    "s_cbranch_scc0 .Ldin_A3\n"
    ".Ldin_A3s:\n"
    "v_mfma_f32_16x16x32_bf16 v[128:131], v[0:3], v[160:163], v[112:115]\n"
    "v_exp_f32 v144, v144 clamp\n"
    "v_mfma_f32_16x16x32_bf16 v[128:131], v[4:7], v[164:167], v[128:131]\n"
    "v_exp_f32 v145, v145 clamp\n"
    "v_mfma_f32_16x16x32_bf16 v[132:135], v[8:11], v[160:163], v[116:119]\n"
    "v_exp_f32 v146, v146 clamp\n"
    "v_mfma_f32_16x16x32_bf16 v[132:135], v[12:15], v[164:167], v[132:135]\n"
    "v_exp_f32 v147, v147 clamp\n"
    "v_mfma_f32_16x16x32_bf16 v[136:139], v[16:19], v[160:163], v[120:123]\n"
    "v_exp_f32 v148, v148 clamp\n"
    "v_mfma_f32_16x16x32_bf16 v[136:139], v[20:23], v[164:167], v[136:139]\n"
    "v_exp_f32 v149, v149 clamp\n"
    "v_mfma_f32_16x16x32_bf16 v[140:143], v[24:27], v[160:163], v[124:127]\n"
    "v_exp_f32 v150, v150 clamp\n"
    "v_mfma_f32_16x16x32_bf16 v[140:143], v[28:31], v[164:167], v[140:143]\n"
    "v_exp_f32 v151, v151 clamp\n"
    "v_exp_f32 v152, v152 clamp\n"
    "s_add_u32 m0, s68, 0\n"
    "v_exp_f32 v153, v153 clamp\n"
    "v_add_u32 v253, s67, v244\n"
    "s_waitcnt lgkmcnt(0)\n"
    "v_mfma_f32_4x4x4_16b_bf16 v[224:227], v[228:229], v[168:169], v[224:227] cbsz:4 abid:0\n"
    "v_exp_f32 v154, v154 clamp\n"
    "v_exp_f32 v155, v155 clamp\n"
    "v_mfma_f32_4x4x4_16b_bf16 v[224:227], v[228:229], v[170:171], v[224:227] cbsz:4 abid:1\n"
    "v_exp_f32 v156, v156 clamp\n"
    "v_exp_f32 v157, v157 clamp\n"
    "v_mfma_f32_4x4x4_16b_bf16 v[224:227], v[228:229], v[172:173], v[224:227] cbsz:4 abid:2\n"
    "v_exp_f32 v158, v158 clamp\n"
    "v_exp_f32 v159, v159 clamp\n"
    "v_mfma_f32_4x4x4_16b_bf16 v[224:227], v[228:229], v[174:175], v[224:227] cbsz:4 abid:3\n"
    "s_waitcnt vmcnt(4)\n"
    "ds_read_b64_tr_b16 v[168:169], v240 offset:0\n"
    "ds_read_b64_tr_b16 v[170:171], v241 offset:256\n"
    "ds_read_b64_tr_b16 v[172:173], v242 offset:512\n"
    "ds_read_b64_tr_b16 v[174:175], v243 offset:768\n"
    "ds_read_b32 v236, v253 offset:0\n"
    "ds_read_b32 v238, v253 offset:4\n"
    "ds_read_b128 v[160:163], v234 offset:2048\n"
    "ds_read_b128 v[164:167], v235 offset:2048\n"
    "s_cbranch_vccz .Ldin_A3s_np\n"
    "s_setprio 1\n"
    ".Ldin_A3s_np:\n"
    "v_pk_fma_f32 v[144:145], v[144:145], s[70:71], v[96:97]\n"
    "v_pk_fma_f32 v[146:147], v[146:147], s[70:71], v[98:99]\n"
    "v_add_f32 v191, v224, v225\n"
    "v_add_f32 v190, v226, v227\n"
    "v_add_f32 v191, v191, v190\n"
    "v_add_u32 v190, s62, v249\n"
    "v_mul_f32 v191, s69, v191\n"
    "v_cvt_pk_bf16_f32 v191, v191, v191\n"
    "ds_write_b16 v190, v191 offset:32768\n"
    "s_bfe_u32 s94, s59, 0x180006\n"
    "s_mul_i32 s86, s94, s56\n"
    "v_mbcnt_lo_u32_b32 v184, s20, 0\n"
    "v_mbcnt_lo_u32_b32 v185, s22, 0\n"
    "v_mbcnt_lo_u32_b32 v186, s24, 0\n"
    "v_mbcnt_lo_u32_b32 v187, s26, 0\n"
    "s_bcnt1_i32_b64 s90, s[20:21]\n"
    "s_bcnt1_i32_b64 s91, s[22:23]\n"
    "s_bcnt1_i32_b64 s92, s[24:25]\n"
    "s_bcnt1_i32_b64 s93, s[26:27]\n"
    "v_mbcnt_hi_u32_b32 v184, s21, v184\n"
    "v_mbcnt_hi_u32_b32 v185, s23, v185\n"
    "v_mbcnt_hi_u32_b32 v186, s25, v186\n"
    "v_mbcnt_hi_u32_b32 v187, s27, v187\n"
    "s_lshl2_add_u32 s90, s90, s65\n"
    "s_lshl2_add_u32 s91, s91, s90\n"
    "s_lshl2_add_u32 s92, s92, s91\n"
    "s_lshl2_add_u32 s93, s93, s92\n"
    "v_add_u32_e64 v188, s86, v251\n"
    "v_pk_add_f32 v[230:231], v[144:145], v[146:147]\n"
    "v_pk_mul_f32 v[232:233], v[144:145], v[146:147]\n"
    "v_pk_fma_f32 v[148:149], v[148:149], s[70:71], v[100:101]\n"
    "v_pk_fma_f32 v[230:231], v[230:231], v[148:149], v[232:233]\n"
    "v_pk_mul_f32 v[232:233], v[232:233], v[148:149]\n"
    "v_lshl_add_u32 v184, v184, 2, s65\n"
    "v_lshl_add_u32 v185, v185, 2, s90\n"
    "v_lshl_add_u32 v186, v186, 2, s91\n"
    "v_lshl_add_u32 v187, v187, 2, s92\n"
    "v_mov_b32 v191, -1\n"
    "v_lshl_add_u32 v190, v249, 1, s93\n"
    "ds_write2_b32 v190, v191, v191 offset1:64\n"
    "s_mov_b64 exec, s[20:21]\n"
    "ds_write_b32 v184, v188\n"
    "s_mov_b64 exec, s[22:23]\n"
    "v_add_u32 v189, 64, v188\n"
    "ds_write_b32 v185, v189\n"
    "s_mov_b64 exec, s[24:25]\n"
    "v_add_u32 v190, 128, v188\n"
    "ds_write_b32 v186, v190\n"
    "s_mov_b64 exec, s[26:27]\n"
    "v_add_u32 v189, 192, v188\n"
    "ds_write_b32 v187, v189\n"
    "s_mov_b64 exec, -1\n"
    "s_add_u32 s95, s93, 60\n"
    "s_add_u32 s94, s82, 128\n"
    "s_andn2_b32 s95, s95, 63\n"
    "s_max_u32 s83, s95, s94\n"
    "v_pk_fma_f32 v[150:151], v[150:151], s[70:71], v[102:103]\n"
    "v_pk_fma_f32 v[230:231], v[230:231], v[150:151], v[232:233]\n"
    "v_pk_mul_f32 v[232:233], v[232:233], v[150:151]\n"
    "v_pk_fma_f32 v[152:153], v[152:153], s[70:71], v[104:105]\n"
    "v_pk_fma_f32 v[230:231], v[230:231], v[152:153], v[232:233]\n"
    "v_pk_mul_f32 v[232:233], v[232:233], v[152:153]\n"
    "v_pk_fma_f32 v[154:155], v[154:155], s[70:71], v[106:107]\n"
    "v_pk_fma_f32 v[230:231], v[230:231], v[154:155], v[232:233]\n"
    "v_pk_mul_f32 v[232:233], v[232:233], v[154:155]\n"
    "v_pk_fma_f32 v[156:157], v[156:157], s[70:71], v[108:109]\n"
    "v_pk_fma_f32 v[230:231], v[230:231], v[156:157], v[232:233]\n"
    "v_pk_mul_f32 v[232:233], v[232:233], v[156:157]\n"
    "v_pk_fma_f32 v[158:159], v[158:159], s[70:71], v[110:111]\n"
    "v_pk_fma_f32 v[230:231], v[230:231], v[158:159], v[232:233]\n"
    "v_pk_mul_f32 v[232:233], v[232:233], v[158:159]\n"
    "v_rcp_f32 v232, v232\n"
    "v_rcp_f32 v233, v233\n"
    "v_fma_f32 v230, v230, v232, v247\n"
    "v_fmac_f32 v230, v231, v233\n"
    "v_lshlrev_b32 v232, 2, v249\n"
    "v_lshrrev_b32 v233, 2, v249\n"
    "v_and_b32 v232, 56, v232\n"
    "v_and_b32 v233, 4, v233\n"
    "v_add3_u32 v232, v232, v233, s81\n"
    "ds_read_b32 v232, v232\n"
    "s_add_u32 s81, s81, 64\n"
    "s_waitcnt lgkmcnt(0)\n"
    "v_lshlrev_b32 v232, 1, v232\n"
    "buffer_load_ushort v232, v232, s[44:47], 0 offen\n"
    "s_waitcnt vmcnt(0)\n"
    "v_lshlrev_b32 v232, 16, v232\n"
    "v_mul_f32 v230, v230, v232\n"
    "v_cvt_pk_bf16_f32 v230, v230, v230\n"
    "s_add_u32 s67, s67, 64\n"
    "s_waitcnt lgkmcnt(0)\n"
    "ds_write_b16 v245, v230\n"
    "ds_read_b64 v[228:229], v246\n"
    "buffer_load_dwordx4 v[236:237], s[40:43], 0 idxen offen offset:0 nt lds\n"
    "buffer_load_dwordx4 v[238:239], s[40:43], 0 idxen offen offset:1024 nt lds\n"
    ".Ldin_B0s:\n"
    "v_mfma_f32_16x16x32_bf16 v[144:147], v[0:3], v[160:163], v[112:115]\n"
    "v_exp_f32 v128, v128 clamp\n"
    "v_mfma_f32_16x16x32_bf16 v[144:147], v[4:7], v[164:167], v[144:147]\n"
    "v_exp_f32 v129, v129 clamp\n"
    "v_mfma_f32_16x16x32_bf16 v[148:151], v[8:11], v[160:163], v[116:119]\n"
    "v_exp_f32 v130, v130 clamp\n"
    "v_mfma_f32_16x16x32_bf16 v[148:151], v[12:15], v[164:167], v[148:151]\n"
    "v_exp_f32 v131, v131 clamp\n"
    "v_mfma_f32_16x16x32_bf16 v[152:155], v[16:19], v[160:163], v[120:123]\n"
    "v_exp_f32 v132, v132 clamp\n"
    "v_mfma_f32_16x16x32_bf16 v[152:155], v[20:23], v[164:167], v[152:155]\n"
    "v_exp_f32 v133, v133 clamp\n"
    "v_mfma_f32_16x16x32_bf16 v[156:159], v[24:27], v[160:163], v[124:127]\n"
    "v_exp_f32 v134, v134 clamp\n"
    "v_mfma_f32_16x16x32_bf16 v[156:159], v[28:31], v[164:167], v[156:159]\n"
    "v_exp_f32 v135, v135 clamp\n"
    "v_exp_f32 v136, v136 clamp\n"
    "s_cmp_eq_u32 s67, s80\n"
    "s_cselect_b32 s67, s65, s67\n"
    "s_and_b32 s94, s59, 63\n"
    "s_mul_i32 s94, s94, 272\n"
    "v_add_u32 v252, s94, v250\n"
    "s_add_u32 m0, s68, 2048\n"
    "v_exp_f32 v137, v137 clamp\n"
    "v_add_u32 v253, s67, v244\n"
    "s_waitcnt lgkmcnt(0)\n"
    "v_mfma_f32_4x4x4_16b_bf16 v[224:227], v[228:229], v[176:177], 0 cbsz:4 abid:0\n"
    "v_exp_f32 v138, v138 clamp\n"
    "v_exp_f32 v139, v139 clamp\n"
    "v_mfma_f32_4x4x4_16b_bf16 v[224:227], v[228:229], v[178:179], v[224:227] cbsz:4 abid:1\n"
    "v_exp_f32 v140, v140 clamp\n"
    "v_exp_f32 v141, v141 clamp\n"
    "v_mfma_f32_4x4x4_16b_bf16 v[224:227], v[228:229], v[180:181], v[224:227] cbsz:4 abid:2\n"
    "v_exp_f32 v142, v142 clamp\n"
    "v_exp_f32 v143, v143 clamp\n"
    "v_mfma_f32_4x4x4_16b_bf16 v[224:227], v[228:229], v[182:183], v[224:227] cbsz:4 abid:3\n"
    "s_waitcnt vmcnt(4)\n"
    "s_memrealtime s[34:35]\n"
    "ds_read_b64_tr_b16 v[176:177], v240 offset:2048\n"
    "ds_read_b64_tr_b16 v[178:179], v241 offset:2304\n"
    "ds_read_b64_tr_b16 v[180:181], v242 offset:2560\n"
    "ds_read_b64_tr_b16 v[182:183], v243 offset:2816\n"
    "ds_read_b32 v236, v253 offset:0\n"
    "ds_read_b32 v238, v253 offset:4\n"
    "ds_read_b128 v[160:163], v234 offset:4096\n"
    "ds_read_b128 v[164:167], v235 offset:4096\n"
    "ds_read_b128 v[184:187], v252 offset:0\n"
    "ds_read_b128 v[188:191], v252 offset:16\n"
    "v_pk_fma_f32 v[128:129], v[128:129], s[70:71], v[96:97]\n"
    "v_pk_fma_f32 v[130:131], v[130:131], s[70:71], v[98:99]\n"
    "v_pk_add_f32 v[230:231], v[128:129], v[130:131]\n"
    "v_pk_mul_f32 v[232:233], v[128:129], v[130:131]\n"
    "v_pk_fma_f32 v[132:133], v[132:133], s[70:71], v[100:101]\n"
    "v_pk_fma_f32 v[230:231], v[230:231], v[132:133], v[232:233]\n"
    "v_pk_mul_f32 v[232:233], v[232:233], v[132:133]\n"
    "v_pk_fma_f32 v[134:135], v[134:135], s[70:71], v[102:103]\n"
    "v_pk_fma_f32 v[230:231], v[230:231], v[134:135], v[232:233]\n"
    "v_pk_mul_f32 v[232:233], v[232:233], v[134:135]\n"
    "v_pk_fma_f32 v[136:137], v[136:137], s[70:71], v[104:105]\n"
    "v_pk_fma_f32 v[230:231], v[230:231], v[136:137], v[232:233]\n"
    "v_pk_mul_f32 v[232:233], v[232:233], v[136:137]\n"
    "v_pk_fma_f32 v[138:139], v[138:139], s[70:71], v[106:107]\n"
    "v_pk_fma_f32 v[230:231], v[230:231], v[138:139], v[232:233]\n"
    "v_pk_mul_f32 v[232:233], v[232:233], v[138:139]\n"
    "v_pk_fma_f32 v[140:141], v[140:141], s[70:71], v[108:109]\n"
    "v_pk_fma_f32 v[230:231], v[230:231], v[140:141], v[232:233]\n"
    "v_pk_mul_f32 v[232:233], v[232:233], v[140:141]\n"
    "v_pk_fma_f32 v[142:143], v[142:143], s[70:71], v[110:111]\n"
    "v_pk_fma_f32 v[230:231], v[230:231], v[142:143], v[232:233]\n"
    "v_pk_mul_f32 v[232:233], v[232:233], v[142:143]\n"
    "v_rcp_f32 v232, v232\n"
    "v_rcp_f32 v233, v233\n"
    "v_fma_f32 v230, v230, v232, v247\n"
    "v_fmac_f32 v230, v231, v233\n"
    "v_lshlrev_b32 v232, 2, v249\n"
    "v_lshrrev_b32 v233, 2, v249\n"
    "v_and_b32 v232, 56, v232\n"
    "v_and_b32 v233, 4, v233\n"
    "v_add3_u32 v232, v232, v233, s81\n"
    "ds_read_b32 v232, v232\n"
    "s_add_u32 s81, s81, 64\n"
    "s_waitcnt lgkmcnt(0)\n"
    "v_lshlrev_b32 v232, 1, v232\n"
    "buffer_load_ushort v232, v232, s[44:47], 0 offen\n"
    "s_waitcnt vmcnt(0)\n"
    "v_lshlrev_b32 v232, 16, v232\n"
    "v_mul_f32 v230, v230, v232\n"
    "v_cvt_pk_bf16_f32 v230, v230, v230\n"
    "s_add_u32 s67, s67, 64\n"
    "s_waitcnt lgkmcnt(0)\n"
    "ds_write_b16 v245, v230\n"
    "ds_read_b64 v[228:229], v246\n"
    "s_cmp_le_u32 s34, s76\n"
    "s_cbranch_scc1 .Ldin_B0s_wait\n"
    ".Ldin_B0s_go:\n"
    "s_sub_u32 s94, s34, 60\n"
    "s_max_u32 s76, s76, s94\n"
    "s_add_u32 s76, s76, s72\n"
    ".Ldin_B0s_dma:\n"
    "buffer_load_dwordx4 v[236:237], s[40:43], 0 idxen offen offset:0 nt lds\n"
    "buffer_load_dwordx4 v[238:239], s[40:43], 0 idxen offen offset:1024 nt lds\n"
    ".Ldin_B0s_nd:\n"
    "s_branch .Ldin_G1s\n"
    ".Ldin_L2s_poll:\n"
    "s_sleep 1\n"
    "ds_read_b64 v[232:233], v253\n"
    "s_waitcnt lgkmcnt(0)\n"
    "v_readfirstlane_b32 s60, v232\n"
    "s_cmp_eq_u32 s60, -1\n"
    "s_cbranch_scc1 .Ldin_L2s_poll\n"
    "s_branch .Ldin_L2s_pr\n"
    ".Ldin_B0s_wait:\n"
    "s_cmp_lt_i32 s59, s57\n"
    "s_cbranch_scc1 .Ldin_B0s_w2\n"
    "s_sub_u32 s94, s67, s65\n"
    "s_cmp_gt_u32 s94, 256\n"
    "s_cbranch_scc1 .Ldin_B0s_dma\n"
    "s_cmp_eq_u32 s94, 64\n"
    "s_waitcnt vmcnt(2)\n"
    "s_cbranch_scc1 .Ldin_B0s_nd\n"
    "s_waitcnt vmcnt(0)\n"
    "s_branch .Ldin_B0s_nd\n"
    ".Ldin_B0s_slp:\n"
    "s_sleep 1\n"
    ".Ldin_B0s_w2:\n"
    "s_memrealtime s[34:35]\n"
    "s_waitcnt lgkmcnt(0)\n"
    "s_sub_u32 s94, s34, s76\n"
    "s_cmp_lt_i32 s94, 0\n"
    "s_cbranch_scc1 .Ldin_B0s_slp\n"
    "s_branch .Ldin_B0s_go\n"
    ".p2align 5\n"
    ".Ldin_L1s:\n"
    "s_setprio 0\n"
    "s_cmp_ge_i32 s59, s57\n"
    "s_cbranch_scc1 .Ldin_E1s\n"
    "s_add_u32 s76, s76, s73\n"
    "v_mov_b32 v253, s63\n"
    "v_and_b32 v184, v254, v184\n"
    "v_and_b32 v185, v255, v185\n"
    "v_and_b32 v186, v254, v186\n"
    "v_and_b32 v187, v255, v187\n"
    "v_and_b32 v188, v254, v188\n"
    "v_and_b32 v189, v255, v189\n"
    "v_and_b32 v190, v254, v190\n"
    "v_and_b32_e64 v191, v255, v191\n"
    "ds_read_b64 v[232:233], v253\n"
    "v_mfma_f32_4x4x4_16b_bf16 v[128:131], v[184:185], v[32:33], v[64:67]\n"
    "ds_read_b128 v[112:115], v252 offset:17408\n"
    "v_mfma_f32_4x4x4_16b_bf16 v[132:135], v[186:187], v[34:35], v[68:71]\n"
    "ds_read_b128 v[116:119], v252 offset:17424\n"
    "v_mfma_f32_4x4x4_16b_bf16 v[136:139], v[188:189], v[36:37], v[72:75]\n"
    "ds_read_b128 v[120:123], v252 offset:17536\n"
    "ds_read_b128 v[124:127], v252 offset:17552\n"
    "v_cvt_pk_bf16_f32 v0, v128, v129\n"
    "v_cvt_pk_bf16_f32 v1, v130, v131\n"
    "v_mfma_f32_4x4x4_16b_bf16 v[140:143], v[190:191], v[38:39], v[76:79]\n"
    "v_cvt_pk_bf16_f32 v2, v132, v133\n"
    "v_cvt_pk_bf16_f32 v3, v134, v135\n"
    "v_mfma_f32_4x4x4_16b_bf16 v[128:131], v[184:185], v[40:41], v[80:83]\n"
    "v_cvt_pk_bf16_f32 v4, v136, v137\n"
    "v_cvt_pk_bf16_f32 v5, v138, v139\n"
    "v_mfma_f32_4x4x4_16b_bf16 v[132:135], v[186:187], v[42:43], v[84:87]\n"
    "v_cvt_pk_bf16_f32 v6, v140, v141\n"
    "v_cvt_pk_bf16_f32 v7, v142, v143\n"
    "v_mfma_f32_4x4x4_16b_bf16 v[136:139], v[188:189], v[44:45], v[88:91]\n"
    "v_cvt_pk_bf16_f32 v8, v128, v129\n"
    "v_cvt_pk_bf16_f32 v9, v130, v131\n"
    "v_mfma_f32_4x4x4_16b_bf16 v[140:143], v[190:191], v[46:47], v[92:95]\n"
    "v_cvt_pk_bf16_f32 v10, v132, v133\n"
    "v_cvt_pk_bf16_f32 v11, v134, v135\n"
    "v_mfma_f32_4x4x4_16b_bf16 v[128:131], v[184:185], v[48:49], v[192:195]\n"
    "v_cvt_pk_bf16_f32 v12, v136, v137\n"
    "v_cvt_pk_bf16_f32 v13, v138, v139\n"
    "v_mfma_f32_4x4x4_16b_bf16 v[132:135], v[186:187], v[50:51], v[196:199]\n"
    "v_cvt_pk_bf16_f32 v14, v140, v141\n"
    "v_cvt_pk_bf16_f32 v15, v142, v143\n"
    "v_mfma_f32_4x4x4_16b_bf16 v[136:139], v[188:189], v[52:53], v[200:203]\n"
    "s_waitcnt lgkmcnt(0)\n"
    "v_readfirstlane_b32 s60, v232\n"
    "v_cvt_pk_bf16_f32 v16, v128, v129\n"
    "v_cvt_pk_bf16_f32 v17, v130, v131\n"
    "v_mfma_f32_4x4x4_16b_bf16 v[140:143], v[190:191], v[54:55], v[204:207]\n"
    "s_cmp_eq_u32 s60, -1\n"
    "s_cbranch_scc1 .Ldin_L1s_poll\n"
    ".Ldin_L1s_pr:\n"
    "v_readfirstlane_b32 s95, v233\n"
    "s_and_b32 s94, s60, 63\n"
    "s_lshl_b32 s94, s94, 6\n"
    "s_add_u32 s94, s94, 68480\n"
    "s_add_u32 s63, s63, 8\n"
    "v_lshl_add_u32 v252, v249, 1, s94\n"
    "ds_read_b32 v252, v252\n"
    "s_sub_u32 s95, s95, s76\n"
    "s_ashr_i32 s95, s95, 4\n"
    "s_min_i32 s95, s95, s74\n"
    "s_max_i32 s72, s95, s75\n"
    "v_cvt_pk_bf16_f32 v18, v132, v133\n"
    "v_cvt_pk_bf16_f32 v19, v134, v135\n"
    "v_mfma_f32_4x4x4_16b_bf16 v[128:131], v[184:185], v[56:57], v[208:211]\n"
    "v_cvt_pk_bf16_f32 v20, v136, v137\n"
    "v_cvt_pk_bf16_f32 v21, v138, v139\n"
    "v_mfma_f32_4x4x4_16b_bf16 v[132:135], v[186:187], v[58:59], v[212:215]\n"
    "v_cvt_pk_bf16_f32 v22, v140, v141\n"
    "v_cvt_pk_bf16_f32 v23, v142, v143\n"
    "v_mfma_f32_4x4x4_16b_bf16 v[136:139], v[188:189], v[60:61], v[216:219]\n"
    "v_cvt_pk_bf16_f32 v24, v128, v129\n"
    "v_cvt_pk_bf16_f32 v25, v130, v131\n"
    "v_mfma_f32_4x4x4_16b_bf16 v[140:143], v[190:191], v[62:63], v[220:223]\n"
    "v_cvt_pk_bf16_f32 v26, v132, v133\n"
    "v_cvt_pk_bf16_f32 v27, v134, v135\n"
    "v_cvt_pk_bf16_f32 v28, v136, v137\n"
    "v_cvt_pk_bf16_f32 v29, v138, v139\n"
    "v_exp_f32 v152, v152 clamp\n"
    "v_cvt_pk_bf16_f32 v30, v140, v141\n"
    "v_cvt_pk_bf16_f32 v31, v142, v143\n"
    "v_mfma_f32_16x16x32_bf16 v[128:131], v[0:3], v[160:163], v[112:115]\n"
    "v_exp_f32 v144, v144 clamp\n"
    "v_mfma_f32_16x16x32_bf16 v[128:131], v[4:7], v[164:167], v[128:131]\n"
    "v_exp_f32 v145, v145 clamp\n"
    "v_mfma_f32_16x16x32_bf16 v[132:135], v[8:11], v[160:163], v[116:119]\n"
    "v_exp_f32 v146, v146 clamp\n"
    "v_mfma_f32_16x16x32_bf16 v[132:135], v[12:15], v[164:167], v[132:135]\n"
    "v_exp_f32 v147, v147 clamp\n"
    "v_mfma_f32_16x16x32_bf16 v[136:139], v[16:19], v[160:163], v[120:123]\n"
    "v_exp_f32 v148, v148 clamp\n"
    "v_mfma_f32_16x16x32_bf16 v[136:139], v[20:23], v[164:167], v[136:139]\n"
    "v_exp_f32 v149, v149 clamp\n"
    "v_mfma_f32_16x16x32_bf16 v[140:143], v[24:27], v[160:163], v[124:127]\n"
    "v_exp_f32 v150, v150 clamp\n"
    "v_mfma_f32_16x16x32_bf16 v[140:143], v[28:31], v[164:167], v[140:143]\n"
    "v_exp_f32 v151, v151 clamp\n"
    "s_add_u32 m0, s68, 4096\n"
    "v_exp_f32 v153, v153 clamp\n"
    "v_add_u32 v253, s67, v244\n"
    "v_mfma_f32_4x4x4_16b_bf16 v[224:227], v[228:229], v[168:169], v[224:227] cbsz:4 abid:0\n"
    "v_exp_f32 v154, v154 clamp\n"
    "v_exp_f32 v155, v155 clamp\n"
    "v_mfma_f32_4x4x4_16b_bf16 v[224:227], v[228:229], v[170:171], v[224:227] cbsz:4 abid:1\n"
    "v_exp_f32 v156, v156 clamp\n"
    "v_exp_f32 v157, v157 clamp\n"
    "v_mfma_f32_4x4x4_16b_bf16 v[224:227], v[228:229], v[172:173], v[224:227] cbsz:4 abid:2\n"
    "v_exp_f32 v158, v158 clamp\n"
    "v_exp_f32 v159, v159 clamp\n"
    "v_mfma_f32_4x4x4_16b_bf16 v[224:227], v[228:229], v[174:175], v[224:227] cbsz:4 abid:3\n"
    "s_waitcnt vmcnt(4)\n"
    "ds_read_b64_tr_b16 v[168:169], v240 offset:4096\n"
    "ds_read_b64_tr_b16 v[170:171], v241 offset:4352\n"
    "ds_read_b64_tr_b16 v[172:173], v242 offset:4608\n"
    "ds_read_b64_tr_b16 v[174:175], v243 offset:4864\n"
    "ds_read_b32 v236, v253 offset:0\n"
    "ds_read_b32 v238, v253 offset:4\n"
    "ds_read_b128 v[160:163], v234 offset:6144\n"
    "ds_read_b128 v[164:167], v235 offset:6144\n"
    "s_waitcnt lgkmcnt(8)\n"
    "s_nop 0\n"
    "v_readlane_b32 s20, v252, 0\n"
    "v_readlane_b32 s21, v252, 1\n"
    "v_readlane_b32 s22, v252, 2\n"
    "v_readlane_b32 s23, v252, 3\n"
    "v_readlane_b32 s24, v252, 4\n"
    "v_readlane_b32 s25, v252, 5\n"
    "v_readlane_b32 s26, v252, 6\n"
    "v_readlane_b32 s27, v252, 7\n"
    "v_pk_fma_f32 v[144:145], v[144:145], s[70:71], v[96:97]\n"
    "v_pk_fma_f32 v[146:147], v[146:147], s[70:71], v[98:99]\n"
    "v_pk_add_f32 v[230:231], v[144:145], v[146:147]\n"
    "v_pk_mul_f32 v[232:233], v[144:145], v[146:147]\n"
    "v_pk_fma_f32 v[148:149], v[148:149], s[70:71], v[100:101]\n"
    "v_pk_fma_f32 v[230:231], v[230:231], v[148:149], v[232:233]\n"
    "v_pk_mul_f32 v[232:233], v[232:233], v[148:149]\n"
    "v_pk_fma_f32 v[150:151], v[150:151], s[70:71], v[102:103]\n"
    "v_pk_fma_f32 v[230:231], v[230:231], v[150:151], v[232:233]\n"
    "v_pk_mul_f32 v[232:233], v[232:233], v[150:151]\n"
    "v_pk_fma_f32 v[152:153], v[152:153], s[70:71], v[104:105]\n"
    "v_pk_fma_f32 v[230:231], v[230:231], v[152:153], v[232:233]\n"
    "v_pk_mul_f32 v[232:233], v[232:233], v[152:153]\n"
    "v_pk_fma_f32 v[154:155], v[154:155], s[70:71], v[106:107]\n"
    "v_pk_fma_f32 v[230:231], v[230:231], v[154:155], v[232:233]\n"
    "v_pk_mul_f32 v[232:233], v[232:233], v[154:155]\n"
    "v_pk_fma_f32 v[156:157], v[156:157], s[70:71], v[108:109]\n"
    "v_pk_fma_f32 v[230:231], v[230:231], v[156:157], v[232:233]\n"
    "v_pk_mul_f32 v[232:233], v[232:233], v[156:157]\n"
    "v_pk_fma_f32 v[158:159], v[158:159], s[70:71], v[110:111]\n"
    "v_pk_fma_f32 v[230:231], v[230:231], v[158:159], v[232:233]\n"
    "v_pk_mul_f32 v[232:233], v[232:233], v[158:159]\n"
    "v_rcp_f32 v232, v232\n"
    "v_rcp_f32 v233, v233\n"
    "v_fma_f32 v230, v230, v232, v247\n"
    "v_fmac_f32 v230, v231, v233\n"
    "v_lshlrev_b32 v232, 2, v249\n"
    "v_lshrrev_b32 v233, 2, v249\n"
    "v_and_b32 v232, 56, v232\n"
    "v_and_b32 v233, 4, v233\n"
    "v_add3_u32 v232, v232, v233, s81\n"
    "ds_read_b32 v232, v232\n"
    "s_add_u32 s81, s81, 64\n"
    "s_waitcnt lgkmcnt(0)\n"
    "v_lshlrev_b32 v232, 1, v232\n"
    "buffer_load_ushort v232, v232, s[44:47], 0 offen\n"
    "s_waitcnt vmcnt(0)\n"
    "v_lshlrev_b32 v232, 16, v232\n"
    "v_mul_f32 v230, v230, v232\n"
    "v_cvt_pk_bf16_f32 v230, v230, v230\n"
    "s_add_u32 s67, s67, 64\n"
    "s_waitcnt lgkmcnt(0)\n"
    "ds_write_b16 v245, v230\n"
    "ds_read_b64 v[228:229], v246\n"
    "s_lshl1_add_u32 s76, s72, s76\n"
    "buffer_load_dwordx4 v[236:237], s[40:43], 0 idxen offen offset:0 nt lds\n"
    "buffer_load_dwordx4 v[238:239], s[40:43], 0 idxen offen offset:1024 nt lds\n"
    "s_and_b32 s62, s58, 63\n"
    "s_mul_i32 s62, s62, 272\n"
    "s_mov_b32 s58, s59\n"
    "s_mov_b32 s59, s60\n"
    "s_mov_b32 s81, s65\n"
    "s_xor_b32 s65, s65, s64\n"
    "s_mov_b32 s80, s83\n"
    "s_add_u32 s82, s65, 256\n"
    "s_cmp_lt_i32 s59, s57\n"
    "s_cselect_b32 s76, s76, -1\n"
    "s_bitcmp1_b32 s58, 30\n"
    "s_cbranch_scc0 .Ldin_A2\n"
    ".Ldin_A2s:\n"
    "v_mfma_f32_16x16x32_bf16 v[144:147], v[0:3], v[160:163], v[112:115]\n"
    "v_exp_f32 v128, v128 clamp\n"
    "v_mfma_f32_16x16x32_bf16 v[144:147], v[4:7], v[164:167], v[144:147]\n"
    "v_exp_f32 v129, v129 clamp\n"
    "v_mfma_f32_16x16x32_bf16 v[148:151], v[8:11], v[160:163], v[116:119]\n"
    "v_exp_f32 v130, v130 clamp\n"
    "v_mfma_f32_16x16x32_bf16 v[148:151], v[12:15], v[164:167], v[148:151]\n"
    "v_exp_f32 v131, v131 clamp\n"
    "v_mfma_f32_16x16x32_bf16 v[152:155], v[16:19], v[160:163], v[120:123]\n"
    "v_exp_f32 v132, v132 clamp\n"
    "v_mfma_f32_16x16x32_bf16 v[152:155], v[20:23], v[164:167], v[152:155]\n"
    "v_exp_f32 v133, v133 clamp\n"
    "v_mfma_f32_16x16x32_bf16 v[156:159], v[24:27], v[160:163], v[124:127]\n"
    "v_exp_f32 v134, v134 clamp\n"
    "v_mfma_f32_16x16x32_bf16 v[156:159], v[28:31], v[164:167], v[156:159]\n"
    "v_exp_f32 v135, v135 clamp\n"
    "v_exp_f32 v136, v136 clamp\n"
    "s_add_u32 m0, s68, 6144\n"
    "v_exp_f32 v137, v137 clamp\n"
    "v_add_u32_e64 v253, s67, v244\n"
    "s_waitcnt lgkmcnt(0)\n"
    "v_mfma_f32_4x4x4_16b_bf16 v[224:227], v[228:229], v[176:177], v[224:227] cbsz:4 abid:0\n"
    "v_exp_f32 v138, v138 clamp\n"
    "v_exp_f32 v139, v139 clamp\n"
    "v_mfma_f32_4x4x4_16b_bf16 v[224:227], v[228:229], v[178:179], v[224:227] cbsz:4 abid:1\n"
    "v_exp_f32 v140, v140 clamp\n"
    "v_exp_f32 v141, v141 clamp\n"
    "v_mfma_f32_4x4x4_16b_bf16 v[224:227], v[228:229], v[180:181], v[224:227] cbsz:4 abid:2\n"
    "v_exp_f32 v142, v142 clamp\n"
    "v_exp_f32 v143, v143 clamp\n"
    "v_mfma_f32_4x4x4_16b_bf16 v[224:227], v[228:229], v[182:183], v[224:227] cbsz:4 abid:3\n"
    "s_waitcnt vmcnt(4)\n"
    "ds_read_b64_tr_b16 v[176:177], v240 offset:6144\n"
    "ds_read_b64_tr_b16 v[178:179], v241 offset:6400\n"
    "ds_read_b64_tr_b16 v[180:181], v242 offset:6656\n"
    "ds_read_b64_tr_b16 v[182:183], v243 offset:6912\n"
    "ds_read_b32 v236, v253 offset:0\n"
    "ds_read_b32 v238, v253 offset:4\n"
    "ds_read_b128 v[160:163], v234 offset:0\n"
    "ds_read_b128 v[164:167], v235 offset:0\n"
    "s_cbranch_vccz .Ldin_A2s_np\n"
    "s_setprio 1\n"
    ".Ldin_A2s_np:\n"
    "v_pk_fma_f32 v[128:129], v[128:129], s[70:71], v[96:97]\n"
    "v_pk_fma_f32 v[130:131], v[130:131], s[70:71], v[98:99]\n"
    "v_add_f32 v191, v224, v225\n"
    "v_add_f32 v190, v226, v227\n"
    "v_add_f32 v191, v191, v190\n"
    "v_add_u32 v190, s62, v249\n"
    "v_mul_f32 v191, s69, v191\n"
    "v_cvt_pk_bf16_f32 v191, v191, v191\n"
    "ds_write_b16 v190, v191 offset:32768\n"
    "s_bfe_u32 s94, s59, 0x180006\n"
    "s_mul_i32 s86, s94, s56\n"
    "v_mbcnt_lo_u32_b32 v184, s20, 0\n"
    "v_mbcnt_lo_u32_b32 v185, s22, 0\n"
    "v_mbcnt_lo_u32_b32 v186, s24, 0\n"
    "v_mbcnt_lo_u32_b32 v187, s26, 0\n"
    "s_bcnt1_i32_b64 s90, s[20:21]\n"
    "s_bcnt1_i32_b64 s91, s[22:23]\n"
    "s_bcnt1_i32_b64 s92, s[24:25]\n"
    "s_bcnt1_i32_b64 s93, s[26:27]\n"
    "v_mbcnt_hi_u32_b32 v184, s21, v184\n"
    "v_mbcnt_hi_u32_b32 v185, s23, v185\n"
    "v_mbcnt_hi_u32_b32 v186, s25, v186\n"
    "v_mbcnt_hi_u32_b32 v187, s27, v187\n"
    "s_lshl2_add_u32 s90, s90, s65\n"
    "s_lshl2_add_u32 s91, s91, s90\n"
    "s_lshl2_add_u32 s92, s92, s91\n"
    "s_lshl2_add_u32 s93, s93, s92\n"
    "v_add_u32_e64 v188, s86, v251\n"
    "v_pk_add_f32 v[230:231], v[128:129], v[130:131]\n"
    "v_pk_mul_f32 v[232:233], v[128:129], v[130:131]\n"
    "v_pk_fma_f32 v[132:133], v[132:133], s[70:71], v[100:101]\n"
    "v_pk_fma_f32 v[230:231], v[230:231], v[132:133], v[232:233]\n"
    "v_pk_mul_f32 v[232:233], v[232:233], v[132:133]\n"
    "v_lshl_add_u32 v184, v184, 2, s65\n"
    "v_lshl_add_u32 v185, v185, 2, s90\n"
    "v_lshl_add_u32 v186, v186, 2, s91\n"
    "v_lshl_add_u32 v187, v187, 2, s92\n"
    "v_mov_b32 v191, -1\n"
    "v_lshl_add_u32 v190, v249, 1, s93\n"
    "ds_write2_b32 v190, v191, v191 offset1:64\n"
    "s_mov_b64 exec, s[20:21]\n"
    "ds_write_b32 v184, v188\n"
    "s_mov_b64 exec, s[22:23]\n"
    "v_add_u32 v189, 64, v188\n"
    "ds_write_b32 v185, v189\n"
    "s_mov_b64 exec, s[24:25]\n"
    "v_add_u32 v190, 128, v188\n"
    "ds_write_b32 v186, v190\n"
    "s_mov_b64 exec, s[26:27]\n"
    "v_add_u32 v189, 192, v188\n"
    "ds_write_b32 v187, v189\n"
    "s_mov_b64 exec, -1\n"
    "s_add_u32 s95, s93, 60\n"
    "s_add_u32 s94, s82, 128\n"
    "s_andn2_b32 s95, s95, 63\n"
    "s_max_u32 s83, s95, s94\n"
    "v_pk_fma_f32 v[134:135], v[134:135], s[70:71], v[102:103]\n"
    "v_pk_fma_f32 v[230:231], v[230:231], v[134:135], v[232:233]\n"
    "v_pk_mul_f32 v[232:233], v[232:233], v[134:135]\n"
    "v_pk_fma_f32 v[136:137], v[136:137], s[70:71], v[104:105]\n"
    "v_pk_fma_f32 v[230:231], v[230:231], v[136:137], v[232:233]\n"
    "v_pk_mul_f32 v[232:233], v[232:233], v[136:137]\n"
    "v_pk_fma_f32 v[138:139], v[138:139], s[70:71], v[106:107]\n"
    "v_pk_fma_f32 v[230:231], v[230:231], v[138:139], v[232:233]\n"
    "v_pk_mul_f32 v[232:233], v[232:233], v[138:139]\n"
    "v_pk_fma_f32 v[140:141], v[140:141], s[70:71], v[108:109]\n"
    "v_pk_fma_f32 v[230:231], v[230:231], v[140:141], v[232:233]\n"
    "v_pk_mul_f32 v[232:233], v[232:233], v[140:141]\n"
    "v_pk_fma_f32 v[142:143], v[142:143], s[70:71], v[110:111]\n"
    "v_pk_fma_f32 v[230:231], v[230:231], v[142:143], v[232:233]\n"
    "v_pk_mul_f32 v[232:233], v[232:233], v[142:143]\n"
    "v_rcp_f32 v232, v232\n"
    "v_rcp_f32 v233, v233\n"
    "v_fma_f32 v230, v230, v232, v247\n"
    "v_fmac_f32 v230, v231, v233\n"
    "v_lshlrev_b32 v232, 2, v249\n"
    "v_lshrrev_b32 v233, 2, v249\n"
    "v_and_b32 v232, 56, v232\n"
    "v_and_b32 v233, 4, v233\n"
    "v_add3_u32 v232, v232, v233, s81\n"
    "ds_read_b32 v232, v232\n"
    "s_add_u32 s81, s81, 64\n"
    "s_waitcnt lgkmcnt(0)\n"
    "v_lshlrev_b32 v232, 1, v232\n"
    "buffer_load_ushort v232, v232, s[44:47], 0 offen\n"
    "s_waitcnt vmcnt(0)\n"
    "v_lshlrev_b32 v232, 16, v232\n"
    "v_mul_f32 v230, v230, v232\n"
    "v_cvt_pk_bf16_f32 v230, v230, v230\n"
    "s_add_u32 s67, s67, 64\n"
    "s_waitcnt lgkmcnt(0)\n"
    "ds_write_b16 v245, v230\n"
    "ds_read_b64 v[228:229], v246\n"
    "buffer_load_dwordx4 v[236:237], s[40:43], 0 idxen offen offset:0 nt lds\n"
    "buffer_load_dwordx4 v[238:239], s[40:43], 0 idxen offen offset:1024 nt lds\n"
    ".Ldin_B3s:\n"
    "v_mfma_f32_16x16x32_bf16 v[128:131], v[0:3], v[160:163], v[112:115]\n"
    "v_exp_f32 v144, v144 clamp\n"
    "v_mfma_f32_16x16x32_bf16 v[128:131], v[4:7], v[164:167], v[128:131]\n"
    "v_exp_f32 v145, v145 clamp\n"
    "v_mfma_f32_16x16x32_bf16 v[132:135], v[8:11], v[160:163], v[116:119]\n"
    "v_exp_f32 v146, v146 clamp\n"
    "v_mfma_f32_16x16x32_bf16 v[132:135], v[12:15], v[164:167], v[132:135]\n"
    "v_exp_f32 v147, v147 clamp\n"
    "v_mfma_f32_16x16x32_bf16 v[136:139], v[16:19], v[160:163], v[120:123]\n"
    "v_exp_f32 v148, v148 clamp\n"
    "v_mfma_f32_16x16x32_bf16 v[136:139], v[20:23], v[164:167], v[136:139]\n"
    "v_exp_f32 v149, v149 clamp\n"
    "v_mfma_f32_16x16x32_bf16 v[140:143], v[24:27], v[160:163], v[124:127]\n"
    "v_exp_f32 v150, v150 clamp\n"
    "v_mfma_f32_16x16x32_bf16 v[140:143], v[28:31], v[164:167], v[140:143]\n"
    "v_exp_f32 v151, v151 clamp\n"
    "v_exp_f32 v152, v152 clamp\n"
    "s_cmp_eq_u32 s67, s80\n"
    "s_cselect_b32 s67, s65, s67\n"
    "s_and_b32 s94, s59, 63\n"
    "s_mul_i32 s94, s94, 272\n"
    "v_add_u32 v252, s94, v250\n"
    "s_add_u32 m0, s68, 0\n"
    "v_exp_f32 v153, v153 clamp\n"
    "v_add_u32_e64 v253, s67, v244\n"
    "s_waitcnt lgkmcnt(0)\n"
    "v_mfma_f32_4x4x4_16b_bf16 v[224:227], v[228:229], v[168:169], 0 cbsz:4 abid:0\n"
    "v_exp_f32 v154, v154 clamp\n"
    "v_exp_f32 v155, v155 clamp\n"
    "v_mfma_f32_4x4x4_16b_bf16 v[224:227], v[228:229], v[170:171], v[224:227] cbsz:4 abid:1\n"
    "v_exp_f32 v156, v156 clamp\n"
    "v_exp_f32 v157, v157 clamp\n"
    "v_mfma_f32_4x4x4_16b_bf16 v[224:227], v[228:229], v[172:173], v[224:227] cbsz:4 abid:2\n"
    "v_exp_f32 v158, v158 clamp\n"
    "v_exp_f32 v159, v159 clamp\n"
    "v_mfma_f32_4x4x4_16b_bf16 v[224:227], v[228:229], v[174:175], v[224:227] cbsz:4 abid:3\n"
    "s_waitcnt vmcnt(4)\n"
    "s_memrealtime s[34:35]\n"
    "ds_read_b64_tr_b16 v[168:169], v240 offset:0\n"
    "ds_read_b64_tr_b16 v[170:171], v241 offset:256\n"
    "ds_read_b64_tr_b16 v[172:173], v242 offset:512\n"
    "ds_read_b64_tr_b16 v[174:175], v243 offset:768\n"
    "ds_read_b32 v236, v253 offset:0\n"
    "ds_read_b32 v238, v253 offset:4\n"
    "ds_read_b128 v[160:163], v234 offset:2048\n"
    "ds_read_b128 v[164:167], v235 offset:2048\n"
    "ds_read_b128 v[184:187], v252 offset:0\n"
    "ds_read_b128 v[188:191], v252 offset:16\n"
    "v_pk_fma_f32 v[144:145], v[144:145], s[70:71], v[96:97]\n"
    "v_pk_fma_f32 v[146:147], v[146:147], s[70:71], v[98:99]\n"
    "v_pk_add_f32 v[230:231], v[144:145], v[146:147]\n"
    "v_pk_mul_f32 v[232:233], v[144:145], v[146:147]\n"
    "v_pk_fma_f32 v[148:149], v[148:149], s[70:71], v[100:101]\n"
    "v_pk_fma_f32 v[230:231], v[230:231], v[148:149], v[232:233]\n"
    "v_pk_mul_f32 v[232:233], v[232:233], v[148:149]\n"
    "v_pk_fma_f32 v[150:151], v[150:151], s[70:71], v[102:103]\n"
    "v_pk_fma_f32 v[230:231], v[230:231], v[150:151], v[232:233]\n"
    "v_pk_mul_f32 v[232:233], v[232:233], v[150:151]\n"
    "v_pk_fma_f32 v[152:153], v[152:153], s[70:71], v[104:105]\n"
    "v_pk_fma_f32 v[230:231], v[230:231], v[152:153], v[232:233]\n"
    "v_pk_mul_f32 v[232:233], v[232:233], v[152:153]\n"
    "v_pk_fma_f32 v[154:155], v[154:155], s[70:71], v[106:107]\n"
    "v_pk_fma_f32 v[230:231], v[230:231], v[154:155], v[232:233]\n"
    "v_pk_mul_f32 v[232:233], v[232:233], v[154:155]\n"
    "v_pk_fma_f32 v[156:157], v[156:157], s[70:71], v[108:109]\n"
    "v_pk_fma_f32 v[230:231], v[230:231], v[156:157], v[232:233]\n"
    "v_pk_mul_f32 v[232:233], v[232:233], v[156:157]\n"
    "v_pk_fma_f32 v[158:159], v[158:159], s[70:71], v[110:111]\n"
    "v_pk_fma_f32 v[230:231], v[230:231], v[158:159], v[232:233]\n"
    "v_pk_mul_f32 v[232:233], v[232:233], v[158:159]\n"
    "v_rcp_f32 v232, v232\n"
    "v_rcp_f32 v233, v233\n"
    "v_fma_f32 v230, v230, v232, v247\n"
    "v_fmac_f32 v230, v231, v233\n"
    "v_lshlrev_b32 v232, 2, v249\n"
    "v_lshrrev_b32 v233, 2, v249\n"
    "v_and_b32 v232, 56, v232\n"
    "v_and_b32 v233, 4, v233\n"
    "v_add3_u32 v232, v232, v233, s81\n"
    "ds_read_b32 v232, v232\n"
    "s_add_u32 s81, s81, 64\n"
    "s_waitcnt lgkmcnt(0)\n"
    "v_lshlrev_b32 v232, 1, v232\n"
    "buffer_load_ushort v232, v232, s[44:47], 0 offen\n"
    "s_waitcnt vmcnt(0)\n"
    "v_lshlrev_b32 v232, 16, v232\n"
    "v_mul_f32 v230, v230, v232\n"
    "v_cvt_pk_bf16_f32 v230, v230, v230\n"
    "s_add_u32 s67, s67, 64\n"
    "s_waitcnt lgkmcnt(0)\n"
    "ds_write_b16 v245, v230\n"
    "ds_read_b64 v[228:229], v246\n"
    "s_cmp_le_u32 s34, s76\n"
    "s_cbranch_scc1 .Ldin_B3s_wait\n"
    ".Ldin_B3s_go:\n"
    "s_sub_u32 s94, s34, 60\n"
    "s_max_u32 s76, s76, s94\n"
    "s_add_u32 s76, s76, s72\n"
    ".Ldin_B3s_dma:\n"
    "buffer_load_dwordx4 v[236:237], s[40:43], 0 idxen offen offset:0 nt lds\n"
    "buffer_load_dwordx4 v[238:239], s[40:43], 0 idxen offen offset:1024 nt lds\n"
    ".Ldin_B3s_nd:\n"
    "s_branch .Ldin_G0s\n"
    ".Ldin_L1s_poll:\n"
    "s_sleep 1\n"
    "ds_read_b64 v[232:233], v253\n"
    "s_waitcnt lgkmcnt(0)\n"
    "v_readfirstlane_b32 s60, v232\n"
    "s_cmp_eq_u32 s60, -1\n"
    "s_cbranch_scc1 .Ldin_L1s_poll\n"
    "s_branch .Ldin_L1s_pr\n"
    ".Ldin_B3s_wait:\n"
    "s_cmp_lt_i32 s59, s57\n"
    "s_cbranch_scc1 .Ldin_B3s_w2\n"
    "s_sub_u32 s94, s67, s65\n"
    "s_cmp_gt_u32 s94, 256\n"
    "s_cbranch_scc1 .Ldin_B3s_dma\n"
    "s_cmp_eq_u32 s94, 64\n"
    "s_waitcnt vmcnt(2)\n"
    "s_cbranch_scc1 .Ldin_B3s_nd\n"
    "s_waitcnt vmcnt(0)\n"
    "s_branch .Ldin_B3s_nd\n"
    ".Ldin_B3s_slp:\n"
    "s_sleep 1\n"
    ".Ldin_B3s_w2:\n"
    "s_memrealtime s[34:35]\n"
    "s_waitcnt lgkmcnt(0)\n"
    "s_sub_u32 s94, s34, s76\n"
    "s_cmp_lt_i32 s94, 0\n"
    "s_cbranch_scc1 .Ldin_B3s_slp\n"
    "s_branch .Ldin_B3s_go\n"
    ".p2align 5\n"
    ".Ldin_L0s:\n"
    "s_setprio 0\n"
    "s_cmp_ge_i32 s59, s57\n"
    "s_cbranch_scc1 .Ldin_E0s\n"
    "s_add_u32 s76, s76, s73\n"
    "v_mov_b32 v253, s63\n"
    "v_and_b32 v184, v254, v184\n"
    "v_and_b32 v185, v255, v185\n"
    "v_and_b32 v186, v254, v186\n"
    "v_and_b32 v187, v255, v187\n"
    "v_and_b32 v188, v254, v188\n"
    "v_and_b32 v189, v255, v189\n"
    "v_and_b32 v190, v254, v190\n"
    "v_and_b32_e64 v191, v255, v191\n"
    "ds_read_b64 v[232:233], v253\n"
    "v_mfma_f32_4x4x4_16b_bf16 v[144:147], v[184:185], v[32:33], v[64:67]\n"
    "ds_read_b128 v[112:115], v252 offset:17408\n"
    "v_mfma_f32_4x4x4_16b_bf16 v[148:151], v[186:187], v[34:35], v[68:71]\n"
    "ds_read_b128 v[116:119], v252 offset:17424\n"
    "v_mfma_f32_4x4x4_16b_bf16 v[152:155], v[188:189], v[36:37], v[72:75]\n"
    "ds_read_b128 v[120:123], v252 offset:17536\n"
    "ds_read_b128 v[124:127], v252 offset:17552\n"
    "v_cvt_pk_bf16_f32 v0, v144, v145\n"
    "v_cvt_pk_bf16_f32 v1, v146, v147\n"
    "v_mfma_f32_4x4x4_16b_bf16 v[156:159], v[190:191], v[38:39], v[76:79]\n"
    "v_cvt_pk_bf16_f32 v2, v148, v149\n"
    "v_cvt_pk_bf16_f32 v3, v150, v151\n"
    "v_mfma_f32_4x4x4_16b_bf16 v[144:147], v[184:185], v[40:41], v[80:83]\n"
    "v_cvt_pk_bf16_f32 v4, v152, v153\n"
    "v_cvt_pk_bf16_f32 v5, v154, v155\n"
    "v_mfma_f32_4x4x4_16b_bf16 v[148:151], v[186:187], v[42:43], v[84:87]\n"
    "v_cvt_pk_bf16_f32 v6, v156, v157\n"
    "v_cvt_pk_bf16_f32 v7, v158, v159\n"
    "v_mfma_f32_4x4x4_16b_bf16 v[152:155], v[188:189], v[44:45], v[88:91]\n"
    "v_cvt_pk_bf16_f32 v8, v144, v145\n"
    "v_cvt_pk_bf16_f32 v9, v146, v147\n"
    "v_mfma_f32_4x4x4_16b_bf16 v[156:159], v[190:191], v[46:47], v[92:95]\n"
    "v_cvt_pk_bf16_f32 v10, v148, v149\n"
    "v_cvt_pk_bf16_f32 v11, v150, v151\n"
    "v_mfma_f32_4x4x4_16b_bf16 v[144:147], v[184:185], v[48:49], v[192:195]\n"
    "v_cvt_pk_bf16_f32 v12, v152, v153\n"
    "v_cvt_pk_bf16_f32 v13, v154, v155\n"
    "v_mfma_f32_4x4x4_16b_bf16 v[148:151], v[186:187], v[50:51], v[196:199]\n"
    "v_cvt_pk_bf16_f32 v14, v156, v157\n"
    "v_cvt_pk_bf16_f32 v15, v158, v159\n"
    "v_mfma_f32_4x4x4_16b_bf16 v[152:155], v[188:189], v[52:53], v[200:203]\n"
    "s_waitcnt lgkmcnt(0)\n"
    "v_readfirstlane_b32 s60, v232\n"
    "v_cvt_pk_bf16_f32 v16, v144, v145\n"
    "v_cvt_pk_bf16_f32 v17, v146, v147\n"
    "v_mfma_f32_4x4x4_16b_bf16 v[156:159], v[190:191], v[54:55], v[204:207]\n"
    "s_cmp_eq_u32 s60, -1\n"
    "s_cbranch_scc1 .Ldin_L0s_poll\n"
    ".Ldin_L0s_pr:\n"
    "v_readfirstlane_b32 s95, v233\n"
    "s_and_b32 s94, s60, 63\n"
    "s_lshl_b32 s94, s94, 6\n"
    "s_add_u32 s94, s94, 68480\n"
    "s_add_u32 s63, s63, 8\n"
    "v_lshl_add_u32 v252, v249, 1, s94\n"
    "ds_read_b32 v252, v252\n"
    "s_sub_u32 s95, s95, s76\n"
    "s_ashr_i32 s95, s95, 4\n"
    "s_min_i32 s95, s95, s74\n"
    "s_max_i32 s72, s95, s75\n"
    "v_cvt_pk_bf16_f32 v18, v148, v149\n"
    "v_cvt_pk_bf16_f32 v19, v150, v151\n"
    "v_mfma_f32_4x4x4_16b_bf16 v[144:147], v[184:185], v[56:57], v[208:211]\n"
    "v_cvt_pk_bf16_f32 v20, v152, v153\n"
    "v_cvt_pk_bf16_f32 v21, v154, v155\n"
    "v_mfma_f32_4x4x4_16b_bf16 v[148:151], v[186:187], v[58:59], v[212:215]\n"
    "v_cvt_pk_bf16_f32 v22, v156, v157\n"
    "v_cvt_pk_bf16_f32 v23, v158, v159\n"
    "v_mfma_f32_4x4x4_16b_bf16 v[152:155], v[188:189], v[60:61], v[216:219]\n"
    "v_cvt_pk_bf16_f32 v24, v144, v145\n"
    "v_cvt_pk_bf16_f32 v25, v146, v147\n"
    "v_mfma_f32_4x4x4_16b_bf16 v[156:159], v[190:191], v[62:63], v[220:223]\n"
    "v_cvt_pk_bf16_f32 v26, v148, v149\n"
    "v_cvt_pk_bf16_f32 v27, v150, v151\n"
    "v_cvt_pk_bf16_f32 v28, v152, v153\n"
    "v_cvt_pk_bf16_f32 v29, v154, v155\n"
    "v_exp_f32 v136, v136 clamp\n"
    "v_cvt_pk_bf16_f32 v30, v156, v157\n"
    "v_cvt_pk_bf16_f32 v31, v158, v159\n"
    "v_mfma_f32_16x16x32_bf16 v[144:147], v[0:3], v[160:163], v[112:115]\n"
    "v_exp_f32 v128, v128 clamp\n"
    "v_mfma_f32_16x16x32_bf16 v[144:147], v[4:7], v[164:167], v[144:147]\n"
    "v_exp_f32 v129, v129 clamp\n"
    "v_mfma_f32_16x16x32_bf16 v[148:151], v[8:11], v[160:163], v[116:119]\n"
    "v_exp_f32 v130, v130 clamp\n"
    "v_mfma_f32_16x16x32_bf16 v[148:151], v[12:15], v[164:167], v[148:151]\n"
    "v_exp_f32 v131, v131 clamp\n"
    "v_mfma_f32_16x16x32_bf16 v[152:155], v[16:19], v[160:163], v[120:123]\n"
    "v_exp_f32 v132, v132 clamp\n"
    "v_mfma_f32_16x16x32_bf16 v[152:155], v[20:23], v[164:167], v[152:155]\n"
    "v_exp_f32 v133, v133 clamp\n"
    "v_mfma_f32_16x16x32_bf16 v[156:159], v[24:27], v[160:163], v[124:127]\n"
    "v_exp_f32 v134, v134 clamp\n"
    "v_mfma_f32_16x16x32_bf16 v[156:159], v[28:31], v[164:167], v[156:159]\n"
    "v_exp_f32 v135, v135 clamp\n"
    "s_add_u32 m0, s68, 2048\n"
    "v_exp_f32 v137, v137 clamp\n"
    "v_add_u32 v253, s67, v244\n"
    "v_mfma_f32_4x4x4_16b_bf16 v[224:227], v[228:229], v[176:177], v[224:227] cbsz:4 abid:0\n"
    "v_exp_f32 v138, v138 clamp\n"
    "v_exp_f32 v139, v139 clamp\n"
    "v_mfma_f32_4x4x4_16b_bf16 v[224:227], v[228:229], v[178:179], v[224:227] cbsz:4 abid:1\n"
    "v_exp_f32 v140, v140 clamp\n"
    "v_exp_f32 v141, v141 clamp\n"
    "v_mfma_f32_4x4x4_16b_bf16 v[224:227], v[228:229], v[180:181], v[224:227] cbsz:4 abid:2\n"
    "v_exp_f32 v142, v142 clamp\n"
    "v_exp_f32 v143, v143 clamp\n"
    "v_mfma_f32_4x4x4_16b_bf16 v[224:227], v[228:229], v[182:183], v[224:227] cbsz:4 abid:3\n"
    "s_waitcnt vmcnt(4)\n"
    "ds_read_b64_tr_b16 v[176:177], v240 offset:2048\n"
    "ds_read_b64_tr_b16 v[178:179], v241 offset:2304\n"
    "ds_read_b64_tr_b16 v[180:181], v242 offset:2560\n"
    "ds_read_b64_tr_b16 v[182:183], v243 offset:2816\n"
    "ds_read_b32 v236, v253 offset:0\n"
    "ds_read_b32 v238, v253 offset:4\n"
    "ds_read_b128 v[160:163], v234 offset:4096\n"
    "ds_read_b128 v[164:167], v235 offset:4096\n"
    "s_waitcnt lgkmcnt(8)\n"
    "s_nop 0\n"
    "v_readlane_b32 s20, v252, 0\n"
    "v_readlane_b32 s21, v252, 1\n"
    "v_readlane_b32 s22, v252, 2\n"
    "v_readlane_b32 s23, v252, 3\n"
    "v_readlane_b32 s24, v252, 4\n"
    "v_readlane_b32 s25, v252, 5\n"
    "v_readlane_b32 s26, v252, 6\n"
    "v_readlane_b32 s27, v252, 7\n"
    "v_pk_fma_f32 v[128:129], v[128:129], s[70:71], v[96:97]\n"
    "v_pk_fma_f32 v[130:131], v[130:131], s[70:71], v[98:99]\n"
    "v_pk_add_f32 v[230:231], v[128:129], v[130:131]\n"
    "v_pk_mul_f32 v[232:233], v[128:129], v[130:131]\n"
    "v_pk_fma_f32 v[132:133], v[132:133], s[70:71], v[100:101]\n"
    "v_pk_fma_f32 v[230:231], v[230:231], v[132:133], v[232:233]\n"
    "v_pk_mul_f32 v[232:233], v[232:233], v[132:133]\n"
    "v_pk_fma_f32 v[134:135], v[134:135], s[70:71], v[102:103]\n"
    "v_pk_fma_f32 v[230:231], v[230:231], v[134:135], v[232:233]\n"
    "v_pk_mul_f32 v[232:233], v[232:233], v[134:135]\n"
    "v_pk_fma_f32 v[136:137], v[136:137], s[70:71], v[104:105]\n"
    "v_pk_fma_f32 v[230:231], v[230:231], v[136:137], v[232:233]\n"
    "v_pk_mul_f32 v[232:233], v[232:233], v[136:137]\n"
    "v_pk_fma_f32 v[138:139], v[138:139], s[70:71], v[106:107]\n"
    "v_pk_fma_f32 v[230:231], v[230:231], v[138:139], v[232:233]\n"
    "v_pk_mul_f32 v[232:233], v[232:233], v[138:139]\n"
    "v_pk_fma_f32 v[140:141], v[140:141], s[70:71], v[108:109]\n"
    "v_pk_fma_f32 v[230:231], v[230:231], v[140:141], v[232:233]\n"
    "v_pk_mul_f32 v[232:233], v[232:233], v[140:141]\n"
    "v_pk_fma_f32 v[142:143], v[142:143], s[70:71], v[110:111]\n"
    "v_pk_fma_f32 v[230:231], v[230:231], v[142:143], v[232:233]\n"
    "v_pk_mul_f32 v[232:233], v[232:233], v[142:143]\n"
    "v_rcp_f32 v232, v232\n"
    "v_rcp_f32 v233, v233\n"
    "v_fma_f32 v230, v230, v232, v247\n"
    "v_fmac_f32 v230, v231, v233\n"
    "v_lshlrev_b32 v232, 2, v249\n"
    "v_lshrrev_b32 v233, 2, v249\n"
    "v_and_b32 v232, 56, v232\n"
    "v_and_b32 v233, 4, v233\n"
    "v_add3_u32 v232, v232, v233, s81\n"
    "ds_read_b32 v232, v232\n"
    "s_add_u32 s81, s81, 64\n"
    "s_waitcnt lgkmcnt(0)\n"
    "v_lshlrev_b32 v232, 1, v232\n"
    "buffer_load_ushort v232, v232, s[44:47], 0 offen\n"
    "s_waitcnt vmcnt(0)\n"
    "v_lshlrev_b32 v232, 16, v232\n"
    "v_mul_f32 v230, v230, v232\n"
    "v_cvt_pk_bf16_f32 v230, v230, v230\n"
    "s_add_u32 s67, s67, 64\n"
    "s_waitcnt lgkmcnt(0)\n"
    "ds_write_b16 v245, v230\n"
    "ds_read_b64 v[228:229], v246\n"
    "s_lshl1_add_u32 s76, s72, s76\n"
    "buffer_load_dwordx4 v[236:237], s[40:43], 0 idxen offen offset:0 nt lds\n"
    "buffer_load_dwordx4 v[238:239], s[40:43], 0 idxen offen offset:1024 nt lds\n"
    "s_and_b32 s62, s58, 63\n"
    "s_mul_i32 s62, s62, 272\n"
    "s_mov_b32 s58, s59\n"
    "s_mov_b32 s59, s60\n"
    "s_mov_b32 s81, s65\n"
    "s_xor_b32 s65, s65, s64\n"
    "s_mov_b32 s80, s83\n"
    "s_add_u32 s82, s65, 256\n"
    "s_cmp_lt_i32 s59, s57\n"
    "s_cselect_b32 s76, s76, -1\n"
    "s_bitcmp1_b32 s58, 30\n"
    "s_cbranch_scc0 .Ldin_A1\n"
    ".Ldin_A1s:\n"
    "v_mfma_f32_16x16x32_bf16 v[128:131], v[0:3], v[160:163], v[112:115]\n"
    "v_exp_f32 v144, v144 clamp\n"
    "v_mfma_f32_16x16x32_bf16 v[128:131], v[4:7], v[164:167], v[128:131]\n"
    "v_exp_f32 v145, v145 clamp\n"
    "v_mfma_f32_16x16x32_bf16 v[132:135], v[8:11], v[160:163], v[116:119]\n"
    "v_exp_f32 v146, v146 clamp\n"
    "v_mfma_f32_16x16x32_bf16 v[132:135], v[12:15], v[164:167], v[132:135]\n"
    "v_exp_f32 v147, v147 clamp\n"
    "v_mfma_f32_16x16x32_bf16 v[136:139], v[16:19], v[160:163], v[120:123]\n"
    "v_exp_f32 v148, v148 clamp\n"
    "v_mfma_f32_16x16x32_bf16 v[136:139], v[20:23], v[164:167], v[136:139]\n"
    "v_exp_f32 v149, v149 clamp\n"
    "v_mfma_f32_16x16x32_bf16 v[140:143], v[24:27], v[160:163], v[124:127]\n"
    "v_exp_f32 v150, v150 clamp\n"
    "v_mfma_f32_16x16x32_bf16 v[140:143], v[28:31], v[164:167], v[140:143]\n"
    "v_exp_f32 v151, v151 clamp\n"
    "v_exp_f32 v152, v152 clamp\n"
    "s_add_u32 m0, s68, 4096\n"
    "v_exp_f32 v153, v153 clamp\n"
    "v_add_u32_e64 v253, s67, v244\n"
    "s_waitcnt lgkmcnt(0)\n"
    "v_mfma_f32_4x4x4_16b_bf16 v[224:227], v[228:229], v[168:169], v[224:227] cbsz:4 abid:0\n"
    "v_exp_f32 v154, v154 clamp\n"
    "v_exp_f32 v155, v155 clamp\n"
    "v_mfma_f32_4x4x4_16b_bf16 v[224:227], v[228:229], v[170:171], v[224:227] cbsz:4 abid:1\n"
    "v_exp_f32 v156, v156 clamp\n"
    "v_exp_f32 v157, v157 clamp\n"
    "v_mfma_f32_4x4x4_16b_bf16 v[224:227], v[228:229], v[172:173], v[224:227] cbsz:4 abid:2\n"
    "v_exp_f32 v158, v158 clamp\n"
    "v_exp_f32 v159, v159 clamp\n"
    "v_mfma_f32_4x4x4_16b_bf16 v[224:227], v[228:229], v[174:175], v[224:227] cbsz:4 abid:3\n"
    "s_waitcnt vmcnt(4)\n"
    "ds_read_b64_tr_b16 v[168:169], v240 offset:4096\n"
    "ds_read_b64_tr_b16 v[170:171], v241 offset:4352\n"
    "ds_read_b64_tr_b16 v[172:173], v242 offset:4608\n"
    "ds_read_b64_tr_b16 v[174:175], v243 offset:4864\n"
    "ds_read_b32 v236, v253 offset:0\n"
    "ds_read_b32 v238, v253 offset:4\n"
    "ds_read_b128 v[160:163], v234 offset:6144\n"
    "ds_read_b128 v[164:167], v235 offset:6144\n"
    "s_cbranch_vccz .Ldin_A1s_np\n"
    "s_setprio 1\n"
    ".Ldin_A1s_np:\n"
    "v_pk_fma_f32 v[144:145], v[144:145], s[70:71], v[96:97]\n"
    "v_pk_fma_f32 v[146:147], v[146:147], s[70:71], v[98:99]\n"
    "v_add_f32 v191, v224, v225\n"
    "v_add_f32 v190, v226, v227\n"
    "v_add_f32 v191, v191, v190\n"
    "v_add_u32 v190, s62, v249\n"
    "v_mul_f32 v191, s69, v191\n"
    "v_cvt_pk_bf16_f32 v191, v191, v191\n"
    "ds_write_b16 v190, v191 offset:32768\n"
    "s_bfe_u32 s94, s59, 0x180006\n"
    "s_mul_i32 s86, s94, s56\n"
    "v_mbcnt_lo_u32_b32 v184, s20, 0\n"
    "v_mbcnt_lo_u32_b32 v185, s22, 0\n"
    "v_mbcnt_lo_u32_b32 v186, s24, 0\n"
    "v_mbcnt_lo_u32_b32 v187, s26, 0\n"
    "s_bcnt1_i32_b64 s90, s[20:21]\n"
    "s_bcnt1_i32_b64 s91, s[22:23]\n"
    "s_bcnt1_i32_b64 s92, s[24:25]\n"
    "s_bcnt1_i32_b64 s93, s[26:27]\n"
    "v_mbcnt_hi_u32_b32 v184, s21, v184\n"
    "v_mbcnt_hi_u32_b32 v185, s23, v185\n"
    "v_mbcnt_hi_u32_b32 v186, s25, v186\n"
    "v_mbcnt_hi_u32_b32 v187, s27, v187\n"
    "s_lshl2_add_u32 s90, s90, s65\n"
    "s_lshl2_add_u32 s91, s91, s90\n"
    "s_lshl2_add_u32 s92, s92, s91\n"
    "s_lshl2_add_u32 s93, s93, s92\n"
    "v_add_u32_e64 v188, s86, v251\n"
    "v_pk_add_f32 v[230:231], v[144:145], v[146:147]\n"
    "v_pk_mul_f32 v[232:233], v[144:145], v[146:147]\n"
    "v_pk_fma_f32 v[148:149], v[148:149], s[70:71], v[100:101]\n"
    "v_pk_fma_f32 v[230:231], v[230:231], v[148:149], v[232:233]\n"
    "v_pk_mul_f32 v[232:233], v[232:233], v[148:149]\n"
    "v_lshl_add_u32 v184, v184, 2, s65\n"
    "v_lshl_add_u32 v185, v185, 2, s90\n"
    "v_lshl_add_u32 v186, v186, 2, s91\n"
    "v_lshl_add_u32 v187, v187, 2, s92\n"
    "v_mov_b32 v191, -1\n"
    "v_lshl_add_u32 v190, v249, 1, s93\n"
    "ds_write2_b32 v190, v191, v191 offset1:64\n"
    "s_mov_b64 exec, s[20:21]\n"
    "ds_write_b32 v184, v188\n"
    "s_mov_b64 exec, s[22:23]\n"
    "v_add_u32 v189, 64, v188\n"
    "ds_write_b32 v185, v189\n"
    "s_mov_b64 exec, s[24:25]\n"
    "v_add_u32 v190, 128, v188\n"
    "ds_write_b32 v186, v190\n"
    "s_mov_b64 exec, s[26:27]\n"
    "v_add_u32 v189, 192, v188\n"
    "ds_write_b32 v187, v189\n"
    "s_mov_b64 exec, -1\n"
    "s_add_u32 s95, s93, 60\n"
    "s_add_u32 s94, s82, 128\n"
    "s_andn2_b32 s95, s95, 63\n"
    "s_max_u32 s83, s95, s94\n"
    "v_pk_fma_f32 v[150:151], v[150:151], s[70:71], v[102:103]\n"
    "v_pk_fma_f32 v[230:231], v[230:231], v[150:151], v[232:233]\n"
    "v_pk_mul_f32 v[232:233], v[232:233], v[150:151]\n"
    "v_pk_fma_f32 v[152:153], v[152:153], s[70:71], v[104:105]\n"
    "v_pk_fma_f32 v[230:231], v[230:231], v[152:153], v[232:233]\n"
    "v_pk_mul_f32 v[232:233], v[232:233], v[152:153]\n"
    "v_pk_fma_f32 v[154:155], v[154:155], s[70:71], v[106:107]\n"
    "v_pk_fma_f32 v[230:231], v[230:231], v[154:155], v[232:233]\n"
    "v_pk_mul_f32 v[232:233], v[232:233], v[154:155]\n"
    "v_pk_fma_f32 v[156:157], v[156:157], s[70:71], v[108:109]\n"
    "v_pk_fma_f32 v[230:231], v[230:231], v[156:157], v[232:233]\n"
    "v_pk_mul_f32 v[232:233], v[232:233], v[156:157]\n"
    "v_pk_fma_f32 v[158:159], v[158:159], s[70:71], v[110:111]\n"
    "v_pk_fma_f32 v[230:231], v[230:231], v[158:159], v[232:233]\n"
    "v_pk_mul_f32 v[232:233], v[232:233], v[158:159]\n"
    "v_rcp_f32 v232, v232\n"
    "v_rcp_f32 v233, v233\n"
    "v_fma_f32 v230, v230, v232, v247\n"
    "v_fmac_f32 v230, v231, v233\n"
    "v_lshlrev_b32 v232, 2, v249\n"
    "v_lshrrev_b32 v233, 2, v249\n"
    "v_and_b32 v232, 56, v232\n"
    "v_and_b32 v233, 4, v233\n"
    "v_add3_u32 v232, v232, v233, s81\n"
    "ds_read_b32 v232, v232\n"
    "s_add_u32 s81, s81, 64\n"
    "s_waitcnt lgkmcnt(0)\n"
    "v_lshlrev_b32 v232, 1, v232\n"
    "buffer_load_ushort v232, v232, s[44:47], 0 offen\n"
    "s_waitcnt vmcnt(0)\n"
    "v_lshlrev_b32 v232, 16, v232\n"
    "v_mul_f32 v230, v230, v232\n"
    "v_cvt_pk_bf16_f32 v230, v230, v230\n"
    "s_add_u32 s67, s67, 64\n"
    "s_waitcnt lgkmcnt(0)\n"
    "ds_write_b16 v245, v230\n"
    "ds_read_b64 v[228:229], v246\n"
    "buffer_load_dwordx4 v[236:237], s[40:43], 0 idxen offen offset:0 nt lds\n"
    "buffer_load_dwordx4 v[238:239], s[40:43], 0 idxen offen offset:1024 nt lds\n"
    ".Ldin_B2s:\n"
    "v_mfma_f32_16x16x32_bf16 v[144:147], v[0:3], v[160:163], v[112:115]\n"
    "v_exp_f32 v128, v128 clamp\n"
    "v_mfma_f32_16x16x32_bf16 v[144:147], v[4:7], v[164:167], v[144:147]\n"
    "v_exp_f32 v129, v129 clamp\n"
    "v_mfma_f32_16x16x32_bf16 v[148:151], v[8:11], v[160:163], v[116:119]\n"
    "v_exp_f32 v130, v130 clamp\n"
    "v_mfma_f32_16x16x32_bf16 v[148:151], v[12:15], v[164:167], v[148:151]\n"
    "v_exp_f32 v131, v131 clamp\n"
    "v_mfma_f32_16x16x32_bf16 v[152:155], v[16:19], v[160:163], v[120:123]\n"
    "v_exp_f32 v132, v132 clamp\n"
    "v_mfma_f32_16x16x32_bf16 v[152:155], v[20:23], v[164:167], v[152:155]\n"
    "v_exp_f32 v133, v133 clamp\n"
    "v_mfma_f32_16x16x32_bf16 v[156:159], v[24:27], v[160:163], v[124:127]\n"
    "v_exp_f32 v134, v134 clamp\n"
    "v_mfma_f32_16x16x32_bf16 v[156:159], v[28:31], v[164:167], v[156:159]\n"
    "v_exp_f32 v135, v135 clamp\n"
    "v_exp_f32 v136, v136 clamp\n"
    "s_cmp_eq_u32 s67, s80\n"
    "s_cselect_b32 s67, s65, s67\n"
    "s_and_b32 s94, s59, 63\n"
    "s_mul_i32 s94, s94, 272\n"
    "v_add_u32 v252, s94, v250\n"
    "s_add_u32 m0, s68, 6144\n"
    "v_exp_f32 v137, v137 clamp\n"
    "v_add_u32 v253, s67, v244\n"
    "s_waitcnt lgkmcnt(0)\n"
    "v_mfma_f32_4x4x4_16b_bf16 v[224:227], v[228:229], v[176:177], 0 cbsz:4 abid:0\n"
    "v_exp_f32 v138, v138 clamp\n"
    "v_exp_f32 v139, v139 clamp\n"
    "v_mfma_f32_4x4x4_16b_bf16 v[224:227], v[228:229], v[178:179], v[224:227] cbsz:4 abid:1\n"
    "v_exp_f32 v140, v140 clamp\n"
    "v_exp_f32 v141, v141 clamp\n"
    "v_mfma_f32_4x4x4_16b_bf16 v[224:227], v[228:229], v[180:181], v[224:227] cbsz:4 abid:2\n"
    "v_exp_f32 v142, v142 clamp\n"
    "v_exp_f32 v143, v143 clamp\n"
    "v_mfma_f32_4x4x4_16b_bf16 v[224:227], v[228:229], v[182:183], v[224:227] cbsz:4 abid:3\n"
    "s_waitcnt vmcnt(4)\n"
    "s_memrealtime s[34:35]\n"
    "ds_read_b64_tr_b16 v[176:177], v240 offset:6144\n"
    "ds_read_b64_tr_b16 v[178:179], v241 offset:6400\n"
    "ds_read_b64_tr_b16 v[180:181], v242 offset:6656\n"
    "ds_read_b64_tr_b16 v[182:183], v243 offset:6912\n"
    "ds_read_b32 v236, v253 offset:0\n"
    "ds_read_b32 v238, v253 offset:4\n"
    "ds_read_b128 v[160:163], v234 offset:0\n"
    "ds_read_b128 v[164:167], v235 offset:0\n"
    "ds_read_b128 v[184:187], v252 offset:0\n"
    "ds_read_b128 v[188:191], v252 offset:16\n"
    "v_pk_fma_f32 v[128:129], v[128:129], s[70:71], v[96:97]\n"
    "v_pk_fma_f32 v[130:131], v[130:131], s[70:71], v[98:99]\n"
    "v_pk_add_f32 v[230:231], v[128:129], v[130:131]\n"
    "v_pk_mul_f32 v[232:233], v[128:129], v[130:131]\n"
    "v_pk_fma_f32 v[132:133], v[132:133], s[70:71], v[100:101]\n"
    "v_pk_fma_f32 v[230:231], v[230:231], v[132:133], v[232:233]\n"
    "v_pk_mul_f32 v[232:233], v[232:233], v[132:133]\n"
    "v_pk_fma_f32 v[134:135], v[134:135], s[70:71], v[102:103]\n"
    "v_pk_fma_f32 v[230:231], v[230:231], v[134:135], v[232:233]\n"
    "v_pk_mul_f32 v[232:233], v[232:233], v[134:135]\n"
    "v_pk_fma_f32 v[136:137], v[136:137], s[70:71], v[104:105]\n"
    "v_pk_fma_f32 v[230:231], v[230:231], v[136:137], v[232:233]\n"
    "v_pk_mul_f32 v[232:233], v[232:233], v[136:137]\n"
    "v_pk_fma_f32 v[138:139], v[138:139], s[70:71], v[106:107]\n"
    "v_pk_fma_f32 v[230:231], v[230:231], v[138:139], v[232:233]\n"
    "v_pk_mul_f32 v[232:233], v[232:233], v[138:139]\n"
    "v_pk_fma_f32 v[140:141], v[140:141], s[70:71], v[108:109]\n"
    "v_pk_fma_f32 v[230:231], v[230:231], v[140:141], v[232:233]\n"
    "v_pk_mul_f32 v[232:233], v[232:233], v[140:141]\n"
    "v_pk_fma_f32 v[142:143], v[142:143], s[70:71], v[110:111]\n"
    "v_pk_fma_f32 v[230:231], v[230:231], v[142:143], v[232:233]\n"
    "v_pk_mul_f32 v[232:233], v[232:233], v[142:143]\n"
    "v_rcp_f32 v232, v232\n"
    "v_rcp_f32 v233, v233\n"
    "v_fma_f32 v230, v230, v232, v247\n"
    "v_fmac_f32 v230, v231, v233\n"
    "v_lshlrev_b32 v232, 2, v249\n"
    "v_lshrrev_b32 v233, 2, v249\n"
    "v_and_b32 v232, 56, v232\n"
    "v_and_b32 v233, 4, v233\n"
    "v_add3_u32 v232, v232, v233, s81\n"
    "ds_read_b32 v232, v232\n"
    "s_add_u32 s81, s81, 64\n"
    "s_waitcnt lgkmcnt(0)\n"
    "v_lshlrev_b32 v232, 1, v232\n"
    "buffer_load_ushort v232, v232, s[44:47], 0 offen\n"
    "s_waitcnt vmcnt(0)\n"
    "v_lshlrev_b32 v232, 16, v232\n"
    "v_mul_f32 v230, v230, v232\n"
    "v_cvt_pk_bf16_f32 v230, v230, v230\n"
    "s_add_u32 s67, s67, 64\n"
    "s_waitcnt lgkmcnt(0)\n"
    "ds_write_b16 v245, v230\n"
    "ds_read_b64 v[228:229], v246\n"
    "s_cmp_le_u32 s34, s76\n"
    "s_cbranch_scc1 .Ldin_B2s_wait\n"
    ".Ldin_B2s_go:\n"
    "s_sub_u32 s94, s34, 60\n"
    "s_max_u32 s76, s76, s94\n"
    "s_add_u32 s76, s76, s72\n"
    ".Ldin_B2s_dma:\n"
    "buffer_load_dwordx4 v[236:237], s[40:43], 0 idxen offen offset:0 nt lds\n"
    "buffer_load_dwordx4 v[238:239], s[40:43], 0 idxen offen offset:1024 nt lds\n"
    ".Ldin_B2s_nd:\n"
    "s_branch .Ldin_G3s\n"
    ".Ldin_L0s_poll:\n"
    "s_sleep 1\n"
    "ds_read_b64 v[232:233], v253\n"
    "s_waitcnt lgkmcnt(0)\n"
    "v_readfirstlane_b32 s60, v232\n"
    "s_cmp_eq_u32 s60, -1\n"
    "s_cbranch_scc1 .Ldin_L0s_poll\n"
    "s_branch .Ldin_L0s_pr\n"
    ".Ldin_B2s_wait:\n"
    "s_cmp_lt_i32 s59, s57\n"
    "s_cbranch_scc1 .Ldin_B2s_w2\n"
    "s_sub_u32 s94, s67, s65\n"
    "s_cmp_gt_u32 s94, 256\n"
    "s_cbranch_scc1 .Ldin_B2s_dma\n"
    "s_cmp_eq_u32 s94, 64\n"
    "s_waitcnt vmcnt(2)\n"
    "s_cbranch_scc1 .Ldin_B2s_nd\n"
    "s_waitcnt vmcnt(0)\n"
    "s_branch .Ldin_B2s_nd\n"
    ".Ldin_B2s_slp:\n"
    "s_sleep 1\n"
    ".Ldin_B2s_w2:\n"
    "s_memrealtime s[34:35]\n"
    "s_waitcnt lgkmcnt(0)\n"
    "s_sub_u32 s94, s34, s76\n"
    "s_cmp_lt_i32 s94, 0\n"
    "s_cbranch_scc1 .Ldin_B2s_slp\n"
    "s_branch .Ldin_B2s_go\n"
    ".p2align 5\n"
    ".Ldin_L3s:\n"
    "s_setprio 0\n"
    "s_cmp_ge_i32 s59, s57\n"
    "s_cbranch_scc1 .Ldin_E1s\n"
    "s_add_u32 s76, s76, s73\n"
    "v_mov_b32 v253, s63\n"
    "v_and_b32 v184, v254, v184\n"
    "v_and_b32 v185, v255, v185\n"
    "v_and_b32 v186, v254, v186\n"
    "v_and_b32 v187, v255, v187\n"
    "v_and_b32 v188, v254, v188\n"
    "v_and_b32 v189, v255, v189\n"
    "v_and_b32 v190, v254, v190\n"
    "v_and_b32_e64 v191, v255, v191\n"
    "ds_read_b64 v[232:233], v253\n"
    "v_mfma_f32_4x4x4_16b_bf16 v[128:131], v[184:185], v[32:33], v[64:67]\n"
    "ds_read_b128 v[112:115], v252 offset:17408\n"
    "v_mfma_f32_4x4x4_16b_bf16 v[132:135], v[186:187], v[34:35], v[68:71]\n"
    "ds_read_b128 v[116:119], v252 offset:17424\n"
    "v_mfma_f32_4x4x4_16b_bf16 v[136:139], v[188:189], v[36:37], v[72:75]\n"
    "ds_read_b128 v[120:123], v252 offset:17536\n"
    "ds_read_b128 v[124:127], v252 offset:17552\n"
    "v_cvt_pk_bf16_f32 v0, v128, v129\n"
    "v_cvt_pk_bf16_f32 v1, v130, v131\n"
    "v_mfma_f32_4x4x4_16b_bf16 v[140:143], v[190:191], v[38:39], v[76:79]\n"
    "v_cvt_pk_bf16_f32 v2, v132, v133\n"
    "v_cvt_pk_bf16_f32 v3, v134, v135\n"
    "v_mfma_f32_4x4x4_16b_bf16 v[128:131], v[184:185], v[40:41], v[80:83]\n"
    "v_cvt_pk_bf16_f32 v4, v136, v137\n"
    "v_cvt_pk_bf16_f32 v5, v138, v139\n"
    "v_mfma_f32_4x4x4_16b_bf16 v[132:135], v[186:187], v[42:43], v[84:87]\n"
    "v_cvt_pk_bf16_f32 v6, v140, v141\n"
    "v_cvt_pk_bf16_f32 v7, v142, v143\n"
    "v_mfma_f32_4x4x4_16b_bf16 v[136:139], v[188:189], v[44:45], v[88:91]\n"
    "v_cvt_pk_bf16_f32 v8, v128, v129\n"
    "v_cvt_pk_bf16_f32 v9, v130, v131\n"
    "v_mfma_f32_4x4x4_16b_bf16 v[140:143], v[190:191], v[46:47], v[92:95]\n"
    "v_cvt_pk_bf16_f32 v10, v132, v133\n"
    "v_cvt_pk_bf16_f32 v11, v134, v135\n"
    "v_mfma_f32_4x4x4_16b_bf16 v[128:131], v[184:185], v[48:49], v[192:195]\n"
    "v_cvt_pk_bf16_f32 v12, v136, v137\n"
    "v_cvt_pk_bf16_f32 v13, v138, v139\n"
    "v_mfma_f32_4x4x4_16b_bf16 v[132:135], v[186:187], v[50:51], v[196:199]\n"
    "v_cvt_pk_bf16_f32 v14, v140, v141\n"
    "v_cvt_pk_bf16_f32 v15, v142, v143\n"
    "v_mfma_f32_4x4x4_16b_bf16 v[136:139], v[188:189], v[52:53], v[200:203]\n"
    "s_waitcnt lgkmcnt(0)\n"
    "v_readfirstlane_b32 s60, v232\n"
    "v_cvt_pk_bf16_f32 v16, v128, v129\n"
    "v_cvt_pk_bf16_f32 v17, v130, v131\n"
    "v_mfma_f32_4x4x4_16b_bf16 v[140:143], v[190:191], v[54:55], v[204:207]\n"
    "s_cmp_eq_u32 s60, -1\n"
    "s_cbranch_scc1 .Ldin_L3s_poll\n"
    ".Ldin_L3s_pr:\n"
    "v_readfirstlane_b32 s95, v233\n"
    "s_and_b32 s94, s60, 63\n"
    "s_lshl_b32 s94, s94, 6\n"
    "s_add_u32 s94, s94, 68480\n"
    "s_add_u32 s63, s63, 8\n"
    "v_lshl_add_u32 v252, v249, 1, s94\n"
    "ds_read_b32 v252, v252\n"
    "s_sub_u32 s95, s95, s76\n"
    "s_ashr_i32 s95, s95, 4\n"
    "s_min_i32 s95, s95, s74\n"
    "s_max_i32 s72, s95, s75\n"
    "v_cvt_pk_bf16_f32 v18, v132, v133\n"
    "v_cvt_pk_bf16_f32 v19, v134, v135\n"
    "v_mfma_f32_4x4x4_16b_bf16 v[128:131], v[184:185], v[56:57], v[208:211]\n"
    "v_cvt_pk_bf16_f32 v20, v136, v137\n"
    "v_cvt_pk_bf16_f32 v21, v138, v139\n"
    "v_mfma_f32_4x4x4_16b_bf16 v[132:135], v[186:187], v[58:59], v[212:215]\n"
    "v_cvt_pk_bf16_f32 v22, v140, v141\n"
    "v_cvt_pk_bf16_f32 v23, v142, v143\n"
    "v_mfma_f32_4x4x4_16b_bf16 v[136:139], v[188:189], v[60:61], v[216:219]\n"
    "v_cvt_pk_bf16_f32 v24, v128, v129\n"
    "v_cvt_pk_bf16_f32 v25, v130, v131\n"
    "v_mfma_f32_4x4x4_16b_bf16 v[140:143], v[190:191], v[62:63], v[220:223]\n"
    "v_cvt_pk_bf16_f32 v26, v132, v133\n"
    "v_cvt_pk_bf16_f32 v27, v134, v135\n"
    "v_cvt_pk_bf16_f32 v28, v136, v137\n"
    "v_cvt_pk_bf16_f32 v29, v138, v139\n"
    "v_exp_f32 v152, v152 clamp\n"
    "v_cvt_pk_bf16_f32 v30, v140, v141\n"
    "v_cvt_pk_bf16_f32 v31, v142, v143\n"
    "v_mfma_f32_16x16x32_bf16 v[128:131], v[0:3], v[160:163], v[112:115]\n"
    "v_exp_f32 v144, v144 clamp\n"
    "v_mfma_f32_16x16x32_bf16 v[128:131], v[4:7], v[164:167], v[128:131]\n"
    "v_exp_f32 v145, v145 clamp\n"
    "v_mfma_f32_16x16x32_bf16 v[132:135], v[8:11], v[160:163], v[116:119]\n"
    "v_exp_f32 v146, v146 clamp\n"
    "v_mfma_f32_16x16x32_bf16 v[132:135], v[12:15], v[164:167], v[132:135]\n"
    "v_exp_f32 v147, v147 clamp\n"
    "v_mfma_f32_16x16x32_bf16 v[136:139], v[16:19], v[160:163], v[120:123]\n"
    "v_exp_f32 v148, v148 clamp\n"
    "v_mfma_f32_16x16x32_bf16 v[136:139], v[20:23], v[164:167], v[136:139]\n"
    "v_exp_f32 v149, v149 clamp\n"
    "v_mfma_f32_16x16x32_bf16 v[140:143], v[24:27], v[160:163], v[124:127]\n"
    "v_exp_f32 v150, v150 clamp\n"
    "v_mfma_f32_16x16x32_bf16 v[140:143], v[28:31], v[164:167], v[140:143]\n"
    "v_exp_f32 v151, v151 clamp\n"
    "s_add_u32 m0, s68, 0\n"
    "v_exp_f32 v153, v153 clamp\n"
    "v_add_u32_e64 v253, s67, v244\n"
    "v_mfma_f32_4x4x4_16b_bf16 v[224:227], v[228:229], v[168:169], v[224:227] cbsz:4 abid:0\n"
    "v_exp_f32 v154, v154 clamp\n"
    "v_exp_f32 v155, v155 clamp\n"
    "v_mfma_f32_4x4x4_16b_bf16 v[224:227], v[228:229], v[170:171], v[224:227] cbsz:4 abid:1\n"
    "v_exp_f32 v156, v156 clamp\n"
    "v_exp_f32 v157, v157 clamp\n"
    "v_mfma_f32_4x4x4_16b_bf16 v[224:227], v[228:229], v[172:173], v[224:227] cbsz:4 abid:2\n"
    "v_exp_f32 v158, v158 clamp\n"
    "v_exp_f32 v159, v159 clamp\n"
    "v_mfma_f32_4x4x4_16b_bf16 v[224:227], v[228:229], v[174:175], v[224:227] cbsz:4 abid:3\n"
    "s_waitcnt vmcnt(4)\n"
    "ds_read_b64_tr_b16 v[168:169], v240 offset:0\n"
    "ds_read_b64_tr_b16 v[170:171], v241 offset:256\n"
    "ds_read_b64_tr_b16 v[172:173], v242 offset:512\n"
    "ds_read_b64_tr_b16 v[174:175], v243 offset:768\n"
    "ds_read_b32 v236, v253 offset:0\n"
    "ds_read_b32 v238, v253 offset:4\n"
    "ds_read_b128 v[160:163], v234 offset:2048\n"
    "ds_read_b128 v[164:167], v235 offset:2048\n"
    "s_waitcnt lgkmcnt(8)\n"
    "s_nop 0\n"
    "v_readlane_b32 s20, v252, 0\n"
    "v_readlane_b32 s21, v252, 1\n"
    "v_readlane_b32 s22, v252, 2\n"
    "v_readlane_b32 s23, v252, 3\n"
    "v_readlane_b32 s24, v252, 4\n"
    "v_readlane_b32 s25, v252, 5\n"
    "v_readlane_b32 s26, v252, 6\n"
    "v_readlane_b32 s27, v252, 7\n"
    "v_pk_fma_f32 v[144:145], v[144:145], s[70:71], v[96:97]\n"
    "v_pk_fma_f32 v[146:147], v[146:147], s[70:71], v[98:99]\n"
    "v_pk_add_f32 v[230:231], v[144:145], v[146:147]\n"
    "v_pk_mul_f32 v[232:233], v[144:145], v[146:147]\n"
    "v_pk_fma_f32 v[148:149], v[148:149], s[70:71], v[100:101]\n"
    "v_pk_fma_f32 v[230:231], v[230:231], v[148:149], v[232:233]\n"
    "v_pk_mul_f32 v[232:233], v[232:233], v[148:149]\n"
    "v_pk_fma_f32 v[150:151], v[150:151], s[70:71], v[102:103]\n"
    "v_pk_fma_f32 v[230:231], v[230:231], v[150:151], v[232:233]\n"
    "v_pk_mul_f32 v[232:233], v[232:233], v[150:151]\n"
    "v_pk_fma_f32 v[152:153], v[152:153], s[70:71], v[104:105]\n"
    "v_pk_fma_f32 v[230:231], v[230:231], v[152:153], v[232:233]\n"
    "v_pk_mul_f32 v[232:233], v[232:233], v[152:153]\n"
    "v_pk_fma_f32 v[154:155], v[154:155], s[70:71], v[106:107]\n"
    "v_pk_fma_f32 v[230:231], v[230:231], v[154:155], v[232:233]\n"
    "v_pk_mul_f32 v[232:233], v[232:233], v[154:155]\n"
    "v_pk_fma_f32 v[156:157], v[156:157], s[70:71], v[108:109]\n"
    "v_pk_fma_f32 v[230:231], v[230:231], v[156:157], v[232:233]\n"
    "v_pk_mul_f32 v[232:233], v[232:233], v[156:157]\n"
    "v_pk_fma_f32 v[158:159], v[158:159], s[70:71], v[110:111]\n"
    "v_pk_fma_f32 v[230:231], v[230:231], v[158:159], v[232:233]\n"
    "v_pk_mul_f32 v[232:233], v[232:233], v[158:159]\n"
    "v_rcp_f32 v232, v232\n"
    "v_rcp_f32 v233, v233\n"
    "v_fma_f32 v230, v230, v232, v247\n"
    "v_fmac_f32 v230, v231, v233\n"
    "v_lshlrev_b32 v232, 2, v249\n"
    "v_lshrrev_b32 v233, 2, v249\n"
    "v_and_b32 v232, 56, v232\n"
    "v_and_b32 v233, 4, v233\n"
    "v_add3_u32 v232, v232, v233, s81\n"
    "ds_read_b32 v232, v232\n"
    "s_add_u32 s81, s81, 64\n"
    "s_waitcnt lgkmcnt(0)\n"
    "v_lshlrev_b32 v232, 1, v232\n"
    "buffer_load_ushort v232, v232, s[44:47], 0 offen\n"
    "s_waitcnt vmcnt(0)\n"
    "v_lshlrev_b32 v232, 16, v232\n"
    "v_mul_f32 v230, v230, v232\n"
    "v_cvt_pk_bf16_f32 v230, v230, v230\n"
    "s_sub_u32 s67, s67, -64\n"
    "s_waitcnt lgkmcnt(0)\n"
    "ds_write_b16 v245, v230\n"
    "ds_read_b64 v[228:229], v246\n"
    "s_lshl1_add_u32 s76, s72, s76\n"
    "buffer_load_dwordx4 v[236:237], s[40:43], 0 idxen offen offset:0 nt lds\n"
    "buffer_load_dwordx4 v[238:239], s[40:43], 0 idxen offen offset:1024 nt lds\n"
    "s_and_b32 s62, s58, 63\n"
    "s_mul_i32 s62, s62, 272\n"
    "s_mov_b32 s58, s59\n"
    "s_mov_b32 s59, s60\n"
    "s_mov_b32 s81, s65\n"
    "s_xor_b32 s65, s65, s64\n"
    "s_mov_b32 s80, s83\n"
    "s_add_u32 s82, s65, 256\n"
    "s_cmp_lt_i32 s59, s57\n"
    "s_cselect_b32 s76, s76, -1\n"
    "s_bitcmp1_b32 s58, 30\n"
    "s_cbranch_scc0 .Ldin_A0\n"
    "s_branch .Ldin_A0s\n"
    ".Ldin_L3s_poll:\n"
    "s_sleep 1\n"
    "ds_read_b64 v[232:233], v253\n"
    "s_waitcnt lgkmcnt(0)\n"
    "v_readfirstlane_b32 s60, v232\n"
    "s_cmp_eq_u32 s60, -1\n"
    "s_cbranch_scc1 .Ldin_L3s_poll\n"
    "s_branch .Ldin_L3s_pr\n"
    ".Ldin_E0s:\n"
    "v_exp_f32 v128, v128 clamp\n"
    "v_exp_f32 v129, v129 clamp\n"
    "v_exp_f32 v130, v130 clamp\n"
    "v_exp_f32 v131, v131 clamp\n"
    "v_exp_f32 v132, v132 clamp\n"
    "v_exp_f32 v133, v133 clamp\n"
    "v_exp_f32 v134, v134 clamp\n"
    "v_exp_f32 v135, v135 clamp\n"
    "s_and_b32 s62, s58, 63\n"
    "s_mul_i32 s62, s62, 272\n"
    "s_waitcnt lgkmcnt(0)\n"
    "v_mfma_f32_4x4x4_16b_bf16 v[224:227], v[228:229], v[176:177], v[224:227] cbsz:4 abid:0\n"
    "v_exp_f32 v136, v136 clamp\n"
    "v_exp_f32 v137, v137 clamp\n"
    "v_mfma_f32_4x4x4_16b_bf16 v[224:227], v[228:229], v[178:179], v[224:227] cbsz:4 abid:1\n"
    "v_exp_f32 v138, v138 clamp\n"
    "v_exp_f32 v139, v139 clamp\n"
    "v_mfma_f32_4x4x4_16b_bf16 v[224:227], v[228:229], v[180:181], v[224:227] cbsz:4 abid:2\n"
    "v_exp_f32 v140, v140 clamp\n"
    "v_exp_f32 v141, v141 clamp\n"
    "v_mfma_f32_4x4x4_16b_bf16 v[224:227], v[228:229], v[182:183], v[224:227] cbsz:4 abid:3\n"
    "v_exp_f32 v142, v142 clamp\n"
    "v_exp_f32 v143, v143 clamp\n"
    "v_pk_fma_f32 v[128:129], v[128:129], s[70:71], v[96:97]\n"
    "v_pk_fma_f32 v[130:131], v[130:131], s[70:71], v[98:99]\n"
    "v_pk_add_f32 v[230:231], v[128:129], v[130:131]\n"
    "v_pk_mul_f32 v[232:233], v[128:129], v[130:131]\n"
    "v_pk_fma_f32 v[132:133], v[132:133], s[70:71], v[100:101]\n"
    "v_pk_fma_f32 v[230:231], v[230:231], v[132:133], v[232:233]\n"
    "v_pk_mul_f32 v[232:233], v[232:233], v[132:133]\n"
    "v_pk_fma_f32 v[134:135], v[134:135], s[70:71], v[102:103]\n"
    "v_pk_fma_f32 v[230:231], v[230:231], v[134:135], v[232:233]\n"
    "v_pk_mul_f32 v[232:233], v[232:233], v[134:135]\n"
    "v_pk_fma_f32 v[136:137], v[136:137], s[70:71], v[104:105]\n"
    "v_pk_fma_f32 v[230:231], v[230:231], v[136:137], v[232:233]\n"
    "v_pk_mul_f32 v[232:233], v[232:233], v[136:137]\n"
    "v_pk_fma_f32 v[138:139], v[138:139], s[70:71], v[106:107]\n"
    "v_pk_fma_f32 v[230:231], v[230:231], v[138:139], v[232:233]\n"
    "v_pk_mul_f32 v[232:233], v[232:233], v[138:139]\n"
    "v_pk_fma_f32 v[140:141], v[140:141], s[70:71], v[108:109]\n"
    "v_pk_fma_f32 v[230:231], v[230:231], v[140:141], v[232:233]\n"
    "v_pk_mul_f32 v[232:233], v[232:233], v[140:141]\n"
    "v_pk_fma_f32 v[142:143], v[142:143], s[70:71], v[110:111]\n"
    "v_pk_fma_f32 v[230:231], v[230:231], v[142:143], v[232:233]\n"
    "v_pk_mul_f32 v[232:233], v[232:233], v[142:143]\n"
    "v_rcp_f32 v232, v232\n"
    "v_rcp_f32 v233, v233\n"
    "v_fma_f32 v230, v230, v232, v247\n"
    "v_fmac_f32 v230, v231, v233\n"
    "v_lshlrev_b32 v232, 2, v249\n"
    "v_lshrrev_b32 v233, 2, v249\n"
    "v_and_b32 v232, 56, v232\n"
    "v_and_b32 v233, 4, v233\n"
    "v_add3_u32 v232, v232, v233, s81\n"
    "ds_read_b32 v232, v232\n"
    "s_add_u32 s81, s81, 64\n"
    "s_waitcnt lgkmcnt(0)\n"
    "v_lshlrev_b32 v232, 1, v232\n"
    "buffer_load_ushort v232, v232, s[44:47], 0 offen\n"
    "s_waitcnt vmcnt(0)\n"
    "v_lshlrev_b32 v232, 16, v232\n"
    "v_mul_f32 v230, v230, v232\n"
    "v_cvt_pk_bf16_f32 v230, v230, v230\n"
    "ds_write_b16 v245, v230\n"
    "ds_read_b64 v[228:229], v246\n"
    "s_branch .Ldin_drain0\n"
    ".Ldin_E1s:\n"
    "v_exp_f32 v144, v144 clamp\n"
    "v_exp_f32 v145, v145 clamp\n"
    "v_exp_f32 v146, v146 clamp\n"
    "v_exp_f32 v147, v147 clamp\n"
    "v_exp_f32 v148, v148 clamp\n"
    "v_exp_f32 v149, v149 clamp\n"
    "v_exp_f32 v150, v150 clamp\n"
    "v_exp_f32 v151, v151 clamp\n"
    "s_and_b32 s62, s58, 63\n"
    "s_mul_i32 s62, s62, 272\n"
    "s_waitcnt lgkmcnt(0)\n"
    "v_mfma_f32_4x4x4_16b_bf16 v[224:227], v[228:229], v[168:169], v[224:227] cbsz:4 abid:0\n"
    "v_exp_f32 v152, v152 clamp\n"
    "v_exp_f32 v153, v153 clamp\n"
    "v_mfma_f32_4x4x4_16b_bf16 v[224:227], v[228:229], v[170:171], v[224:227] cbsz:4 abid:1\n"
    "v_exp_f32 v154, v154 clamp\n"
    "v_exp_f32 v155, v155 clamp\n"
    "v_mfma_f32_4x4x4_16b_bf16 v[224:227], v[228:229], v[172:173], v[224:227] cbsz:4 abid:2\n"
    "v_exp_f32 v156, v156 clamp\n"
    "v_exp_f32 v157, v157 clamp\n"
    "v_mfma_f32_4x4x4_16b_bf16 v[224:227], v[228:229], v[174:175], v[224:227] cbsz:4 abid:3\n"
    "v_exp_f32 v158, v158 clamp\n"
    "v_exp_f32 v159, v159 clamp\n"
    "v_pk_fma_f32 v[144:145], v[144:145], s[70:71], v[96:97]\n"
    "v_pk_fma_f32 v[146:147], v[146:147], s[70:71], v[98:99]\n"
    "v_pk_add_f32 v[230:231], v[144:145], v[146:147]\n"
    "v_pk_mul_f32 v[232:233], v[144:145], v[146:147]\n"
    "v_pk_fma_f32 v[148:149], v[148:149], s[70:71], v[100:101]\n"
    "v_pk_fma_f32 v[230:231], v[230:231], v[148:149], v[232:233]\n"
    "v_pk_mul_f32 v[232:233], v[232:233], v[148:149]\n"
    "v_pk_fma_f32 v[150:151], v[150:151], s[70:71], v[102:103]\n"
    "v_pk_fma_f32 v[230:231], v[230:231], v[150:151], v[232:233]\n"
    "v_pk_mul_f32 v[232:233], v[232:233], v[150:151]\n"
    "v_pk_fma_f32 v[152:153], v[152:153], s[70:71], v[104:105]\n"
    "v_pk_fma_f32 v[230:231], v[230:231], v[152:153], v[232:233]\n"
    "v_pk_mul_f32 v[232:233], v[232:233], v[152:153]\n"
    "v_pk_fma_f32 v[154:155], v[154:155], s[70:71], v[106:107]\n"
    "v_pk_fma_f32 v[230:231], v[230:231], v[154:155], v[232:233]\n"
    "v_pk_mul_f32 v[232:233], v[232:233], v[154:155]\n"
    "v_pk_fma_f32 v[156:157], v[156:157], s[70:71], v[108:109]\n"
    "v_pk_fma_f32 v[230:231], v[230:231], v[156:157], v[232:233]\n"
    "v_pk_mul_f32 v[232:233], v[232:233], v[156:157]\n"
    "v_pk_fma_f32 v[158:159], v[158:159], s[70:71], v[110:111]\n"
    "v_pk_fma_f32 v[230:231], v[230:231], v[158:159], v[232:233]\n"
    "v_pk_mul_f32 v[232:233], v[232:233], v[158:159]\n"
    "v_rcp_f32 v232, v232\n"
    "v_rcp_f32 v233, v233\n"
    "v_fma_f32 v230, v230, v232, v247\n"
    "v_fmac_f32 v230, v231, v233\n"
    "v_lshlrev_b32 v232, 2, v249\n"
    "v_lshrrev_b32 v233, 2, v249\n"
    "v_and_b32 v232, 56, v232\n"
    "v_and_b32 v233, 4, v233\n"
    "v_add3_u32 v232, v232, v233, s81\n"
    "ds_read_b32 v232, v232\n"
    "s_add_u32 s81, s81, 64\n"
    "s_waitcnt lgkmcnt(0)\n"
    "v_lshlrev_b32 v232, 1, v232\n"
    "buffer_load_ushort v232, v232, s[44:47], 0 offen\n"
    "s_waitcnt vmcnt(0)\n"
    "v_lshlrev_b32 v232, 16, v232\n"
    "v_mul_f32 v230, v230, v232\n"
    "v_cvt_pk_bf16_f32 v230, v230, v230\n"
    "ds_write_b16 v245, v230\n"
    "ds_read_b64 v[228:229], v246\n"
    "s_branch .Ldin_drain1\n"
    ".Ldin_exit:\n"

      : "+{s[0:1]}"(kp), "+{v0}"(tid)
      : "{s2}"(blockIdx.x)
      : "memory", "vcc", "scc", "v1", "v2", "v3", "v4", "v5", "v6", "v7", "v8", "v9", "v10", "v11", "v12", "v13", "v14", "v15", "v16", "v17", "v18", "v19", "v20", "v21", "v22", "v23", "v24", "v25", "v26", "v27", "v28", "v29", "v30", "v31", "v32", "v33", "v34", "v35", "v36", "v37", "v38", "v39", "v40", "v41", "v42", "v43", "v44", "v45", "v46", "v47", "v48", "v49", "v50", "v51", "v52", "v53", "v54", "v55", "v56", "v57", "v58", "v59", "v60", "v61", "v62", "v63", "v64", "v65", "v66", "v67", "v68", "v69", "v70", "v71", "v72", "v73", "v74", "v75", "v76", "v77", "v78", "v79", "v80", "v81", "v82", "v83", "v84", "v85", "v86", "v87", "v88", "v89", "v90", "v91", "v92", "v93", "v94", "v95", "v96", "v97", "v98", "v99", "v100", "v101", "v102", "v103", "v104", "v105", "v106", "v107", "v108", "v109", "v110", "v111", "v112", "v113", "v114", "v115", "v116", "v117", "v118", "v119", "v120", "v121", "v122", "v123", "v124", "v125", "v126", "v127", "v128", "v129", "v130", "v131", "v132", "v133", "v134", "v135", "v136", "v137", "v138", "v139", "v140", "v141", "v142", "v143", "v144", "v145", "v146", "v147", "v148", "v149", "v150", "v151", "v152", "v153", "v154", "v155", "v156", "v157", "v158", "v159", "v160", "v161", "v162", "v163", "v164", "v165", "v166", "v167", "v168", "v169", "v170", "v171", "v172", "v173", "v174", "v175", "v176", "v177", "v178", "v179", "v180", "v181", "v182", "v183", "v184", "v185", "v186", "v187", "v188", "v189", "v190", "v191", "v192", "v193", "v194", "v195", "v196", "v197", "v198", "v199", "v200", "v201", "v202", "v203", "v204", "v205", "v206", "v207", "v208", "v209", "v210", "v211", "v212", "v213", "v214", "v215", "v216", "v217", "v218", "v219", "v220", "v221", "v222", "v223", "v224", "v225", "v226", "v227", "v228", "v229", "v230", "v231", "v232", "v233", "v234", "v235", "v236", "v237", "v238", "v239", "v240", "v241", "v242", "v243", "v244", "v245", "v246", "v247", "v248", "v249", "v250", "v251", "v252", "v253", "v254", "v255", "s3", "s4", "s5", "s6", "s7", "s8", "s9", "s10", "s11", "s12", "s13", "s14", "s15", "s16", "s17", "s18", "s19", "s20", "s21", "s22", "s23", "s24", "s25", "s26", "s27", "s28", "s29", "s30", "s31", "s33", "s34", "s35", "s36", "s37", "s38", "s39", "s40", "s41", "s42", "s43", "s44", "s45", "s46", "s47", "s48", "s49", "s50", "s51", "s52", "s53", "s54", "s55", "s56", "s57", "s58", "s59", "s60", "s61", "s62", "s63", "s64", "s65", "s66", "s67", "s68", "s69", "s70", "s71", "s72", "s73", "s74", "s75", "s76", "s77", "s78", "s79", "s80", "s81", "s82", "s83", "s84", "s85", "s86", "s87", "s88", "s89", "s90", "s91", "s92", "s93", "s94", "s95", "s96");
  asm volatile("" ::"s"((uint32_t)(uintptr_t)smem));
}

__global__ __launch_bounds__(64) void din_fallback_kernel(
    const unsigned short* __restrict__ target, const unsigned short* __restrict__ hist,
    const unsigned short* __restrict__ mask, const unsigned short* __restrict__ w1,
    const unsigned short* __restrict__ b1, const unsigned short* __restrict__ w2,
    const unsigned short* __restrict__ b2, unsigned short* __restrict__ out, int B, int L) {
  __shared__ float s_w[64 * 64];
  __shared__ float s_x[64];
  const int b = blockIdx.x, h = threadIdx.x;
  float bias = bf2f(b1[h]);
  for (int d = 0; d < 64; ++d) {
    const float t = bf2f(target[(size_t)b * 64 + d]);
    const float wc = bf2f(w1[(128 + d) * 64 + h]);
    bias += t * (bf2f(w1[(64 + d) * 64 + h]) - wc);
    s_w[d * 64 + h] = bf2f(w1[d * 64 + h]) + wc + t * bf2f(w1[(192 + d) * 64 + h]);
  }
  const float w2h = bf2f(w2[h]), b2v = bf2f(b2[0]);
  float acc = 0.f;
  for (int l = 0; l < L; ++l) {
    const float m = bf2f(mask[(size_t)b * L + l]);
    if (m == 0.f) continue;
    const float x = bf2f(hist[((size_t)b * L + l) * 64 + h]);
    __syncthreads();
    s_x[h] = x;
    __syncthreads();
    float z = bias;
    for (int d = 0; d < 64; ++d) z += s_x[d] * s_w[d * 64 + h];
    float s = w2h / (1.0f + __expf(-z));
    for (int o = 32; o > 0; o >>= 1) s += __shfl_xor(s, o);
    acc += (s + b2v) * m * x;
  }
  const uint32_t u = __float_as_uint(acc);
  out[(size_t)b * 64 + h] = (unsigned short)((u + 0x7fffu + ((u >> 16) & 1u)) >> 16);
}

extern "C" void kernel_launch(void* const* d_in, const int* in_sizes, int n_in, void* d_out,
                              int out_size, void* d_ws, size_t ws_size, hipStream_t stream) {
  const unsigned short* target = (const unsigned short*)d_in[0];
  const unsigned short* hist = (const unsigned short*)d_in[1];
  const unsigned short* mask = (const unsigned short*)d_in[2];
  const unsigned short* w1 = (const unsigned short*)d_in[3];
  const unsigned short* b1 = (const unsigned short*)d_in[4];
  const unsigned short* w2 = (const unsigned short*)d_in[5];
  const unsigned short* b2 = (const unsigned short*)d_in[6];
  const int B = in_sizes[0] / 64;
  const int L = in_sizes[2] / B;
  if ((L & 1) || L > 256 || B >= (1 << 24) - 64) {
    din_fallback_kernel<<<B, 64, 0, stream>>>(target, hist, mask, w1, b1, w2, b2, (unsigned short*)d_out, B, L);
    return;
  }
  const uint32_t grid = (B + 63) / 64, fl = 0x20000u;
  const bool paced = grid >= 224;
  din_kernel<<<grid, 512, 0, stream>>>(hist, (uint32_t)B * L, fl, mask, (uint32_t)B * L * 2u, fl, target, (uint32_t)B * 128u, fl,
                                       (unsigned short*)d_out, (uint32_t)B * 128u, fl, w1, w2, b1, b2, (uint32_t)L, NO_BATCH, (uint32_t)B,
                                       grid * 8u, 2u * L, grid * 16u * L, paced ? 52u : 0u, paced ? 15u : 0u);
}
```

```cpp
#include <hip/hip_runtime.h>
#include <stdint.h>

constexpr int SMEM_B = 163712;
constexpr uint32_t NO_BATCH = 0x7fffffc0u;

__device__ __forceinline__ float bf2f(unsigned short u) { return __uint_as_float(((uint32_t)u) << 16); }

__global__ __launch_bounds__(512, 2) __attribute__((aligned(4096))) void din_kernel(
    const unsigned short* hist, uint32_t rows, uint32_t f0, const unsigned short* mask, uint32_t mask_bytes, uint32_t f1,
    const unsigned short* target, uint32_t target_bytes, uint32_t f2, unsigned short* out, uint32_t out_bytes, uint32_t f3,
    const unsigned short* w1, const unsigned short* w2, const unsigned short* b1, const unsigned short* b2,
    uint32_t L, uint32_t no_batch, uint32_t B, uint32_t round_rows, uint32_t row_bytes, uint32_t round_bytes, uint32_t period, uint32_t extra) {
  __shared__ __attribute__((aligned(128))) char smem[SMEM_B];
  unsigned long long kp = (unsigned long long)(uintptr_t)__builtin_amdgcn_kernarg_segment_ptr();
  uint32_t tid = threadIdx.x;
  asm volatile(
    "s_load_dwordx16 s[40:55], s[0:1], 0x0\n"
    "s_load_dwordx16 s[4:19], s[0:1], 0x40\n"
    "s_memrealtime s[76:77]\n"
    "v_readfirstlane_b32 s3, v0\n"
    "v_and_b32 v251, 63, v0\n"
    "s_lshr_b32 s3, s3, 6\n"
    "s_lshl3_add_u32 s61, s2, s3\n"
    "s_cmp_lt_u32 s3, 4\n"
    "s_cbranch_scc0 .Ldin_young\n"
    "v_mul_u32_u24 v32, 0x9e3779, v251\n"
    "s_mov_b32 s90, 0x807f807f\n"
    "v_mov_b32 v47, 0x3f003f00\n"
    "v_add_u32 v32, 0x7f4a7c15, v32\n"
    "v_mul_lo_u32 v33, v32, v32\n"
    "v_mul_lo_u32 v34, v33, v32\n"
    "v_mul_lo_u32 v35, v34, v32\n"
    "v_mul_lo_u32 v36, v35, v32\n"
    "v_mul_lo_u32 v37, v36, v32\n"
    "v_mul_lo_u32 v38, v37, v32\n"
    "v_mul_lo_u32 v39, v38, v32\n"
    "v_and_or_b32 v32, v32, s90, v47\n"
    "v_and_or_b32 v33, v33, s90, v47\n"
    "v_and_or_b32 v34, v34, s90, v47\n"
    "v_and_or_b32 v35, v35, s90, v47\n"
    "v_and_or_b32 v36, v36, s90, v47\n"
    "v_and_or_b32 v37, v37, s90, v47\n"
    "v_and_or_b32 v38, v38, s90, v47\n"
    "v_and_or_b32 v39, v39, s90, v47\n"
    "s_nop 1\n"
    "v_lshlrev_b32 v105, 3, v251\n"
    "v_mov_b32 v8, 0\n"
    "v_mov_b32 v9, 0\n"
    "v_mov_b32 v10, 0\n"
    "v_lshlrev_b32 v249, 1, v251\n"
    "s_mul_i32 s68, s3, 11392\n"
    "v_lshlrev_b32_e64 v248, 4, v251\n"
    "s_add_u32 s68, s68, 72576\n"
    "v_and_b32 v24, 0x60, v249\n"
    "s_add_u32 s67, s68, 8192\n"
    "v_mfma_f32_32x32x16_bf16 v[64:79], v[36:39], v[32:35], 0\n"
    "v_or_b32 v250, 0x8000, v24\n"
    "s_add_u32 s65, s68, 9728\n"
    "v_lshrrev_b32 v20, 4, v251\n"
    "s_xor_b32 s64, s67, s65\n"
    "v_and_b32 v244, 56, v251\n"
    "s_add_u32 s96, s68, 11264\n"
    "v_bitop3_b32 v21, v20, v251, 7 bitop3:0x78\n"
    "v_mfma_f32_32x32x16_bf16 v[48:63], v[32:35], v[36:39], 0\n"
    "s_lshl_b32 s66, s3, 7\n"
    "v_lshlrev_b32 v21, 4, v21\n"
    "s_add_u32 s66, s66, 68512\n"
    "v_or_b32 v237, 0x400, v21\n"
    "s_lshr_b32 s95, s67, 2\n"
    "v_xor_b32 v239, 64, v21\n"
    "s_mov_b32 s38, 0xffff\n"
    "v_mfma_f32_32x32x16_bf16 v[64:79], v[36:39], v[32:35], 0\n"
    "v_add_u32 v253, s67, v244\n"
    "v_lshlrev_b32 v105, 3, v251\n"
    "v_mov_b32 v201, v237\n"
    "v_mov_b32 v203, v239\n"
    "v_mov_b32 v205, v237\n"
    "v_mov_b32 v207, v239\n"
    "v_mov_b32 v209, v237\n"
    "v_mfma_f32_32x32x16_bf16 v[48:63], v[32:35], v[36:39], 0\n"
    "v_mov_b32 v211, v239\n"
    "v_mov_b32 v213, v237\n"
    "v_mov_b32 v215, v239\n"
    "v_mov_b32 v220, s95\n"
    "v_mov_b32 v224, 0\n"
    "v_mov_b32 v225, 0\n"
    "v_mov_b32 v226, 0\n"
    "v_mfma_f32_32x32x16_bf16 v[64:79], v[36:39], v[32:35], 0\n"
    "v_mov_b32 v227, 0\n"
    "v_bfe_u32 v21, v251, 1, 3\n"
    "v_xor_b32 v21, v20, v21\n"
    "v_and_b32 v22, 0xf0, v248\n"
    "s_mov_b32 s70, 0x47000000\n"
    "v_lshl_add_u32 v22, v22, 3, s68\n"
    "s_mov_b32 s71, s70\n"
    "v_mfma_f32_32x32x16_bf16 v[48:63], v[32:35], v[36:39], 0\n"
    "v_lshl_add_u32 v234, v21, 4, v22\n"
    "s_add_u32 s63, s66, 16\n"
    "v_xor_b32 v235, 64, v234\n"
    "s_mov_b32 s81, s67\n"
    "v_and_b32 v20, 3, v251\n"
    "s_mov_b64 s[92:93], 0xffff\n"
    "v_lshlrev_b64 v[254:255], v248, s[92:93]\n"
    "v_mfma_f32_32x32x16_bf16 v[64:79], v[36:39], v[32:35], 0\n"
    "s_add_u32 s82, s65, 256\n"
    "v_lshl_or_b32 v21, v20, 3, v24\n"
    "s_mul_i32 s62, s3, 272\n"
    "v_and_b32 v22, 8, v251\n"
    "v_mul_u32_u24 v23, 136, v22\n"
    "v_xor_b32 v21, v21, v23\n"
    "v_and_b32 v23, 4, v251\n"
    "v_mfma_f32_32x32x16_bf16 v[48:63], v[32:35], v[36:39], 0\n"
    "v_lshl_or_b32 v21, v23, 5, v21\n"
    "v_add_u32 v240, s68, v21\n"
    "v_xor_b32 v241, 16, v240\n"
    "v_xor_b32 v242, 32, v240\n"
    "v_xor_b32 v243, 48, v240\n"
    "v_add_u32 v252, s62, v250\n"
    "v_and_b32 v21, 24, v249\n"
    "v_mfma_f32_32x32x16_bf16 v[64:79], v[36:39], v[32:35], 0\n"
    "v_add_u32 v21, s96, v21\n"
    "v_lshl_add_u32 v246, v20, 5, v21\n"
    "v_and_b32 v21, 6, v251\n"
    "v_lshl_or_b32 v21, v21, 2, v24\n"
    "v_and_b32 v23, 2, v249\n"
    "v_lshrrev_b32 v22, 1, v22\n"
    "v_or3_b32 v21, v21, v22, v23\n"
    "v_mfma_f32_32x32x16_bf16 v[48:63], v[32:35], v[36:39], 0\n"
    "v_add_u32 v245, s96, v21\n"
    "s_setprio 2\n"
    "s_waitcnt lgkmcnt(0)\n"
    "s_mul_i32 s36, s61, s16\n"
    "s_add_u32 s94, s36, s16\n"
    "s_cmp_lt_u32 s61, s14\n"
    "s_cselect_b32 s46, s94, 0\n"
    "buffer_load_dwordx2 v[228:229], v105, s[44:47], s36 offen nt\n"
    "s_load_dword s10, s[10:11], 0x0\n"
    "global_load_short_d16_hi v8, v249, s[6:7]\n"
    "global_load_short_d16_hi v9, v249, s[8:9]\n"
    "s_setprio 0\n"
    "s_branch .Ldin_old2\n"
    ".Ldin_young:\n"
    "s_setprio 1\n"
    "v_and_b32 v16, 7, v251\n"
    "s_lshl_b32 s94, s3, 11\n"
    "v_lshlrev_b32 v2, 4, v16\n"
    "v_and_b32 v3, 56, v251\n"
    "v_lshl_or_b32 v3, v3, 5, v2\n"
    "v_add_u32 v6, s94, v3\n"
    "v_add_u32 v3, 0x2000, v6\n"
    "v_add_u32 v4, 0xffffe000, v6\n"
    "v_add_u32 v5, 0x4000, v6\n"
    "v_and_b32 v17, 8, v251\n"
    "s_and_b32 s88, s3, 1\n"
    "v_and_b32 v18, 32, v251\n"
    "s_and_b32 s89, s3, 2\n"
    "v_lshl_or_b32 v18, v16, 2, v18\n"
    "s_lshl_b32 s84, s88, 9\n"
    "v_lshl_add_u32 v18, v18, 3, s84\n"
    "s_lshl_b32 s84, s89, 10\n"
    "v_and_b32 v19, 16, v251\n"
    "s_lshl_b32 s85, s89, 9\n"
    "v_lshl_or_b32 v19, v19, 6, v17\n"
    "s_add_u32 s85, s85, 0x4000\n"
    "v_add3_u32 v12, v18, v19, s84\n"
    "s_lshl2_add_u32 s86, s89, s88\n"
    "v_add_u32 v13, 16, v12\n"
    "s_lshl_b32 s86, s86, 4\n"
    "v_and_b32 v19, 24, v251\n"
    "s_add_u32 s87, s94, 0x4000\n"
    "v_lshrrev_b32 v19, 1, v19\n"
    "v_add3_u32 v14, v18, v19, s85\n"
    "v_add_u32 v15, 16, v14\n"
    "v_bfrev_b32 v7, 1\n"
    "v_lshlrev_b32 v26, 4, v251\n"
    "v_and_b32 v26, 0x1f0, v26\n"
    "v_lshrrev_b32 v27, 5, v251\n"
    "s_add_u32 s39, s61, s3\n"
    "s_sub_u32 s39, s39, 8\n"
    "v_lshlrev_b32 v249, 1, v251\n"
    "s_mul_i32 s68, s3, 11392\n"
    "v_lshlrev_b32_e64 v248, 4, v251\n"
    "s_add_u32 s68, s68, 72576\n"
    "v_and_b32 v24, 0x60, v249\n"
    "s_add_u32 s67, s68, 8192\n"
    "v_or_b32 v250, 0x8000, v24\n"
    "s_add_u32 s65, s68, 9728\n"
    "v_lshrrev_b32 v20, 4, v251\n"
    "s_xor_b32 s64, s67, s65\n"
    "v_and_b32 v244, 56, v251\n"
    "s_add_u32 s96, s68, 11264\n"
    "v_bitop3_b32 v21, v20, v251, 7 bitop3:0x78\n"
    "s_lshl_b32 s66, s3, 7\n"
    "v_lshlrev_b32 v21, 4, v21\n"
    "s_add_u32 s66, s66, 68512\n"
    "v_or_b32 v237, 0x400, v21\n"
    "s_lshr_b32 s95, s67, 2\n"
    "v_xor_b32 v239, 64, v21\n"
    "s_mov_b32 s38, 0xffff\n"
    "v_add_u32 v253, s67, v244\n"
    "v_lshlrev_b32 v105, 3, v251\n"
    "v_mov_b32 v201, v237\n"
    "v_mov_b32 v203, v239\n"
    "v_mov_b32 v205, v237\n"
    "v_mov_b32 v207, v239\n"
    "v_mov_b32 v209, v237\n"
    "v_mov_b32 v211, v239\n"
    "v_mov_b32 v213, v237\n"
    "v_mov_b32 v215, v239\n"
    "v_mov_b32 v220, s95\n"
    "v_mov_b32 v224, 0\n"
    "v_mov_b32 v225, 0\n"
    "s_waitcnt lgkmcnt(0)\n"
    "global_load_dwordx4 v[8:11], v2, s[6:7]\n"
    "global_load_dwordx4 v[128:131], v3, s[4:5] offset:0\n"
    "global_load_dwordx4 v[132:135], v3, s[4:5] offset:128\n"
    "v_mov_b32 v226, 0\n"
    "v_mov_b32 v227, 0\n"
    "v_bfe_u32 v21, v251, 1, 3\n"
    "v_xor_b32 v21, v20, v21\n"
    "v_and_b32 v22, 0xf0, v248\n"
    "s_mov_b32 s70, 0x47000000\n"
    "v_lshl_add_u32 v22, v22, 3, s68\n"
    "s_mov_b32 s71, s70\n"
    "v_lshl_add_u32 v234, v21, 4, v22\n"
    "s_add_u32 s63, s66, 16\n"
    "v_xor_b32 v235, 64, v234\n"
    "s_mov_b32 s81, s67\n"
    "global_load_dwordx4 v[136:139], v4, s[4:5] offset:0\n"
    "v_and_b32_e64 v20, 3, v251\n"
    "s_mov_b64 s[92:93], 0xffff\n"
    "v_lshlrev_b64 v[254:255], v248, s[92:93]\n"
    "s_add_u32 s82, s65, 256\n"
    "v_lshl_or_b32 v21, v20, 3, v24\n"
    "s_mul_i32 s62, s3, 272\n"
    "v_and_b32 v22, 8, v251\n"
    "v_mul_u32_u24 v23, 136, v22\n"
    "v_xor_b32 v21, v21, v23\n"
    "v_and_b32 v23, 4, v251\n"
    "v_lshl_or_b32 v21, v23, 5, v21\n"
    "v_add_u32 v240, s68, v21\n"
    "global_load_dwordx4 v[140:143], v4, s[4:5] offset:128\n"
    "v_xor_b32 v241, 16, v240\n"
    "v_xor_b32 v242, 32, v240\n"
    "v_xor_b32 v243, 48, v240\n"
    "v_add_u32 v252, s62, v250\n"
    "v_and_b32 v21, 24, v249\n"
    "v_add_u32 v21, s96, v21\n"
    "v_lshl_add_u32 v246, v20, 5, v21\n"
    "v_and_b32 v21, 6, v251\n"
    "v_lshl_or_b32 v21, v21, 2, v24\n"
    "v_and_b32 v23, 2, v249\n"
    "v_lshrrev_b32 v22, 1, v22\n"
    "v_or3_b32 v21, v21, v22, v23\n"
    "global_load_dwordx4 v[144:147], v5, s[4:5] offset:0\n"
    "v_add_u32 v245, s96, v21\n"
    "v_cmp_gt_u32 vcc, s16, v26\n"
    "v_mad_u32_u24 v27, v27, s16, v26\n"
    "s_mul_i32 s39, s39, s16\n"
    "s_lshl1_add_u32 s39, s17, s39\n"
    "v_cndmask_b32 v27, v7, v27, vcc\n"
    "s_add_u32 s40, s40, 0xfffffc00\n"
    "s_addc_u32 s41, s41, -1\n"
    "s_mul_i32 s94, s61, s12\n"
    "s_bitset1_b32 s41, 23\n"
    "v_lshl_add_u32 v216, v251, 2, s94\n"
    "s_mov_b64 s[56:57], s[12:13]\n"
    "global_load_dwordx4 v[148:151], v5, s[4:5] offset:128\n"
    "v_add_u32 v217, 1, v216\n"
    "v_add_u32 v218, 2, v216\n"
    "v_add_u32 v219, 3, v216\n"
    "s_mov_b64 s[72:73], s[18:19]\n"
    "s_cmp_lg_u32 s72, 0\n"
    "s_cselect_b32 s94, 6, 0\n"
    "s_cselect_b32 s95, 4, 0\n"
    "s_add_u32 s74, s72, s94\n"
    "s_sub_u32 s75, s72, s95\n"
    "v_mov_b32 v20, s13\n"
    "s_mov_b64 exec, 0xffffff\n"
    "v_mov_b32 v20, -1\n"
    "s_mov_b64 exec, 0xff\n"
    "v_mov_b32 v20, 0\n"
    "s_mov_b32 exec_lo, -1\n"
    "v_lshl_add_u32 v21, v249, 1, s66\n"
    "v_add_u32 v21, -32, v21\n"
    "ds_write_b32 v21, v20\n"
    "s_mov_b32 exec_hi, -1\n"
    "global_load_dwordx4 v[152:155], v6, s[4:5] offset:0\n"
    "s_mov_b32 s37, 0xffff0000\n"
    "v_and_b32 v19, 15, v251\n"
    "v_mul_u32_u24 v19, 272, v19\n"
    "v_add_u32 v97, v19, v250\n"
    "v_add_u32 v98, s87, v248\n"
    "v_add_u32 v99, s86, v250\n"
    "v_add_u32 v100, s86, v97\n"
    "s_mov_b32 s84, 0xbfb8aa3b\n"
    "global_load_dwordx4 v[156:159], v6, s[4:5] offset:128\n"
    "s_mov_b64 exec, 0\n"
    "s_bitcmp1_b32 s3, 1\n"
    "s_cbranch_scc1 .Ldin_t1_hi\n"
    "s_bitcmp1_b32 s3, 0\n"
    "s_cbranch_scc0 .Ldin_stub_4\n"
    "s_branch .Ldin_stub_5\n"
    ".Ldin_t1_hi:\n"
    "s_bitcmp1_b32 s3, 0\n"
    "s_cbranch_scc0 .Ldin_stub_6\n"
    "s_branch .Ldin_stub_7\n"
    ".Ldin_old2:\n"
    "v_and_b32 v2, 48, v251\n"
    "v_and_b32 v3, 1, v251\n"
    "v_lshl_or_b32 v2, v3, 6, v2\n"
    "s_lshl_b32 s39, s61, 7\n"
    "s_add_u32 s40, s40, 0xfffffc00\n"
    "s_addc_u32 s41, s41, -1\n"
    "s_mul_i32 s94, s61, s12\n"
    "s_bitset1_b32 s41, 23\n"
    "v_lshl_add_u32 v216, v251, 2, s94\n"
    "s_mov_b64 s[56:57], s[12:13]\n"
    "v_add_u32 v217, 1, v216\n"
    "v_add_u32 v218, 2, v216\n"
    "v_add_u32 v219, 3, v216\n"
    "s_mov_b64 s[72:73], s[18:19]\n"
    "s_cmp_lg_u32 s72, 0\n"
    "s_cselect_b32 s94, 6, 0\n"
    "s_cselect_b32 s95, 4, 0\n"
    "s_add_u32 s74, s72, s94\n"
    "s_sub_u32 s75, s72, s95\n"
    "v_mov_b32 v20, s13\n"
    "s_mov_b64 exec, 0xffffff\n"
    "v_mov_b32 v20, -1\n"
    "s_mov_b64 exec, 0xff\n"
    "v_mov_b32 v20, 0\n"
    "s_mov_b32 exec_lo, -1\n"
    "v_lshl_add_u32 v21, v249, 1, s66\n"
    "v_add_u32 v21, -32, v21\n"
    "ds_write_b32 v21, v20\n"
    "s_mov_b32 exec_hi, -1\n"
    "v_and_b32 v4, 2, v251\n"
    "v_lshl_add_u32 v4, v4, 6, v250\n"
    "v_and_b32 v5, 16, v248\n"
    "v_and_b32 v6, 12, v251\n"
    "v_or3_b32 v104, v4, v5, v6\n"
    "s_mul_i32 s84, s3, s15\n"
    "v_mul_u32_u24 v4, 34, v244\n"
    "s_lshl3_add_u32 s84, s2, s84\n"
    "v_and_b32 v5, 3, v251\n"
    "s_lshl_b32 s84, s84, 7\n"
    "v_lshl_add_u32 v4, v5, 5, v4\n"
    "v_and_b32 v5, 4, v251\n"
    "v_lshl_add_u32 v4, v5, 2, v4\n"
    "s_lshl_b32 s85, s15, 9\n"
    "s_lshl_b32 s86, s62, 3\n"
    "s_add_u32 s85, s85, s84\n"
    "v_add_u32 v18, s86, v4\n"
    "s_lshl_b32 s94, s61, 6\n"
    "s_add_u32 s94, s3, s94\n"
    "s_cmp_lt_u32 s61, s14\n"
    "s_cselect_b32 s58, s94, s13\n"
    "s_add_u32 s95, s61, s15\n"
    "s_add_u32 s36, s36, s17\n"
    "s_lshl_b32 s94, s95, 6\n"
    "s_add_u32 s94, s3, s94\n"
    "s_add_u32 s96, s36, s16\n"
    "s_add_u32 s94, s94, 8\n"
    "s_cmp_lt_u32 s95, s14\n"
    "s_cselect_b32 s46, s96, 0\n"
    "s_cselect_b32 s59, s94, s13\n"
    "s_setprio 2\n"
    "buffer_load_dwordx4 v[160:163], v2, s[48:51], s39 offen nt\n"
    "buffer_load_dwordx4 v[128:131], v248, s[48:51], s84 offen nt\n"
    "buffer_load_dwordx4 v[132:135], v248, s[48:51], s85 offen nt\n"
    "buffer_load_short_d16_hi v224, v249, s[44:47], s36 offen offset:0 nt\n"
    "buffer_load_short_d16_hi v225, v249, s[44:47], s36 offen offset:128 nt\n"
    "buffer_load_short_d16_hi v226, v249, s[44:47], s36 offen offset:256 nt\n"
    "buffer_load_short_d16_hi v227, v249, s[44:47], s36 offen offset:384 nt\n"
    "s_mul_i32 s46, s14, s16\n"
    "s_mov_b64 exec, 0\n"
    "s_bitcmp1_b32 s3, 1\n"
    "s_cbranch_scc1 .Ldin_t0_hi\n"
    "s_bitcmp1_b32 s3, 0\n"
    "s_cbranch_scc0 .Ldin_stub_0\n"
    "s_branch .Ldin_stub_1\n"
    ".Ldin_t0_hi:\n"
    "s_bitcmp1_b32 s3, 0\n"
    "s_cbranch_scc0 .Ldin_stub_2\n"
    "s_branch .Ldin_stub_3\n"
    ".Ldin_t0_ret:\n"
    "s_mov_b64 exec, -1\n"
    "s_setprio 2\n"
    "s_waitcnt vmcnt(9)\n"
    "v_cmp_ne_u16_e64 s[84:85], 0, v228\n"
    "v_cmp_lt_u32_e64 s[86:87], s38, v228\n"
    "v_cmp_ne_u16_e64 s[88:89], 0, v229\n"
    "v_cmp_lt_u32_e64 s[90:91], s38, v229\n"
    "v_mbcnt_lo_u32_b32 v220, s84, v220\n"
    "s_bcnt1_i32_b64 s92, s[84:85]\n"
    "v_mbcnt_hi_u32_b32 v220, s85, v220\n"
    "s_lshl2_add_u32 s92, s92, s67\n"
    "v_mbcnt_lo_u32_b32 v220, s86, v220\n"
    "s_bcnt1_i32_b64 s93, s[86:87]\n"
    "v_mbcnt_hi_u32_b32 v220, s87, v220\n"
    "s_lshl2_add_u32 s93, s93, s92\n"
    "v_mbcnt_lo_u32_b32 v220, s88, v220\n"
    "s_bcnt1_i32_b64 s94, s[88:89]\n"
    "v_mbcnt_hi_u32_b32 v220, s89, v220\n"
    "s_lshl2_add_u32 s94, s94, s93\n"
    "v_mbcnt_lo_u32_b32 v220, s90, v220\n"
    "s_bcnt1_i32_b64 s95, s[90:91]\n"
    "v_mbcnt_hi_u32_b32 v220, s91, v220\n"
    "s_lshl2_add_u32 s95, s95, s94\n"
    "v_mov_b32 v21, -1\n"
    "v_lshl_add_u32 v20, v249, 1, s95\n"
    "v_addc_co_u32_e64 v221, vcc, v220, 0, s[84:85]\n"
    "v_addc_co_u32_e64 v222, vcc, v221, 0, s[86:87]\n"
    "v_addc_co_u32_e64 v223, vcc, v222, 0, s[88:89]\n"
    "ds_write2_b32 v20, v21, v21 offset1:64\n"
    "v_lshlrev_b32 v220, 2, v220\n"
    "v_lshlrev_b32 v221, 2, v221\n"
    "v_lshlrev_b32 v222, 2, v222\n"
    "v_lshlrev_b32_e64 v223, 2, v223\n"
    "s_mov_b64 exec, s[84:85]\n"
    "ds_write_b32 v220, v216\n"
    "s_mov_b64 exec, s[86:87]\n"
    "ds_write_b32 v221, v217\n"
    "s_mov_b64 exec, s[88:89]\n"
    "ds_write_b32 v222, v218\n"
    "s_mov_b64 exec, s[90:91]\n"
    "ds_write_b32 v223, v219\n"
    "s_mov_b64 exec, -1\n"
    "ds_read_b32 v200, v253 offset:0\n"
    "ds_read_b32 v202, v253 offset:4\n"
    "ds_read_b32 v204, v253 offset:64\n"
    "ds_read_b32 v206, v253 offset:68\n"
    "ds_read_b32 v208, v253 offset:128\n"
    "ds_read_b32 v210, v253 offset:132\n"
    "ds_read_b32 v212, v253 offset:192\n"
    "ds_read_b32 v214, v253 offset:196\n"
    "s_add_u32 m0, s68, 0\n"
    "s_waitcnt lgkmcnt(6)\n"
    "buffer_load_dwordx4 v[200:201], s[40:43], 0 idxen offen offset:0 nt lds\n"
    "buffer_load_dwordx4 v[202:203], s[40:43], 0 idxen offen offset:1024 nt lds\n"
    "s_add_u32 s95, s95, 60\n"
    "s_add_u32 s94, s67, 384\n"
    "s_andn2_b32 s95, s95, 63\n"
    "s_max_u32 s80, s95, s94\n"
    "s_add_u32 m0, s68, 2048\n"
    "s_waitcnt lgkmcnt(0)\n"
    "buffer_load_dwordx4 v[204:205], s[40:43], 0 idxen offen offset:0 nt lds\n"
    "s_lshl_b32 s10, s10, 16\n"
    "v_writelane_b32 v10, s10, 0\n"
    "s_waitcnt vmcnt(10)\n"
    "v_and_b32 v11, 0x7fffffff, v8\n"
    "v_and_b32 v12, 0x7fffffff, v10\n"
    "v_min_f32 v14, 0, v8\n"
    "v_add_u32 v12, 0xf6000000, v12\n"
    "v_add_f32 v14, v14, v10\n"
    "v_max_i32 v12, v11, v12\n"
    "v_rcp_f32 v11, v11\n"
    "v_add_f32_dpp v14, v14, v14 row_ror:8 row_mask:0xf bank_mask:0xf\n"
    "v_max_u32_dpp v12, v12, v12 row_ror:8 row_mask:0xf bank_mask:0xf\n"
    "s_mov_b32 s84, 0xbfb8aa3b\n"
    "v_add_f32_dpp v14, v14, v14 row_ror:4 row_mask:0xf bank_mask:0xf\n"
    "v_max_u32_dpp v12, v12, v12 row_ror:4 row_mask:0xf bank_mask:0xf\n"
    "v_bfrev_b32 v13, 1\n"
    "v_add_f32_dpp v14, v14, v14 row_ror:2 row_mask:0xf bank_mask:0xf\n"
    "v_max_u32_dpp v12, v12, v12 row_ror:2 row_mask:0xf bank_mask:0xf\n"
    "v_bitop3_b32 v8, v8, s84, v13 bitop3:0x6c\n"
    "v_add_f32_dpp v14, v14, v14 row_ror:1 row_mask:0xf bank_mask:0xf\n"
    "v_max_u32_dpp v12, v12, v12 row_ror:1 row_mask:0xf bank_mask:0xf\n"
    "s_nop 0\n"
    ".Ldin_stub_6:\n"
    "s_cbranch_execz .Ldin_t1_ret\n"
    "s_nop 0\n"
    "v_readlane_b32 s84, v12, 0\n"
    "v_readlane_b32 s85, v12, 16\n"
    "v_readlane_b32 s86, v12, 32\n"
    "v_readlane_b32 s87, v12, 48\n"
    "s_max_u32 s84, s84, s85\n"
    "s_max_u32 s86, s86, s87\n"
    "s_max_u32 s84, s84, s86\n"
    "s_lshr_b32 s84, s84, 23\n"
    "buffer_load_dwordx4 v[206:207], s[40:43], 0 idxen offen offset:1024 nt lds\n"
    "s_max_u32 s84, s84, 15\n"
    "s_min_u32 s84, s84, 253\n"
    "s_lshl_b32 s84, s84, 23\n"
    "s_add_u32 s85, s84, 0xf9000000\n"
    "s_sub_u32 s86, 0x86000000, s84\n"
    "v_mul_f32 v11, s85, v11\n"
    "v_min_f32 v11, 0x46800000, v11\n"
    "v_log_f32 v13, v11\n"
    "v_mul_f32 v14, s86, v14\n"
    "v_mov_b32 v15, s85\n"
    "v_add_f32 v13, 0xc1700000, v13\n"
    "v_fmac_f32 v13, v9, v8\n"
    "ds_write_b32 v104, v13 offset:35200\n"
    "ds_write_b32 v104, v11 offset:34816\n"
    "v_mov_b32_e64 v247, v14\n"
    "s_mov_b32 s69, s85\n"
    "ds_write_b64 v250, v[14:15] offset:35072\n"
    "s_mov_b32 s94, 0x10001\n"
    "s_mov_b32 s95, 0xc080c080\n"
    "v_pk_min_u16 v22, v228, s94\n"
    "v_pk_min_u16 v23, v229, s94\n"
    "v_pk_mad_u16 v228, v22, s95, v228\n"
    "v_pk_mad_u16 v229, v23, s95, v229\n"
    "s_sub_u32 s87, s80, s81\n"
    "v_or_b32 v228, v228, v229\n"
    "s_lshr_b32 s87, s87, 6\n"
    "s_lshl2_add_u32 s86, s3, 68224\n"
    "s_waitcnt vmcnt(8)\n"
    "ds_write_b128 v18, v[128:131] offset:32768\n"
    "ds_write_b128 v18, v[132:135] offset:41472\n"
    "v_and_b32_dpp v184, v160, v254 quad_perm:[0,0,0,0] row_mask:0xf bank_mask:0xf\n"
    "v_and_b32_dpp v185, v161, v255 quad_perm:[0,0,0,0] row_mask:0xf bank_mask:0xf\n"
    "v_and_b32_dpp v186, v162, v254 quad_perm:[0,0,0,0] row_mask:0xf bank_mask:0xf\n"
    "v_and_b32_dpp v187, v163, v255 quad_perm:[0,0,0,0] row_mask:0xf bank_mask:0xf\n"
    "v_and_b32_dpp v188, v160, v254 quad_perm:[1,1,1,1] row_mask:0xf bank_mask:0xf\n"
    "v_and_b32_dpp v189, v161, v255 quad_perm:[1,1,1,1] row_mask:0xf bank_mask:0xf\n"
    "v_and_b32_dpp v190, v162, v254 quad_perm:[1,1,1,1] row_mask:0xf bank_mask:0xf\n"
    "v_and_b32_dpp v191, v163, v255 quad_perm:[1,1,1,1] row_mask:0xf bank_mask:0xf\n"
    "v_mov_b32 v106, s86\n"
    "v_mov_b32 v108, s66\n"
    "v_mov_b32 v109, s58\n"
    "v_mov_b32_e64 v110, s59\n"
    "s_waitcnt vmcnt(4)\n"
    "v_cmp_neq_f32_e64 s[20:21], 0, v224\n"
    "v_cmp_neq_f32_e64 s[22:23], 0, v225\n"
    "v_cmp_neq_f32_e64 s[24:25], 0, v226\n"
    "v_cmp_neq_f32_e64 s[26:27], 0, v227\n"
    "v_fma_f32 v224, v224, v224, -v224\n"
    "v_fma_f32 v225, v225, v225, -v225\n"
    "v_fma_f32 v226, v226, v226, -v226\n"
    "v_fma_f32 v227, v227, v227, -v227\n"
    "s_bcnt1_i32_b64 s88, s[20:21]\n"
    "s_bcnt1_i32_b64 s89, s[22:23]\n"
    "s_bcnt1_i32_b64 s90, s[24:25]\n"
    "s_bcnt1_i32_b64 s91, s[26:27]\n"
    "v_or3_b32 v228, v228, v224, v225\n"
    "s_add_u32 s88, s88, s89\n"
    "v_or3_b32 v228, v228, v226, v227\n"
    "s_add_u32 s90, s90, s91\n"
    "v_cmp_ne_u32_e64 s[84:85], 0, v228\n"
    "s_add_u32 s88, s88, s90\n"
    "s_pack_ll_b32_b16 s88, s87, s88\n"
    "s_mov_b64 exec, 1\n"
    "v_mov_b32_e64 v107, s88\n"
    "ds_write2_b32 v108, v109, v110 offset1:2\n"
    "s_cmp_lg_u64 s[84:85], 0\n"
    "ds_write_b32 v106, v107\n"
    "s_cselect_b32 s91, 2.0, 0\n"
    "s_mov_b64 exec, -1\n"
    "s_or_b32 s58, s58, s91\n"
    "s_or_b32 s59, s59, s91\n"
    "s_mov_b64 vcc, 0\n"
    "s_waitcnt lgkmcnt(0)\n"
    "s_barrier\n"
    "s_setprio 0\n"
    ".Ldin_entry:\n"
    "ds_read_b128 v[32:35], v248 offset:16384\n"
    "ds_read_b128 v[64:67], v248 offset:0\n"
    "ds_read_b128 v[68:71], v248 offset:1024\n"
    "ds_read_b128 v[36:39], v248 offset:17408\n"
    "ds_read_b128 v[72:75], v248 offset:2048\n"
    "ds_read_b128 v[76:79], v248 offset:3072\n"
    "ds_read_b128 v[40:43], v248 offset:18432\n"
    "ds_read_b128 v[80:83], v248 offset:4096\n"
    "ds_read_b128 v[84:87], v248 offset:5120\n"
    "ds_read_b128 v[44:47], v248 offset:19456\n"
    "ds_read_b128 v[88:91], v248 offset:6144\n"
    "ds_read_b128 v[92:95], v248 offset:7168\n"
    "s_add_u32 s67, s67, 320\n"
    "s_waitcnt lgkmcnt(9)\n"
    "ds_read_b128 v[48:51], v248 offset:20480\n"
    "ds_read_b128 v[192:195], v248 offset:8192\n"
    "ds_read_b128 v[196:199], v248 offset:9216\n"
    "v_mfma_f32_4x4x4_16b_bf16 v[144:147], v[184:185], v[32:33], v[64:67]\n"
    "s_nop 0\n"
    "v_mfma_f32_4x4x4_16b_bf16 v[148:151], v[186:187], v[34:35], v[68:71]\n"
    "s_add_u32 m0, s68, 4096\n"
    "s_cbranch_vccnz .Ldin_e1\n"
    "buffer_load_dwordx4 v[208:209], s[40:43], 0 idxen offen offset:0 nt lds\n"
    "buffer_load_dwordx4 v[210:211], s[40:43], 0 idxen offen offset:1024 nt lds\n"
    ".Ldin_e1:\n"
    "s_waitcnt lgkmcnt(9)\n"
    "ds_read_b128 v[52:55], v248 offset:21504\n"
    "ds_read_b128 v[200:203], v248 offset:10240\n"
    "ds_read_b128 v[204:207], v248 offset:11264\n"
    "v_mfma_f32_4x4x4_16b_bf16 v[152:155], v[188:189], v[36:37], v[72:75]\n"
    "s_nop 1\n"
    "v_cvt_pk_bf16_f32 v0, v144, v145\n"
    "v_cvt_pk_bf16_f32 v1, v146, v147\n"
    "v_mfma_f32_4x4x4_16b_bf16 v[156:159], v[190:191], v[38:39], v[76:79]\n"
    "s_add_u32 m0, s68, 6144\n"
    "s_cbranch_vccnz .Ldin_e2\n"
    "buffer_load_dwordx4 v[212:213], s[40:43], 0 idxen offen offset:0 nt lds\n"
    "buffer_load_dwordx4 v[214:215], s[40:43], 0 idxen offen offset:1024 nt lds\n"
    ".Ldin_e2:\n"
    "s_waitcnt lgkmcnt(9)\n"
    "s_nop 0\n"
    "ds_read_b128 v[56:59], v248 offset:22528\n"
    "ds_read_b128 v[208:211], v248 offset:12288\n"
    "ds_read_b128 v[212:215], v248 offset:13312\n"
    "v_cvt_pk_bf16_f32 v2, v148, v149\n"
    "v_cvt_pk_bf16_f32 v3, v150, v151\n"
    "v_mfma_f32_4x4x4_16b_bf16 v[144:147], v[184:185], v[40:41], v[80:83]\n"
    "v_cvt_pk_bf16_f32 v4, v152, v153\n"
    "v_cvt_pk_bf16_f32 v5, v154, v155\n"
    "v_mfma_f32_4x4x4_16b_bf16 v[148:151], v[186:187], v[42:43], v[84:87]\n"
    "s_cbranch_vccnz .Ldin_e3\n"
    "s_barrier\n"
    ".Ldin_e3:\n"
    "s_waitcnt lgkmcnt(9)\n"
    "s_nop 0\n"
    "ds_read_b128 v[60:63], v248 offset:23552\n"
    "ds_read_b128 v[216:219], v248 offset:14336\n"
    "ds_read_b128 v[220:223], v248 offset:15360\n"
    "v_cvt_pk_bf16_f32 v6, v156, v157\n"
    "v_cvt_pk_bf16_f32 v7, v158, v159\n"
    "v_mfma_f32_4x4x4_16b_bf16 v[152:155], v[188:189], v[44:45], v[88:91]\n"
    "v_cvt_pk_bf16_f32 v8, v144, v145\n"
    "v_cvt_pk_bf16_f32 v9, v146, v147\n"
    "v_mfma_f32_4x4x4_16b_bf16 v[156:159], v[190:191], v[46:47], v[92:95]\n"
    "s_waitcnt lgkmcnt(9)\n"
    "v_cvt_pk_bf16_f32 v10, v148, v149\n"
    "v_cvt_pk_bf16_f32 v11, v150, v151\n"
    "v_mfma_f32_4x4x4_16b_bf16 v[144:147], v[184:185], v[48:49], v[192:195]\n"
    "v_cvt_pk_bf16_f32 v12, v152, v153\n"
    "v_cvt_pk_bf16_f32 v13, v154, v155\n"
    "v_mfma_f32_4x4x4_16b_bf16 v[148:151], v[186:187], v[50:51], v[196:199]\n"
    "s_waitcnt lgkmcnt(6)\n"
    ".Ldin_stub_7:\n"
    "s_cbranch_execz .Ldin_t1_ret\n"
    "s_nop 0\n"
    "v_cvt_pk_bf16_f32 v14, v156, v157\n"
    "v_cvt_pk_bf16_f32 v15, v158, v159\n"
    "v_mfma_f32_4x4x4_16b_bf16 v[152:155], v[188:189], v[52:53], v[200:203]\n"
    "v_cvt_pk_bf16_f32 v16, v144, v145\n"
    "v_cvt_pk_bf16_f32 v17, v146, v147\n"
    "v_mfma_f32_4x4x4_16b_bf16 v[156:159], v[190:191], v[54:55], v[204:207]\n"
    "s_waitcnt lgkmcnt(3)\n"
    "s_waitcnt vmcnt(6)\n"
    "ds_read_b128 v[160:163], v234 offset:0\n"
    "ds_read_b128 v[164:167], v235 offset:0\n"
    "ds_read_b32 v236, v253 offset:256\n"
    "ds_read_b32 v238, v253 offset:260\n"
    "ds_read_b64_tr_b16 v[168:169], v240 offset:0\n"
    "ds_read_b64_tr_b16 v[170:171], v241 offset:256\n"
    "ds_read_b64_tr_b16 v[172:173], v242 offset:512\n"
    "ds_read_b64_tr_b16 v[174:175], v243 offset:768\n"
    "v_cvt_pk_bf16_f32 v18, v148, v149\n"
    "v_cvt_pk_bf16_f32 v19, v150, v151\n"
    "v_mfma_f32_4x4x4_16b_bf16 v[144:147], v[184:185], v[56:57], v[208:211]\n"
    "v_cvt_pk_bf16_f32 v20, v152, v153\n"
    "v_cvt_pk_bf16_f32 v21, v154, v155\n"
    "v_mfma_f32_4x4x4_16b_bf16 v[148:151], v[186:187], v[58:59], v[212:215]\n"
    "s_waitcnt lgkmcnt(8)\n"
    "v_cvt_pk_bf16_f32 v22, v156, v157\n"
    "v_cvt_pk_bf16_f32 v23, v158, v159\n"
    "v_mfma_f32_4x4x4_16b_bf16 v[152:155], v[188:189], v[60:61], v[216:219]\n"
    "v_cvt_pk_bf16_f32 v24, v144, v145\n"
    "v_cvt_pk_bf16_f32 v25, v146, v147\n"
    "v_mfma_f32_4x4x4_16b_bf16 v[156:159], v[190:191], v[62:63], v[220:223]\n"
    "s_cbranch_vccnz .Ldin_e8\n"
    "s_barrier\n"
    ".Ldin_e8:\n"
    "s_nop 0\n"
    "ds_read_b128 v[112:115], v252 offset:17408\n"
    "ds_read_b128 v[116:119], v252 offset:17424\n"
    "ds_read_b128 v[120:123], v252 offset:17536\n"
    "ds_read_b128 v[124:127], v252 offset:17552\n"
    "v_cvt_pk_bf16_f32 v26, v148, v149\n"
    "v_cvt_pk_bf16_f32 v27, v150, v151\n"
    "v_cvt_pk_bf16_f32 v28, v152, v153\n"
    "v_cvt_pk_bf16_f32 v29, v154, v155\n"
    "s_nop 0\n"
    "v_cvt_pk_bf16_f32 v30, v156, v157\n"
    "v_cvt_pk_bf16_f32 v31, v158, v159\n"
    "s_add_u32 m0, s68, 0\n"
    "s_waitcnt lgkmcnt(0)\n"
    "v_mfma_f32_16x16x32_bf16 v[128:131], v[0:3], v[160:163], v[112:115]\n"
    "ds_read_b128 v[96:99], v250 offset:34816\n"
    "v_mfma_f32_16x16x32_bf16 v[128:131], v[4:7], v[164:167], v[128:131]\n"
    "ds_read_b128 v[100:103], v250 offset:34832\n"
    "v_mfma_f32_16x16x32_bf16 v[132:135], v[8:11], v[160:163], v[116:119]\n"
    "ds_read_b128 v[104:107], v250 offset:34944\n"
    "v_mfma_f32_16x16x32_bf16 v[132:135], v[12:15], v[164:167], v[132:135]\n"
    "ds_read_b128 v[108:111], v250 offset:34960\n"
    "v_mfma_f32_16x16x32_bf16 v[136:139], v[16:19], v[160:163], v[120:123]\n"
    "v_mfma_f32_16x16x32_bf16 v[136:139], v[20:23], v[164:167], v[136:139]\n"
    "v_mfma_f32_16x16x32_bf16 v[140:143], v[24:27], v[160:163], v[124:127]\n"
    "v_mfma_f32_16x16x32_bf16 v[140:143], v[28:31], v[164:167], v[140:143]\n"
    "s_waitcnt vmcnt(4)\n"
    "ds_read_b128 v[160:163], v234 offset:2048\n"
    "ds_read_b128 v[164:167], v235 offset:2048\n"
    "buffer_load_dwordx4 v[236:237], s[40:43], 0 idxen offen offset:0 nt lds\n"
    "buffer_load_dwordx4 v[238:239], s[40:43], 0 idxen offen offset:1024 nt lds\n"
    "s_memrealtime s[34:35]\n"
    "s_cmp_lg_u64 vcc, 0\n"
    "s_cselect_b32 s94, 58, 52\n"
    "s_cselect_b32 s95, 35, 20\n"
    "s_cmp_lg_u32 s72, 0\n"
    "s_cselect_b32 s72, s94, 0\n"
    "s_cselect_b32 s95, s95, 0\n"
    "s_waitcnt lgkmcnt(0)\n"
    "s_lshl1_add_u32 s76, s95, s34\n"
    "s_cmp_lt_i32 s59, s57\n"
    "s_cselect_b32 s76, s76, -1\n"
    ".Ldin_stub_1:\n"
    "s_cbranch_execz .Ldin_t0_ret\n"
    "s_nop 0\n"
    "s_bitcmp1_b32 s58, 30\n"
    "s_cbranch_scc1 .Ldin_odd\n"
    "s_nop 0\n"
    ".Ldin_A0:\n"
    "v_mfma_f32_16x16x32_bf16 v[144:147], v[0:3], v[160:163], v[112:115]\n"
    "v_exp_f32 v128, v128 clamp\n"
    "v_mfma_f32_16x16x32_bf16 v[144:147], v[4:7], v[164:167], v[144:147]\n"
    "v_exp_f32 v129, v129 clamp\n"
    "v_mfma_f32_16x16x32_bf16 v[148:151], v[8:11], v[160:163], v[116:119]\n"
    "v_exp_f32 v130, v130 clamp\n"
    "v_mfma_f32_16x16x32_bf16 v[148:151], v[12:15], v[164:167], v[148:151]\n"
    "v_exp_f32 v131, v131 clamp\n"
    "v_mfma_f32_16x16x32_bf16 v[152:155], v[16:19], v[160:163], v[120:123]\n"
    "v_exp_f32 v132, v132 clamp\n"
    "v_mfma_f32_16x16x32_bf16 v[152:155], v[20:23], v[164:167], v[152:155]\n"
    "v_exp_f32 v133, v133 clamp\n"
    "v_mfma_f32_16x16x32_bf16 v[156:159], v[24:27], v[160:163], v[124:127]\n"
    "v_exp_f32 v134, v134 clamp\n"
    "v_mfma_f32_16x16x32_bf16 v[156:159], v[28:31], v[164:167], v[156:159]\n"
    "v_exp_f32 v135, v135 clamp\n"
    "v_exp_f32 v136, v136 clamp\n"
    "s_add_u32 m0, s68, 2048\n"
    "v_exp_f32 v137, v137 clamp\n"
    "v_add_u32 v253, s67, v244\n"
    "s_waitcnt lgkmcnt(0)\n"
    "v_mfma_f32_4x4x4_16b_bf16 v[224:227], v[228:229], v[176:177], v[224:227] cbsz:4 abid:0\n"
    "v_exp_f32 v138, v138 clamp\n"
    "v_exp_f32 v139, v139 clamp\n"
    "v_mfma_f32_4x4x4_16b_bf16 v[224:227], v[228:229], v[178:179], v[224:227] cbsz:4 abid:1\n"
    "v_exp_f32 v140, v140 clamp\n"
    "v_exp_f32 v141, v141 clamp\n"
    "v_mfma_f32_4x4x4_16b_bf16 v[224:227], v[228:229], v[180:181], v[224:227] cbsz:4 abid:2\n"
    "v_exp_f32 v142, v142 clamp\n"
    "v_exp_f32 v143, v143 clamp\n"
    "v_mfma_f32_4x4x4_16b_bf16 v[224:227], v[228:229], v[182:183], v[224:227] cbsz:4 abid:3\n"
    "s_waitcnt vmcnt(4)\n"
    "ds_read_b64_tr_b16 v[176:177], v240 offset:2048\n"
    "ds_read_b64_tr_b16 v[178:179], v241 offset:2304\n"
    "ds_read_b64_tr_b16 v[180:181], v242 offset:2560\n"
    "ds_read_b64_tr_b16 v[182:183], v243 offset:2816\n"
    "ds_read_b32 v236, v253 offset:0\n"
    "ds_read_b32 v238, v253 offset:4\n"
    "ds_read_b128 v[160:163], v234 offset:4096\n"
    "ds_read_b128 v[164:167], v235 offset:4096\n"
    "s_cbranch_vccz .Ldin_A0_np\n"
    "s_setprio 1\n"
    ".Ldin_A0_np:\n"
    "v_pk_fma_f32 v[128:129], v[128:129], s[70:71], v[96:97]\n"
    "v_pk_fma_f32 v[130:131], v[130:131], s[70:71], v[98:99]\n"
    "v_add_f32 v191, v224, v225\n"
    "v_add_f32 v190, v226, v227\n"
    "v_add_f32 v191, v191, v190\n"
    "v_add_u32 v190, s62, v249\n"
    "v_mul_f32 v191, s69, v191\n"
    "v_cvt_pk_bf16_f32 v191, v191, v191\n"
    "ds_write_b16 v190, v191 offset:32768\n"
    "s_bfe_u32 s94, s59, 0x180006\n"
    "s_mul_i32 s86, s94, s56\n"
    "v_mbcnt_lo_u32_b32 v184, s20, 0\n"
    "v_mbcnt_lo_u32_b32 v185, s22, 0\n"
    "v_mbcnt_lo_u32_b32 v186, s24, 0\n"
    "v_mbcnt_lo_u32_b32 v187, s26, 0\n"
    "s_bcnt1_i32_b64 s90, s[20:21]\n"
    "s_bcnt1_i32_b64 s91, s[22:23]\n"
    "s_bcnt1_i32_b64 s92, s[24:25]\n"
    "s_bcnt1_i32_b64 s93, s[26:27]\n"
    "v_mbcnt_hi_u32_b32 v184, s21, v184\n"
    "v_mbcnt_hi_u32_b32 v185, s23, v185\n"
    "v_mbcnt_hi_u32_b32 v186, s25, v186\n"
    "v_mbcnt_hi_u32_b32 v187, s27, v187\n"
    "s_lshl2_add_u32 s90, s90, s65\n"
    "s_lshl2_add_u32 s91, s91, s90\n"
    "s_lshl2_add_u32 s92, s92, s91\n"
    "s_lshl2_add_u32 s93, s93, s92\n"
    "v_add_u32_e64 v188, s86, v251\n"
    "v_pk_add_f32 v[230:231], v[128:129], v[130:131]\n"
    "v_pk_mul_f32 v[232:233], v[128:129], v[130:131]\n"
    "v_pk_fma_f32 v[132:133], v[132:133], s[70:71], v[100:101]\n"
    "v_pk_fma_f32 v[230:231], v[230:231], v[132:133], v[232:233]\n"
    "v_pk_mul_f32 v[232:233], v[232:233], v[132:133]\n"
    "v_lshl_add_u32 v184, v184, 2, s65\n"
    "v_lshl_add_u32 v185, v185, 2, s90\n"
    "v_lshl_add_u32 v186, v186, 2, s91\n"
    "v_lshl_add_u32 v187, v187, 2, s92\n"
    "v_mov_b32 v191, -1\n"
    "v_lshl_add_u32 v190, v249, 1, s93\n"
    "ds_write2_b32 v190, v191, v191 offset1:64\n"
    "s_mov_b64 exec, s[20:21]\n"
    "ds_write_b32 v184, v188\n"
    "s_mov_b64 exec, s[22:23]\n"
    "v_add_u32 v189, 64, v188\n"
    "ds_write_b32 v185, v189\n"
    "s_mov_b64 exec, s[24:25]\n"
    "v_add_u32 v190, 128, v188\n"
    "ds_write_b32 v186, v190\n"
    "s_mov_b64 exec, s[26:27]\n"
    "v_add_u32 v189, 192, v188\n"
    "ds_write_b32 v187, v189\n"
    "s_mov_b64 exec, -1\n"
    "s_add_u32 s95, s93, 60\n"
    "s_add_u32 s94, s82, 128\n"
    "s_andn2_b32 s95, s95, 63\n"
    "s_max_u32 s83, s95, s94\n"
    "v_pk_fma_f32 v[134:135], v[134:135], s[70:71], v[102:103]\n"
    "v_pk_fma_f32 v[230:231], v[230:231], v[134:135], v[232:233]\n"
    "v_pk_mul_f32 v[232:233], v[232:233], v[134:135]\n"
    "v_pk_fma_f32 v[136:137], v[136:137], s[70:71], v[104:105]\n"
    "v_pk_fma_f32 v[230:231], v[230:231], v[136:137], v[232:233]\n"
    "v_pk_mul_f32 v[232:233], v[232:233], v[136:137]\n"
    "v_pk_fma_f32 v[138:139], v[138:139], s[70:71], v[106:107]\n"
    "v_pk_fma_f32 v[230:231], v[230:231], v[138:139], v[232:233]\n"
    "v_pk_mul_f32 v[232:233], v[232:233], v[138:139]\n"
    "v_pk_fma_f32 v[140:141], v[140:141], s[70:71], v[108:109]\n"
    "v_pk_fma_f32 v[230:231], v[230:231], v[140:141], v[232:233]\n"
    "v_pk_mul_f32 v[232:233], v[232:233], v[140:141]\n"
    "v_pk_fma_f32 v[142:143], v[142:143], s[70:71], v[110:111]\n"
    "v_pk_fma_f32 v[230:231], v[230:231], v[142:143], v[232:233]\n"
    "v_pk_mul_f32 v[232:233], v[232:233], v[142:143]\n"
    "v_rcp_f32 v232, v232\n"
    "v_rcp_f32 v233, v233\n"
    "v_fma_f32 v230, v230, v232, v247\n"
    "v_fmac_f32 v230, v231, v233\n"
    "v_cvt_pk_bf16_f32 v230, v230, v230\n"
    "s_add_u32 s67, s67, 64\n"
    "s_waitcnt lgkmcnt(0)\n"
    "ds_write_b16 v245, v230\n"
    "ds_read_b64 v[228:229], v246\n"
    "buffer_load_dwordx4 v[236:237], s[40:43], 0 idxen offen offset:0 nt lds\n"
    "buffer_load_dwordx4 v[238:239], s[40:43], 0 idxen offen offset:1024 nt lds\n"
    ".Ldin_B1:\n"
    "v_mfma_f32_16x16x32_bf16 v[128:131], v[0:3], v[160:163], v[112:115]\n"
    "v_exp_f32 v144, v144 clamp\n"
    "v_mfma_f32_16x16x32_bf16 v[128:131], v[4:7], v[164:167], v[128:131]\n"
    "v_exp_f32 v145, v145 clamp\n"
    "v_mfma_f32_16x16x32_bf16 v[132:135], v[8:11], v[160:163], v[116:119]\n"
    "v_exp_f32 v146, v146 clamp\n"
    "v_mfma_f32_16x16x32_bf16 v[132:135], v[12:15], v[164:167], v[132:135]\n"
    "v_exp_f32 v147, v147 clamp\n"
    "v_mfma_f32_16x16x32_bf16 v[136:139], v[16:19], v[160:163], v[120:123]\n"
    "v_exp_f32 v148, v148 clamp\n"
    "v_mfma_f32_16x16x32_bf16 v[136:139], v[20:23], v[164:167], v[136:139]\n"
    "v_exp_f32 v149, v149 clamp\n"
    "v_mfma_f32_16x16x32_bf16 v[140:143], v[24:27], v[160:163], v[124:127]\n"
    "v_exp_f32 v150, v150 clamp\n"
    "v_mfma_f32_16x16x32_bf16 v[140:143], v[28:31], v[164:167], v[140:143]\n"
    "v_exp_f32 v151, v151 clamp\n"
    "v_exp_f32 v152, v152 clamp\n"
    "s_cmp_eq_u32 s67, s80\n"
    "s_cselect_b32 s67, s65, s67\n"
    "s_and_b32 s94, s59, 63\n"
    ".Ldin_stub_3:\n"
    "s_cbranch_execz .Ldin_t0_ret\n"
    "s_nop 0\n"
    "s_mul_i32 s94, s94, 272\n"
    "v_add_u32 v252, s94, v250\n"
    "s_add_u32 m0, s68, 4096\n"
    "v_exp_f32 v153, v153 clamp\n"
    "v_add_u32 v253, s67, v244\n"
    "s_waitcnt lgkmcnt(0)\n"
    "v_mfma_f32_4x4x4_16b_bf16 v[224:227], v[228:229], v[168:169], 0 cbsz:4 abid:0\n"
    "v_exp_f32 v154, v154 clamp\n"
    "v_exp_f32 v155, v155 clamp\n"
    "v_mfma_f32_4x4x4_16b_bf16 v[224:227], v[228:229], v[170:171], v[224:227] cbsz:4 abid:1\n"
    "v_exp_f32 v156, v156 clamp\n"
    "v_exp_f32 v157, v157 clamp\n"
    "v_mfma_f32_4x4x4_16b_bf16 v[224:227], v[228:229], v[172:173], v[224:227] cbsz:4 abid:2\n"
    "v_exp_f32 v158, v158 clamp\n"
    "v_exp_f32 v159, v159 clamp\n"
    "v_mfma_f32_4x4x4_16b_bf16 v[224:227], v[228:229], v[174:175], v[224:227] cbsz:4 abid:3\n"
    "s_waitcnt vmcnt(4)\n"
    "s_memrealtime s[34:35]\n"
    "ds_read_b64_tr_b16 v[168:169], v240 offset:4096\n"
    "ds_read_b64_tr_b16 v[170:171], v241 offset:4352\n"
    "ds_read_b64_tr_b16 v[172:173], v242 offset:4608\n"
    "ds_read_b64_tr_b16 v[174:175], v243 offset:4864\n"
    "ds_read_b32 v236, v253 offset:0\n"
    "ds_read_b32 v238, v253 offset:4\n"
    "ds_read_b128 v[160:163], v234 offset:6144\n"
    "ds_read_b128 v[164:167], v235 offset:6144\n"
    "ds_read_b128 v[184:187], v252 offset:0\n"
    "ds_read_b128 v[188:191], v252 offset:16\n"
    "v_pk_fma_f32 v[144:145], v[144:145], s[70:71], v[96:97]\n"
    "v_pk_fma_f32 v[146:147], v[146:147], s[70:71], v[98:99]\n"
    "v_pk_add_f32 v[230:231], v[144:145], v[146:147]\n"
    "v_pk_mul_f32 v[232:233], v[144:145], v[146:147]\n"
    "v_pk_fma_f32 v[148:149], v[148:149], s[70:71], v[100:101]\n"
    "v_pk_fma_f32 v[230:231], v[230:231], v[148:149], v[232:233]\n"
    "v_pk_mul_f32 v[232:233], v[232:233], v[148:149]\n"
    "v_pk_fma_f32 v[150:151], v[150:151], s[70:71], v[102:103]\n"
    "v_pk_fma_f32 v[230:231], v[230:231], v[150:151], v[232:233]\n"
    "v_pk_mul_f32 v[232:233], v[232:233], v[150:151]\n"
    "v_pk_fma_f32 v[152:153], v[152:153], s[70:71], v[104:105]\n"
    "v_pk_fma_f32 v[230:231], v[230:231], v[152:153], v[232:233]\n"
    "v_pk_mul_f32 v[232:233], v[232:233], v[152:153]\n"
    "v_pk_fma_f32 v[154:155], v[154:155], s[70:71], v[106:107]\n"
    "v_pk_fma_f32 v[230:231], v[230:231], v[154:155], v[232:233]\n"
    "v_pk_mul_f32 v[232:233], v[232:233], v[154:155]\n"
    "v_pk_fma_f32 v[156:157], v[156:157], s[70:71], v[108:109]\n"
    "v_pk_fma_f32 v[230:231], v[230:231], v[156:157], v[232:233]\n"
    "v_pk_mul_f32 v[232:233], v[232:233], v[156:157]\n"
    "v_pk_fma_f32 v[158:159], v[158:159], s[70:71], v[110:111]\n"
    "v_pk_fma_f32 v[230:231], v[230:231], v[158:159], v[232:233]\n"
    "v_pk_mul_f32 v[232:233], v[232:233], v[158:159]\n"
    "v_rcp_f32 v232, v232\n"
    "v_rcp_f32 v233, v233\n"
    "v_fma_f32 v230, v230, v232, v247\n"
    "v_fmac_f32 v230, v231, v233\n"
    "v_cvt_pk_bf16_f32 v230, v230, v230\n"
    "s_add_u32 s67, s67, 64\n"
    "s_waitcnt lgkmcnt(0)\n"
    "ds_write_b16 v245, v230\n"
    "ds_read_b64 v[228:229], v246\n"
    "s_cmp_le_u32 s34, s76\n"
    "s_cbranch_scc1 .Ldin_B1_wait\n"
    ".Ldin_B1_go:\n"
    "s_sub_u32 s94, s34, 60\n"
    "s_max_u32 s76, s76, s94\n"
    "s_add_u32 s76, s76, s72\n"
    ".Ldin_B1_dma:\n"
    "buffer_load_dwordx4 v[236:237], s[40:43], 0 idxen offen offset:0 nt lds\n"
    "buffer_load_dwordx4 v[238:239], s[40:43], 0 idxen offen offset:1024 nt lds\n"
    ".Ldin_B1_nd:\n"
    ".Ldin_G2:\n"
    "v_mfma_f32_16x16x32_bf16 v[144:147], v[0:3], v[160:163], v[112:115]\n"
    "v_exp_f32 v128, v128 clamp\n"
    "v_mfma_f32_16x16x32_bf16 v[144:147], v[4:7], v[164:167], v[144:147]\n"
    "v_exp_f32 v129, v129 clamp\n"
    "v_mfma_f32_16x16x32_bf16 v[148:151], v[8:11], v[160:163], v[116:119]\n"
    "v_exp_f32 v130, v130 clamp\n"
    "v_mfma_f32_16x16x32_bf16 v[148:151], v[12:15], v[164:167], v[148:151]\n"
    "v_exp_f32 v131, v131 clamp\n"
    "v_mfma_f32_16x16x32_bf16 v[152:155], v[16:19], v[160:163], v[120:123]\n"
    "v_exp_f32 v132, v132 clamp\n"
    "v_mfma_f32_16x16x32_bf16 v[152:155], v[20:23], v[164:167], v[152:155]\n"
    "v_exp_f32 v133, v133 clamp\n"
    "v_mfma_f32_16x16x32_bf16 v[156:159], v[24:27], v[160:163], v[124:127]\n"
    "v_exp_f32 v134, v134 clamp\n"
    "v_mfma_f32_16x16x32_bf16 v[156:159], v[28:31], v[164:167], v[156:159]\n"
    "v_exp_f32 v135, v135 clamp\n"
    "v_exp_f32 v136, v136 clamp\n"
    "s_cmp_eq_u32 s67, s80\n"
    "s_cselect_b32 s67, s65, s67\n"
    "s_add_u32 m0, s68, 6144\n"
    "v_exp_f32 v137, v137 clamp\n"
    "v_add_u32_e64 v253, s67, v244\n"
    "s_waitcnt lgkmcnt(0)\n"
    "v_mfma_f32_4x4x4_16b_bf16 v[224:227], v[228:229], v[176:177], v[224:227] cbsz:4 abid:0\n"
    "v_exp_f32 v138, v138 clamp\n"
    "v_exp_f32 v139, v139 clamp\n"
    "v_mfma_f32_4x4x4_16b_bf16 v[224:227], v[228:229], v[178:179], v[224:227] cbsz:4 abid:1\n"
    "v_exp_f32 v140, v140 clamp\n"
    "v_exp_f32 v141, v141 clamp\n"
    "v_mfma_f32_4x4x4_16b_bf16 v[224:227], v[228:229], v[180:181], v[224:227] cbsz:4 abid:2\n"
    "v_exp_f32 v142, v142 clamp\n"
    "v_exp_f32 v143, v143 clamp\n"
    "v_mfma_f32_4x4x4_16b_bf16 v[224:227], v[228:229], v[182:183], v[224:227] cbsz:4 abid:3\n"
    "s_waitcnt vmcnt(4)\n"
    "s_memrealtime s[34:35]\n"
    "ds_read_b64_tr_b16 v[176:177], v240 offset:6144\n"
    "ds_read_b64_tr_b16 v[178:179], v241 offset:6400\n"
    "ds_read_b64_tr_b16 v[180:181], v242 offset:6656\n"
    "ds_read_b64_tr_b16 v[182:183], v243 offset:6912\n"
    "ds_read_b32 v236, v253 offset:0\n"
    "ds_read_b32 v238, v253 offset:4\n"
    "ds_read_b128 v[160:163], v234 offset:0\n"
    "ds_read_b128 v[164:167], v235 offset:0\n"
    "v_pk_fma_f32 v[128:129], v[128:129], s[70:71], v[96:97]\n"
    "v_pk_fma_f32 v[130:131], v[130:131], s[70:71], v[98:99]\n"
    "v_pk_add_f32 v[230:231], v[128:129], v[130:131]\n"
    "v_pk_mul_f32 v[232:233], v[128:129], v[130:131]\n"
    "v_pk_fma_f32 v[132:133], v[132:133], s[70:71], v[100:101]\n"
    "v_pk_fma_f32 v[230:231], v[230:231], v[132:133], v[232:233]\n"
    "v_pk_mul_f32 v[232:233], v[232:233], v[132:133]\n"
    "v_pk_fma_f32 v[134:135], v[134:135], s[70:71], v[102:103]\n"
    "v_pk_fma_f32 v[230:231], v[230:231], v[134:135], v[232:233]\n"
    "v_pk_mul_f32 v[232:233], v[232:233], v[134:135]\n"
    "v_pk_fma_f32 v[136:137], v[136:137], s[70:71], v[104:105]\n"
    "v_pk_fma_f32 v[230:231], v[230:231], v[136:137], v[232:233]\n"
    "v_pk_mul_f32 v[232:233], v[232:233], v[136:137]\n"
    "v_pk_fma_f32 v[138:139], v[138:139], s[70:71], v[106:107]\n"
    "v_pk_fma_f32 v[230:231], v[230:231], v[138:139], v[232:233]\n"
    "v_pk_mul_f32 v[232:233], v[232:233], v[138:139]\n"
    "v_pk_fma_f32 v[140:141], v[140:141], s[70:71], v[108:109]\n"
    "v_pk_fma_f32 v[230:231], v[230:231], v[140:141], v[232:233]\n"
    "v_pk_mul_f32 v[232:233], v[232:233], v[140:141]\n"
    "v_pk_fma_f32 v[142:143], v[142:143], s[70:71], v[110:111]\n"
    "v_pk_fma_f32 v[230:231], v[230:231], v[142:143], v[232:233]\n"
    "v_pk_mul_f32 v[232:233], v[232:233], v[142:143]\n"
    "v_rcp_f32 v232, v232\n"
    "v_rcp_f32 v233, v233\n"
    "v_fma_f32 v230, v230, v232, v247\n"
    "v_fmac_f32 v230, v231, v233\n"
    "v_cvt_pk_bf16_f32 v230, v230, v230\n"
    "s_add_u32 s67, s67, 64\n"
    "s_waitcnt lgkmcnt(0)\n"
    "ds_write_b16 v245, v230\n"
    "ds_read_b64 v[228:229], v246\n"
    "s_cmp_le_u32 s34, s76\n"
    "s_cbranch_scc1 .Ldin_G2_wait\n"
    ".Ldin_G2_go:\n"
    "s_sub_u32 s94, s34, 60\n"
    "s_max_u32 s76, s76, s94\n"
    "s_add_u32 s76, s76, s72\n"
    ".Ldin_G2_dma:\n"
    "buffer_load_dwordx4 v[236:237], s[40:43], 0 idxen offen offset:0 nt lds\n"
    "buffer_load_dwordx4 v[238:239], s[40:43], 0 idxen offen offset:1024 nt lds\n"
    ".Ldin_G2_nd:\n"
    "s_cmp_eq_u32 s67, s82\n"
    "s_cbranch_scc1 .Ldin_L3\n"
    ".Ldin_G3:\n"
    "v_mfma_f32_16x16x32_bf16 v[128:131], v[0:3], v[160:163], v[112:115]\n"
    "v_exp_f32 v144, v144 clamp\n"
    "v_mfma_f32_16x16x32_bf16 v[128:131], v[4:7], v[164:167], v[128:131]\n"
    "v_exp_f32 v145, v145 clamp\n"
    "v_mfma_f32_16x16x32_bf16 v[132:135], v[8:11], v[160:163], v[116:119]\n"
    "v_exp_f32 v146, v146 clamp\n"
    "v_mfma_f32_16x16x32_bf16 v[132:135], v[12:15], v[164:167], v[132:135]\n"
    "v_exp_f32 v147, v147 clamp\n"
    "v_mfma_f32_16x16x32_bf16 v[136:139], v[16:19], v[160:163], v[120:123]\n"
    "v_exp_f32 v148, v148 clamp\n"
    "v_mfma_f32_16x16x32_bf16 v[136:139], v[20:23], v[164:167], v[136:139]\n"
    "v_exp_f32 v149, v149 clamp\n"
    "v_mfma_f32_16x16x32_bf16 v[140:143], v[24:27], v[160:163], v[124:127]\n"
    "v_exp_f32 v150, v150 clamp\n"
    "v_mfma_f32_16x16x32_bf16 v[140:143], v[28:31], v[164:167], v[140:143]\n"
    "v_exp_f32 v151, v151 clamp\n"
    "v_exp_f32 v152, v152 clamp\n"
    "s_cmp_eq_u32 s67, s80\n"
    "s_cselect_b32 s67, s65, s67\n"
    "s_add_u32 m0, s68, 0\n"
    "v_exp_f32 v153, v153 clamp\n"
    "v_add_u32 v253, s67, v244\n"
    "s_waitcnt lgkmcnt(0)\n"
    "v_mfma_f32_4x4x4_16b_bf16 v[224:227], v[228:229], v[168:169], v[224:227] cbsz:4 abid:0\n"
    "v_exp_f32 v154, v154 clamp\n"
    "v_exp_f32 v155, v155 clamp\n"
    "v_mfma_f32_4x4x4_16b_bf16 v[224:227], v[228:229], v[170:171], v[224:227] cbsz:4 abid:1\n"
    "v_exp_f32 v156, v156 clamp\n"
    "v_exp_f32 v157, v157 clamp\n"
    "v_mfma_f32_4x4x4_16b_bf16 v[224:227], v[228:229], v[172:173], v[224:227] cbsz:4 abid:2\n"
    "v_exp_f32 v158, v158 clamp\n"
    "v_exp_f32 v159, v159 clamp\n"
    "v_mfma_f32_4x4x4_16b_bf16 v[224:227], v[228:229], v[174:175], v[224:227] cbsz:4 abid:3\n"
    "s_waitcnt vmcnt(4)\n"
    "s_memrealtime s[34:35]\n"
    "ds_read_b64_tr_b16 v[168:169], v240 offset:0\n"
    "ds_read_b64_tr_b16 v[170:171], v241 offset:256\n"
    "ds_read_b64_tr_b16 v[172:173], v242 offset:512\n"
    "ds_read_b64_tr_b16 v[174:175], v243 offset:768\n"
    "ds_read_b32 v236, v253 offset:0\n"
    "ds_read_b32 v238, v253 offset:4\n"
    "ds_read_b128 v[160:163], v234 offset:2048\n"
    "ds_read_b128 v[164:167], v235 offset:2048\n"
    "v_pk_fma_f32 v[144:145], v[144:145], s[70:71], v[96:97]\n"
    "v_pk_fma_f32 v[146:147], v[146:147], s[70:71], v[98:99]\n"
    "v_pk_add_f32 v[230:231], v[144:145], v[146:147]\n"
    "v_pk_mul_f32 v[232:233], v[144:145], v[146:147]\n"
    "v_pk_fma_f32 v[148:149], v[148:149], s[70:71], v[100:101]\n"
    "v_pk_fma_f32 v[230:231], v[230:231], v[148:149], v[232:233]\n"
    "v_pk_mul_f32 v[232:233], v[232:233], v[148:149]\n"
    "v_pk_fma_f32 v[150:151], v[150:151], s[70:71], v[102:103]\n"
    "v_pk_fma_f32 v[230:231], v[230:231], v[150:151], v[232:233]\n"
    "v_pk_mul_f32 v[232:233], v[232:233], v[150:151]\n"
    "v_pk_fma_f32 v[152:153], v[152:153], s[70:71], v[104:105]\n"
    "v_pk_fma_f32 v[230:231], v[230:231], v[152:153], v[232:233]\n"
    "v_pk_mul_f32 v[232:233], v[232:233], v[152:153]\n"
    "v_pk_fma_f32 v[154:155], v[154:155], s[70:71], v[106:107]\n"
    "v_pk_fma_f32 v[230:231], v[230:231], v[154:155], v[232:233]\n"
    "v_pk_mul_f32 v[232:233], v[232:233], v[154:155]\n"
    "v_pk_fma_f32 v[156:157], v[156:157], s[70:71], v[108:109]\n"
    "v_pk_fma_f32 v[230:231], v[230:231], v[156:157], v[232:233]\n"
    "v_pk_mul_f32 v[232:233], v[232:233], v[156:157]\n"
    "v_pk_fma_f32 v[158:159], v[158:159], s[70:71], v[110:111]\n"
    "v_pk_fma_f32 v[230:231], v[230:231], v[158:159], v[232:233]\n"
    "v_pk_mul_f32 v[232:233], v[232:233], v[158:159]\n"
    "v_rcp_f32 v232, v232\n"
    "v_rcp_f32 v233, v233\n"
    "v_fma_f32 v230, v230, v232, v247\n"
    "v_fmac_f32 v230, v231, v233\n"
    "v_cvt_pk_bf16_f32 v230, v230, v230\n"
    "s_add_u32 s67, s67, 64\n"
    "s_waitcnt lgkmcnt(0)\n"
    "ds_write_b16 v245, v230\n"
    "ds_read_b64 v[228:229], v246\n"
    "s_cmp_le_u32 s34, s76\n"
    "s_cbranch_scc1 .Ldin_G3_wait\n"
    ".Ldin_G3_go:\n"
    "s_add_u32 s76, s76, s72\n"
    ".Ldin_G3_dma:\n"
    "buffer_load_dwordx4 v[236:237], s[40:43], 0 idxen offen offset:0 nt lds\n"
    "buffer_load_dwordx4 v[238:239], s[40:43], 0 idxen offen offset:1024 nt lds\n"
    ".Ldin_G3_nd:\n"
    "s_cmp_eq_u32 s67, s82\n"
    "s_cbranch_scc1 .Ldin_L0\n"
    ".Ldin_G0:\n"
    "v_mfma_f32_16x16x32_bf16 v[144:147], v[0:3], v[160:163], v[112:115]\n"
    "v_exp_f32 v128, v128 clamp\n"
    "v_mfma_f32_16x16x32_bf16 v[144:147], v[4:7], v[164:167], v[144:147]\n"
    "v_exp_f32 v129, v129 clamp\n"
    "v_mfma_f32_16x16x32_bf16 v[148:151], v[8:11], v[160:163], v[116:119]\n"
    "v_exp_f32 v130, v130 clamp\n"
    "v_mfma_f32_16x16x32_bf16 v[148:151], v[12:15], v[164:167], v[148:151]\n"
    "v_exp_f32 v131, v131 clamp\n"
    "v_mfma_f32_16x16x32_bf16 v[152:155], v[16:19], v[160:163], v[120:123]\n"
    "v_exp_f32 v132, v132 clamp\n"
    "v_mfma_f32_16x16x32_bf16 v[152:155], v[20:23], v[164:167], v[152:155]\n"
    "v_exp_f32 v133, v133 clamp\n"
    "v_mfma_f32_16x16x32_bf16 v[156:159], v[24:27], v[160:163], v[124:127]\n"
    "v_exp_f32 v134, v134 clamp\n"
    "v_mfma_f32_16x16x32_bf16 v[156:159], v[28:31], v[164:167], v[156:159]\n"
    "v_exp_f32 v135, v135 clamp\n"
    "v_exp_f32 v136, v136 clamp\n"
    "s_cmp_eq_u32 s67, s80\n"
    "s_cselect_b32 s67, s65, s67\n"
    "s_add_u32 m0, s68, 2048\n"
    "v_exp_f32 v137, v137 clamp\n"
    "v_add_u32_e64 v253, s67, v244\n"
    "s_waitcnt lgkmcnt(0)\n"
    "v_mfma_f32_4x4x4_16b_bf16 v[224:227], v[228:229], v[176:177], v[224:227] cbsz:4 abid:0\n"
    "v_exp_f32 v138, v138 clamp\n"
    "v_exp_f32 v139, v139 clamp\n"
    "v_mfma_f32_4x4x4_16b_bf16 v[224:227], v[228:229], v[178:179], v[224:227] cbsz:4 abid:1\n"
    "v_exp_f32 v140, v140 clamp\n"
    "v_exp_f32 v141, v141 clamp\n"
    "v_mfma_f32_4x4x4_16b_bf16 v[224:227], v[228:229], v[180:181], v[224:227] cbsz:4 abid:2\n"
    "v_exp_f32 v142, v142 clamp\n"
    "v_exp_f32 v143, v143 clamp\n"
    "v_mfma_f32_4x4x4_16b_bf16 v[224:227], v[228:229], v[182:183], v[224:227] cbsz:4 abid:3\n"
    "s_waitcnt vmcnt(4)\n"
    "s_memrealtime s[34:35]\n"
    "ds_read_b64_tr_b16 v[176:177], v240 offset:2048\n"
    "ds_read_b64_tr_b16 v[178:179], v241 offset:2304\n"
    "ds_read_b64_tr_b16 v[180:181], v242 offset:2560\n"
    "ds_read_b64_tr_b16 v[182:183], v243 offset:2816\n"
    "ds_read_b32 v236, v253 offset:0\n"
    "ds_read_b32 v238, v253 offset:4\n"
    "ds_read_b128 v[160:163], v234 offset:4096\n"
    "ds_read_b128 v[164:167], v235 offset:4096\n"
    "v_pk_fma_f32 v[128:129], v[128:129], s[70:71], v[96:97]\n"
    "v_pk_fma_f32 v[130:131], v[130:131], s[70:71], v[98:99]\n"
    "v_pk_add_f32 v[230:231], v[128:129], v[130:131]\n"
    "v_pk_mul_f32 v[232:233], v[128:129], v[130:131]\n"
    "v_pk_fma_f32 v[132:133], v[132:133], s[70:71], v[100:101]\n"
    "v_pk_fma_f32 v[230:231], v[230:231], v[132:133], v[232:233]\n"
    "v_pk_mul_f32 v[232:233], v[232:233], v[132:133]\n"
    "v_pk_fma_f32 v[134:135], v[134:135], s[70:71], v[102:103]\n"
    "v_pk_fma_f32 v[230:231], v[230:231], v[134:135], v[232:233]\n"
    "v_pk_mul_f32 v[232:233], v[232:233], v[134:135]\n"
    "v_pk_fma_f32 v[136:137], v[136:137], s[70:71], v[104:105]\n"
    "v_pk_fma_f32 v[230:231], v[230:231], v[136:137], v[232:233]\n"
    "v_pk_mul_f32 v[232:233], v[232:233], v[136:137]\n"
    "v_pk_fma_f32 v[138:139], v[138:139], s[70:71], v[106:107]\n"
    "v_pk_fma_f32 v[230:231], v[230:231], v[138:139], v[232:233]\n"
    "v_pk_mul_f32 v[232:233], v[232:233], v[138:139]\n"
    "v_pk_fma_f32 v[140:141], v[140:141], s[70:71], v[108:109]\n"
    "v_pk_fma_f32 v[230:231], v[230:231], v[140:141], v[232:233]\n"
    "v_pk_mul_f32 v[232:233], v[232:233], v[140:141]\n"
    "v_pk_fma_f32 v[142:143], v[142:143], s[70:71], v[110:111]\n"
    "v_pk_fma_f32 v[230:231], v[230:231], v[142:143], v[232:233]\n"
    "v_pk_mul_f32 v[232:233], v[232:233], v[142:143]\n"
    "v_rcp_f32 v232, v232\n"
    "v_rcp_f32 v233, v233\n"
    "v_fma_f32 v230, v230, v232, v247\n"
    "v_fmac_f32 v230, v231, v233\n"
    "v_cvt_pk_bf16_f32 v230, v230, v230\n"
    "s_add_u32 s67, s67, 64\n"
    "s_waitcnt lgkmcnt(0)\n"
    "ds_write_b16 v245, v230\n"
    "ds_read_b64 v[228:229], v246\n"
    "s_cmp_le_u32 s34, s76\n"
    "s_cbranch_scc1 .Ldin_G0_wait\n"
    ".Ldin_G0_go:\n"
    "s_sub_u32 s94, s34, 60\n"
    "s_max_u32 s76, s76, s94\n"
    "s_add_u32 s76, s76, s72\n"
    ".Ldin_G0_dma:\n"
    "buffer_load_dwordx4 v[236:237], s[40:43], 0 idxen offen offset:0 nt lds\n"
    "buffer_load_dwordx4 v[238:239], s[40:43], 0 idxen offen offset:1024 nt lds\n"
    ".Ldin_G0_nd:\n"
    "s_cmp_eq_u32 s67, s82\n"
    "s_cbranch_scc1 .Ldin_L1\n"
    ".Ldin_G1:\n"
    "v_mfma_f32_16x16x32_bf16 v[128:131], v[0:3], v[160:163], v[112:115]\n"
    "v_exp_f32 v144, v144 clamp\n"
    "v_mfma_f32_16x16x32_bf16 v[128:131], v[4:7], v[164:167], v[128:131]\n"
    "v_exp_f32 v145, v145 clamp\n"
    "v_mfma_f32_16x16x32_bf16 v[132:135], v[8:11], v[160:163], v[116:119]\n"
    "v_exp_f32 v146, v146 clamp\n"
    "v_mfma_f32_16x16x32_bf16 v[132:135], v[12:15], v[164:167], v[132:135]\n"
    "v_exp_f32 v147, v147 clamp\n"
    "v_mfma_f32_16x16x32_bf16 v[136:139], v[16:19], v[160:163], v[120:123]\n"
    "v_exp_f32 v148, v148 clamp\n"
    "v_mfma_f32_16x16x32_bf16 v[136:139], v[20:23], v[164:167], v[136:139]\n"
    "v_exp_f32 v149, v149 clamp\n"
    "v_mfma_f32_16x16x32_bf16 v[140:143], v[24:27], v[160:163], v[124:127]\n"
    "v_exp_f32 v150, v150 clamp\n"
    "v_mfma_f32_16x16x32_bf16 v[140:143], v[28:31], v[164:167], v[140:143]\n"
    "v_exp_f32 v151, v151 clamp\n"
    "v_exp_f32 v152, v152 clamp\n"
    "s_cmp_eq_u32 s67, s80\n"
    "s_cselect_b32 s67, s65, s67\n"
    "s_add_u32 m0, s68, 4096\n"
    "v_exp_f32 v153, v153 clamp\n"
    "v_add_u32_e64 v253, s67, v244\n"
    "s_waitcnt lgkmcnt(0)\n"
    "v_mfma_f32_4x4x4_16b_bf16 v[224:227], v[228:229], v[168:169], v[224:227] cbsz:4 abid:0\n"
    "v_exp_f32 v154, v154 clamp\n"
    "v_exp_f32 v155, v155 clamp\n"
    "v_mfma_f32_4x4x4_16b_bf16 v[224:227], v[228:229], v[170:171], v[224:227] cbsz:4 abid:1\n"
    "v_exp_f32 v156, v156 clamp\n"
    "v_exp_f32 v157, v157 clamp\n"
    "v_mfma_f32_4x4x4_16b_bf16 v[224:227], v[228:229], v[172:173], v[224:227] cbsz:4 abid:2\n"
    "v_exp_f32 v158, v158 clamp\n"
    "v_exp_f32 v159, v159 clamp\n"
    "v_mfma_f32_4x4x4_16b_bf16 v[224:227], v[228:229], v[174:175], v[224:227] cbsz:4 abid:3\n"
    "s_waitcnt vmcnt(4)\n"
    "s_memrealtime s[34:35]\n"
    "ds_read_b64_tr_b16 v[168:169], v240 offset:4096\n"
    "ds_read_b64_tr_b16 v[170:171], v241 offset:4352\n"
    "ds_read_b64_tr_b16 v[172:173], v242 offset:4608\n"
    "ds_read_b64_tr_b16 v[174:175], v243 offset:4864\n"
    "ds_read_b32 v236, v253 offset:0\n"
    "ds_read_b32 v238, v253 offset:4\n"
    "ds_read_b128 v[160:163], v234 offset:6144\n"
    "ds_read_b128 v[164:167], v235 offset:6144\n"
    "v_pk_fma_f32 v[144:145], v[144:145], s[70:71], v[96:97]\n"
    "v_pk_fma_f32 v[146:147], v[146:147], s[70:71], v[98:99]\n"
    "v_pk_add_f32 v[230:231], v[144:145], v[146:147]\n"
    "v_pk_mul_f32 v[232:233], v[144:145], v[146:147]\n"
    "v_pk_fma_f32 v[148:149], v[148:149], s[70:71], v[100:101]\n"
    "v_pk_fma_f32 v[230:231], v[230:231], v[148:149], v[232:233]\n"
    "v_pk_mul_f32 v[232:233], v[232:233], v[148:149]\n"
    "v_pk_fma_f32 v[150:151], v[150:151], s[70:71], v[102:103]\n"
    "v_pk_fma_f32 v[230:231], v[230:231], v[150:151], v[232:233]\n"
    "v_pk_mul_f32 v[232:233], v[232:233], v[150:151]\n"
    "v_pk_fma_f32 v[152:153], v[152:153], s[70:71], v[104:105]\n"
    "v_pk_fma_f32 v[230:231], v[230:231], v[152:153], v[232:233]\n"
    "v_pk_mul_f32 v[232:233], v[232:233], v[152:153]\n"
    "v_pk_fma_f32 v[154:155], v[154:155], s[70:71], v[106:107]\n"
    "v_pk_fma_f32 v[230:231], v[230:231], v[154:155], v[232:233]\n"
    "v_pk_mul_f32 v[232:233], v[232:233], v[154:155]\n"
    "v_pk_fma_f32 v[156:157], v[156:157], s[70:71], v[108:109]\n"
    "v_pk_fma_f32 v[230:231], v[230:231], v[156:157], v[232:233]\n"
    "v_pk_mul_f32 v[232:233], v[232:233], v[156:157]\n"
    "v_pk_fma_f32 v[158:159], v[158:159], s[70:71], v[110:111]\n"
    "v_pk_fma_f32 v[230:231], v[230:231], v[158:159], v[232:233]\n"
    "v_pk_mul_f32 v[232:233], v[232:233], v[158:159]\n"
    "v_rcp_f32 v232, v232\n"
    "v_rcp_f32 v233, v233\n"
    "v_fma_f32 v230, v230, v232, v247\n"
    "v_fmac_f32 v230, v231, v233\n"
    "v_cvt_pk_bf16_f32 v230, v230, v230\n"
    "s_add_u32 s67, s67, 64\n"
    "s_waitcnt lgkmcnt(0)\n"
    "ds_write_b16 v245, v230\n"
    "ds_read_b64 v[228:229], v246\n"
    "s_cmp_le_u32 s34, s76\n"
    "s_cbranch_scc1 .Ldin_G1_wait\n"
    ".Ldin_G1_go:\n"
    "s_add_u32 s76, s76, s72\n"
    ".Ldin_G1_dma:\n"
    "buffer_load_dwordx4 v[236:237], s[40:43], 0 idxen offen offset:0 nt lds\n"
    "buffer_load_dwordx4 v[238:239], s[40:43], 0 idxen offen offset:1024 nt lds\n"
    ".Ldin_G1_nd:\n"
    "s_cmp_eq_u32 s67, s82\n"
    "s_cbranch_scc1 .Ldin_L2\n"
    "s_branch .Ldin_G2\n"
    ".Ldin_B1_wait:\n"
    "s_cmp_lt_i32 s59, s57\n"
    "s_cbranch_scc1 .Ldin_B1_w2\n"
    "s_sub_u32 s94, s67, s65\n"
    "s_cmp_gt_u32 s94, 256\n"
    "s_cbranch_scc1 .Ldin_B1_dma\n"
    "s_cmp_eq_u32 s94, 64\n"
    "s_waitcnt vmcnt(2)\n"
    "s_cbranch_scc1 .Ldin_B1_nd\n"
    "s_waitcnt vmcnt(0)\n"
    "s_branch .Ldin_B1_nd\n"
    ".Ldin_B1_slp:\n"
    "s_sleep 1\n"
    ".Ldin_B1_w2:\n"
    "s_memrealtime s[34:35]\n"
    "s_waitcnt lgkmcnt(0)\n"
    "s_sub_u32 s94, s34, s76\n"
    "s_cmp_lt_i32 s94, 0\n"
    "s_cbranch_scc1 .Ldin_B1_slp\n"
    "s_branch .Ldin_B1_go\n"
    ".Ldin_G2_wait:\n"
    "s_cmp_lt_i32 s59, s57\n"
    "s_cbranch_scc1 .Ldin_G2_w2\n"
    "s_sub_u32 s94, s67, s65\n"
    "s_cmp_gt_u32 s94, 256\n"
    "s_cbranch_scc1 .Ldin_G2_dma\n"
    "s_cmp_eq_u32 s94, 64\n"
    "s_waitcnt vmcnt(2)\n"
    "s_cbranch_scc1 .Ldin_G2_nd\n"
    "s_waitcnt vmcnt(0)\n"
    "s_branch .Ldin_G2_nd\n"
    ".Ldin_G2_slp:\n"
    "s_sleep 1\n"
    ".Ldin_G2_w2:\n"
    "s_memrealtime s[34:35]\n"
    "s_waitcnt lgkmcnt(0)\n"
    "s_sub_u32 s94, s34, s76\n"
    "s_cmp_lt_i32 s94, 0\n"
    "s_cbranch_scc1 .Ldin_G2_slp\n"
    "s_branch .Ldin_G2_go\n"
    ".Ldin_G3_wait:\n"
    "s_cmp_lt_i32 s59, s57\n"
    "s_cbranch_scc1 .Ldin_G3_w2\n"
    "s_sub_u32 s94, s67, s65\n"
    "s_cmp_gt_u32 s94, 256\n"
    "s_cbranch_scc1 .Ldin_G3_dma\n"
    "s_cmp_eq_u32 s94, 64\n"
    "s_waitcnt vmcnt(2)\n"
    "s_cbranch_scc1 .Ldin_G3_nd\n"
    "s_waitcnt vmcnt(0)\n"
    "s_branch .Ldin_G3_nd\n"
    ".Ldin_G3_slp:\n"
    "s_sleep 1\n"
    ".Ldin_G3_w2:\n"
    "s_memrealtime s[34:35]\n"
    "s_waitcnt lgkmcnt(0)\n"
    "s_sub_u32 s94, s34, s76\n"
    "s_cmp_lt_i32 s94, 0\n"
    "s_cbranch_scc1 .Ldin_G3_slp\n"
    "s_branch .Ldin_G3_go\n"
    ".Ldin_G0_wait:\n"
    "s_cmp_lt_i32 s59, s57\n"
    "s_cbranch_scc1 .Ldin_G0_w2\n"
    "s_sub_u32 s94, s67, s65\n"
    "s_cmp_gt_u32 s94, 256\n"
    "s_cbranch_scc1 .Ldin_G0_dma\n"
    "s_cmp_eq_u32 s94, 64\n"
    "s_waitcnt vmcnt(2)\n"
    "s_cbranch_scc1 .Ldin_G0_nd\n"
    "s_waitcnt vmcnt(0)\n"
    "s_branch .Ldin_G0_nd\n"
    ".Ldin_G0_slp:\n"
    "s_sleep 1\n"
    ".Ldin_G0_w2:\n"
    "s_memrealtime s[34:35]\n"
    "s_waitcnt lgkmcnt(0)\n"
    "s_sub_u32 s94, s34, s76\n"
    "s_cmp_lt_i32 s94, 0\n"
    "s_cbranch_scc1 .Ldin_G0_slp\n"
    "s_branch .Ldin_G0_go\n"
    ".Ldin_G1_wait:\n"
    "s_cmp_lt_i32 s59, s57\n"
    "s_cbranch_scc1 .Ldin_G1_w2\n"
    "s_sub_u32 s94, s67, s65\n"
    "s_cmp_gt_u32 s94, 256\n"
    "s_cbranch_scc1 .Ldin_G1_dma\n"
    "s_cmp_eq_u32 s94, 64\n"
    "s_waitcnt vmcnt(2)\n"
    "s_cbranch_scc1 .Ldin_G1_nd\n"
    "s_waitcnt vmcnt(0)\n"
    "s_branch .Ldin_G1_nd\n"
    ".Ldin_G1_slp:\n"
    "s_sleep 1\n"
    ".Ldin_G1_w2:\n"
    "s_memrealtime s[34:35]\n"
    "s_waitcnt lgkmcnt(0)\n"
    "s_sub_u32 s94, s34, s76\n"
    "s_cmp_lt_i32 s94, 0\n"
    "s_cbranch_scc1 .Ldin_G1_slp\n"
    "s_branch .Ldin_G1_go\n"
    ".p2align 5\n"
    ".Ldin_stub_4:\n"
    "s_cbranch_execz .Ldin_t1_ret\n"
    "s_nop 0\n"
    ".Ldin_y2:\n"
    ".Ldin_t1_ret:\n"
    "s_mov_b64 exec, -1\n"
    "s_waitcnt vmcnt(8)\n"
    "v_lshlrev_b32 v16, 16, v8\n"
    "v_lshlrev_b32 v18, 16, v9\n"
    "v_lshlrev_b32 v20, 16, v10\n"
    "v_lshlrev_b32 v22, 16, v11\n"
    "v_bitop3_b32 v16, v16, s84, v7 bitop3:0x6c\n"
    "v_bitop3_b32 v17, v8, s84, v7 bitop3:0x6c\n"
    "v_bitop3_b32 v18, v18, s84, v7 bitop3:0x6c\n"
    "v_bitop3_b32 v19, v9, s84, v7 bitop3:0x6c\n"
    "v_bitop3_b32 v20, v20, s84, v7 bitop3:0x6c\n"
    "v_bitop3_b32 v21, v10, s84, v7 bitop3:0x6c\n"
    "v_bitop3_b32 v22, v22, s84, v7 bitop3:0x6c\n"
    "v_bitop3_b32 v23, v11, s84, v7 bitop3:0x6c\n"
    "s_waitcnt vmcnt(6)\n"
    "buffer_load_dwordx4 v[32:35], v27, s[44:47], s39 offen nt\n"
    "s_add_u32 s39, s39, s17\n"
    "v_lshlrev_b32 v112, 16, v128\n"
    "v_and_b32 v114, s37, v128\n"
    "v_lshlrev_b32 v116, 16, v129\n"
    "v_and_b32 v118, s37, v129\n"
    "v_lshlrev_b32 v120, 16, v130\n"
    "v_and_b32 v122, s37, v130\n"
    "v_lshlrev_b32 v124, 16, v131\n"
    "v_and_b32 v126, s37, v131\n"
    "buffer_load_dwordx4 v[36:39], v27, s[44:47], s39 offen nt\n"
    "s_add_u32 s39, s39, s17\n"
    "v_lshlrev_b32 v113, 16, v132\n"
    "v_and_b32 v115, s37, v132\n"
    "v_lshlrev_b32 v117, 16, v133\n"
    "v_and_b32 v119, s37, v133\n"
    "v_lshlrev_b32 v121, 16, v134\n"
    "v_and_b32 v123, s37, v134\n"
    "v_lshlrev_b32 v125, 16, v135\n"
    "v_and_b32_e64 v127, s37, v135\n"
    "v_pk_mul_f32 v[112:113], v[112:113], v[16:17] op_sel_hi:[1,0]\n"
    "v_pk_mul_f32 v[114:115], v[114:115], v[16:17] op_sel:[0,1] op_sel_hi:[1,1]\n"
    "v_pk_mul_f32 v[116:117], v[116:117], v[18:19] op_sel_hi:[1,0]\n"
    "v_pk_mul_f32 v[118:119], v[118:119], v[18:19] op_sel:[0,1] op_sel_hi:[1,1]\n"
    "v_pk_mul_f32 v[120:121], v[120:121], v[20:21] op_sel_hi:[1,0]\n"
    "v_pk_mul_f32 v[122:123], v[122:123], v[20:21] op_sel:[0,1] op_sel_hi:[1,1]\n"
    "v_pk_mul_f32 v[124:125], v[124:125], v[22:23] op_sel_hi:[1,0]\n"
    "v_pk_mul_f32 v[126:127], v[126:127], v[22:23] op_sel:[0,1] op_sel_hi:[1,1]\n"
    "s_waitcnt vmcnt(6)\n"
    "buffer_load_dwordx4 v[40:43], v27, s[44:47], s39 offen nt\n"
    "s_add_u32 s39, s39, s17\n"
    "v_lshlrev_b32 v168, 16, v136\n"
    "v_and_b32 v170, s37, v136\n"
    "v_lshlrev_b32 v172, 16, v137\n"
    "v_and_b32 v174, s37, v137\n"
    "v_lshlrev_b32 v176, 16, v138\n"
    "v_and_b32 v178, s37, v138\n"
    "v_lshlrev_b32 v180, 16, v139\n"
    "v_and_b32 v182, s37, v139\n"
    "buffer_load_dwordx4 v[44:47], v27, s[44:47], s39 offen nt\n"
    "s_add_u32 s39, s39, s17\n"
    "v_lshlrev_b32 v169, 16, v140\n"
    "v_and_b32 v171, s37, v140\n"
    "v_lshlrev_b32 v173, 16, v141\n"
    "v_and_b32 v175, s37, v141\n"
    "v_lshlrev_b32 v177, 16, v142\n"
    "v_and_b32 v179, s37, v142\n"
    "v_lshlrev_b32 v181, 16, v143\n"
    "v_and_b32_e64 v183, s37, v143\n"
    "v_pk_fma_f32 v[168:169], v[168:169], v[16:17], v[112:113] op_sel_hi:[1,0,1]\n"
    "v_pk_fma_f32 v[170:171], v[170:171], v[16:17], v[114:115] op_sel:[0,1,0] op_sel_hi:[1,1,1]\n"
    "ds_write2st64_b64 v12, v[168:169], v[170:171] offset0:0 offset1:8\n"
    "v_pk_fma_f32 v[172:173], v[172:173], v[18:19], v[116:117] op_sel_hi:[1,0,1]\n"
    "v_pk_fma_f32 v[174:175], v[174:175], v[18:19], v[118:119] op_sel:[0,1,0] op_sel_hi:[1,1,1]\n"
    "ds_write2st64_b64 v12, v[172:173], v[174:175] offset0:16 offset1:24\n"
    "v_pk_fma_f32 v[176:177], v[176:177], v[20:21], v[120:121] op_sel_hi:[1,0,1]\n"
    "v_pk_fma_f32 v[178:179], v[178:179], v[20:21], v[122:123] op_sel:[0,1,0] op_sel_hi:[1,1,1]\n"
    "ds_write2st64_b64 v13, v[176:177], v[178:179] offset0:0 offset1:8\n"
    "v_pk_fma_f32 v[180:181], v[180:181], v[22:23], v[124:125] op_sel_hi:[1,0,1]\n"
    "v_pk_fma_f32 v[182:183], v[182:183], v[22:23], v[126:127] op_sel:[0,1,0] op_sel_hi:[1,1,1]\n"
    "ds_write2st64_b64 v13, v[180:181], v[182:183] offset0:16 offset1:24\n"
    "s_waitcnt vmcnt(6)\n"
    "buffer_load_dwordx4 v[48:51], v27, s[44:47], s39 offen nt\n"
    "s_add_u32 s39, s39, s17\n"
    "v_lshlrev_b32 v80, 16, v144\n"
    "v_and_b32 v82, s37, v144\n"
    "v_lshlrev_b32 v84, 16, v145\n"
    "v_and_b32 v86, s37, v145\n"
    "v_lshlrev_b32 v88, 16, v146\n"
    "v_and_b32 v90, s37, v146\n"
    "v_lshlrev_b32 v92, 16, v147\n"
    "v_and_b32 v94, s37, v147\n"
    "buffer_load_dwordx4 v[52:55], v27, s[44:47], s39 offen nt\n"
    "v_lshlrev_b32 v81, 16, v148\n"
    "v_and_b32 v83, s37, v148\n"
    "v_lshlrev_b32 v85, 16, v149\n"
    "v_and_b32 v87, s37, v149\n"
    "v_lshlrev_b32 v89, 16, v150\n"
    "v_and_b32 v91, s37, v150\n"
    "v_lshlrev_b32 v93, 16, v151\n"
    "v_and_b32 v95, s37, v151\n"
    "v_pk_mul_f32 v[80:81], v[80:81], v[16:17] op_sel_hi:[1,0]\n"
    "v_cvt_pk_bf16_f32 v80, v80, v81\n"
    "v_pk_mul_f32 v[82:83], v[82:83], v[16:17] op_sel:[0,1] op_sel_hi:[1,1]\n"
    "v_cvt_pk_bf16_f32 v82, v82, v83\n"
    "ds_write2st64_b32 v14, v80, v82 offset0:0 offset1:8\n"
    "v_pk_mul_f32 v[84:85], v[84:85], v[18:19] op_sel_hi:[1,0]\n"
    "v_cvt_pk_bf16_f32 v84, v84, v85\n"
    "v_pk_mul_f32 v[86:87], v[86:87], v[18:19] op_sel:[0,1] op_sel_hi:[1,1]\n"
    "v_cvt_pk_bf16_f32 v86, v86, v87\n"
    "ds_write2st64_b32 v14, v84, v86 offset0:16 offset1:24\n"
    "v_pk_mul_f32 v[88:89], v[88:89], v[20:21] op_sel_hi:[1,0]\n"
    "v_cvt_pk_bf16_f32 v88, v88, v89\n"
    "v_pk_mul_f32 v[90:91], v[90:91], v[20:21] op_sel:[0,1] op_sel_hi:[1,1]\n"
    "v_cvt_pk_bf16_f32 v90, v90, v91\n"
    "ds_write2st64_b32 v15, v88, v90 offset0:0 offset1:8\n"
    "v_pk_mul_f32 v[92:93], v[92:93], v[22:23] op_sel_hi:[1,0]\n"
    "v_cvt_pk_bf16_f32 v92, v92, v93\n"
    "v_pk_mul_f32 v[94:95], v[94:95], v[22:23] op_sel:[0,1] op_sel_hi:[1,1]\n"
    "v_cvt_pk_bf16_f32 v94, v94, v95\n"
    "ds_write2st64_b32 v15, v92, v94 offset0:16 offset1:24\n"
    "s_waitcnt vmcnt(6)\n"
    "v_lshlrev_b32 v184, 16, v152\n"
    "v_and_b32 v186, s37, v152\n"
    "v_lshlrev_b32 v188, 16, v153\n"
    "v_and_b32 v190, s37, v153\n"
    "v_lshlrev_b32 v192, 16, v154\n"
    "v_and_b32 v194, s37, v154\n"
    "v_lshlrev_b32 v196, 16, v155\n"
    "v_and_b32 v198, s37, v155\n"
    "v_lshlrev_b32 v185, 16, v156\n"
    "v_and_b32 v187, s37, v156\n"
    "v_lshlrev_b32 v189, 16, v157\n"
    "v_and_b32 v191, s37, v157\n"
    "v_lshlrev_b32 v193, 16, v158\n"
    "v_and_b32 v195, s37, v158\n"
    "v_lshlrev_b32 v197, 16, v159\n"
    "v_and_b32_e64 v199, s37, v159\n"
    "s_mul_i32 s36, s61, s16\n"
    "s_add_u32 s94, s36, s16\n"
    "s_cmp_lt_u32 s61, s14\n"
    "s_cselect_b32 s46, s94, 0\n"
    "buffer_load_dwordx2 v[228:229], v105, s[44:47], s36 offen nt\n"
    "s_lshl_b32 s94, s61, 6\n"
    "s_add_u32 s94, s3, s94\n"
    "s_cmp_lt_u32 s61, s14\n"
    "s_cselect_b32 s58, s94, s13\n"
    "s_add_u32 s95, s61, s15\n"
    "s_add_u32 s36, s36, s17\n"
    "s_lshl_b32 s94, s95, 6\n"
    "s_add_u32 s94, s3, s94\n"
    "s_add_u32 s96, s36, s16\n"
    "s_add_u32 s94, s94, 8\n"
    "s_cmp_lt_u32 s95, s14\n"
    "s_cselect_b32 s46, s96, 0\n"
    "s_cselect_b32 s59, s94, s13\n"
    "buffer_load_short_d16_hi v224, v249, s[44:47], s36 offen offset:0 nt\n"
    "buffer_load_short_d16_hi v225, v249, s[44:47], s36 offen offset:128 nt\n"
    "buffer_load_short_d16_hi v226, v249, s[44:47], s36 offen offset:256 nt\n"
    "buffer_load_short_d16_hi v227, v249, s[44:47], s36 offen offset:384 nt\n"
    "s_mul_i32 s46, s14, s16\n"
    "s_waitcnt lgkmcnt(0)\n"
    "s_barrier\n"
    "ds_read_b128 v[128:131], v97 offset:0\n"
    "ds_read_b128 v[132:135], v97 offset:16\n"
    "ds_read_b128 v[28:31], v99 offset:35200\n"
    "v_pk_fma_f32 v[184:185], v[184:185], v[16:17], v[112:113] op_sel_hi:[1,0,1] neg_lo:[0,0,1] neg_hi:[0,0,1]\n"
    "v_cvt_pk_bf16_f32 v184, v184, v185\n"
    "v_pk_fma_f32 v[186:187], v[186:187], v[16:17], v[114:115] op_sel:[0,1,0] op_sel_hi:[1,1,1] neg_lo:[0,0,1] neg_hi:[0,0,1]\n"
    "v_cvt_pk_bf16_f32 v186, v186, v187\n"
    "ds_write2st64_b32 v14, v184, v186 offset0:32 offset1:40\n"
    "v_pk_fma_f32 v[188:189], v[188:189], v[18:19], v[116:117] op_sel_hi:[1,0,1] neg_lo:[0,0,1] neg_hi:[0,0,1]\n"
    "v_cvt_pk_bf16_f32 v188, v188, v189\n"
    "v_pk_fma_f32 v[190:191], v[190:191], v[18:19], v[118:119] op_sel:[0,1,0] op_sel_hi:[1,1,1] neg_lo:[0,0,1] neg_hi:[0,0,1]\n"
    "v_cvt_pk_bf16_f32 v190, v190, v191\n"
    "ds_write2st64_b32 v14, v188, v190 offset0:48 offset1:56\n"
    "v_pk_fma_f32 v[192:193], v[192:193], v[20:21], v[120:121] op_sel_hi:[1,0,1] neg_lo:[0,0,1] neg_hi:[0,0,1]\n"
    "v_cvt_pk_bf16_f32 v192, v192, v193\n"
    "v_pk_fma_f32 v[194:195], v[194:195], v[20:21], v[122:123] op_sel:[0,1,0] op_sel_hi:[1,1,1] neg_lo:[0,0,1] neg_hi:[0,0,1]\n"
    "v_cvt_pk_bf16_f32 v194, v194, v195\n"
    "ds_write2st64_b32 v15, v192, v194 offset0:32 offset1:40\n"
    ".Ldin_stub_5:\n"
    "s_cbranch_execz .Ldin_t1_ret\n"
    "s_nop 0\n"
    "v_pk_fma_f32 v[196:197], v[196:197], v[22:23], v[124:125] op_sel_hi:[1,0,1] neg_lo:[0,0,1] neg_hi:[0,0,1]\n"
    "v_cvt_pk_bf16_f32 v196, v196, v197\n"
    "v_pk_fma_f32 v[198:199], v[198:199], v[22:23], v[126:127] op_sel:[0,1,0] op_sel_hi:[1,1,1] neg_lo:[0,0,1] neg_hi:[0,0,1]\n"
    "v_cvt_pk_bf16_f32 v198, v198, v199\n"
    "ds_write2st64_b32 v15, v196, v198 offset0:48 offset1:56\n"
    "s_waitcnt lgkmcnt(0)\n"
    "s_barrier\n"
    "ds_read_b128 v[8:11], v98 offset:0\n"
    "ds_read_b128 v[12:15], v98 offset:1024\n"
    "s_waitcnt lgkmcnt(1)\n"
    "v_mfma_f32_16x16x32_bf16 v[112:115], v[8:11], v[128:131], v[28:31]\n"
    "s_waitcnt lgkmcnt(0)\n"
    "v_mfma_f32_16x16x32_bf16 v[112:115], v[12:15], v[132:135], v[112:115]\n"
    "s_nop 7\n"
    "ds_write_b128 v100, v[112:115] offset:17408\n"
    "s_waitcnt lgkmcnt(0)\n"
    "s_barrier\n"
    "s_setprio 0\n"
    "ds_read_b128 v[184:187], v252 offset:0\n"
    "ds_read_b128 v[188:191], v252 offset:16\n"
    "ds_read_b32 v247, v250 offset:35072\n"
    "ds_read_b32 v232, v250 offset:35076\n"
    "s_and_b32 s84, s12, 7\n"
    "v_sub_u32 v4, s16, v26\n"
    "s_lshl_b32 s84, -1, s84\n"
    "v_cmp_gt_i32 vcc, 16, v4\n"
    "s_not_b32 s84, s84\n"
    "v_mov_b32 v6, s84\n"
    "s_add_u32 s85, s66, 480\n"
    "v_cndmask_b32 v6, -1, v6, vcc\n"
    "v_and_b32 v4, 32, v251\n"
    "v_add3_u32 v7, v4, v251, s85\n"
    "v_mov_b32 v5, 0\n"
    "s_mov_b32 s4, 0x10001\n"
    "s_mov_b32 s5, 0xc080c080\n"
    "s_waitcnt lgkmcnt(0)\n"
    "v_readfirstlane_b32 s69, v232\n"
    "v_and_b32 v184, v254, v184\n"
    "v_and_b32 v185, v255, v185\n"
    "v_and_b32 v186, v254, v186\n"
    "v_and_b32 v187, v255, v187\n"
    "v_and_b32 v188, v254, v188\n"
    "v_and_b32 v189, v255, v189\n"
    "v_and_b32 v190, v254, v190\n"
    "v_and_b32 v191, v255, v191\n"
    "s_waitcnt vmcnt(4)\n"
    "v_cmp_ne_u16_e64 s[84:85], 0, v228\n"
    "v_cmp_lt_u32_e64 s[86:87], s38, v228\n"
    "v_cmp_ne_u16_e64 s[88:89], 0, v229\n"
    "v_cmp_lt_u32_e64 s[90:91], s38, v229\n"
    "v_mbcnt_lo_u32_b32 v220, s84, v220\n"
    "s_bcnt1_i32_b64 s92, s[84:85]\n"
    "v_mbcnt_hi_u32_b32 v220, s85, v220\n"
    "s_lshl2_add_u32 s92, s92, s67\n"
    "v_mbcnt_lo_u32_b32 v220, s86, v220\n"
    "s_bcnt1_i32_b64 s93, s[86:87]\n"
    "v_mbcnt_hi_u32_b32 v220, s87, v220\n"
    "s_lshl2_add_u32 s93, s93, s92\n"
    "v_mbcnt_lo_u32_b32 v220, s88, v220\n"
    "s_bcnt1_i32_b64 s94, s[88:89]\n"
    "v_mbcnt_hi_u32_b32 v220, s89, v220\n"
    "s_lshl2_add_u32 s94, s94, s93\n"
    "v_mbcnt_lo_u32_b32 v220, s90, v220\n"
    "s_bcnt1_i32_b64 s95, s[90:91]\n"
    "v_mbcnt_hi_u32_b32 v220, s91, v220\n"
    "s_lshl2_add_u32 s95, s95, s94\n"
    "v_mov_b32_e64 v21, -1\n"
    "v_lshl_add_u32 v20, v249, 1, s95\n"
    "v_addc_co_u32_e64 v221, vcc, v220, 0, s[84:85]\n"
    "v_addc_co_u32_e64 v222, vcc, v221, 0, s[86:87]\n"
    "v_addc_co_u32_e64 v223, vcc, v222, 0, s[88:89]\n"
    "ds_write2_b32 v20, v21, v21 offset1:64\n"
    "v_lshlrev_b32 v220, 2, v220\n"
    "v_lshlrev_b32 v221, 2, v221\n"
    "v_lshlrev_b32 v222, 2, v222\n"
    "v_lshlrev_b32_e64 v223, 2, v223\n"
    "s_mov_b64 exec, s[84:85]\n"
    "ds_write_b32 v220, v216\n"
    "s_mov_b64 exec, s[86:87]\n"
    "ds_write_b32 v221, v217\n"
    "s_mov_b64 exec, s[88:89]\n"
    "ds_write_b32 v222, v218\n"
    "s_mov_b64 exec, s[90:91]\n"
    "ds_write_b32 v223, v219\n"
    "s_mov_b64 exec, -1\n"
    "ds_read_b32 v200, v253 offset:0\n"
    "ds_read_b32 v202, v253 offset:4\n"
    "ds_read_b32 v204, v253 offset:64\n"
    "ds_read_b32 v206, v253 offset:68\n"
    "ds_read_b32 v208, v253 offset:128\n"
    "ds_read_b32 v210, v253 offset:132\n"
    "ds_read_b32 v212, v253 offset:192\n"
    "ds_read_b32 v214, v253 offset:196\n"
    "s_add_u32 m0, s68, 0\n"
    "s_waitcnt lgkmcnt(6)\n"
    "buffer_load_dwordx4 v[200:201], s[40:43], 0 idxen offen offset:0 nt lds\n"
    "buffer_load_dwordx4 v[202:203], s[40:43], 0 idxen offen offset:1024 nt lds\n"
    "s_add_u32 s95, s95, 60\n"
    "s_add_u32 s94, s67, 384\n"
    "s_andn2_b32 s95, s95, 63\n"
    "s_max_u32 s80, s95, s94\n"
    "s_add_u32 m0, s68, 2048\n"
    "s_waitcnt lgkmcnt(0)\n"
    "buffer_load_dwordx4 v[204:205], s[40:43], 0 idxen offen offset:0 nt lds\n"
    "s_mov_b32 s94, 0x10001\n"
    "s_mov_b32 s95, 0xc080c080\n"
    "v_pk_min_u16 v22, v228, s94\n"
    "v_pk_min_u16 v23, v229, s94\n"
    "v_pk_mad_u16 v228, v22, s95, v228\n"
    "v_pk_mad_u16 v229, v23, s95, v229\n"
    "s_sub_u32 s87, s80, s81\n"
    "v_or_b32 v228, v228, v229\n"
    "s_lshr_b32 s87, s87, 6\n"
    "s_lshl2_add_u32 s86, s3, 68224\n"
    "v_mov_b32 v106, s86\n"
    "v_mov_b32 v108, s66\n"
    "v_mov_b32 v109, s58\n"
    "v_mov_b32 v110, s59\n"
    "s_waitcnt vmcnt(3)\n"
    "v_cmp_neq_f32_e64 s[20:21], 0, v224\n"
    "v_cmp_neq_f32_e64 s[22:23], 0, v225\n"
    "v_cmp_neq_f32_e64 s[24:25], 0, v226\n"
    "v_cmp_neq_f32_e64 s[26:27], 0, v227\n"
    "v_fma_f32 v224, v224, v224, -v224\n"
    "v_fma_f32 v225, v225, v225, -v225\n"
    "v_fma_f32 v226, v226, v226, -v226\n"
    "v_fma_f32 v227, v227, v227, -v227\n"
    "s_bcnt1_i32_b64 s88, s[20:21]\n"
    "s_bcnt1_i32_b64 s89, s[22:23]\n"
    "buffer_load_dwordx4 v[206:207], s[40:43], 0 idxen offen offset:1024 nt lds\n"
    "s_bcnt1_i32_b64 s90, s[24:25]\n"
    "s_bcnt1_i32_b64 s91, s[26:27]\n"
    "v_or3_b32 v228, v228, v224, v225\n"
    "s_add_u32 s88, s88, s89\n"
    "v_or3_b32 v228, v228, v226, v227\n"
    "s_add_u32 s90, s90, s91\n"
    "v_cmp_ne_u32_e64 s[84:85], 0, v228\n"
    "s_add_u32 s88, s88, s90\n"
    "s_pack_ll_b32_b16 s88, s87, s88\n"
    "s_mov_b64 exec, 1\n"
    "v_mov_b32_e64 v107, s88\n"
    "ds_write2_b32 v108, v109, v110 offset1:2\n"
    "s_cmp_lg_u64 s[84:85], 0\n"
    "ds_write_b32 v106, v107\n"
    "s_cselect_b32 s91, 2.0, 0\n"
    "s_mov_b64 exec, -1\n"
    "s_or_b32 s58, s58, s91\n"
    "s_or_b32 s59, s59, s91\n"
    "v_pk_min_u16 v0, v32, s4\n"
    "v_pk_min_u16 v1, v33, s4\n"
    "v_pk_min_u16 v2, v34, s4\n"
    "v_pk_min_u16 v3, v35, s4\n"
    "v_pk_mad_u16 v32, v0, s5, v32\n"
    "v_pk_mad_u16 v33, v1, s5, v33\n"
    "s_add_u32 m0, s68, 4096\n"
    "s_nop 0\n"
    "buffer_load_dwordx4 v[208:209], s[40:43], 0 idxen offen offset:0 nt lds\n"
    "v_pk_mad_u16 v34, v2, s5, v34\n"
    "v_pk_mad_u16 v35, v3, s5, v35\n"
    "v_lshl_or_b32 v0, v1, 2, v0\n"
    "v_lshl_or_b32 v0, v2, 4, v0\n"
    "v_lshl_or_b32 v0, v3, 6, v0\n"
    "v_or3_b32 v5, v5, v32, v33\n"
    "v_lshrrev_b32 v1, 15, v0\n"
    ".Ldin_stub_0:\n"
    "s_cbranch_execz .Ldin_t0_ret\n"
    "s_nop 0\n"
    "v_or3_b32 v5, v5, v34, v35\n"
    "v_bitop3_b32 v16, v1, v6, v0 bitop3:0xc8\n"
    "ds_write_b8 v7, v16 offset:0\n"
    "v_pk_min_u16 v0, v36, s4\n"
    "v_pk_min_u16 v1, v37, s4\n"
    "v_pk_min_u16 v2, v38, s4\n"
    "v_pk_min_u16 v3, v39, s4\n"
    "v_pk_mad_u16 v36, v0, s5, v36\n"
    "v_pk_mad_u16 v37, v1, s5, v37\n"
    "v_pk_mad_u16 v38, v2, s5, v38\n"
    "v_pk_mad_u16 v39, v3, s5, v39\n"
    "v_lshl_or_b32 v0, v1, 2, v0\n"
    "v_lshl_or_b32 v0, v2, 4, v0\n"
    "v_lshl_or_b32 v0, v3, 6, v0\n"
    "v_or3_b32 v5, v5, v36, v37\n"
    "v_lshrrev_b32_e64 v1, 15, v0\n"
    "v_or3_b32 v5, v5, v38, v39\n"
    "v_bitop3_b32 v17, v1, v6, v0 bitop3:0xc8\n"
    "buffer_load_dwordx4 v[210:211], s[40:43], 0 idxen offen offset:1024 nt lds\n"
    "ds_write_b8 v7, v17 offset:512\n"
    "v_pk_min_u16 v0, v40, s4\n"
    "v_pk_min_u16 v1, v41, s4\n"
    "v_pk_min_u16 v2, v42, s4\n"
    "v_pk_min_u16 v3, v43, s4\n"
    "v_pk_mad_u16 v40, v0, s5, v40\n"
    "v_pk_mad_u16 v41, v1, s5, v41\n"
    "v_pk_mad_u16 v42, v2, s5, v42\n"
    "v_pk_mad_u16 v43, v3, s5, v43\n"
    "v_lshl_or_b32 v0, v1, 2, v0\n"
    "v_lshl_or_b32 v0, v2, 4, v0\n"
    "v_lshl_or_b32 v0, v3, 6, v0\n"
    "v_or3_b32 v5, v5, v40, v41\n"
    "v_lshrrev_b32_e64 v1, 15, v0\n"
    "v_or3_b32 v5, v5, v42, v43\n"
    "v_bitop3_b32 v18, v1, v6, v0 bitop3:0xc8\n"
    "ds_write_b8 v7, v18 offset:1024\n"
    "v_pk_min_u16 v0, v44, s4\n"
    "v_pk_min_u16 v1, v45, s4\n"
    "v_pk_min_u16 v2, v46, s4\n"
    "v_pk_min_u16 v3, v47, s4\n"
    "v_pk_mad_u16 v44, v0, s5, v44\n"
    "v_pk_mad_u16 v45, v1, s5, v45\n"
    "v_pk_mad_u16 v46, v2, s5, v46\n"
    "v_pk_mad_u16 v47, v3, s5, v47\n"
    "s_add_u32 m0, s68, 6144\n"
    "s_nop 0\n"
    "buffer_load_dwordx4 v[212:213], s[40:43], 0 idxen offen offset:0 nt lds\n"
    "v_lshl_or_b32 v0, v1, 2, v0\n"
    "v_lshl_or_b32 v0, v2, 4, v0\n"
    "v_lshl_or_b32 v0, v3, 6, v0\n"
    "v_or3_b32 v5, v5, v44, v45\n"
    "v_lshrrev_b32 v1, 15, v0\n"
    "v_or3_b32 v5, v5, v46, v47\n"
    "v_bitop3_b32 v19, v1, v6, v0 bitop3:0xc8\n"
    "ds_write_b8 v7, v19 offset:1536\n"
    "v_pk_min_u16 v0, v48, s4\n"
    "v_pk_min_u16 v1, v49, s4\n"
    "v_pk_min_u16 v2, v50, s4\n"
    "v_pk_min_u16 v3, v51, s4\n"
    "v_pk_mad_u16 v48, v0, s5, v48\n"
    "v_pk_mad_u16 v49, v1, s5, v49\n"
    "v_pk_mad_u16 v50, v2, s5, v50\n"
    "v_pk_mad_u16 v51, v3, s5, v51\n"
    "v_lshl_or_b32 v0, v1, 2, v0\n"
    "v_lshl_or_b32 v0, v2, 4, v0\n"
    "v_lshl_or_b32 v0, v3, 6, v0\n"
    "v_or3_b32 v5, v5, v48, v49\n"
    "v_lshrrev_b32_e64 v1, 15, v0\n"
    "v_or3_b32 v5, v5, v50, v51\n"
    "v_bitop3_b32 v16, v1, v6, v0 bitop3:0xc8\n"
    "ds_write_b8 v7, v16 offset:2048\n"
    "v_pk_min_u16 v0, v52, s4\n"
    "buffer_load_dwordx4 v[214:215], s[40:43], 0 idxen offen offset:1024 nt lds\n"
    "v_pk_min_u16 v1, v53, s4\n"
    "v_pk_min_u16 v2, v54, s4\n"
    "v_pk_min_u16 v3, v55, s4\n"
    "v_pk_mad_u16 v52, v0, s5, v52\n"
    "v_pk_mad_u16 v53, v1, s5, v53\n"
    "v_pk_mad_u16 v54, v2, s5, v54\n"
    "v_pk_mad_u16 v55, v3, s5, v55\n"
    "v_lshl_or_b32 v0, v1, 2, v0\n"
    "v_lshl_or_b32 v0, v2, 4, v0\n"
    "v_lshl_or_b32 v0, v3, 6, v0\n"
    "v_or3_b32 v5, v5, v52, v53\n"
    "v_lshrrev_b32_e64 v1, 15, v0\n"
    "v_or3_b32 v5, v5, v54, v55\n"
    "v_bitop3_b32 v17, v1, v6, v0 bitop3:0xc8\n"
    "ds_write_b8 v7, v17 offset:2560\n"
    "ds_read_b128 v[128:131], v97 offset:4352\n"
    "ds_read_b128 v[132:135], v97 offset:4368\n"
    "ds_read_b128 v[144:147], v97 offset:8704\n"
    "ds_read_b128 v[148:151], v97 offset:8720\n"
    "ds_read_b128 v[160:163], v97 offset:13056\n"
    "ds_read_b128 v[164:167], v97 offset:13072\n"
    "s_waitcnt lgkmcnt(4)\n"
    "v_mfma_f32_16x16x32_bf16 v[116:119], v[8:11], v[128:131], v[28:31]\n"
    "v_mfma_f32_16x16x32_bf16 v[116:119], v[12:15], v[132:135], v[116:119]\n"
    "s_waitcnt lgkmcnt(2)\n"
    "v_mfma_f32_16x16x32_bf16 v[120:123], v[8:11], v[144:147], v[28:31]\n"
    "v_mfma_f32_16x16x32_bf16 v[120:123], v[12:15], v[148:151], v[120:123]\n"
    "s_waitcnt lgkmcnt(0)\n"
    "v_mfma_f32_16x16x32_bf16 v[124:127], v[8:11], v[160:163], v[28:31]\n"
    "v_mfma_f32_16x16x32_bf16 v[124:127], v[12:15], v[164:167], v[124:127]\n"
    "s_nop 7\n"
    "ds_write_b128 v100, v[116:119] offset:21760\n"
    "ds_write_b128 v100, v[120:123] offset:26112\n"
    "ds_write_b128 v100, v[124:127] offset:30464\n"
    "v_cmp_ne_u32 vcc, 0, v5\n"
    "s_cmp_lg_u64 vcc, 0\n"
    "s_cselect_b32 s94, 0x101, 1\n"
    "s_mov_b64 exec, 1\n"
    "v_mov_b32 v10, s94\n"
    "ds_add_rtn_u32 v10, v250, v10 offset:35840\n"
    "s_mov_b64 exec, -1\n"
    "s_waitcnt lgkmcnt(0)\n"
    "v_readfirstlane_b32 s95, v10\n"
    "s_add_u32 s96, s95, s94\n"
    "s_bitcmp1_b32 s96, 2\n"
    "s_cbranch_scc1 .Ldin_sched\n"
    ".Ldin_ygate:\n"
    ".Ldin_yentry:\n"
    "s_mov_b64 vcc, -1\n"
    "s_branch .Ldin_entry\n"
    ".Ldin_swait:\n"
    "s_sleep 1\n"
    "ds_read_b32 v10, v250 offset:35840\n"
    "s_waitcnt lgkmcnt(0)\n"
    "v_readfirstlane_b32 s96, v10\n"
    ".Ldin_sched:\n"
    "s_setprio 1\n"
    "v_lshrrev_b32 v2, 3, v251\n"
    "v_and_b32 v3, 7, v251\n"
    "v_lshlrev_b32 v6, 3, v3\n"
    "v_add_u32 v7, 16, v251\n"
    "s_mov_b32 s84, 0x10b80\n"
    "v_lshl_add_u32 v12, v7, 6, s84\n"
    "ds_read_b128 v[32:35], v12\n"
    "ds_read_b128 v[36:39], v12 offset:16\n"
    "s_mov_b32 s85, 0x10a80\n"
    "v_lshl_add_u32 v4, v2, 2, s85\n"
    "v_lshl_add_u32 v5, v3, 2, s85\n"
    "ds_read_u16 v17, v4 offset:2\n"
    "ds_read_u16 v4, v4\n"
    "ds_read_u16 v16, v5 offset:2\n"
    "ds_read_u16 v5, v5\n"
    "s_lshl_b32 s85, s2, 3\n"
    "s_lshl1_add_u32 s85, s15, s85\n"
    "v_mad_u32_u24 v8, v2, s15, v3\n"
    "v_add_u32 v8, s85, v8\n"
    "s_cmp_ge_u32 s96, 256\n"
    "v_cmp_gt_u32_e64 s[86:87], s14, v8\n"
    "s_cselect_b32 s85, 2.0, 0\n"
    "v_lshlrev_b32 v8, 6, v8\n"
    "s_and_b32 s87, s87, 0xffff\n"
    "v_or3_b32 v8, v8, v7, s85\n"
    "v_mov_b32 v12, s13\n"
    "v_cndmask_b32_e64 v8, v12, v8, s[86:87]\n"
    "v_cmp_eq_u32 vcc, s3, v2\n"
    "v_lshrrev_b32 v14, 2, v2\n"
    "v_mul_u32_u24 v14, 2, v14\n"
    "v_addc_co_u32_e64 v14, s[88:89], v14, 0, vcc\n"
    "v_cmp_eq_u32 vcc, s3, v3\n"
    "v_lshrrev_b32_e64 v15, 2, v3\n"
    "v_mul_u32_u24 v15, 2, v15\n"
    "v_addc_co_u32_e64 v15, s[88:89], v15, 0, vcc\n"
    "s_waitcnt lgkmcnt(4)\n"
    "v_bcnt_u32_b32 v9, v32, 15\n"
    "v_bcnt_u32_b32 v9, v33, v9\n"
    "v_bcnt_u32_b32 v9, v34, v9\n"
    "v_bcnt_u32_b32 v9, v35, v9\n"
    "v_bcnt_u32_b32 v9, v36, v9\n"
    "v_bcnt_u32_b32 v9, v37, v9\n"
    "v_bcnt_u32_b32 v9, v38, v9\n"
    "v_bcnt_u32_b32 v9, v39, v9\n"
    "v_lshrrev_b32 v9, 4, v9\n"
    "v_max_u32 v9, 6, v9\n"
    "v_lshlrev_b32 v9, 3, v9\n"
    "v_cndmask_b32_e64 v9, 0, v9, s[86:87]\n"
    "v_sub_u32 v10, v3, v9\n"
    "s_waitcnt lgkmcnt(0)\n"
    "v_add_u32 v16, 15, v16\n"
    "v_lshrrev_b32 v16, 4, v16\n"
    "v_max_u32 v16, 6, v16\n"
    "v_add_u32 v17, 15, v17\n"
    "v_lshrrev_b32_e64 v17, 4, v17\n"
    "v_max_u32 v17, 6, v17\n"
    "v_add3_u32 v4, v4, v14, v17\n"
    "v_add3_u32 v5, v5, v15, v16\n"
    "v_mov_b32_dpp v12, v10 row_half_mirror row_mask:0xf bank_mask:0xf\n"
    "v_lshl_or_b32 v4, v4, 3, v2\n"
    "v_lshl_or_b32 v5, v5, 3, v3\n"
    "v_sub_u32_dpp v32, v10, v10 quad_perm:[1,0,3,2] row_mask:0xf bank_mask:0xf\n"
    "v_sub_u32_dpp v33, v10, v10 quad_perm:[2,3,0,1] row_mask:0xf bank_mask:0xf\n"
    "v_sub_u32_dpp v34, v10, v10 quad_perm:[3,2,1,0] row_mask:0xf bank_mask:0xf\n"
    "v_sub_u32_dpp v35, v12, v10 quad_perm:[0,1,2,3] row_mask:0xf bank_mask:0xf\n"
    "v_sub_u32_dpp v36, v12, v10 quad_perm:[1,0,3,2] row_mask:0xf bank_mask:0xf\n"
    "v_sub_u32_dpp v37, v12, v10 quad_perm:[2,3,0,1] row_mask:0xf bank_mask:0xf\n"
    "v_sub_u32_dpp v38, v12, v10 quad_perm:[3,2,1,0] row_mask:0xf bank_mask:0xf\n"
    "v_lshrrev_b32 v32, 31, v32\n"
    "v_lshrrev_b32 v33, 31, v33\n"
    "v_lshrrev_b32 v34, 31, v34\n"
    "v_lshrrev_b32 v35, 31, v35\n"
    "v_lshrrev_b32 v36, 31, v36\n"
    "v_lshrrev_b32 v37, 31, v37\n"
    "v_lshrrev_b32 v38, 31, v38\n"
    "v_add3_u32 v11, v32, v33, v244\n"
    "v_add3_u32 v11, v34, v35, v11\n"
    "v_add3_u32 v11, v36, v37, v11\n"
    "v_add_u32 v11, v38, v11\n"
    "v_add_u32 v12, s65, v11\n"
    "v_lshl_add_u32 v14, v11, 3, s65\n"
    "ds_write_b8 v12, v9\n"
    "ds_write_b32 v14, v8 offset:64\n"
    "v_mov_b32 v12, s65\n"
    "ds_read_b128 v[40:43], v12 offset:0\n"
    "ds_read_b128 v[44:47], v12 offset:16\n"
    "ds_read_b128 v[48:51], v12 offset:32\n"
    "v_lshlrev_b32 v16, 3, v16\n"
    "v_sub_u32 v72, v5, v16\n"
    "v_cmp_lt_i32 vcc, v5, v4\n"
    "v_mov_b32_e64 v73, v5\n"
    "s_waitcnt lgkmcnt(0)\n"
    "v_lshrrev_b64 v[20:21], v6, vcc\n"
    "v_lshrrev_b64 v[18:19], v244, vcc\n"
    "v_and_b32 v20, 0xff, v20\n"
    "v_and_b32 v18, 0xff, v18\n"
    "v_bcnt_u32_b32 v20, v20, 0\n"
    "v_bcnt_u32_b32 v18, v18, 0\n"
    "v_lshl_add_u32 v20, v20, 3, s65\n"
    "v_lshlrev_b32_e64 v18, 3, v18\n"
    "ds_read_b32 v56, v20 offset:64\n"
    "v_lshrrev_b64 v[22:23], v20, v[40:41]\n"
    "v_lshrrev_b64 v[18:19], v18, v[40:41]\n"
    "v_and_b32 v22, 0xff, v22\n"
    "v_and_b32 v18, 0xff, v18\n"
    "v_add_u32 v5, v5, v22\n"
    "v_add_u32 v4, v4, v18\n"
    "v_cmp_lt_i32 vcc, v5, v4\n"
    "v_mov_b32_e64 v74, v5\n"
    "s_nop 0\n"
    "v_lshrrev_b64 v[20:21], v6, vcc\n"
    "v_lshrrev_b64 v[18:19], v244, vcc\n"
    "v_and_b32 v20, 0xff, v20\n"
    "v_and_b32 v18, 0xff, v18\n"
    "v_bcnt_u32_b32 v20, v20, 0\n"
    "v_bcnt_u32_b32 v18, v18, 0\n"
    "v_lshl_add_u32 v20, v20, 3, s65\n"
    "v_lshlrev_b32_e64 v18, 3, v18\n"
    "ds_read_b32 v58, v20 offset:128\n"
    "v_lshrrev_b64 v[22:23], v20, v[42:43]\n"
    "v_lshrrev_b64 v[18:19], v18, v[42:43]\n"
    "v_and_b32 v22, 0xff, v22\n"
    "v_and_b32 v18, 0xff, v18\n"
    "v_add_u32 v5, v5, v22\n"
    "v_add_u32 v4, v4, v18\n"
    "v_cmp_lt_i32 vcc, v5, v4\n"
    "v_mov_b32_e64 v75, v5\n"
    "s_nop 0\n"
    "v_lshrrev_b64 v[20:21], v6, vcc\n"
    "v_lshrrev_b64 v[18:19], v244, vcc\n"
    "v_and_b32 v20, 0xff, v20\n"
    "v_and_b32 v18, 0xff, v18\n"
    "v_bcnt_u32_b32 v20, v20, 0\n"
    "v_bcnt_u32_b32 v18, v18, 0\n"
    "v_lshl_add_u32 v20, v20, 3, s65\n"
    "v_lshlrev_b32_e64 v18, 3, v18\n"
    "ds_read_b32 v60, v20 offset:192\n"
    "v_lshrrev_b64 v[22:23], v20, v[44:45]\n"
    "v_lshrrev_b64 v[18:19], v18, v[44:45]\n"
    ".Ldin_stub_2:\n"
    "s_cbranch_execz .Ldin_t0_ret\n"
    "s_nop 0\n"
    "v_and_b32 v22, 0xff, v22\n"
    "v_and_b32 v18, 0xff, v18\n"
    "v_add_u32 v5, v5, v22\n"
    "v_add_u32 v4, v4, v18\n"
    "v_cmp_lt_i32 vcc, v5, v4\n"
    "v_mov_b32_e64 v76, v5\n"
    "s_nop 0\n"
    "v_lshrrev_b64 v[20:21], v6, vcc\n"
    "v_lshrrev_b64 v[18:19], v244, vcc\n"
    "v_and_b32 v20, 0xff, v20\n"
    "v_and_b32 v18, 0xff, v18\n"
    "v_bcnt_u32_b32 v20, v20, 0\n"
    "v_bcnt_u32_b32 v18, v18, 0\n"
    "v_lshl_add_u32 v20, v20, 3, s65\n"
    "v_lshlrev_b32_e64 v18, 3, v18\n"
    "ds_read_b32 v62, v20 offset:256\n"
    "v_lshrrev_b64 v[22:23], v20, v[46:47]\n"
    "v_lshrrev_b64 v[18:19], v18, v[46:47]\n"
    "v_and_b32 v22, 0xff, v22\n"
    "v_and_b32 v18, 0xff, v18\n"
    "v_add_u32 v5, v5, v22\n"
    "v_add_u32 v4, v4, v18\n"
    "v_cmp_lt_i32 vcc, v5, v4\n"
    "v_mov_b32_e64 v77, v5\n"
    "s_nop 0\n"
    "v_lshrrev_b64 v[20:21], v6, vcc\n"
    "v_lshrrev_b64 v[18:19], v244, vcc\n"
    "v_and_b32 v20, 0xff, v20\n"
    "v_and_b32 v18, 0xff, v18\n"
    "v_bcnt_u32_b32 v20, v20, 0\n"
    "v_bcnt_u32_b32 v18, v18, 0\n"
    "v_lshl_add_u32 v20, v20, 3, s65\n"
    "v_lshlrev_b32_e64 v18, 3, v18\n"
    "ds_read_b32 v64, v20 offset:320\n"
    "v_lshrrev_b64 v[22:23], v20, v[48:49]\n"
    "v_lshrrev_b64 v[18:19], v18, v[48:49]\n"
    "v_and_b32 v22, 0xff, v22\n"
    "v_and_b32 v18, 0xff, v18\n"
    "v_add_u32 v5, v5, v22\n"
    "v_add_u32 v4, v4, v18\n"
    "v_cmp_lt_i32 vcc, v5, v4\n"
    "v_mov_b32_e64 v78, v5\n"
    "s_nop 0\n"
    "v_lshrrev_b64 v[20:21], v6, vcc\n"
    "v_and_b32 v20, 0xff, v20\n"
    "v_bcnt_u32_b32 v20, v20, 0\n"
    "v_lshl_add_u32 v20, v20, 3, s65\n"
    "ds_read_b32 v66, v20 offset:384\n"
    "v_lshrrev_b64 v[22:23], v20, v[50:51]\n"
    "v_and_b32 v22, 0xff, v22\n"
    "v_add_u32 v5, v5, v22\n"
    "s_add_u32 s85, s76, 316\n"
    "s_lshl_b32 s86, s18, 4\n"
    "v_add_u32_dpp v12, v5, v5 quad_perm:[1,0,3,2] row_mask:0xf bank_mask:0xf\n"
    "s_add_u32 s85, s85, s86\n"
    "s_nop 0\n"
    "v_add_u32_dpp v12, v12, v12 quad_perm:[2,3,0,1] row_mask:0xf bank_mask:0xf\n"
    "s_nop 1\n"
    "v_add_u32_dpp v12, v12, v12 row_half_mirror row_mask:0xf bank_mask:0xf\n"
    "v_lshrrev_b32 v12, 3, v12\n"
    "v_sub_u32 v12, v12, v5\n"
    "v_add_u32 v72, v72, v12\n"
    "v_mul_u32_u24 v72, s18, v72\n"
    "v_lshrrev_b32 v72, 3, v72\n"
    "s_add_u32 s85, s85, s19\n"
    "v_add_u32 v57, s85, v72\n"
    "v_add_u32 v73, v73, v12\n"
    "v_mul_u32_u24 v73, s18, v73\n"
    "v_lshrrev_b32 v73, 3, v73\n"
    "s_add_u32 s85, s85, s19\n"
    "v_add_u32 v59, s85, v73\n"
    "v_add_u32 v74, v74, v12\n"
    "v_mul_u32_u24 v74, s18, v74\n"
    "v_lshrrev_b32 v74, 3, v74\n"
    "s_add_u32 s85, s85, s19\n"
    "v_add_u32 v61, s85, v74\n"
    "v_add_u32 v75, v75, v12\n"
    "v_mul_u32_u24 v75, s18, v75\n"
    "v_lshrrev_b32 v75, 3, v75\n"
    "s_add_u32 s85, s85, s19\n"
    "v_add_u32 v63, s85, v75\n"
    "v_add_u32 v76, v76, v12\n"
    "v_mul_u32_u24 v76, s18, v76\n"
    "v_lshrrev_b32 v76, 3, v76\n"
    "s_add_u32 s85, s85, s19\n"
    "v_add_u32 v65, s85, v76\n"
    "v_add_u32 v77, v77, v12\n"
    "v_mul_u32_u24 v77, s18, v77\n"
    "v_lshrrev_b32 v77, 3, v77\n"
    "s_add_u32 s85, s85, s19\n"
    "v_add_u32 v67, s85, v77\n"
    "v_lshl_add_u32 v12, v3, 7, s84\n"
    "s_mov_b64 exec, 0xff\n"
    "s_waitcnt lgkmcnt(0)\n"
    "ds_write_b128 v12, v[56:59] offset:48\n"
    "ds_write_b128 v12, v[60:63] offset:64\n"
    "ds_write_b128 v12, v[64:67] offset:80\n"
    "s_mov_b64 exec, -1\n"
    "s_branch .Ldin_ygate\n"
    ".Ldin_odd:\n"
    "s_cmp_lt_i32 s58, s57\n"
    "s_cbranch_scc1 .Ldin_A0s\n"
    "s_waitcnt vmcnt(0)\n"
    "s_branch .Ldin_exit\n"
    ".p2align 5\n"
    ".Ldin_L2:\n"
    "s_setprio 0\n"
    "s_cmp_ge_i32 s59, s57\n"
    "s_cbranch_scc1 .Ldin_E0\n"
    "s_add_u32 s76, s76, s73\n"
    "v_mov_b32 v253, s63\n"
    "v_and_b32 v184, v254, v184\n"
    "v_and_b32 v185, v255, v185\n"
    "v_and_b32 v186, v254, v186\n"
    "v_and_b32 v187, v255, v187\n"
    "v_and_b32 v188, v254, v188\n"
    "v_and_b32 v189, v255, v189\n"
    "v_and_b32 v190, v254, v190\n"
    "v_and_b32_e64 v191, v255, v191\n"
    "ds_read_b64 v[232:233], v253\n"
    "v_mfma_f32_4x4x4_16b_bf16 v[144:147], v[184:185], v[32:33], v[64:67]\n"
    "ds_read_b128 v[112:115], v252 offset:17408\n"
    "v_mfma_f32_4x4x4_16b_bf16 v[148:151], v[186:187], v[34:35], v[68:71]\n"
    "ds_read_b128 v[116:119], v252 offset:17424\n"
    "v_mfma_f32_4x4x4_16b_bf16 v[152:155], v[188:189], v[36:37], v[72:75]\n"
    "ds_read_b128 v[120:123], v252 offset:17536\n"
    "ds_read_b128 v[124:127], v252 offset:17552\n"
    "v_cvt_pk_bf16_f32 v0, v144, v145\n"
    "v_cvt_pk_bf16_f32 v1, v146, v147\n"
    "v_mfma_f32_4x4x4_16b_bf16 v[156:159], v[190:191], v[38:39], v[76:79]\n"
    "v_cvt_pk_bf16_f32 v2, v148, v149\n"
    "v_cvt_pk_bf16_f32 v3, v150, v151\n"
    "v_mfma_f32_4x4x4_16b_bf16 v[144:147], v[184:185], v[40:41], v[80:83]\n"
    "v_cvt_pk_bf16_f32 v4, v152, v153\n"
    "v_cvt_pk_bf16_f32 v5, v154, v155\n"
    "v_mfma_f32_4x4x4_16b_bf16 v[148:151], v[186:187], v[42:43], v[84:87]\n"
    "v_cvt_pk_bf16_f32 v6, v156, v157\n"
    "v_cvt_pk_bf16_f32 v7, v158, v159\n"
    "v_mfma_f32_4x4x4_16b_bf16 v[152:155], v[188:189], v[44:45], v[88:91]\n"
    "v_cvt_pk_bf16_f32 v8, v144, v145\n"
    "v_cvt_pk_bf16_f32 v9, v146, v147\n"
    "v_mfma_f32_4x4x4_16b_bf16 v[156:159], v[190:191], v[46:47], v[92:95]\n"
    "v_cvt_pk_bf16_f32 v10, v148, v149\n"
    "v_cvt_pk_bf16_f32 v11, v150, v151\n"
    "v_mfma_f32_4x4x4_16b_bf16 v[144:147], v[184:185], v[48:49], v[192:195]\n"
    "v_cvt_pk_bf16_f32 v12, v152, v153\n"
    "v_cvt_pk_bf16_f32 v13, v154, v155\n"
    "v_mfma_f32_4x4x4_16b_bf16 v[148:151], v[186:187], v[50:51], v[196:199]\n"
    "v_cvt_pk_bf16_f32 v14, v156, v157\n"
    "v_cvt_pk_bf16_f32 v15, v158, v159\n"
    "v_mfma_f32_4x4x4_16b_bf16 v[152:155], v[188:189], v[52:53], v[200:203]\n"
    "s_waitcnt lgkmcnt(0)\n"
    "v_readfirstlane_b32 s60, v232\n"
    "v_cvt_pk_bf16_f32 v16, v144, v145\n"
    "v_cvt_pk_bf16_f32 v17, v146, v147\n"
    "v_mfma_f32_4x4x4_16b_bf16 v[156:159], v[190:191], v[54:55], v[204:207]\n"
    "s_cmp_eq_u32 s60, -1\n"
    "s_cbranch_scc1 .Ldin_L2_poll\n"
    ".Ldin_L2_pr:\n"
    "v_readfirstlane_b32 s95, v233\n"
    "s_and_b32 s94, s60, 63\n"
    "s_lshl_b32 s94, s94, 6\n"
    "s_add_u32 s94, s94, 68480\n"
    "s_add_u32 s63, s63, 8\n"
    "v_lshl_add_u32 v252, v249, 1, s94\n"
    "ds_read_b32 v252, v252\n"
    "s_sub_u32 s95, s95, s76\n"
    "s_ashr_i32 s95, s95, 4\n"
    "s_min_i32 s95, s95, s74\n"
    "s_max_i32 s72, s95, s75\n"
    "v_cvt_pk_bf16_f32 v18, v148, v149\n"
    "v_cvt_pk_bf16_f32 v19, v150, v151\n"
    "v_mfma_f32_4x4x4_16b_bf16 v[144:147], v[184:185], v[56:57], v[208:211]\n"
    "v_cvt_pk_bf16_f32 v20, v152, v153\n"
    "v_cvt_pk_bf16_f32 v21, v154, v155\n"
    "v_mfma_f32_4x4x4_16b_bf16 v[148:151], v[186:187], v[58:59], v[212:215]\n"
    "v_cvt_pk_bf16_f32 v22, v156, v157\n"
    "v_cvt_pk_bf16_f32 v23, v158, v159\n"
    "v_mfma_f32_4x4x4_16b_bf16 v[152:155], v[188:189], v[60:61], v[216:219]\n"
    "v_cvt_pk_bf16_f32 v24, v144, v145\n"
    "v_cvt_pk_bf16_f32 v25, v146, v147\n"
    "v_mfma_f32_4x4x4_16b_bf16 v[156:159], v[190:191], v[62:63], v[220:223]\n"
    "v_cvt_pk_bf16_f32 v26, v148, v149\n"
    "v_cvt_pk_bf16_f32 v27, v150, v151\n"
    "v_cvt_pk_bf16_f32 v28, v152, v153\n"
    "v_cvt_pk_bf16_f32 v29, v154, v155\n"
    "v_exp_f32 v136, v136 clamp\n"
    "v_cvt_pk_bf16_f32 v30, v156, v157\n"
    "v_cvt_pk_bf16_f32 v31, v158, v159\n"
    "v_mfma_f32_16x16x32_bf16 v[144:147], v[0:3], v[160:163], v[112:115]\n"
    "v_exp_f32 v128, v128 clamp\n"
    "v_mfma_f32_16x16x32_bf16 v[144:147], v[4:7], v[164:167], v[144:147]\n"
    "v_exp_f32 v129, v129 clamp\n"
    "v_mfma_f32_16x16x32_bf16 v[148:151], v[8:11], v[160:163], v[116:119]\n"
    "v_exp_f32 v130, v130 clamp\n"
    "v_mfma_f32_16x16x32_bf16 v[148:151], v[12:15], v[164:167], v[148:151]\n"
    "v_exp_f32 v131, v131 clamp\n"
    "v_mfma_f32_16x16x32_bf16 v[152:155], v[16:19], v[160:163], v[120:123]\n"
    "v_exp_f32 v132, v132 clamp\n"
    "v_mfma_f32_16x16x32_bf16 v[152:155], v[20:23], v[164:167], v[152:155]\n"
    "v_exp_f32 v133, v133 clamp\n"
    "v_mfma_f32_16x16x32_bf16 v[156:159], v[24:27], v[160:163], v[124:127]\n"
    "v_exp_f32 v134, v134 clamp\n"
    "v_mfma_f32_16x16x32_bf16 v[156:159], v[28:31], v[164:167], v[156:159]\n"
    "v_exp_f32 v135, v135 clamp\n"
    "s_add_u32 m0, s68, 6144\n"
    "v_exp_f32 v137, v137 clamp\n"
    "v_add_u32 v253, s67, v244\n"
    "v_mfma_f32_4x4x4_16b_bf16 v[224:227], v[228:229], v[176:177], v[224:227] cbsz:4 abid:0\n"
    "v_exp_f32 v138, v138 clamp\n"
    "v_exp_f32 v139, v139 clamp\n"
    "v_mfma_f32_4x4x4_16b_bf16 v[224:227], v[228:229], v[178:179], v[224:227] cbsz:4 abid:1\n"
    "v_exp_f32 v140, v140 clamp\n"
    "v_exp_f32 v141, v141 clamp\n"
    "v_mfma_f32_4x4x4_16b_bf16 v[224:227], v[228:229], v[180:181], v[224:227] cbsz:4 abid:2\n"
    "v_exp_f32 v142, v142 clamp\n"
    "v_exp_f32 v143, v143 clamp\n"
    "v_mfma_f32_4x4x4_16b_bf16 v[224:227], v[228:229], v[182:183], v[224:227] cbsz:4 abid:3\n"
    "s_waitcnt vmcnt(4)\n"
    "ds_read_b64_tr_b16 v[176:177], v240 offset:6144\n"
    "ds_read_b64_tr_b16 v[178:179], v241 offset:6400\n"
    "ds_read_b64_tr_b16 v[180:181], v242 offset:6656\n"
    "ds_read_b64_tr_b16 v[182:183], v243 offset:6912\n"
    "ds_read_b32 v236, v253 offset:0\n"
    "ds_read_b32 v238, v253 offset:4\n"
    "ds_read_b128 v[160:163], v234 offset:0\n"
    "ds_read_b128 v[164:167], v235 offset:0\n"
    "s_waitcnt lgkmcnt(8)\n"
    "s_nop 0\n"
    "v_readlane_b32 s20, v252, 0\n"
    "v_readlane_b32 s21, v252, 1\n"
    "v_readlane_b32 s22, v252, 2\n"
    "v_readlane_b32 s23, v252, 3\n"
    "v_readlane_b32 s24, v252, 4\n"
    "v_readlane_b32 s25, v252, 5\n"
    "v_readlane_b32 s26, v252, 6\n"
    "v_readlane_b32 s27, v252, 7\n"
    "v_pk_fma_f32 v[128:129], v[128:129], s[70:71], v[96:97]\n"
    "v_pk_fma_f32 v[130:131], v[130:131], s[70:71], v[98:99]\n"
    "v_pk_add_f32 v[230:231], v[128:129], v[130:131]\n"
    "v_pk_mul_f32 v[232:233], v[128:129], v[130:131]\n"
    "v_pk_fma_f32 v[132:133], v[132:133], s[70:71], v[100:101]\n"
    "v_pk_fma_f32 v[230:231], v[230:231], v[132:133], v[232:233]\n"
    "v_pk_mul_f32 v[232:233], v[232:233], v[132:133]\n"
    "v_pk_fma_f32 v[134:135], v[134:135], s[70:71], v[102:103]\n"
    "v_pk_fma_f32 v[230:231], v[230:231], v[134:135], v[232:233]\n"
    "v_pk_mul_f32 v[232:233], v[232:233], v[134:135]\n"
    "v_pk_fma_f32 v[136:137], v[136:137], s[70:71], v[104:105]\n"
    "v_pk_fma_f32 v[230:231], v[230:231], v[136:137], v[232:233]\n"
    "v_pk_mul_f32 v[232:233], v[232:233], v[136:137]\n"
    "v_pk_fma_f32 v[138:139], v[138:139], s[70:71], v[106:107]\n"
    "v_pk_fma_f32 v[230:231], v[230:231], v[138:139], v[232:233]\n"
    "v_pk_mul_f32 v[232:233], v[232:233], v[138:139]\n"
    "v_pk_fma_f32 v[140:141], v[140:141], s[70:71], v[108:109]\n"
    "v_pk_fma_f32 v[230:231], v[230:231], v[140:141], v[232:233]\n"
    "v_pk_mul_f32 v[232:233], v[232:233], v[140:141]\n"
    "v_pk_fma_f32 v[142:143], v[142:143], s[70:71], v[110:111]\n"
    "v_pk_fma_f32 v[230:231], v[230:231], v[142:143], v[232:233]\n"
    "v_pk_mul_f32 v[232:233], v[232:233], v[142:143]\n"
    "v_rcp_f32 v232, v232\n"
    "v_rcp_f32 v233, v233\n"
    "v_fma_f32 v230, v230, v232, v247\n"
    "v_fmac_f32 v230, v231, v233\n"
    "v_cvt_pk_bf16_f32 v230, v230, v230\n"
    "s_add_u32 s67, s67, 64\n"
    "s_waitcnt lgkmcnt(0)\n"
    "ds_write_b16 v245, v230\n"
    "ds_read_b64 v[228:229], v246\n"
    "s_lshl1_add_u32 s76, s72, s76\n"
    "buffer_load_dwordx4 v[236:237], s[40:43], 0 idxen offen offset:0 nt lds\n"
    "buffer_load_dwordx4 v[238:239], s[40:43], 0 idxen offen offset:1024 nt lds\n"
    "s_and_b32 s62, s58, 63\n"
    "s_mul_i32 s62, s62, 272\n"
    "s_mov_b32 s58, s59\n"
    "s_mov_b32 s59, s60\n"
    "s_mov_b32 s81, s65\n"
    "s_xor_b32 s65, s65, s64\n"
    "s_mov_b32 s80, s83\n"
    "s_add_u32 s82, s65, 256\n"
    "s_cmp_lt_i32 s59, s57\n"
    "s_cselect_b32 s76, s76, -1\n"
    "s_bitcmp1_b32 s58, 30\n"
    "s_cbranch_scc1 .Ldin_A3s\n"
    ".Ldin_A3:\n"
    "v_mfma_f32_16x16x32_bf16 v[128:131], v[0:3], v[160:163], v[112:115]\n"
    "v_exp_f32 v144, v144 clamp\n"
    "v_mfma_f32_16x16x32_bf16 v[128:131], v[4:7], v[164:167], v[128:131]\n"
    "v_exp_f32 v145, v145 clamp\n"
    "v_mfma_f32_16x16x32_bf16 v[132:135], v[8:11], v[160:163], v[116:119]\n"
    "v_exp_f32 v146, v146 clamp\n"
    "v_mfma_f32_16x16x32_bf16 v[132:135], v[12:15], v[164:167], v[132:135]\n"
    "v_exp_f32 v147, v147 clamp\n"
    "v_mfma_f32_16x16x32_bf16 v[136:139], v[16:19], v[160:163], v[120:123]\n"
    "v_exp_f32 v148, v148 clamp\n"
    "v_mfma_f32_16x16x32_bf16 v[136:139], v[20:23], v[164:167], v[136:139]\n"
    "v_exp_f32 v149, v149 clamp\n"
    "v_mfma_f32_16x16x32_bf16 v[140:143], v[24:27], v[160:163], v[124:127]\n"
    "v_exp_f32 v150, v150 clamp\n"
    "v_mfma_f32_16x16x32_bf16 v[140:143], v[28:31], v[164:167], v[140:143]\n"
    "v_exp_f32 v151, v151 clamp\n"
    "v_exp_f32 v152, v152 clamp\n"
    "s_add_u32 m0, s68, 0\n"
    "v_exp_f32 v153, v153 clamp\n"
    "v_add_u32 v253, s67, v244\n"
    "s_waitcnt lgkmcnt(0)\n"
    "v_mfma_f32_4x4x4_16b_bf16 v[224:227], v[228:229], v[168:169], v[224:227] cbsz:4 abid:0\n"
    "v_exp_f32 v154, v154 clamp\n"
    "v_exp_f32 v155, v155 clamp\n"
    "v_mfma_f32_4x4x4_16b_bf16 v[224:227], v[228:229], v[170:171], v[224:227] cbsz:4 abid:1\n"
    "v_exp_f32 v156, v156 clamp\n"
    "v_exp_f32 v157, v157 clamp\n"
    "v_mfma_f32_4x4x4_16b_bf16 v[224:227], v[228:229], v[172:173], v[224:227] cbsz:4 abid:2\n"
    "v_exp_f32 v158, v158 clamp\n"
    "v_exp_f32 v159, v159 clamp\n"
    "v_mfma_f32_4x4x4_16b_bf16 v[224:227], v[228:229], v[174:175], v[224:227] cbsz:4 abid:3\n"
    "s_waitcnt vmcnt(4)\n"
    "ds_read_b64_tr_b16 v[168:169], v240 offset:0\n"
    "ds_read_b64_tr_b16 v[170:171], v241 offset:256\n"
    "ds_read_b64_tr_b16 v[172:173], v242 offset:512\n"
    "ds_read_b64_tr_b16 v[174:175], v243 offset:768\n"
    "ds_read_b32 v236, v253 offset:0\n"
    "ds_read_b32 v238, v253 offset:4\n"
    "ds_read_b128 v[160:163], v234 offset:2048\n"
    "ds_read_b128 v[164:167], v235 offset:2048\n"
    "s_cbranch_vccz .Ldin_A3_np\n"
    "s_setprio 1\n"
    ".Ldin_A3_np:\n"
    "v_pk_fma_f32 v[144:145], v[144:145], s[70:71], v[96:97]\n"
    "v_pk_fma_f32 v[146:147], v[146:147], s[70:71], v[98:99]\n"
    "v_add_f32 v191, v224, v225\n"
    "v_add_f32 v190, v226, v227\n"
    "v_add_f32 v191, v191, v190\n"
    "v_add_u32 v190, s62, v249\n"
    "v_mul_f32 v191, s69, v191\n"
    "v_cvt_pk_bf16_f32 v191, v191, v191\n"
    "ds_write_b16 v190, v191 offset:32768\n"
    "s_bfe_u32 s94, s59, 0x180006\n"
    "s_mul_i32 s86, s94, s56\n"
    "v_mbcnt_lo_u32_b32 v184, s20, 0\n"
    "v_mbcnt_lo_u32_b32 v185, s22, 0\n"
    "v_mbcnt_lo_u32_b32 v186, s24, 0\n"
    "v_mbcnt_lo_u32_b32 v187, s26, 0\n"
    "s_bcnt1_i32_b64 s90, s[20:21]\n"
    "s_bcnt1_i32_b64 s91, s[22:23]\n"
    "s_bcnt1_i32_b64 s92, s[24:25]\n"
    "s_bcnt1_i32_b64 s93, s[26:27]\n"
    "v_mbcnt_hi_u32_b32 v184, s21, v184\n"
    "v_mbcnt_hi_u32_b32 v185, s23, v185\n"
    "v_mbcnt_hi_u32_b32 v186, s25, v186\n"
    "v_mbcnt_hi_u32_b32 v187, s27, v187\n"
    "s_lshl2_add_u32 s90, s90, s65\n"
    "s_lshl2_add_u32 s91, s91, s90\n"
    "s_lshl2_add_u32 s92, s92, s91\n"
    "s_lshl2_add_u32 s93, s93, s92\n"
    "v_add_u32_e64 v188, s86, v251\n"
    "v_pk_add_f32 v[230:231], v[144:145], v[146:147]\n"
    "v_pk_mul_f32 v[232:233], v[144:145], v[146:147]\n"
    "v_pk_fma_f32 v[148:149], v[148:149], s[70:71], v[100:101]\n"
    "v_pk_fma_f32 v[230:231], v[230:231], v[148:149], v[232:233]\n"
    "v_pk_mul_f32 v[232:233], v[232:233], v[148:149]\n"
    "v_lshl_add_u32 v184, v184, 2, s65\n"
    "v_lshl_add_u32 v185, v185, 2, s90\n"
    "v_lshl_add_u32 v186, v186, 2, s91\n"
    "v_lshl_add_u32 v187, v187, 2, s92\n"
    "v_mov_b32 v191, -1\n"
    "v_lshl_add_u32 v190, v249, 1, s93\n"
    "ds_write2_b32 v190, v191, v191 offset1:64\n"
    "s_mov_b64 exec, s[20:21]\n"
    "ds_write_b32 v184, v188\n"
    "s_mov_b64 exec, s[22:23]\n"
    "v_add_u32 v189, 64, v188\n"
    "ds_write_b32 v185, v189\n"
    "s_mov_b64 exec, s[24:25]\n"
    "v_add_u32 v190, 128, v188\n"
    "ds_write_b32 v186, v190\n"
    "s_mov_b64 exec, s[26:27]\n"
    "v_add_u32 v189, 192, v188\n"
    "ds_write_b32 v187, v189\n"
    "s_mov_b64 exec, -1\n"
    "s_add_u32 s95, s93, 60\n"
    "s_add_u32 s94, s82, 128\n"
    "s_andn2_b32 s95, s95, 63\n"
    "s_max_u32 s83, s95, s94\n"
    "v_pk_fma_f32 v[150:151], v[150:151], s[70:71], v[102:103]\n"
    "v_pk_fma_f32 v[230:231], v[230:231], v[150:151], v[232:233]\n"
    "v_pk_mul_f32 v[232:233], v[232:233], v[150:151]\n"
    "v_pk_fma_f32 v[152:153], v[152:153], s[70:71], v[104:105]\n"
    "v_pk_fma_f32 v[230:231], v[230:231], v[152:153], v[232:233]\n"
    "v_pk_mul_f32 v[232:233], v[232:233], v[152:153]\n"
    "v_pk_fma_f32 v[154:155], v[154:155], s[70:71], v[106:107]\n"
    "v_pk_fma_f32 v[230:231], v[230:231], v[154:155], v[232:233]\n"
    "v_pk_mul_f32 v[232:233], v[232:233], v[154:155]\n"
    "v_pk_fma_f32 v[156:157], v[156:157], s[70:71], v[108:109]\n"
    "v_pk_fma_f32 v[230:231], v[230:231], v[156:157], v[232:233]\n"
    "v_pk_mul_f32 v[232:233], v[232:233], v[156:157]\n"
    "v_pk_fma_f32 v[158:159], v[158:159], s[70:71], v[110:111]\n"
    "v_pk_fma_f32 v[230:231], v[230:231], v[158:159], v[232:233]\n"
    "v_pk_mul_f32 v[232:233], v[232:233], v[158:159]\n"
    "v_rcp_f32 v232, v232\n"
    "v_rcp_f32 v233, v233\n"
    "v_fma_f32 v230, v230, v232, v247\n"
    "v_fmac_f32 v230, v231, v233\n"
    "v_cvt_pk_bf16_f32 v230, v230, v230\n"
    "s_add_u32 s67, s67, 64\n"
    "s_waitcnt lgkmcnt(0)\n"
    "ds_write_b16 v245, v230\n"
    "ds_read_b64 v[228:229], v246\n"
    "buffer_load_dwordx4 v[236:237], s[40:43], 0 idxen offen offset:0 nt lds\n"
    "buffer_load_dwordx4 v[238:239], s[40:43], 0 idxen offen offset:1024 nt lds\n"
    ".Ldin_B0:\n"
    "v_mfma_f32_16x16x32_bf16 v[144:147], v[0:3], v[160:163], v[112:115]\n"
    "v_exp_f32 v128, v128 clamp\n"
    "v_mfma_f32_16x16x32_bf16 v[144:147], v[4:7], v[164:167], v[144:147]\n"
    "v_exp_f32 v129, v129 clamp\n"
    "v_mfma_f32_16x16x32_bf16 v[148:151], v[8:11], v[160:163], v[116:119]\n"
    "v_exp_f32 v130, v130 clamp\n"
    "v_mfma_f32_16x16x32_bf16 v[148:151], v[12:15], v[164:167], v[148:151]\n"
    "v_exp_f32 v131, v131 clamp\n"
    "v_mfma_f32_16x16x32_bf16 v[152:155], v[16:19], v[160:163], v[120:123]\n"
    "v_exp_f32 v132, v132 clamp\n"
    "v_mfma_f32_16x16x32_bf16 v[152:155], v[20:23], v[164:167], v[152:155]\n"
    "v_exp_f32 v133, v133 clamp\n"
    "v_mfma_f32_16x16x32_bf16 v[156:159], v[24:27], v[160:163], v[124:127]\n"
    "v_exp_f32 v134, v134 clamp\n"
    "v_mfma_f32_16x16x32_bf16 v[156:159], v[28:31], v[164:167], v[156:159]\n"
    "v_exp_f32 v135, v135 clamp\n"
    "v_exp_f32 v136, v136 clamp\n"
    "s_cmp_eq_u32 s67, s80\n"
    "s_cselect_b32 s67, s65, s67\n"
    "s_and_b32 s94, s59, 63\n"
    "s_mul_i32 s94, s94, 272\n"
    "v_add_u32 v252, s94, v250\n"
    "s_add_u32 m0, s68, 2048\n"
    "v_exp_f32 v137, v137 clamp\n"
    "v_add_u32 v253, s67, v244\n"
    "s_waitcnt lgkmcnt(0)\n"
    "v_mfma_f32_4x4x4_16b_bf16 v[224:227], v[228:229], v[176:177], 0 cbsz:4 abid:0\n"
    "v_exp_f32 v138, v138 clamp\n"
    "v_exp_f32 v139, v139 clamp\n"
    "v_mfma_f32_4x4x4_16b_bf16 v[224:227], v[228:229], v[178:179], v[224:227] cbsz:4 abid:1\n"
    "v_exp_f32 v140, v140 clamp\n"
    "v_exp_f32 v141, v141 clamp\n"
    "v_mfma_f32_4x4x4_16b_bf16 v[224:227], v[228:229], v[180:181], v[224:227] cbsz:4 abid:2\n"
    "v_exp_f32 v142, v142 clamp\n"
    "v_exp_f32 v143, v143 clamp\n"
    "v_mfma_f32_4x4x4_16b_bf16 v[224:227], v[228:229], v[182:183], v[224:227] cbsz:4 abid:3\n"
    "s_waitcnt vmcnt(4)\n"
    "s_memrealtime s[34:35]\n"
    "ds_read_b64_tr_b16 v[176:177], v240 offset:2048\n"
    "ds_read_b64_tr_b16 v[178:179], v241 offset:2304\n"
    "ds_read_b64_tr_b16 v[180:181], v242 offset:2560\n"
    "ds_read_b64_tr_b16 v[182:183], v243 offset:2816\n"
    "ds_read_b32 v236, v253 offset:0\n"
    "ds_read_b32 v238, v253 offset:4\n"
    "ds_read_b128 v[160:163], v234 offset:4096\n"
    "ds_read_b128 v[164:167], v235 offset:4096\n"
    "ds_read_b128 v[184:187], v252 offset:0\n"
    "ds_read_b128 v[188:191], v252 offset:16\n"
    "v_pk_fma_f32 v[128:129], v[128:129], s[70:71], v[96:97]\n"
    "v_pk_fma_f32 v[130:131], v[130:131], s[70:71], v[98:99]\n"
    "v_pk_add_f32 v[230:231], v[128:129], v[130:131]\n"
    "v_pk_mul_f32 v[232:233], v[128:129], v[130:131]\n"
    "v_pk_fma_f32 v[132:133], v[132:133], s[70:71], v[100:101]\n"
    "v_pk_fma_f32 v[230:231], v[230:231], v[132:133], v[232:233]\n"
    "v_pk_mul_f32 v[232:233], v[232:233], v[132:133]\n"
    "v_pk_fma_f32 v[134:135], v[134:135], s[70:71], v[102:103]\n"
    "v_pk_fma_f32 v[230:231], v[230:231], v[134:135], v[232:233]\n"
    "v_pk_mul_f32 v[232:233], v[232:233], v[134:135]\n"
    "v_pk_fma_f32 v[136:137], v[136:137], s[70:71], v[104:105]\n"
    "v_pk_fma_f32 v[230:231], v[230:231], v[136:137], v[232:233]\n"
    "v_pk_mul_f32 v[232:233], v[232:233], v[136:137]\n"
    "v_pk_fma_f32 v[138:139], v[138:139], s[70:71], v[106:107]\n"
    "v_pk_fma_f32 v[230:231], v[230:231], v[138:139], v[232:233]\n"
    "v_pk_mul_f32 v[232:233], v[232:233], v[138:139]\n"
    "v_pk_fma_f32 v[140:141], v[140:141], s[70:71], v[108:109]\n"
    "v_pk_fma_f32 v[230:231], v[230:231], v[140:141], v[232:233]\n"
    "v_pk_mul_f32 v[232:233], v[232:233], v[140:141]\n"
    "v_pk_fma_f32 v[142:143], v[142:143], s[70:71], v[110:111]\n"
    "v_pk_fma_f32 v[230:231], v[230:231], v[142:143], v[232:233]\n"
    "v_pk_mul_f32 v[232:233], v[232:233], v[142:143]\n"
    "v_rcp_f32 v232, v232\n"
    "v_rcp_f32 v233, v233\n"
    "v_fma_f32 v230, v230, v232, v247\n"
    "v_fmac_f32 v230, v231, v233\n"
    "v_cvt_pk_bf16_f32 v230, v230, v230\n"
    "s_add_u32 s67, s67, 64\n"
    "s_waitcnt lgkmcnt(0)\n"
    "ds_write_b16 v245, v230\n"
    "ds_read_b64 v[228:229], v246\n"
    "s_cmp_le_u32 s34, s76\n"
    "s_cbranch_scc1 .Ldin_B0_wait\n"
    ".Ldin_B0_go:\n"
    "s_sub_u32 s94, s34, 60\n"
    "s_max_u32 s76, s76, s94\n"
    "s_add_u32 s76, s76, s72\n"
    ".Ldin_B0_dma:\n"
    "buffer_load_dwordx4 v[236:237], s[40:43], 0 idxen offen offset:0 nt lds\n"
    "buffer_load_dwordx4 v[238:239], s[40:43], 0 idxen offen offset:1024 nt lds\n"
    ".Ldin_B0_nd:\n"
    "s_branch .Ldin_G1\n"
    ".Ldin_L2_poll:\n"
    "s_sleep 1\n"
    "ds_read_b64 v[232:233], v253\n"
    "s_waitcnt lgkmcnt(0)\n"
    "v_readfirstlane_b32 s60, v232\n"
    "s_cmp_eq_u32 s60, -1\n"
    "s_cbranch_scc1 .Ldin_L2_poll\n"
    "s_branch .Ldin_L2_pr\n"
    ".Ldin_B0_wait:\n"
    "s_cmp_lt_i32 s59, s57\n"
    "s_cbranch_scc1 .Ldin_B0_w2\n"
    "s_sub_u32 s94, s67, s65\n"
    "s_cmp_gt_u32 s94, 256\n"
    "s_cbranch_scc1 .Ldin_B0_dma\n"
    "s_cmp_eq_u32 s94, 64\n"
    "s_waitcnt vmcnt(2)\n"
    "s_cbranch_scc1 .Ldin_B0_nd\n"
    "s_waitcnt vmcnt(0)\n"
    "s_branch .Ldin_B0_nd\n"
    ".Ldin_B0_slp:\n"
    "s_sleep 1\n"
    ".Ldin_B0_w2:\n"
    "s_memrealtime s[34:35]\n"
    "s_waitcnt lgkmcnt(0)\n"
    "s_sub_u32 s94, s34, s76\n"
    "s_cmp_lt_i32 s94, 0\n"
    "s_cbranch_scc1 .Ldin_B0_slp\n"
    "s_branch .Ldin_B0_go\n"
    ".p2align 5\n"
    ".Ldin_L1:\n"
    "s_setprio 0\n"
    "s_cmp_ge_i32 s59, s57\n"
    "s_cbranch_scc1 .Ldin_E1\n"
    "s_add_u32 s76, s76, s73\n"
    "v_mov_b32 v253, s63\n"
    "v_and_b32 v184, v254, v184\n"
    "v_and_b32 v185, v255, v185\n"
    "v_and_b32 v186, v254, v186\n"
    "v_and_b32 v187, v255, v187\n"
    "v_and_b32 v188, v254, v188\n"
    "v_and_b32 v189, v255, v189\n"
    "v_and_b32 v190, v254, v190\n"
    "v_and_b32_e64 v191, v255, v191\n"
    "ds_read_b64 v[232:233], v253\n"
    "v_mfma_f32_4x4x4_16b_bf16 v[128:131], v[184:185], v[32:33], v[64:67]\n"
    "ds_read_b128 v[112:115], v252 offset:17408\n"
    "v_mfma_f32_4x4x4_16b_bf16 v[132:135], v[186:187], v[34:35], v[68:71]\n"
    "ds_read_b128 v[116:119], v252 offset:17424\n"
    "v_mfma_f32_4x4x4_16b_bf16 v[136:139], v[188:189], v[36:37], v[72:75]\n"
    "ds_read_b128 v[120:123], v252 offset:17536\n"
    "ds_read_b128 v[124:127], v252 offset:17552\n"
    "v_cvt_pk_bf16_f32 v0, v128, v129\n"
    "v_cvt_pk_bf16_f32 v1, v130, v131\n"
    "v_mfma_f32_4x4x4_16b_bf16 v[140:143], v[190:191], v[38:39], v[76:79]\n"
    "v_cvt_pk_bf16_f32 v2, v132, v133\n"
    "v_cvt_pk_bf16_f32 v3, v134, v135\n"
    "v_mfma_f32_4x4x4_16b_bf16 v[128:131], v[184:185], v[40:41], v[80:83]\n"
    "v_cvt_pk_bf16_f32 v4, v136, v137\n"
    "v_cvt_pk_bf16_f32 v5, v138, v139\n"
    "v_mfma_f32_4x4x4_16b_bf16 v[132:135], v[186:187], v[42:43], v[84:87]\n"
    "v_cvt_pk_bf16_f32 v6, v140, v141\n"
    "v_cvt_pk_bf16_f32 v7, v142, v143\n"
    "v_mfma_f32_4x4x4_16b_bf16 v[136:139], v[188:189], v[44:45], v[88:91]\n"
    "v_cvt_pk_bf16_f32 v8, v128, v129\n"
    "v_cvt_pk_bf16_f32 v9, v130, v131\n"
    "v_mfma_f32_4x4x4_16b_bf16 v[140:143], v[190:191], v[46:47], v[92:95]\n"
    "v_cvt_pk_bf16_f32 v10, v132, v133\n"
    "v_cvt_pk_bf16_f32 v11, v134, v135\n"
    "v_mfma_f32_4x4x4_16b_bf16 v[128:131], v[184:185], v[48:49], v[192:195]\n"
    "v_cvt_pk_bf16_f32 v12, v136, v137\n"
    "v_cvt_pk_bf16_f32 v13, v138, v139\n"
    "v_mfma_f32_4x4x4_16b_bf16 v[132:135], v[186:187], v[50:51], v[196:199]\n"
    "v_cvt_pk_bf16_f32 v14, v140, v141\n"
    "v_cvt_pk_bf16_f32 v15, v142, v143\n"
    "v_mfma_f32_4x4x4_16b_bf16 v[136:139], v[188:189], v[52:53], v[200:203]\n"
    "s_waitcnt lgkmcnt(0)\n"
    "v_readfirstlane_b32 s60, v232\n"
    "v_cvt_pk_bf16_f32 v16, v128, v129\n"
    "v_cvt_pk_bf16_f32 v17, v130, v131\n"
    "v_mfma_f32_4x4x4_16b_bf16 v[140:143], v[190:191], v[54:55], v[204:207]\n"
    "s_cmp_eq_u32 s60, -1\n"
    "s_cbranch_scc1 .Ldin_L1_poll\n"
    ".Ldin_L1_pr:\n"
    "v_readfirstlane_b32 s95, v233\n"
    "s_and_b32 s94, s60, 63\n"
    "s_lshl_b32 s94, s94, 6\n"
    "s_add_u32 s94, s94, 68480\n"
    "s_add_u32 s63, s63, 8\n"
    "v_lshl_add_u32 v252, v249, 1, s94\n"
    "ds_read_b32 v252, v252\n"
    "s_sub_u32 s95, s95, s76\n"
    "s_ashr_i32 s95, s95, 4\n"
    "s_min_i32 s95, s95, s74\n"
    "s_max_i32 s72, s95, s75\n"
    "v_cvt_pk_bf16_f32 v18, v132, v133\n"
    "v_cvt_pk_bf16_f32 v19, v134, v135\n"
    "v_mfma_f32_4x4x4_16b_bf16 v[128:131], v[184:185], v[56:57], v[208:211]\n"
    "v_cvt_pk_bf16_f32 v20, v136, v137\n"
    "v_cvt_pk_bf16_f32 v21, v138, v139\n"
    "v_mfma_f32_4x4x4_16b_bf16 v[132:135], v[186:187], v[58:59], v[212:215]\n"
    "v_cvt_pk_bf16_f32 v22, v140, v141\n"
    "v_cvt_pk_bf16_f32 v23, v142, v143\n"
    "v_mfma_f32_4x4x4_16b_bf16 v[136:139], v[188:189], v[60:61], v[216:219]\n"
    "v_cvt_pk_bf16_f32 v24, v128, v129\n"
    "v_cvt_pk_bf16_f32 v25, v130, v131\n"
    "v_mfma_f32_4x4x4_16b_bf16 v[140:143], v[190:191], v[62:63], v[220:223]\n"
    "v_cvt_pk_bf16_f32 v26, v132, v133\n"
    "v_cvt_pk_bf16_f32 v27, v134, v135\n"
    "v_cvt_pk_bf16_f32 v28, v136, v137\n"
    "v_cvt_pk_bf16_f32 v29, v138, v139\n"
    "v_exp_f32 v152, v152 clamp\n"
    "v_cvt_pk_bf16_f32 v30, v140, v141\n"
    "v_cvt_pk_bf16_f32 v31, v142, v143\n"
    "v_mfma_f32_16x16x32_bf16 v[128:131], v[0:3], v[160:163], v[112:115]\n"
    "v_exp_f32 v144, v144 clamp\n"
    "v_mfma_f32_16x16x32_bf16 v[128:131], v[4:7], v[164:167], v[128:131]\n"
    "v_exp_f32 v145, v145 clamp\n"
    "v_mfma_f32_16x16x32_bf16 v[132:135], v[8:11], v[160:163], v[116:119]\n"
    "v_exp_f32 v146, v146 clamp\n"
    "v_mfma_f32_16x16x32_bf16 v[132:135], v[12:15], v[164:167], v[132:135]\n"
    "v_exp_f32 v147, v147 clamp\n"
    "v_mfma_f32_16x16x32_bf16 v[136:139], v[16:19], v[160:163], v[120:123]\n"
    "v_exp_f32 v148, v148 clamp\n"
    "v_mfma_f32_16x16x32_bf16 v[136:139], v[20:23], v[164:167], v[136:139]\n"
    "v_exp_f32 v149, v149 clamp\n"
    "v_mfma_f32_16x16x32_bf16 v[140:143], v[24:27], v[160:163], v[124:127]\n"
    "v_exp_f32 v150, v150 clamp\n"
    "v_mfma_f32_16x16x32_bf16 v[140:143], v[28:31], v[164:167], v[140:143]\n"
    "v_exp_f32 v151, v151 clamp\n"
    "s_add_u32 m0, s68, 4096\n"
    "v_exp_f32 v153, v153 clamp\n"
    "v_add_u32 v253, s67, v244\n"
    "v_mfma_f32_4x4x4_16b_bf16 v[224:227], v[228:229], v[168:169], v[224:227] cbsz:4 abid:0\n"
    "v_exp_f32 v154, v154 clamp\n"
    "v_exp_f32 v155, v155 clamp\n"
    "v_mfma_f32_4x4x4_16b_bf16 v[224:227], v[228:229], v[170:171], v[224:227] cbsz:4 abid:1\n"
    "v_exp_f32 v156, v156 clamp\n"
    "v_exp_f32 v157, v157 clamp\n"
    "v_mfma_f32_4x4x4_16b_bf16 v[224:227], v[228:229], v[172:173], v[224:227] cbsz:4 abid:2\n"
    "v_exp_f32 v158, v158 clamp\n"
    "v_exp_f32 v159, v159 clamp\n"
    "v_mfma_f32_4x4x4_16b_bf16 v[224:227], v[228:229], v[174:175], v[224:227] cbsz:4 abid:3\n"
    "s_waitcnt vmcnt(4)\n"
    "ds_read_b64_tr_b16 v[168:169], v240 offset:4096\n"
    "ds_read_b64_tr_b16 v[170:171], v241 offset:4352\n"
    "ds_read_b64_tr_b16 v[172:173], v242 offset:4608\n"
    "ds_read_b64_tr_b16 v[174:175], v243 offset:4864\n"
    "ds_read_b32 v236, v253 offset:0\n"
    "ds_read_b32 v238, v253 offset:4\n"
    "ds_read_b128 v[160:163], v234 offset:6144\n"
    "ds_read_b128 v[164:167], v235 offset:6144\n"
    "s_waitcnt lgkmcnt(8)\n"
    "s_nop 0\n"
    "v_readlane_b32 s20, v252, 0\n"
    "v_readlane_b32 s21, v252, 1\n"
    "v_readlane_b32 s22, v252, 2\n"
    "v_readlane_b32 s23, v252, 3\n"
    "v_readlane_b32 s24, v252, 4\n"
    "v_readlane_b32 s25, v252, 5\n"
    "v_readlane_b32 s26, v252, 6\n"
    "v_readlane_b32 s27, v252, 7\n"
    "v_pk_fma_f32 v[144:145], v[144:145], s[70:71], v[96:97]\n"
    "v_pk_fma_f32 v[146:147], v[146:147], s[70:71], v[98:99]\n"
    "v_pk_add_f32 v[230:231], v[144:145], v[146:147]\n"
    "v_pk_mul_f32 v[232:233], v[144:145], v[146:147]\n"
    "v_pk_fma_f32 v[148:149], v[148:149], s[70:71], v[100:101]\n"
    "v_pk_fma_f32 v[230:231], v[230:231], v[148:149], v[232:233]\n"
    "v_pk_mul_f32 v[232:233], v[232:233], v[148:149]\n"
    "v_pk_fma_f32 v[150:151], v[150:151], s[70:71], v[102:103]\n"
    "v_pk_fma_f32 v[230:231], v[230:231], v[150:151], v[232:233]\n"
    "v_pk_mul_f32 v[232:233], v[232:233], v[150:151]\n"
    "v_pk_fma_f32 v[152:153], v[152:153], s[70:71], v[104:105]\n"
    "v_pk_fma_f32 v[230:231], v[230:231], v[152:153], v[232:233]\n"
    "v_pk_mul_f32 v[232:233], v[232:233], v[152:153]\n"
    "v_pk_fma_f32 v[154:155], v[154:155], s[70:71], v[106:107]\n"
    "v_pk_fma_f32 v[230:231], v[230:231], v[154:155], v[232:233]\n"
    "v_pk_mul_f32 v[232:233], v[232:233], v[154:155]\n"
    "v_pk_fma_f32 v[156:157], v[156:157], s[70:71], v[108:109]\n"
    "v_pk_fma_f32 v[230:231], v[230:231], v[156:157], v[232:233]\n"
    "v_pk_mul_f32 v[232:233], v[232:233], v[156:157]\n"
    "v_pk_fma_f32 v[158:159], v[158:159], s[70:71], v[110:111]\n"
    "v_pk_fma_f32 v[230:231], v[230:231], v[158:159], v[232:233]\n"
    "v_pk_mul_f32 v[232:233], v[232:233], v[158:159]\n"
    "v_rcp_f32 v232, v232\n"
    "v_rcp_f32 v233, v233\n"
    "v_fma_f32 v230, v230, v232, v247\n"
    "v_fmac_f32 v230, v231, v233\n"
    "v_cvt_pk_bf16_f32 v230, v230, v230\n"
    "s_add_u32 s67, s67, 64\n"
    "s_waitcnt lgkmcnt(0)\n"
    "ds_write_b16 v245, v230\n"
    "ds_read_b64 v[228:229], v246\n"
    "s_lshl1_add_u32 s76, s72, s76\n"
    "buffer_load_dwordx4 v[236:237], s[40:43], 0 idxen offen offset:0 nt lds\n"
    "buffer_load_dwordx4 v[238:239], s[40:43], 0 idxen offen offset:1024 nt lds\n"
    "s_and_b32 s62, s58, 63\n"
    "s_mul_i32 s62, s62, 272\n"
    "s_mov_b32 s58, s59\n"
    "s_mov_b32 s59, s60\n"
    "s_mov_b32 s81, s65\n"
    "s_xor_b32 s65, s65, s64\n"
    "s_mov_b32 s80, s83\n"
    "s_add_u32 s82, s65, 256\n"
    "s_cmp_lt_i32 s59, s57\n"
    "s_cselect_b32 s76, s76, -1\n"
    "s_bitcmp1_b32 s58, 30\n"
    "s_cbranch_scc1 .Ldin_A2s\n"
    ".Ldin_A2:\n"
    "v_mfma_f32_16x16x32_bf16 v[144:147], v[0:3], v[160:163], v[112:115]\n"
    "v_exp_f32 v128, v128 clamp\n"
    "v_mfma_f32_16x16x32_bf16 v[144:147], v[4:7], v[164:167], v[144:147]\n"
    "v_exp_f32 v129, v129 clamp\n"
    "v_mfma_f32_16x16x32_bf16 v[148:151], v[8:11], v[160:163], v[116:119]\n"
    "v_exp_f32 v130, v130 clamp\n"
    "v_mfma_f32_16x16x32_bf16 v[148:151], v[12:15], v[164:167], v[148:151]\n"
    "v_exp_f32 v131, v131 clamp\n"
    "v_mfma_f32_16x16x32_bf16 v[152:155], v[16:19], v[160:163], v[120:123]\n"
    "v_exp_f32 v132, v132 clamp\n"
    "v_mfma_f32_16x16x32_bf16 v[152:155], v[20:23], v[164:167], v[152:155]\n"
    "v_exp_f32 v133, v133 clamp\n"
    "v_mfma_f32_16x16x32_bf16 v[156:159], v[24:27], v[160:163], v[124:127]\n"
    "v_exp_f32 v134, v134 clamp\n"
    "v_mfma_f32_16x16x32_bf16 v[156:159], v[28:31], v[164:167], v[156:159]\n"
    "v_exp_f32 v135, v135 clamp\n"
    "v_exp_f32 v136, v136 clamp\n"
    "s_add_u32 m0, s68, 6144\n"
    "v_exp_f32 v137, v137 clamp\n"
    "v_add_u32_e64 v253, s67, v244\n"
    "s_waitcnt lgkmcnt(0)\n"
    "v_mfma_f32_4x4x4_16b_bf16 v[224:227], v[228:229], v[176:177], v[224:227] cbsz:4 abid:0\n"
    "v_exp_f32 v138, v138 clamp\n"
    "v_exp_f32 v139, v139 clamp\n"
    "v_mfma_f32_4x4x4_16b_bf16 v[224:227], v[228:229], v[178:179], v[224:227] cbsz:4 abid:1\n"
    "v_exp_f32 v140, v140 clamp\n"
    "v_exp_f32 v141, v141 clamp\n"
    "v_mfma_f32_4x4x4_16b_bf16 v[224:227], v[228:229], v[180:181], v[224:227] cbsz:4 abid:2\n"
    "v_exp_f32 v142, v142 clamp\n"
    "v_exp_f32 v143, v143 clamp\n"
    "v_mfma_f32_4x4x4_16b_bf16 v[224:227], v[228:229], v[182:183], v[224:227] cbsz:4 abid:3\n"
    "s_waitcnt vmcnt(4)\n"
    "ds_read_b64_tr_b16 v[176:177], v240 offset:6144\n"
    "ds_read_b64_tr_b16 v[178:179], v241 offset:6400\n"
    "ds_read_b64_tr_b16 v[180:181], v242 offset:6656\n"
    "ds_read_b64_tr_b16 v[182:183], v243 offset:6912\n"
    "ds_read_b32 v236, v253 offset:0\n"
    "ds_read_b32 v238, v253 offset:4\n"
    "ds_read_b128 v[160:163], v234 offset:0\n"
    "ds_read_b128 v[164:167], v235 offset:0\n"
    "s_cbranch_vccz .Ldin_A2_np\n"
    "s_setprio 1\n"
    ".Ldin_A2_np:\n"
    "v_pk_fma_f32 v[128:129], v[128:129], s[70:71], v[96:97]\n"
    "v_pk_fma_f32 v[130:131], v[130:131], s[70:71], v[98:99]\n"
    "v_add_f32 v191, v224, v225\n"
    "v_add_f32 v190, v226, v227\n"
    "v_add_f32 v191, v191, v190\n"
    "v_add_u32 v190, s62, v249\n"
    "v_mul_f32 v191, s69, v191\n"
    "v_cvt_pk_bf16_f32 v191, v191, v191\n"
    "ds_write_b16 v190, v191 offset:32768\n"
    "s_bfe_u32 s94, s59, 0x180006\n"
    "s_mul_i32 s86, s94, s56\n"
    "v_mbcnt_lo_u32_b32 v184, s20, 0\n"
    "v_mbcnt_lo_u32_b32 v185, s22, 0\n"
    "v_mbcnt_lo_u32_b32 v186, s24, 0\n"
    "v_mbcnt_lo_u32_b32 v187, s26, 0\n"
    "s_bcnt1_i32_b64 s90, s[20:21]\n"
    "s_bcnt1_i32_b64 s91, s[22:23]\n"
    "s_bcnt1_i32_b64 s92, s[24:25]\n"
    "s_bcnt1_i32_b64 s93, s[26:27]\n"
    "v_mbcnt_hi_u32_b32 v184, s21, v184\n"
    "v_mbcnt_hi_u32_b32 v185, s23, v185\n"
    "v_mbcnt_hi_u32_b32 v186, s25, v186\n"
    "v_mbcnt_hi_u32_b32 v187, s27, v187\n"
    "s_lshl2_add_u32 s90, s90, s65\n"
    "s_lshl2_add_u32 s91, s91, s90\n"
    "s_lshl2_add_u32 s92, s92, s91\n"
    "s_lshl2_add_u32 s93, s93, s92\n"
    "v_add_u32_e64 v188, s86, v251\n"
    "v_pk_add_f32 v[230:231], v[128:129], v[130:131]\n"
    "v_pk_mul_f32 v[232:233], v[128:129], v[130:131]\n"
    "v_pk_fma_f32 v[132:133], v[132:133], s[70:71], v[100:101]\n"
    "v_pk_fma_f32 v[230:231], v[230:231], v[132:133], v[232:233]\n"
    "v_pk_mul_f32 v[232:233], v[232:233], v[132:133]\n"
    "v_lshl_add_u32 v184, v184, 2, s65\n"
    "v_lshl_add_u32 v185, v185, 2, s90\n"
    "v_lshl_add_u32 v186, v186, 2, s91\n"
    "v_lshl_add_u32 v187, v187, 2, s92\n"
    "v_mov_b32 v191, -1\n"
    "v_lshl_add_u32 v190, v249, 1, s93\n"
    "ds_write2_b32 v190, v191, v191 offset1:64\n"
    "s_mov_b64 exec, s[20:21]\n"
    "ds_write_b32 v184, v188\n"
    "s_mov_b64 exec, s[22:23]\n"
    "v_add_u32 v189, 64, v188\n"
    "ds_write_b32 v185, v189\n"
    "s_mov_b64 exec, s[24:25]\n"
    "v_add_u32 v190, 128, v188\n"
    "ds_write_b32 v186, v190\n"
    "s_mov_b64 exec, s[26:27]\n"
    "v_add_u32 v189, 192, v188\n"
    "ds_write_b32 v187, v189\n"
    "s_mov_b64 exec, -1\n"
    "s_add_u32 s95, s93, 60\n"
    "s_add_u32 s94, s82, 128\n"
    "s_andn2_b32 s95, s95, 63\n"
    "s_max_u32 s83, s95, s94\n"
    "v_pk_fma_f32 v[134:135], v[134:135], s[70:71], v[102:103]\n"
    "v_pk_fma_f32 v[230:231], v[230:231], v[134:135], v[232:233]\n"
    "v_pk_mul_f32 v[232:233], v[232:233], v[134:135]\n"
    "v_pk_fma_f32 v[136:137], v[136:137], s[70:71], v[104:105]\n"
    "v_pk_fma_f32 v[230:231], v[230:231], v[136:137], v[232:233]\n"
    "v_pk_mul_f32 v[232:233], v[232:233], v[136:137]\n"
    "v_pk_fma_f32 v[138:139], v[138:139], s[70:71], v[106:107]\n"
    "v_pk_fma_f32 v[230:231], v[230:231], v[138:139], v[232:233]\n"
    "v_pk_mul_f32 v[232:233], v[232:233], v[138:139]\n"
    "v_pk_fma_f32 v[140:141], v[140:141], s[70:71], v[108:109]\n"
    "v_pk_fma_f32 v[230:231], v[230:231], v[140:141], v[232:233]\n"
    "v_pk_mul_f32 v[232:233], v[232:233], v[140:141]\n"
    "v_pk_fma_f32 v[142:143], v[142:143], s[70:71], v[110:111]\n"
    "v_pk_fma_f32 v[230:231], v[230:231], v[142:143], v[232:233]\n"
    "v_pk_mul_f32 v[232:233], v[232:233], v[142:143]\n"
    "v_rcp_f32 v232, v232\n"
    "v_rcp_f32 v233, v233\n"
    "v_fma_f32 v230, v230, v232, v247\n"
    "v_fmac_f32 v230, v231, v233\n"
    "v_cvt_pk_bf16_f32 v230, v230, v230\n"
    "s_add_u32 s67, s67, 64\n"
    "s_waitcnt lgkmcnt(0)\n"
    "ds_write_b16 v245, v230\n"
    "ds_read_b64 v[228:229], v246\n"
    "buffer_load_dwordx4 v[236:237], s[40:43], 0 idxen offen offset:0 nt lds\n"
    "buffer_load_dwordx4 v[238:239], s[40:43], 0 idxen offen offset:1024 nt lds\n"
    ".Ldin_B3:\n"
    "v_mfma_f32_16x16x32_bf16 v[128:131], v[0:3], v[160:163], v[112:115]\n"
    "v_exp_f32 v144, v144 clamp\n"
    "v_mfma_f32_16x16x32_bf16 v[128:131], v[4:7], v[164:167], v[128:131]\n"
    "v_exp_f32 v145, v145 clamp\n"
    "v_mfma_f32_16x16x32_bf16 v[132:135], v[8:11], v[160:163], v[116:119]\n"
    "v_exp_f32 v146, v146 clamp\n"
    "v_mfma_f32_16x16x32_bf16 v[132:135], v[12:15], v[164:167], v[132:135]\n"
    "v_exp_f32 v147, v147 clamp\n"
    "v_mfma_f32_16x16x32_bf16 v[136:139], v[16:19], v[160:163], v[120:123]\n"
    "v_exp_f32 v148, v148 clamp\n"
    "v_mfma_f32_16x16x32_bf16 v[136:139], v[20:23], v[164:167], v[136:139]\n"
    "v_exp_f32 v149, v149 clamp\n"
    "v_mfma_f32_16x16x32_bf16 v[140:143], v[24:27], v[160:163], v[124:127]\n"
    "v_exp_f32 v150, v150 clamp\n"
    "v_mfma_f32_16x16x32_bf16 v[140:143], v[28:31], v[164:167], v[140:143]\n"
    "v_exp_f32 v151, v151 clamp\n"
    "v_exp_f32 v152, v152 clamp\n"
    "s_cmp_eq_u32 s67, s80\n"
    "s_cselect_b32 s67, s65, s67\n"
    "s_and_b32 s94, s59, 63\n"
    "s_mul_i32 s94, s94, 272\n"
    "v_add_u32 v252, s94, v250\n"
    "s_add_u32 m0, s68, 0\n"
    "v_exp_f32 v153, v153 clamp\n"
    "v_add_u32_e64 v253, s67, v244\n"
    "s_waitcnt lgkmcnt(0)\n"
    "v_mfma_f32_4x4x4_16b_bf16 v[224:227], v[228:229], v[168:169], 0 cbsz:4 abid:0\n"
    "v_exp_f32 v154, v154 clamp\n"
    "v_exp_f32 v155, v155 clamp\n"
    "v_mfma_f32_4x4x4_16b_bf16 v[224:227], v[228:229], v[170:171], v[224:227] cbsz:4 abid:1\n"
    "v_exp_f32 v156, v156 clamp\n"
    "v_exp_f32 v157, v157 clamp\n"
    "v_mfma_f32_4x4x4_16b_bf16 v[224:227], v[228:229], v[172:173], v[224:227] cbsz:4 abid:2\n"
    "v_exp_f32 v158, v158 clamp\n"
    "v_exp_f32 v159, v159 clamp\n"
    "v_mfma_f32_4x4x4_16b_bf16 v[224:227], v[228:229], v[174:175], v[224:227] cbsz:4 abid:3\n"
    "s_waitcnt vmcnt(4)\n"
    "s_memrealtime s[34:35]\n"
    "ds_read_b64_tr_b16 v[168:169], v240 offset:0\n"
    "ds_read_b64_tr_b16 v[170:171], v241 offset:256\n"
    "ds_read_b64_tr_b16 v[172:173], v242 offset:512\n"
    "ds_read_b64_tr_b16 v[174:175], v243 offset:768\n"
    "ds_read_b32 v236, v253 offset:0\n"
    "ds_read_b32 v238, v253 offset:4\n"
    "ds_read_b128 v[160:163], v234 offset:2048\n"
    "ds_read_b128 v[164:167], v235 offset:2048\n"
    "ds_read_b128 v[184:187], v252 offset:0\n"
    "ds_read_b128 v[188:191], v252 offset:16\n"
    "v_pk_fma_f32 v[144:145], v[144:145], s[70:71], v[96:97]\n"
    "v_pk_fma_f32 v[146:147], v[146:147], s[70:71], v[98:99]\n"
    "v_pk_add_f32 v[230:231], v[144:145], v[146:147]\n"
    "v_pk_mul_f32 v[232:233], v[144:145], v[146:147]\n"
    "v_pk_fma_f32 v[148:149], v[148:149], s[70:71], v[100:101]\n"
    "v_pk_fma_f32 v[230:231], v[230:231], v[148:149], v[232:233]\n"
    "v_pk_mul_f32 v[232:233], v[232:233], v[148:149]\n"
    "v_pk_fma_f32 v[150:151], v[150:151], s[70:71], v[102:103]\n"
    "v_pk_fma_f32 v[230:231], v[230:231], v[150:151], v[232:233]\n"
    "v_pk_mul_f32 v[232:233], v[232:233], v[150:151]\n"
    "v_pk_fma_f32 v[152:153], v[152:153], s[70:71], v[104:105]\n"
    "v_pk_fma_f32 v[230:231], v[230:231], v[152:153], v[232:233]\n"
    "v_pk_mul_f32 v[232:233], v[232:233], v[152:153]\n"
    "v_pk_fma_f32 v[154:155], v[154:155], s[70:71], v[106:107]\n"
    "v_pk_fma_f32 v[230:231], v[230:231], v[154:155], v[232:233]\n"
    "v_pk_mul_f32 v[232:233], v[232:233], v[154:155]\n"
    "v_pk_fma_f32 v[156:157], v[156:157], s[70:71], v[108:109]\n"
    "v_pk_fma_f32 v[230:231], v[230:231], v[156:157], v[232:233]\n"
    "v_pk_mul_f32 v[232:233], v[232:233], v[156:157]\n"
    "v_pk_fma_f32 v[158:159], v[158:159], s[70:71], v[110:111]\n"
    "v_pk_fma_f32 v[230:231], v[230:231], v[158:159], v[232:233]\n"
    "v_pk_mul_f32 v[232:233], v[232:233], v[158:159]\n"
    "v_rcp_f32 v232, v232\n"
    "v_rcp_f32 v233, v233\n"
    "v_fma_f32 v230, v230, v232, v247\n"
    "v_fmac_f32 v230, v231, v233\n"
    "v_cvt_pk_bf16_f32 v230, v230, v230\n"
    "s_add_u32 s67, s67, 64\n"
    "s_waitcnt lgkmcnt(0)\n"
    "ds_write_b16 v245, v230\n"
    "ds_read_b64 v[228:229], v246\n"
    "s_cmp_le_u32 s34, s76\n"
    "s_cbranch_scc1 .Ldin_B3_wait\n"
    ".Ldin_B3_go:\n"
    "s_sub_u32 s94, s34, 60\n"
    "s_max_u32 s76, s76, s94\n"
    "s_add_u32 s76, s76, s72\n"
    ".Ldin_B3_dma:\n"
    "buffer_load_dwordx4 v[236:237], s[40:43], 0 idxen offen offset:0 nt lds\n"
    "buffer_load_dwordx4 v[238:239], s[40:43], 0 idxen offen offset:1024 nt lds\n"
    ".Ldin_B3_nd:\n"
    "s_branch .Ldin_G0\n"
    ".Ldin_L1_poll:\n"
    "s_sleep 1\n"
    "ds_read_b64 v[232:233], v253\n"
    "s_waitcnt lgkmcnt(0)\n"
    "v_readfirstlane_b32 s60, v232\n"
    "s_cmp_eq_u32 s60, -1\n"
    "s_cbranch_scc1 .Ldin_L1_poll\n"
    "s_branch .Ldin_L1_pr\n"
    ".Ldin_B3_wait:\n"
    "s_cmp_lt_i32 s59, s57\n"
    "s_cbranch_scc1 .Ldin_B3_w2\n"
    "s_sub_u32 s94, s67, s65\n"
    "s_cmp_gt_u32 s94, 256\n"
    "s_cbranch_scc1 .Ldin_B3_dma\n"
    "s_cmp_eq_u32 s94, 64\n"
    "s_waitcnt vmcnt(2)\n"
    "s_cbranch_scc1 .Ldin_B3_nd\n"
    "s_waitcnt vmcnt(0)\n"
    "s_branch .Ldin_B3_nd\n"
    ".Ldin_B3_slp:\n"
    "s_sleep 1\n"
    ".Ldin_B3_w2:\n"
    "s_memrealtime s[34:35]\n"
    "s_waitcnt lgkmcnt(0)\n"
    "s_sub_u32 s94, s34, s76\n"
    "s_cmp_lt_i32 s94, 0\n"
    "s_cbranch_scc1 .Ldin_B3_slp\n"
    "s_branch .Ldin_B3_go\n"
    ".p2align 5\n"
    ".Ldin_L0:\n"
    "s_setprio 0\n"
    "s_cmp_ge_i32 s59, s57\n"
    "s_cbranch_scc1 .Ldin_E0\n"
    "s_add_u32 s76, s76, s73\n"
    "v_mov_b32 v253, s63\n"
    "v_and_b32 v184, v254, v184\n"
    "v_and_b32 v185, v255, v185\n"
    "v_and_b32 v186, v254, v186\n"
    "v_and_b32 v187, v255, v187\n"
    "v_and_b32 v188, v254, v188\n"
    "v_and_b32 v189, v255, v189\n"
    "v_and_b32 v190, v254, v190\n"
    "v_and_b32_e64 v191, v255, v191\n"
    "ds_read_b64 v[232:233], v253\n"
    "v_mfma_f32_4x4x4_16b_bf16 v[144:147], v[184:185], v[32:33], v[64:67]\n"
    "ds_read_b128 v[112:115], v252 offset:17408\n"
    "v_mfma_f32_4x4x4_16b_bf16 v[148:151], v[186:187], v[34:35], v[68:71]\n"
    "ds_read_b128 v[116:119], v252 offset:17424\n"
    "v_mfma_f32_4x4x4_16b_bf16 v[152:155], v[188:189], v[36:37], v[72:75]\n"
    "ds_read_b128 v[120:123], v252 offset:17536\n"
    "ds_read_b128 v[124:127], v252 offset:17552\n"
    "v_cvt_pk_bf16_f32 v0, v144, v145\n"
    "v_cvt_pk_bf16_f32 v1, v146, v147\n"
    "v_mfma_f32_4x4x4_16b_bf16 v[156:159], v[190:191], v[38:39], v[76:79]\n"
    "v_cvt_pk_bf16_f32 v2, v148, v149\n"
    "v_cvt_pk_bf16_f32 v3, v150, v151\n"
    "v_mfma_f32_4x4x4_16b_bf16 v[144:147], v[184:185], v[40:41], v[80:83]\n"
    "v_cvt_pk_bf16_f32 v4, v152, v153\n"
    "v_cvt_pk_bf16_f32 v5, v154, v155\n"
    "v_mfma_f32_4x4x4_16b_bf16 v[148:151], v[186:187], v[42:43], v[84:87]\n"
    "v_cvt_pk_bf16_f32 v6, v156, v157\n"
    "v_cvt_pk_bf16_f32 v7, v158, v159\n"
    "v_mfma_f32_4x4x4_16b_bf16 v[152:155], v[188:189], v[44:45], v[88:91]\n"
    "v_cvt_pk_bf16_f32 v8, v144, v145\n"
    "v_cvt_pk_bf16_f32 v9, v146, v147\n"
    "v_mfma_f32_4x4x4_16b_bf16 v[156:159], v[190:191], v[46:47], v[92:95]\n"
    "v_cvt_pk_bf16_f32 v10, v148, v149\n"
    "v_cvt_pk_bf16_f32 v11, v150, v151\n"
    "v_mfma_f32_4x4x4_16b_bf16 v[144:147], v[184:185], v[48:49], v[192:195]\n"
    "v_cvt_pk_bf16_f32 v12, v152, v153\n"
    "v_cvt_pk_bf16_f32 v13, v154, v155\n"
    "v_mfma_f32_4x4x4_16b_bf16 v[148:151], v[186:187], v[50:51], v[196:199]\n"
    "v_cvt_pk_bf16_f32 v14, v156, v157\n"
    "v_cvt_pk_bf16_f32 v15, v158, v159\n"
    "v_mfma_f32_4x4x4_16b_bf16 v[152:155], v[188:189], v[52:53], v[200:203]\n"
    "s_waitcnt lgkmcnt(0)\n"
    "v_readfirstlane_b32 s60, v232\n"
    "v_cvt_pk_bf16_f32 v16, v144, v145\n"
    "v_cvt_pk_bf16_f32 v17, v146, v147\n"
    "v_mfma_f32_4x4x4_16b_bf16 v[156:159], v[190:191], v[54:55], v[204:207]\n"
    "s_cmp_eq_u32 s60, -1\n"
    "s_cbranch_scc1 .Ldin_L0_poll\n"
    ".Ldin_L0_pr:\n"
    "v_readfirstlane_b32 s95, v233\n"
    "s_and_b32 s94, s60, 63\n"
    "s_lshl_b32 s94, s94, 6\n"
    "s_add_u32 s94, s94, 68480\n"
    "s_add_u32 s63, s63, 8\n"
    "v_lshl_add_u32 v252, v249, 1, s94\n"
    "ds_read_b32 v252, v252\n"
    "s_sub_u32 s95, s95, s76\n"
    "s_ashr_i32 s95, s95, 4\n"
    "s_min_i32 s95, s95, s74\n"
    "s_max_i32 s72, s95, s75\n"
    "v_cvt_pk_bf16_f32 v18, v148, v149\n"
    "v_cvt_pk_bf16_f32 v19, v150, v151\n"
    "v_mfma_f32_4x4x4_16b_bf16 v[144:147], v[184:185], v[56:57], v[208:211]\n"
    "v_cvt_pk_bf16_f32 v20, v152, v153\n"
    "v_cvt_pk_bf16_f32 v21, v154, v155\n"
    "v_mfma_f32_4x4x4_16b_bf16 v[148:151], v[186:187], v[58:59], v[212:215]\n"
    "v_cvt_pk_bf16_f32 v22, v156, v157\n"
    "v_cvt_pk_bf16_f32 v23, v158, v159\n"
    "v_mfma_f32_4x4x4_16b_bf16 v[152:155], v[188:189], v[60:61], v[216:219]\n"
    "v_cvt_pk_bf16_f32 v24, v144, v145\n"
    "v_cvt_pk_bf16_f32 v25, v146, v147\n"
    "v_mfma_f32_4x4x4_16b_bf16 v[156:159], v[190:191], v[62:63], v[220:223]\n"
    "v_cvt_pk_bf16_f32 v26, v148, v149\n"
    "v_cvt_pk_bf16_f32 v27, v150, v151\n"
    "v_cvt_pk_bf16_f32 v28, v152, v153\n"
    "v_cvt_pk_bf16_f32 v29, v154, v155\n"
    "v_exp_f32 v136, v136 clamp\n"
    "v_cvt_pk_bf16_f32 v30, v156, v157\n"
    "v_cvt_pk_bf16_f32 v31, v158, v159\n"
    "v_mfma_f32_16x16x32_bf16 v[144:147], v[0:3], v[160:163], v[112:115]\n"
    "v_exp_f32 v128, v128 clamp\n"
    "v_mfma_f32_16x16x32_bf16 v[144:147], v[4:7], v[164:167], v[144:147]\n"
    "v_exp_f32 v129, v129 clamp\n"
    "v_mfma_f32_16x16x32_bf16 v[148:151], v[8:11], v[160:163], v[116:119]\n"
    "v_exp_f32 v130, v130 clamp\n"
    "v_mfma_f32_16x16x32_bf16 v[148:151], v[12:15], v[164:167], v[148:151]\n"
    "v_exp_f32 v131, v131 clamp\n"
    "v_mfma_f32_16x16x32_bf16 v[152:155], v[16:19], v[160:163], v[120:123]\n"
    "v_exp_f32 v132, v132 clamp\n"
    "v_mfma_f32_16x16x32_bf16 v[152:155], v[20:23], v[164:167], v[152:155]\n"
    "v_exp_f32 v133, v133 clamp\n"
    "v_mfma_f32_16x16x32_bf16 v[156:159], v[24:27], v[160:163], v[124:127]\n"
    "v_exp_f32 v134, v134 clamp\n"
    "v_mfma_f32_16x16x32_bf16 v[156:159], v[28:31], v[164:167], v[156:159]\n"
    "v_exp_f32 v135, v135 clamp\n"
    "s_add_u32 m0, s68, 2048\n"
    "v_exp_f32 v137, v137 clamp\n"
    "v_add_u32 v253, s67, v244\n"
    "v_mfma_f32_4x4x4_16b_bf16 v[224:227], v[228:229], v[176:177], v[224:227] cbsz:4 abid:0\n"
    "v_exp_f32 v138, v138 clamp\n"
    "v_exp_f32 v139, v139 clamp\n"
    "v_mfma_f32_4x4x4_16b_bf16 v[224:227], v[228:229], v[178:179], v[224:227] cbsz:4 abid:1\n"
    "v_exp_f32 v140, v140 clamp\n"
    "v_exp_f32 v141, v141 clamp\n"
    "v_mfma_f32_4x4x4_16b_bf16 v[224:227], v[228:229], v[180:181], v[224:227] cbsz:4 abid:2\n"
    "v_exp_f32 v142, v142 clamp\n"
    "v_exp_f32 v143, v143 clamp\n"
    "v_mfma_f32_4x4x4_16b_bf16 v[224:227], v[228:229], v[182:183], v[224:227] cbsz:4 abid:3\n"
    "s_waitcnt vmcnt(4)\n"
    "ds_read_b64_tr_b16 v[176:177], v240 offset:2048\n"
    "ds_read_b64_tr_b16 v[178:179], v241 offset:2304\n"
    "ds_read_b64_tr_b16 v[180:181], v242 offset:2560\n"
    "ds_read_b64_tr_b16 v[182:183], v243 offset:2816\n"
    "ds_read_b32 v236, v253 offset:0\n"
    "ds_read_b32 v238, v253 offset:4\n"
    "ds_read_b128 v[160:163], v234 offset:4096\n"
    "ds_read_b128 v[164:167], v235 offset:4096\n"
    "s_waitcnt lgkmcnt(8)\n"
    "s_nop 0\n"
    "v_readlane_b32 s20, v252, 0\n"
    "v_readlane_b32 s21, v252, 1\n"
    "v_readlane_b32 s22, v252, 2\n"
    "v_readlane_b32 s23, v252, 3\n"
    "v_readlane_b32 s24, v252, 4\n"
    "v_readlane_b32 s25, v252, 5\n"
    "v_readlane_b32 s26, v252, 6\n"
    "v_readlane_b32 s27, v252, 7\n"
    "v_pk_fma_f32 v[128:129], v[128:129], s[70:71], v[96:97]\n"
    "v_pk_fma_f32 v[130:131], v[130:131], s[70:71], v[98:99]\n"
    "v_pk_add_f32 v[230:231], v[128:129], v[130:131]\n"
    "v_pk_mul_f32 v[232:233], v[128:129], v[130:131]\n"
    "v_pk_fma_f32 v[132:133], v[132:133], s[70:71], v[100:101]\n"
    "v_pk_fma_f32 v[230:231], v[230:231], v[132:133], v[232:233]\n"
    "v_pk_mul_f32 v[232:233], v[232:233], v[132:133]\n"
    "v_pk_fma_f32 v[134:135], v[134:135], s[70:71], v[102:103]\n"
    "v_pk_fma_f32 v[230:231], v[230:231], v[134:135], v[232:233]\n"
    "v_pk_mul_f32 v[232:233], v[232:233], v[134:135]\n"
    "v_pk_fma_f32 v[136:137], v[136:137], s[70:71], v[104:105]\n"
    "v_pk_fma_f32 v[230:231], v[230:231], v[136:137], v[232:233]\n"
    "v_pk_mul_f32 v[232:233], v[232:233], v[136:137]\n"
    "v_pk_fma_f32 v[138:139], v[138:139], s[70:71], v[106:107]\n"
    "v_pk_fma_f32 v[230:231], v[230:231], v[138:139], v[232:233]\n"
    "v_pk_mul_f32 v[232:233], v[232:233], v[138:139]\n"
    "v_pk_fma_f32 v[140:141], v[140:141], s[70:71], v[108:109]\n"
    "v_pk_fma_f32 v[230:231], v[230:231], v[140:141], v[232:233]\n"
    "v_pk_mul_f32 v[232:233], v[232:233], v[140:141]\n"
    "v_pk_fma_f32 v[142:143], v[142:143], s[70:71], v[110:111]\n"
    "v_pk_fma_f32 v[230:231], v[230:231], v[142:143], v[232:233]\n"
    "v_pk_mul_f32 v[232:233], v[232:233], v[142:143]\n"
    "v_rcp_f32 v232, v232\n"
    "v_rcp_f32 v233, v233\n"
    "v_fma_f32 v230, v230, v232, v247\n"
    "v_fmac_f32 v230, v231, v233\n"
    "v_cvt_pk_bf16_f32 v230, v230, v230\n"
    "s_add_u32 s67, s67, 64\n"
    "s_waitcnt lgkmcnt(0)\n"
    "ds_write_b16 v245, v230\n"
    "ds_read_b64 v[228:229], v246\n"
    "s_lshl1_add_u32 s76, s72, s76\n"
    "buffer_load_dwordx4 v[236:237], s[40:43], 0 idxen offen offset:0 nt lds\n"
    "buffer_load_dwordx4 v[238:239], s[40:43], 0 idxen offen offset:1024 nt lds\n"
    "s_and_b32 s62, s58, 63\n"
    "s_mul_i32 s62, s62, 272\n"
    "s_mov_b32 s58, s59\n"
    "s_mov_b32 s59, s60\n"
    "s_mov_b32 s81, s65\n"
    "s_xor_b32 s65, s65, s64\n"
    "s_mov_b32 s80, s83\n"
    "s_add_u32 s82, s65, 256\n"
    "s_cmp_lt_i32 s59, s57\n"
    "s_cselect_b32 s76, s76, -1\n"
    "s_bitcmp1_b32 s58, 30\n"
    "s_cbranch_scc1 .Ldin_A1s\n"
    ".Ldin_A1:\n"
    "v_mfma_f32_16x16x32_bf16 v[128:131], v[0:3], v[160:163], v[112:115]\n"
    "v_exp_f32 v144, v144 clamp\n"
    "v_mfma_f32_16x16x32_bf16 v[128:131], v[4:7], v[164:167], v[128:131]\n"
    "v_exp_f32 v145, v145 clamp\n"
    "v_mfma_f32_16x16x32_bf16 v[132:135], v[8:11], v[160:163], v[116:119]\n"
    "v_exp_f32 v146, v146 clamp\n"
    "v_mfma_f32_16x16x32_bf16 v[132:135], v[12:15], v[164:167], v[132:135]\n"
    "v_exp_f32 v147, v147 clamp\n"
    "v_mfma_f32_16x16x32_bf16 v[136:139], v[16:19], v[160:163], v[120:123]\n"
    "v_exp_f32 v148, v148 clamp\n"
    "v_mfma_f32_16x16x32_bf16 v[136:139], v[20:23], v[164:167], v[136:139]\n"
    "v_exp_f32 v149, v149 clamp\n"
    "v_mfma_f32_16x16x32_bf16 v[140:143], v[24:27], v[160:163], v[124:127]\n"
    "v_exp_f32 v150, v150 clamp\n"
    "v_mfma_f32_16x16x32_bf16 v[140:143], v[28:31], v[164:167], v[140:143]\n"
    "v_exp_f32 v151, v151 clamp\n"
    "v_exp_f32 v152, v152 clamp\n"
    "s_add_u32 m0, s68, 4096\n"
    "v_exp_f32 v153, v153 clamp\n"
    "v_add_u32_e64 v253, s67, v244\n"
    "s_waitcnt lgkmcnt(0)\n"
    "v_mfma_f32_4x4x4_16b_bf16 v[224:227], v[228:229], v[168:169], v[224:227] cbsz:4 abid:0\n"
    "v_exp_f32 v154, v154 clamp\n"
    "v_exp_f32 v155, v155 clamp\n"
    "v_mfma_f32_4x4x4_16b_bf16 v[224:227], v[228:229], v[170:171], v[224:227] cbsz:4 abid:1\n"
    "v_exp_f32 v156, v156 clamp\n"
    "v_exp_f32 v157, v157 clamp\n"
    "v_mfma_f32_4x4x4_16b_bf16 v[224:227], v[228:229], v[172:173], v[224:227] cbsz:4 abid:2\n"
    "v_exp_f32 v158, v158 clamp\n"
    "v_exp_f32 v159, v159 clamp\n"
    "v_mfma_f32_4x4x4_16b_bf16 v[224:227], v[228:229], v[174:175], v[224:227] cbsz:4 abid:3\n"
    "s_waitcnt vmcnt(4)\n"
    "ds_read_b64_tr_b16 v[168:169], v240 offset:4096\n"
    "ds_read_b64_tr_b16 v[170:171], v241 offset:4352\n"
    "ds_read_b64_tr_b16 v[172:173], v242 offset:4608\n"
    "ds_read_b64_tr_b16 v[174:175], v243 offset:4864\n"
    "ds_read_b32 v236, v253 offset:0\n"
    "ds_read_b32 v238, v253 offset:4\n"
    "ds_read_b128 v[160:163], v234 offset:6144\n"
    "ds_read_b128 v[164:167], v235 offset:6144\n"
    "s_cbranch_vccz .Ldin_A1_np\n"
    "s_setprio 1\n"
    ".Ldin_A1_np:\n"
    "v_pk_fma_f32 v[144:145], v[144:145], s[70:71], v[96:97]\n"
    "v_pk_fma_f32 v[146:147], v[146:147], s[70:71], v[98:99]\n"
    "v_add_f32 v191, v224, v225\n"
    "v_add_f32 v190, v226, v227\n"
    "v_add_f32 v191, v191, v190\n"
    "v_add_u32 v190, s62, v249\n"
    "v_mul_f32 v191, s69, v191\n"
    "v_cvt_pk_bf16_f32 v191, v191, v191\n"
    "ds_write_b16 v190, v191 offset:32768\n"
    "s_bfe_u32 s94, s59, 0x180006\n"
    "s_mul_i32 s86, s94, s56\n"
    "v_mbcnt_lo_u32_b32 v184, s20, 0\n"
    "v_mbcnt_lo_u32_b32 v185, s22, 0\n"
    "v_mbcnt_lo_u32_b32 v186, s24, 0\n"
    "v_mbcnt_lo_u32_b32 v187, s26, 0\n"
    "s_bcnt1_i32_b64 s90, s[20:21]\n"
    "s_bcnt1_i32_b64 s91, s[22:23]\n"
    "s_bcnt1_i32_b64 s92, s[24:25]\n"
    "s_bcnt1_i32_b64 s93, s[26:27]\n"
    "v_mbcnt_hi_u32_b32 v184, s21, v184\n"
    "v_mbcnt_hi_u32_b32 v185, s23, v185\n"
    "v_mbcnt_hi_u32_b32 v186, s25, v186\n"
    "v_mbcnt_hi_u32_b32 v187, s27, v187\n"
    "s_lshl2_add_u32 s90, s90, s65\n"
    "s_lshl2_add_u32 s91, s91, s90\n"
    "s_lshl2_add_u32 s92, s92, s91\n"
    "s_lshl2_add_u32 s93, s93, s92\n"
    "v_add_u32_e64 v188, s86, v251\n"
    "v_pk_add_f32 v[230:231], v[144:145], v[146:147]\n"
    "v_pk_mul_f32 v[232:233], v[144:145], v[146:147]\n"
    "v_pk_fma_f32 v[148:149], v[148:149], s[70:71], v[100:101]\n"
    "v_pk_fma_f32 v[230:231], v[230:231], v[148:149], v[232:233]\n"
    "v_pk_mul_f32 v[232:233], v[232:233], v[148:149]\n"
    "v_lshl_add_u32 v184, v184, 2, s65\n"
    "v_lshl_add_u32 v185, v185, 2, s90\n"
    "v_lshl_add_u32 v186, v186, 2, s91\n"
    "v_lshl_add_u32 v187, v187, 2, s92\n"
    "v_mov_b32 v191, -1\n"
    "v_lshl_add_u32 v190, v249, 1, s93\n"
    "ds_write2_b32 v190, v191, v191 offset1:64\n"
    "s_mov_b64 exec, s[20:21]\n"
    "ds_write_b32 v184, v188\n"
    "s_mov_b64 exec, s[22:23]\n"
    "v_add_u32 v189, 64, v188\n"
    "ds_write_b32 v185, v189\n"
    "s_mov_b64 exec, s[24:25]\n"
    "v_add_u32 v190, 128, v188\n"
    "ds_write_b32 v186, v190\n"
    "s_mov_b64 exec, s[26:27]\n"
    "v_add_u32 v189, 192, v188\n"
    "ds_write_b32 v187, v189\n"
    "s_mov_b64 exec, -1\n"
    "s_add_u32 s95, s93, 60\n"
    "s_add_u32 s94, s82, 128\n"
    "s_andn2_b32 s95, s95, 63\n"
    "s_max_u32 s83, s95, s94\n"
    "v_pk_fma_f32 v[150:151], v[150:151], s[70:71], v[102:103]\n"
    "v_pk_fma_f32 v[230:231], v[230:231], v[150:151], v[232:233]\n"
    "v_pk_mul_f32 v[232:233], v[232:233], v[150:151]\n"
    "v_pk_fma_f32 v[152:153], v[152:153], s[70:71], v[104:105]\n"
    "v_pk_fma_f32 v[230:231], v[230:231], v[152:153], v[232:233]\n"
    "v_pk_mul_f32 v[232:233], v[232:233], v[152:153]\n"
    "v_pk_fma_f32 v[154:155], v[154:155], s[70:71], v[106:107]\n"
    "v_pk_fma_f32 v[230:231], v[230:231], v[154:155], v[232:233]\n"
    "v_pk_mul_f32 v[232:233], v[232:233], v[154:155]\n"
    "v_pk_fma_f32 v[156:157], v[156:157], s[70:71], v[108:109]\n"
    "v_pk_fma_f32 v[230:231], v[230:231], v[156:157], v[232:233]\n"
    "v_pk_mul_f32 v[232:233], v[232:233], v[156:157]\n"
    "v_pk_fma_f32 v[158:159], v[158:159], s[70:71], v[110:111]\n"
    "v_pk_fma_f32 v[230:231], v[230:231], v[158:159], v[232:233]\n"
    "v_pk_mul_f32 v[232:233], v[232:233], v[158:159]\n"
    "v_rcp_f32 v232, v232\n"
    "v_rcp_f32 v233, v233\n"
    "v_fma_f32 v230, v230, v232, v247\n"
    "v_fmac_f32 v230, v231, v233\n"
    "v_cvt_pk_bf16_f32 v230, v230, v230\n"
    "s_add_u32 s67, s67, 64\n"
    "s_waitcnt lgkmcnt(0)\n"
    "ds_write_b16 v245, v230\n"
    "ds_read_b64 v[228:229], v246\n"
    "buffer_load_dwordx4 v[236:237], s[40:43], 0 idxen offen offset:0 nt lds\n"
    "buffer_load_dwordx4 v[238:239], s[40:43], 0 idxen offen offset:1024 nt lds\n"
    ".Ldin_B2:\n"
    "v_mfma_f32_16x16x32_bf16 v[144:147], v[0:3], v[160:163], v[112:115]\n"
    "v_exp_f32 v128, v128 clamp\n"
    "v_mfma_f32_16x16x32_bf16 v[144:147], v[4:7], v[164:167], v[144:147]\n"
    "v_exp_f32 v129, v129 clamp\n"
    "v_mfma_f32_16x16x32_bf16 v[148:151], v[8:11], v[160:163], v[116:119]\n"
    "v_exp_f32 v130, v130 clamp\n"
    "v_mfma_f32_16x16x32_bf16 v[148:151], v[12:15], v[164:167], v[148:151]\n"
    "v_exp_f32 v131, v131 clamp\n"
    "v_mfma_f32_16x16x32_bf16 v[152:155], v[16:19], v[160:163], v[120:123]\n"
    "v_exp_f32 v132, v132 clamp\n"
    "v_mfma_f32_16x16x32_bf16 v[152:155], v[20:23], v[164:167], v[152:155]\n"
    "v_exp_f32 v133, v133 clamp\n"
    "v_mfma_f32_16x16x32_bf16 v[156:159], v[24:27], v[160:163], v[124:127]\n"
    "v_exp_f32 v134, v134 clamp\n"
    "v_mfma_f32_16x16x32_bf16 v[156:159], v[28:31], v[164:167], v[156:159]\n"
    "v_exp_f32 v135, v135 clamp\n"
    "v_exp_f32 v136, v136 clamp\n"
    "s_cmp_eq_u32 s67, s80\n"
    "s_cselect_b32 s67, s65, s67\n"
    "s_and_b32 s94, s59, 63\n"
    "s_mul_i32 s94, s94, 272\n"
    "v_add_u32 v252, s94, v250\n"
    "s_add_u32 m0, s68, 6144\n"
    "v_exp_f32 v137, v137 clamp\n"
    "v_add_u32 v253, s67, v244\n"
    "s_waitcnt lgkmcnt(0)\n"
    "v_mfma_f32_4x4x4_16b_bf16 v[224:227], v[228:229], v[176:177], 0 cbsz:4 abid:0\n"
    "v_exp_f32 v138, v138 clamp\n"
    "v_exp_f32 v139, v139 clamp\n"
    "v_mfma_f32_4x4x4_16b_bf16 v[224:227], v[228:229], v[178:179], v[224:227] cbsz:4 abid:1\n"
    "v_exp_f32 v140, v140 clamp\n"
    "v_exp_f32 v141, v141 clamp\n"
    "v_mfma_f32_4x4x4_16b_bf16 v[224:227], v[228:229], v[180:181], v[224:227] cbsz:4 abid:2\n"
    "v_exp_f32 v142, v142 clamp\n"
    "v_exp_f32 v143, v143 clamp\n"
    "v_mfma_f32_4x4x4_16b_bf16 v[224:227], v[228:229], v[182:183], v[224:227] cbsz:4 abid:3\n"
    "s_waitcnt vmcnt(4)\n"
    "s_memrealtime s[34:35]\n"
    "ds_read_b64_tr_b16 v[176:177], v240 offset:6144\n"
    "ds_read_b64_tr_b16 v[178:179], v241 offset:6400\n"
    "ds_read_b64_tr_b16 v[180:181], v242 offset:6656\n"
    "ds_read_b64_tr_b16 v[182:183], v243 offset:6912\n"
    "ds_read_b32 v236, v253 offset:0\n"
    "ds_read_b32 v238, v253 offset:4\n"
    "ds_read_b128 v[160:163], v234 offset:0\n"
    "ds_read_b128 v[164:167], v235 offset:0\n"
    "ds_read_b128 v[184:187], v252 offset:0\n"
    "ds_read_b128 v[188:191], v252 offset:16\n"
    "v_pk_fma_f32 v[128:129], v[128:129], s[70:71], v[96:97]\n"
    "v_pk_fma_f32 v[130:131], v[130:131], s[70:71], v[98:99]\n"
    "v_pk_add_f32 v[230:231], v[128:129], v[130:131]\n"
    "v_pk_mul_f32 v[232:233], v[128:129], v[130:131]\n"
    "v_pk_fma_f32 v[132:133], v[132:133], s[70:71], v[100:101]\n"
    "v_pk_fma_f32 v[230:231], v[230:231], v[132:133], v[232:233]\n"
    "v_pk_mul_f32 v[232:233], v[232:233], v[132:133]\n"
    "v_pk_fma_f32 v[134:135], v[134:135], s[70:71], v[102:103]\n"
    "v_pk_fma_f32 v[230:231], v[230:231], v[134:135], v[232:233]\n"
    "v_pk_mul_f32 v[232:233], v[232:233], v[134:135]\n"
    "v_pk_fma_f32 v[136:137], v[136:137], s[70:71], v[104:105]\n"
    "v_pk_fma_f32 v[230:231], v[230:231], v[136:137], v[232:233]\n"
    "v_pk_mul_f32 v[232:233], v[232:233], v[136:137]\n"
    "v_pk_fma_f32 v[138:139], v[138:139], s[70:71], v[106:107]\n"
    "v_pk_fma_f32 v[230:231], v[230:231], v[138:139], v[232:233]\n"
    "v_pk_mul_f32 v[232:233], v[232:233], v[138:139]\n"
    "v_pk_fma_f32 v[140:141], v[140:141], s[70:71], v[108:109]\n"
    "v_pk_fma_f32 v[230:231], v[230:231], v[140:141], v[232:233]\n"
    "v_pk_mul_f32 v[232:233], v[232:233], v[140:141]\n"
    "v_pk_fma_f32 v[142:143], v[142:143], s[70:71], v[110:111]\n"
    "v_pk_fma_f32 v[230:231], v[230:231], v[142:143], v[232:233]\n"
    "v_pk_mul_f32 v[232:233], v[232:233], v[142:143]\n"
    "v_rcp_f32 v232, v232\n"
    "v_rcp_f32 v233, v233\n"
    "v_fma_f32 v230, v230, v232, v247\n"
    "v_fmac_f32 v230, v231, v233\n"
    "v_cvt_pk_bf16_f32 v230, v230, v230\n"
    "s_add_u32 s67, s67, 64\n"
    "s_waitcnt lgkmcnt(0)\n"
    "ds_write_b16 v245, v230\n"
    "ds_read_b64 v[228:229], v246\n"
    "s_cmp_le_u32 s34, s76\n"
    "s_cbranch_scc1 .Ldin_B2_wait\n"
    ".Ldin_B2_go:\n"
    "s_sub_u32 s94, s34, 60\n"
    "s_max_u32 s76, s76, s94\n"
    "s_add_u32 s76, s76, s72\n"
    ".Ldin_B2_dma:\n"
    "buffer_load_dwordx4 v[236:237], s[40:43], 0 idxen offen offset:0 nt lds\n"
    "buffer_load_dwordx4 v[238:239], s[40:43], 0 idxen offen offset:1024 nt lds\n"
    ".Ldin_B2_nd:\n"
    "s_branch .Ldin_G3\n"
    ".Ldin_L0_poll:\n"
    "s_sleep 1\n"
    "ds_read_b64 v[232:233], v253\n"
    "s_waitcnt lgkmcnt(0)\n"
    "v_readfirstlane_b32 s60, v232\n"
    "s_cmp_eq_u32 s60, -1\n"
    "s_cbranch_scc1 .Ldin_L0_poll\n"
    "s_branch .Ldin_L0_pr\n"
    ".Ldin_B2_wait:\n"
    "s_cmp_lt_i32 s59, s57\n"
    "s_cbranch_scc1 .Ldin_B2_w2\n"
    "s_sub_u32 s94, s67, s65\n"
    "s_cmp_gt_u32 s94, 256\n"
    "s_cbranch_scc1 .Ldin_B2_dma\n"
    "s_cmp_eq_u32 s94, 64\n"
    "s_waitcnt vmcnt(2)\n"
    "s_cbranch_scc1 .Ldin_B2_nd\n"
    "s_waitcnt vmcnt(0)\n"
    "s_branch .Ldin_B2_nd\n"
    ".Ldin_B2_slp:\n"
    "s_sleep 1\n"
    ".Ldin_B2_w2:\n"
    "s_memrealtime s[34:35]\n"
    "s_waitcnt lgkmcnt(0)\n"
    "s_sub_u32 s94, s34, s76\n"
    "s_cmp_lt_i32 s94, 0\n"
    "s_cbranch_scc1 .Ldin_B2_slp\n"
    "s_branch .Ldin_B2_go\n"
    ".p2align 5\n"
    ".Ldin_L3:\n"
    "s_setprio 0\n"
    "s_cmp_ge_i32 s59, s57\n"
    "s_cbranch_scc1 .Ldin_E1\n"
    "s_add_u32 s76, s76, s73\n"
    "v_mov_b32 v253, s63\n"
    "v_and_b32 v184, v254, v184\n"
    "v_and_b32 v185, v255, v185\n"
    "v_and_b32 v186, v254, v186\n"
    "v_and_b32 v187, v255, v187\n"
    "v_and_b32 v188, v254, v188\n"
    "v_and_b32 v189, v255, v189\n"
    "v_and_b32 v190, v254, v190\n"
    "v_and_b32_e64 v191, v255, v191\n"
    "ds_read_b64 v[232:233], v253\n"
    "v_mfma_f32_4x4x4_16b_bf16 v[128:131], v[184:185], v[32:33], v[64:67]\n"
    "ds_read_b128 v[112:115], v252 offset:17408\n"
    "v_mfma_f32_4x4x4_16b_bf16 v[132:135], v[186:187], v[34:35], v[68:71]\n"
    "ds_read_b128 v[116:119], v252 offset:17424\n"
    "v_mfma_f32_4x4x4_16b_bf16 v[136:139], v[188:189], v[36:37], v[72:75]\n"
    "ds_read_b128 v[120:123], v252 offset:17536\n"
    "ds_read_b128 v[124:127], v252 offset:17552\n"
    "v_cvt_pk_bf16_f32 v0, v128, v129\n"
    "v_cvt_pk_bf16_f32 v1, v130, v131\n"
    "v_mfma_f32_4x4x4_16b_bf16 v[140:143], v[190:191], v[38:39], v[76:79]\n"
    "v_cvt_pk_bf16_f32 v2, v132, v133\n"
    "v_cvt_pk_bf16_f32 v3, v134, v135\n"
    "v_mfma_f32_4x4x4_16b_bf16 v[128:131], v[184:185], v[40:41], v[80:83]\n"
    "v_cvt_pk_bf16_f32 v4, v136, v137\n"
    "v_cvt_pk_bf16_f32 v5, v138, v139\n"
    "v_mfma_f32_4x4x4_16b_bf16 v[132:135], v[186:187], v[42:43], v[84:87]\n"
    "v_cvt_pk_bf16_f32 v6, v140, v141\n"
    "v_cvt_pk_bf16_f32 v7, v142, v143\n"
    "v_mfma_f32_4x4x4_16b_bf16 v[136:139], v[188:189], v[44:45], v[88:91]\n"
    "v_cvt_pk_bf16_f32 v8, v128, v129\n"
    "v_cvt_pk_bf16_f32 v9, v130, v131\n"
    "v_mfma_f32_4x4x4_16b_bf16 v[140:143], v[190:191], v[46:47], v[92:95]\n"
    "v_cvt_pk_bf16_f32 v10, v132, v133\n"
    "v_cvt_pk_bf16_f32 v11, v134, v135\n"
    "v_mfma_f32_4x4x4_16b_bf16 v[128:131], v[184:185], v[48:49], v[192:195]\n"
    "v_cvt_pk_bf16_f32 v12, v136, v137\n"
    "v_cvt_pk_bf16_f32 v13, v138, v139\n"
    "v_mfma_f32_4x4x4_16b_bf16 v[132:135], v[186:187], v[50:51], v[196:199]\n"
    "v_cvt_pk_bf16_f32 v14, v140, v141\n"
    "v_cvt_pk_bf16_f32 v15, v142, v143\n"
    "v_mfma_f32_4x4x4_16b_bf16 v[136:139], v[188:189], v[52:53], v[200:203]\n"
    "s_waitcnt lgkmcnt(0)\n"
    "v_readfirstlane_b32 s60, v232\n"
    "v_cvt_pk_bf16_f32 v16, v128, v129\n"
    "v_cvt_pk_bf16_f32 v17, v130, v131\n"
    "v_mfma_f32_4x4x4_16b_bf16 v[140:143], v[190:191], v[54:55], v[204:207]\n"
    "s_cmp_eq_u32 s60, -1\n"
    "s_cbranch_scc1 .Ldin_L3_poll\n"
    ".Ldin_L3_pr:\n"
    "v_readfirstlane_b32 s95, v233\n"
    "s_and_b32 s94, s60, 63\n"
    "s_lshl_b32 s94, s94, 6\n"
    "s_add_u32 s94, s94, 68480\n"
    "s_add_u32 s63, s63, 8\n"
    "v_lshl_add_u32 v252, v249, 1, s94\n"
    "ds_read_b32 v252, v252\n"
    "s_sub_u32 s95, s95, s76\n"
    "s_ashr_i32 s95, s95, 4\n"
    "s_min_i32 s95, s95, s74\n"
    "s_max_i32 s72, s95, s75\n"
    "v_cvt_pk_bf16_f32 v18, v132, v133\n"
    "v_cvt_pk_bf16_f32 v19, v134, v135\n"
    "v_mfma_f32_4x4x4_16b_bf16 v[128:131], v[184:185], v[56:57], v[208:211]\n"
    "v_cvt_pk_bf16_f32 v20, v136, v137\n"
    "v_cvt_pk_bf16_f32 v21, v138, v139\n"
    "v_mfma_f32_4x4x4_16b_bf16 v[132:135], v[186:187], v[58:59], v[212:215]\n"
    "v_cvt_pk_bf16_f32 v22, v140, v141\n"
    "v_cvt_pk_bf16_f32 v23, v142, v143\n"
    "v_mfma_f32_4x4x4_16b_bf16 v[136:139], v[188:189], v[60:61], v[216:219]\n"
    "v_cvt_pk_bf16_f32 v24, v128, v129\n"
    "v_cvt_pk_bf16_f32 v25, v130, v131\n"
    "v_mfma_f32_4x4x4_16b_bf16 v[140:143], v[190:191], v[62:63], v[220:223]\n"
    "v_cvt_pk_bf16_f32 v26, v132, v133\n"
    "v_cvt_pk_bf16_f32 v27, v134, v135\n"
    "v_cvt_pk_bf16_f32 v28, v136, v137\n"
    "v_cvt_pk_bf16_f32 v29, v138, v139\n"
    "v_exp_f32 v152, v152 clamp\n"
    "v_cvt_pk_bf16_f32 v30, v140, v141\n"
    "v_cvt_pk_bf16_f32 v31, v142, v143\n"
    "v_mfma_f32_16x16x32_bf16 v[128:131], v[0:3], v[160:163], v[112:115]\n"
    "v_exp_f32 v144, v144 clamp\n"
    "v_mfma_f32_16x16x32_bf16 v[128:131], v[4:7], v[164:167], v[128:131]\n"
    "v_exp_f32 v145, v145 clamp\n"
    "v_mfma_f32_16x16x32_bf16 v[132:135], v[8:11], v[160:163], v[116:119]\n"
    "v_exp_f32 v146, v146 clamp\n"
    "v_mfma_f32_16x16x32_bf16 v[132:135], v[12:15], v[164:167], v[132:135]\n"
    "v_exp_f32 v147, v147 clamp\n"
    "v_mfma_f32_16x16x32_bf16 v[136:139], v[16:19], v[160:163], v[120:123]\n"
    "v_exp_f32 v148, v148 clamp\n"
    "v_mfma_f32_16x16x32_bf16 v[136:139], v[20:23], v[164:167], v[136:139]\n"
    "v_exp_f32 v149, v149 clamp\n"
    "v_mfma_f32_16x16x32_bf16 v[140:143], v[24:27], v[160:163], v[124:127]\n"
    "v_exp_f32 v150, v150 clamp\n"
    "v_mfma_f32_16x16x32_bf16 v[140:143], v[28:31], v[164:167], v[140:143]\n"
    "v_exp_f32 v151, v151 clamp\n"
    "s_add_u32 m0, s68, 0\n"
    "v_exp_f32 v153, v153 clamp\n"
    "v_add_u32_e64 v253, s67, v244\n"
    "v_mfma_f32_4x4x4_16b_bf16 v[224:227], v[228:229], v[168:169], v[224:227] cbsz:4 abid:0\n"
    "v_exp_f32 v154, v154 clamp\n"
    "v_exp_f32 v155, v155 clamp\n"
    "v_mfma_f32_4x4x4_16b_bf16 v[224:227], v[228:229], v[170:171], v[224:227] cbsz:4 abid:1\n"
    "v_exp_f32 v156, v156 clamp\n"
    "v_exp_f32 v157, v157 clamp\n"
    "v_mfma_f32_4x4x4_16b_bf16 v[224:227], v[228:229], v[172:173], v[224:227] cbsz:4 abid:2\n"
    "v_exp_f32 v158, v158 clamp\n"
    "v_exp_f32 v159, v159 clamp\n"
    "v_mfma_f32_4x4x4_16b_bf16 v[224:227], v[228:229], v[174:175], v[224:227] cbsz:4 abid:3\n"
    "s_waitcnt vmcnt(4)\n"
    "ds_read_b64_tr_b16 v[168:169], v240 offset:0\n"
    "ds_read_b64_tr_b16 v[170:171], v241 offset:256\n"
    "ds_read_b64_tr_b16 v[172:173], v242 offset:512\n"
    "ds_read_b64_tr_b16 v[174:175], v243 offset:768\n"
    "ds_read_b32 v236, v253 offset:0\n"
    "ds_read_b32 v238, v253 offset:4\n"
    "ds_read_b128 v[160:163], v234 offset:2048\n"
    "ds_read_b128 v[164:167], v235 offset:2048\n"
    "s_waitcnt lgkmcnt(8)\n"
    "s_nop 0\n"
    "v_readlane_b32 s20, v252, 0\n"
    "v_readlane_b32 s21, v252, 1\n"
    "v_readlane_b32 s22, v252, 2\n"
    "v_readlane_b32 s23, v252, 3\n"
    "v_readlane_b32 s24, v252, 4\n"
    "v_readlane_b32 s25, v252, 5\n"
    "v_readlane_b32 s26, v252, 6\n"
    "v_readlane_b32 s27, v252, 7\n"
    "v_pk_fma_f32 v[144:145], v[144:145], s[70:71], v[96:97]\n"
    "v_pk_fma_f32 v[146:147], v[146:147], s[70:71], v[98:99]\n"
    "v_pk_add_f32 v[230:231], v[144:145], v[146:147]\n"
    "v_pk_mul_f32 v[232:233], v[144:145], v[146:147]\n"
    "v_pk_fma_f32 v[148:149], v[148:149], s[70:71], v[100:101]\n"
    "v_pk_fma_f32 v[230:231], v[230:231], v[148:149], v[232:233]\n"
    "v_pk_mul_f32 v[232:233], v[232:233], v[148:149]\n"
    "v_pk_fma_f32 v[150:151], v[150:151], s[70:71], v[102:103]\n"
    "v_pk_fma_f32 v[230:231], v[230:231], v[150:151], v[232:233]\n"
    "v_pk_mul_f32 v[232:233], v[232:233], v[150:151]\n"
    "v_pk_fma_f32 v[152:153], v[152:153], s[70:71], v[104:105]\n"
    "v_pk_fma_f32 v[230:231], v[230:231], v[152:153], v[232:233]\n"
    "v_pk_mul_f32 v[232:233], v[232:233], v[152:153]\n"
    "v_pk_fma_f32 v[154:155], v[154:155], s[70:71], v[106:107]\n"
    "v_pk_fma_f32 v[230:231], v[230:231], v[154:155], v[232:233]\n"
    "v_pk_mul_f32 v[232:233], v[232:233], v[154:155]\n"
    "v_pk_fma_f32 v[156:157], v[156:157], s[70:71], v[108:109]\n"
    "v_pk_fma_f32 v[230:231], v[230:231], v[156:157], v[232:233]\n"
    "v_pk_mul_f32 v[232:233], v[232:233], v[156:157]\n"
    "v_pk_fma_f32 v[158:159], v[158:159], s[70:71], v[110:111]\n"
    "v_pk_fma_f32 v[230:231], v[230:231], v[158:159], v[232:233]\n"
    "v_pk_mul_f32 v[232:233], v[232:233], v[158:159]\n"
    "v_rcp_f32 v232, v232\n"
    "v_rcp_f32 v233, v233\n"
    "v_fma_f32 v230, v230, v232, v247\n"
    "v_fmac_f32 v230, v231, v233\n"
    "v_cvt_pk_bf16_f32 v230, v230, v230\n"
    "s_add_u32 s67, s67, 64\n"
    "s_waitcnt lgkmcnt(0)\n"
    "ds_write_b16 v245, v230\n"
    "ds_read_b64 v[228:229], v246\n"
    "s_lshl1_add_u32 s76, s72, s76\n"
    "buffer_load_dwordx4 v[236:237], s[40:43], 0 idxen offen offset:0 nt lds\n"
    "buffer_load_dwordx4 v[238:239], s[40:43], 0 idxen offen offset:1024 nt lds\n"
    "s_and_b32 s62, s58, 63\n"
    "s_mul_i32 s62, s62, 272\n"
    "s_mov_b32 s58, s59\n"
    "s_mov_b32 s59, s60\n"
    "s_mov_b32 s81, s65\n"
    "s_xor_b32 s65, s65, s64\n"
    "s_mov_b32 s80, s83\n"
    "s_add_u32 s82, s65, 256\n"
    "s_cmp_lt_i32 s59, s57\n"
    "s_cselect_b32 s76, s76, -1\n"
    "s_bitcmp1_b32 s58, 30\n"
    "s_cbranch_scc1 .Ldin_A0s\n"
    "s_branch .Ldin_A0\n"
    ".Ldin_L3_poll:\n"
    "s_sleep 1\n"
    "ds_read_b64 v[232:233], v253\n"
    "s_waitcnt lgkmcnt(0)\n"
    "v_readfirstlane_b32 s60, v232\n"
    "s_cmp_eq_u32 s60, -1\n"
    "s_cbranch_scc1 .Ldin_L3_poll\n"
    "s_branch .Ldin_L3_pr\n"
    ".p2align 5\n"
    ".Ldin_E0:\n"
    "v_exp_f32 v128, v128 clamp\n"
    "v_exp_f32 v129, v129 clamp\n"
    "v_exp_f32 v130, v130 clamp\n"
    "v_exp_f32 v131, v131 clamp\n"
    "v_exp_f32 v132, v132 clamp\n"
    "v_exp_f32 v133, v133 clamp\n"
    "v_exp_f32 v134, v134 clamp\n"
    "v_exp_f32 v135, v135 clamp\n"
    "s_and_b32 s62, s58, 63\n"
    "s_mul_i32 s62, s62, 272\n"
    "s_waitcnt lgkmcnt(0)\n"
    "v_mfma_f32_4x4x4_16b_bf16 v[224:227], v[228:229], v[176:177], v[224:227] cbsz:4 abid:0\n"
    "v_exp_f32 v136, v136 clamp\n"
    "v_exp_f32 v137, v137 clamp\n"
    "v_mfma_f32_4x4x4_16b_bf16 v[224:227], v[228:229], v[178:179], v[224:227] cbsz:4 abid:1\n"
    "v_exp_f32 v138, v138 clamp\n"
    "v_exp_f32 v139, v139 clamp\n"
    "v_mfma_f32_4x4x4_16b_bf16 v[224:227], v[228:229], v[180:181], v[224:227] cbsz:4 abid:2\n"
    "v_exp_f32 v140, v140 clamp\n"
    "v_exp_f32 v141, v141 clamp\n"
    "v_mfma_f32_4x4x4_16b_bf16 v[224:227], v[228:229], v[182:183], v[224:227] cbsz:4 abid:3\n"
    "v_exp_f32 v142, v142 clamp\n"
    "v_exp_f32 v143, v143 clamp\n"
    "v_pk_fma_f32 v[128:129], v[128:129], s[70:71], v[96:97]\n"
    "v_pk_fma_f32 v[130:131], v[130:131], s[70:71], v[98:99]\n"
    "v_pk_add_f32 v[230:231], v[128:129], v[130:131]\n"
    "v_pk_mul_f32 v[232:233], v[128:129], v[130:131]\n"
    "v_pk_fma_f32 v[132:133], v[132:133], s[70:71], v[100:101]\n"
    "v_pk_fma_f32 v[230:231], v[230:231], v[132:133], v[232:233]\n"
    "v_pk_mul_f32 v[232:233], v[232:233], v[132:133]\n"
    "v_pk_fma_f32 v[134:135], v[134:135], s[70:71], v[102:103]\n"
    "v_pk_fma_f32 v[230:231], v[230:231], v[134:135], v[232:233]\n"
    "v_pk_mul_f32 v[232:233], v[232:233], v[134:135]\n"
    "v_pk_fma_f32 v[136:137], v[136:137], s[70:71], v[104:105]\n"
    "v_pk_fma_f32 v[230:231], v[230:231], v[136:137], v[232:233]\n"
    "v_pk_mul_f32 v[232:233], v[232:233], v[136:137]\n"
    "v_pk_fma_f32 v[138:139], v[138:139], s[70:71], v[106:107]\n"
    "v_pk_fma_f32 v[230:231], v[230:231], v[138:139], v[232:233]\n"
    "v_pk_mul_f32 v[232:233], v[232:233], v[138:139]\n"
    "v_pk_fma_f32 v[140:141], v[140:141], s[70:71], v[108:109]\n"
    "v_pk_fma_f32 v[230:231], v[230:231], v[140:141], v[232:233]\n"
    "v_pk_mul_f32 v[232:233], v[232:233], v[140:141]\n"
    "v_pk_fma_f32 v[142:143], v[142:143], s[70:71], v[110:111]\n"
    "v_pk_fma_f32 v[230:231], v[230:231], v[142:143], v[232:233]\n"
    "v_pk_mul_f32 v[232:233], v[232:233], v[142:143]\n"
    "v_rcp_f32 v232, v232\n"
    "v_rcp_f32 v233, v233\n"
    "v_fma_f32 v230, v230, v232, v247\n"
    "v_fmac_f32 v230, v231, v233\n"
    "v_cvt_pk_bf16_f32 v230, v230, v230\n"
    "ds_write_b16 v245, v230\n"
    "ds_read_b64 v[228:229], v246\n"
    ".Ldin_drain0:\n"
    "v_add_u32 v253, s66, v244\n"
    "ds_read_b32 v253, v253\n"
    "v_lshlrev_b32 v202, 3, v249\n"
    "s_waitcnt lgkmcnt(0)\n"
    "v_mfma_f32_4x4x4_16b_bf16 v[224:227], v[228:229], v[168:169], v[224:227] cbsz:4 abid:0\n"
    "v_and_b32_e64 v201, 63, v253\n"
    "v_and_b32 v202, 0x70, v202\n"
    "v_mfma_f32_4x4x4_16b_bf16 v[224:227], v[228:229], v[170:171], v[224:227] cbsz:4 abid:1\n"
    "v_and_b32 v200, 0x3fffffc0, v253\n"
    "v_mul_u32_u24 v201, 272, v201\n"
    "v_mfma_f32_4x4x4_16b_bf16 v[224:227], v[228:229], v[172:173], v[224:227] cbsz:4 abid:2\n"
    "v_lshl_add_u32 v200, v200, 1, v202\n"
    "v_add_u32 v201, v201, v202\n"
    "v_mfma_f32_4x4x4_16b_bf16 v[224:227], v[228:229], v[174:175], v[224:227] cbsz:4 abid:3\n"
    "v_cmp_gt_u32 vcc, s57, v253\n"
    "v_bfrev_b32 v202, 1\n"
    "s_nop 0\n"
    "v_cndmask_b32 v200, v202, v200, vcc\n"
    "s_nop 0\n"
    "v_add_f32 v232, v224, v225\n"
    "v_add_f32 v233, v226, v227\n"
    "v_add_f32 v232, v232, v233\n"
    "v_add_u32 v233, s62, v249\n"
    "v_mul_f32 v232, s69, v232\n"
    "v_cvt_pk_bf16_f32 v232, v232, v232\n"
    "ds_write_b16 v233, v232 offset:32768\n"
    "ds_read_b128 v[216:219], v201 offset:32768\n"
    "s_waitcnt vmcnt(0) lgkmcnt(0)\n"
    "buffer_store_dwordx4 v[216:219], v200, s[52:55], 0 offen sc0 sc1 nt\n"
    "s_branch .Ldin_exit\n"
    ".p2align 5\n"
    ".Ldin_E1:\n"
    "v_exp_f32 v144, v144 clamp\n"
    "v_exp_f32 v145, v145 clamp\n"
    "v_exp_f32 v146, v146 clamp\n"
    "v_exp_f32 v147, v147 clamp\n"
    "v_exp_f32 v148, v148 clamp\n"
    "v_exp_f32 v149, v149 clamp\n"
    "v_exp_f32 v150, v150 clamp\n"
    "v_exp_f32 v151, v151 clamp\n"
    "s_and_b32 s62, s58, 63\n"
    "s_mul_i32 s62, s62, 272\n"
    "s_waitcnt lgkmcnt(0)\n"
    "v_mfma_f32_4x4x4_16b_bf16 v[224:227], v[228:229], v[168:169], v[224:227] cbsz:4 abid:0\n"
    "v_exp_f32 v152, v152 clamp\n"
    "v_exp_f32 v153, v153 clamp\n"
    "v_mfma_f32_4x4x4_16b_bf16 v[224:227], v[228:229], v[170:171], v[224:227] cbsz:4 abid:1\n"
    "v_exp_f32 v154, v154 clamp\n"
    "v_exp_f32 v155, v155 clamp\n"
    "v_mfma_f32_4x4x4_16b_bf16 v[224:227], v[228:229], v[172:173], v[224:227] cbsz:4 abid:2\n"
    "v_exp_f32 v156, v156 clamp\n"
    "v_exp_f32 v157, v157 clamp\n"
    "v_mfma_f32_4x4x4_16b_bf16 v[224:227], v[228:229], v[174:175], v[224:227] cbsz:4 abid:3\n"
    "v_exp_f32 v158, v158 clamp\n"
    "v_exp_f32 v159, v159 clamp\n"
    "v_pk_fma_f32 v[144:145], v[144:145], s[70:71], v[96:97]\n"
    "v_pk_fma_f32 v[146:147], v[146:147], s[70:71], v[98:99]\n"
    "v_pk_add_f32 v[230:231], v[144:145], v[146:147]\n"
    "v_pk_mul_f32 v[232:233], v[144:145], v[146:147]\n"
    "v_pk_fma_f32 v[148:149], v[148:149], s[70:71], v[100:101]\n"
    "v_pk_fma_f32 v[230:231], v[230:231], v[148:149], v[232:233]\n"
    "v_pk_mul_f32 v[232:233], v[232:233], v[148:149]\n"
    "v_pk_fma_f32 v[150:151], v[150:151], s[70:71], v[102:103]\n"
    "v_pk_fma_f32 v[230:231], v[230:231], v[150:151], v[232:233]\n"
    "v_pk_mul_f32 v[232:233], v[232:233], v[150:151]\n"
    "v_pk_fma_f32 v[152:153], v[152:153], s[70:71], v[104:105]\n"
    "v_pk_fma_f32 v[230:231], v[230:231], v[152:153], v[232:233]\n"
    "v_pk_mul_f32 v[232:233], v[232:233], v[152:153]\n"
    "v_pk_fma_f32 v[154:155], v[154:155], s[70:71], v[106:107]\n"
    "v_pk_fma_f32 v[230:231], v[230:231], v[154:155], v[232:233]\n"
    "v_pk_mul_f32 v[232:233], v[232:233], v[154:155]\n"
    "v_pk_fma_f32 v[156:157], v[156:157], s[70:71], v[108:109]\n"
    "v_pk_fma_f32 v[230:231], v[230:231], v[156:157], v[232:233]\n"
    "v_pk_mul_f32 v[232:233], v[232:233], v[156:157]\n"
    "v_pk_fma_f32 v[158:159], v[158:159], s[70:71], v[110:111]\n"
    "v_pk_fma_f32 v[230:231], v[230:231], v[158:159], v[232:233]\n"
    "v_pk_mul_f32 v[232:233], v[232:233], v[158:159]\n"
    "v_rcp_f32 v232, v232\n"
    "v_rcp_f32 v233, v233\n"
    "v_fma_f32 v230, v230, v232, v247\n"
    "v_fmac_f32 v230, v231, v233\n"
    "v_cvt_pk_bf16_f32 v230, v230, v230\n"
    "ds_write_b16 v245, v230\n"
    "ds_read_b64 v[228:229], v246\n"
    ".Ldin_drain1:\n"
    "v_add_u32 v253, s66, v244\n"
    "ds_read_b32 v253, v253\n"
    "v_lshlrev_b32 v202, 3, v249\n"
    "s_waitcnt lgkmcnt(0)\n"
    "v_mfma_f32_4x4x4_16b_bf16 v[224:227], v[228:229], v[176:177], v[224:227] cbsz:4 abid:0\n"
    "v_and_b32_e64 v201, 63, v253\n"
    "v_and_b32 v202, 0x70, v202\n"
    "v_mfma_f32_4x4x4_16b_bf16 v[224:227], v[228:229], v[178:179], v[224:227] cbsz:4 abid:1\n"
    "v_and_b32 v200, 0x3fffffc0, v253\n"
    "v_mul_u32_u24 v201, 272, v201\n"
    "v_mfma_f32_4x4x4_16b_bf16 v[224:227], v[228:229], v[180:181], v[224:227] cbsz:4 abid:2\n"
    "v_lshl_add_u32 v200, v200, 1, v202\n"
    "v_add_u32 v201, v201, v202\n"
    "v_mfma_f32_4x4x4_16b_bf16 v[224:227], v[228:229], v[182:183], v[224:227] cbsz:4 abid:3\n"
    "v_cmp_gt_u32 vcc, s57, v253\n"
    "v_bfrev_b32 v202, 1\n"
    "s_nop 0\n"
    "v_cndmask_b32 v200, v202, v200, vcc\n"
    "s_nop 0\n"
    "v_add_f32 v232, v224, v225\n"
    "v_add_f32 v233, v226, v227\n"
    "v_add_f32 v232, v232, v233\n"
    "v_add_u32 v233, s62, v249\n"
    "v_mul_f32 v232, s69, v232\n"
    "v_cvt_pk_bf16_f32 v232, v232, v232\n"
    "ds_write_b16 v233, v232 offset:32768\n"
    "ds_read_b128 v[216:219], v201 offset:32768\n"
    "s_waitcnt vmcnt(0) lgkmcnt(0)\n"
    "buffer_store_dwordx4 v[216:219], v200, s[52:55], 0 offen sc0 sc1 nt\n"
    "s_branch .Ldin_exit\n"
    ".Ldin_A0s:\n"
    "v_mfma_f32_16x16x32_bf16 v[144:147], v[0:3], v[160:163], v[112:115]\n"
    "v_exp_f32 v128, v128 clamp\n"
    "v_mfma_f32_16x16x32_bf16 v[144:147], v[4:7], v[164:167], v[144:147]\n"
    "v_exp_f32 v129, v129 clamp\n"
    "v_mfma_f32_16x16x32_bf16 v[148:151], v[8:11], v[160:163], v[116:119]\n"
    "v_exp_f32 v130, v130 clamp\n"
    "v_mfma_f32_16x16x32_bf16 v[148:151], v[12:15], v[164:167], v[148:151]\n"
    "v_exp_f32 v131, v131 clamp\n"
    "v_mfma_f32_16x16x32_bf16 v[152:155], v[16:19], v[160:163], v[120:123]\n"
    "v_exp_f32 v132, v132 clamp\n"
    "v_mfma_f32_16x16x32_bf16 v[152:155], v[20:23], v[164:167], v[152:155]\n"
    "v_exp_f32 v133, v133 clamp\n"
    "v_mfma_f32_16x16x32_bf16 v[156:159], v[24:27], v[160:163], v[124:127]\n"
    "v_exp_f32 v134, v134 clamp\n"
    "v_mfma_f32_16x16x32_bf16 v[156:159], v[28:31], v[164:167], v[156:159]\n"
    "v_exp_f32 v135, v135 clamp\n"
    "v_exp_f32 v136, v136 clamp\n"
    "s_add_u32 m0, s68, 2048\n"
    "v_exp_f32 v137, v137 clamp\n"
    "v_add_u32 v253, s67, v244\n"
    "s_waitcnt lgkmcnt(0)\n"
    "v_mfma_f32_4x4x4_16b_bf16 v[224:227], v[228:229], v[176:177], v[224:227] cbsz:4 abid:0\n"
    "v_exp_f32 v138, v138 clamp\n"
    "v_exp_f32 v139, v139 clamp\n"
    "v_mfma_f32_4x4x4_16b_bf16 v[224:227], v[228:229], v[178:179], v[224:227] cbsz:4 abid:1\n"
    "v_exp_f32 v140, v140 clamp\n"
    "v_exp_f32 v141, v141 clamp\n"
    "v_mfma_f32_4x4x4_16b_bf16 v[224:227], v[228:229], v[180:181], v[224:227] cbsz:4 abid:2\n"
    "v_exp_f32 v142, v142 clamp\n"
    "v_exp_f32 v143, v143 clamp\n"
    "v_mfma_f32_4x4x4_16b_bf16 v[224:227], v[228:229], v[182:183], v[224:227] cbsz:4 abid:3\n"
    "s_waitcnt vmcnt(4)\n"
    "ds_read_b64_tr_b16 v[176:177], v240 offset:2048\n"
    "ds_read_b64_tr_b16 v[178:179], v241 offset:2304\n"
    "ds_read_b64_tr_b16 v[180:181], v242 offset:2560\n"
    "ds_read_b64_tr_b16 v[182:183], v243 offset:2816\n"
    "ds_read_b32 v236, v253 offset:0\n"
    "ds_read_b32 v238, v253 offset:4\n"
    "ds_read_b128 v[160:163], v234 offset:4096\n"
    "ds_read_b128 v[164:167], v235 offset:4096\n"
    "s_cbranch_vccz .Ldin_A0s_np\n"
    "s_setprio 1\n"
    ".Ldin_A0s_np:\n"
    "v_pk_fma_f32 v[128:129], v[128:129], s[70:71], v[96:97]\n"
    "v_pk_fma_f32 v[130:131], v[130:131], s[70:71], v[98:99]\n"
    "v_add_f32 v191, v224, v225\n"
    "v_add_f32 v190, v226, v227\n"
    "v_add_f32 v191, v191, v190\n"
    "v_add_u32 v190, s62, v249\n"
    "v_mul_f32 v191, s69, v191\n"
    "v_cvt_pk_bf16_f32 v191, v191, v191\n"
    "ds_write_b16 v190, v191 offset:32768\n"
    "s_bfe_u32 s94, s59, 0x180006\n"
    "s_mul_i32 s86, s94, s56\n"
    "v_mbcnt_lo_u32_b32 v184, s20, 0\n"
    "v_mbcnt_lo_u32_b32 v185, s22, 0\n"
    "v_mbcnt_lo_u32_b32 v186, s24, 0\n"
    "v_mbcnt_lo_u32_b32 v187, s26, 0\n"
    "s_bcnt1_i32_b64 s90, s[20:21]\n"
    "s_bcnt1_i32_b64 s91, s[22:23]\n"
    "s_bcnt1_i32_b64 s92, s[24:25]\n"
    "s_bcnt1_i32_b64 s93, s[26:27]\n"
    "v_mbcnt_hi_u32_b32 v184, s21, v184\n"
    "v_mbcnt_hi_u32_b32 v185, s23, v185\n"
    "v_mbcnt_hi_u32_b32 v186, s25, v186\n"
    "v_mbcnt_hi_u32_b32 v187, s27, v187\n"
    "s_lshl2_add_u32 s90, s90, s65\n"
    "s_lshl2_add_u32 s91, s91, s90\n"
    "s_lshl2_add_u32 s92, s92, s91\n"
    "s_lshl2_add_u32 s93, s93, s92\n"
    "v_add_u32_e64 v188, s86, v251\n"
    "v_pk_add_f32 v[230:231], v[128:129], v[130:131]\n"
    "v_pk_mul_f32 v[232:233], v[128:129], v[130:131]\n"
    "v_pk_fma_f32 v[132:133], v[132:133], s[70:71], v[100:101]\n"
    "v_pk_fma_f32 v[230:231], v[230:231], v[132:133], v[232:233]\n"
    "v_pk_mul_f32 v[232:233], v[232:233], v[132:133]\n"
    "v_lshl_add_u32 v184, v184, 2, s65\n"
    "v_lshl_add_u32 v185, v185, 2, s90\n"
    "v_lshl_add_u32 v186, v186, 2, s91\n"
    "v_lshl_add_u32 v187, v187, 2, s92\n"
    "v_mov_b32 v191, -1\n"
    "v_lshl_add_u32 v190, v249, 1, s93\n"
    "ds_write2_b32 v190, v191, v191 offset1:64\n"
    "s_mov_b64 exec, s[20:21]\n"
    "ds_write_b32 v184, v188\n"
    "s_mov_b64 exec, s[22:23]\n"
    "v_add_u32 v189, 64, v188\n"
    "ds_write_b32 v185, v189\n"
    "s_mov_b64 exec, s[24:25]\n"
    "v_add_u32 v190, 128, v188\n"
    "ds_write_b32 v186, v190\n"
    "s_mov_b64 exec, s[26:27]\n"
    "v_add_u32 v189, 192, v188\n"
    "ds_write_b32 v187, v189\n"
    "s_mov_b64 exec, -1\n"
    "s_add_u32 s95, s93, 60\n"
    "s_add_u32 s94, s82, 128\n"
    "s_andn2_b32 s95, s95, 63\n"
    "s_max_u32 s83, s95, s94\n"
    "v_pk_fma_f32 v[134:135], v[134:135], s[70:71], v[102:103]\n"
    "v_pk_fma_f32 v[230:231], v[230:231], v[134:135], v[232:233]\n"
    "v_pk_mul_f32 v[232:233], v[232:233], v[134:135]\n"
    "v_pk_fma_f32 v[136:137], v[136:137], s[70:71], v[104:105]\n"
    "v_pk_fma_f32 v[230:231], v[230:231], v[136:137], v[232:233]\n"
    "v_pk_mul_f32 v[232:233], v[232:233], v[136:137]\n"
    "v_pk_fma_f32 v[138:139], v[138:139], s[70:71], v[106:107]\n"
    "v_pk_fma_f32 v[230:231], v[230:231], v[138:139], v[232:233]\n"
    "v_pk_mul_f32 v[232:233], v[232:233], v[138:139]\n"
    "v_pk_fma_f32 v[140:141], v[140:141], s[70:71], v[108:109]\n"
    "v_pk_fma_f32 v[230:231], v[230:231], v[140:141], v[232:233]\n"
    "v_pk_mul_f32 v[232:233], v[232:233], v[140:141]\n"
    "v_pk_fma_f32 v[142:143], v[142:143], s[70:71], v[110:111]\n"
    "v_pk_fma_f32 v[230:231], v[230:231], v[142:143], v[232:233]\n"
    "v_pk_mul_f32 v[232:233], v[232:233], v[142:143]\n"
    "v_rcp_f32 v232, v232\n"
    "v_rcp_f32 v233, v233\n"
    "v_fma_f32 v230, v230, v232, v247\n"
    "v_fmac_f32 v230, v231, v233\n"
    "v_lshlrev_b32 v232, 2, v249\n"
    "v_lshrrev_b32 v233, 2, v249\n"
    "v_and_b32 v232, 56, v232\n"
    "v_and_b32 v233, 4, v233\n"
    "v_add3_u32 v232, v232, v233, s81\n"
    "ds_read_b32 v232, v232\n"
    "s_add_u32 s81, s81, 64\n"
    "s_waitcnt lgkmcnt(0)\n"
    "v_lshlrev_b32 v232, 1, v232\n"
    "buffer_load_ushort v232, v232, s[44:47], 0 offen\n"
    "s_waitcnt vmcnt(0)\n"
    "v_lshlrev_b32 v232, 16, v232\n"
    "v_mul_f32 v230, v230, v232\n"
    "v_cvt_pk_bf16_f32 v230, v230, v230\n"
    "s_add_u32 s67, s67, 64\n"
    "s_waitcnt lgkmcnt(0)\n"
    "ds_write_b16 v245, v230\n"
    "ds_read_b64 v[228:229], v246\n"
    "buffer_load_dwordx4 v[236:237], s[40:43], 0 idxen offen offset:0 nt lds\n"
    "buffer_load_dwordx4 v[238:239], s[40:43], 0 idxen offen offset:1024 nt lds\n"
    ".Ldin_B1s:\n"
    "v_mfma_f32_16x16x32_bf16 v[128:131], v[0:3], v[160:163], v[112:115]\n"
    "v_exp_f32 v144, v144 clamp\n"
    "v_mfma_f32_16x16x32_bf16 v[128:131], v[4:7], v[164:167], v[128:131]\n"
    "v_exp_f32 v145, v145 clamp\n"
    "v_mfma_f32_16x16x32_bf16 v[132:135], v[8:11], v[160:163], v[116:119]\n"
    "v_exp_f32 v146, v146 clamp\n"
    "v_mfma_f32_16x16x32_bf16 v[132:135], v[12:15], v[164:167], v[132:135]\n"
    "v_exp_f32 v147, v147 clamp\n"
    "v_mfma_f32_16x16x32_bf16 v[136:139], v[16:19], v[160:163], v[120:123]\n"
    "v_exp_f32 v148, v148 clamp\n"
    "v_mfma_f32_16x16x32_bf16 v[136:139], v[20:23], v[164:167], v[136:139]\n"
    "v_exp_f32 v149, v149 clamp\n"
    "v_mfma_f32_16x16x32_bf16 v[140:143], v[24:27], v[160:163], v[124:127]\n"
    "v_exp_f32 v150, v150 clamp\n"
    "v_mfma_f32_16x16x32_bf16 v[140:143], v[28:31], v[164:167], v[140:143]\n"
    "v_exp_f32 v151, v151 clamp\n"
    "v_exp_f32 v152, v152 clamp\n"
    "s_cmp_eq_u32 s67, s80\n"
    "s_cselect_b32 s67, s65, s67\n"
    "s_and_b32 s94, s59, 63\n"
    "s_mul_i32 s94, s94, 272\n"
    "v_add_u32 v252, s94, v250\n"
    "s_add_u32 m0, s68, 4096\n"
    "v_exp_f32 v153, v153 clamp\n"
    "v_add_u32 v253, s67, v244\n"
    "s_waitcnt lgkmcnt(0)\n"
    "v_mfma_f32_4x4x4_16b_bf16 v[224:227], v[228:229], v[168:169], 0 cbsz:4 abid:0\n"
    "v_exp_f32 v154, v154 clamp\n"
    "v_exp_f32 v155, v155 clamp\n"
    "v_mfma_f32_4x4x4_16b_bf16 v[224:227], v[228:229], v[170:171], v[224:227] cbsz:4 abid:1\n"
    "v_exp_f32 v156, v156 clamp\n"
    "v_exp_f32 v157, v157 clamp\n"
    "v_mfma_f32_4x4x4_16b_bf16 v[224:227], v[228:229], v[172:173], v[224:227] cbsz:4 abid:2\n"
    "v_exp_f32 v158, v158 clamp\n"
    "v_exp_f32 v159, v159 clamp\n"
    "v_mfma_f32_4x4x4_16b_bf16 v[224:227], v[228:229], v[174:175], v[224:227] cbsz:4 abid:3\n"
    "s_waitcnt vmcnt(4)\n"
    "s_memrealtime s[34:35]\n"
    "ds_read_b64_tr_b16 v[168:169], v240 offset:4096\n"
    "ds_read_b64_tr_b16 v[170:171], v241 offset:4352\n"
    "ds_read_b64_tr_b16 v[172:173], v242 offset:4608\n"
    "ds_read_b64_tr_b16 v[174:175], v243 offset:4864\n"
    "ds_read_b32 v236, v253 offset:0\n"
    "ds_read_b32 v238, v253 offset:4\n"
    "ds_read_b128 v[160:163], v234 offset:6144\n"
    "ds_read_b128 v[164:167], v235 offset:6144\n"
    "ds_read_b128 v[184:187], v252 offset:0\n"
    "ds_read_b128 v[188:191], v252 offset:16\n"
    "v_pk_fma_f32 v[144:145], v[144:145], s[70:71], v[96:97]\n"
    "v_pk_fma_f32 v[146:147], v[146:147], s[70:71], v[98:99]\n"
    "v_pk_add_f32 v[230:231], v[144:145], v[146:147]\n"
    "v_pk_mul_f32 v[232:233], v[144:145], v[146:147]\n"
    "v_pk_fma_f32 v[148:149], v[148:149], s[70:71], v[100:101]\n"
    "v_pk_fma_f32 v[230:231], v[230:231], v[148:149], v[232:233]\n"
    "v_pk_mul_f32 v[232:233], v[232:233], v[148:149]\n"
    "v_pk_fma_f32 v[150:151], v[150:151], s[70:71], v[102:103]\n"
    "v_pk_fma_f32 v[230:231], v[230:231], v[150:151], v[232:233]\n"
    "v_pk_mul_f32 v[232:233], v[232:233], v[150:151]\n"
    "v_pk_fma_f32 v[152:153], v[152:153], s[70:71], v[104:105]\n"
    "v_pk_fma_f32 v[230:231], v[230:231], v[152:153], v[232:233]\n"
    "v_pk_mul_f32 v[232:233], v[232:233], v[152:153]\n"
    "v_pk_fma_f32 v[154:155], v[154:155], s[70:71], v[106:107]\n"
    "v_pk_fma_f32 v[230:231], v[230:231], v[154:155], v[232:233]\n"
    "v_pk_mul_f32 v[232:233], v[232:233], v[154:155]\n"
    "v_pk_fma_f32 v[156:157], v[156:157], s[70:71], v[108:109]\n"
    "v_pk_fma_f32 v[230:231], v[230:231], v[156:157], v[232:233]\n"
    "v_pk_mul_f32 v[232:233], v[232:233], v[156:157]\n"
    "v_pk_fma_f32 v[158:159], v[158:159], s[70:71], v[110:111]\n"
    "v_pk_fma_f32 v[230:231], v[230:231], v[158:159], v[232:233]\n"
    "v_pk_mul_f32 v[232:233], v[232:233], v[158:159]\n"
    "v_rcp_f32 v232, v232\n"
    "v_rcp_f32 v233, v233\n"
    "v_fma_f32 v230, v230, v232, v247\n"
    "v_fmac_f32 v230, v231, v233\n"
    "v_lshlrev_b32 v232, 2, v249\n"
    "v_lshrrev_b32 v233, 2, v249\n"
    "v_and_b32 v232, 56, v232\n"
    "v_and_b32 v233, 4, v233\n"
    "v_add3_u32 v232, v232, v233, s81\n"
    "ds_read_b32 v232, v232\n"
    "s_add_u32 s81, s81, 64\n"
    "s_waitcnt lgkmcnt(0)\n"
    "v_lshlrev_b32 v232, 1, v232\n"
    "buffer_load_ushort v232, v232, s[44:47], 0 offen\n"
    "s_waitcnt vmcnt(0)\n"
    "v_lshlrev_b32 v232, 16, v232\n"
    "v_mul_f32 v230, v230, v232\n"
    "v_cvt_pk_bf16_f32 v230, v230, v230\n"
    "s_add_u32 s67, s67, 64\n"
    "s_waitcnt lgkmcnt(0)\n"
    "ds_write_b16 v245, v230\n"
    "ds_read_b64 v[228:229], v246\n"
    "s_cmp_le_u32 s34, s76\n"
    "s_cbranch_scc1 .Ldin_B1s_wait\n"
    ".Ldin_B1s_go:\n"
    "s_sub_u32 s94, s34, 60\n"
    "s_max_u32 s76, s76, s94\n"
    "s_add_u32 s76, s76, s72\n"
    ".Ldin_B1s_dma:\n"
    "buffer_load_dwordx4 v[236:237], s[40:43], 0 idxen offen offset:0 nt lds\n"
    "buffer_load_dwordx4 v[238:239], s[40:43], 0 idxen offen offset:1024 nt lds\n"
    ".Ldin_B1s_nd:\n"
    ".Ldin_G2s:\n"
    "v_mfma_f32_16x16x32_bf16 v[144:147], v[0:3], v[160:163], v[112:115]\n"
    "v_exp_f32 v128, v128 clamp\n"
    "v_mfma_f32_16x16x32_bf16 v[144:147], v[4:7], v[164:167], v[144:147]\n"
    "v_exp_f32 v129, v129 clamp\n"
    "v_mfma_f32_16x16x32_bf16 v[148:151], v[8:11], v[160:163], v[116:119]\n"
    "v_exp_f32 v130, v130 clamp\n"
    "v_mfma_f32_16x16x32_bf16 v[148:151], v[12:15], v[164:167], v[148:151]\n"
    "v_exp_f32 v131, v131 clamp\n"
    "v_mfma_f32_16x16x32_bf16 v[152:155], v[16:19], v[160:163], v[120:123]\n"
    "v_exp_f32 v132, v132 clamp\n"
    "v_mfma_f32_16x16x32_bf16 v[152:155], v[20:23], v[164:167], v[152:155]\n"
    "v_exp_f32 v133, v133 clamp\n"
    "v_mfma_f32_16x16x32_bf16 v[156:159], v[24:27], v[160:163], v[124:127]\n"
    "v_exp_f32 v134, v134 clamp\n"
    "v_mfma_f32_16x16x32_bf16 v[156:159], v[28:31], v[164:167], v[156:159]\n"
    "v_exp_f32 v135, v135 clamp\n"
    "v_exp_f32 v136, v136 clamp\n"
    "s_cmp_eq_u32 s67, s80\n"
    "s_cselect_b32 s67, s65, s67\n"
    "s_add_u32 m0, s68, 6144\n"
    "v_exp_f32 v137, v137 clamp\n"
    "v_add_u32_e64 v253, s67, v244\n"
    "s_waitcnt lgkmcnt(0)\n"
    "v_mfma_f32_4x4x4_16b_bf16 v[224:227], v[228:229], v[176:177], v[224:227] cbsz:4 abid:0\n"
    "v_exp_f32 v138, v138 clamp\n"
    "v_exp_f32 v139, v139 clamp\n"
    "v_mfma_f32_4x4x4_16b_bf16 v[224:227], v[228:229], v[178:179], v[224:227] cbsz:4 abid:1\n"
    "v_exp_f32 v140, v140 clamp\n"
    "v_exp_f32 v141, v141 clamp\n"
    "v_mfma_f32_4x4x4_16b_bf16 v[224:227], v[228:229], v[180:181], v[224:227] cbsz:4 abid:2\n"
    "v_exp_f32 v142, v142 clamp\n"
    "v_exp_f32 v143, v143 clamp\n"
    "v_mfma_f32_4x4x4_16b_bf16 v[224:227], v[228:229], v[182:183], v[224:227] cbsz:4 abid:3\n"
    "s_waitcnt vmcnt(4)\n"
    "s_memrealtime s[34:35]\n"
    "ds_read_b64_tr_b16 v[176:177], v240 offset:6144\n"
    "ds_read_b64_tr_b16 v[178:179], v241 offset:6400\n"
    "ds_read_b64_tr_b16 v[180:181], v242 offset:6656\n"
    "ds_read_b64_tr_b16 v[182:183], v243 offset:6912\n"
    "ds_read_b32 v236, v253 offset:0\n"
    "ds_read_b32 v238, v253 offset:4\n"
    "ds_read_b128 v[160:163], v234 offset:0\n"
    "ds_read_b128 v[164:167], v235 offset:0\n"
    "v_pk_fma_f32 v[128:129], v[128:129], s[70:71], v[96:97]\n"
    "v_pk_fma_f32 v[130:131], v[130:131], s[70:71], v[98:99]\n"
    "v_pk_add_f32 v[230:231], v[128:129], v[130:131]\n"
    "v_pk_mul_f32 v[232:233], v[128:129], v[130:131]\n"
    "v_pk_fma_f32 v[132:133], v[132:133], s[70:71], v[100:101]\n"
    "v_pk_fma_f32 v[230:231], v[230:231], v[132:133], v[232:233]\n"
    "v_pk_mul_f32 v[232:233], v[232:233], v[132:133]\n"
    "v_pk_fma_f32 v[134:135], v[134:135], s[70:71], v[102:103]\n"
    "v_pk_fma_f32 v[230:231], v[230:231], v[134:135], v[232:233]\n"
    "v_pk_mul_f32 v[232:233], v[232:233], v[134:135]\n"
    "v_pk_fma_f32 v[136:137], v[136:137], s[70:71], v[104:105]\n"
    "v_pk_fma_f32 v[230:231], v[230:231], v[136:137], v[232:233]\n"
    "v_pk_mul_f32 v[232:233], v[232:233], v[136:137]\n"
    "v_pk_fma_f32 v[138:139], v[138:139], s[70:71], v[106:107]\n"
    "v_pk_fma_f32 v[230:231], v[230:231], v[138:139], v[232:233]\n"
    "v_pk_mul_f32 v[232:233], v[232:233], v[138:139]\n"
    "v_pk_fma_f32 v[140:141], v[140:141], s[70:71], v[108:109]\n"
    "v_pk_fma_f32 v[230:231], v[230:231], v[140:141], v[232:233]\n"
    "v_pk_mul_f32 v[232:233], v[232:233], v[140:141]\n"
    "v_pk_fma_f32 v[142:143], v[142:143], s[70:71], v[110:111]\n"
    "v_pk_fma_f32 v[230:231], v[230:231], v[142:143], v[232:233]\n"
    "v_pk_mul_f32 v[232:233], v[232:233], v[142:143]\n"
    "v_rcp_f32 v232, v232\n"
    "v_rcp_f32 v233, v233\n"
    "v_fma_f32 v230, v230, v232, v247\n"
    "v_fmac_f32 v230, v231, v233\n"
    "v_lshlrev_b32 v232, 2, v249\n"
    "v_lshrrev_b32 v233, 2, v249\n"
    "v_and_b32 v232, 56, v232\n"
    "v_and_b32 v233, 4, v233\n"
    "v_add3_u32 v232, v232, v233, s81\n"
    "ds_read_b32 v232, v232\n"
    "s_add_u32 s81, s81, 64\n"
    "s_waitcnt lgkmcnt(0)\n"
    "v_lshlrev_b32 v232, 1, v232\n"
    "buffer_load_ushort v232, v232, s[44:47], 0 offen\n"
    "s_waitcnt vmcnt(0)\n"
    "v_lshlrev_b32 v232, 16, v232\n"
    "v_mul_f32 v230, v230, v232\n"
    "v_cvt_pk_bf16_f32 v230, v230, v230\n"
    "s_add_u32 s67, s67, 64\n"
    "s_waitcnt lgkmcnt(0)\n"
    "ds_write_b16 v245, v230\n"
    "ds_read_b64 v[228:229], v246\n"
    "s_cmp_le_u32 s34, s76\n"
    "s_cbranch_scc1 .Ldin_G2s_wait\n"
    ".Ldin_G2s_go:\n"
    "s_sub_u32 s94, s34, 60\n"
    "s_max_u32 s76, s76, s94\n"
    "s_add_u32 s76, s76, s72\n"
    ".Ldin_G2s_dma:\n"
    "buffer_load_dwordx4 v[236:237], s[40:43], 0 idxen offen offset:0 nt lds\n"
    "buffer_load_dwordx4 v[238:239], s[40:43], 0 idxen offen offset:1024 nt lds\n"
    ".Ldin_G2s_nd:\n"
    "s_cmp_eq_u32 s67, s82\n"
    "s_cbranch_scc1 .Ldin_L3s\n"
    ".Ldin_G3s:\n"
    "v_mfma_f32_16x16x32_bf16 v[128:131], v[0:3], v[160:163], v[112:115]\n"
    "v_exp_f32 v144, v144 clamp\n"
    "v_mfma_f32_16x16x32_bf16 v[128:131], v[4:7], v[164:167], v[128:131]\n"
    "v_exp_f32 v145, v145 clamp\n"
    "v_mfma_f32_16x16x32_bf16 v[132:135], v[8:11], v[160:163], v[116:119]\n"
    "v_exp_f32 v146, v146 clamp\n"
    "v_mfma_f32_16x16x32_bf16 v[132:135], v[12:15], v[164:167], v[132:135]\n"
    "v_exp_f32 v147, v147 clamp\n"
    "v_mfma_f32_16x16x32_bf16 v[136:139], v[16:19], v[160:163], v[120:123]\n"
    "v_exp_f32 v148, v148 clamp\n"
    "v_mfma_f32_16x16x32_bf16 v[136:139], v[20:23], v[164:167], v[136:139]\n"
    "v_exp_f32 v149, v149 clamp\n"
    "v_mfma_f32_16x16x32_bf16 v[140:143], v[24:27], v[160:163], v[124:127]\n"
    "v_exp_f32 v150, v150 clamp\n"
    "v_mfma_f32_16x16x32_bf16 v[140:143], v[28:31], v[164:167], v[140:143]\n"
    "v_exp_f32 v151, v151 clamp\n"
    "v_exp_f32 v152, v152 clamp\n"
    "s_cmp_eq_u32 s67, s80\n"
    "s_cselect_b32 s67, s65, s67\n"
    "s_add_u32 m0, s68, 0\n"
    "v_exp_f32 v153, v153 clamp\n"
    "v_add_u32 v253, s67, v244\n"
    "s_waitcnt lgkmcnt(0)\n"
    "v_mfma_f32_4x4x4_16b_bf16 v[224:227], v[228:229], v[168:169], v[224:227] cbsz:4 abid:0\n"
    "v_exp_f32 v154, v154 clamp\n"
    "v_exp_f32 v155, v155 clamp\n"
    "v_mfma_f32_4x4x4_16b_bf16 v[224:227], v[228:229], v[170:171], v[224:227] cbsz:4 abid:1\n"
    "v_exp_f32 v156, v156 clamp\n"
    "v_exp_f32 v157, v157 clamp\n"
    "v_mfma_f32_4x4x4_16b_bf16 v[224:227], v[228:229], v[172:173], v[224:227] cbsz:4 abid:2\n"
    "v_exp_f32 v158, v158 clamp\n"
    "v_exp_f32 v159, v159 clamp\n"
    "v_mfma_f32_4x4x4_16b_bf16 v[224:227], v[228:229], v[174:175], v[224:227] cbsz:4 abid:3\n"
    "s_waitcnt vmcnt(4)\n"
    "s_memrealtime s[34:35]\n"
    "ds_read_b64_tr_b16 v[168:169], v240 offset:0\n"
    "ds_read_b64_tr_b16 v[170:171], v241 offset:256\n"
    "ds_read_b64_tr_b16 v[172:173], v242 offset:512\n"
    "ds_read_b64_tr_b16 v[174:175], v243 offset:768\n"
    "ds_read_b32 v236, v253 offset:0\n"
    "ds_read_b32 v238, v253 offset:4\n"
    "ds_read_b128 v[160:163], v234 offset:2048\n"
    "ds_read_b128 v[164:167], v235 offset:2048\n"
    "v_pk_fma_f32 v[144:145], v[144:145], s[70:71], v[96:97]\n"
    "v_pk_fma_f32 v[146:147], v[146:147], s[70:71], v[98:99]\n"
    "v_pk_add_f32 v[230:231], v[144:145], v[146:147]\n"
    "v_pk_mul_f32 v[232:233], v[144:145], v[146:147]\n"
    "v_pk_fma_f32 v[148:149], v[148:149], s[70:71], v[100:101]\n"
    "v_pk_fma_f32 v[230:231], v[230:231], v[148:149], v[232:233]\n"
    "v_pk_mul_f32 v[232:233], v[232:233], v[148:149]\n"
    "v_pk_fma_f32 v[150:151], v[150:151], s[70:71], v[102:103]\n"
    "v_pk_fma_f32 v[230:231], v[230:231], v[150:151], v[232:233]\n"
    "v_pk_mul_f32 v[232:233], v[232:233], v[150:151]\n"
    "v_pk_fma_f32 v[152:153], v[152:153], s[70:71], v[104:105]\n"
    "v_pk_fma_f32 v[230:231], v[230:231], v[152:153], v[232:233]\n"
    "v_pk_mul_f32 v[232:233], v[232:233], v[152:153]\n"
    "v_pk_fma_f32 v[154:155], v[154:155], s[70:71], v[106:107]\n"
    "v_pk_fma_f32 v[230:231], v[230:231], v[154:155], v[232:233]\n"
    "v_pk_mul_f32 v[232:233], v[232:233], v[154:155]\n"
    "v_pk_fma_f32 v[156:157], v[156:157], s[70:71], v[108:109]\n"
    "v_pk_fma_f32 v[230:231], v[230:231], v[156:157], v[232:233]\n"
    "v_pk_mul_f32 v[232:233], v[232:233], v[156:157]\n"
    "v_pk_fma_f32 v[158:159], v[158:159], s[70:71], v[110:111]\n"
    "v_pk_fma_f32 v[230:231], v[230:231], v[158:159], v[232:233]\n"
    "v_pk_mul_f32 v[232:233], v[232:233], v[158:159]\n"
    "v_rcp_f32 v232, v232\n"
    "v_rcp_f32 v233, v233\n"
    "v_fma_f32 v230, v230, v232, v247\n"
    "v_fmac_f32 v230, v231, v233\n"
    "v_lshlrev_b32 v232, 2, v249\n"
    "v_lshrrev_b32 v233, 2, v249\n"
    "v_and_b32 v232, 56, v232\n"
    "v_and_b32 v233, 4, v233\n"
    "v_add3_u32 v232, v232, v233, s81\n"
    "ds_read_b32 v232, v232\n"
    "s_add_u32 s81, s81, 64\n"
    "s_waitcnt lgkmcnt(0)\n"
    "v_lshlrev_b32 v232, 1, v232\n"
    "buffer_load_ushort v232, v232, s[44:47], 0 offen\n"
    "s_waitcnt vmcnt(0)\n"
    "v_lshlrev_b32 v232, 16, v232\n"
    "v_mul_f32 v230, v230, v232\n"
    "v_cvt_pk_bf16_f32 v230, v230, v230\n"
    "s_add_u32 s67, s67, 64\n"
    "s_waitcnt lgkmcnt(0)\n"
    "ds_write_b16 v245, v230\n"
    "ds_read_b64 v[228:229], v246\n"
    "s_cmp_le_u32 s34, s76\n"
    "s_cbranch_scc1 .Ldin_G3s_wait\n"
    ".Ldin_G3s_go:\n"
    "s_add_u32 s76, s76, s72\n"
    ".Ldin_G3s_dma:\n"
    "buffer_load_dwordx4 v[236:237], s[40:43], 0 idxen offen offset:0 nt lds\n"
    "buffer_load_dwordx4 v[238:239], s[40:43], 0 idxen offen offset:1024 nt lds\n"
    ".Ldin_G3s_nd:\n"
    "s_cmp_eq_u32 s67, s82\n"
    "s_cbranch_scc1 .Ldin_L0s\n"
    ".Ldin_G0s:\n"
    "v_mfma_f32_16x16x32_bf16 v[144:147], v[0:3], v[160:163], v[112:115]\n"
    "v_exp_f32 v128, v128 clamp\n"
    "v_mfma_f32_16x16x32_bf16 v[144:147], v[4:7], v[164:167], v[144:147]\n"
    "v_exp_f32 v129, v129 clamp\n"
    "v_mfma_f32_16x16x32_bf16 v[148:151], v[8:11], v[160:163], v[116:119]\n"
    "v_exp_f32 v130, v130 clamp\n"
    "v_mfma_f32_16x16x32_bf16 v[148:151], v[12:15], v[164:167], v[148:151]\n"
    "v_exp_f32 v131, v131 clamp\n"
    "v_mfma_f32_16x16x32_bf16 v[152:155], v[16:19], v[160:163], v[120:123]\n"
    "v_exp_f32 v132, v132 clamp\n"
    "v_mfma_f32_16x16x32_bf16 v[152:155], v[20:23], v[164:167], v[152:155]\n"
    "v_exp_f32 v133, v133 clamp\n"
    "v_mfma_f32_16x16x32_bf16 v[156:159], v[24:27], v[160:163], v[124:127]\n"
    "v_exp_f32 v134, v134 clamp\n"
    "v_mfma_f32_16x16x32_bf16 v[156:159], v[28:31], v[164:167], v[156:159]\n"
    "v_exp_f32 v135, v135 clamp\n"
    "v_exp_f32 v136, v136 clamp\n"
    "s_cmp_eq_u32 s67, s80\n"
    "s_cselect_b32 s67, s65, s67\n"
    "s_add_u32 m0, s68, 2048\n"
    "v_exp_f32 v137, v137 clamp\n"
    "v_add_u32_e64 v253, s67, v244\n"
    "s_waitcnt lgkmcnt(0)\n"
    "v_mfma_f32_4x4x4_16b_bf16 v[224:227], v[228:229], v[176:177], v[224:227] cbsz:4 abid:0\n"
    "v_exp_f32 v138, v138 clamp\n"
    "v_exp_f32 v139, v139 clamp\n"
    "v_mfma_f32_4x4x4_16b_bf16 v[224:227], v[228:229], v[178:179], v[224:227] cbsz:4 abid:1\n"
    "v_exp_f32 v140, v140 clamp\n"
    "v_exp_f32 v141, v141 clamp\n"
    "v_mfma_f32_4x4x4_16b_bf16 v[224:227], v[228:229], v[180:181], v[224:227] cbsz:4 abid:2\n"
    "v_exp_f32 v142, v142 clamp\n"
    "v_exp_f32 v143, v143 clamp\n"
    "v_mfma_f32_4x4x4_16b_bf16 v[224:227], v[228:229], v[182:183], v[224:227] cbsz:4 abid:3\n"
    "s_waitcnt vmcnt(4)\n"
    "s_memrealtime s[34:35]\n"
    "ds_read_b64_tr_b16 v[176:177], v240 offset:2048\n"
    "ds_read_b64_tr_b16 v[178:179], v241 offset:2304\n"
    "ds_read_b64_tr_b16 v[180:181], v242 offset:2560\n"
    "ds_read_b64_tr_b16 v[182:183], v243 offset:2816\n"
    "ds_read_b32 v236, v253 offset:0\n"
    "ds_read_b32 v238, v253 offset:4\n"
    "ds_read_b128 v[160:163], v234 offset:4096\n"
    "ds_read_b128 v[164:167], v235 offset:4096\n"
    "v_pk_fma_f32 v[128:129], v[128:129], s[70:71], v[96:97]\n"
    "v_pk_fma_f32 v[130:131], v[130:131], s[70:71], v[98:99]\n"
    "v_pk_add_f32 v[230:231], v[128:129], v[130:131]\n"
    "v_pk_mul_f32 v[232:233], v[128:129], v[130:131]\n"
    "v_pk_fma_f32 v[132:133], v[132:133], s[70:71], v[100:101]\n"
    "v_pk_fma_f32 v[230:231], v[230:231], v[132:133], v[232:233]\n"
    "v_pk_mul_f32 v[232:233], v[232:233], v[132:133]\n"
    "v_pk_fma_f32 v[134:135], v[134:135], s[70:71], v[102:103]\n"
    "v_pk_fma_f32 v[230:231], v[230:231], v[134:135], v[232:233]\n"
    "v_pk_mul_f32 v[232:233], v[232:233], v[134:135]\n"
    "v_pk_fma_f32 v[136:137], v[136:137], s[70:71], v[104:105]\n"
    "v_pk_fma_f32 v[230:231], v[230:231], v[136:137], v[232:233]\n"
    "v_pk_mul_f32 v[232:233], v[232:233], v[136:137]\n"
    "v_pk_fma_f32 v[138:139], v[138:139], s[70:71], v[106:107]\n"
    "v_pk_fma_f32 v[230:231], v[230:231], v[138:139], v[232:233]\n"
    "v_pk_mul_f32 v[232:233], v[232:233], v[138:139]\n"
    "v_pk_fma_f32 v[140:141], v[140:141], s[70:71], v[108:109]\n"
    "v_pk_fma_f32 v[230:231], v[230:231], v[140:141], v[232:233]\n"
    "v_pk_mul_f32 v[232:233], v[232:233], v[140:141]\n"
    "v_pk_fma_f32 v[142:143], v[142:143], s[70:71], v[110:111]\n"
    "v_pk_fma_f32 v[230:231], v[230:231], v[142:143], v[232:233]\n"
    "v_pk_mul_f32 v[232:233], v[232:233], v[142:143]\n"
    "v_rcp_f32 v232, v232\n"
    "v_rcp_f32 v233, v233\n"
    "v_fma_f32 v230, v230, v232, v247\n"
    "v_fmac_f32 v230, v231, v233\n"
    "v_lshlrev_b32 v232, 2, v249\n"
    "v_lshrrev_b32 v233, 2, v249\n"
    "v_and_b32 v232, 56, v232\n"
    "v_and_b32 v233, 4, v233\n"
    "v_add3_u32 v232, v232, v233, s81\n"
    "ds_read_b32 v232, v232\n"
    "s_add_u32 s81, s81, 64\n"
    "s_waitcnt lgkmcnt(0)\n"
    "v_lshlrev_b32 v232, 1, v232\n"
    "buffer_load_ushort v232, v232, s[44:47], 0 offen\n"
    "s_waitcnt vmcnt(0)\n"
    "v_lshlrev_b32 v232, 16, v232\n"
    "v_mul_f32 v230, v230, v232\n"
    "v_cvt_pk_bf16_f32 v230, v230, v230\n"
    "s_add_u32 s67, s67, 64\n"
    "s_waitcnt lgkmcnt(0)\n"
    "ds_write_b16 v245, v230\n"
    "ds_read_b64 v[228:229], v246\n"
    "s_cmp_le_u32 s34, s76\n"
    "s_cbranch_scc1 .Ldin_G0s_wait\n"
    ".Ldin_G0s_go:\n"
    "s_sub_u32 s94, s34, 60\n"
    "s_max_u32 s76, s76, s94\n"
    "s_add_u32 s76, s76, s72\n"
    ".Ldin_G0s_dma:\n"
    "buffer_load_dwordx4 v[236:237], s[40:43], 0 idxen offen offset:0 nt lds\n"
    "buffer_load_dwordx4 v[238:239], s[40:43], 0 idxen offen offset:1024 nt lds\n"
    ".Ldin_G0s_nd:\n"
    "s_cmp_eq_u32 s67, s82\n"
    "s_cbranch_scc1 .Ldin_L1s\n"
    ".Ldin_G1s:\n"
    "v_mfma_f32_16x16x32_bf16 v[128:131], v[0:3], v[160:163], v[112:115]\n"
    "v_exp_f32 v144, v144 clamp\n"
    "v_mfma_f32_16x16x32_bf16 v[128:131], v[4:7], v[164:167], v[128:131]\n"
    "v_exp_f32 v145, v145 clamp\n"
    "v_mfma_f32_16x16x32_bf16 v[132:135], v[8:11], v[160:163], v[116:119]\n"
    "v_exp_f32 v146, v146 clamp\n"
    "v_mfma_f32_16x16x32_bf16 v[132:135], v[12:15], v[164:167], v[132:135]\n"
    "v_exp_f32 v147, v147 clamp\n"
    "v_mfma_f32_16x16x32_bf16 v[136:139], v[16:19], v[160:163], v[120:123]\n"
    "v_exp_f32 v148, v148 clamp\n"
    "v_mfma_f32_16x16x32_bf16 v[136:139], v[20:23], v[164:167], v[136:139]\n"
    "v_exp_f32 v149, v149 clamp\n"
    "v_mfma_f32_16x16x32_bf16 v[140:143], v[24:27], v[160:163], v[124:127]\n"
    "v_exp_f32 v150, v150 clamp\n"
    "v_mfma_f32_16x16x32_bf16 v[140:143], v[28:31], v[164:167], v[140:143]\n"
    "v_exp_f32 v151, v151 clamp\n"
    "v_exp_f32 v152, v152 clamp\n"
    "s_cmp_eq_u32 s67, s80\n"
    "s_cselect_b32 s67, s65, s67\n"
    "s_add_u32 m0, s68, 4096\n"
    "v_exp_f32 v153, v153 clamp\n"
    "v_add_u32_e64 v253, s67, v244\n"
    "s_waitcnt lgkmcnt(0)\n"
    "v_mfma_f32_4x4x4_16b_bf16 v[224:227], v[228:229], v[168:169], v[224:227] cbsz:4 abid:0\n"
    "v_exp_f32 v154, v154 clamp\n"
    "v_exp_f32 v155, v155 clamp\n"
    "v_mfma_f32_4x4x4_16b_bf16 v[224:227], v[228:229], v[170:171], v[224:227] cbsz:4 abid:1\n"
    "v_exp_f32 v156, v156 clamp\n"
    "v_exp_f32 v157, v157 clamp\n"
    "v_mfma_f32_4x4x4_16b_bf16 v[224:227], v[228:229], v[172:173], v[224:227] cbsz:4 abid:2\n"
    "v_exp_f32 v158, v158 clamp\n"
    "v_exp_f32 v159, v159 clamp\n"
    "v_mfma_f32_4x4x4_16b_bf16 v[224:227], v[228:229], v[174:175], v[224:227] cbsz:4 abid:3\n"
    "s_waitcnt vmcnt(4)\n"
    "s_memrealtime s[34:35]\n"
    "ds_read_b64_tr_b16 v[168:169], v240 offset:4096\n"
    "ds_read_b64_tr_b16 v[170:171], v241 offset:4352\n"
    "ds_read_b64_tr_b16 v[172:173], v242 offset:4608\n"
    "ds_read_b64_tr_b16 v[174:175], v243 offset:4864\n"
    "ds_read_b32 v236, v253 offset:0\n"
    "ds_read_b32 v238, v253 offset:4\n"
    "ds_read_b128 v[160:163], v234 offset:6144\n"
    "ds_read_b128 v[164:167], v235 offset:6144\n"
    "v_pk_fma_f32 v[144:145], v[144:145], s[70:71], v[96:97]\n"
    "v_pk_fma_f32 v[146:147], v[146:147], s[70:71], v[98:99]\n"
    "v_pk_add_f32 v[230:231], v[144:145], v[146:147]\n"
    "v_pk_mul_f32 v[232:233], v[144:145], v[146:147]\n"
    "v_pk_fma_f32 v[148:149], v[148:149], s[70:71], v[100:101]\n"
    "v_pk_fma_f32 v[230:231], v[230:231], v[148:149], v[232:233]\n"
    "v_pk_mul_f32 v[232:233], v[232:233], v[148:149]\n"
    "v_pk_fma_f32 v[150:151], v[150:151], s[70:71], v[102:103]\n"
    "v_pk_fma_f32 v[230:231], v[230:231], v[150:151], v[232:233]\n"
    "v_pk_mul_f32 v[232:233], v[232:233], v[150:151]\n"
    "v_pk_fma_f32 v[152:153], v[152:153], s[70:71], v[104:105]\n"
    "v_pk_fma_f32 v[230:231], v[230:231], v[152:153], v[232:233]\n"
    "v_pk_mul_f32 v[232:233], v[232:233], v[152:153]\n"
    "v_pk_fma_f32 v[154:155], v[154:155], s[70:71], v[106:107]\n"
    "v_pk_fma_f32 v[230:231], v[230:231], v[154:155], v[232:233]\n"
    "v_pk_mul_f32 v[232:233], v[232:233], v[154:155]\n"
    "v_pk_fma_f32 v[156:157], v[156:157], s[70:71], v[108:109]\n"
    "v_pk_fma_f32 v[230:231], v[230:231], v[156:157], v[232:233]\n"
    "v_pk_mul_f32 v[232:233], v[232:233], v[156:157]\n"
    "v_pk_fma_f32 v[158:159], v[158:159], s[70:71], v[110:111]\n"
    "v_pk_fma_f32 v[230:231], v[230:231], v[158:159], v[232:233]\n"
    "v_pk_mul_f32 v[232:233], v[232:233], v[158:159]\n"
    "v_rcp_f32 v232, v232\n"
    "v_rcp_f32 v233, v233\n"
    "v_fma_f32 v230, v230, v232, v247\n"
    "v_fmac_f32 v230, v231, v233\n"
    "v_lshlrev_b32 v232, 2, v249\n"
    "v_lshrrev_b32 v233, 2, v249\n"
    "v_and_b32 v232, 56, v232\n"
    "v_and_b32 v233, 4, v233\n"
    "v_add3_u32 v232, v232, v233, s81\n"
    "ds_read_b32 v232, v232\n"
    "s_add_u32 s81, s81, 64\n"
    "s_waitcnt lgkmcnt(0)\n"
    "v_lshlrev_b32 v232, 1, v232\n"
    "buffer_load_ushort v232, v232, s[44:47], 0 offen\n"
    "s_waitcnt vmcnt(0)\n"
    "v_lshlrev_b32 v232, 16, v232\n"
    "v_mul_f32 v230, v230, v232\n"
    "v_cvt_pk_bf16_f32 v230, v230, v230\n"
    "s_add_u32 s67, s67, 64\n"
    "s_waitcnt lgkmcnt(0)\n"
    "ds_write_b16 v245, v230\n"
    "ds_read_b64 v[228:229], v246\n"
    "s_cmp_le_u32 s34, s76\n"
    "s_cbranch_scc1 .Ldin_G1s_wait\n"
    ".Ldin_G1s_go:\n"
    "s_add_u32 s76, s76, s72\n"
    ".Ldin_G1s_dma:\n"
    "buffer_load_dwordx4 v[236:237], s[40:43], 0 idxen offen offset:0 nt lds\n"
    "buffer_load_dwordx4 v[238:239], s[40:43], 0 idxen offen offset:1024 nt lds\n"
    ".Ldin_G1s_nd:\n"
    "s_cmp_eq_u32 s67, s82\n"
    "s_cbranch_scc1 .Ldin_L2s\n"
    "s_branch .Ldin_G2s\n"
    ".Ldin_B1s_wait:\n"
    "s_cmp_lt_i32 s59, s57\n"
    "s_cbranch_scc1 .Ldin_B1s_w2\n"
    "s_sub_u32 s94, s67, s65\n"
    "s_cmp_gt_u32 s94, 256\n"
    "s_cbranch_scc1 .Ldin_B1s_dma\n"
    "s_cmp_eq_u32 s94, 64\n"
    "s_waitcnt vmcnt(2)\n"
    "s_cbranch_scc1 .Ldin_B1s_nd\n"
    "s_waitcnt vmcnt(0)\n"
    "s_branch .Ldin_B1s_nd\n"
    ".Ldin_B1s_slp:\n"
    "s_sleep 1\n"
    ".Ldin_B1s_w2:\n"
    "s_memrealtime s[34:35]\n"
    "s_waitcnt lgkmcnt(0)\n"
    "s_sub_u32 s94, s34, s76\n"
    "s_cmp_lt_i32 s94, 0\n"
    "s_cbranch_scc1 .Ldin_B1s_slp\n"
    "s_branch .Ldin_B1s_go\n"
    ".Ldin_G2s_wait:\n"
    "s_cmp_lt_i32 s59, s57\n"
    "s_cbranch_scc1 .Ldin_G2s_w2\n"
    "s_sub_u32 s94, s67, s65\n"
    "s_cmp_gt_u32 s94, 256\n"
    "s_cbranch_scc1 .Ldin_G2s_dma\n"
    "s_cmp_eq_u32 s94, 64\n"
    "s_waitcnt vmcnt(2)\n"
    "s_cbranch_scc1 .Ldin_G2s_nd\n"
    "s_waitcnt vmcnt(0)\n"
    "s_branch .Ldin_G2s_nd\n"
    ".Ldin_G2s_slp:\n"
    "s_sleep 1\n"
    ".Ldin_G2s_w2:\n"
    "s_memrealtime s[34:35]\n"
    "s_waitcnt lgkmcnt(0)\n"
    "s_sub_u32 s94, s34, s76\n"
    "s_cmp_lt_i32 s94, 0\n"
    "s_cbranch_scc1 .Ldin_G2s_slp\n"
    "s_branch .Ldin_G2s_go\n"
    ".Ldin_G3s_wait:\n"
    "s_cmp_lt_i32 s59, s57\n"
    "s_cbranch_scc1 .Ldin_G3s_w2\n"
    "s_sub_u32 s94, s67, s65\n"
    "s_cmp_gt_u32 s94, 256\n"
    "s_cbranch_scc1 .Ldin_G3s_dma\n"
    "s_cmp_eq_u32 s94, 64\n"
    "s_waitcnt vmcnt(2)\n"
    "s_cbranch_scc1 .Ldin_G3s_nd\n"
    "s_waitcnt vmcnt(0)\n"
    "s_branch .Ldin_G3s_nd\n"
    ".Ldin_G3s_slp:\n"
    "s_sleep 1\n"
    ".Ldin_G3s_w2:\n"
    "s_memrealtime s[34:35]\n"
    "s_waitcnt lgkmcnt(0)\n"
    "s_sub_u32 s94, s34, s76\n"
    "s_cmp_lt_i32 s94, 0\n"
    "s_cbranch_scc1 .Ldin_G3s_slp\n"
    "s_branch .Ldin_G3s_go\n"
    ".Ldin_G0s_wait:\n"
    "s_cmp_lt_i32 s59, s57\n"
    "s_cbranch_scc1 .Ldin_G0s_w2\n"
    "s_sub_u32 s94, s67, s65\n"
    "s_cmp_gt_u32 s94, 256\n"
    "s_cbranch_scc1 .Ldin_G0s_dma\n"
    "s_cmp_eq_u32 s94, 64\n"
    "s_waitcnt vmcnt(2)\n"
    "s_cbranch_scc1 .Ldin_G0s_nd\n"
    "s_waitcnt vmcnt(0)\n"
    "s_branch .Ldin_G0s_nd\n"
    ".Ldin_G0s_slp:\n"
    "s_sleep 1\n"
    ".Ldin_G0s_w2:\n"
    "s_memrealtime s[34:35]\n"
    "s_waitcnt lgkmcnt(0)\n"
    "s_sub_u32 s94, s34, s76\n"
    "s_cmp_lt_i32 s94, 0\n"
    "s_cbranch_scc1 .Ldin_G0s_slp\n"
    "s_branch .Ldin_G0s_go\n"
    ".Ldin_G1s_wait:\n"
    "s_cmp_lt_i32 s59, s57\n"
    "s_cbranch_scc1 .Ldin_G1s_w2\n"
    "s_sub_u32 s94, s67, s65\n"
    "s_cmp_gt_u32 s94, 256\n"
    "s_cbranch_scc1 .Ldin_G1s_dma\n"
    "s_cmp_eq_u32 s94, 64\n"
    "s_waitcnt vmcnt(2)\n"
    "s_cbranch_scc1 .Ldin_G1s_nd\n"
    "s_waitcnt vmcnt(0)\n"
    "s_branch .Ldin_G1s_nd\n"
    ".Ldin_G1s_slp:\n"
    "s_sleep 1\n"
    ".Ldin_G1s_w2:\n"
    "s_memrealtime s[34:35]\n"
    "s_waitcnt lgkmcnt(0)\n"
    "s_sub_u32 s94, s34, s76\n"
    "s_cmp_lt_i32 s94, 0\n"
    "s_cbranch_scc1 .Ldin_G1s_slp\n"
    "s_branch .Ldin_G1s_go\n"
    ".p2align 5\n"
    ".Ldin_L2s:\n"
    "s_setprio 0\n"
    "s_cmp_ge_i32 s59, s57\n"
    "s_cbranch_scc1 .Ldin_E0s\n"
    "s_add_u32 s76, s76, s73\n"
    "v_mov_b32 v253, s63\n"
    "v_and_b32 v184, v254, v184\n"
    "v_and_b32 v185, v255, v185\n"
    "v_and_b32 v186, v254, v186\n"
    "v_and_b32 v187, v255, v187\n"
    "v_and_b32 v188, v254, v188\n"
    "v_and_b32 v189, v255, v189\n"
    "v_and_b32 v190, v254, v190\n"
    "v_and_b32_e64 v191, v255, v191\n"
    "ds_read_b64 v[232:233], v253\n"
    "v_mfma_f32_4x4x4_16b_bf16 v[144:147], v[184:185], v[32:33], v[64:67]\n"
    "ds_read_b128 v[112:115], v252 offset:17408\n"
    "v_mfma_f32_4x4x4_16b_bf16 v[148:151], v[186:187], v[34:35], v[68:71]\n"
    "ds_read_b128 v[116:119], v252 offset:17424\n"
    "v_mfma_f32_4x4x4_16b_bf16 v[152:155], v[188:189], v[36:37], v[72:75]\n"
    "ds_read_b128 v[120:123], v252 offset:17536\n"
    "ds_read_b128 v[124:127], v252 offset:17552\n"
    "v_cvt_pk_bf16_f32 v0, v144, v145\n"
    "v_cvt_pk_bf16_f32 v1, v146, v147\n"
    "v_mfma_f32_4x4x4_16b_bf16 v[156:159], v[190:191], v[38:39], v[76:79]\n"
    "v_cvt_pk_bf16_f32 v2, v148, v149\n"
    "v_cvt_pk_bf16_f32 v3, v150, v151\n"
    "v_mfma_f32_4x4x4_16b_bf16 v[144:147], v[184:185], v[40:41], v[80:83]\n"
    "v_cvt_pk_bf16_f32 v4, v152, v153\n"
    "v_cvt_pk_bf16_f32 v5, v154, v155\n"
    "v_mfma_f32_4x4x4_16b_bf16 v[148:151], v[186:187], v[42:43], v[84:87]\n"
    "v_cvt_pk_bf16_f32 v6, v156, v157\n"
    "v_cvt_pk_bf16_f32 v7, v158, v159\n"
    "v_mfma_f32_4x4x4_16b_bf16 v[152:155], v[188:189], v[44:45], v[88:91]\n"
    "v_cvt_pk_bf16_f32 v8, v144, v145\n"
    "v_cvt_pk_bf16_f32 v9, v146, v147\n"
    "v_mfma_f32_4x4x4_16b_bf16 v[156:159], v[190:191], v[46:47], v[92:95]\n"
    "v_cvt_pk_bf16_f32 v10, v148, v149\n"
    "v_cvt_pk_bf16_f32 v11, v150, v151\n"
    "v_mfma_f32_4x4x4_16b_bf16 v[144:147], v[184:185], v[48:49], v[192:195]\n"
    "v_cvt_pk_bf16_f32 v12, v152, v153\n"
    "v_cvt_pk_bf16_f32 v13, v154, v155\n"
    "v_mfma_f32_4x4x4_16b_bf16 v[148:151], v[186:187], v[50:51], v[196:199]\n"
    "v_cvt_pk_bf16_f32 v14, v156, v157\n"
    "v_cvt_pk_bf16_f32 v15, v158, v159\n"
    "v_mfma_f32_4x4x4_16b_bf16 v[152:155], v[188:189], v[52:53], v[200:203]\n"
    "s_waitcnt lgkmcnt(0)\n"
    "v_readfirstlane_b32 s60, v232\n"
    "v_cvt_pk_bf16_f32 v16, v144, v145\n"
    "v_cvt_pk_bf16_f32 v17, v146, v147\n"
    "v_mfma_f32_4x4x4_16b_bf16 v[156:159], v[190:191], v[54:55], v[204:207]\n"
    "s_cmp_eq_u32 s60, -1\n"
    "s_cbranch_scc1 .Ldin_L2s_poll\n"
    ".Ldin_L2s_pr:\n"
    "v_readfirstlane_b32 s95, v233\n"
    "s_and_b32 s94, s60, 63\n"
    "s_lshl_b32 s94, s94, 6\n"
    "s_add_u32 s94, s94, 68480\n"
    "s_add_u32 s63, s63, 8\n"
    "v_lshl_add_u32 v252, v249, 1, s94\n"
    "ds_read_b32 v252, v252\n"
    "s_sub_u32 s95, s95, s76\n"
    "s_ashr_i32 s95, s95, 4\n"
    "s_min_i32 s95, s95, s74\n"
    "s_max_i32 s72, s95, s75\n"
    "v_cvt_pk_bf16_f32 v18, v148, v149\n"
    "v_cvt_pk_bf16_f32 v19, v150, v151\n"
    "v_mfma_f32_4x4x4_16b_bf16 v[144:147], v[184:185], v[56:57], v[208:211]\n"
    "v_cvt_pk_bf16_f32 v20, v152, v153\n"
    "v_cvt_pk_bf16_f32 v21, v154, v155\n"
    "v_mfma_f32_4x4x4_16b_bf16 v[148:151], v[186:187], v[58:59], v[212:215]\n"
    "v_cvt_pk_bf16_f32 v22, v156, v157\n"
    "v_cvt_pk_bf16_f32 v23, v158, v159\n"
    "v_mfma_f32_4x4x4_16b_bf16 v[152:155], v[188:189], v[60:61], v[216:219]\n"
    "v_cvt_pk_bf16_f32 v24, v144, v145\n"
    "v_cvt_pk_bf16_f32 v25, v146, v147\n"
    "v_mfma_f32_4x4x4_16b_bf16 v[156:159], v[190:191], v[62:63], v[220:223]\n"
    "v_cvt_pk_bf16_f32 v26, v148, v149\n"
    "v_cvt_pk_bf16_f32 v27, v150, v151\n"
    "v_cvt_pk_bf16_f32 v28, v152, v153\n"
    "v_cvt_pk_bf16_f32 v29, v154, v155\n"
    "v_exp_f32 v136, v136 clamp\n"
    "v_cvt_pk_bf16_f32 v30, v156, v157\n"
    "v_cvt_pk_bf16_f32 v31, v158, v159\n"
    "v_mfma_f32_16x16x32_bf16 v[144:147], v[0:3], v[160:163], v[112:115]\n"
    "v_exp_f32 v128, v128 clamp\n"
    "v_mfma_f32_16x16x32_bf16 v[144:147], v[4:7], v[164:167], v[144:147]\n"
    "v_exp_f32 v129, v129 clamp\n"
    "v_mfma_f32_16x16x32_bf16 v[148:151], v[8:11], v[160:163], v[116:119]\n"
    "v_exp_f32 v130, v130 clamp\n"
    "v_mfma_f32_16x16x32_bf16 v[148:151], v[12:15], v[164:167], v[148:151]\n"
    "v_exp_f32 v131, v131 clamp\n"
    "v_mfma_f32_16x16x32_bf16 v[152:155], v[16:19], v[160:163], v[120:123]\n"
    "v_exp_f32 v132, v132 clamp\n"
    "v_mfma_f32_16x16x32_bf16 v[152:155], v[20:23], v[164:167], v[152:155]\n"
    "v_exp_f32 v133, v133 clamp\n"
    "v_mfma_f32_16x16x32_bf16 v[156:159], v[24:27], v[160:163], v[124:127]\n"
    "v_exp_f32 v134, v134 clamp\n"
    "v_mfma_f32_16x16x32_bf16 v[156:159], v[28:31], v[164:167], v[156:159]\n"
    "v_exp_f32 v135, v135 clamp\n"
    "s_add_u32 m0, s68, 6144\n"
    "v_exp_f32 v137, v137 clamp\n"
    "v_add_u32 v253, s67, v244\n"
    "v_mfma_f32_4x4x4_16b_bf16 v[224:227], v[228:229], v[176:177], v[224:227] cbsz:4 abid:0\n"
    "v_exp_f32 v138, v138 clamp\n"
    "v_exp_f32 v139, v139 clamp\n"
    "v_mfma_f32_4x4x4_16b_bf16 v[224:227], v[228:229], v[178:179], v[224:227] cbsz:4 abid:1\n"
    "v_exp_f32 v140, v140 clamp\n"
    "v_exp_f32 v141, v141 clamp\n"
    "v_mfma_f32_4x4x4_16b_bf16 v[224:227], v[228:229], v[180:181], v[224:227] cbsz:4 abid:2\n"
    "v_exp_f32 v142, v142 clamp\n"
    "v_exp_f32 v143, v143 clamp\n"
    "v_mfma_f32_4x4x4_16b_bf16 v[224:227], v[228:229], v[182:183], v[224:227] cbsz:4 abid:3\n"
    "s_waitcnt vmcnt(4)\n"
    "ds_read_b64_tr_b16 v[176:177], v240 offset:6144\n"
    "ds_read_b64_tr_b16 v[178:179], v241 offset:6400\n"
    "ds_read_b64_tr_b16 v[180:181], v242 offset:6656\n"
    "ds_read_b64_tr_b16 v[182:183], v243 offset:6912\n"
    "ds_read_b32 v236, v253 offset:0\n"
    "ds_read_b32 v238, v253 offset:4\n"
    "ds_read_b128 v[160:163], v234 offset:0\n"
    "ds_read_b128 v[164:167], v235 offset:0\n"
    "s_waitcnt lgkmcnt(8)\n"
    "s_nop 0\n"
    "v_readlane_b32 s20, v252, 0\n"
    "v_readlane_b32 s21, v252, 1\n"
    "v_readlane_b32 s22, v252, 2\n"
    "v_readlane_b32 s23, v252, 3\n"
    "v_readlane_b32 s24, v252, 4\n"
    "v_readlane_b32 s25, v252, 5\n"
    "v_readlane_b32 s26, v252, 6\n"
    "v_readlane_b32 s27, v252, 7\n"
    "v_pk_fma_f32 v[128:129], v[128:129], s[70:71], v[96:97]\n"
    "v_pk_fma_f32 v[130:131], v[130:131], s[70:71], v[98:99]\n"
    "v_pk_add_f32 v[230:231], v[128:129], v[130:131]\n"
    "v_pk_mul_f32 v[232:233], v[128:129], v[130:131]\n"
    "v_pk_fma_f32 v[132:133], v[132:133], s[70:71], v[100:101]\n"
    "v_pk_fma_f32 v[230:231], v[230:231], v[132:133], v[232:233]\n"
    "v_pk_mul_f32 v[232:233], v[232:233], v[132:133]\n"
    "v_pk_fma_f32 v[134:135], v[134:135], s[70:71], v[102:103]\n"
    "v_pk_fma_f32 v[230:231], v[230:231], v[134:135], v[232:233]\n"
    "v_pk_mul_f32 v[232:233], v[232:233], v[134:135]\n"
    "v_pk_fma_f32 v[136:137], v[136:137], s[70:71], v[104:105]\n"
    "v_pk_fma_f32 v[230:231], v[230:231], v[136:137], v[232:233]\n"
    "v_pk_mul_f32 v[232:233], v[232:233], v[136:137]\n"
    "v_pk_fma_f32 v[138:139], v[138:139], s[70:71], v[106:107]\n"
    "v_pk_fma_f32 v[230:231], v[230:231], v[138:139], v[232:233]\n"
    "v_pk_mul_f32 v[232:233], v[232:233], v[138:139]\n"
    "v_pk_fma_f32 v[140:141], v[140:141], s[70:71], v[108:109]\n"
    "v_pk_fma_f32 v[230:231], v[230:231], v[140:141], v[232:233]\n"
    "v_pk_mul_f32 v[232:233], v[232:233], v[140:141]\n"
    "v_pk_fma_f32 v[142:143], v[142:143], s[70:71], v[110:111]\n"
    "v_pk_fma_f32 v[230:231], v[230:231], v[142:143], v[232:233]\n"
    "v_pk_mul_f32 v[232:233], v[232:233], v[142:143]\n"
    "v_rcp_f32 v232, v232\n"
    "v_rcp_f32 v233, v233\n"
    "v_fma_f32 v230, v230, v232, v247\n"
    "v_fmac_f32 v230, v231, v233\n"
    "v_lshlrev_b32 v232, 2, v249\n"
    "v_lshrrev_b32 v233, 2, v249\n"
    "v_and_b32 v232, 56, v232\n"
    "v_and_b32 v233, 4, v233\n"
    "v_add3_u32 v232, v232, v233, s81\n"
    "ds_read_b32 v232, v232\n"
    "s_add_u32 s81, s81, 64\n"
    "s_waitcnt lgkmcnt(0)\n"
    "v_lshlrev_b32 v232, 1, v232\n"
    "buffer_load_ushort v232, v232, s[44:47], 0 offen\n"
    "s_waitcnt vmcnt(0)\n"
    "v_lshlrev_b32 v232, 16, v232\n"
    "v_mul_f32 v230, v230, v232\n"
    "v_cvt_pk_bf16_f32 v230, v230, v230\n"
    "s_add_u32 s67, s67, 64\n"
    "s_waitcnt lgkmcnt(0)\n"
    "ds_write_b16 v245, v230\n"
    "ds_read_b64 v[228:229], v246\n"
    "s_lshl1_add_u32 s76, s72, s76\n"
    "buffer_load_dwordx4 v[236:237], s[40:43], 0 idxen offen offset:0 nt lds\n"
    "buffer_load_dwordx4 v[238:239], s[40:43], 0 idxen offen offset:1024 nt lds\n"
    "s_and_b32 s62, s58, 63\n"
    "s_mul_i32 s62, s62, 272\n"
    "s_mov_b32 s58, s59\n"
    "s_mov_b32 s59, s60\n"
    "s_mov_b32 s81, s65\n"
    "s_xor_b32 s65, s65, s64\n"
    "s_mov_b32 s80, s83\n"
    "s_add_u32 s82, s65, 256\n"
    "s_cmp_lt_i32 s59, s57\n"
    "s_cselect_b32 s76, s76, -1\n"
    "s_bitcmp1_b32 s58, 30\n"
    "s_cbranch_scc0 .Ldin_A3\n"
    ".Ldin_A3s:\n"
    "v_mfma_f32_16x16x32_bf16 v[128:131], v[0:3], v[160:163], v[112:115]\n"
    "v_exp_f32 v144, v144 clamp\n"
    "v_mfma_f32_16x16x32_bf16 v[128:131], v[4:7], v[164:167], v[128:131]\n"
    "v_exp_f32 v145, v145 clamp\n"
    "v_mfma_f32_16x16x32_bf16 v[132:135], v[8:11], v[160:163], v[116:119]\n"
    "v_exp_f32 v146, v146 clamp\n"
    "v_mfma_f32_16x16x32_bf16 v[132:135], v[12:15], v[164:167], v[132:135]\n"
    "v_exp_f32 v147, v147 clamp\n"
    "v_mfma_f32_16x16x32_bf16 v[136:139], v[16:19], v[160:163], v[120:123]\n"
    "v_exp_f32 v148, v148 clamp\n"
    "v_mfma_f32_16x16x32_bf16 v[136:139], v[20:23], v[164:167], v[136:139]\n"
    "v_exp_f32 v149, v149 clamp\n"
    "v_mfma_f32_16x16x32_bf16 v[140:143], v[24:27], v[160:163], v[124:127]\n"
    "v_exp_f32 v150, v150 clamp\n"
    "v_mfma_f32_16x16x32_bf16 v[140:143], v[28:31], v[164:167], v[140:143]\n"
    "v_exp_f32 v151, v151 clamp\n"
    "v_exp_f32 v152, v152 clamp\n"
    "s_add_u32 m0, s68, 0\n"
    "v_exp_f32 v153, v153 clamp\n"
    "v_add_u32 v253, s67, v244\n"
    "s_waitcnt lgkmcnt(0)\n"
    "v_mfma_f32_4x4x4_16b_bf16 v[224:227], v[228:229], v[168:169], v[224:227] cbsz:4 abid:0\n"
    "v_exp_f32 v154, v154 clamp\n"
    "v_exp_f32 v155, v155 clamp\n"
    "v_mfma_f32_4x4x4_16b_bf16 v[224:227], v[228:229], v[170:171], v[224:227] cbsz:4 abid:1\n"
    "v_exp_f32 v156, v156 clamp\n"
    "v_exp_f32 v157, v157 clamp\n"
    "v_mfma_f32_4x4x4_16b_bf16 v[224:227], v[228:229], v[172:173], v[224:227] cbsz:4 abid:2\n"
    "v_exp_f32 v158, v158 clamp\n"
    "v_exp_f32 v159, v159 clamp\n"
    "v_mfma_f32_4x4x4_16b_bf16 v[224:227], v[228:229], v[174:175], v[224:227] cbsz:4 abid:3\n"
    "s_waitcnt vmcnt(4)\n"
    "ds_read_b64_tr_b16 v[168:169], v240 offset:0\n"
    "ds_read_b64_tr_b16 v[170:171], v241 offset:256\n"
    "ds_read_b64_tr_b16 v[172:173], v242 offset:512\n"
    "ds_read_b64_tr_b16 v[174:175], v243 offset:768\n"
    "ds_read_b32 v236, v253 offset:0\n"
    "ds_read_b32 v238, v253 offset:4\n"
    "ds_read_b128 v[160:163], v234 offset:2048\n"
    "ds_read_b128 v[164:167], v235 offset:2048\n"
    "s_cbranch_vccz .Ldin_A3s_np\n"
    "s_setprio 1\n"
    ".Ldin_A3s_np:\n"
    "v_pk_fma_f32 v[144:145], v[144:145], s[70:71], v[96:97]\n"
    "v_pk_fma_f32 v[146:147], v[146:147], s[70:71], v[98:99]\n"
    "v_add_f32 v191, v224, v225\n"
    "v_add_f32 v190, v226, v227\n"
    "v_add_f32 v191, v191, v190\n"
    "v_add_u32 v190, s62, v249\n"
    "v_mul_f32 v191, s69, v191\n"
    "v_cvt_pk_bf16_f32 v191, v191, v191\n"
    "ds_write_b16 v190, v191 offset:32768\n"
    "s_bfe_u32 s94, s59, 0x180006\n"
    "s_mul_i32 s86, s94, s56\n"
    "v_mbcnt_lo_u32_b32 v184, s20, 0\n"
    "v_mbcnt_lo_u32_b32 v185, s22, 0\n"
    "v_mbcnt_lo_u32_b32 v186, s24, 0\n"
    "v_mbcnt_lo_u32_b32 v187, s26, 0\n"
    "s_bcnt1_i32_b64 s90, s[20:21]\n"
    "s_bcnt1_i32_b64 s91, s[22:23]\n"
    "s_bcnt1_i32_b64 s92, s[24:25]\n"
    "s_bcnt1_i32_b64 s93, s[26:27]\n"
    "v_mbcnt_hi_u32_b32 v184, s21, v184\n"
    "v_mbcnt_hi_u32_b32 v185, s23, v185\n"
    "v_mbcnt_hi_u32_b32 v186, s25, v186\n"
    "v_mbcnt_hi_u32_b32 v187, s27, v187\n"
    "s_lshl2_add_u32 s90, s90, s65\n"
    "s_lshl2_add_u32 s91, s91, s90\n"
    "s_lshl2_add_u32 s92, s92, s91\n"
    "s_lshl2_add_u32 s93, s93, s92\n"
    "v_add_u32_e64 v188, s86, v251\n"
    "v_pk_add_f32 v[230:231], v[144:145], v[146:147]\n"
    "v_pk_mul_f32 v[232:233], v[144:145], v[146:147]\n"
    "v_pk_fma_f32 v[148:149], v[148:149], s[70:71], v[100:101]\n"
    "v_pk_fma_f32 v[230:231], v[230:231], v[148:149], v[232:233]\n"
    "v_pk_mul_f32 v[232:233], v[232:233], v[148:149]\n"
    "v_lshl_add_u32 v184, v184, 2, s65\n"
    "v_lshl_add_u32 v185, v185, 2, s90\n"
    "v_lshl_add_u32 v186, v186, 2, s91\n"
    "v_lshl_add_u32 v187, v187, 2, s92\n"
    "v_mov_b32 v191, -1\n"
    "v_lshl_add_u32 v190, v249, 1, s93\n"
    "ds_write2_b32 v190, v191, v191 offset1:64\n"
    "s_mov_b64 exec, s[20:21]\n"
    "ds_write_b32 v184, v188\n"
    "s_mov_b64 exec, s[22:23]\n"
    "v_add_u32 v189, 64, v188\n"
    "ds_write_b32 v185, v189\n"
    "s_mov_b64 exec, s[24:25]\n"
    "v_add_u32 v190, 128, v188\n"
    "ds_write_b32 v186, v190\n"
    "s_mov_b64 exec, s[26:27]\n"
    "v_add_u32 v189, 192, v188\n"
    "ds_write_b32 v187, v189\n"
    "s_mov_b64 exec, -1\n"
    "s_add_u32 s95, s93, 60\n"
    "s_add_u32 s94, s82, 128\n"
    "s_andn2_b32 s95, s95, 63\n"
    "s_max_u32 s83, s95, s94\n"
    "v_pk_fma_f32 v[150:151], v[150:151], s[70:71], v[102:103]\n"
    "v_pk_fma_f32 v[230:231], v[230:231], v[150:151], v[232:233]\n"
    "v_pk_mul_f32 v[232:233], v[232:233], v[150:151]\n"
    "v_pk_fma_f32 v[152:153], v[152:153], s[70:71], v[104:105]\n"
    "v_pk_fma_f32 v[230:231], v[230:231], v[152:153], v[232:233]\n"
    "v_pk_mul_f32 v[232:233], v[232:233], v[152:153]\n"
    "v_pk_fma_f32 v[154:155], v[154:155], s[70:71], v[106:107]\n"
    "v_pk_fma_f32 v[230:231], v[230:231], v[154:155], v[232:233]\n"
    "v_pk_mul_f32 v[232:233], v[232:233], v[154:155]\n"
    "v_pk_fma_f32 v[156:157], v[156:157], s[70:71], v[108:109]\n"
    "v_pk_fma_f32 v[230:231], v[230:231], v[156:157], v[232:233]\n"
    "v_pk_mul_f32 v[232:233], v[232:233], v[156:157]\n"
    "v_pk_fma_f32 v[158:159], v[158:159], s[70:71], v[110:111]\n"
    "v_pk_fma_f32 v[230:231], v[230:231], v[158:159], v[232:233]\n"
    "v_pk_mul_f32 v[232:233], v[232:233], v[158:159]\n"
    "v_rcp_f32 v232, v232\n"
    "v_rcp_f32 v233, v233\n"
    "v_fma_f32 v230, v230, v232, v247\n"
    "v_fmac_f32 v230, v231, v233\n"
    "v_lshlrev_b32 v232, 2, v249\n"
    "v_lshrrev_b32 v233, 2, v249\n"
    "v_and_b32 v232, 56, v232\n"
    "v_and_b32 v233, 4, v233\n"
    "v_add3_u32 v232, v232, v233, s81\n"
    "ds_read_b32 v232, v232\n"
    "s_add_u32 s81, s81, 64\n"
    "s_waitcnt lgkmcnt(0)\n"
    "v_lshlrev_b32 v232, 1, v232\n"
    "buffer_load_ushort v232, v232, s[44:47], 0 offen\n"
    "s_waitcnt vmcnt(0)\n"
    "v_lshlrev_b32 v232, 16, v232\n"
    "v_mul_f32 v230, v230, v232\n"
    "v_cvt_pk_bf16_f32 v230, v230, v230\n"
    "s_add_u32 s67, s67, 64\n"
    "s_waitcnt lgkmcnt(0)\n"
    "ds_write_b16 v245, v230\n"
    "ds_read_b64 v[228:229], v246\n"
    "buffer_load_dwordx4 v[236:237], s[40:43], 0 idxen offen offset:0 nt lds\n"
    "buffer_load_dwordx4 v[238:239], s[40:43], 0 idxen offen offset:1024 nt lds\n"
    ".Ldin_B0s:\n"
    "v_mfma_f32_16x16x32_bf16 v[144:147], v[0:3], v[160:163], v[112:115]\n"
    "v_exp_f32 v128, v128 clamp\n"
    "v_mfma_f32_16x16x32_bf16 v[144:147], v[4:7], v[164:167], v[144:147]\n"
    "v_exp_f32 v129, v129 clamp\n"
    "v_mfma_f32_16x16x32_bf16 v[148:151], v[8:11], v[160:163], v[116:119]\n"
    "v_exp_f32 v130, v130 clamp\n"
    "v_mfma_f32_16x16x32_bf16 v[148:151], v[12:15], v[164:167], v[148:151]\n"
    "v_exp_f32 v131, v131 clamp\n"
    "v_mfma_f32_16x16x32_bf16 v[152:155], v[16:19], v[160:163], v[120:123]\n"
    "v_exp_f32 v132, v132 clamp\n"
    "v_mfma_f32_16x16x32_bf16 v[152:155], v[20:23], v[164:167], v[152:155]\n"
    "v_exp_f32 v133, v133 clamp\n"
    "v_mfma_f32_16x16x32_bf16 v[156:159], v[24:27], v[160:163], v[124:127]\n"
    "v_exp_f32 v134, v134 clamp\n"
    "v_mfma_f32_16x16x32_bf16 v[156:159], v[28:31], v[164:167], v[156:159]\n"
    "v_exp_f32 v135, v135 clamp\n"
    "v_exp_f32 v136, v136 clamp\n"
    "s_cmp_eq_u32 s67, s80\n"
    "s_cselect_b32 s67, s65, s67\n"
    "s_and_b32 s94, s59, 63\n"
    "s_mul_i32 s94, s94, 272\n"
    "v_add_u32 v252, s94, v250\n"
    "s_add_u32 m0, s68, 2048\n"
    "v_exp_f32 v137, v137 clamp\n"
    "v_add_u32 v253, s67, v244\n"
    "s_waitcnt lgkmcnt(0)\n"
    "v_mfma_f32_4x4x4_16b_bf16 v[224:227], v[228:229], v[176:177], 0 cbsz:4 abid:0\n"
    "v_exp_f32 v138, v138 clamp\n"
    "v_exp_f32 v139, v139 clamp\n"
    "v_mfma_f32_4x4x4_16b_bf16 v[224:227], v[228:229], v[178:179], v[224:227] cbsz:4 abid:1\n"
    "v_exp_f32 v140, v140 clamp\n"
    "v_exp_f32 v141, v141 clamp\n"
    "v_mfma_f32_4x4x4_16b_bf16 v[224:227], v[228:229], v[180:181], v[224:227] cbsz:4 abid:2\n"
    "v_exp_f32 v142, v142 clamp\n"
    "v_exp_f32 v143, v143 clamp\n"
    "v_mfma_f32_4x4x4_16b_bf16 v[224:227], v[228:229], v[182:183], v[224:227] cbsz:4 abid:3\n"
    "s_waitcnt vmcnt(4)\n"
    "s_memrealtime s[34:35]\n"
    "ds_read_b64_tr_b16 v[176:177], v240 offset:2048\n"
    "ds_read_b64_tr_b16 v[178:179], v241 offset:2304\n"
    "ds_read_b64_tr_b16 v[180:181], v242 offset:2560\n"
    "ds_read_b64_tr_b16 v[182:183], v243 offset:2816\n"
    "ds_read_b32 v236, v253 offset:0\n"
    "ds_read_b32 v238, v253 offset:4\n"
    "ds_read_b128 v[160:163], v234 offset:4096\n"
    "ds_read_b128 v[164:167], v235 offset:4096\n"
    "ds_read_b128 v[184:187], v252 offset:0\n"
    "ds_read_b128 v[188:191], v252 offset:16\n"
    "v_pk_fma_f32 v[128:129], v[128:129], s[70:71], v[96:97]\n"
    "v_pk_fma_f32 v[130:131], v[130:131], s[70:71], v[98:99]\n"
    "v_pk_add_f32 v[230:231], v[128:129], v[130:131]\n"
    "v_pk_mul_f32 v[232:233], v[128:129], v[130:131]\n"
    "v_pk_fma_f32 v[132:133], v[132:133], s[70:71], v[100:101]\n"
    "v_pk_fma_f32 v[230:231], v[230:231], v[132:133], v[232:233]\n"
    "v_pk_mul_f32 v[232:233], v[232:233], v[132:133]\n"
    "v_pk_fma_f32 v[134:135], v[134:135], s[70:71], v[102:103]\n"
    "v_pk_fma_f32 v[230:231], v[230:231], v[134:135], v[232:233]\n"
    "v_pk_mul_f32 v[232:233], v[232:233], v[134:135]\n"
    "v_pk_fma_f32 v[136:137], v[136:137], s[70:71], v[104:105]\n"
    "v_pk_fma_f32 v[230:231], v[230:231], v[136:137], v[232:233]\n"
    "v_pk_mul_f32 v[232:233], v[232:233], v[136:137]\n"
    "v_pk_fma_f32 v[138:139], v[138:139], s[70:71], v[106:107]\n"
    "v_pk_fma_f32 v[230:231], v[230:231], v[138:139], v[232:233]\n"
    "v_pk_mul_f32 v[232:233], v[232:233], v[138:139]\n"
    "v_pk_fma_f32 v[140:141], v[140:141], s[70:71], v[108:109]\n"
    "v_pk_fma_f32 v[230:231], v[230:231], v[140:141], v[232:233]\n"
    "v_pk_mul_f32 v[232:233], v[232:233], v[140:141]\n"
    "v_pk_fma_f32 v[142:143], v[142:143], s[70:71], v[110:111]\n"
    "v_pk_fma_f32 v[230:231], v[230:231], v[142:143], v[232:233]\n"
    "v_pk_mul_f32 v[232:233], v[232:233], v[142:143]\n"
    "v_rcp_f32 v232, v232\n"
    "v_rcp_f32 v233, v233\n"
    "v_fma_f32 v230, v230, v232, v247\n"
    "v_fmac_f32 v230, v231, v233\n"
    "v_lshlrev_b32 v232, 2, v249\n"
    "v_lshrrev_b32 v233, 2, v249\n"
    "v_and_b32 v232, 56, v232\n"
    "v_and_b32 v233, 4, v233\n"
    "v_add3_u32 v232, v232, v233, s81\n"
    "ds_read_b32 v232, v232\n"
    "s_add_u32 s81, s81, 64\n"
    "s_waitcnt lgkmcnt(0)\n"
    "v_lshlrev_b32 v232, 1, v232\n"
    "buffer_load_ushort v232, v232, s[44:47], 0 offen\n"
    "s_waitcnt vmcnt(0)\n"
    "v_lshlrev_b32 v232, 16, v232\n"
    "v_mul_f32 v230, v230, v232\n"
    "v_cvt_pk_bf16_f32 v230, v230, v230\n"
    "s_add_u32 s67, s67, 64\n"
    "s_waitcnt lgkmcnt(0)\n"
    "ds_write_b16 v245, v230\n"
    "ds_read_b64 v[228:229], v246\n"
    "s_cmp_le_u32 s34, s76\n"
    "s_cbranch_scc1 .Ldin_B0s_wait\n"
    ".Ldin_B0s_go:\n"
    "s_sub_u32 s94, s34, 60\n"
    "s_max_u32 s76, s76, s94\n"
    "s_add_u32 s76, s76, s72\n"
    ".Ldin_B0s_dma:\n"
    "buffer_load_dwordx4 v[236:237], s[40:43], 0 idxen offen offset:0 nt lds\n"
    "buffer_load_dwordx4 v[238:239], s[40:43], 0 idxen offen offset:1024 nt lds\n"
    ".Ldin_B0s_nd:\n"
    "s_branch .Ldin_G1s\n"
    ".Ldin_L2s_poll:\n"
    "s_sleep 1\n"
    "ds_read_b64 v[232:233], v253\n"
    "s_waitcnt lgkmcnt(0)\n"
    "v_readfirstlane_b32 s60, v232\n"
    "s_cmp_eq_u32 s60, -1\n"
    "s_cbranch_scc1 .Ldin_L2s_poll\n"
    "s_branch .Ldin_L2s_pr\n"
    ".Ldin_B0s_wait:\n"
    "s_cmp_lt_i32 s59, s57\n"
    "s_cbranch_scc1 .Ldin_B0s_w2\n"
    "s_sub_u32 s94, s67, s65\n"
    "s_cmp_gt_u32 s94, 256\n"
    "s_cbranch_scc1 .Ldin_B0s_dma\n"
    "s_cmp_eq_u32 s94, 64\n"
    "s_waitcnt vmcnt(2)\n"
    "s_cbranch_scc1 .Ldin_B0s_nd\n"
    "s_waitcnt vmcnt(0)\n"
    "s_branch .Ldin_B0s_nd\n"
    ".Ldin_B0s_slp:\n"
    "s_sleep 1\n"
    ".Ldin_B0s_w2:\n"
    "s_memrealtime s[34:35]\n"
    "s_waitcnt lgkmcnt(0)\n"
    "s_sub_u32 s94, s34, s76\n"
    "s_cmp_lt_i32 s94, 0\n"
    "s_cbranch_scc1 .Ldin_B0s_slp\n"
    "s_branch .Ldin_B0s_go\n"
    ".p2align 5\n"
    ".Ldin_L1s:\n"
    "s_setprio 0\n"
    "s_cmp_ge_i32 s59, s57\n"
    "s_cbranch_scc1 .Ldin_E1s\n"
    "s_add_u32 s76, s76, s73\n"
    "v_mov_b32 v253, s63\n"
    "v_and_b32 v184, v254, v184\n"
    "v_and_b32 v185, v255, v185\n"
    "v_and_b32 v186, v254, v186\n"
    "v_and_b32 v187, v255, v187\n"
    "v_and_b32 v188, v254, v188\n"
    "v_and_b32 v189, v255, v189\n"
    "v_and_b32 v190, v254, v190\n"
    "v_and_b32_e64 v191, v255, v191\n"
    "ds_read_b64 v[232:233], v253\n"
    "v_mfma_f32_4x4x4_16b_bf16 v[128:131], v[184:185], v[32:33], v[64:67]\n"
    "ds_read_b128 v[112:115], v252 offset:17408\n"
    "v_mfma_f32_4x4x4_16b_bf16 v[132:135], v[186:187], v[34:35], v[68:71]\n"
    "ds_read_b128 v[116:119], v252 offset:17424\n"
    "v_mfma_f32_4x4x4_16b_bf16 v[136:139], v[188:189], v[36:37], v[72:75]\n"
    "ds_read_b128 v[120:123], v252 offset:17536\n"
    "ds_read_b128 v[124:127], v252 offset:17552\n"
    "v_cvt_pk_bf16_f32 v0, v128, v129\n"
    "v_cvt_pk_bf16_f32 v1, v130, v131\n"
    "v_mfma_f32_4x4x4_16b_bf16 v[140:143], v[190:191], v[38:39], v[76:79]\n"
    "v_cvt_pk_bf16_f32 v2, v132, v133\n"
    "v_cvt_pk_bf16_f32 v3, v134, v135\n"
    "v_mfma_f32_4x4x4_16b_bf16 v[128:131], v[184:185], v[40:41], v[80:83]\n"
    "v_cvt_pk_bf16_f32 v4, v136, v137\n"
    "v_cvt_pk_bf16_f32 v5, v138, v139\n"
    "v_mfma_f32_4x4x4_16b_bf16 v[132:135], v[186:187], v[42:43], v[84:87]\n"
    "v_cvt_pk_bf16_f32 v6, v140, v141\n"
    "v_cvt_pk_bf16_f32 v7, v142, v143\n"
    "v_mfma_f32_4x4x4_16b_bf16 v[136:139], v[188:189], v[44:45], v[88:91]\n"
    "v_cvt_pk_bf16_f32 v8, v128, v129\n"
    "v_cvt_pk_bf16_f32 v9, v130, v131\n"
    "v_mfma_f32_4x4x4_16b_bf16 v[140:143], v[190:191], v[46:47], v[92:95]\n"
    "v_cvt_pk_bf16_f32 v10, v132, v133\n"
    "v_cvt_pk_bf16_f32 v11, v134, v135\n"
    "v_mfma_f32_4x4x4_16b_bf16 v[128:131], v[184:185], v[48:49], v[192:195]\n"
    "v_cvt_pk_bf16_f32 v12, v136, v137\n"
    "v_cvt_pk_bf16_f32 v13, v138, v139\n"
    "v_mfma_f32_4x4x4_16b_bf16 v[132:135], v[186:187], v[50:51], v[196:199]\n"
    "v_cvt_pk_bf16_f32 v14, v140, v141\n"
    "v_cvt_pk_bf16_f32 v15, v142, v143\n"
    "v_mfma_f32_4x4x4_16b_bf16 v[136:139], v[188:189], v[52:53], v[200:203]\n"
    "s_waitcnt lgkmcnt(0)\n"
    "v_readfirstlane_b32 s60, v232\n"
    "v_cvt_pk_bf16_f32 v16, v128, v129\n"
    "v_cvt_pk_bf16_f32 v17, v130, v131\n"
    "v_mfma_f32_4x4x4_16b_bf16 v[140:143], v[190:191], v[54:55], v[204:207]\n"
    "s_cmp_eq_u32 s60, -1\n"
    "s_cbranch_scc1 .Ldin_L1s_poll\n"
    ".Ldin_L1s_pr:\n"
    "v_readfirstlane_b32 s95, v233\n"
    "s_and_b32 s94, s60, 63\n"
    "s_lshl_b32 s94, s94, 6\n"
    "s_add_u32 s94, s94, 68480\n"
    "s_add_u32 s63, s63, 8\n"
    "v_lshl_add_u32 v252, v249, 1, s94\n"
    "ds_read_b32 v252, v252\n"
    "s_sub_u32 s95, s95, s76\n"
    "s_ashr_i32 s95, s95, 4\n"
    "s_min_i32 s95, s95, s74\n"
    "s_max_i32 s72, s95, s75\n"
    "v_cvt_pk_bf16_f32 v18, v132, v133\n"
    "v_cvt_pk_bf16_f32 v19, v134, v135\n"
    "v_mfma_f32_4x4x4_16b_bf16 v[128:131], v[184:185], v[56:57], v[208:211]\n"
    "v_cvt_pk_bf16_f32 v20, v136, v137\n"
    "v_cvt_pk_bf16_f32 v21, v138, v139\n"
    "v_mfma_f32_4x4x4_16b_bf16 v[132:135], v[186:187], v[58:59], v[212:215]\n"
    "v_cvt_pk_bf16_f32 v22, v140, v141\n"
    "v_cvt_pk_bf16_f32 v23, v142, v143\n"
    "v_mfma_f32_4x4x4_16b_bf16 v[136:139], v[188:189], v[60:61], v[216:219]\n"
    "v_cvt_pk_bf16_f32 v24, v128, v129\n"
    "v_cvt_pk_bf16_f32 v25, v130, v131\n"
    "v_mfma_f32_4x4x4_16b_bf16 v[140:143], v[190:191], v[62:63], v[220:223]\n"
    "v_cvt_pk_bf16_f32 v26, v132, v133\n"
    "v_cvt_pk_bf16_f32 v27, v134, v135\n"
    "v_cvt_pk_bf16_f32 v28, v136, v137\n"
    "v_cvt_pk_bf16_f32 v29, v138, v139\n"
    "v_exp_f32 v152, v152 clamp\n"
    "v_cvt_pk_bf16_f32 v30, v140, v141\n"
    "v_cvt_pk_bf16_f32 v31, v142, v143\n"
    "v_mfma_f32_16x16x32_bf16 v[128:131], v[0:3], v[160:163], v[112:115]\n"
    "v_exp_f32 v144, v144 clamp\n"
    "v_mfma_f32_16x16x32_bf16 v[128:131], v[4:7], v[164:167], v[128:131]\n"
    "v_exp_f32 v145, v145 clamp\n"
    "v_mfma_f32_16x16x32_bf16 v[132:135], v[8:11], v[160:163], v[116:119]\n"
    "v_exp_f32 v146, v146 clamp\n"
    "v_mfma_f32_16x16x32_bf16 v[132:135], v[12:15], v[164:167], v[132:135]\n"
    "v_exp_f32 v147, v147 clamp\n"
    "v_mfma_f32_16x16x32_bf16 v[136:139], v[16:19], v[160:163], v[120:123]\n"
    "v_exp_f32 v148, v148 clamp\n"
    "v_mfma_f32_16x16x32_bf16 v[136:139], v[20:23], v[164:167], v[136:139]\n"
    "v_exp_f32 v149, v149 clamp\n"
    "v_mfma_f32_16x16x32_bf16 v[140:143], v[24:27], v[160:163], v[124:127]\n"
    "v_exp_f32 v150, v150 clamp\n"
    "v_mfma_f32_16x16x32_bf16 v[140:143], v[28:31], v[164:167], v[140:143]\n"
    "v_exp_f32 v151, v151 clamp\n"
    "s_add_u32 m0, s68, 4096\n"
    "v_exp_f32 v153, v153 clamp\n"
    "v_add_u32 v253, s67, v244\n"
    "v_mfma_f32_4x4x4_16b_bf16 v[224:227], v[228:229], v[168:169], v[224:227] cbsz:4 abid:0\n"
    "v_exp_f32 v154, v154 clamp\n"
    "v_exp_f32 v155, v155 clamp\n"
    "v_mfma_f32_4x4x4_16b_bf16 v[224:227], v[228:229], v[170:171], v[224:227] cbsz:4 abid:1\n"
    "v_exp_f32 v156, v156 clamp\n"
    "v_exp_f32 v157, v157 clamp\n"
    "v_mfma_f32_4x4x4_16b_bf16 v[224:227], v[228:229], v[172:173], v[224:227] cbsz:4 abid:2\n"
    "v_exp_f32 v158, v158 clamp\n"
    "v_exp_f32 v159, v159 clamp\n"
    "v_mfma_f32_4x4x4_16b_bf16 v[224:227], v[228:229], v[174:175], v[224:227] cbsz:4 abid:3\n"
    "s_waitcnt vmcnt(4)\n"
    "ds_read_b64_tr_b16 v[168:169], v240 offset:4096\n"
    "ds_read_b64_tr_b16 v[170:171], v241 offset:4352\n"
    "ds_read_b64_tr_b16 v[172:173], v242 offset:4608\n"
    "ds_read_b64_tr_b16 v[174:175], v243 offset:4864\n"
    "ds_read_b32 v236, v253 offset:0\n"
    "ds_read_b32 v238, v253 offset:4\n"
    "ds_read_b128 v[160:163], v234 offset:6144\n"
    "ds_read_b128 v[164:167], v235 offset:6144\n"
    "s_waitcnt lgkmcnt(8)\n"
    "s_nop 0\n"
    "v_readlane_b32 s20, v252, 0\n"
    "v_readlane_b32 s21, v252, 1\n"
    "v_readlane_b32 s22, v252, 2\n"
    "v_readlane_b32 s23, v252, 3\n"
    "v_readlane_b32 s24, v252, 4\n"
    "v_readlane_b32 s25, v252, 5\n"
    "v_readlane_b32 s26, v252, 6\n"
    "v_readlane_b32 s27, v252, 7\n"
    "v_pk_fma_f32 v[144:145], v[144:145], s[70:71], v[96:97]\n"
    "v_pk_fma_f32 v[146:147], v[146:147], s[70:71], v[98:99]\n"
    "v_pk_add_f32 v[230:231], v[144:145], v[146:147]\n"
    "v_pk_mul_f32 v[232:233], v[144:145], v[146:147]\n"
    "v_pk_fma_f32 v[148:149], v[148:149], s[70:71], v[100:101]\n"
    "v_pk_fma_f32 v[230:231], v[230:231], v[148:149], v[232:233]\n"
    "v_pk_mul_f32 v[232:233], v[232:233], v[148:149]\n"
    "v_pk_fma_f32 v[150:151], v[150:151], s[70:71], v[102:103]\n"
    "v_pk_fma_f32 v[230:231], v[230:231], v[150:151], v[232:233]\n"
    "v_pk_mul_f32 v[232:233], v[232:233], v[150:151]\n"
    "v_pk_fma_f32 v[152:153], v[152:153], s[70:71], v[104:105]\n"
    "v_pk_fma_f32 v[230:231], v[230:231], v[152:153], v[232:233]\n"
    "v_pk_mul_f32 v[232:233], v[232:233], v[152:153]\n"
    "v_pk_fma_f32 v[154:155], v[154:155], s[70:71], v[106:107]\n"
    "v_pk_fma_f32 v[230:231], v[230:231], v[154:155], v[232:233]\n"
    "v_pk_mul_f32 v[232:233], v[232:233], v[154:155]\n"
    "v_pk_fma_f32 v[156:157], v[156:157], s[70:71], v[108:109]\n"
    "v_pk_fma_f32 v[230:231], v[230:231], v[156:157], v[232:233]\n"
    "v_pk_mul_f32 v[232:233], v[232:233], v[156:157]\n"
    "v_pk_fma_f32 v[158:159], v[158:159], s[70:71], v[110:111]\n"
    "v_pk_fma_f32 v[230:231], v[230:231], v[158:159], v[232:233]\n"
    "v_pk_mul_f32 v[232:233], v[232:233], v[158:159]\n"
    "v_rcp_f32 v232, v232\n"
    "v_rcp_f32 v233, v233\n"
    "v_fma_f32 v230, v230, v232, v247\n"
    "v_fmac_f32 v230, v231, v233\n"
    "v_lshlrev_b32 v232, 2, v249\n"
    "v_lshrrev_b32 v233, 2, v249\n"
    "v_and_b32 v232, 56, v232\n"
    "v_and_b32 v233, 4, v233\n"
    "v_add3_u32 v232, v232, v233, s81\n"
    "ds_read_b32 v232, v232\n"
    "s_add_u32 s81, s81, 64\n"
    "s_waitcnt lgkmcnt(0)\n"
    "v_lshlrev_b32 v232, 1, v232\n"
    "buffer_load_ushort v232, v232, s[44:47], 0 offen\n"
    "s_waitcnt vmcnt(0)\n"
    "v_lshlrev_b32 v232, 16, v232\n"
    "v_mul_f32 v230, v230, v232\n"
    "v_cvt_pk_bf16_f32 v230, v230, v230\n"
    "s_add_u32 s67, s67, 64\n"
    "s_waitcnt lgkmcnt(0)\n"
    "ds_write_b16 v245, v230\n"
    "ds_read_b64 v[228:229], v246\n"
    "s_lshl1_add_u32 s76, s72, s76\n"
    "buffer_load_dwordx4 v[236:237], s[40:43], 0 idxen offen offset:0 nt lds\n"
    "buffer_load_dwordx4 v[238:239], s[40:43], 0 idxen offen offset:1024 nt lds\n"
    "s_and_b32 s62, s58, 63\n"
    "s_mul_i32 s62, s62, 272\n"
    "s_mov_b32 s58, s59\n"
    "s_mov_b32 s59, s60\n"
    "s_mov_b32 s81, s65\n"
    "s_xor_b32 s65, s65, s64\n"
    "s_mov_b32 s80, s83\n"
    "s_add_u32 s82, s65, 256\n"
    "s_cmp_lt_i32 s59, s57\n"
    "s_cselect_b32 s76, s76, -1\n"
    "s_bitcmp1_b32 s58, 30\n"
    "s_cbranch_scc0 .Ldin_A2\n"
    ".Ldin_A2s:\n"
    "v_mfma_f32_16x16x32_bf16 v[144:147], v[0:3], v[160:163], v[112:115]\n"
    "v_exp_f32 v128, v128 clamp\n"
    "v_mfma_f32_16x16x32_bf16 v[144:147], v[4:7], v[164:167], v[144:147]\n"
    "v_exp_f32 v129, v129 clamp\n"
    "v_mfma_f32_16x16x32_bf16 v[148:151], v[8:11], v[160:163], v[116:119]\n"
    "v_exp_f32 v130, v130 clamp\n"
    "v_mfma_f32_16x16x32_bf16 v[148:151], v[12:15], v[164:167], v[148:151]\n"
    "v_exp_f32 v131, v131 clamp\n"
    "v_mfma_f32_16x16x32_bf16 v[152:155], v[16:19], v[160:163], v[120:123]\n"
    "v_exp_f32 v132, v132 clamp\n"
    "v_mfma_f32_16x16x32_bf16 v[152:155], v[20:23], v[164:167], v[152:155]\n"
    "v_exp_f32 v133, v133 clamp\n"
    "v_mfma_f32_16x16x32_bf16 v[156:159], v[24:27], v[160:163], v[124:127]\n"
    "v_exp_f32 v134, v134 clamp\n"
    "v_mfma_f32_16x16x32_bf16 v[156:159], v[28:31], v[164:167], v[156:159]\n"
    "v_exp_f32 v135, v135 clamp\n"
    "v_exp_f32 v136, v136 clamp\n"
    "s_add_u32 m0, s68, 6144\n"
    "v_exp_f32 v137, v137 clamp\n"
    "v_add_u32_e64 v253, s67, v244\n"
    "s_waitcnt lgkmcnt(0)\n"
    "v_mfma_f32_4x4x4_16b_bf16 v[224:227], v[228:229], v[176:177], v[224:227] cbsz:4 abid:0\n"
    "v_exp_f32 v138, v138 clamp\n"
    "v_exp_f32 v139, v139 clamp\n"
    "v_mfma_f32_4x4x4_16b_bf16 v[224:227], v[228:229], v[178:179], v[224:227] cbsz:4 abid:1\n"
    "v_exp_f32 v140, v140 clamp\n"
    "v_exp_f32 v141, v141 clamp\n"
    "v_mfma_f32_4x4x4_16b_bf16 v[224:227], v[228:229], v[180:181], v[224:227] cbsz:4 abid:2\n"
    "v_exp_f32 v142, v142 clamp\n"
    "v_exp_f32 v143, v143 clamp\n"
    "v_mfma_f32_4x4x4_16b_bf16 v[224:227], v[228:229], v[182:183], v[224:227] cbsz:4 abid:3\n"
    "s_waitcnt vmcnt(4)\n"
    "ds_read_b64_tr_b16 v[176:177], v240 offset:6144\n"
    "ds_read_b64_tr_b16 v[178:179], v241 offset:6400\n"
    "ds_read_b64_tr_b16 v[180:181], v242 offset:6656\n"
    "ds_read_b64_tr_b16 v[182:183], v243 offset:6912\n"
    "ds_read_b32 v236, v253 offset:0\n"
    "ds_read_b32 v238, v253 offset:4\n"
    "ds_read_b128 v[160:163], v234 offset:0\n"
    "ds_read_b128 v[164:167], v235 offset:0\n"
    "s_cbranch_vccz .Ldin_A2s_np\n"
    "s_setprio 1\n"
    ".Ldin_A2s_np:\n"
    "v_pk_fma_f32 v[128:129], v[128:129], s[70:71], v[96:97]\n"
    "v_pk_fma_f32 v[130:131], v[130:131], s[70:71], v[98:99]\n"
    "v_add_f32 v191, v224, v225\n"
    "v_add_f32 v190, v226, v227\n"
    "v_add_f32 v191, v191, v190\n"
    "v_add_u32 v190, s62, v249\n"
    "v_mul_f32 v191, s69, v191\n"
    "v_cvt_pk_bf16_f32 v191, v191, v191\n"
    "ds_write_b16 v190, v191 offset:32768\n"
    "s_bfe_u32 s94, s59, 0x180006\n"
    "s_mul_i32 s86, s94, s56\n"
    "v_mbcnt_lo_u32_b32 v184, s20, 0\n"
    "v_mbcnt_lo_u32_b32 v185, s22, 0\n"
    "v_mbcnt_lo_u32_b32 v186, s24, 0\n"
    "v_mbcnt_lo_u32_b32 v187, s26, 0\n"
    "s_bcnt1_i32_b64 s90, s[20:21]\n"
    "s_bcnt1_i32_b64 s91, s[22:23]\n"
    "s_bcnt1_i32_b64 s92, s[24:25]\n"
    "s_bcnt1_i32_b64 s93, s[26:27]\n"
    "v_mbcnt_hi_u32_b32 v184, s21, v184\n"
    "v_mbcnt_hi_u32_b32 v185, s23, v185\n"
    "v_mbcnt_hi_u32_b32 v186, s25, v186\n"
    "v_mbcnt_hi_u32_b32 v187, s27, v187\n"
    "s_lshl2_add_u32 s90, s90, s65\n"
    "s_lshl2_add_u32 s91, s91, s90\n"
    "s_lshl2_add_u32 s92, s92, s91\n"
    "s_lshl2_add_u32 s93, s93, s92\n"
    "v_add_u32_e64 v188, s86, v251\n"
    "v_pk_add_f32 v[230:231], v[128:129], v[130:131]\n"
    "v_pk_mul_f32 v[232:233], v[128:129], v[130:131]\n"
    "v_pk_fma_f32 v[132:133], v[132:133], s[70:71], v[100:101]\n"
    "v_pk_fma_f32 v[230:231], v[230:231], v[132:133], v[232:233]\n"
    "v_pk_mul_f32 v[232:233], v[232:233], v[132:133]\n"
    "v_lshl_add_u32 v184, v184, 2, s65\n"
    "v_lshl_add_u32 v185, v185, 2, s90\n"
    "v_lshl_add_u32 v186, v186, 2, s91\n"
    "v_lshl_add_u32 v187, v187, 2, s92\n"
    "v_mov_b32 v191, -1\n"
    "v_lshl_add_u32 v190, v249, 1, s93\n"
    "ds_write2_b32 v190, v191, v191 offset1:64\n"
    "s_mov_b64 exec, s[20:21]\n"
    "ds_write_b32 v184, v188\n"
    "s_mov_b64 exec, s[22:23]\n"
    "v_add_u32 v189, 64, v188\n"
    "ds_write_b32 v185, v189\n"
    "s_mov_b64 exec, s[24:25]\n"
    "v_add_u32 v190, 128, v188\n"
    "ds_write_b32 v186, v190\n"
    "s_mov_b64 exec, s[26:27]\n"
    "v_add_u32 v189, 192, v188\n"
    "ds_write_b32 v187, v189\n"
    "s_mov_b64 exec, -1\n"
    "s_add_u32 s95, s93, 60\n"
    "s_add_u32 s94, s82, 128\n"
    "s_andn2_b32 s95, s95, 63\n"
    "s_max_u32 s83, s95, s94\n"
    "v_pk_fma_f32 v[134:135], v[134:135], s[70:71], v[102:103]\n"
    "v_pk_fma_f32 v[230:231], v[230:231], v[134:135], v[232:233]\n"
    "v_pk_mul_f32 v[232:233], v[232:233], v[134:135]\n"
    "v_pk_fma_f32 v[136:137], v[136:137], s[70:71], v[104:105]\n"
    "v_pk_fma_f32 v[230:231], v[230:231], v[136:137], v[232:233]\n"
    "v_pk_mul_f32 v[232:233], v[232:233], v[136:137]\n"
    "v_pk_fma_f32 v[138:139], v[138:139], s[70:71], v[106:107]\n"
    "v_pk_fma_f32 v[230:231], v[230:231], v[138:139], v[232:233]\n"
    "v_pk_mul_f32 v[232:233], v[232:233], v[138:139]\n"
    "v_pk_fma_f32 v[140:141], v[140:141], s[70:71], v[108:109]\n"
    "v_pk_fma_f32 v[230:231], v[230:231], v[140:141], v[232:233]\n"
    "v_pk_mul_f32 v[232:233], v[232:233], v[140:141]\n"
    "v_pk_fma_f32 v[142:143], v[142:143], s[70:71], v[110:111]\n"
    "v_pk_fma_f32 v[230:231], v[230:231], v[142:143], v[232:233]\n"
    "v_pk_mul_f32 v[232:233], v[232:233], v[142:143]\n"
    "v_rcp_f32 v232, v232\n"
    "v_rcp_f32 v233, v233\n"
    "v_fma_f32 v230, v230, v232, v247\n"
    "v_fmac_f32 v230, v231, v233\n"
    "v_lshlrev_b32 v232, 2, v249\n"
    "v_lshrrev_b32 v233, 2, v249\n"
    "v_and_b32 v232, 56, v232\n"
    "v_and_b32 v233, 4, v233\n"
    "v_add3_u32 v232, v232, v233, s81\n"
    "ds_read_b32 v232, v232\n"
    "s_add_u32 s81, s81, 64\n"
    "s_waitcnt lgkmcnt(0)\n"
    "v_lshlrev_b32 v232, 1, v232\n"
    "buffer_load_ushort v232, v232, s[44:47], 0 offen\n"
    "s_waitcnt vmcnt(0)\n"
    "v_lshlrev_b32 v232, 16, v232\n"
    "v_mul_f32 v230, v230, v232\n"
    "v_cvt_pk_bf16_f32 v230, v230, v230\n"
    "s_add_u32 s67, s67, 64\n"
    "s_waitcnt lgkmcnt(0)\n"
    "ds_write_b16 v245, v230\n"
    "ds_read_b64 v[228:229], v246\n"
    "buffer_load_dwordx4 v[236:237], s[40:43], 0 idxen offen offset:0 nt lds\n"
    "buffer_load_dwordx4 v[238:239], s[40:43], 0 idxen offen offset:1024 nt lds\n"
    ".Ldin_B3s:\n"
    "v_mfma_f32_16x16x32_bf16 v[128:131], v[0:3], v[160:163], v[112:115]\n"
    "v_exp_f32 v144, v144 clamp\n"
    "v_mfma_f32_16x16x32_bf16 v[128:131], v[4:7], v[164:167], v[128:131]\n"
    "v_exp_f32 v145, v145 clamp\n"
    "v_mfma_f32_16x16x32_bf16 v[132:135], v[8:11], v[160:163], v[116:119]\n"
    "v_exp_f32 v146, v146 clamp\n"
    "v_mfma_f32_16x16x32_bf16 v[132:135], v[12:15], v[164:167], v[132:135]\n"
    "v_exp_f32 v147, v147 clamp\n"
    "v_mfma_f32_16x16x32_bf16 v[136:139], v[16:19], v[160:163], v[120:123]\n"
    "v_exp_f32 v148, v148 clamp\n"
    "v_mfma_f32_16x16x32_bf16 v[136:139], v[20:23], v[164:167], v[136:139]\n"
    "v_exp_f32 v149, v149 clamp\n"
    "v_mfma_f32_16x16x32_bf16 v[140:143], v[24:27], v[160:163], v[124:127]\n"
    "v_exp_f32 v150, v150 clamp\n"
    "v_mfma_f32_16x16x32_bf16 v[140:143], v[28:31], v[164:167], v[140:143]\n"
    "v_exp_f32 v151, v151 clamp\n"
    "v_exp_f32 v152, v152 clamp\n"
    "s_cmp_eq_u32 s67, s80\n"
    "s_cselect_b32 s67, s65, s67\n"
    "s_and_b32 s94, s59, 63\n"
    "s_mul_i32 s94, s94, 272\n"
    "v_add_u32 v252, s94, v250\n"
    "s_add_u32 m0, s68, 0\n"
    "v_exp_f32 v153, v153 clamp\n"
    "v_add_u32_e64 v253, s67, v244\n"
    "s_waitcnt lgkmcnt(0)\n"
    "v_mfma_f32_4x4x4_16b_bf16 v[224:227], v[228:229], v[168:169], 0 cbsz:4 abid:0\n"
    "v_exp_f32 v154, v154 clamp\n"
    "v_exp_f32 v155, v155 clamp\n"
    "v_mfma_f32_4x4x4_16b_bf16 v[224:227], v[228:229], v[170:171], v[224:227] cbsz:4 abid:1\n"
    "v_exp_f32 v156, v156 clamp\n"
    "v_exp_f32 v157, v157 clamp\n"
    "v_mfma_f32_4x4x4_16b_bf16 v[224:227], v[228:229], v[172:173], v[224:227] cbsz:4 abid:2\n"
    "v_exp_f32 v158, v158 clamp\n"
    "v_exp_f32 v159, v159 clamp\n"
    "v_mfma_f32_4x4x4_16b_bf16 v[224:227], v[228:229], v[174:175], v[224:227] cbsz:4 abid:3\n"
    "s_waitcnt vmcnt(4)\n"
    "s_memrealtime s[34:35]\n"
    "ds_read_b64_tr_b16 v[168:169], v240 offset:0\n"
    "ds_read_b64_tr_b16 v[170:171], v241 offset:256\n"
    "ds_read_b64_tr_b16 v[172:173], v242 offset:512\n"
    "ds_read_b64_tr_b16 v[174:175], v243 offset:768\n"
    "ds_read_b32 v236, v253 offset:0\n"
    "ds_read_b32 v238, v253 offset:4\n"
    "ds_read_b128 v[160:163], v234 offset:2048\n"
    "ds_read_b128 v[164:167], v235 offset:2048\n"
    "ds_read_b128 v[184:187], v252 offset:0\n"
    "ds_read_b128 v[188:191], v252 offset:16\n"
    "v_pk_fma_f32 v[144:145], v[144:145], s[70:71], v[96:97]\n"
    "v_pk_fma_f32 v[146:147], v[146:147], s[70:71], v[98:99]\n"
    "v_pk_add_f32 v[230:231], v[144:145], v[146:147]\n"
    "v_pk_mul_f32 v[232:233], v[144:145], v[146:147]\n"
    "v_pk_fma_f32 v[148:149], v[148:149], s[70:71], v[100:101]\n"
    "v_pk_fma_f32 v[230:231], v[230:231], v[148:149], v[232:233]\n"
    "v_pk_mul_f32 v[232:233], v[232:233], v[148:149]\n"
    "v_pk_fma_f32 v[150:151], v[150:151], s[70:71], v[102:103]\n"
    "v_pk_fma_f32 v[230:231], v[230:231], v[150:151], v[232:233]\n"
    "v_pk_mul_f32 v[232:233], v[232:233], v[150:151]\n"
    "v_pk_fma_f32 v[152:153], v[152:153], s[70:71], v[104:105]\n"
    "v_pk_fma_f32 v[230:231], v[230:231], v[152:153], v[232:233]\n"
    "v_pk_mul_f32 v[232:233], v[232:233], v[152:153]\n"
    "v_pk_fma_f32 v[154:155], v[154:155], s[70:71], v[106:107]\n"
    "v_pk_fma_f32 v[230:231], v[230:231], v[154:155], v[232:233]\n"
    "v_pk_mul_f32 v[232:233], v[232:233], v[154:155]\n"
    "v_pk_fma_f32 v[156:157], v[156:157], s[70:71], v[108:109]\n"
    "v_pk_fma_f32 v[230:231], v[230:231], v[156:157], v[232:233]\n"
    "v_pk_mul_f32 v[232:233], v[232:233], v[156:157]\n"
    "v_pk_fma_f32 v[158:159], v[158:159], s[70:71], v[110:111]\n"
    "v_pk_fma_f32 v[230:231], v[230:231], v[158:159], v[232:233]\n"
    "v_pk_mul_f32 v[232:233], v[232:233], v[158:159]\n"
    "v_rcp_f32 v232, v232\n"
    "v_rcp_f32 v233, v233\n"
    "v_fma_f32 v230, v230, v232, v247\n"
    "v_fmac_f32 v230, v231, v233\n"
    "v_lshlrev_b32 v232, 2, v249\n"
    "v_lshrrev_b32 v233, 2, v249\n"
    "v_and_b32 v232, 56, v232\n"
    "v_and_b32 v233, 4, v233\n"
    "v_add3_u32 v232, v232, v233, s81\n"
    "ds_read_b32 v232, v232\n"
    "s_add_u32 s81, s81, 64\n"
    "s_waitcnt lgkmcnt(0)\n"
    "v_lshlrev_b32 v232, 1, v232\n"
    "buffer_load_ushort v232, v232, s[44:47], 0 offen\n"
    "s_waitcnt vmcnt(0)\n"
    "v_lshlrev_b32 v232, 16, v232\n"
    "v_mul_f32 v230, v230, v232\n"
    "v_cvt_pk_bf16_f32 v230, v230, v230\n"
    "s_add_u32 s67, s67, 64\n"
    "s_waitcnt lgkmcnt(0)\n"
    "ds_write_b16 v245, v230\n"
    "ds_read_b64 v[228:229], v246\n"
    "s_cmp_le_u32 s34, s76\n"
    "s_cbranch_scc1 .Ldin_B3s_wait\n"
    ".Ldin_B3s_go:\n"
    "s_sub_u32 s94, s34, 60\n"
    "s_max_u32 s76, s76, s94\n"
    "s_add_u32 s76, s76, s72\n"
    ".Ldin_B3s_dma:\n"
    "buffer_load_dwordx4 v[236:237], s[40:43], 0 idxen offen offset:0 nt lds\n"
    "buffer_load_dwordx4 v[238:239], s[40:43], 0 idxen offen offset:1024 nt lds\n"
    ".Ldin_B3s_nd:\n"
    "s_branch .Ldin_G0s\n"
    ".Ldin_L1s_poll:\n"
    "s_sleep 1\n"
    "ds_read_b64 v[232:233], v253\n"
    "s_waitcnt lgkmcnt(0)\n"
    "v_readfirstlane_b32 s60, v232\n"
    "s_cmp_eq_u32 s60, -1\n"
    "s_cbranch_scc1 .Ldin_L1s_poll\n"
    "s_branch .Ldin_L1s_pr\n"
    ".Ldin_B3s_wait:\n"
    "s_cmp_lt_i32 s59, s57\n"
    "s_cbranch_scc1 .Ldin_B3s_w2\n"
    "s_sub_u32 s94, s67, s65\n"
    "s_cmp_gt_u32 s94, 256\n"
    "s_cbranch_scc1 .Ldin_B3s_dma\n"
    "s_cmp_eq_u32 s94, 64\n"
    "s_waitcnt vmcnt(2)\n"
    "s_cbranch_scc1 .Ldin_B3s_nd\n"
    "s_waitcnt vmcnt(0)\n"
    "s_branch .Ldin_B3s_nd\n"
    ".Ldin_B3s_slp:\n"
    "s_sleep 1\n"
    ".Ldin_B3s_w2:\n"
    "s_memrealtime s[34:35]\n"
    "s_waitcnt lgkmcnt(0)\n"
    "s_sub_u32 s94, s34, s76\n"
    "s_cmp_lt_i32 s94, 0\n"
    "s_cbranch_scc1 .Ldin_B3s_slp\n"
    "s_branch .Ldin_B3s_go\n"
    ".p2align 5\n"
    ".Ldin_L0s:\n"
    "s_setprio 0\n"
    "s_cmp_ge_i32 s59, s57\n"
    "s_cbranch_scc1 .Ldin_E0s\n"
    "s_add_u32 s76, s76, s73\n"
    "v_mov_b32 v253, s63\n"
    "v_and_b32 v184, v254, v184\n"
    "v_and_b32 v185, v255, v185\n"
    "v_and_b32 v186, v254, v186\n"
    "v_and_b32 v187, v255, v187\n"
    "v_and_b32 v188, v254, v188\n"
    "v_and_b32 v189, v255, v189\n"
    "v_and_b32 v190, v254, v190\n"
    "v_and_b32_e64 v191, v255, v191\n"
    "ds_read_b64 v[232:233], v253\n"
    "v_mfma_f32_4x4x4_16b_bf16 v[144:147], v[184:185], v[32:33], v[64:67]\n"
    "ds_read_b128 v[112:115], v252 offset:17408\n"
    "v_mfma_f32_4x4x4_16b_bf16 v[148:151], v[186:187], v[34:35], v[68:71]\n"
    "ds_read_b128 v[116:119], v252 offset:17424\n"
    "v_mfma_f32_4x4x4_16b_bf16 v[152:155], v[188:189], v[36:37], v[72:75]\n"
    "ds_read_b128 v[120:123], v252 offset:17536\n"
    "ds_read_b128 v[124:127], v252 offset:17552\n"
    "v_cvt_pk_bf16_f32 v0, v144, v145\n"
    "v_cvt_pk_bf16_f32 v1, v146, v147\n"
    "v_mfma_f32_4x4x4_16b_bf16 v[156:159], v[190:191], v[38:39], v[76:79]\n"
    "v_cvt_pk_bf16_f32 v2, v148, v149\n"
    "v_cvt_pk_bf16_f32 v3, v150, v151\n"
    "v_mfma_f32_4x4x4_16b_bf16 v[144:147], v[184:185], v[40:41], v[80:83]\n"
    "v_cvt_pk_bf16_f32 v4, v152, v153\n"
    "v_cvt_pk_bf16_f32 v5, v154, v155\n"
    "v_mfma_f32_4x4x4_16b_bf16 v[148:151], v[186:187], v[42:43], v[84:87]\n"
    "v_cvt_pk_bf16_f32 v6, v156, v157\n"
    "v_cvt_pk_bf16_f32 v7, v158, v159\n"
    "v_mfma_f32_4x4x4_16b_bf16 v[152:155], v[188:189], v[44:45], v[88:91]\n"
    "v_cvt_pk_bf16_f32 v8, v144, v145\n"
    "v_cvt_pk_bf16_f32 v9, v146, v147\n"
    "v_mfma_f32_4x4x4_16b_bf16 v[156:159], v[190:191], v[46:47], v[92:95]\n"
    "v_cvt_pk_bf16_f32 v10, v148, v149\n"
    "v_cvt_pk_bf16_f32 v11, v150, v151\n"
    "v_mfma_f32_4x4x4_16b_bf16 v[144:147], v[184:185], v[48:49], v[192:195]\n"
    "v_cvt_pk_bf16_f32 v12, v152, v153\n"
    "v_cvt_pk_bf16_f32 v13, v154, v155\n"
    "v_mfma_f32_4x4x4_16b_bf16 v[148:151], v[186:187], v[50:51], v[196:199]\n"
    "v_cvt_pk_bf16_f32 v14, v156, v157\n"
    "v_cvt_pk_bf16_f32 v15, v158, v159\n"
    "v_mfma_f32_4x4x4_16b_bf16 v[152:155], v[188:189], v[52:53], v[200:203]\n"
    "s_waitcnt lgkmcnt(0)\n"
    "v_readfirstlane_b32 s60, v232\n"
    "v_cvt_pk_bf16_f32 v16, v144, v145\n"
    "v_cvt_pk_bf16_f32 v17, v146, v147\n"
    "v_mfma_f32_4x4x4_16b_bf16 v[156:159], v[190:191], v[54:55], v[204:207]\n"
    "s_cmp_eq_u32 s60, -1\n"
    "s_cbranch_scc1 .Ldin_L0s_poll\n"
    ".Ldin_L0s_pr:\n"
    "v_readfirstlane_b32 s95, v233\n"
    "s_and_b32 s94, s60, 63\n"
    "s_lshl_b32 s94, s94, 6\n"
    "s_add_u32 s94, s94, 68480\n"
    "s_add_u32 s63, s63, 8\n"
    "v_lshl_add_u32 v252, v249, 1, s94\n"
    "ds_read_b32 v252, v252\n"
    "s_sub_u32 s95, s95, s76\n"
    "s_ashr_i32 s95, s95, 4\n"
    "s_min_i32 s95, s95, s74\n"
    "s_max_i32 s72, s95, s75\n"
    "v_cvt_pk_bf16_f32 v18, v148, v149\n"
    "v_cvt_pk_bf16_f32 v19, v150, v151\n"
    "v_mfma_f32_4x4x4_16b_bf16 v[144:147], v[184:185], v[56:57], v[208:211]\n"
    "v_cvt_pk_bf16_f32 v20, v152, v153\n"
    "v_cvt_pk_bf16_f32 v21, v154, v155\n"
    "v_mfma_f32_4x4x4_16b_bf16 v[148:151], v[186:187], v[58:59], v[212:215]\n"
    "v_cvt_pk_bf16_f32 v22, v156, v157\n"
    "v_cvt_pk_bf16_f32 v23, v158, v159\n"
    "v_mfma_f32_4x4x4_16b_bf16 v[152:155], v[188:189], v[60:61], v[216:219]\n"
    "v_cvt_pk_bf16_f32 v24, v144, v145\n"
    "v_cvt_pk_bf16_f32 v25, v146, v147\n"
    "v_mfma_f32_4x4x4_16b_bf16 v[156:159], v[190:191], v[62:63], v[220:223]\n"
    "v_cvt_pk_bf16_f32 v26, v148, v149\n"
    "v_cvt_pk_bf16_f32 v27, v150, v151\n"
    "v_cvt_pk_bf16_f32 v28, v152, v153\n"
    "v_cvt_pk_bf16_f32 v29, v154, v155\n"
    "v_exp_f32 v136, v136 clamp\n"
    "v_cvt_pk_bf16_f32 v30, v156, v157\n"
    "v_cvt_pk_bf16_f32 v31, v158, v159\n"
    "v_mfma_f32_16x16x32_bf16 v[144:147], v[0:3], v[160:163], v[112:115]\n"
    "v_exp_f32 v128, v128 clamp\n"
    "v_mfma_f32_16x16x32_bf16 v[144:147], v[4:7], v[164:167], v[144:147]\n"
    "v_exp_f32 v129, v129 clamp\n"
    "v_mfma_f32_16x16x32_bf16 v[148:151], v[8:11], v[160:163], v[116:119]\n"
    "v_exp_f32 v130, v130 clamp\n"
    "v_mfma_f32_16x16x32_bf16 v[148:151], v[12:15], v[164:167], v[148:151]\n"
    "v_exp_f32 v131, v131 clamp\n"
    "v_mfma_f32_16x16x32_bf16 v[152:155], v[16:19], v[160:163], v[120:123]\n"
    "v_exp_f32 v132, v132 clamp\n"
    "v_mfma_f32_16x16x32_bf16 v[152:155], v[20:23], v[164:167], v[152:155]\n"
    "v_exp_f32 v133, v133 clamp\n"
    "v_mfma_f32_16x16x32_bf16 v[156:159], v[24:27], v[160:163], v[124:127]\n"
    "v_exp_f32 v134, v134 clamp\n"
    "v_mfma_f32_16x16x32_bf16 v[156:159], v[28:31], v[164:167], v[156:159]\n"
    "v_exp_f32 v135, v135 clamp\n"
    "s_add_u32 m0, s68, 2048\n"
    "v_exp_f32 v137, v137 clamp\n"
    "v_add_u32 v253, s67, v244\n"
    "v_mfma_f32_4x4x4_16b_bf16 v[224:227], v[228:229], v[176:177], v[224:227] cbsz:4 abid:0\n"
    "v_exp_f32 v138, v138 clamp\n"
    "v_exp_f32 v139, v139 clamp\n"
    "v_mfma_f32_4x4x4_16b_bf16 v[224:227], v[228:229], v[178:179], v[224:227] cbsz:4 abid:1\n"
    "v_exp_f32 v140, v140 clamp\n"
    "v_exp_f32 v141, v141 clamp\n"
    "v_mfma_f32_4x4x4_16b_bf16 v[224:227], v[228:229], v[180:181], v[224:227] cbsz:4 abid:2\n"
    "v_exp_f32 v142, v142 clamp\n"
    "v_exp_f32 v143, v143 clamp\n"
    "v_mfma_f32_4x4x4_16b_bf16 v[224:227], v[228:229], v[182:183], v[224:227] cbsz:4 abid:3\n"
    "s_waitcnt vmcnt(4)\n"
    "ds_read_b64_tr_b16 v[176:177], v240 offset:2048\n"
    "ds_read_b64_tr_b16 v[178:179], v241 offset:2304\n"
    "ds_read_b64_tr_b16 v[180:181], v242 offset:2560\n"
    "ds_read_b64_tr_b16 v[182:183], v243 offset:2816\n"
    "ds_read_b32 v236, v253 offset:0\n"
    "ds_read_b32 v238, v253 offset:4\n"
    "ds_read_b128 v[160:163], v234 offset:4096\n"
    "ds_read_b128 v[164:167], v235 offset:4096\n"
    "s_waitcnt lgkmcnt(8)\n"
    "s_nop 0\n"
    "v_readlane_b32 s20, v252, 0\n"
    "v_readlane_b32 s21, v252, 1\n"
    "v_readlane_b32 s22, v252, 2\n"
    "v_readlane_b32 s23, v252, 3\n"
    "v_readlane_b32 s24, v252, 4\n"
    "v_readlane_b32 s25, v252, 5\n"
    "v_readlane_b32 s26, v252, 6\n"
    "v_readlane_b32 s27, v252, 7\n"
    "v_pk_fma_f32 v[128:129], v[128:129], s[70:71], v[96:97]\n"
    "v_pk_fma_f32 v[130:131], v[130:131], s[70:71], v[98:99]\n"
    "v_pk_add_f32 v[230:231], v[128:129], v[130:131]\n"
    "v_pk_mul_f32 v[232:233], v[128:129], v[130:131]\n"
    "v_pk_fma_f32 v[132:133], v[132:133], s[70:71], v[100:101]\n"
    "v_pk_fma_f32 v[230:231], v[230:231], v[132:133], v[232:233]\n"
    "v_pk_mul_f32 v[232:233], v[232:233], v[132:133]\n"
    "v_pk_fma_f32 v[134:135], v[134:135], s[70:71], v[102:103]\n"
    "v_pk_fma_f32 v[230:231], v[230:231], v[134:135], v[232:233]\n"
    "v_pk_mul_f32 v[232:233], v[232:233], v[134:135]\n"
    "v_pk_fma_f32 v[136:137], v[136:137], s[70:71], v[104:105]\n"
    "v_pk_fma_f32 v[230:231], v[230:231], v[136:137], v[232:233]\n"
    "v_pk_mul_f32 v[232:233], v[232:233], v[136:137]\n"
    "v_pk_fma_f32 v[138:139], v[138:139], s[70:71], v[106:107]\n"
    "v_pk_fma_f32 v[230:231], v[230:231], v[138:139], v[232:233]\n"
    "v_pk_mul_f32 v[232:233], v[232:233], v[138:139]\n"
    "v_pk_fma_f32 v[140:141], v[140:141], s[70:71], v[108:109]\n"
    "v_pk_fma_f32 v[230:231], v[230:231], v[140:141], v[232:233]\n"
    "v_pk_mul_f32 v[232:233], v[232:233], v[140:141]\n"
    "v_pk_fma_f32 v[142:143], v[142:143], s[70:71], v[110:111]\n"
    "v_pk_fma_f32 v[230:231], v[230:231], v[142:143], v[232:233]\n"
    "v_pk_mul_f32 v[232:233], v[232:233], v[142:143]\n"
    "v_rcp_f32 v232, v232\n"
    "v_rcp_f32 v233, v233\n"
    "v_fma_f32 v230, v230, v232, v247\n"
    "v_fmac_f32 v230, v231, v233\n"
    "v_lshlrev_b32 v232, 2, v249\n"
    "v_lshrrev_b32 v233, 2, v249\n"
    "v_and_b32 v232, 56, v232\n"
    "v_and_b32 v233, 4, v233\n"
    "v_add3_u32 v232, v232, v233, s81\n"
    "ds_read_b32 v232, v232\n"
    "s_add_u32 s81, s81, 64\n"
    "s_waitcnt lgkmcnt(0)\n"
    "v_lshlrev_b32 v232, 1, v232\n"
    "buffer_load_ushort v232, v232, s[44:47], 0 offen\n"
    "s_waitcnt vmcnt(0)\n"
    "v_lshlrev_b32 v232, 16, v232\n"
    "v_mul_f32 v230, v230, v232\n"
    "v_cvt_pk_bf16_f32 v230, v230, v230\n"
    "s_add_u32 s67, s67, 64\n"
    "s_waitcnt lgkmcnt(0)\n"
    "ds_write_b16 v245, v230\n"
    "ds_read_b64 v[228:229], v246\n"
    "s_lshl1_add_u32 s76, s72, s76\n"
    "buffer_load_dwordx4 v[236:237], s[40:43], 0 idxen offen offset:0 nt lds\n"
    "buffer_load_dwordx4 v[238:239], s[40:43], 0 idxen offen offset:1024 nt lds\n"
    "s_and_b32 s62, s58, 63\n"
    "s_mul_i32 s62, s62, 272\n"
    "s_mov_b32 s58, s59\n"
    "s_mov_b32 s59, s60\n"
    "s_mov_b32 s81, s65\n"
    "s_xor_b32 s65, s65, s64\n"
    "s_mov_b32 s80, s83\n"
    "s_add_u32 s82, s65, 256\n"
    "s_cmp_lt_i32 s59, s57\n"
    "s_cselect_b32 s76, s76, -1\n"
    "s_bitcmp1_b32 s58, 30\n"
    "s_cbranch_scc0 .Ldin_A1\n"
    ".Ldin_A1s:\n"
    "v_mfma_f32_16x16x32_bf16 v[128:131], v[0:3], v[160:163], v[112:115]\n"
    "v_exp_f32 v144, v144 clamp\n"
    "v_mfma_f32_16x16x32_bf16 v[128:131], v[4:7], v[164:167], v[128:131]\n"
    "v_exp_f32 v145, v145 clamp\n"
    "v_mfma_f32_16x16x32_bf16 v[132:135], v[8:11], v[160:163], v[116:119]\n"
    "v_exp_f32 v146, v146 clamp\n"
    "v_mfma_f32_16x16x32_bf16 v[132:135], v[12:15], v[164:167], v[132:135]\n"
    "v_exp_f32 v147, v147 clamp\n"
    "v_mfma_f32_16x16x32_bf16 v[136:139], v[16:19], v[160:163], v[120:123]\n"
    "v_exp_f32 v148, v148 clamp\n"
    "v_mfma_f32_16x16x32_bf16 v[136:139], v[20:23], v[164:167], v[136:139]\n"
    "v_exp_f32 v149, v149 clamp\n"
    "v_mfma_f32_16x16x32_bf16 v[140:143], v[24:27], v[160:163], v[124:127]\n"
    "v_exp_f32 v150, v150 clamp\n"
    "v_mfma_f32_16x16x32_bf16 v[140:143], v[28:31], v[164:167], v[140:143]\n"
    "v_exp_f32 v151, v151 clamp\n"
    "v_exp_f32 v152, v152 clamp\n"
    "s_add_u32 m0, s68, 4096\n"
    "v_exp_f32 v153, v153 clamp\n"
    "v_add_u32_e64 v253, s67, v244\n"
    "s_waitcnt lgkmcnt(0)\n"
    "v_mfma_f32_4x4x4_16b_bf16 v[224:227], v[228:229], v[168:169], v[224:227] cbsz:4 abid:0\n"
    "v_exp_f32 v154, v154 clamp\n"
    "v_exp_f32 v155, v155 clamp\n"
    "v_mfma_f32_4x4x4_16b_bf16 v[224:227], v[228:229], v[170:171], v[224:227] cbsz:4 abid:1\n"
    "v_exp_f32 v156, v156 clamp\n"
    "v_exp_f32 v157, v157 clamp\n"
    "v_mfma_f32_4x4x4_16b_bf16 v[224:227], v[228:229], v[172:173], v[224:227] cbsz:4 abid:2\n"
    "v_exp_f32 v158, v158 clamp\n"
    "v_exp_f32 v159, v159 clamp\n"
    "v_mfma_f32_4x4x4_16b_bf16 v[224:227], v[228:229], v[174:175], v[224:227] cbsz:4 abid:3\n"
    "s_waitcnt vmcnt(4)\n"
    "ds_read_b64_tr_b16 v[168:169], v240 offset:4096\n"
    "ds_read_b64_tr_b16 v[170:171], v241 offset:4352\n"
    "ds_read_b64_tr_b16 v[172:173], v242 offset:4608\n"
    "ds_read_b64_tr_b16 v[174:175], v243 offset:4864\n"
    "ds_read_b32 v236, v253 offset:0\n"
    "ds_read_b32 v238, v253 offset:4\n"
    "ds_read_b128 v[160:163], v234 offset:6144\n"
    "ds_read_b128 v[164:167], v235 offset:6144\n"
    "s_cbranch_vccz .Ldin_A1s_np\n"
    "s_setprio 1\n"
    ".Ldin_A1s_np:\n"
    "v_pk_fma_f32 v[144:145], v[144:145], s[70:71], v[96:97]\n"
    "v_pk_fma_f32 v[146:147], v[146:147], s[70:71], v[98:99]\n"
    "v_add_f32 v191, v224, v225\n"
    "v_add_f32 v190, v226, v227\n"
    "v_add_f32 v191, v191, v190\n"
    "v_add_u32 v190, s62, v249\n"
    "v_mul_f32 v191, s69, v191\n"
    "v_cvt_pk_bf16_f32 v191, v191, v191\n"
    "ds_write_b16 v190, v191 offset:32768\n"
    "s_bfe_u32 s94, s59, 0x180006\n"
    "s_mul_i32 s86, s94, s56\n"
    "v_mbcnt_lo_u32_b32 v184, s20, 0\n"
    "v_mbcnt_lo_u32_b32 v185, s22, 0\n"
    "v_mbcnt_lo_u32_b32 v186, s24, 0\n"
    "v_mbcnt_lo_u32_b32 v187, s26, 0\n"
    "s_bcnt1_i32_b64 s90, s[20:21]\n"
    "s_bcnt1_i32_b64 s91, s[22:23]\n"
    "s_bcnt1_i32_b64 s92, s[24:25]\n"
    "s_bcnt1_i32_b64 s93, s[26:27]\n"
    "v_mbcnt_hi_u32_b32 v184, s21, v184\n"
    "v_mbcnt_hi_u32_b32 v185, s23, v185\n"
    "v_mbcnt_hi_u32_b32 v186, s25, v186\n"
    "v_mbcnt_hi_u32_b32 v187, s27, v187\n"
    "s_lshl2_add_u32 s90, s90, s65\n"
    "s_lshl2_add_u32 s91, s91, s90\n"
    "s_lshl2_add_u32 s92, s92, s91\n"
    "s_lshl2_add_u32 s93, s93, s92\n"
    "v_add_u32_e64 v188, s86, v251\n"
    "v_pk_add_f32 v[230:231], v[144:145], v[146:147]\n"
    "v_pk_mul_f32 v[232:233], v[144:145], v[146:147]\n"
    "v_pk_fma_f32 v[148:149], v[148:149], s[70:71], v[100:101]\n"
    "v_pk_fma_f32 v[230:231], v[230:231], v[148:149], v[232:233]\n"
    "v_pk_mul_f32 v[232:233], v[232:233], v[148:149]\n"
    "v_lshl_add_u32 v184, v184, 2, s65\n"
    "v_lshl_add_u32 v185, v185, 2, s90\n"
    "v_lshl_add_u32 v186, v186, 2, s91\n"
    "v_lshl_add_u32 v187, v187, 2, s92\n"
    "v_mov_b32 v191, -1\n"
    "v_lshl_add_u32 v190, v249, 1, s93\n"
    "ds_write2_b32 v190, v191, v191 offset1:64\n"
    "s_mov_b64 exec, s[20:21]\n"
    "ds_write_b32 v184, v188\n"
    "s_mov_b64 exec, s[22:23]\n"
    "v_add_u32 v189, 64, v188\n"
    "ds_write_b32 v185, v189\n"
    "s_mov_b64 exec, s[24:25]\n"
    "v_add_u32 v190, 128, v188\n"
    "ds_write_b32 v186, v190\n"
    "s_mov_b64 exec, s[26:27]\n"
    "v_add_u32 v189, 192, v188\n"
    "ds_write_b32 v187, v189\n"
    "s_mov_b64 exec, -1\n"
    "s_add_u32 s95, s93, 60\n"
    "s_add_u32 s94, s82, 128\n"
    "s_andn2_b32 s95, s95, 63\n"
    "s_max_u32 s83, s95, s94\n"
    "v_pk_fma_f32 v[150:151], v[150:151], s[70:71], v[102:103]\n"
    "v_pk_fma_f32 v[230:231], v[230:231], v[150:151], v[232:233]\n"
    "v_pk_mul_f32 v[232:233], v[232:233], v[150:151]\n"
    "v_pk_fma_f32 v[152:153], v[152:153], s[70:71], v[104:105]\n"
    "v_pk_fma_f32 v[230:231], v[230:231], v[152:153], v[232:233]\n"
    "v_pk_mul_f32 v[232:233], v[232:233], v[152:153]\n"
    "v_pk_fma_f32 v[154:155], v[154:155], s[70:71], v[106:107]\n"
    "v_pk_fma_f32 v[230:231], v[230:231], v[154:155], v[232:233]\n"
    "v_pk_mul_f32 v[232:233], v[232:233], v[154:155]\n"
    "v_pk_fma_f32 v[156:157], v[156:157], s[70:71], v[108:109]\n"
    "v_pk_fma_f32 v[230:231], v[230:231], v[156:157], v[232:233]\n"
    "v_pk_mul_f32 v[232:233], v[232:233], v[156:157]\n"
    "v_pk_fma_f32 v[158:159], v[158:159], s[70:71], v[110:111]\n"
    "v_pk_fma_f32 v[230:231], v[230:231], v[158:159], v[232:233]\n"
    "v_pk_mul_f32 v[232:233], v[232:233], v[158:159]\n"
    "v_rcp_f32 v232, v232\n"
    "v_rcp_f32 v233, v233\n"
    "v_fma_f32 v230, v230, v232, v247\n"
    "v_fmac_f32 v230, v231, v233\n"
    "v_lshlrev_b32 v232, 2, v249\n"
    "v_lshrrev_b32 v233, 2, v249\n"
    "v_and_b32 v232, 56, v232\n"
    "v_and_b32 v233, 4, v233\n"
    "v_add3_u32 v232, v232, v233, s81\n"
    "ds_read_b32 v232, v232\n"
    "s_add_u32 s81, s81, 64\n"
    "s_waitcnt lgkmcnt(0)\n"
    "v_lshlrev_b32 v232, 1, v232\n"
    "buffer_load_ushort v232, v232, s[44:47], 0 offen\n"
    "s_waitcnt vmcnt(0)\n"
    "v_lshlrev_b32 v232, 16, v232\n"
    "v_mul_f32 v230, v230, v232\n"
    "v_cvt_pk_bf16_f32 v230, v230, v230\n"
    "s_add_u32 s67, s67, 64\n"
    "s_waitcnt lgkmcnt(0)\n"
    "ds_write_b16 v245, v230\n"
    "ds_read_b64 v[228:229], v246\n"
    "buffer_load_dwordx4 v[236:237], s[40:43], 0 idxen offen offset:0 nt lds\n"
    "buffer_load_dwordx4 v[238:239], s[40:43], 0 idxen offen offset:1024 nt lds\n"
    ".Ldin_B2s:\n"
    "v_mfma_f32_16x16x32_bf16 v[144:147], v[0:3], v[160:163], v[112:115]\n"
    "v_exp_f32 v128, v128 clamp\n"
    "v_mfma_f32_16x16x32_bf16 v[144:147], v[4:7], v[164:167], v[144:147]\n"
    "v_exp_f32 v129, v129 clamp\n"
    "v_mfma_f32_16x16x32_bf16 v[148:151], v[8:11], v[160:163], v[116:119]\n"
    "v_exp_f32 v130, v130 clamp\n"
    "v_mfma_f32_16x16x32_bf16 v[148:151], v[12:15], v[164:167], v[148:151]\n"
    "v_exp_f32 v131, v131 clamp\n"
    "v_mfma_f32_16x16x32_bf16 v[152:155], v[16:19], v[160:163], v[120:123]\n"
    "v_exp_f32 v132, v132 clamp\n"
    "v_mfma_f32_16x16x32_bf16 v[152:155], v[20:23], v[164:167], v[152:155]\n"
    "v_exp_f32 v133, v133 clamp\n"
    "v_mfma_f32_16x16x32_bf16 v[156:159], v[24:27], v[160:163], v[124:127]\n"
    "v_exp_f32 v134, v134 clamp\n"
    "v_mfma_f32_16x16x32_bf16 v[156:159], v[28:31], v[164:167], v[156:159]\n"
    "v_exp_f32 v135, v135 clamp\n"
    "v_exp_f32 v136, v136 clamp\n"
    "s_cmp_eq_u32 s67, s80\n"
    "s_cselect_b32 s67, s65, s67\n"
    "s_and_b32 s94, s59, 63\n"
    "s_mul_i32 s94, s94, 272\n"
    "v_add_u32 v252, s94, v250\n"
    "s_add_u32 m0, s68, 6144\n"
    "v_exp_f32 v137, v137 clamp\n"
    "v_add_u32 v253, s67, v244\n"
    "s_waitcnt lgkmcnt(0)\n"
    "v_mfma_f32_4x4x4_16b_bf16 v[224:227], v[228:229], v[176:177], 0 cbsz:4 abid:0\n"
    "v_exp_f32 v138, v138 clamp\n"
    "v_exp_f32 v139, v139 clamp\n"
    "v_mfma_f32_4x4x4_16b_bf16 v[224:227], v[228:229], v[178:179], v[224:227] cbsz:4 abid:1\n"
    "v_exp_f32 v140, v140 clamp\n"
    "v_exp_f32 v141, v141 clamp\n"
    "v_mfma_f32_4x4x4_16b_bf16 v[224:227], v[228:229], v[180:181], v[224:227] cbsz:4 abid:2\n"
    "v_exp_f32 v142, v142 clamp\n"
    "v_exp_f32 v143, v143 clamp\n"
    "v_mfma_f32_4x4x4_16b_bf16 v[224:227], v[228:229], v[182:183], v[224:227] cbsz:4 abid:3\n"
    "s_waitcnt vmcnt(4)\n"
    "s_memrealtime s[34:35]\n"
    "ds_read_b64_tr_b16 v[176:177], v240 offset:6144\n"
    "ds_read_b64_tr_b16 v[178:179], v241 offset:6400\n"
    "ds_read_b64_tr_b16 v[180:181], v242 offset:6656\n"
    "ds_read_b64_tr_b16 v[182:183], v243 offset:6912\n"
    "ds_read_b32 v236, v253 offset:0\n"
    "ds_read_b32 v238, v253 offset:4\n"
    "ds_read_b128 v[160:163], v234 offset:0\n"
    "ds_read_b128 v[164:167], v235 offset:0\n"
    "ds_read_b128 v[184:187], v252 offset:0\n"
    "ds_read_b128 v[188:191], v252 offset:16\n"
    "v_pk_fma_f32 v[128:129], v[128:129], s[70:71], v[96:97]\n"
    "v_pk_fma_f32 v[130:131], v[130:131], s[70:71], v[98:99]\n"
    "v_pk_add_f32 v[230:231], v[128:129], v[130:131]\n"
    "v_pk_mul_f32 v[232:233], v[128:129], v[130:131]\n"
    "v_pk_fma_f32 v[132:133], v[132:133], s[70:71], v[100:101]\n"
    "v_pk_fma_f32 v[230:231], v[230:231], v[132:133], v[232:233]\n"
    "v_pk_mul_f32 v[232:233], v[232:233], v[132:133]\n"
    "v_pk_fma_f32 v[134:135], v[134:135], s[70:71], v[102:103]\n"
    "v_pk_fma_f32 v[230:231], v[230:231], v[134:135], v[232:233]\n"
    "v_pk_mul_f32 v[232:233], v[232:233], v[134:135]\n"
    "v_pk_fma_f32 v[136:137], v[136:137], s[70:71], v[104:105]\n"
    "v_pk_fma_f32 v[230:231], v[230:231], v[136:137], v[232:233]\n"
    "v_pk_mul_f32 v[232:233], v[232:233], v[136:137]\n"
    "v_pk_fma_f32 v[138:139], v[138:139], s[70:71], v[106:107]\n"
    "v_pk_fma_f32 v[230:231], v[230:231], v[138:139], v[232:233]\n"
    "v_pk_mul_f32 v[232:233], v[232:233], v[138:139]\n"
    "v_pk_fma_f32 v[140:141], v[140:141], s[70:71], v[108:109]\n"
    "v_pk_fma_f32 v[230:231], v[230:231], v[140:141], v[232:233]\n"
    "v_pk_mul_f32 v[232:233], v[232:233], v[140:141]\n"
    "v_pk_fma_f32 v[142:143], v[142:143], s[70:71], v[110:111]\n"
    "v_pk_fma_f32 v[230:231], v[230:231], v[142:143], v[232:233]\n"
    "v_pk_mul_f32 v[232:233], v[232:233], v[142:143]\n"
    "v_rcp_f32 v232, v232\n"
    "v_rcp_f32 v233, v233\n"
    "v_fma_f32 v230, v230, v232, v247\n"
    "v_fmac_f32 v230, v231, v233\n"
    "v_lshlrev_b32 v232, 2, v249\n"
    "v_lshrrev_b32 v233, 2, v249\n"
    "v_and_b32 v232, 56, v232\n"
    "v_and_b32 v233, 4, v233\n"
    "v_add3_u32 v232, v232, v233, s81\n"
    "ds_read_b32 v232, v232\n"
    "s_add_u32 s81, s81, 64\n"
    "s_waitcnt lgkmcnt(0)\n"
    "v_lshlrev_b32 v232, 1, v232\n"
    "buffer_load_ushort v232, v232, s[44:47], 0 offen\n"
    "s_waitcnt vmcnt(0)\n"
    "v_lshlrev_b32 v232, 16, v232\n"
    "v_mul_f32 v230, v230, v232\n"
    "v_cvt_pk_bf16_f32 v230, v230, v230\n"
    "s_add_u32 s67, s67, 64\n"
    "s_waitcnt lgkmcnt(0)\n"
    "ds_write_b16 v245, v230\n"
    "ds_read_b64 v[228:229], v246\n"
    "s_cmp_le_u32 s34, s76\n"
    "s_cbranch_scc1 .Ldin_B2s_wait\n"
    ".Ldin_B2s_go:\n"
    "s_sub_u32 s94, s34, 60\n"
    "s_max_u32 s76, s76, s94\n"
    "s_add_u32 s76, s76, s72\n"
    ".Ldin_B2s_dma:\n"
    "buffer_load_dwordx4 v[236:237], s[40:43], 0 idxen offen offset:0 nt lds\n"
    "buffer_load_dwordx4 v[238:239], s[40:43], 0 idxen offen offset:1024 nt lds\n"
    ".Ldin_B2s_nd:\n"
    "s_branch .Ldin_G3s\n"
    ".Ldin_L0s_poll:\n"
    "s_sleep 1\n"
    "ds_read_b64 v[232:233], v253\n"
    "s_waitcnt lgkmcnt(0)\n"
    "v_readfirstlane_b32 s60, v232\n"
    "s_cmp_eq_u32 s60, -1\n"
    "s_cbranch_scc1 .Ldin_L0s_poll\n"
    "s_branch .Ldin_L0s_pr\n"
    ".Ldin_B2s_wait:\n"
    "s_cmp_lt_i32 s59, s57\n"
    "s_cbranch_scc1 .Ldin_B2s_w2\n"
    "s_sub_u32 s94, s67, s65\n"
    "s_cmp_gt_u32 s94, 256\n"
    "s_cbranch_scc1 .Ldin_B2s_dma\n"
    "s_cmp_eq_u32 s94, 64\n"
    "s_waitcnt vmcnt(2)\n"
    "s_cbranch_scc1 .Ldin_B2s_nd\n"
    "s_waitcnt vmcnt(0)\n"
    "s_branch .Ldin_B2s_nd\n"
    ".Ldin_B2s_slp:\n"
    "s_sleep 1\n"
    ".Ldin_B2s_w2:\n"
    "s_memrealtime s[34:35]\n"
    "s_waitcnt lgkmcnt(0)\n"
    "s_sub_u32 s94, s34, s76\n"
    "s_cmp_lt_i32 s94, 0\n"
    "s_cbranch_scc1 .Ldin_B2s_slp\n"
    "s_branch .Ldin_B2s_go\n"
    ".p2align 5\n"
    ".Ldin_L3s:\n"
    "s_setprio 0\n"
    "s_cmp_ge_i32 s59, s57\n"
    "s_cbranch_scc1 .Ldin_E1s\n"
    "s_add_u32 s76, s76, s73\n"
    "v_mov_b32 v253, s63\n"
    "v_and_b32 v184, v254, v184\n"
    "v_and_b32 v185, v255, v185\n"
    "v_and_b32 v186, v254, v186\n"
    "v_and_b32 v187, v255, v187\n"
    "v_and_b32 v188, v254, v188\n"
    "v_and_b32 v189, v255, v189\n"
    "v_and_b32 v190, v254, v190\n"
    "v_and_b32_e64 v191, v255, v191\n"
    "ds_read_b64 v[232:233], v253\n"
    "v_mfma_f32_4x4x4_16b_bf16 v[128:131], v[184:185], v[32:33], v[64:67]\n"
    "ds_read_b128 v[112:115], v252 offset:17408\n"
    "v_mfma_f32_4x4x4_16b_bf16 v[132:135], v[186:187], v[34:35], v[68:71]\n"
    "ds_read_b128 v[116:119], v252 offset:17424\n"
    "v_mfma_f32_4x4x4_16b_bf16 v[136:139], v[188:189], v[36:37], v[72:75]\n"
    "ds_read_b128 v[120:123], v252 offset:17536\n"
    "ds_read_b128 v[124:127], v252 offset:17552\n"
    "v_cvt_pk_bf16_f32 v0, v128, v129\n"
    "v_cvt_pk_bf16_f32 v1, v130, v131\n"
    "v_mfma_f32_4x4x4_16b_bf16 v[140:143], v[190:191], v[38:39], v[76:79]\n"
    "v_cvt_pk_bf16_f32 v2, v132, v133\n"
    "v_cvt_pk_bf16_f32 v3, v134, v135\n"
    "v_mfma_f32_4x4x4_16b_bf16 v[128:131], v[184:185], v[40:41], v[80:83]\n"
    "v_cvt_pk_bf16_f32 v4, v136, v137\n"
    "v_cvt_pk_bf16_f32 v5, v138, v139\n"
    "v_mfma_f32_4x4x4_16b_bf16 v[132:135], v[186:187], v[42:43], v[84:87]\n"
    "v_cvt_pk_bf16_f32 v6, v140, v141\n"
    "v_cvt_pk_bf16_f32 v7, v142, v143\n"
    "v_mfma_f32_4x4x4_16b_bf16 v[136:139], v[188:189], v[44:45], v[88:91]\n"
    "v_cvt_pk_bf16_f32 v8, v128, v129\n"
    "v_cvt_pk_bf16_f32 v9, v130, v131\n"
    "v_mfma_f32_4x4x4_16b_bf16 v[140:143], v[190:191], v[46:47], v[92:95]\n"
    "v_cvt_pk_bf16_f32 v10, v132, v133\n"
    "v_cvt_pk_bf16_f32 v11, v134, v135\n"
    "v_mfma_f32_4x4x4_16b_bf16 v[128:131], v[184:185], v[48:49], v[192:195]\n"
    "v_cvt_pk_bf16_f32 v12, v136, v137\n"
    "v_cvt_pk_bf16_f32 v13, v138, v139\n"
    "v_mfma_f32_4x4x4_16b_bf16 v[132:135], v[186:187], v[50:51], v[196:199]\n"
    "v_cvt_pk_bf16_f32 v14, v140, v141\n"
    "v_cvt_pk_bf16_f32 v15, v142, v143\n"
    "v_mfma_f32_4x4x4_16b_bf16 v[136:139], v[188:189], v[52:53], v[200:203]\n"
    "s_waitcnt lgkmcnt(0)\n"
    "v_readfirstlane_b32 s60, v232\n"
    "v_cvt_pk_bf16_f32 v16, v128, v129\n"
    "v_cvt_pk_bf16_f32 v17, v130, v131\n"
    "v_mfma_f32_4x4x4_16b_bf16 v[140:143], v[190:191], v[54:55], v[204:207]\n"
    "s_cmp_eq_u32 s60, -1\n"
    "s_cbranch_scc1 .Ldin_L3s_poll\n"
    ".Ldin_L3s_pr:\n"
    "v_readfirstlane_b32 s95, v233\n"
    "s_and_b32 s94, s60, 63\n"
    "s_lshl_b32 s94, s94, 6\n"
    "s_add_u32 s94, s94, 68480\n"
    "s_add_u32 s63, s63, 8\n"
    "v_lshl_add_u32 v252, v249, 1, s94\n"
    "ds_read_b32 v252, v252\n"
    "s_sub_u32 s95, s95, s76\n"
    "s_ashr_i32 s95, s95, 4\n"
    "s_min_i32 s95, s95, s74\n"
    "s_max_i32 s72, s95, s75\n"
    "v_cvt_pk_bf16_f32 v18, v132, v133\n"
    "v_cvt_pk_bf16_f32 v19, v134, v135\n"
    "v_mfma_f32_4x4x4_16b_bf16 v[128:131], v[184:185], v[56:57], v[208:211]\n"
    "v_cvt_pk_bf16_f32 v20, v136, v137\n"
    "v_cvt_pk_bf16_f32 v21, v138, v139\n"
    "v_mfma_f32_4x4x4_16b_bf16 v[132:135], v[186:187], v[58:59], v[212:215]\n"
    "v_cvt_pk_bf16_f32 v22, v140, v141\n"
    "v_cvt_pk_bf16_f32 v23, v142, v143\n"
    "v_mfma_f32_4x4x4_16b_bf16 v[136:139], v[188:189], v[60:61], v[216:219]\n"
    "v_cvt_pk_bf16_f32 v24, v128, v129\n"
    "v_cvt_pk_bf16_f32 v25, v130, v131\n"
    "v_mfma_f32_4x4x4_16b_bf16 v[140:143], v[190:191], v[62:63], v[220:223]\n"
    "v_cvt_pk_bf16_f32 v26, v132, v133\n"
    "v_cvt_pk_bf16_f32 v27, v134, v135\n"
    "v_cvt_pk_bf16_f32 v28, v136, v137\n"
    "v_cvt_pk_bf16_f32 v29, v138, v139\n"
    "v_exp_f32 v152, v152 clamp\n"
    "v_cvt_pk_bf16_f32 v30, v140, v141\n"
    "v_cvt_pk_bf16_f32 v31, v142, v143\n"
    "v_mfma_f32_16x16x32_bf16 v[128:131], v[0:3], v[160:163], v[112:115]\n"
    "v_exp_f32 v144, v144 clamp\n"
    "v_mfma_f32_16x16x32_bf16 v[128:131], v[4:7], v[164:167], v[128:131]\n"
    "v_exp_f32 v145, v145 clamp\n"
    "v_mfma_f32_16x16x32_bf16 v[132:135], v[8:11], v[160:163], v[116:119]\n"
    "v_exp_f32 v146, v146 clamp\n"
    "v_mfma_f32_16x16x32_bf16 v[132:135], v[12:15], v[164:167], v[132:135]\n"
    "v_exp_f32 v147, v147 clamp\n"
    "v_mfma_f32_16x16x32_bf16 v[136:139], v[16:19], v[160:163], v[120:123]\n"
    "v_exp_f32 v148, v148 clamp\n"
    "v_mfma_f32_16x16x32_bf16 v[136:139], v[20:23], v[164:167], v[136:139]\n"
    "v_exp_f32 v149, v149 clamp\n"
    "v_mfma_f32_16x16x32_bf16 v[140:143], v[24:27], v[160:163], v[124:127]\n"
    "v_exp_f32 v150, v150 clamp\n"
    "v_mfma_f32_16x16x32_bf16 v[140:143], v[28:31], v[164:167], v[140:143]\n"
    "v_exp_f32 v151, v151 clamp\n"
    "s_add_u32 m0, s68, 0\n"
    "v_exp_f32 v153, v153 clamp\n"
    "v_add_u32_e64 v253, s67, v244\n"
    "v_mfma_f32_4x4x4_16b_bf16 v[224:227], v[228:229], v[168:169], v[224:227] cbsz:4 abid:0\n"
    "v_exp_f32 v154, v154 clamp\n"
    "v_exp_f32 v155, v155 clamp\n"
    "v_mfma_f32_4x4x4_16b_bf16 v[224:227], v[228:229], v[170:171], v[224:227] cbsz:4 abid:1\n"
    "v_exp_f32 v156, v156 clamp\n"
    "v_exp_f32 v157, v157 clamp\n"
    "v_mfma_f32_4x4x4_16b_bf16 v[224:227], v[228:229], v[172:173], v[224:227] cbsz:4 abid:2\n"
    "v_exp_f32 v158, v158 clamp\n"
    "v_exp_f32 v159, v159 clamp\n"
    "v_mfma_f32_4x4x4_16b_bf16 v[224:227], v[228:229], v[174:175], v[224:227] cbsz:4 abid:3\n"
    "s_waitcnt vmcnt(4)\n"
    "ds_read_b64_tr_b16 v[168:169], v240 offset:0\n"
    "ds_read_b64_tr_b16 v[170:171], v241 offset:256\n"
    "ds_read_b64_tr_b16 v[172:173], v242 offset:512\n"
    "ds_read_b64_tr_b16 v[174:175], v243 offset:768\n"
    "ds_read_b32 v236, v253 offset:0\n"
    "ds_read_b32 v238, v253 offset:4\n"
    "ds_read_b128 v[160:163], v234 offset:2048\n"
    "ds_read_b128 v[164:167], v235 offset:2048\n"
    "s_waitcnt lgkmcnt(8)\n"
    "s_nop 0\n"
    "v_readlane_b32 s20, v252, 0\n"
    "v_readlane_b32 s21, v252, 1\n"
    "v_readlane_b32 s22, v252, 2\n"
    "v_readlane_b32 s23, v252, 3\n"
    "v_readlane_b32 s24, v252, 4\n"
    "v_readlane_b32 s25, v252, 5\n"
    "v_readlane_b32 s26, v252, 6\n"
    "v_readlane_b32 s27, v252, 7\n"
    "v_pk_fma_f32 v[144:145], v[144:145], s[70:71], v[96:97]\n"
    "v_pk_fma_f32 v[146:147], v[146:147], s[70:71], v[98:99]\n"
    "v_pk_add_f32 v[230:231], v[144:145], v[146:147]\n"
    "v_pk_mul_f32 v[232:233], v[144:145], v[146:147]\n"
    "v_pk_fma_f32 v[148:149], v[148:149], s[70:71], v[100:101]\n"
    "v_pk_fma_f32 v[230:231], v[230:231], v[148:149], v[232:233]\n"
    "v_pk_mul_f32 v[232:233], v[232:233], v[148:149]\n"
    "v_pk_fma_f32 v[150:151], v[150:151], s[70:71], v[102:103]\n"
    "v_pk_fma_f32 v[230:231], v[230:231], v[150:151], v[232:233]\n"
    "v_pk_mul_f32 v[232:233], v[232:233], v[150:151]\n"
    "v_pk_fma_f32 v[152:153], v[152:153], s[70:71], v[104:105]\n"
    "v_pk_fma_f32 v[230:231], v[230:231], v[152:153], v[232:233]\n"
    "v_pk_mul_f32 v[232:233], v[232:233], v[152:153]\n"
    "v_pk_fma_f32 v[154:155], v[154:155], s[70:71], v[106:107]\n"
    "v_pk_fma_f32 v[230:231], v[230:231], v[154:155], v[232:233]\n"
    "v_pk_mul_f32 v[232:233], v[232:233], v[154:155]\n"
    "v_pk_fma_f32 v[156:157], v[156:157], s[70:71], v[108:109]\n"
    "v_pk_fma_f32 v[230:231], v[230:231], v[156:157], v[232:233]\n"
    "v_pk_mul_f32 v[232:233], v[232:233], v[156:157]\n"
    "v_pk_fma_f32 v[158:159], v[158:159], s[70:71], v[110:111]\n"
    "v_pk_fma_f32 v[230:231], v[230:231], v[158:159], v[232:233]\n"
    "v_pk_mul_f32 v[232:233], v[232:233], v[158:159]\n"
    "v_rcp_f32 v232, v232\n"
    "v_rcp_f32 v233, v233\n"
    "v_fma_f32 v230, v230, v232, v247\n"
    "v_fmac_f32 v230, v231, v233\n"
    "v_lshlrev_b32 v232, 2, v249\n"
    "v_lshrrev_b32 v233, 2, v249\n"
    "v_and_b32 v232, 56, v232\n"
    "v_and_b32 v233, 4, v233\n"
    "v_add3_u32 v232, v232, v233, s81\n"
    "ds_read_b32 v232, v232\n"
    "s_add_u32 s81, s81, 64\n"
    "s_waitcnt lgkmcnt(0)\n"
    "v_lshlrev_b32 v232, 1, v232\n"
    "buffer_load_ushort v232, v232, s[44:47], 0 offen\n"
    "s_waitcnt vmcnt(0)\n"
    "v_lshlrev_b32 v232, 16, v232\n"
    "v_mul_f32 v230, v230, v232\n"
    "v_cvt_pk_bf16_f32 v230, v230, v230\n"
    "s_sub_u32 s67, s67, -64\n"
    "s_waitcnt lgkmcnt(0)\n"
    "ds_write_b16 v245, v230\n"
    "ds_read_b64 v[228:229], v246\n"
    "s_lshl1_add_u32 s76, s72, s76\n"
    "buffer_load_dwordx4 v[236:237], s[40:43], 0 idxen offen offset:0 nt lds\n"
    "buffer_load_dwordx4 v[238:239], s[40:43], 0 idxen offen offset:1024 nt lds\n"
    "s_and_b32 s62, s58, 63\n"
    "s_mul_i32 s62, s62, 272\n"
    "s_mov_b32 s58, s59\n"
    "s_mov_b32 s59, s60\n"
    "s_mov_b32 s81, s65\n"
    "s_xor_b32 s65, s65, s64\n"
    "s_mov_b32 s80, s83\n"
    "s_add_u32 s82, s65, 256\n"
    "s_cmp_lt_i32 s59, s57\n"
    "s_cselect_b32 s76, s76, -1\n"
    "s_bitcmp1_b32 s58, 30\n"
    "s_cbranch_scc0 .Ldin_A0\n"
    "s_branch .Ldin_A0s\n"
    ".Ldin_L3s_poll:\n"
    "s_sleep 1\n"
    "ds_read_b64 v[232:233], v253\n"
    "s_waitcnt lgkmcnt(0)\n"
    "v_readfirstlane_b32 s60, v232\n"
    "s_cmp_eq_u32 s60, -1\n"
    "s_cbranch_scc1 .Ldin_L3s_poll\n"
    "s_branch .Ldin_L3s_pr\n"
    ".Ldin_E0s:\n"
    "v_exp_f32 v128, v128 clamp\n"
    "v_exp_f32 v129, v129 clamp\n"
    "v_exp_f32 v130, v130 clamp\n"
    "v_exp_f32 v131, v131 clamp\n"
    "v_exp_f32 v132, v132 clamp\n"
    "v_exp_f32 v133, v133 clamp\n"
    "v_exp_f32 v134, v134 clamp\n"
    "v_exp_f32 v135, v135 clamp\n"
    "s_and_b32 s62, s58, 63\n"
    "s_mul_i32 s62, s62, 272\n"
    "s_waitcnt lgkmcnt(0)\n"
    "v_mfma_f32_4x4x4_16b_bf16 v[224:227], v[228:229], v[176:177], v[224:227] cbsz:4 abid:0\n"
    "v_exp_f32 v136, v136 clamp\n"
    "v_exp_f32 v137, v137 clamp\n"
    "v_mfma_f32_4x4x4_16b_bf16 v[224:227], v[228:229], v[178:179], v[224:227] cbsz:4 abid:1\n"
    "v_exp_f32 v138, v138 clamp\n"
    "v_exp_f32 v139, v139 clamp\n"
    "v_mfma_f32_4x4x4_16b_bf16 v[224:227], v[228:229], v[180:181], v[224:227] cbsz:4 abid:2\n"
    "v_exp_f32 v140, v140 clamp\n"
    "v_exp_f32 v141, v141 clamp\n"
    "v_mfma_f32_4x4x4_16b_bf16 v[224:227], v[228:229], v[182:183], v[224:227] cbsz:4 abid:3\n"
    "v_exp_f32 v142, v142 clamp\n"
    "v_exp_f32 v143, v143 clamp\n"
    "v_pk_fma_f32 v[128:129], v[128:129], s[70:71], v[96:97]\n"
    "v_pk_fma_f32 v[130:131], v[130:131], s[70:71], v[98:99]\n"
    "v_pk_add_f32 v[230:231], v[128:129], v[130:131]\n"
    "v_pk_mul_f32 v[232:233], v[128:129], v[130:131]\n"
    "v_pk_fma_f32 v[132:133], v[132:133], s[70:71], v[100:101]\n"
    "v_pk_fma_f32 v[230:231], v[230:231], v[132:133], v[232:233]\n"
    "v_pk_mul_f32 v[232:233], v[232:233], v[132:133]\n"
    "v_pk_fma_f32 v[134:135], v[134:135], s[70:71], v[102:103]\n"
    "v_pk_fma_f32 v[230:231], v[230:231], v[134:135], v[232:233]\n"
    "v_pk_mul_f32 v[232:233], v[232:233], v[134:135]\n"
    "v_pk_fma_f32 v[136:137], v[136:137], s[70:71], v[104:105]\n"
    "v_pk_fma_f32 v[230:231], v[230:231], v[136:137], v[232:233]\n"
    "v_pk_mul_f32 v[232:233], v[232:233], v[136:137]\n"
    "v_pk_fma_f32 v[138:139], v[138:139], s[70:71], v[106:107]\n"
    "v_pk_fma_f32 v[230:231], v[230:231], v[138:139], v[232:233]\n"
    "v_pk_mul_f32 v[232:233], v[232:233], v[138:139]\n"
    "v_pk_fma_f32 v[140:141], v[140:141], s[70:71], v[108:109]\n"
    "v_pk_fma_f32 v[230:231], v[230:231], v[140:141], v[232:233]\n"
    "v_pk_mul_f32 v[232:233], v[232:233], v[140:141]\n"
    "v_pk_fma_f32 v[142:143], v[142:143], s[70:71], v[110:111]\n"
    "v_pk_fma_f32 v[230:231], v[230:231], v[142:143], v[232:233]\n"
    "v_pk_mul_f32 v[232:233], v[232:233], v[142:143]\n"
    "v_rcp_f32 v232, v232\n"
    "v_rcp_f32 v233, v233\n"
    "v_fma_f32 v230, v230, v232, v247\n"
    "v_fmac_f32 v230, v231, v233\n"
    "v_lshlrev_b32 v232, 2, v249\n"
    "v_lshrrev_b32 v233, 2, v249\n"
    "v_and_b32 v232, 56, v232\n"
    "v_and_b32 v233, 4, v233\n"
    "v_add3_u32 v232, v232, v233, s81\n"
    "ds_read_b32 v232, v232\n"
    "s_add_u32 s81, s81, 64\n"
    "s_waitcnt lgkmcnt(0)\n"
    "v_lshlrev_b32 v232, 1, v232\n"
    "buffer_load_ushort v232, v232, s[44:47], 0 offen\n"
    "s_waitcnt vmcnt(0)\n"
    "v_lshlrev_b32 v232, 16, v232\n"
    "v_mul_f32 v230, v230, v232\n"
    "v_cvt_pk_bf16_f32 v230, v230, v230\n"
    "ds_write_b16 v245, v230\n"
    "ds_read_b64 v[228:229], v246\n"
    "s_branch .Ldin_drain0\n"
    ".Ldin_E1s:\n"
    "v_exp_f32 v144, v144 clamp\n"
    "v_exp_f32 v145, v145 clamp\n"
    "v_exp_f32 v146, v146 clamp\n"
    "v_exp_f32 v147, v147 clamp\n"
    "v_exp_f32 v148, v148 clamp\n"
    "v_exp_f32 v149, v149 clamp\n"
    "v_exp_f32 v150, v150 clamp\n"
    "v_exp_f32 v151, v151 clamp\n"
    "s_and_b32 s62, s58, 63\n"
    "s_mul_i32 s62, s62, 272\n"
    "s_waitcnt lgkmcnt(0)\n"
    "v_mfma_f32_4x4x4_16b_bf16 v[224:227], v[228:229], v[168:169], v[224:227] cbsz:4 abid:0\n"
    "v_exp_f32 v152, v152 clamp\n"
    "v_exp_f32 v153, v153 clamp\n"
    "v_mfma_f32_4x4x4_16b_bf16 v[224:227], v[228:229], v[170:171], v[224:227] cbsz:4 abid:1\n"
    "v_exp_f32 v154, v154 clamp\n"
    "v_exp_f32 v155, v155 clamp\n"
    "v_mfma_f32_4x4x4_16b_bf16 v[224:227], v[228:229], v[172:173], v[224:227] cbsz:4 abid:2\n"
    "v_exp_f32 v156, v156 clamp\n"
    "v_exp_f32 v157, v157 clamp\n"
    "v_mfma_f32_4x4x4_16b_bf16 v[224:227], v[228:229], v[174:175], v[224:227] cbsz:4 abid:3\n"
    "v_exp_f32 v158, v158 clamp\n"
    "v_exp_f32 v159, v159 clamp\n"
    "v_pk_fma_f32 v[144:145], v[144:145], s[70:71], v[96:97]\n"
    "v_pk_fma_f32 v[146:147], v[146:147], s[70:71], v[98:99]\n"
    "v_pk_add_f32 v[230:231], v[144:145], v[146:147]\n"
    "v_pk_mul_f32 v[232:233], v[144:145], v[146:147]\n"
    "v_pk_fma_f32 v[148:149], v[148:149], s[70:71], v[100:101]\n"
    "v_pk_fma_f32 v[230:231], v[230:231], v[148:149], v[232:233]\n"
    "v_pk_mul_f32 v[232:233], v[232:233], v[148:149]\n"
    "v_pk_fma_f32 v[150:151], v[150:151], s[70:71], v[102:103]\n"
    "v_pk_fma_f32 v[230:231], v[230:231], v[150:151], v[232:233]\n"
    "v_pk_mul_f32 v[232:233], v[232:233], v[150:151]\n"
    "v_pk_fma_f32 v[152:153], v[152:153], s[70:71], v[104:105]\n"
    "v_pk_fma_f32 v[230:231], v[230:231], v[152:153], v[232:233]\n"
    "v_pk_mul_f32 v[232:233], v[232:233], v[152:153]\n"
    "v_pk_fma_f32 v[154:155], v[154:155], s[70:71], v[106:107]\n"
    "v_pk_fma_f32 v[230:231], v[230:231], v[154:155], v[232:233]\n"
    "v_pk_mul_f32 v[232:233], v[232:233], v[154:155]\n"
    "v_pk_fma_f32 v[156:157], v[156:157], s[70:71], v[108:109]\n"
    "v_pk_fma_f32 v[230:231], v[230:231], v[156:157], v[232:233]\n"
    "v_pk_mul_f32 v[232:233], v[232:233], v[156:157]\n"
    "v_pk_fma_f32 v[158:159], v[158:159], s[70:71], v[110:111]\n"
    "v_pk_fma_f32 v[230:231], v[230:231], v[158:159], v[232:233]\n"
    "v_pk_mul_f32 v[232:233], v[232:233], v[158:159]\n"
    "v_rcp_f32 v232, v232\n"
    "v_rcp_f32 v233, v233\n"
    "v_fma_f32 v230, v230, v232, v247\n"
    "v_fmac_f32 v230, v231, v233\n"
    "v_lshlrev_b32 v232, 2, v249\n"
    "v_lshrrev_b32 v233, 2, v249\n"
    "v_and_b32 v232, 56, v232\n"
    "v_and_b32 v233, 4, v233\n"
    "v_add3_u32 v232, v232, v233, s81\n"
    "ds_read_b32 v232, v232\n"
    "s_add_u32 s81, s81, 64\n"
    "s_waitcnt lgkmcnt(0)\n"
    "v_lshlrev_b32 v232, 1, v232\n"
    "buffer_load_ushort v232, v232, s[44:47], 0 offen\n"
    "s_waitcnt vmcnt(0)\n"
    "v_lshlrev_b32 v232, 16, v232\n"
    "v_mul_f32 v230, v230, v232\n"
    "v_cvt_pk_bf16_f32 v230, v230, v230\n"
    "ds_write_b16 v245, v230\n"
    "ds_read_b64 v[228:229], v246\n"
    "s_branch .Ldin_drain1\n"
    ".Ldin_exit:\n"

      : "+{s[0:1]}"(kp), "+{v0}"(tid)
      : "{s2}"(blockIdx.x)
      : "memory", "vcc", "scc", "v1", "v2", "v3", "v4", "v5", "v6", "v7", "v8", "v9", "v10", "v11", "v12", "v13", "v14", "v15", "v16", "v17", "v18", "v19", "v20", "v21", "v22", "v23", "v24", "v25", "v26", "v27", "v28", "v29", "v30", "v31", "v32", "v33", "v34", "v35", "v36", "v37", "v38", "v39", "v40", "v41", "v42", "v43", "v44", "v45", "v46", "v47", "v48", "v49", "v50", "v51", "v52", "v53", "v54", "v55", "v56", "v57", "v58", "v59", "v60", "v61", "v62", "v63", "v64", "v65", "v66", "v67", "v68", "v69", "v70", "v71", "v72", "v73", "v74", "v75", "v76", "v77", "v78", "v79", "v80", "v81", "v82", "v83", "v84", "v85", "v86", "v87", "v88", "v89", "v90", "v91", "v92", "v93", "v94", "v95", "v96", "v97", "v98", "v99", "v100", "v101", "v102", "v103", "v104", "v105", "v106", "v107", "v108", "v109", "v110", "v111", "v112", "v113", "v114", "v115", "v116", "v117", "v118", "v119", "v120", "v121", "v122", "v123", "v124", "v125", "v126", "v127", "v128", "v129", "v130", "v131", "v132", "v133", "v134", "v135", "v136", "v137", "v138", "v139", "v140", "v141", "v142", "v143", "v144", "v145", "v146", "v147", "v148", "v149", "v150", "v151", "v152", "v153", "v154", "v155", "v156", "v157", "v158", "v159", "v160", "v161", "v162", "v163", "v164", "v165", "v166", "v167", "v168", "v169", "v170", "v171", "v172", "v173", "v174", "v175", "v176", "v177", "v178", "v179", "v180", "v181", "v182", "v183", "v184", "v185", "v186", "v187", "v188", "v189", "v190", "v191", "v192", "v193", "v194", "v195", "v196", "v197", "v198", "v199", "v200", "v201", "v202", "v203", "v204", "v205", "v206", "v207", "v208", "v209", "v210", "v211", "v212", "v213", "v214", "v215", "v216", "v217", "v218", "v219", "v220", "v221", "v222", "v223", "v224", "v225", "v226", "v227", "v228", "v229", "v230", "v231", "v232", "v233", "v234", "v235", "v236", "v237", "v238", "v239", "v240", "v241", "v242", "v243", "v244", "v245", "v246", "v247", "v248", "v249", "v250", "v251", "v252", "v253", "v254", "v255", "s3", "s4", "s5", "s6", "s7", "s8", "s9", "s10", "s11", "s12", "s13", "s14", "s15", "s16", "s17", "s18", "s19", "s20", "s21", "s22", "s23", "s24", "s25", "s26", "s27", "s28", "s29", "s30", "s31", "s33", "s34", "s35", "s36", "s37", "s38", "s39", "s40", "s41", "s42", "s43", "s44", "s45", "s46", "s47", "s48", "s49", "s50", "s51", "s52", "s53", "s54", "s55", "s56", "s57", "s58", "s59", "s60", "s61", "s62", "s63", "s64", "s65", "s66", "s67", "s68", "s69", "s70", "s71", "s72", "s73", "s74", "s75", "s76", "s77", "s78", "s79", "s80", "s81", "s82", "s83", "s84", "s85", "s86", "s87", "s88", "s89", "s90", "s91", "s92", "s93", "s94", "s95", "s96");
  asm volatile("" ::"s"((uint32_t)(uintptr_t)smem));
}

__global__ __launch_bounds__(64) void din_fallback_kernel(
    const unsigned short* __restrict__ target, const unsigned short* __restrict__ hist,
    const unsigned short* __restrict__ mask, const unsigned short* __restrict__ w1,
    const unsigned short* __restrict__ b1, const unsigned short* __restrict__ w2,
    const unsigned short* __restrict__ b2, unsigned short* __restrict__ out, int B, int L) {
  __shared__ float s_w[64 * 64];
  __shared__ float s_x[64];
  const int b = blockIdx.x, h = threadIdx.x;
  float bias = bf2f(b1[h]);
  for (int d = 0; d < 64; ++d) {
    const float t = bf2f(target[(size_t)b * 64 + d]);
    const float wc = bf2f(w1[(128 + d) * 64 + h]);
    bias += t * (bf2f(w1[(64 + d) * 64 + h]) - wc);
    s_w[d * 64 + h] = bf2f(w1[d * 64 + h]) + wc + t * bf2f(w1[(192 + d) * 64 + h]);
  }
  const float w2h = bf2f(w2[h]), b2v = bf2f(b2[0]);
  float acc = 0.f;
  for (int l = 0; l < L; ++l) {
    const float m = bf2f(mask[(size_t)b * L + l]);
    if (m == 0.f) continue;
    const float x = bf2f(hist[((size_t)b * L + l) * 64 + h]);
    __syncthreads();
    s_x[h] = x;
    __syncthreads();
    float z = bias;
    for (int d = 0; d < 64; ++d) z += s_x[d] * s_w[d * 64 + h];
    float s = w2h / (1.0f + __expf(-z));
    for (int o = 32; o > 0; o >>= 1) s += __shfl_xor(s, o);
    acc += (s + b2v) * m * x;
  }
  const uint32_t u = __float_as_uint(acc);
  out[(size_t)b * 64 + h] = (unsigned short)((u + 0x7fffu + ((u >> 16) & 1u)) >> 16);
}

extern "C" void kernel_launch(void* const* d_in, const int* in_sizes, int n_in, void* d_out,
                              int out_size, void* d_ws, size_t ws_size, hipStream_t stream) {
  const unsigned short* target = (const unsigned short*)d_in[0];
  const unsigned short* hist = (const unsigned short*)d_in[1];
  const unsigned short* mask = (const unsigned short*)d_in[2];
  const unsigned short* w1 = (const unsigned short*)d_in[3];
  const unsigned short* b1 = (const unsigned short*)d_in[4];
  const unsigned short* w2 = (const unsigned short*)d_in[5];
  const unsigned short* b2 = (const unsigned short*)d_in[6];
  const int B = in_sizes[0] / 64;
  const int L = in_sizes[2] / B;
  if ((L & 1) || L > 256 || B >= (1 << 24) - 64) {
    din_fallback_kernel<<<B, 64, 0, stream>>>(target, hist, mask, w1, b1, w2, b2, (unsigned short*)d_out, B, L);
    return;
  }
  const uint32_t grid = (B + 63) / 64, fl = 0x20000u;
  const bool paced = grid >= 224;
  din_kernel<<<grid, 512, 0, stream>>>(hist, (uint32_t)B * L, fl, mask, (uint32_t)B * L * 2u, fl, target, (uint32_t)B * 128u, fl,
                                       (unsigned short*)d_out, (uint32_t)B * 128u, fl, w1, w2, b1, b2, (uint32_t)L, NO_BATCH, (uint32_t)B,
                                       grid * 8u, 2u * L, grid * 16u * L, paced ? 52u : 0u, paced ? 15u : 0u);
}
```

```cpp
#include <hip/hip_runtime.h>
#include <stdint.h>

constexpr int SMEM_B = 163712;
constexpr uint32_t NO_BATCH = 0x7fffffc0u;

__device__ __forceinline__ float bf2f(unsigned short u) { return __uint_as_float(((uint32_t)u) << 16); }

__global__ __launch_bounds__(512, 2) __attribute__((aligned(4096))) void din_kernel(
    const unsigned short* hist, uint32_t rows, uint32_t f0, const unsigned short* mask, uint32_t mask_bytes, uint32_t f1,
    const unsigned short* target, uint32_t target_bytes, uint32_t f2, unsigned short* out, uint32_t out_bytes, uint32_t f3,
    const unsigned short* w1, const unsigned short* w2, const unsigned short* b1, const unsigned short* b2,
    uint32_t L, uint32_t no_batch, uint32_t B, uint32_t round_rows, uint32_t row_bytes, uint32_t round_bytes, uint32_t period, uint32_t extra) {
  __shared__ __attribute__((aligned(128))) char smem[SMEM_B];
  unsigned long long kp = (unsigned long long)(uintptr_t)__builtin_amdgcn_kernarg_segment_ptr();
  uint32_t tid = threadIdx.x;
  asm volatile(
    "s_load_dwordx16 s[40:55], s[0:1], 0x0\n"
    "s_load_dwordx16 s[4:19], s[0:1], 0x40\n"
    "s_memrealtime s[76:77]\n"
    "v_readfirstlane_b32 s3, v0\n"
    "v_and_b32 v251, 63, v0\n"
    "s_lshr_b32 s3, s3, 6\n"
    "s_lshl3_add_u32 s61, s2, s3\n"
    "s_cmp_lt_u32 s3, 4\n"
    "s_cbranch_scc0 .Ldin_young\n"
    "v_mul_u32_u24 v32, 0x9e3779, v251\n"
    "s_mov_b32 s90, 0x807f807f\n"
    "v_mov_b32 v47, 0x3f003f00\n"
    "v_add_u32 v32, 0x7f4a7c15, v32\n"
    "v_mul_lo_u32 v33, v32, v32\n"
    "v_mul_lo_u32 v34, v33, v32\n"
    "v_mul_lo_u32 v35, v34, v32\n"
    "v_mul_lo_u32 v36, v35, v32\n"
    "v_mul_lo_u32 v37, v36, v32\n"
    "v_mul_lo_u32 v38, v37, v32\n"
    "v_mul_lo_u32 v39, v38, v32\n"
    "v_and_or_b32 v32, v32, s90, v47\n"
    "v_and_or_b32 v33, v33, s90, v47\n"
    "v_and_or_b32 v34, v34, s90, v47\n"
    "v_and_or_b32 v35, v35, s90, v47\n"
    "v_and_or_b32 v36, v36, s90, v47\n"
    "v_and_or_b32 v37, v37, s90, v47\n"
    "v_and_or_b32 v38, v38, s90, v47\n"
    "v_and_or_b32 v39, v39, s90, v47\n"
    "s_nop 1\n"
    "v_lshlrev_b32 v105, 3, v251\n"
    "v_mov_b32 v8, 0\n"
    "v_mov_b32 v9, 0\n"
    "v_mov_b32 v10, 0\n"
    "v_lshlrev_b32 v249, 1, v251\n"
    "s_mul_i32 s68, s3, 11392\n"
    "v_lshlrev_b32_e64 v248, 4, v251\n"
    "s_add_u32 s68, s68, 72576\n"
    "v_and_b32 v24, 0x60, v249\n"
    "s_add_u32 s67, s68, 8192\n"
    "v_mfma_f32_32x32x16_bf16 v[64:79], v[36:39], v[32:35], 0\n"
    "v_or_b32 v250, 0x8000, v24\n"
    "s_add_u32 s65, s68, 9728\n"
    "v_lshrrev_b32 v20, 4, v251\n"
    "s_xor_b32 s64, s67, s65\n"
    "v_and_b32 v244, 56, v251\n"
    "s_add_u32 s96, s68, 11264\n"
    "v_bitop3_b32 v21, v20, v251, 7 bitop3:0x78\n"
    "v_mfma_f32_32x32x16_bf16 v[48:63], v[32:35], v[36:39], 0\n"
    "s_lshl_b32 s66, s3, 7\n"
    "v_lshlrev_b32 v21, 4, v21\n"
    "s_add_u32 s66, s66, 68512\n"
    "v_or_b32 v237, 0x400, v21\n"
    "s_lshr_b32 s95, s67, 2\n"
    "v_xor_b32 v239, 64, v21\n"
    "s_mov_b32 s38, 0xffff\n"
    "v_mfma_f32_32x32x16_bf16 v[64:79], v[36:39], v[32:35], 0\n"
    "v_add_u32 v253, s67, v244\n"
    "v_lshlrev_b32 v105, 3, v251\n"
    "v_mov_b32 v201, v237\n"
    "v_mov_b32 v203, v239\n"
    "v_mov_b32 v205, v237\n"
    "v_mov_b32 v207, v239\n"
    "v_mov_b32 v209, v237\n"
    "v_mfma_f32_32x32x16_bf16 v[48:63], v[32:35], v[36:39], 0\n"
    "v_mov_b32 v211, v239\n"
    "v_mov_b32 v213, v237\n"
    "v_mov_b32 v215, v239\n"
    "v_mov_b32 v220, s95\n"
    "v_mov_b32 v224, 0\n"
    "v_mov_b32 v225, 0\n"
    "v_mov_b32 v226, 0\n"
    "v_mfma_f32_32x32x16_bf16 v[64:79], v[36:39], v[32:35], 0\n"
    "v_mov_b32 v227, 0\n"
    "v_bfe_u32 v21, v251, 1, 3\n"
    "v_xor_b32 v21, v20, v21\n"
    "v_and_b32 v22, 0xf0, v248\n"
    "s_mov_b32 s70, 0x47000000\n"
    "v_lshl_add_u32 v22, v22, 3, s68\n"
    "s_mov_b32 s71, s70\n"
    "v_mfma_f32_32x32x16_bf16 v[48:63], v[32:35], v[36:39], 0\n"
    "v_lshl_add_u32 v234, v21, 4, v22\n"
    "s_add_u32 s63, s66, 16\n"
    "v_xor_b32 v235, 64, v234\n"
    "s_mov_b32 s81, s67\n"
    "v_and_b32 v20, 3, v251\n"
    "s_mov_b64 s[92:93], 0xffff\n"
    "v_lshlrev_b64 v[254:255], v248, s[92:93]\n"
    "v_mfma_f32_32x32x16_bf16 v[64:79], v[36:39], v[32:35], 0\n"
    "s_add_u32 s82, s65, 256\n"
    "v_lshl_or_b32 v21, v20, 3, v24\n"
    "s_mul_i32 s62, s3, 272\n"
    "v_and_b32 v22, 8, v251\n"
    "v_mul_u32_u24 v23, 136, v22\n"
    "v_xor_b32 v21, v21, v23\n"
    "v_and_b32 v23, 4, v251\n"
    "v_mfma_f32_32x32x16_bf16 v[48:63], v[32:35], v[36:39], 0\n"
    "v_lshl_or_b32 v21, v23, 5, v21\n"
    "v_add_u32 v240, s68, v21\n"
    "v_xor_b32 v241, 16, v240\n"
    "v_xor_b32 v242, 32, v240\n"
    "v_xor_b32 v243, 48, v240\n"
    "v_add_u32 v252, s62, v250\n"
    "v_and_b32 v21, 24, v249\n"
    "v_mfma_f32_32x32x16_bf16 v[64:79], v[36:39], v[32:35], 0\n"
    "v_add_u32 v21, s96, v21\n"
    "v_lshl_add_u32 v246, v20, 5, v21\n"
    "v_and_b32 v21, 6, v251\n"
    "v_lshl_or_b32 v21, v21, 2, v24\n"
    "v_and_b32 v23, 2, v249\n"
    "v_lshrrev_b32 v22, 1, v22\n"
    "v_or3_b32 v21, v21, v22, v23\n"
    "v_mfma_f32_32x32x16_bf16 v[48:63], v[32:35], v[36:39], 0\n"
    "v_add_u32 v245, s96, v21\n"
    "s_setprio 2\n"
    "s_waitcnt lgkmcnt(0)\n"
    "s_mul_i32 s36, s61, s16\n"
    "s_add_u32 s94, s36, s16\n"
    "s_cmp_lt_u32 s61, s14\n"
    "s_cselect_b32 s46, s94, 0\n"
    "buffer_load_dwordx2 v[228:229], v105, s[44:47], s36 offen nt\n"
    "s_load_dword s10, s[10:11], 0x0\n"
    "global_load_short_d16_hi v8, v249, s[6:7]\n"
    "global_load_short_d16_hi v9, v249, s[8:9]\n"
    "s_setprio 0\n"
    "s_branch .Ldin_old2\n"
    ".Ldin_young:\n"
    "s_setprio 1\n"
    "v_and_b32 v16, 7, v251\n"
    "s_lshl_b32 s94, s3, 11\n"
    "v_lshlrev_b32 v2, 4, v16\n"
    "v_and_b32 v3, 56, v251\n"
    "v_lshl_or_b32 v3, v3, 5, v2\n"
    "v_add_u32 v6, s94, v3\n"
    "v_add_u32 v3, 0x2000, v6\n"
    "v_add_u32 v4, 0xffffe000, v6\n"
    "v_add_u32 v5, 0x4000, v6\n"
    "v_and_b32 v17, 8, v251\n"
    "s_and_b32 s88, s3, 1\n"
    "v_and_b32 v18, 32, v251\n"
    "s_and_b32 s89, s3, 2\n"
    "v_lshl_or_b32 v18, v16, 2, v18\n"
    "s_lshl_b32 s84, s88, 9\n"
    "v_lshl_add_u32 v18, v18, 3, s84\n"
    "s_lshl_b32 s84, s89, 10\n"
    "v_and_b32 v19, 16, v251\n"
    "s_lshl_b32 s85, s89, 9\n"
    "v_lshl_or_b32 v19, v19, 6, v17\n"
    "s_add_u32 s85, s85, 0x4000\n"
    "v_add3_u32 v12, v18, v19, s84\n"
    "s_lshl2_add_u32 s86, s89, s88\n"
    "v_add_u32 v13, 16, v12\n"
    "s_lshl_b32 s86, s86, 4\n"
    "v_and_b32 v19, 24, v251\n"
    "s_add_u32 s87, s94, 0x4000\n"
    "v_lshrrev_b32 v19, 1, v19\n"
    "v_add3_u32 v14, v18, v19, s85\n"
    "v_add_u32 v15, 16, v14\n"
    "v_bfrev_b32 v7, 1\n"
    "v_lshlrev_b32 v26, 4, v251\n"
    "v_and_b32 v26, 0x1f0, v26\n"
    "v_lshrrev_b32 v27, 5, v251\n"
    "s_add_u32 s39, s61, s3\n"
    "s_sub_u32 s39, s39, 8\n"
    "v_lshlrev_b32 v249, 1, v251\n"
    "s_mul_i32 s68, s3, 11392\n"
    "v_lshlrev_b32_e64 v248, 4, v251\n"
    "s_add_u32 s68, s68, 72576\n"
    "v_and_b32 v24, 0x60, v249\n"
    "s_add_u32 s67, s68, 8192\n"
    "v_or_b32 v250, 0x8000, v24\n"
    "s_add_u32 s65, s68, 9728\n"
    "v_lshrrev_b32 v20, 4, v251\n"
    "s_xor_b32 s64, s67, s65\n"
    "v_and_b32 v244, 56, v251\n"
    "s_add_u32 s96, s68, 11264\n"
    "v_bitop3_b32 v21, v20, v251, 7 bitop3:0x78\n"
    "s_lshl_b32 s66, s3, 7\n"
    "v_lshlrev_b32 v21, 4, v21\n"
    "s_add_u32 s66, s66, 68512\n"
    "v_or_b32 v237, 0x400, v21\n"
    "s_lshr_b32 s95, s67, 2\n"
    "v_xor_b32 v239, 64, v21\n"
    "s_mov_b32 s38, 0xffff\n"
    "v_add_u32 v253, s67, v244\n"
    "v_lshlrev_b32 v105, 3, v251\n"
    "v_mov_b32 v201, v237\n"
    "v_mov_b32 v203, v239\n"
    "v_mov_b32 v205, v237\n"
    "v_mov_b32 v207, v239\n"
    "v_mov_b32 v209, v237\n"
    "v_mov_b32 v211, v239\n"
    "v_mov_b32 v213, v237\n"
    "v_mov_b32 v215, v239\n"
    "v_mov_b32 v220, s95\n"
    "v_mov_b32 v224, 0\n"
    "v_mov_b32 v225, 0\n"
    "s_waitcnt lgkmcnt(0)\n"
    "global_load_dwordx4 v[8:11], v2, s[6:7]\n"
    "global_load_dwordx4 v[128:131], v3, s[4:5] offset:0\n"
    "global_load_dwordx4 v[132:135], v3, s[4:5] offset:128\n"
    "v_mov_b32 v226, 0\n"
    "v_mov_b32 v227, 0\n"
    "v_bfe_u32 v21, v251, 1, 3\n"
    "v_xor_b32 v21, v20, v21\n"
    "v_and_b32 v22, 0xf0, v248\n"
    "s_mov_b32 s70, 0x47000000\n"
    "v_lshl_add_u32 v22, v22, 3, s68\n"
    "s_mov_b32 s71, s70\n"
    "v_lshl_add_u32 v234, v21, 4, v22\n"
    "s_add_u32 s63, s66, 16\n"
    "v_xor_b32 v235, 64, v234\n"
    "s_mov_b32 s81, s67\n"
    "global_load_dwordx4 v[136:139], v4, s[4:5] offset:0\n"
    "v_and_b32_e64 v20, 3, v251\n"
    "s_mov_b64 s[92:93], 0xffff\n"
    "v_lshlrev_b64 v[254:255], v248, s[92:93]\n"
    "s_add_u32 s82, s65, 256\n"
    "v_lshl_or_b32 v21, v20, 3, v24\n"
    "s_mul_i32 s62, s3, 272\n"
    "v_and_b32 v22, 8, v251\n"
    "v_mul_u32_u24 v23, 136, v22\n"
    "v_xor_b32 v21, v21, v23\n"
    "v_and_b32 v23, 4, v251\n"
    "v_lshl_or_b32 v21, v23, 5, v21\n"
    "v_add_u32 v240, s68, v21\n"
    "global_load_dwordx4 v[140:143], v4, s[4:5] offset:128\n"
    "v_xor_b32 v241, 16, v240\n"
    "v_xor_b32 v242, 32, v240\n"
    "v_xor_b32 v243, 48, v240\n"
    "v_add_u32 v252, s62, v250\n"
    "v_and_b32 v21, 24, v249\n"
    "v_add_u32 v21, s96, v21\n"
    "v_lshl_add_u32 v246, v20, 5, v21\n"
    "v_and_b32 v21, 6, v251\n"
    "v_lshl_or_b32 v21, v21, 2, v24\n"
    "v_and_b32 v23, 2, v249\n"
    "v_lshrrev_b32 v22, 1, v22\n"
    "v_or3_b32 v21, v21, v22, v23\n"
    "global_load_dwordx4 v[144:147], v5, s[4:5] offset:0\n"
    "v_add_u32 v245, s96, v21\n"
    "v_cmp_gt_u32 vcc, s16, v26\n"
    "v_mad_u32_u24 v27, v27, s16, v26\n"
    "s_mul_i32 s39, s39, s16\n"
    "s_lshl1_add_u32 s39, s17, s39\n"
    "v_cndmask_b32 v27, v7, v27, vcc\n"
    "s_add_u32 s40, s40, 0xfffffc00\n"
    "s_addc_u32 s41, s41, -1\n"
    "s_mul_i32 s94, s61, s12\n"
    "s_bitset1_b32 s41, 23\n"
    "v_lshl_add_u32 v216, v251, 2, s94\n"
    "s_mov_b64 s[56:57], s[12:13]\n"
    "global_load_dwordx4 v[148:151], v5, s[4:5] offset:128\n"
    "v_add_u32 v217, 1, v216\n"
    "v_add_u32 v218, 2, v216\n"
    "v_add_u32 v219, 3, v216\n"
    "s_mov_b64 s[72:73], s[18:19]\n"
    "s_cmp_lg_u32 s72, 0\n"
    "s_cselect_b32 s94, 6, 0\n"
    "s_cselect_b32 s95, 4, 0\n"
    "s_add_u32 s74, s72, s94\n"
    "s_sub_u32 s75, s72, s95\n"
    "v_mov_b32 v20, s13\n"
    "s_mov_b64 exec, 0xffffff\n"
    "v_mov_b32 v20, -1\n"
    "s_mov_b64 exec, 0xff\n"
    "v_mov_b32 v20, 0\n"
    "s_mov_b32 exec_lo, -1\n"
    "v_lshl_add_u32 v21, v249, 1, s66\n"
    "v_add_u32 v21, -32, v21\n"
    "ds_write_b32 v21, v20\n"
    "s_mov_b32 exec_hi, -1\n"
    "global_load_dwordx4 v[152:155], v6, s[4:5] offset:0\n"
    "s_mov_b32 s37, 0xffff0000\n"
    "v_and_b32 v19, 15, v251\n"
    "v_mul_u32_u24 v19, 272, v19\n"
    "v_add_u32 v97, v19, v250\n"
    "v_add_u32 v98, s87, v248\n"
    "v_add_u32 v99, s86, v250\n"
    "v_add_u32 v100, s86, v97\n"
    "s_mov_b32 s84, 0xbfb8aa3b\n"
    "global_load_dwordx4 v[156:159], v6, s[4:5] offset:128\n"
    "s_mov_b64 exec, 0\n"
    "s_bitcmp1_b32 s3, 1\n"
    "s_cbranch_scc1 .Ldin_t1_hi\n"
    "s_bitcmp1_b32 s3, 0\n"
    "s_cbranch_scc0 .Ldin_stub_4\n"
    "s_branch .Ldin_stub_5\n"
    ".Ldin_t1_hi:\n"
    "s_bitcmp1_b32 s3, 0\n"
    "s_cbranch_scc0 .Ldin_stub_6\n"
    "s_branch .Ldin_stub_7\n"
    ".Ldin_old2:\n"
    "v_and_b32 v2, 48, v251\n"
    "v_and_b32 v3, 1, v251\n"
    "v_lshl_or_b32 v2, v3, 6, v2\n"
    "s_lshl_b32 s39, s61, 7\n"
    "s_add_u32 s40, s40, 0xfffffc00\n"
    "s_addc_u32 s41, s41, -1\n"
    "s_mul_i32 s94, s61, s12\n"
    "s_bitset1_b32 s41, 23\n"
    "v_lshl_add_u32 v216, v251, 2, s94\n"
    "s_mov_b64 s[56:57], s[12:13]\n"
    "v_add_u32 v217, 1, v216\n"
    "v_add_u32 v218, 2, v216\n"
    "v_add_u32 v219, 3, v216\n"
    "s_mov_b64 s[72:73], s[18:19]\n"
    "s_cmp_lg_u32 s72, 0\n"
    "s_cselect_b32 s94, 6, 0\n"
    "s_cselect_b32 s95, 4, 0\n"
    "s_add_u32 s74, s72, s94\n"
    "s_sub_u32 s75, s72, s95\n"
    "v_mov_b32 v20, s13\n"
    "s_mov_b64 exec, 0xffffff\n"
    "v_mov_b32 v20, -1\n"
    "s_mov_b64 exec, 0xff\n"
    "v_mov_b32 v20, 0\n"
    "s_mov_b32 exec_lo, -1\n"
    "v_lshl_add_u32 v21, v249, 1, s66\n"
    "v_add_u32 v21, -32, v21\n"
    "ds_write_b32 v21, v20\n"
    "s_mov_b32 exec_hi, -1\n"
    "v_and_b32 v4, 2, v251\n"
    "v_lshl_add_u32 v4, v4, 6, v250\n"
    "v_and_b32 v5, 16, v248\n"
    "v_and_b32 v6, 12, v251\n"
    "v_or3_b32 v104, v4, v5, v6\n"
    "s_mul_i32 s84, s3, s15\n"
    "v_mul_u32_u24 v4, 34, v244\n"
    "s_lshl3_add_u32 s84, s2, s84\n"
    "v_and_b32 v5, 3, v251\n"
    "s_lshl_b32 s84, s84, 7\n"
    "v_lshl_add_u32 v4, v5, 5, v4\n"
    "v_and_b32 v5, 4, v251\n"
    "v_lshl_add_u32 v4, v5, 2, v4\n"
    "s_lshl_b32 s85, s15, 9\n"
    "s_lshl_b32 s86, s62, 3\n"
    "s_add_u32 s85, s85, s84\n"
    "v_add_u32 v18, s86, v4\n"
    "s_lshl_b32 s94, s61, 6\n"
    "s_add_u32 s94, s3, s94\n"
    "s_cmp_lt_u32 s61, s14\n"
    "s_cselect_b32 s58, s94, s13\n"
    "s_add_u32 s95, s61, s15\n"
    "s_add_u32 s36, s36, s17\n"
    "s_lshl_b32 s94, s95, 6\n"
    "s_add_u32 s94, s3, s94\n"
    "s_add_u32 s96, s36, s16\n"
    "s_add_u32 s94, s94, 8\n"
    "s_cmp_lt_u32 s95, s14\n"
    "s_cselect_b32 s46, s96, 0\n"
    "s_cselect_b32 s59, s94, s13\n"
    "s_setprio 2\n"
    "buffer_load_dwordx4 v[160:163], v2, s[48:51], s39 offen nt\n"
    "buffer_load_dwordx4 v[128:131], v248, s[48:51], s84 offen nt\n"
    "buffer_load_dwordx4 v[132:135], v248, s[48:51], s85 offen nt\n"
    "buffer_load_short_d16_hi v224, v249, s[44:47], s36 offen offset:0 nt\n"
    "buffer_load_short_d16_hi v225, v249, s[44:47], s36 offen offset:128 nt\n"
    "buffer_load_short_d16_hi v226, v249, s[44:47], s36 offen offset:256 nt\n"
    "buffer_load_short_d16_hi v227, v249, s[44:47], s36 offen offset:384 nt\n"
    "s_mul_i32 s46, s14, s16\n"
    "s_mov_b64 exec, 0\n"
    "s_bitcmp1_b32 s3, 1\n"
    "s_cbranch_scc1 .Ldin_t0_hi\n"
    "s_bitcmp1_b32 s3, 0\n"
    "s_cbranch_scc0 .Ldin_stub_0\n"
    "s_branch .Ldin_stub_1\n"
    ".Ldin_t0_hi:\n"
    "s_bitcmp1_b32 s3, 0\n"
    "s_cbranch_scc0 .Ldin_stub_2\n"
    "s_branch .Ldin_stub_3\n"
    ".Ldin_t0_ret:\n"
    "s_mov_b64 exec, -1\n"
    "s_setprio 2\n"
    "s_waitcnt vmcnt(9)\n"
    "v_cmp_ne_u16_e64 s[84:85], 0, v228\n"
    "v_cmp_lt_u32_e64 s[86:87], s38, v228\n"
    "v_cmp_ne_u16_e64 s[88:89], 0, v229\n"
    "v_cmp_lt_u32_e64 s[90:91], s38, v229\n"
    "v_mbcnt_lo_u32_b32 v220, s84, v220\n"
    "s_bcnt1_i32_b64 s92, s[84:85]\n"
    "v_mbcnt_hi_u32_b32 v220, s85, v220\n"
    "s_lshl2_add_u32 s92, s92, s67\n"
    "v_mbcnt_lo_u32_b32 v220, s86, v220\n"
    "s_bcnt1_i32_b64 s93, s[86:87]\n"
    "v_mbcnt_hi_u32_b32 v220, s87, v220\n"
    "s_lshl2_add_u32 s93, s93, s92\n"
    "v_mbcnt_lo_u32_b32 v220, s88, v220\n"
    "s_bcnt1_i32_b64 s94, s[88:89]\n"
    "v_mbcnt_hi_u32_b32 v220, s89, v220\n"
    "s_lshl2_add_u32 s94, s94, s93\n"
    "v_mbcnt_lo_u32_b32 v220, s90, v220\n"
    "s_bcnt1_i32_b64 s95, s[90:91]\n"
    "v_mbcnt_hi_u32_b32 v220, s91, v220\n"
    "s_lshl2_add_u32 s95, s95, s94\n"
    "v_mov_b32 v21, -1\n"
    "v_lshl_add_u32 v20, v249, 1, s95\n"
    "v_addc_co_u32_e64 v221, vcc, v220, 0, s[84:85]\n"
    "v_addc_co_u32_e64 v222, vcc, v221, 0, s[86:87]\n"
    "v_addc_co_u32_e64 v223, vcc, v222, 0, s[88:89]\n"
    "ds_write2_b32 v20, v21, v21 offset1:64\n"
    "v_lshlrev_b32 v220, 2, v220\n"
    "v_lshlrev_b32 v221, 2, v221\n"
    "v_lshlrev_b32 v222, 2, v222\n"
    "v_lshlrev_b32_e64 v223, 2, v223\n"
    "s_mov_b64 exec, s[84:85]\n"
    "ds_write_b32 v220, v216\n"
    "s_mov_b64 exec, s[86:87]\n"
    "ds_write_b32 v221, v217\n"
    "s_mov_b64 exec, s[88:89]\n"
    "ds_write_b32 v222, v218\n"
    "s_mov_b64 exec, s[90:91]\n"
    "ds_write_b32 v223, v219\n"
    "s_mov_b64 exec, -1\n"
    "ds_read_b32 v200, v253 offset:0\n"
    "ds_read_b32 v202, v253 offset:4\n"
    "ds_read_b32 v204, v253 offset:64\n"
    "ds_read_b32 v206, v253 offset:68\n"
    "ds_read_b32 v208, v253 offset:128\n"
    "ds_read_b32 v210, v253 offset:132\n"
    "ds_read_b32 v212, v253 offset:192\n"
    "ds_read_b32 v214, v253 offset:196\n"
    "s_add_u32 m0, s68, 0\n"
    "s_waitcnt lgkmcnt(6)\n"
    "buffer_load_dwordx4 v[200:201], s[40:43], 0 idxen offen offset:0 nt lds\n"
    "buffer_load_dwordx4 v[202:203], s[40:43], 0 idxen offen offset:1024 nt lds\n"
    "s_add_u32 s95, s95, 60\n"
    "s_add_u32 s94, s67, 384\n"
    "s_andn2_b32 s95, s95, 63\n"
    "s_max_u32 s80, s95, s94\n"
    "s_add_u32 m0, s68, 2048\n"
    "s_waitcnt lgkmcnt(0)\n"
    "buffer_load_dwordx4 v[204:205], s[40:43], 0 idxen offen offset:0 nt lds\n"
    "s_lshl_b32 s10, s10, 16\n"
    "v_writelane_b32 v10, s10, 0\n"
    "s_waitcnt vmcnt(10)\n"
    "v_and_b32 v11, 0x7fffffff, v8\n"
    "v_and_b32 v12, 0x7fffffff, v10\n"
    "v_min_f32 v14, 0, v8\n"
    "v_add_u32 v12, 0xf6000000, v12\n"
    "v_add_f32 v14, v14, v10\n"
    "v_max_i32 v12, v11, v12\n"
    "v_rcp_f32 v11, v11\n"
    "v_add_f32_dpp v14, v14, v14 row_ror:8 row_mask:0xf bank_mask:0xf\n"
    "v_max_u32_dpp v12, v12, v12 row_ror:8 row_mask:0xf bank_mask:0xf\n"
    "s_mov_b32 s84, 0xbfb8aa3b\n"
    "v_add_f32_dpp v14, v14, v14 row_ror:4 row_mask:0xf bank_mask:0xf\n"
    "v_max_u32_dpp v12, v12, v12 row_ror:4 row_mask:0xf bank_mask:0xf\n"
    "v_bfrev_b32 v13, 1\n"
    "v_add_f32_dpp v14, v14, v14 row_ror:2 row_mask:0xf bank_mask:0xf\n"
    "v_max_u32_dpp v12, v12, v12 row_ror:2 row_mask:0xf bank_mask:0xf\n"
    "v_bitop3_b32 v8, v8, s84, v13 bitop3:0x6c\n"
    "v_add_f32_dpp v14, v14, v14 row_ror:1 row_mask:0xf bank_mask:0xf\n"
    "v_max_u32_dpp v12, v12, v12 row_ror:1 row_mask:0xf bank_mask:0xf\n"
    "s_nop 0\n"
    ".Ldin_stub_6:\n"
    "s_cbranch_execz .Ldin_t1_ret\n"
    "s_nop 0\n"
    "v_readlane_b32 s84, v12, 0\n"
    "v_readlane_b32 s85, v12, 16\n"
    "v_readlane_b32 s86, v12, 32\n"
    "v_readlane_b32 s87, v12, 48\n"
    "s_max_u32 s84, s84, s85\n"
    "s_max_u32 s86, s86, s87\n"
    "s_max_u32 s84, s84, s86\n"
    "s_lshr_b32 s84, s84, 23\n"
    "buffer_load_dwordx4 v[206:207], s[40:43], 0 idxen offen offset:1024 nt lds\n"
    "s_max_u32 s84, s84, 15\n"
    "s_min_u32 s84, s84, 253\n"
    "s_lshl_b32 s84, s84, 23\n"
    "s_add_u32 s85, s84, 0xf9000000\n"
    "s_sub_u32 s86, 0x86000000, s84\n"
    "v_mul_f32 v11, s85, v11\n"
    "v_min_f32 v11, 0x46800000, v11\n"
    "v_log_f32 v13, v11\n"
    "v_mul_f32 v14, s86, v14\n"
    "v_mov_b32 v15, s85\n"
    "v_add_f32 v13, 0xc1700000, v13\n"
    "v_fmac_f32 v13, v9, v8\n"
    "ds_write_b32 v104, v13 offset:35200\n"
    "ds_write_b32 v104, v11 offset:34816\n"
    "v_mov_b32_e64 v247, v14\n"
    "s_mov_b32 s69, s85\n"
    "ds_write_b64 v250, v[14:15] offset:35072\n"
    "s_mov_b32 s94, 0x10001\n"
    "s_mov_b32 s95, 0xc080c080\n"
    "v_pk_min_u16 v22, v228, s94\n"
    "v_pk_min_u16 v23, v229, s94\n"
    "v_pk_mad_u16 v228, v22, s95, v228\n"
    "v_pk_mad_u16 v229, v23, s95, v229\n"
    "s_sub_u32 s87, s80, s81\n"
    "v_or_b32 v228, v228, v229\n"
    "s_lshr_b32 s87, s87, 6\n"
    "s_lshl2_add_u32 s86, s3, 68224\n"
    "s_waitcnt vmcnt(8)\n"
    "ds_write_b128 v18, v[128:131] offset:32768\n"
    "ds_write_b128 v18, v[132:135] offset:41472\n"
    "v_and_b32_dpp v184, v160, v254 quad_perm:[0,0,0,0] row_mask:0xf bank_mask:0xf\n"
    "v_and_b32_dpp v185, v161, v255 quad_perm:[0,0,0,0] row_mask:0xf bank_mask:0xf\n"
    "v_and_b32_dpp v186, v162, v254 quad_perm:[0,0,0,0] row_mask:0xf bank_mask:0xf\n"
    "v_and_b32_dpp v187, v163, v255 quad_perm:[0,0,0,0] row_mask:0xf bank_mask:0xf\n"
    "v_and_b32_dpp v188, v160, v254 quad_perm:[1,1,1,1] row_mask:0xf bank_mask:0xf\n"
    "v_and_b32_dpp v189, v161, v255 quad_perm:[1,1,1,1] row_mask:0xf bank_mask:0xf\n"
    "v_and_b32_dpp v190, v162, v254 quad_perm:[1,1,1,1] row_mask:0xf bank_mask:0xf\n"
    "v_and_b32_dpp v191, v163, v255 quad_perm:[1,1,1,1] row_mask:0xf bank_mask:0xf\n"
    "v_mov_b32 v106, s86\n"
    "v_mov_b32 v108, s66\n"
    "v_mov_b32 v109, s58\n"
    "v_mov_b32_e64 v110, s59\n"
    "s_waitcnt vmcnt(4)\n"
    "v_cmp_neq_f32_e64 s[20:21], 0, v224\n"
    "v_cmp_neq_f32_e64 s[22:23], 0, v225\n"
    "v_cmp_neq_f32_e64 s[24:25], 0, v226\n"
    "v_cmp_neq_f32_e64 s[26:27], 0, v227\n"
    "v_fma_f32 v224, v224, v224, -v224\n"
    "v_fma_f32 v225, v225, v225, -v225\n"
    "v_fma_f32 v226, v226, v226, -v226\n"
    "v_fma_f32 v227, v227, v227, -v227\n"
    "s_bcnt1_i32_b64 s88, s[20:21]\n"
    "s_bcnt1_i32_b64 s89, s[22:23]\n"
    "s_bcnt1_i32_b64 s90, s[24:25]\n"
    "s_bcnt1_i32_b64 s91, s[26:27]\n"
    "v_or3_b32 v228, v228, v224, v225\n"
    "s_add_u32 s88, s88, s89\n"
    "v_or3_b32 v228, v228, v226, v227\n"
    "s_add_u32 s90, s90, s91\n"
    "v_cmp_ne_u32_e64 s[84:85], 0, v228\n"
    "s_add_u32 s88, s88, s90\n"
    "s_pack_ll_b32_b16 s88, s87, s88\n"
    "s_mov_b64 exec, 1\n"
    "v_mov_b32_e64 v107, s88\n"
    "ds_write2_b32 v108, v109, v110 offset1:2\n"
    "s_cmp_lg_u64 s[84:85], 0\n"
    "ds_write_b32 v106, v107\n"
    "s_cselect_b32 s91, 2.0, 0\n"
    "s_mov_b64 exec, -1\n"
    "s_or_b32 s58, s58, s91\n"
    "s_or_b32 s59, s59, s91\n"
    "s_mov_b64 vcc, 0\n"
    "s_waitcnt lgkmcnt(0)\n"
    "s_barrier\n"
    "s_setprio 0\n"
    ".Ldin_entry:\n"
    "ds_read_b128 v[32:35], v248 offset:16384\n"
    "ds_read_b128 v[64:67], v248 offset:0\n"
    "ds_read_b128 v[68:71], v248 offset:1024\n"
    "ds_read_b128 v[36:39], v248 offset:17408\n"
    "ds_read_b128 v[72:75], v248 offset:2048\n"
    "ds_read_b128 v[76:79], v248 offset:3072\n"
    "ds_read_b128 v[40:43], v248 offset:18432\n"
    "ds_read_b128 v[80:83], v248 offset:4096\n"
    "ds_read_b128 v[84:87], v248 offset:5120\n"
    "ds_read_b128 v[44:47], v248 offset:19456\n"
    "ds_read_b128 v[88:91], v248 offset:6144\n"
    "ds_read_b128 v[92:95], v248 offset:7168\n"
    "s_add_u32 s67, s67, 320\n"
    "s_waitcnt lgkmcnt(9)\n"
    "ds_read_b128 v[48:51], v248 offset:20480\n"
    "ds_read_b128 v[192:195], v248 offset:8192\n"
    "ds_read_b128 v[196:199], v248 offset:9216\n"
    "v_mfma_f32_4x4x4_16b_bf16 v[144:147], v[184:185], v[32:33], v[64:67]\n"
    "s_nop 0\n"
    "v_mfma_f32_4x4x4_16b_bf16 v[148:151], v[186:187], v[34:35], v[68:71]\n"
    "s_add_u32 m0, s68, 4096\n"
    "s_cbranch_vccnz .Ldin_e1\n"
    "buffer_load_dwordx4 v[208:209], s[40:43], 0 idxen offen offset:0 nt lds\n"
    "buffer_load_dwordx4 v[210:211], s[40:43], 0 idxen offen offset:1024 nt lds\n"
    ".Ldin_e1:\n"
    "s_waitcnt lgkmcnt(9)\n"
    "ds_read_b128 v[52:55], v248 offset:21504\n"
    "ds_read_b128 v[200:203], v248 offset:10240\n"
    "ds_read_b128 v[204:207], v248 offset:11264\n"
    "v_mfma_f32_4x4x4_16b_bf16 v[152:155], v[188:189], v[36:37], v[72:75]\n"
    "s_nop 1\n"
    "v_cvt_pk_bf16_f32 v0, v144, v145\n"
    "v_cvt_pk_bf16_f32 v1, v146, v147\n"
    "v_mfma_f32_4x4x4_16b_bf16 v[156:159], v[190:191], v[38:39], v[76:79]\n"
    "s_add_u32 m0, s68, 6144\n"
    "s_cbranch_vccnz .Ldin_e2\n"
    "buffer_load_dwordx4 v[212:213], s[40:43], 0 idxen offen offset:0 nt lds\n"
    "buffer_load_dwordx4 v[214:215], s[40:43], 0 idxen offen offset:1024 nt lds\n"
    ".Ldin_e2:\n"
    "s_waitcnt lgkmcnt(9)\n"
    "s_nop 0\n"
    "ds_read_b128 v[56:59], v248 offset:22528\n"
    "ds_read_b128 v[208:211], v248 offset:12288\n"
    "ds_read_b128 v[212:215], v248 offset:13312\n"
    "v_cvt_pk_bf16_f32 v2, v148, v149\n"
    "v_cvt_pk_bf16_f32 v3, v150, v151\n"
    "v_mfma_f32_4x4x4_16b_bf16 v[144:147], v[184:185], v[40:41], v[80:83]\n"
    "v_cvt_pk_bf16_f32 v4, v152, v153\n"
    "v_cvt_pk_bf16_f32 v5, v154, v155\n"
    "v_mfma_f32_4x4x4_16b_bf16 v[148:151], v[186:187], v[42:43], v[84:87]\n"
    "s_cbranch_vccnz .Ldin_e3\n"
    "s_barrier\n"
    ".Ldin_e3:\n"
    "s_waitcnt lgkmcnt(9)\n"
    "s_nop 0\n"
    "ds_read_b128 v[60:63], v248 offset:23552\n"
    "ds_read_b128 v[216:219], v248 offset:14336\n"
    "ds_read_b128 v[220:223], v248 offset:15360\n"
    "v_cvt_pk_bf16_f32 v6, v156, v157\n"
    "v_cvt_pk_bf16_f32 v7, v158, v159\n"
    "v_mfma_f32_4x4x4_16b_bf16 v[152:155], v[188:189], v[44:45], v[88:91]\n"
    "v_cvt_pk_bf16_f32 v8, v144, v145\n"
    "v_cvt_pk_bf16_f32 v9, v146, v147\n"
    "v_mfma_f32_4x4x4_16b_bf16 v[156:159], v[190:191], v[46:47], v[92:95]\n"
    "s_waitcnt lgkmcnt(9)\n"
    "v_cvt_pk_bf16_f32 v10, v148, v149\n"
    "v_cvt_pk_bf16_f32 v11, v150, v151\n"
    "v_mfma_f32_4x4x4_16b_bf16 v[144:147], v[184:185], v[48:49], v[192:195]\n"
    "v_cvt_pk_bf16_f32 v12, v152, v153\n"
    "v_cvt_pk_bf16_f32 v13, v154, v155\n"
    "v_mfma_f32_4x4x4_16b_bf16 v[148:151], v[186:187], v[50:51], v[196:199]\n"
    "s_waitcnt lgkmcnt(6)\n"
    ".Ldin_stub_7:\n"
    "s_cbranch_execz .Ldin_t1_ret\n"
    "s_nop 0\n"
    "v_cvt_pk_bf16_f32 v14, v156, v157\n"
    "v_cvt_pk_bf16_f32 v15, v158, v159\n"
    "v_mfma_f32_4x4x4_16b_bf16 v[152:155], v[188:189], v[52:53], v[200:203]\n"
    "v_cvt_pk_bf16_f32 v16, v144, v145\n"
    "v_cvt_pk_bf16_f32 v17, v146, v147\n"
    "v_mfma_f32_4x4x4_16b_bf16 v[156:159], v[190:191], v[54:55], v[204:207]\n"
    "s_waitcnt lgkmcnt(3)\n"
    "s_waitcnt vmcnt(6)\n"
    "ds_read_b128 v[160:163], v234 offset:0\n"
    "ds_read_b128 v[164:167], v235 offset:0\n"
    "ds_read_b32 v236, v253 offset:256\n"
    "ds_read_b32 v238, v253 offset:260\n"
    "ds_read_b64_tr_b16 v[168:169], v240 offset:0\n"
    "ds_read_b64_tr_b16 v[170:171], v241 offset:256\n"
    "ds_read_b64_tr_b16 v[172:173], v242 offset:512\n"
    "ds_read_b64_tr_b16 v[174:175], v243 offset:768\n"
    "v_cvt_pk_bf16_f32 v18, v148, v149\n"
    "v_cvt_pk_bf16_f32 v19, v150, v151\n"
    "v_mfma_f32_4x4x4_16b_bf16 v[144:147], v[184:185], v[56:57], v[208:211]\n"
    "v_cvt_pk_bf16_f32 v20, v152, v153\n"
    "v_cvt_pk_bf16_f32 v21, v154, v155\n"
    "v_mfma_f32_4x4x4_16b_bf16 v[148:151], v[186:187], v[58:59], v[212:215]\n"
    "s_waitcnt lgkmcnt(8)\n"
    "v_cvt_pk_bf16_f32 v22, v156, v157\n"
    "v_cvt_pk_bf16_f32 v23, v158, v159\n"
    "v_mfma_f32_4x4x4_16b_bf16 v[152:155], v[188:189], v[60:61], v[216:219]\n"
    "v_cvt_pk_bf16_f32 v24, v144, v145\n"
    "v_cvt_pk_bf16_f32 v25, v146, v147\n"
    "v_mfma_f32_4x4x4_16b_bf16 v[156:159], v[190:191], v[62:63], v[220:223]\n"
    "s_cbranch_vccnz .Ldin_e8\n"
    "s_barrier\n"
    ".Ldin_e8:\n"
    "s_nop 0\n"
    "ds_read_b128 v[112:115], v252 offset:17408\n"
    "ds_read_b128 v[116:119], v252 offset:17424\n"
    "ds_read_b128 v[120:123], v252 offset:17536\n"
    "ds_read_b128 v[124:127], v252 offset:17552\n"
    "v_cvt_pk_bf16_f32 v26, v148, v149\n"
    "v_cvt_pk_bf16_f32 v27, v150, v151\n"
    "v_cvt_pk_bf16_f32 v28, v152, v153\n"
    "v_cvt_pk_bf16_f32 v29, v154, v155\n"
    "s_nop 0\n"
    "v_cvt_pk_bf16_f32 v30, v156, v157\n"
    "v_cvt_pk_bf16_f32 v31, v158, v159\n"
    "s_add_u32 m0, s68, 0\n"
    "s_waitcnt lgkmcnt(0)\n"
    "v_mfma_f32_16x16x32_bf16 v[128:131], v[0:3], v[160:163], v[112:115]\n"
    "ds_read_b128 v[96:99], v250 offset:34816\n"
    "v_mfma_f32_16x16x32_bf16 v[128:131], v[4:7], v[164:167], v[128:131]\n"
    "ds_read_b128 v[100:103], v250 offset:34832\n"
    "v_mfma_f32_16x16x32_bf16 v[132:135], v[8:11], v[160:163], v[116:119]\n"
    "ds_read_b128 v[104:107], v250 offset:34944\n"
    "v_mfma_f32_16x16x32_bf16 v[132:135], v[12:15], v[164:167], v[132:135]\n"
    "ds_read_b128 v[108:111], v250 offset:34960\n"
    "v_mfma_f32_16x16x32_bf16 v[136:139], v[16:19], v[160:163], v[120:123]\n"
    "v_mfma_f32_16x16x32_bf16 v[136:139], v[20:23], v[164:167], v[136:139]\n"
    "v_mfma_f32_16x16x32_bf16 v[140:143], v[24:27], v[160:163], v[124:127]\n"
    "v_mfma_f32_16x16x32_bf16 v[140:143], v[28:31], v[164:167], v[140:143]\n"
    "s_waitcnt vmcnt(4)\n"
    "ds_read_b128 v[160:163], v234 offset:2048\n"
    "ds_read_b128 v[164:167], v235 offset:2048\n"
    "buffer_load_dwordx4 v[236:237], s[40:43], 0 idxen offen offset:0 nt lds\n"
    "buffer_load_dwordx4 v[238:239], s[40:43], 0 idxen offen offset:1024 nt lds\n"
    "s_memrealtime s[34:35]\n"
    "s_cmp_lg_u64 vcc, 0\n"
    "s_cselect_b32 s94, 58, 52\n"
    "s_cselect_b32 s95, 35, 20\n"
    "s_cmp_lg_u32 s72, 0\n"
    "s_cselect_b32 s72, s94, 0\n"
    "s_cselect_b32 s95, s95, 0\n"
    "s_waitcnt lgkmcnt(0)\n"
    "s_lshl1_add_u32 s76, s95, s34\n"
    "s_cmp_lt_i32 s59, s57\n"
    "s_cselect_b32 s76, s76, -1\n"
    ".Ldin_stub_1:\n"
    "s_cbranch_execz .Ldin_t0_ret\n"
    "s_nop 0\n"
    "s_bitcmp1_b32 s58, 30\n"
    "s_cbranch_scc1 .Ldin_odd\n"
    "s_nop 0\n"
    ".Ldin_A0:\n"
    "v_mfma_f32_16x16x32_bf16 v[144:147], v[0:3], v[160:163], v[112:115]\n"
    "v_exp_f32 v128, v128 clamp\n"
    "v_mfma_f32_16x16x32_bf16 v[144:147], v[4:7], v[164:167], v[144:147]\n"
    "v_exp_f32 v129, v129 clamp\n"
    "v_mfma_f32_16x16x32_bf16 v[148:151], v[8:11], v[160:163], v[116:119]\n"
    "v_exp_f32 v130, v130 clamp\n"
    "v_mfma_f32_16x16x32_bf16 v[148:151], v[12:15], v[164:167], v[148:151]\n"
    "v_exp_f32 v131, v131 clamp\n"
    "v_mfma_f32_16x16x32_bf16 v[152:155], v[16:19], v[160:163], v[120:123]\n"
    "v_exp_f32 v132, v132 clamp\n"
    "v_mfma_f32_16x16x32_bf16 v[152:155], v[20:23], v[164:167], v[152:155]\n"
    "v_exp_f32 v133, v133 clamp\n"
    "v_mfma_f32_16x16x32_bf16 v[156:159], v[24:27], v[160:163], v[124:127]\n"
    "v_exp_f32 v134, v134 clamp\n"
    "v_mfma_f32_16x16x32_bf16 v[156:159], v[28:31], v[164:167], v[156:159]\n"
    "v_exp_f32 v135, v135 clamp\n"
    "v_exp_f32 v136, v136 clamp\n"
    "s_add_u32 m0, s68, 2048\n"
    "v_exp_f32 v137, v137 clamp\n"
    "v_add_u32 v253, s67, v244\n"
    "s_waitcnt lgkmcnt(0)\n"
    "v_mfma_f32_4x4x4_16b_bf16 v[224:227], v[228:229], v[176:177], v[224:227] cbsz:4 abid:0\n"
    "v_exp_f32 v138, v138 clamp\n"
    "v_exp_f32 v139, v139 clamp\n"
    "v_mfma_f32_4x4x4_16b_bf16 v[224:227], v[228:229], v[178:179], v[224:227] cbsz:4 abid:1\n"
    "v_exp_f32 v140, v140 clamp\n"
    "v_exp_f32 v141, v141 clamp\n"
    "v_mfma_f32_4x4x4_16b_bf16 v[224:227], v[228:229], v[180:181], v[224:227] cbsz:4 abid:2\n"
    "v_exp_f32 v142, v142 clamp\n"
    "v_exp_f32 v143, v143 clamp\n"
    "v_mfma_f32_4x4x4_16b_bf16 v[224:227], v[228:229], v[182:183], v[224:227] cbsz:4 abid:3\n"
    "s_waitcnt vmcnt(4)\n"
    "ds_read_b64_tr_b16 v[176:177], v240 offset:2048\n"
    "ds_read_b64_tr_b16 v[178:179], v241 offset:2304\n"
    "ds_read_b64_tr_b16 v[180:181], v242 offset:2560\n"
    "ds_read_b64_tr_b16 v[182:183], v243 offset:2816\n"
    "ds_read_b32 v236, v253 offset:0\n"
    "ds_read_b32 v238, v253 offset:4\n"
    "ds_read_b128 v[160:163], v234 offset:4096\n"
    "ds_read_b128 v[164:167], v235 offset:4096\n"
    "s_cbranch_vccz .Ldin_A0_np\n"
    "s_setprio 1\n"
    ".Ldin_A0_np:\n"
    "v_pk_fma_f32 v[128:129], v[128:129], s[70:71], v[96:97]\n"
    "v_pk_fma_f32 v[130:131], v[130:131], s[70:71], v[98:99]\n"
    "v_add_f32 v191, v224, v225\n"
    "v_add_f32 v190, v226, v227\n"
    "v_add_f32 v191, v191, v190\n"
    "v_add_u32 v190, s62, v249\n"
    "v_mul_f32 v191, s69, v191\n"
    "v_cvt_pk_bf16_f32 v191, v191, v191\n"
    "ds_write_b16 v190, v191 offset:32768\n"
    "s_bfe_u32 s94, s59, 0x180006\n"
    "s_mul_i32 s86, s94, s56\n"
    "v_mbcnt_lo_u32_b32 v184, s20, 0\n"
    "v_mbcnt_lo_u32_b32 v185, s22, 0\n"
    "v_mbcnt_lo_u32_b32 v186, s24, 0\n"
    "v_mbcnt_lo_u32_b32 v187, s26, 0\n"
    "s_bcnt1_i32_b64 s90, s[20:21]\n"
    "s_bcnt1_i32_b64 s91, s[22:23]\n"
    "s_bcnt1_i32_b64 s92, s[24:25]\n"
    "s_bcnt1_i32_b64 s93, s[26:27]\n"
    "v_mbcnt_hi_u32_b32 v184, s21, v184\n"
    "v_mbcnt_hi_u32_b32 v185, s23, v185\n"
    "v_mbcnt_hi_u32_b32 v186, s25, v186\n"
    "v_mbcnt_hi_u32_b32 v187, s27, v187\n"
    "s_lshl2_add_u32 s90, s90, s65\n"
    "s_lshl2_add_u32 s91, s91, s90\n"
    "s_lshl2_add_u32 s92, s92, s91\n"
    "s_lshl2_add_u32 s93, s93, s92\n"
    "v_add_u32_e64 v188, s86, v251\n"
    "v_pk_add_f32 v[230:231], v[128:129], v[130:131]\n"
    "v_pk_mul_f32 v[232:233], v[128:129], v[130:131]\n"
    "v_pk_fma_f32 v[132:133], v[132:133], s[70:71], v[100:101]\n"
    "v_pk_fma_f32 v[230:231], v[230:231], v[132:133], v[232:233]\n"
    "v_pk_mul_f32 v[232:233], v[232:233], v[132:133]\n"
    "v_lshl_add_u32 v184, v184, 2, s65\n"
    "v_lshl_add_u32 v185, v185, 2, s90\n"
    "v_lshl_add_u32 v186, v186, 2, s91\n"
    "v_lshl_add_u32 v187, v187, 2, s92\n"
    "v_mov_b32 v191, -1\n"
    "v_lshl_add_u32 v190, v249, 1, s93\n"
    "ds_write2_b32 v190, v191, v191 offset1:64\n"
    "s_mov_b64 exec, s[20:21]\n"
    "ds_write_b32 v184, v188\n"
    "s_mov_b64 exec, s[22:23]\n"
    "v_add_u32 v189, 64, v188\n"
    "ds_write_b32 v185, v189\n"
    "s_mov_b64 exec, s[24:25]\n"
    "v_add_u32 v190, 128, v188\n"
    "ds_write_b32 v186, v190\n"
    "s_mov_b64 exec, s[26:27]\n"
    "v_add_u32 v189, 192, v188\n"
    "ds_write_b32 v187, v189\n"
    "s_mov_b64 exec, -1\n"
    "s_add_u32 s95, s93, 60\n"
    "s_add_u32 s94, s82, 128\n"
    "s_andn2_b32 s95, s95, 63\n"
    "s_max_u32 s83, s95, s94\n"
    "v_pk_fma_f32 v[134:135], v[134:135], s[70:71], v[102:103]\n"
    "v_pk_fma_f32 v[230:231], v[230:231], v[134:135], v[232:233]\n"
    "v_pk_mul_f32 v[232:233], v[232:233], v[134:135]\n"
    "v_pk_fma_f32 v[136:137], v[136:137], s[70:71], v[104:105]\n"
    "v_pk_fma_f32 v[230:231], v[230:231], v[136:137], v[232:233]\n"
    "v_pk_mul_f32 v[232:233], v[232:233], v[136:137]\n"
    "v_pk_fma_f32 v[138:139], v[138:139], s[70:71], v[106:107]\n"
    "v_pk_fma_f32 v[230:231], v[230:231], v[138:139], v[232:233]\n"
    "v_pk_mul_f32 v[232:233], v[232:233], v[138:139]\n"
    "v_pk_fma_f32 v[140:141], v[140:141], s[70:71], v[108:109]\n"
    "v_pk_fma_f32 v[230:231], v[230:231], v[140:141], v[232:233]\n"
    "v_pk_mul_f32 v[232:233], v[232:233], v[140:141]\n"
    "v_pk_fma_f32 v[142:143], v[142:143], s[70:71], v[110:111]\n"
    "v_pk_fma_f32 v[230:231], v[230:231], v[142:143], v[232:233]\n"
    "v_pk_mul_f32 v[232:233], v[232:233], v[142:143]\n"
    "v_rcp_f32 v232, v232\n"
    "v_rcp_f32 v233, v233\n"
    "v_fma_f32 v230, v230, v232, v247\n"
    "v_fmac_f32 v230, v231, v233\n"
    "v_cvt_pk_bf16_f32 v230, v230, v230\n"
    "s_add_u32 s67, s67, 64\n"
    "s_waitcnt lgkmcnt(0)\n"
    "ds_write_b16 v245, v230\n"
    "ds_read_b64 v[228:229], v246\n"
    "buffer_load_dwordx4 v[236:237], s[40:43], 0 idxen offen offset:0 nt lds\n"
    "buffer_load_dwordx4 v[238:239], s[40:43], 0 idxen offen offset:1024 nt lds\n"
    ".Ldin_B1:\n"
    "v_mfma_f32_16x16x32_bf16 v[128:131], v[0:3], v[160:163], v[112:115]\n"
    "v_exp_f32 v144, v144 clamp\n"
    "v_mfma_f32_16x16x32_bf16 v[128:131], v[4:7], v[164:167], v[128:131]\n"
    "v_exp_f32 v145, v145 clamp\n"
    "v_mfma_f32_16x16x32_bf16 v[132:135], v[8:11], v[160:163], v[116:119]\n"
    "v_exp_f32 v146, v146 clamp\n"
    "v_mfma_f32_16x16x32_bf16 v[132:135], v[12:15], v[164:167], v[132:135]\n"
    "v_exp_f32 v147, v147 clamp\n"
    "v_mfma_f32_16x16x32_bf16 v[136:139], v[16:19], v[160:163], v[120:123]\n"
    "v_exp_f32 v148, v148 clamp\n"
    "v_mfma_f32_16x16x32_bf16 v[136:139], v[20:23], v[164:167], v[136:139]\n"
    "v_exp_f32 v149, v149 clamp\n"
    "v_mfma_f32_16x16x32_bf16 v[140:143], v[24:27], v[160:163], v[124:127]\n"
    "v_exp_f32 v150, v150 clamp\n"
    "v_mfma_f32_16x16x32_bf16 v[140:143], v[28:31], v[164:167], v[140:143]\n"
    "v_exp_f32 v151, v151 clamp\n"
    "v_exp_f32 v152, v152 clamp\n"
    "s_cmp_eq_u32 s67, s80\n"
    "s_cselect_b32 s67, s65, s67\n"
    "s_and_b32 s94, s59, 63\n"
    ".Ldin_stub_3:\n"
    "s_cbranch_execz .Ldin_t0_ret\n"
    "s_nop 0\n"
    "s_mul_i32 s94, s94, 272\n"
    "v_add_u32 v252, s94, v250\n"
    "s_add_u32 m0, s68, 4096\n"
    "v_exp_f32 v153, v153 clamp\n"
    "v_add_u32 v253, s67, v244\n"
    "s_waitcnt lgkmcnt(0)\n"
    "v_mfma_f32_4x4x4_16b_bf16 v[224:227], v[228:229], v[168:169], 0 cbsz:4 abid:0\n"
    "v_exp_f32 v154, v154 clamp\n"
    "v_exp_f32 v155, v155 clamp\n"
    "v_mfma_f32_4x4x4_16b_bf16 v[224:227], v[228:229], v[170:171], v[224:227] cbsz:4 abid:1\n"
    "v_exp_f32 v156, v156 clamp\n"
    "v_exp_f32 v157, v157 clamp\n"
    "v_mfma_f32_4x4x4_16b_bf16 v[224:227], v[228:229], v[172:173], v[224:227] cbsz:4 abid:2\n"
    "v_exp_f32 v158, v158 clamp\n"
    "v_exp_f32 v159, v159 clamp\n"
    "v_mfma_f32_4x4x4_16b_bf16 v[224:227], v[228:229], v[174:175], v[224:227] cbsz:4 abid:3\n"
    "s_waitcnt vmcnt(4)\n"
    "s_memrealtime s[34:35]\n"
    "ds_read_b64_tr_b16 v[168:169], v240 offset:4096\n"
    "ds_read_b64_tr_b16 v[170:171], v241 offset:4352\n"
    "ds_read_b64_tr_b16 v[172:173], v242 offset:4608\n"
    "ds_read_b64_tr_b16 v[174:175], v243 offset:4864\n"
    "ds_read_b32 v236, v253 offset:0\n"
    "ds_read_b32 v238, v253 offset:4\n"
    "ds_read_b128 v[160:163], v234 offset:6144\n"
    "ds_read_b128 v[164:167], v235 offset:6144\n"
    "ds_read_b128 v[184:187], v252 offset:0\n"
    "ds_read_b128 v[188:191], v252 offset:16\n"
    "v_pk_fma_f32 v[144:145], v[144:145], s[70:71], v[96:97]\n"
    "v_pk_fma_f32 v[146:147], v[146:147], s[70:71], v[98:99]\n"
    "v_pk_add_f32 v[230:231], v[144:145], v[146:147]\n"
    "v_pk_mul_f32 v[232:233], v[144:145], v[146:147]\n"
    "v_pk_fma_f32 v[148:149], v[148:149], s[70:71], v[100:101]\n"
    "v_pk_fma_f32 v[230:231], v[230:231], v[148:149], v[232:233]\n"
    "v_pk_mul_f32 v[232:233], v[232:233], v[148:149]\n"
    "v_pk_fma_f32 v[150:151], v[150:151], s[70:71], v[102:103]\n"
    "v_pk_fma_f32 v[230:231], v[230:231], v[150:151], v[232:233]\n"
    "v_pk_mul_f32 v[232:233], v[232:233], v[150:151]\n"
    "v_pk_fma_f32 v[152:153], v[152:153], s[70:71], v[104:105]\n"
    "v_pk_fma_f32 v[230:231], v[230:231], v[152:153], v[232:233]\n"
    "v_pk_mul_f32 v[232:233], v[232:233], v[152:153]\n"
    "v_pk_fma_f32 v[154:155], v[154:155], s[70:71], v[106:107]\n"
    "v_pk_fma_f32 v[230:231], v[230:231], v[154:155], v[232:233]\n"
    "v_pk_mul_f32 v[232:233], v[232:233], v[154:155]\n"
    "v_pk_fma_f32 v[156:157], v[156:157], s[70:71], v[108:109]\n"
    "v_pk_fma_f32 v[230:231], v[230:231], v[156:157], v[232:233]\n"
    "v_pk_mul_f32 v[232:233], v[232:233], v[156:157]\n"
    "v_pk_fma_f32 v[158:159], v[158:159], s[70:71], v[110:111]\n"
    "v_pk_fma_f32 v[230:231], v[230:231], v[158:159], v[232:233]\n"
    "v_pk_mul_f32 v[232:233], v[232:233], v[158:159]\n"
    "v_rcp_f32 v232, v232\n"
    "v_rcp_f32 v233, v233\n"
    "v_fma_f32 v230, v230, v232, v247\n"
    "v_fmac_f32 v230, v231, v233\n"
    "v_cvt_pk_bf16_f32 v230, v230, v230\n"
    "s_add_u32 s67, s67, 64\n"
    "s_waitcnt lgkmcnt(0)\n"
    "ds_write_b16 v245, v230\n"
    "ds_read_b64 v[228:229], v246\n"
    "s_cmp_le_u32 s34, s76\n"
    "s_cbranch_scc1 .Ldin_B1_wait\n"
    ".Ldin_B1_go:\n"
    "s_sub_u32 s94, s34, 60\n"
    "s_max_u32 s76, s76, s94\n"
    "s_add_u32 s76, s76, s72\n"
    ".Ldin_B1_dma:\n"
    "buffer_load_dwordx4 v[236:237], s[40:43], 0 idxen offen offset:0 nt lds\n"
    "buffer_load_dwordx4 v[238:239], s[40:43], 0 idxen offen offset:1024 nt lds\n"
    ".Ldin_B1_nd:\n"
    ".Ldin_G2:\n"
    "v_mfma_f32_16x16x32_bf16 v[144:147], v[0:3], v[160:163], v[112:115]\n"
    "v_exp_f32 v128, v128 clamp\n"
    "v_mfma_f32_16x16x32_bf16 v[144:147], v[4:7], v[164:167], v[144:147]\n"
    "v_exp_f32 v129, v129 clamp\n"
    "v_mfma_f32_16x16x32_bf16 v[148:151], v[8:11], v[160:163], v[116:119]\n"
    "v_exp_f32 v130, v130 clamp\n"
    "v_mfma_f32_16x16x32_bf16 v[148:151], v[12:15], v[164:167], v[148:151]\n"
    "v_exp_f32 v131, v131 clamp\n"
    "v_mfma_f32_16x16x32_bf16 v[152:155], v[16:19], v[160:163], v[120:123]\n"
    "v_exp_f32 v132, v132 clamp\n"
    "v_mfma_f32_16x16x32_bf16 v[152:155], v[20:23], v[164:167], v[152:155]\n"
    "v_exp_f32 v133, v133 clamp\n"
    "v_mfma_f32_16x16x32_bf16 v[156:159], v[24:27], v[160:163], v[124:127]\n"
    "v_exp_f32 v134, v134 clamp\n"
    "v_mfma_f32_16x16x32_bf16 v[156:159], v[28:31], v[164:167], v[156:159]\n"
    "v_exp_f32 v135, v135 clamp\n"
    "v_exp_f32 v136, v136 clamp\n"
    "s_cmp_eq_u32 s67, s80\n"
    "s_cselect_b32 s67, s65, s67\n"
    "s_add_u32 m0, s68, 6144\n"
    "v_exp_f32 v137, v137 clamp\n"
    "v_add_u32_e64 v253, s67, v244\n"
    "s_waitcnt lgkmcnt(0)\n"
    "v_mfma_f32_4x4x4_16b_bf16 v[224:227], v[228:229], v[176:177], v[224:227] cbsz:4 abid:0\n"
    "v_exp_f32 v138, v138 clamp\n"
    "v_exp_f32 v139, v139 clamp\n"
    "v_mfma_f32_4x4x4_16b_bf16 v[224:227], v[228:229], v[178:179], v[224:227] cbsz:4 abid:1\n"
    "v_exp_f32 v140, v140 clamp\n"
    "v_exp_f32 v141, v141 clamp\n"
    "v_mfma_f32_4x4x4_16b_bf16 v[224:227], v[228:229], v[180:181], v[224:227] cbsz:4 abid:2\n"
    "v_exp_f32 v142, v142 clamp\n"
    "v_exp_f32 v143, v143 clamp\n"
    "v_mfma_f32_4x4x4_16b_bf16 v[224:227], v[228:229], v[182:183], v[224:227] cbsz:4 abid:3\n"
    "s_waitcnt vmcnt(4)\n"
    "s_memrealtime s[34:35]\n"
    "ds_read_b64_tr_b16 v[176:177], v240 offset:6144\n"
    "ds_read_b64_tr_b16 v[178:179], v241 offset:6400\n"
    "ds_read_b64_tr_b16 v[180:181], v242 offset:6656\n"
    "ds_read_b64_tr_b16 v[182:183], v243 offset:6912\n"
    "ds_read_b32 v236, v253 offset:0\n"
    "ds_read_b32 v238, v253 offset:4\n"
    "ds_read_b128 v[160:163], v234 offset:0\n"
    "ds_read_b128 v[164:167], v235 offset:0\n"
    "v_pk_fma_f32 v[128:129], v[128:129], s[70:71], v[96:97]\n"
    "v_pk_fma_f32 v[130:131], v[130:131], s[70:71], v[98:99]\n"
    "v_pk_add_f32 v[230:231], v[128:129], v[130:131]\n"
    "v_pk_mul_f32 v[232:233], v[128:129], v[130:131]\n"
    "v_pk_fma_f32 v[132:133], v[132:133], s[70:71], v[100:101]\n"
    "v_pk_fma_f32 v[230:231], v[230:231], v[132:133], v[232:233]\n"
    "v_pk_mul_f32 v[232:233], v[232:233], v[132:133]\n"
    "v_pk_fma_f32 v[134:135], v[134:135], s[70:71], v[102:103]\n"
    "v_pk_fma_f32 v[230:231], v[230:231], v[134:135], v[232:233]\n"
    "v_pk_mul_f32 v[232:233], v[232:233], v[134:135]\n"
    "v_pk_fma_f32 v[136:137], v[136:137], s[70:71], v[104:105]\n"
    "v_pk_fma_f32 v[230:231], v[230:231], v[136:137], v[232:233]\n"
    "v_pk_mul_f32 v[232:233], v[232:233], v[136:137]\n"
    "v_pk_fma_f32 v[138:139], v[138:139], s[70:71], v[106:107]\n"
    "v_pk_fma_f32 v[230:231], v[230:231], v[138:139], v[232:233]\n"
    "v_pk_mul_f32 v[232:233], v[232:233], v[138:139]\n"
    "v_pk_fma_f32 v[140:141], v[140:141], s[70:71], v[108:109]\n"
    "v_pk_fma_f32 v[230:231], v[230:231], v[140:141], v[232:233]\n"
    "v_pk_mul_f32 v[232:233], v[232:233], v[140:141]\n"
    "v_pk_fma_f32 v[142:143], v[142:143], s[70:71], v[110:111]\n"
    "v_pk_fma_f32 v[230:231], v[230:231], v[142:143], v[232:233]\n"
    "v_pk_mul_f32 v[232:233], v[232:233], v[142:143]\n"
    "v_rcp_f32 v232, v232\n"
    "v_rcp_f32 v233, v233\n"
    "v_fma_f32 v230, v230, v232, v247\n"
    "v_fmac_f32 v230, v231, v233\n"
    "v_cvt_pk_bf16_f32 v230, v230, v230\n"
    "s_add_u32 s67, s67, 64\n"
    "s_waitcnt lgkmcnt(0)\n"
    "ds_write_b16 v245, v230\n"
    "ds_read_b64 v[228:229], v246\n"
    "s_cmp_le_u32 s34, s76\n"
    "s_cbranch_scc1 .Ldin_G2_wait\n"
    ".Ldin_G2_go:\n"
    "s_sub_u32 s94, s34, 60\n"
    "s_max_u32 s76, s76, s94\n"
    "s_add_u32 s76, s76, s72\n"
    ".Ldin_G2_dma:\n"
    "buffer_load_dwordx4 v[236:237], s[40:43], 0 idxen offen offset:0 nt lds\n"
    "buffer_load_dwordx4 v[238:239], s[40:43], 0 idxen offen offset:1024 nt lds\n"
    ".Ldin_G2_nd:\n"
    "s_cmp_eq_u32 s67, s82\n"
    "s_cbranch_scc1 .Ldin_L3\n"
    ".Ldin_G3:\n"
    "v_mfma_f32_16x16x32_bf16 v[128:131], v[0:3], v[160:163], v[112:115]\n"
    "v_exp_f32 v144, v144 clamp\n"
    "v_mfma_f32_16x16x32_bf16 v[128:131], v[4:7], v[164:167], v[128:131]\n"
    "v_exp_f32 v145, v145 clamp\n"
    "v_mfma_f32_16x16x32_bf16 v[132:135], v[8:11], v[160:163], v[116:119]\n"
    "v_exp_f32 v146, v146 clamp\n"
    "v_mfma_f32_16x16x32_bf16 v[132:135], v[12:15], v[164:167], v[132:135]\n"
    "v_exp_f32 v147, v147 clamp\n"
    "v_mfma_f32_16x16x32_bf16 v[136:139], v[16:19], v[160:163], v[120:123]\n"
    "v_exp_f32 v148, v148 clamp\n"
    "v_mfma_f32_16x16x32_bf16 v[136:139], v[20:23], v[164:167], v[136:139]\n"
    "v_exp_f32 v149, v149 clamp\n"
    "v_mfma_f32_16x16x32_bf16 v[140:143], v[24:27], v[160:163], v[124:127]\n"
    "v_exp_f32 v150, v150 clamp\n"
    "v_mfma_f32_16x16x32_bf16 v[140:143], v[28:31], v[164:167], v[140:143]\n"
    "v_exp_f32 v151, v151 clamp\n"
    "v_exp_f32 v152, v152 clamp\n"
    "s_cmp_eq_u32 s67, s80\n"
    "s_cselect_b32 s67, s65, s67\n"
    "s_add_u32 m0, s68, 0\n"
    "v_exp_f32 v153, v153 clamp\n"
    "v_add_u32 v253, s67, v244\n"
    "s_waitcnt lgkmcnt(0)\n"
    "v_mfma_f32_4x4x4_16b_bf16 v[224:227], v[228:229], v[168:169], v[224:227] cbsz:4 abid:0\n"
    "v_exp_f32 v154, v154 clamp\n"
    "v_exp_f32 v155, v155 clamp\n"
    "v_mfma_f32_4x4x4_16b_bf16 v[224:227], v[228:229], v[170:171], v[224:227] cbsz:4 abid:1\n"
    "v_exp_f32 v156, v156 clamp\n"
    "v_exp_f32 v157, v157 clamp\n"
    "v_mfma_f32_4x4x4_16b_bf16 v[224:227], v[228:229], v[172:173], v[224:227] cbsz:4 abid:2\n"
    "v_exp_f32 v158, v158 clamp\n"
    "v_exp_f32 v159, v159 clamp\n"
    "v_mfma_f32_4x4x4_16b_bf16 v[224:227], v[228:229], v[174:175], v[224:227] cbsz:4 abid:3\n"
    "s_waitcnt vmcnt(4)\n"
    "s_memrealtime s[34:35]\n"
    "ds_read_b64_tr_b16 v[168:169], v240 offset:0\n"
    "ds_read_b64_tr_b16 v[170:171], v241 offset:256\n"
    "ds_read_b64_tr_b16 v[172:173], v242 offset:512\n"
    "ds_read_b64_tr_b16 v[174:175], v243 offset:768\n"
    "ds_read_b32 v236, v253 offset:0\n"
    "ds_read_b32 v238, v253 offset:4\n"
    "ds_read_b128 v[160:163], v234 offset:2048\n"
    "ds_read_b128 v[164:167], v235 offset:2048\n"
    "v_pk_fma_f32 v[144:145], v[144:145], s[70:71], v[96:97]\n"
    "v_pk_fma_f32 v[146:147], v[146:147], s[70:71], v[98:99]\n"
    "v_pk_add_f32 v[230:231], v[144:145], v[146:147]\n"
    "v_pk_mul_f32 v[232:233], v[144:145], v[146:147]\n"
    "v_pk_fma_f32 v[148:149], v[148:149], s[70:71], v[100:101]\n"
    "v_pk_fma_f32 v[230:231], v[230:231], v[148:149], v[232:233]\n"
    "v_pk_mul_f32 v[232:233], v[232:233], v[148:149]\n"
    "v_pk_fma_f32 v[150:151], v[150:151], s[70:71], v[102:103]\n"
    "v_pk_fma_f32 v[230:231], v[230:231], v[150:151], v[232:233]\n"
    "v_pk_mul_f32 v[232:233], v[232:233], v[150:151]\n"
    "v_pk_fma_f32 v[152:153], v[152:153], s[70:71], v[104:105]\n"
    "v_pk_fma_f32 v[230:231], v[230:231], v[152:153], v[232:233]\n"
    "v_pk_mul_f32 v[232:233], v[232:233], v[152:153]\n"
    "v_pk_fma_f32 v[154:155], v[154:155], s[70:71], v[106:107]\n"
    "v_pk_fma_f32 v[230:231], v[230:231], v[154:155], v[232:233]\n"
    "v_pk_mul_f32 v[232:233], v[232:233], v[154:155]\n"
    "v_pk_fma_f32 v[156:157], v[156:157], s[70:71], v[108:109]\n"
    "v_pk_fma_f32 v[230:231], v[230:231], v[156:157], v[232:233]\n"
    "v_pk_mul_f32 v[232:233], v[232:233], v[156:157]\n"
    "v_pk_fma_f32 v[158:159], v[158:159], s[70:71], v[110:111]\n"
    "v_pk_fma_f32 v[230:231], v[230:231], v[158:159], v[232:233]\n"
    "v_pk_mul_f32 v[232:233], v[232:233], v[158:159]\n"
    "v_rcp_f32 v232, v232\n"
    "v_rcp_f32 v233, v233\n"
    "v_fma_f32 v230, v230, v232, v247\n"
    "v_fmac_f32 v230, v231, v233\n"
    "v_cvt_pk_bf16_f32 v230, v230, v230\n"
    "s_add_u32 s67, s67, 64\n"
    "s_waitcnt lgkmcnt(0)\n"
    "ds_write_b16 v245, v230\n"
    "ds_read_b64 v[228:229], v246\n"
    "s_cmp_le_u32 s34, s76\n"
    "s_cbranch_scc1 .Ldin_G3_wait\n"
    ".Ldin_G3_go:\n"
    "s_add_u32 s76, s76, s72\n"
    ".Ldin_G3_dma:\n"
    "buffer_load_dwordx4 v[236:237], s[40:43], 0 idxen offen offset:0 nt lds\n"
    "buffer_load_dwordx4 v[238:239], s[40:43], 0 idxen offen offset:1024 nt lds\n"
    ".Ldin_G3_nd:\n"
    "s_cmp_eq_u32 s67, s82\n"
    "s_cbranch_scc1 .Ldin_L0\n"
    ".Ldin_G0:\n"
    "v_mfma_f32_16x16x32_bf16 v[144:147], v[0:3], v[160:163], v[112:115]\n"
    "v_exp_f32 v128, v128 clamp\n"
    "v_mfma_f32_16x16x32_bf16 v[144:147], v[4:7], v[164:167], v[144:147]\n"
    "v_exp_f32 v129, v129 clamp\n"
    "v_mfma_f32_16x16x32_bf16 v[148:151], v[8:11], v[160:163], v[116:119]\n"
    "v_exp_f32 v130, v130 clamp\n"
    "v_mfma_f32_16x16x32_bf16 v[148:151], v[12:15], v[164:167], v[148:151]\n"
    "v_exp_f32 v131, v131 clamp\n"
    "v_mfma_f32_16x16x32_bf16 v[152:155], v[16:19], v[160:163], v[120:123]\n"
    "v_exp_f32 v132, v132 clamp\n"
    "v_mfma_f32_16x16x32_bf16 v[152:155], v[20:23], v[164:167], v[152:155]\n"
    "v_exp_f32 v133, v133 clamp\n"
    "v_mfma_f32_16x16x32_bf16 v[156:159], v[24:27], v[160:163], v[124:127]\n"
    "v_exp_f32 v134, v134 clamp\n"
    "v_mfma_f32_16x16x32_bf16 v[156:159], v[28:31], v[164:167], v[156:159]\n"
    "v_exp_f32 v135, v135 clamp\n"
    "v_exp_f32 v136, v136 clamp\n"
    "s_cmp_eq_u32 s67, s80\n"
    "s_cselect_b32 s67, s65, s67\n"
    "s_add_u32 m0, s68, 2048\n"
    "v_exp_f32 v137, v137 clamp\n"
    "v_add_u32_e64 v253, s67, v244\n"
    "s_waitcnt lgkmcnt(0)\n"
    "v_mfma_f32_4x4x4_16b_bf16 v[224:227], v[228:229], v[176:177], v[224:227] cbsz:4 abid:0\n"
    "v_exp_f32 v138, v138 clamp\n"
    "v_exp_f32 v139, v139 clamp\n"
    "v_mfma_f32_4x4x4_16b_bf16 v[224:227], v[228:229], v[178:179], v[224:227] cbsz:4 abid:1\n"
    "v_exp_f32 v140, v140 clamp\n"
    "v_exp_f32 v141, v141 clamp\n"
    "v_mfma_f32_4x4x4_16b_bf16 v[224:227], v[228:229], v[180:181], v[224:227] cbsz:4 abid:2\n"
    "v_exp_f32 v142, v142 clamp\n"
    "v_exp_f32 v143, v143 clamp\n"
    "v_mfma_f32_4x4x4_16b_bf16 v[224:227], v[228:229], v[182:183], v[224:227] cbsz:4 abid:3\n"
    "s_waitcnt vmcnt(4)\n"
    "s_memrealtime s[34:35]\n"
    "ds_read_b64_tr_b16 v[176:177], v240 offset:2048\n"
    "ds_read_b64_tr_b16 v[178:179], v241 offset:2304\n"
    "ds_read_b64_tr_b16 v[180:181], v242 offset:2560\n"
    "ds_read_b64_tr_b16 v[182:183], v243 offset:2816\n"
    "ds_read_b32 v236, v253 offset:0\n"
    "ds_read_b32 v238, v253 offset:4\n"
    "ds_read_b128 v[160:163], v234 offset:4096\n"
    "ds_read_b128 v[164:167], v235 offset:4096\n"
    "v_pk_fma_f32 v[128:129], v[128:129], s[70:71], v[96:97]\n"
    "v_pk_fma_f32 v[130:131], v[130:131], s[70:71], v[98:99]\n"
    "v_pk_add_f32 v[230:231], v[128:129], v[130:131]\n"
    "v_pk_mul_f32 v[232:233], v[128:129], v[130:131]\n"
    "v_pk_fma_f32 v[132:133], v[132:133], s[70:71], v[100:101]\n"
    "v_pk_fma_f32 v[230:231], v[230:231], v[132:133], v[232:233]\n"
    "v_pk_mul_f32 v[232:233], v[232:233], v[132:133]\n"
    "v_pk_fma_f32 v[134:135], v[134:135], s[70:71], v[102:103]\n"
    "v_pk_fma_f32 v[230:231], v[230:231], v[134:135], v[232:233]\n"
    "v_pk_mul_f32 v[232:233], v[232:233], v[134:135]\n"
    "v_pk_fma_f32 v[136:137], v[136:137], s[70:71], v[104:105]\n"
    "v_pk_fma_f32 v[230:231], v[230:231], v[136:137], v[232:233]\n"
    "v_pk_mul_f32 v[232:233], v[232:233], v[136:137]\n"
    "v_pk_fma_f32 v[138:139], v[138:139], s[70:71], v[106:107]\n"
    "v_pk_fma_f32 v[230:231], v[230:231], v[138:139], v[232:233]\n"
    "v_pk_mul_f32 v[232:233], v[232:233], v[138:139]\n"
    "v_pk_fma_f32 v[140:141], v[140:141], s[70:71], v[108:109]\n"
    "v_pk_fma_f32 v[230:231], v[230:231], v[140:141], v[232:233]\n"
    "v_pk_mul_f32 v[232:233], v[232:233], v[140:141]\n"
    "v_pk_fma_f32 v[142:143], v[142:143], s[70:71], v[110:111]\n"
    "v_pk_fma_f32 v[230:231], v[230:231], v[142:143], v[232:233]\n"
    "v_pk_mul_f32 v[232:233], v[232:233], v[142:143]\n"
    "v_rcp_f32 v232, v232\n"
    "v_rcp_f32 v233, v233\n"
    "v_fma_f32 v230, v230, v232, v247\n"
    "v_fmac_f32 v230, v231, v233\n"
    "v_cvt_pk_bf16_f32 v230, v230, v230\n"
    "s_add_u32 s67, s67, 64\n"
    "s_waitcnt lgkmcnt(0)\n"
    "ds_write_b16 v245, v230\n"
    "ds_read_b64 v[228:229], v246\n"
    "s_cmp_le_u32 s34, s76\n"
    "s_cbranch_scc1 .Ldin_G0_wait\n"
    ".Ldin_G0_go:\n"
    "s_sub_u32 s94, s34, 60\n"
    "s_max_u32 s76, s76, s94\n"
    "s_add_u32 s76, s76, s72\n"
    ".Ldin_G0_dma:\n"
    "buffer_load_dwordx4 v[236:237], s[40:43], 0 idxen offen offset:0 nt lds\n"
    "buffer_load_dwordx4 v[238:239], s[40:43], 0 idxen offen offset:1024 nt lds\n"
    ".Ldin_G0_nd:\n"
    "s_cmp_eq_u32 s67, s82\n"
    "s_cbranch_scc1 .Ldin_L1\n"
    ".Ldin_G1:\n"
    "v_mfma_f32_16x16x32_bf16 v[128:131], v[0:3], v[160:163], v[112:115]\n"
    "v_exp_f32 v144, v144 clamp\n"
    "v_mfma_f32_16x16x32_bf16 v[128:131], v[4:7], v[164:167], v[128:131]\n"
    "v_exp_f32 v145, v145 clamp\n"
    "v_mfma_f32_16x16x32_bf16 v[132:135], v[8:11], v[160:163], v[116:119]\n"
    "v_exp_f32 v146, v146 clamp\n"
    "v_mfma_f32_16x16x32_bf16 v[132:135], v[12:15], v[164:167], v[132:135]\n"
    "v_exp_f32 v147, v147 clamp\n"
    "v_mfma_f32_16x16x32_bf16 v[136:139], v[16:19], v[160:163], v[120:123]\n"
    "v_exp_f32 v148, v148 clamp\n"
    "v_mfma_f32_16x16x32_bf16 v[136:139], v[20:23], v[164:167], v[136:139]\n"
    "v_exp_f32 v149, v149 clamp\n"
    "v_mfma_f32_16x16x32_bf16 v[140:143], v[24:27], v[160:163], v[124:127]\n"
    "v_exp_f32 v150, v150 clamp\n"
    "v_mfma_f32_16x16x32_bf16 v[140:143], v[28:31], v[164:167], v[140:143]\n"
    "v_exp_f32 v151, v151 clamp\n"
    "v_exp_f32 v152, v152 clamp\n"
    "s_cmp_eq_u32 s67, s80\n"
    "s_cselect_b32 s67, s65, s67\n"
    "s_add_u32 m0, s68, 4096\n"
    "v_exp_f32 v153, v153 clamp\n"
    "v_add_u32_e64 v253, s67, v244\n"
    "s_waitcnt lgkmcnt(0)\n"
    "v_mfma_f32_4x4x4_16b_bf16 v[224:227], v[228:229], v[168:169], v[224:227] cbsz:4 abid:0\n"
    "v_exp_f32 v154, v154 clamp\n"
    "v_exp_f32 v155, v155 clamp\n"
    "v_mfma_f32_4x4x4_16b_bf16 v[224:227], v[228:229], v[170:171], v[224:227] cbsz:4 abid:1\n"
    "v_exp_f32 v156, v156 clamp\n"
    "v_exp_f32 v157, v157 clamp\n"
    "v_mfma_f32_4x4x4_16b_bf16 v[224:227], v[228:229], v[172:173], v[224:227] cbsz:4 abid:2\n"
    "v_exp_f32 v158, v158 clamp\n"
    "v_exp_f32 v159, v159 clamp\n"
    "v_mfma_f32_4x4x4_16b_bf16 v[224:227], v[228:229], v[174:175], v[224:227] cbsz:4 abid:3\n"
    "s_waitcnt vmcnt(4)\n"
    "s_memrealtime s[34:35]\n"
    "ds_read_b64_tr_b16 v[168:169], v240 offset:4096\n"
    "ds_read_b64_tr_b16 v[170:171], v241 offset:4352\n"
    "ds_read_b64_tr_b16 v[172:173], v242 offset:4608\n"
    "ds_read_b64_tr_b16 v[174:175], v243 offset:4864\n"
    "ds_read_b32 v236, v253 offset:0\n"
    "ds_read_b32 v238, v253 offset:4\n"
    "ds_read_b128 v[160:163], v234 offset:6144\n"
    "ds_read_b128 v[164:167], v235 offset:6144\n"
    "v_pk_fma_f32 v[144:145], v[144:145], s[70:71], v[96:97]\n"
    "v_pk_fma_f32 v[146:147], v[146:147], s[70:71], v[98:99]\n"
    "v_pk_add_f32 v[230:231], v[144:145], v[146:147]\n"
    "v_pk_mul_f32 v[232:233], v[144:145], v[146:147]\n"
    "v_pk_fma_f32 v[148:149], v[148:149], s[70:71], v[100:101]\n"
    "v_pk_fma_f32 v[230:231], v[230:231], v[148:149], v[232:233]\n"
    "v_pk_mul_f32 v[232:233], v[232:233], v[148:149]\n"
    "v_pk_fma_f32 v[150:151], v[150:151], s[70:71], v[102:103]\n"
    "v_pk_fma_f32 v[230:231], v[230:231], v[150:151], v[232:233]\n"
    "v_pk_mul_f32 v[232:233], v[232:233], v[150:151]\n"
    "v_pk_fma_f32 v[152:153], v[152:153], s[70:71], v[104:105]\n"
    "v_pk_fma_f32 v[230:231], v[230:231], v[152:153], v[232:233]\n"
    "v_pk_mul_f32 v[232:233], v[232:233], v[152:153]\n"
    "v_pk_fma_f32 v[154:155], v[154:155], s[70:71], v[106:107]\n"
    "v_pk_fma_f32 v[230:231], v[230:231], v[154:155], v[232:233]\n"
    "v_pk_mul_f32 v[232:233], v[232:233], v[154:155]\n"
    "v_pk_fma_f32 v[156:157], v[156:157], s[70:71], v[108:109]\n"
    "v_pk_fma_f32 v[230:231], v[230:231], v[156:157], v[232:233]\n"
    "v_pk_mul_f32 v[232:233], v[232:233], v[156:157]\n"
    "v_pk_fma_f32 v[158:159], v[158:159], s[70:71], v[110:111]\n"
    "v_pk_fma_f32 v[230:231], v[230:231], v[158:159], v[232:233]\n"
    "v_pk_mul_f32 v[232:233], v[232:233], v[158:159]\n"
    "v_rcp_f32 v232, v232\n"
    "v_rcp_f32 v233, v233\n"
    "v_fma_f32 v230, v230, v232, v247\n"
    "v_fmac_f32 v230, v231, v233\n"
    "v_cvt_pk_bf16_f32 v230, v230, v230\n"
    "s_add_u32 s67, s67, 64\n"
    "s_waitcnt lgkmcnt(0)\n"
    "ds_write_b16 v245, v230\n"
    "ds_read_b64 v[228:229], v246\n"
    "s_cmp_le_u32 s34, s76\n"
    "s_cbranch_scc1 .Ldin_G1_wait\n"
    ".Ldin_G1_go:\n"
    "s_add_u32 s76, s76, s72\n"
    ".Ldin_G1_dma:\n"
    "buffer_load_dwordx4 v[236:237], s[40:43], 0 idxen offen offset:0 nt lds\n"
    "buffer_load_dwordx4 v[238:239], s[40:43], 0 idxen offen offset:1024 nt lds\n"
    ".Ldin_G1_nd:\n"
    "s_cmp_eq_u32 s67, s82\n"
    "s_cbranch_scc1 .Ldin_L2\n"
    "s_branch .Ldin_G2\n"
    ".Ldin_B1_wait:\n"
    "s_cmp_lt_i32 s59, s57\n"
    "s_cbranch_scc1 .Ldin_B1_w2\n"
    "s_sub_u32 s94, s67, s65\n"
    "s_cmp_gt_u32 s94, 256\n"
    "s_cbranch_scc1 .Ldin_B1_dma\n"
    "s_cmp_eq_u32 s94, 64\n"
    "s_waitcnt vmcnt(2)\n"
    "s_cbranch_scc1 .Ldin_B1_nd\n"
    "s_waitcnt vmcnt(0)\n"
    "s_branch .Ldin_B1_nd\n"
    ".Ldin_B1_slp:\n"
    "s_sleep 1\n"
    ".Ldin_B1_w2:\n"
    "s_memrealtime s[34:35]\n"
    "s_waitcnt lgkmcnt(0)\n"
    "s_sub_u32 s94, s34, s76\n"
    "s_cmp_lt_i32 s94, 0\n"
    "s_cbranch_scc1 .Ldin_B1_slp\n"
    "s_branch .Ldin_B1_go\n"
    ".Ldin_G2_wait:\n"
    "s_cmp_lt_i32 s59, s57\n"
    "s_cbranch_scc1 .Ldin_G2_w2\n"
    "s_sub_u32 s94, s67, s65\n"
    "s_cmp_gt_u32 s94, 256\n"
    "s_cbranch_scc1 .Ldin_G2_dma\n"
    "s_cmp_eq_u32 s94, 64\n"
    "s_waitcnt vmcnt(2)\n"
    "s_cbranch_scc1 .Ldin_G2_nd\n"
    "s_waitcnt vmcnt(0)\n"
    "s_branch .Ldin_G2_nd\n"
    ".Ldin_G2_slp:\n"
    "s_sleep 1\n"
    ".Ldin_G2_w2:\n"
    "s_memrealtime s[34:35]\n"
    "s_waitcnt lgkmcnt(0)\n"
    "s_sub_u32 s94, s34, s76\n"
    "s_cmp_lt_i32 s94, 0\n"
    "s_cbranch_scc1 .Ldin_G2_slp\n"
    "s_branch .Ldin_G2_go\n"
    ".Ldin_G3_wait:\n"
    "s_cmp_lt_i32 s59, s57\n"
    "s_cbranch_scc1 .Ldin_G3_w2\n"
    "s_sub_u32 s94, s67, s65\n"
    "s_cmp_gt_u32 s94, 256\n"
    "s_cbranch_scc1 .Ldin_G3_dma\n"
    "s_cmp_eq_u32 s94, 64\n"
    "s_waitcnt vmcnt(2)\n"
    "s_cbranch_scc1 .Ldin_G3_nd\n"
    "s_waitcnt vmcnt(0)\n"
    "s_branch .Ldin_G3_nd\n"
    ".Ldin_G3_slp:\n"
    "s_sleep 1\n"
    ".Ldin_G3_w2:\n"
    "s_memrealtime s[34:35]\n"
    "s_waitcnt lgkmcnt(0)\n"
    "s_sub_u32 s94, s34, s76\n"
    "s_cmp_lt_i32 s94, 0\n"
    "s_cbranch_scc1 .Ldin_G3_slp\n"
    "s_branch .Ldin_G3_go\n"
    ".Ldin_G0_wait:\n"
    "s_cmp_lt_i32 s59, s57\n"
    "s_cbranch_scc1 .Ldin_G0_w2\n"
    "s_sub_u32 s94, s67, s65\n"
    "s_cmp_gt_u32 s94, 256\n"
    "s_cbranch_scc1 .Ldin_G0_dma\n"
    "s_cmp_eq_u32 s94, 64\n"
    "s_waitcnt vmcnt(2)\n"
    "s_cbranch_scc1 .Ldin_G0_nd\n"
    "s_waitcnt vmcnt(0)\n"
    "s_branch .Ldin_G0_nd\n"
    ".Ldin_G0_slp:\n"
    "s_sleep 1\n"
    ".Ldin_G0_w2:\n"
    "s_memrealtime s[34:35]\n"
    "s_waitcnt lgkmcnt(0)\n"
    "s_sub_u32 s94, s34, s76\n"
    "s_cmp_lt_i32 s94, 0\n"
    "s_cbranch_scc1 .Ldin_G0_slp\n"
    "s_branch .Ldin_G0_go\n"
    ".Ldin_G1_wait:\n"
    "s_cmp_lt_i32 s59, s57\n"
    "s_cbranch_scc1 .Ldin_G1_w2\n"
    "s_sub_u32 s94, s67, s65\n"
    "s_cmp_gt_u32 s94, 256\n"
    "s_cbranch_scc1 .Ldin_G1_dma\n"
    "s_cmp_eq_u32 s94, 64\n"
    "s_waitcnt vmcnt(2)\n"
    "s_cbranch_scc1 .Ldin_G1_nd\n"
    "s_waitcnt vmcnt(0)\n"
    "s_branch .Ldin_G1_nd\n"
    ".Ldin_G1_slp:\n"
    "s_sleep 1\n"
    ".Ldin_G1_w2:\n"
    "s_memrealtime s[34:35]\n"
    "s_waitcnt lgkmcnt(0)\n"
    "s_sub_u32 s94, s34, s76\n"
    "s_cmp_lt_i32 s94, 0\n"
    "s_cbranch_scc1 .Ldin_G1_slp\n"
    "s_branch .Ldin_G1_go\n"
    ".p2align 5\n"
    ".Ldin_stub_4:\n"
    "s_cbranch_execz .Ldin_t1_ret\n"
    "s_nop 0\n"
    ".Ldin_y2:\n"
    ".Ldin_t1_ret:\n"
    "s_mov_b64 exec, -1\n"
    "s_waitcnt vmcnt(8)\n"
    "v_lshlrev_b32 v56, 16, v8\n"
    "v_lshlrev_b32 v60, 16, v9\n"
    "v_lshlrev_b32 v64, 16, v10\n"
    "v_lshlrev_b32 v68, 16, v11\n"
    "v_bitop3_b32 v56, v56, s84, v7 bitop3:0x6c\n"
    "v_bitop3_b32 v58, v8, s84, v7 bitop3:0x6c\n"
    "v_bitop3_b32 v60, v60, s84, v7 bitop3:0x6c\n"
    "v_bitop3_b32 v62, v9, s84, v7 bitop3:0x6c\n"
    "v_bitop3_b32 v64, v64, s84, v7 bitop3:0x6c\n"
    "v_bitop3_b32 v66, v10, s84, v7 bitop3:0x6c\n"
    "v_bitop3_b32 v68, v68, s84, v7 bitop3:0x6c\n"
    "v_bitop3_b32 v70, v11, s84, v7 bitop3:0x6c\n"
    "s_waitcnt vmcnt(6)\n"
    "buffer_load_dwordx4 v[32:35], v27, s[44:47], s39 offen nt\n"
    "s_add_u32 s39, s39, s17\n"
    "v_lshlrev_b32 v112, 16, v128\n"
    "v_and_b32 v114, s37, v128\n"
    "v_lshlrev_b32 v116, 16, v129\n"
    "v_and_b32 v118, s37, v129\n"
    "v_lshlrev_b32 v120, 16, v130\n"
    "v_and_b32 v122, s37, v130\n"
    "v_lshlrev_b32 v124, 16, v131\n"
    "v_and_b32 v126, s37, v131\n"
    "buffer_load_dwordx4 v[36:39], v27, s[44:47], s39 offen nt\n"
    "s_add_u32 s39, s39, s17\n"
    "v_lshlrev_b32 v113, 16, v132\n"
    "v_and_b32 v115, s37, v132\n"
    "v_lshlrev_b32 v117, 16, v133\n"
    "v_and_b32 v119, s37, v133\n"
    "v_lshlrev_b32 v121, 16, v134\n"
    "v_and_b32 v123, s37, v134\n"
    "v_lshlrev_b32 v125, 16, v135\n"
    "v_and_b32_e64 v127, s37, v135\n"
    "v_pk_mul_f32 v[112:113], v[112:113], v[56:57] op_sel_hi:[1,0]\n"
    "v_pk_mul_f32 v[114:115], v[114:115], v[58:59] op_sel_hi:[1,0]\n"
    "v_pk_mul_f32 v[116:117], v[116:117], v[60:61] op_sel_hi:[1,0]\n"
    "v_pk_mul_f32 v[118:119], v[118:119], v[62:63] op_sel_hi:[1,0]\n"
    "v_pk_mul_f32 v[120:121], v[120:121], v[64:65] op_sel_hi:[1,0]\n"
    "v_pk_mul_f32 v[122:123], v[122:123], v[66:67] op_sel_hi:[1,0]\n"
    "v_pk_mul_f32 v[124:125], v[124:125], v[68:69] op_sel_hi:[1,0]\n"
    "v_pk_mul_f32 v[126:127], v[126:127], v[70:71] op_sel_hi:[1,0]\n"
    "s_waitcnt vmcnt(6)\n"
    "buffer_load_dwordx4 v[40:43], v27, s[44:47], s39 offen nt\n"
    "s_add_u32 s39, s39, s17\n"
    "v_lshlrev_b32 v168, 16, v136\n"
    "v_and_b32 v170, s37, v136\n"
    "v_lshlrev_b32 v172, 16, v137\n"
    "v_and_b32 v174, s37, v137\n"
    "v_lshlrev_b32 v176, 16, v138\n"
    "v_and_b32 v178, s37, v138\n"
    "v_lshlrev_b32 v180, 16, v139\n"
    "v_and_b32 v182, s37, v139\n"
    "buffer_load_dwordx4 v[44:47], v27, s[44:47], s39 offen nt\n"
    "s_add_u32 s39, s39, s17\n"
    "v_lshlrev_b32 v169, 16, v140\n"
    "v_and_b32 v171, s37, v140\n"
    "v_lshlrev_b32 v173, 16, v141\n"
    "v_and_b32 v175, s37, v141\n"
    "v_lshlrev_b32 v177, 16, v142\n"
    "v_and_b32 v179, s37, v142\n"
    "v_lshlrev_b32 v181, 16, v143\n"
    "v_and_b32_e64 v183, s37, v143\n"
    "v_pk_fma_f32 v[168:169], v[168:169], v[56:57], v[112:113] op_sel_hi:[1,0,1]\n"
    "v_pk_fma_f32 v[170:171], v[170:171], v[58:59], v[114:115] op_sel_hi:[1,0,1]\n"
    "ds_write2st64_b64 v12, v[168:169], v[170:171] offset0:0 offset1:8\n"
    "v_pk_fma_f32 v[172:173], v[172:173], v[60:61], v[116:117] op_sel_hi:[1,0,1]\n"
    "v_pk_fma_f32 v[174:175], v[174:175], v[62:63], v[118:119] op_sel_hi:[1,0,1]\n"
    "ds_write2st64_b64 v12, v[172:173], v[174:175] offset0:16 offset1:24\n"
    "v_pk_fma_f32 v[176:177], v[176:177], v[64:65], v[120:121] op_sel_hi:[1,0,1]\n"
    "v_pk_fma_f32 v[178:179], v[178:179], v[66:67], v[122:123] op_sel_hi:[1,0,1]\n"
    "ds_write2st64_b64 v13, v[176:177], v[178:179] offset0:0 offset1:8\n"
    "v_pk_fma_f32 v[180:181], v[180:181], v[68:69], v[124:125] op_sel_hi:[1,0,1]\n"
    "v_pk_fma_f32 v[182:183], v[182:183], v[70:71], v[126:127] op_sel_hi:[1,0,1]\n"
    "ds_write2st64_b64 v13, v[180:181], v[182:183] offset0:16 offset1:24\n"
    "s_waitcnt vmcnt(6)\n"
    "buffer_load_dwordx4 v[48:51], v27, s[44:47], s39 offen nt\n"
    "s_add_u32 s39, s39, s17\n"
    "v_lshlrev_b32 v80, 16, v144\n"
    "v_and_b32 v82, s37, v144\n"
    "v_lshlrev_b32 v84, 16, v145\n"
    "v_and_b32 v86, s37, v145\n"
    "v_lshlrev_b32 v88, 16, v146\n"
    "v_and_b32 v90, s37, v146\n"
    "v_lshlrev_b32 v92, 16, v147\n"
    "v_and_b32 v94, s37, v147\n"
    "buffer_load_dwordx4 v[52:55], v27, s[44:47], s39 offen nt\n"
    "v_lshlrev_b32 v81, 16, v148\n"
    "v_and_b32 v83, s37, v148\n"
    "v_lshlrev_b32 v85, 16, v149\n"
    "v_and_b32 v87, s37, v149\n"
    "v_lshlrev_b32 v89, 16, v150\n"
    "v_and_b32 v91, s37, v150\n"
    "v_lshlrev_b32 v93, 16, v151\n"
    "v_and_b32 v95, s37, v151\n"
    "v_pk_mul_f32 v[80:81], v[80:81], v[56:57] op_sel_hi:[1,0]\n"
    "v_cvt_pk_bf16_f32 v80, v80, v81\n"
    "v_pk_mul_f32 v[82:83], v[82:83], v[58:59] op_sel_hi:[1,0]\n"
    "v_cvt_pk_bf16_f32 v82, v82, v83\n"
    "ds_write2st64_b32 v14, v80, v82 offset0:0 offset1:8\n"
    "v_pk_mul_f32 v[84:85], v[84:85], v[60:61] op_sel_hi:[1,0]\n"
    "v_cvt_pk_bf16_f32 v84, v84, v85\n"
    "v_pk_mul_f32 v[86:87], v[86:87], v[62:63] op_sel_hi:[1,0]\n"
    "v_cvt_pk_bf16_f32 v86, v86, v87\n"
    "ds_write2st64_b32 v14, v84, v86 offset0:16 offset1:24\n"
    "v_pk_mul_f32 v[88:89], v[88:89], v[64:65] op_sel_hi:[1,0]\n"
    "v_cvt_pk_bf16_f32 v88, v88, v89\n"
    "v_pk_mul_f32 v[90:91], v[90:91], v[66:67] op_sel_hi:[1,0]\n"
    "v_cvt_pk_bf16_f32 v90, v90, v91\n"
    "ds_write2st64_b32 v15, v88, v90 offset0:0 offset1:8\n"
    "v_pk_mul_f32 v[92:93], v[92:93], v[68:69] op_sel_hi:[1,0]\n"
    "v_cvt_pk_bf16_f32 v92, v92, v93\n"
    "v_pk_mul_f32 v[94:95], v[94:95], v[70:71] op_sel_hi:[1,0]\n"
    "v_cvt_pk_bf16_f32 v94, v94, v95\n"
    "ds_write2st64_b32 v15, v92, v94 offset0:16 offset1:24\n"
    "s_waitcnt vmcnt(6)\n"
    "v_lshlrev_b32 v184, 16, v152\n"
    "v_and_b32 v186, s37, v152\n"
    "v_lshlrev_b32 v188, 16, v153\n"
    "v_and_b32 v190, s37, v153\n"
    "v_lshlrev_b32 v192, 16, v154\n"
    "v_and_b32 v194, s37, v154\n"
    "v_lshlrev_b32 v196, 16, v155\n"
    "v_and_b32 v198, s37, v155\n"
    "v_lshlrev_b32 v185, 16, v156\n"
    "v_and_b32 v187, s37, v156\n"
    "v_lshlrev_b32 v189, 16, v157\n"
    "v_and_b32 v191, s37, v157\n"
    "v_lshlrev_b32 v193, 16, v158\n"
    "v_and_b32 v195, s37, v158\n"
    "v_lshlrev_b32 v197, 16, v159\n"
    "v_and_b32_e64 v199, s37, v159\n"
    "s_mul_i32 s36, s61, s16\n"
    "s_add_u32 s94, s36, s16\n"
    "s_cmp_lt_u32 s61, s14\n"
    "s_cselect_b32 s46, s94, 0\n"
    "buffer_load_dwordx2 v[228:229], v105, s[44:47], s36 offen nt\n"
    "s_lshl_b32 s94, s61, 6\n"
    "s_add_u32 s94, s3, s94\n"
    "s_cmp_lt_u32 s61, s14\n"
    "s_cselect_b32 s58, s94, s13\n"
    "s_add_u32 s95, s61, s15\n"
    "s_add_u32 s36, s36, s17\n"
    "s_lshl_b32 s94, s95, 6\n"
    "s_add_u32 s94, s3, s94\n"
    "s_add_u32 s96, s36, s16\n"
    "s_add_u32 s94, s94, 8\n"
    "s_cmp_lt_u32 s95, s14\n"
    "s_cselect_b32 s46, s96, 0\n"
    "s_cselect_b32 s59, s94, s13\n"
    "buffer_load_short_d16_hi v224, v249, s[44:47], s36 offen offset:0 nt\n"
    "buffer_load_short_d16_hi v225, v249, s[44:47], s36 offen offset:128 nt\n"
    "buffer_load_short_d16_hi v226, v249, s[44:47], s36 offen offset:256 nt\n"
    "buffer_load_short_d16_hi v227, v249, s[44:47], s36 offen offset:384 nt\n"
    "s_mul_i32 s46, s14, s16\n"
    "s_waitcnt lgkmcnt(0)\n"
    "s_barrier\n"
    "ds_read_b128 v[128:131], v97 offset:0\n"
    "ds_read_b128 v[132:135], v97 offset:16\n"
    "ds_read_b128 v[28:31], v99 offset:35200\n"
    "v_pk_fma_f32 v[184:185], v[184:185], v[56:57], v[112:113] op_sel_hi:[1,0,1] neg_lo:[0,0,1] neg_hi:[0,0,1]\n"
    "v_cvt_pk_bf16_f32 v184, v184, v185\n"
    "v_pk_fma_f32 v[186:187], v[186:187], v[58:59], v[114:115] op_sel_hi:[1,0,1] neg_lo:[0,0,1] neg_hi:[0,0,1]\n"
    "v_cvt_pk_bf16_f32 v186, v186, v187\n"
    "ds_write2st64_b32 v14, v184, v186 offset0:32 offset1:40\n"
    "v_pk_fma_f32 v[188:189], v[188:189], v[60:61], v[116:117] op_sel_hi:[1,0,1] neg_lo:[0,0,1] neg_hi:[0,0,1]\n"
    "v_cvt_pk_bf16_f32 v188, v188, v189\n"
    "v_pk_fma_f32 v[190:191], v[190:191], v[62:63], v[118:119] op_sel_hi:[1,0,1] neg_lo:[0,0,1] neg_hi:[0,0,1]\n"
    "v_cvt_pk_bf16_f32 v190, v190, v191\n"
    "ds_write2st64_b32 v14, v188, v190 offset0:48 offset1:56\n"
    "v_pk_fma_f32 v[192:193], v[192:193], v[64:65], v[120:121] op_sel_hi:[1,0,1] neg_lo:[0,0,1] neg_hi:[0,0,1]\n"
    "v_cvt_pk_bf16_f32 v192, v192, v193\n"
    "v_pk_fma_f32 v[194:195], v[194:195], v[66:67], v[122:123] op_sel_hi:[1,0,1] neg_lo:[0,0,1] neg_hi:[0,0,1]\n"
    "v_cvt_pk_bf16_f32 v194, v194, v195\n"
    "ds_write2st64_b32 v15, v192, v194 offset0:32 offset1:40\n"
    ".Ldin_stub_5:\n"
    "s_cbranch_execz .Ldin_t1_ret\n"
    "s_nop 0\n"
    "v_pk_fma_f32 v[196:197], v[196:197], v[68:69], v[124:125] op_sel_hi:[1,0,1] neg_lo:[0,0,1] neg_hi:[0,0,1]\n"
    "v_cvt_pk_bf16_f32 v196, v196, v197\n"
    "v_pk_fma_f32 v[198:199], v[198:199], v[70:71], v[126:127] op_sel_hi:[1,0,1] neg_lo:[0,0,1] neg_hi:[0,0,1]\n"
    "v_cvt_pk_bf16_f32 v198, v198, v199\n"
    "ds_write2st64_b32 v15, v196, v198 offset0:48 offset1:56\n"
    "s_waitcnt lgkmcnt(0)\n"
    "s_barrier\n"
    "ds_read_b128 v[8:11], v98 offset:0\n"
    "ds_read_b128 v[12:15], v98 offset:1024\n"
    "s_waitcnt lgkmcnt(1)\n"
    "v_mfma_f32_16x16x32_bf16 v[112:115], v[8:11], v[128:131], v[28:31]\n"
    "s_waitcnt lgkmcnt(0)\n"
    "v_mfma_f32_16x16x32_bf16 v[112:115], v[12:15], v[132:135], v[112:115]\n"
    "s_nop 7\n"
    "ds_write_b128 v100, v[112:115] offset:17408\n"
    "s_waitcnt lgkmcnt(0)\n"
    "s_barrier\n"
    "s_setprio 0\n"
    "ds_read_b128 v[184:187], v252 offset:0\n"
    "ds_read_b128 v[188:191], v252 offset:16\n"
    "ds_read_b32 v247, v250 offset:35072\n"
    "ds_read_b32 v232, v250 offset:35076\n"
    "s_and_b32 s84, s12, 7\n"
    "v_sub_u32 v4, s16, v26\n"
    "s_lshl_b32 s84, -1, s84\n"
    "v_cmp_gt_i32 vcc, 16, v4\n"
    "s_not_b32 s84, s84\n"
    "v_mov_b32 v6, s84\n"
    "s_add_u32 s85, s66, 480\n"
    "v_cndmask_b32 v6, -1, v6, vcc\n"
    "v_and_b32 v4, 32, v251\n"
    "v_add3_u32 v7, v4, v251, s85\n"
    "v_mov_b32 v5, 0\n"
    "s_mov_b32 s4, 0x10001\n"
    "s_mov_b32 s5, 0xc080c080\n"
    "s_waitcnt lgkmcnt(0)\n"
    "v_readfirstlane_b32 s69, v232\n"
    "v_and_b32 v184, v254, v184\n"
    "v_and_b32 v185, v255, v185\n"
    "v_and_b32 v186, v254, v186\n"
    "v_and_b32 v187, v255, v187\n"
    "v_and_b32 v188, v254, v188\n"
    "v_and_b32 v189, v255, v189\n"
    "v_and_b32 v190, v254, v190\n"
    "v_and_b32 v191, v255, v191\n"
    "s_waitcnt vmcnt(4)\n"
    "v_cmp_ne_u16_e64 s[84:85], 0, v228\n"
    "v_cmp_lt_u32_e64 s[86:87], s38, v228\n"
    "v_cmp_ne_u16_e64 s[88:89], 0, v229\n"
    "v_cmp_lt_u32_e64 s[90:91], s38, v229\n"
    "v_mbcnt_lo_u32_b32 v220, s84, v220\n"
    "s_bcnt1_i32_b64 s92, s[84:85]\n"
    "v_mbcnt_hi_u32_b32 v220, s85, v220\n"
    "s_lshl2_add_u32 s92, s92, s67\n"
    "v_mbcnt_lo_u32_b32 v220, s86, v220\n"
    "s_bcnt1_i32_b64 s93, s[86:87]\n"
    "v_mbcnt_hi_u32_b32 v220, s87, v220\n"
    "s_lshl2_add_u32 s93, s93, s92\n"
    "v_mbcnt_lo_u32_b32 v220, s88, v220\n"
    "s_bcnt1_i32_b64 s94, s[88:89]\n"
    "v_mbcnt_hi_u32_b32 v220, s89, v220\n"
    "s_lshl2_add_u32 s94, s94, s93\n"
    "v_mbcnt_lo_u32_b32 v220, s90, v220\n"
    "s_bcnt1_i32_b64 s95, s[90:91]\n"
    "v_mbcnt_hi_u32_b32 v220, s91, v220\n"
    "s_lshl2_add_u32 s95, s95, s94\n"
    "v_mov_b32_e64 v21, -1\n"
    "v_lshl_add_u32 v20, v249, 1, s95\n"
    "v_addc_co_u32_e64 v221, vcc, v220, 0, s[84:85]\n"
    "v_addc_co_u32_e64 v222, vcc, v221, 0, s[86:87]\n"
    "v_addc_co_u32_e64 v223, vcc, v222, 0, s[88:89]\n"
    "ds_write2_b32 v20, v21, v21 offset1:64\n"
    "v_lshlrev_b32 v220, 2, v220\n"
    "v_lshlrev_b32 v221, 2, v221\n"
    "v_lshlrev_b32 v222, 2, v222\n"
    "v_lshlrev_b32_e64 v223, 2, v223\n"
    "s_mov_b64 exec, s[84:85]\n"
    "ds_write_b32 v220, v216\n"
    "s_mov_b64 exec, s[86:87]\n"
    "ds_write_b32 v221, v217\n"
    "s_mov_b64 exec, s[88:89]\n"
    "ds_write_b32 v222, v218\n"
    "s_mov_b64 exec, s[90:91]\n"
    "ds_write_b32 v223, v219\n"
    "s_mov_b64 exec, -1\n"
    "ds_read_b32 v200, v253 offset:0\n"
    "ds_read_b32 v202, v253 offset:4\n"
    "ds_read_b32 v204, v253 offset:64\n"
    "ds_read_b32 v206, v253 offset:68\n"
    "ds_read_b32 v208, v253 offset:128\n"
    "ds_read_b32 v210, v253 offset:132\n"
    "ds_read_b32 v212, v253 offset:192\n"
    "ds_read_b32 v214, v253 offset:196\n"
    "s_add_u32 m0, s68, 0\n"
    "s_waitcnt lgkmcnt(6)\n"
    "buffer_load_dwordx4 v[200:201], s[40:43], 0 idxen offen offset:0 nt lds\n"
    "buffer_load_dwordx4 v[202:203], s[40:43], 0 idxen offen offset:1024 nt lds\n"
    "s_add_u32 s95, s95, 60\n"
    "s_add_u32 s94, s67, 384\n"
    "s_andn2_b32 s95, s95, 63\n"
    "s_max_u32 s80, s95, s94\n"
    "s_add_u32 m0, s68, 2048\n"
    "s_waitcnt lgkmcnt(0)\n"
    "buffer_load_dwordx4 v[204:205], s[40:43], 0 idxen offen offset:0 nt lds\n"
    "s_mov_b32 s94, 0x10001\n"
    "s_mov_b32 s95, 0xc080c080\n"
    "v_pk_min_u16 v22, v228, s94\n"
    "v_pk_min_u16 v23, v229, s94\n"
    "v_pk_mad_u16 v228, v22, s95, v228\n"
    "v_pk_mad_u16 v229, v23, s95, v229\n"
    "s_sub_u32 s87, s80, s81\n"
    "v_or_b32 v228, v228, v229\n"
    "s_lshr_b32 s87, s87, 6\n"
    "s_lshl2_add_u32 s86, s3, 68224\n"
    "v_mov_b32 v106, s86\n"
    "v_mov_b32 v108, s66\n"
    "v_mov_b32 v109, s58\n"
    "v_mov_b32 v110, s59\n"
    "s_waitcnt vmcnt(3)\n"
    "v_cmp_neq_f32_e64 s[20:21], 0, v224\n"
    "v_cmp_neq_f32_e64 s[22:23], 0, v225\n"
    "v_cmp_neq_f32_e64 s[24:25], 0, v226\n"
    "v_cmp_neq_f32_e64 s[26:27], 0, v227\n"
    "v_fma_f32 v224, v224, v224, -v224\n"
    "v_fma_f32 v225, v225, v225, -v225\n"
    "v_fma_f32 v226, v226, v226, -v226\n"
    "v_fma_f32 v227, v227, v227, -v227\n"
    "s_bcnt1_i32_b64 s88, s[20:21]\n"
    "s_bcnt1_i32_b64 s89, s[22:23]\n"
    "buffer_load_dwordx4 v[206:207], s[40:43], 0 idxen offen offset:1024 nt lds\n"
    "s_bcnt1_i32_b64 s90, s[24:25]\n"
    "s_bcnt1_i32_b64 s91, s[26:27]\n"
    "v_or3_b32 v228, v228, v224, v225\n"
    "s_add_u32 s88, s88, s89\n"
    "v_or3_b32 v228, v228, v226, v227\n"
    "s_add_u32 s90, s90, s91\n"
    "v_cmp_ne_u32_e64 s[84:85], 0, v228\n"
    "s_add_u32 s88, s88, s90\n"
    "s_pack_ll_b32_b16 s88, s87, s88\n"
    "s_mov_b64 exec, 1\n"
    "v_mov_b32_e64 v107, s88\n"
    "ds_write2_b32 v108, v109, v110 offset1:2\n"
    "s_cmp_lg_u64 s[84:85], 0\n"
    "ds_write_b32 v106, v107\n"
    "s_cselect_b32 s91, 2.0, 0\n"
    "s_mov_b64 exec, -1\n"
    "s_or_b32 s58, s58, s91\n"
    "s_or_b32 s59, s59, s91\n"
    "v_pk_min_u16 v0, v32, s4\n"
    "v_pk_min_u16 v1, v33, s4\n"
    "v_pk_min_u16 v2, v34, s4\n"
    "v_pk_min_u16 v3, v35, s4\n"
    "v_pk_mad_u16 v32, v0, s5, v32\n"
    "v_pk_mad_u16 v33, v1, s5, v33\n"
    "s_add_u32 m0, s68, 4096\n"
    "s_nop 0\n"
    "buffer_load_dwordx4 v[208:209], s[40:43], 0 idxen offen offset:0 nt lds\n"
    "v_pk_mad_u16 v34, v2, s5, v34\n"
    "v_pk_mad_u16 v35, v3, s5, v35\n"
    "v_lshl_or_b32 v0, v1, 2, v0\n"
    "v_lshl_or_b32 v0, v2, 4, v0\n"
    "v_lshl_or_b32 v0, v3, 6, v0\n"
    "v_or3_b32 v5, v5, v32, v33\n"
    "v_lshrrev_b32 v1, 15, v0\n"
    ".Ldin_stub_0:\n"
    "s_cbranch_execz .Ldin_t0_ret\n"
    "s_nop 0\n"
    "v_or3_b32 v5, v5, v34, v35\n"
    "v_bitop3_b32 v16, v1, v6, v0 bitop3:0xc8\n"
    "ds_write_b8 v7, v16 offset:0\n"
    "v_pk_min_u16 v0, v36, s4\n"
    "v_pk_min_u16 v1, v37, s4\n"
    "v_pk_min_u16 v2, v38, s4\n"
    "v_pk_min_u16 v3, v39, s4\n"
    "v_pk_mad_u16 v36, v0, s5, v36\n"
    "v_pk_mad_u16 v37, v1, s5, v37\n"
    "v_pk_mad_u16 v38, v2, s5, v38\n"
    "v_pk_mad_u16 v39, v3, s5, v39\n"
    "v_lshl_or_b32 v0, v1, 2, v0\n"
    "v_lshl_or_b32 v0, v2, 4, v0\n"
    "v_lshl_or_b32 v0, v3, 6, v0\n"
    "v_or3_b32 v5, v5, v36, v37\n"
    "v_lshrrev_b32_e64 v1, 15, v0\n"
    "v_or3_b32 v5, v5, v38, v39\n"
    "v_bitop3_b32 v17, v1, v6, v0 bitop3:0xc8\n"
    "buffer_load_dwordx4 v[210:211], s[40:43], 0 idxen offen offset:1024 nt lds\n"
    "ds_write_b8 v7, v17 offset:512\n"
    "v_pk_min_u16 v0, v40, s4\n"
    "v_pk_min_u16 v1, v41, s4\n"
    "v_pk_min_u16 v2, v42, s4\n"
    "v_pk_min_u16 v3, v43, s4\n"
    "v_pk_mad_u16 v40, v0, s5, v40\n"
    "v_pk_mad_u16 v41, v1, s5, v41\n"
    "v_pk_mad_u16 v42, v2, s5, v42\n"
    "v_pk_mad_u16 v43, v3, s5, v43\n"
    "v_lshl_or_b32 v0, v1, 2, v0\n"
    "v_lshl_or_b32 v0, v2, 4, v0\n"
    "v_lshl_or_b32 v0, v3, 6, v0\n"
    "v_or3_b32 v5, v5, v40, v41\n"
    "v_lshrrev_b32_e64 v1, 15, v0\n"
    "v_or3_b32 v5, v5, v42, v43\n"
    "v_bitop3_b32 v18, v1, v6, v0 bitop3:0xc8\n"
    "ds_write_b8 v7, v18 offset:1024\n"
    "v_pk_min_u16 v0, v44, s4\n"
    "v_pk_min_u16 v1, v45, s4\n"
    "v_pk_min_u16 v2, v46, s4\n"
    "v_pk_min_u16 v3, v47, s4\n"
    "v_pk_mad_u16 v44, v0, s5, v44\n"
    "v_pk_mad_u16 v45, v1, s5, v45\n"
    "v_pk_mad_u16 v46, v2, s5, v46\n"
    "v_pk_mad_u16 v47, v3, s5, v47\n"
    "s_add_u32 m0, s68, 6144\n"
    "s_nop 0\n"
    "buffer_load_dwordx4 v[212:213], s[40:43], 0 idxen offen offset:0 nt lds\n"
    "v_lshl_or_b32 v0, v1, 2, v0\n"
    "v_lshl_or_b32 v0, v2, 4, v0\n"
    "v_lshl_or_b32 v0, v3, 6, v0\n"
    "v_or3_b32 v5, v5, v44, v45\n"
    "v_lshrrev_b32 v1, 15, v0\n"
    "v_or3_b32 v5, v5, v46, v47\n"
    "v_bitop3_b32 v19, v1, v6, v0 bitop3:0xc8\n"
    "ds_write_b8 v7, v19 offset:1536\n"
    "v_pk_min_u16 v0, v48, s4\n"
    "v_pk_min_u16 v1, v49, s4\n"
    "v_pk_min_u16 v2, v50, s4\n"
    "v_pk_min_u16 v3, v51, s4\n"
    "v_pk_mad_u16 v48, v0, s5, v48\n"
    "v_pk_mad_u16 v49, v1, s5, v49\n"
    "v_pk_mad_u16 v50, v2, s5, v50\n"
    "v_pk_mad_u16 v51, v3, s5, v51\n"
    "v_lshl_or_b32 v0, v1, 2, v0\n"
    "v_lshl_or_b32 v0, v2, 4, v0\n"
    "v_lshl_or_b32 v0, v3, 6, v0\n"
    "v_or3_b32 v5, v5, v48, v49\n"
    "v_lshrrev_b32_e64 v1, 15, v0\n"
    "v_or3_b32 v5, v5, v50, v51\n"
    "v_bitop3_b32 v16, v1, v6, v0 bitop3:0xc8\n"
    "ds_write_b8 v7, v16 offset:2048\n"
    "v_pk_min_u16 v0, v52, s4\n"
    "buffer_load_dwordx4 v[214:215], s[40:43], 0 idxen offen offset:1024 nt lds\n"
    "v_pk_min_u16 v1, v53, s4\n"
    "v_pk_min_u16 v2, v54, s4\n"
    "v_pk_min_u16 v3, v55, s4\n"
    "v_pk_mad_u16 v52, v0, s5, v52\n"
    "v_pk_mad_u16 v53, v1, s5, v53\n"
    "v_pk_mad_u16 v54, v2, s5, v54\n"
    "v_pk_mad_u16 v55, v3, s5, v55\n"
    "v_lshl_or_b32 v0, v1, 2, v0\n"
    "v_lshl_or_b32 v0, v2, 4, v0\n"
    "v_lshl_or_b32 v0, v3, 6, v0\n"
    "v_or3_b32 v5, v5, v52, v53\n"
    "v_lshrrev_b32_e64 v1, 15, v0\n"
    "v_or3_b32 v5, v5, v54, v55\n"
    "v_bitop3_b32 v17, v1, v6, v0 bitop3:0xc8\n"
    "ds_write_b8 v7, v17 offset:2560\n"
    "ds_read_b128 v[128:131], v97 offset:4352\n"
    "ds_read_b128 v[132:135], v97 offset:4368\n"
    "ds_read_b128 v[144:147], v97 offset:8704\n"
    "ds_read_b128 v[148:151], v97 offset:8720\n"
    "ds_read_b128 v[160:163], v97 offset:13056\n"
    "ds_read_b128 v[164:167], v97 offset:13072\n"
    "s_waitcnt lgkmcnt(4)\n"
    "v_mfma_f32_16x16x32_bf16 v[116:119], v[8:11], v[128:131], v[28:31]\n"
    "v_mfma_f32_16x16x32_bf16 v[116:119], v[12:15], v[132:135], v[116:119]\n"
    "s_waitcnt lgkmcnt(2)\n"
    "v_mfma_f32_16x16x32_bf16 v[120:123], v[8:11], v[144:147], v[28:31]\n"
    "v_mfma_f32_16x16x32_bf16 v[120:123], v[12:15], v[148:151], v[120:123]\n"
    "s_waitcnt lgkmcnt(0)\n"
    "v_mfma_f32_16x16x32_bf16 v[124:127], v[8:11], v[160:163], v[28:31]\n"
    "v_mfma_f32_16x16x32_bf16 v[124:127], v[12:15], v[164:167], v[124:127]\n"
    "s_nop 7\n"
    "ds_write_b128 v100, v[116:119] offset:21760\n"
    "ds_write_b128 v100, v[120:123] offset:26112\n"
    "ds_write_b128 v100, v[124:127] offset:30464\n"
    "v_cmp_ne_u32 vcc, 0, v5\n"
    "s_cmp_lg_u64 vcc, 0\n"
    "s_cselect_b32 s94, 0x101, 1\n"
    "s_mov_b64 exec, 1\n"
    "v_mov_b32 v10, s94\n"
    "ds_add_rtn_u32 v10, v250, v10 offset:35840\n"
    "s_mov_b64 exec, -1\n"
    "s_waitcnt lgkmcnt(0)\n"
    "v_readfirstlane_b32 s95, v10\n"
    "s_add_u32 s96, s95, s94\n"
    "s_bitcmp1_b32 s96, 2\n"
    "s_cbranch_scc1 .Ldin_sched\n"
    ".Ldin_ygate:\n"
    ".Ldin_yentry:\n"
    "s_mov_b64 vcc, -1\n"
    "s_branch .Ldin_entry\n"
    ".Ldin_swait:\n"
    "s_sleep 1\n"
    "ds_read_b32 v10, v250 offset:35840\n"
    "s_waitcnt lgkmcnt(0)\n"
    "v_readfirstlane_b32 s96, v10\n"
    ".Ldin_sched:\n"
    "s_setprio 1\n"
    "v_lshrrev_b32 v2, 3, v251\n"
    "v_and_b32 v3, 7, v251\n"
    "v_lshlrev_b32 v6, 3, v3\n"
    "v_add_u32 v7, 16, v251\n"
    "s_mov_b32 s84, 0x10b80\n"
    "v_lshl_add_u32 v12, v7, 6, s84\n"
    "ds_read_b128 v[32:35], v12\n"
    "ds_read_b128 v[36:39], v12 offset:16\n"
    "s_mov_b32 s85, 0x10a80\n"
    "v_lshl_add_u32 v4, v2, 2, s85\n"
    "v_lshl_add_u32 v5, v3, 2, s85\n"
    "ds_read_u16 v17, v4 offset:2\n"
    "ds_read_u16 v4, v4\n"
    "ds_read_u16 v16, v5 offset:2\n"
    "ds_read_u16 v5, v5\n"
    "s_lshl_b32 s85, s2, 3\n"
    "s_lshl1_add_u32 s85, s15, s85\n"
    "v_mad_u32_u24 v8, v2, s15, v3\n"
    "v_add_u32 v8, s85, v8\n"
    "s_cmp_ge_u32 s96, 256\n"
    "v_cmp_gt_u32_e64 s[86:87], s14, v8\n"
    "s_cselect_b32 s85, 2.0, 0\n"
    "v_lshlrev_b32 v8, 6, v8\n"
    "s_and_b32 s87, s87, 0xffff\n"
    "v_or3_b32 v8, v8, v7, s85\n"
    "v_mov_b32 v12, s13\n"
    "v_cndmask_b32_e64 v8, v12, v8, s[86:87]\n"
    "v_cmp_eq_u32 vcc, s3, v2\n"
    "v_lshrrev_b32 v14, 2, v2\n"
    "v_mul_u32_u24 v14, 2, v14\n"
    "v_addc_co_u32_e64 v14, s[88:89], v14, 0, vcc\n"
    "v_cmp_eq_u32 vcc, s3, v3\n"
    "v_lshrrev_b32_e64 v15, 2, v3\n"
    "v_mul_u32_u24 v15, 2, v15\n"
    "v_addc_co_u32_e64 v15, s[88:89], v15, 0, vcc\n"
    "s_waitcnt lgkmcnt(4)\n"
    "v_bcnt_u32_b32 v9, v32, 15\n"
    "v_bcnt_u32_b32 v9, v33, v9\n"
    "v_bcnt_u32_b32 v9, v34, v9\n"
    "v_bcnt_u32_b32 v9, v35, v9\n"
    "v_bcnt_u32_b32 v9, v36, v9\n"
    "v_bcnt_u32_b32 v9, v37, v9\n"
    "v_bcnt_u32_b32 v9, v38, v9\n"
    "v_bcnt_u32_b32 v9, v39, v9\n"
    "v_lshrrev_b32 v9, 4, v9\n"
    "v_max_u32 v9, 6, v9\n"
    "v_lshlrev_b32 v9, 3, v9\n"
    "v_cndmask_b32_e64 v9, 0, v9, s[86:87]\n"
    "v_sub_u32 v10, v3, v9\n"
    "s_waitcnt lgkmcnt(0)\n"
    "v_add_u32 v16, 15, v16\n"
    "v_lshrrev_b32 v16, 4, v16\n"
    "v_max_u32 v16, 6, v16\n"
    "v_add_u32 v17, 15, v17\n"
    "v_lshrrev_b32_e64 v17, 4, v17\n"
    "v_max_u32 v17, 6, v17\n"
    "v_add3_u32 v4, v4, v14, v17\n"
    "v_add3_u32 v5, v5, v15, v16\n"
    "v_mov_b32_dpp v12, v10 row_half_mirror row_mask:0xf bank_mask:0xf\n"
    "v_lshl_or_b32 v4, v4, 3, v2\n"
    "v_lshl_or_b32 v5, v5, 3, v3\n"
    "v_sub_u32_dpp v32, v10, v10 quad_perm:[1,0,3,2] row_mask:0xf bank_mask:0xf\n"
    "v_sub_u32_dpp v33, v10, v10 quad_perm:[2,3,0,1] row_mask:0xf bank_mask:0xf\n"
    "v_sub_u32_dpp v34, v10, v10 quad_perm:[3,2,1,0] row_mask:0xf bank_mask:0xf\n"
    "v_sub_u32_dpp v35, v12, v10 quad_perm:[0,1,2,3] row_mask:0xf bank_mask:0xf\n"
    "v_sub_u32_dpp v36, v12, v10 quad_perm:[1,0,3,2] row_mask:0xf bank_mask:0xf\n"
    "v_sub_u32_dpp v37, v12, v10 quad_perm:[2,3,0,1] row_mask:0xf bank_mask:0xf\n"
    "v_sub_u32_dpp v38, v12, v10 quad_perm:[3,2,1,0] row_mask:0xf bank_mask:0xf\n"
    "v_lshrrev_b32 v32, 31, v32\n"
    "v_lshrrev_b32 v33, 31, v33\n"
    "v_lshrrev_b32 v34, 31, v34\n"
    "v_lshrrev_b32 v35, 31, v35\n"
    "v_lshrrev_b32 v36, 31, v36\n"
    "v_lshrrev_b32 v37, 31, v37\n"
    "v_lshrrev_b32 v38, 31, v38\n"
    "v_add3_u32 v11, v32, v33, v244\n"
    "v_add3_u32 v11, v34, v35, v11\n"
    "v_add3_u32 v11, v36, v37, v11\n"
    "v_add_u32 v11, v38, v11\n"
    "v_add_u32 v12, s65, v11\n"
    "v_lshl_add_u32 v14, v11, 3, s65\n"
    "ds_write_b8 v12, v9\n"
    "ds_write_b32 v14, v8 offset:64\n"
    "v_mov_b32 v12, s65\n"
    "ds_read_b128 v[40:43], v12 offset:0\n"
    "ds_read_b128 v[44:47], v12 offset:16\n"
    "ds_read_b128 v[48:51], v12 offset:32\n"
    "v_lshlrev_b32 v16, 3, v16\n"
    "v_sub_u32 v72, v5, v16\n"
    "v_cmp_lt_i32 vcc, v5, v4\n"
    "v_mov_b32_e64 v73, v5\n"
    "s_waitcnt lgkmcnt(0)\n"
    "v_lshrrev_b64 v[20:21], v6, vcc\n"
    "v_lshrrev_b64 v[18:19], v244, vcc\n"
    "v_and_b32 v20, 0xff, v20\n"
    "v_and_b32 v18, 0xff, v18\n"
    "v_bcnt_u32_b32 v20, v20, 0\n"
    "v_bcnt_u32_b32 v18, v18, 0\n"
    "v_lshl_add_u32 v20, v20, 3, s65\n"
    "v_lshlrev_b32_e64 v18, 3, v18\n"
    "ds_read_b32 v56, v20 offset:64\n"
    "v_lshrrev_b64 v[22:23], v20, v[40:41]\n"
    "v_lshrrev_b64 v[18:19], v18, v[40:41]\n"
    "v_and_b32 v22, 0xff, v22\n"
    "v_and_b32 v18, 0xff, v18\n"
    "v_add_u32 v5, v5, v22\n"
    "v_add_u32 v4, v4, v18\n"
    "v_cmp_lt_i32 vcc, v5, v4\n"
    "v_mov_b32_e64 v74, v5\n"
    "s_nop 0\n"
    "v_lshrrev_b64 v[20:21], v6, vcc\n"
    "v_lshrrev_b64 v[18:19], v244, vcc\n"
    "v_and_b32 v20, 0xff, v20\n"
    "v_and_b32 v18, 0xff, v18\n"
    "v_bcnt_u32_b32 v20, v20, 0\n"
    "v_bcnt_u32_b32 v18, v18, 0\n"
    "v_lshl_add_u32 v20, v20, 3, s65\n"
    "v_lshlrev_b32_e64 v18, 3, v18\n"
    "ds_read_b32 v58, v20 offset:128\n"
    "v_lshrrev_b64 v[22:23], v20, v[42:43]\n"
    "v_lshrrev_b64 v[18:19], v18, v[42:43]\n"
    "v_and_b32 v22, 0xff, v22\n"
    "v_and_b32 v18, 0xff, v18\n"
    "v_add_u32 v5, v5, v22\n"
    "v_add_u32 v4, v4, v18\n"
    "v_cmp_lt_i32 vcc, v5, v4\n"
    "v_mov_b32_e64 v75, v5\n"
    "s_nop 0\n"
    "v_lshrrev_b64 v[20:21], v6, vcc\n"
    "v_lshrrev_b64 v[18:19], v244, vcc\n"
    "v_and_b32 v20, 0xff, v20\n"
    "v_and_b32 v18, 0xff, v18\n"
    "v_bcnt_u32_b32 v20, v20, 0\n"
    "v_bcnt_u32_b32 v18, v18, 0\n"
    "v_lshl_add_u32 v20, v20, 3, s65\n"
    "v_lshlrev_b32_e64 v18, 3, v18\n"
    "ds_read_b32 v60, v20 offset:192\n"
    "v_lshrrev_b64 v[22:23], v20, v[44:45]\n"
    "v_lshrrev_b64 v[18:19], v18, v[44:45]\n"
    ".Ldin_stub_2:\n"
    "s_cbranch_execz .Ldin_t0_ret\n"
    "s_nop 0\n"
    "v_and_b32 v22, 0xff, v22\n"
    "v_and_b32 v18, 0xff, v18\n"
    "v_add_u32 v5, v5, v22\n"
    "v_add_u32 v4, v4, v18\n"
    "v_cmp_lt_i32 vcc, v5, v4\n"
    "v_mov_b32_e64 v76, v5\n"
    "s_nop 0\n"
    "v_lshrrev_b64 v[20:21], v6, vcc\n"
    "v_lshrrev_b64 v[18:19], v244, vcc\n"
    "v_and_b32 v20, 0xff, v20\n"
    "v_and_b32 v18, 0xff, v18\n"
    "v_bcnt_u32_b32 v20, v20, 0\n"
    "v_bcnt_u32_b32 v18, v18, 0\n"
    "v_lshl_add_u32 v20, v20, 3, s65\n"
    "v_lshlrev_b32_e64 v18, 3, v18\n"
    "ds_read_b32 v62, v20 offset:256\n"
    "v_lshrrev_b64 v[22:23], v20, v[46:47]\n"
    "v_lshrrev_b64 v[18:19], v18, v[46:47]\n"
    "v_and_b32 v22, 0xff, v22\n"
    "v_and_b32 v18, 0xff, v18\n"
    "v_add_u32 v5, v5, v22\n"
    "v_add_u32 v4, v4, v18\n"
    "v_cmp_lt_i32 vcc, v5, v4\n"
    "v_mov_b32_e64 v77, v5\n"
    "s_nop 0\n"
    "v_lshrrev_b64 v[20:21], v6, vcc\n"
    "v_lshrrev_b64 v[18:19], v244, vcc\n"
    "v_and_b32 v20, 0xff, v20\n"
    "v_and_b32 v18, 0xff, v18\n"
    "v_bcnt_u32_b32 v20, v20, 0\n"
    "v_bcnt_u32_b32 v18, v18, 0\n"
    "v_lshl_add_u32 v20, v20, 3, s65\n"
    "v_lshlrev_b32_e64 v18, 3, v18\n"
    "ds_read_b32 v64, v20 offset:320\n"
    "v_lshrrev_b64 v[22:23], v20, v[48:49]\n"
    "v_lshrrev_b64 v[18:19], v18, v[48:49]\n"
    "v_and_b32 v22, 0xff, v22\n"
    "v_and_b32 v18, 0xff, v18\n"
    "v_add_u32 v5, v5, v22\n"
    "v_add_u32 v4, v4, v18\n"
    "v_cmp_lt_i32 vcc, v5, v4\n"
    "v_mov_b32_e64 v78, v5\n"
    "s_nop 0\n"
    "v_lshrrev_b64 v[20:21], v6, vcc\n"
    "v_and_b32 v20, 0xff, v20\n"
    "v_bcnt_u32_b32 v20, v20, 0\n"
    "v_lshl_add_u32 v20, v20, 3, s65\n"
    "ds_read_b32 v66, v20 offset:384\n"
    "v_lshrrev_b64 v[22:23], v20, v[50:51]\n"
    "v_and_b32 v22, 0xff, v22\n"
    "v_add_u32 v5, v5, v22\n"
    "s_add_u32 s85, s76, 316\n"
    "s_lshl_b32 s86, s18, 4\n"
    "v_add_u32_dpp v12, v5, v5 quad_perm:[1,0,3,2] row_mask:0xf bank_mask:0xf\n"
    "s_add_u32 s85, s85, s86\n"
    "s_nop 0\n"
    "v_add_u32_dpp v12, v12, v12 quad_perm:[2,3,0,1] row_mask:0xf bank_mask:0xf\n"
    "s_nop 1\n"
    "v_add_u32_dpp v12, v12, v12 row_half_mirror row_mask:0xf bank_mask:0xf\n"
    "v_lshrrev_b32 v12, 3, v12\n"
    "v_sub_u32 v12, v12, v5\n"
    "v_add_u32 v72, v72, v12\n"
    "v_mul_u32_u24 v72, s18, v72\n"
    "v_lshrrev_b32 v72, 3, v72\n"
    "s_add_u32 s85, s85, s19\n"
    "v_add_u32 v57, s85, v72\n"
    "v_add_u32 v73, v73, v12\n"
    "v_mul_u32_u24 v73, s18, v73\n"
    "v_lshrrev_b32 v73, 3, v73\n"
    "s_add_u32 s85, s85, s19\n"
    "v_add_u32 v59, s85, v73\n"
    "v_add_u32 v74, v74, v12\n"
    "v_mul_u32_u24 v74, s18, v74\n"
    "v_lshrrev_b32 v74, 3, v74\n"
    "s_add_u32 s85, s85, s19\n"
    "v_add_u32 v61, s85, v74\n"
    "v_add_u32 v75, v75, v12\n"
    "v_mul_u32_u24 v75, s18, v75\n"
    "v_lshrrev_b32 v75, 3, v75\n"
    "s_add_u32 s85, s85, s19\n"
    "v_add_u32 v63, s85, v75\n"
    "v_add_u32 v76, v76, v12\n"
    "v_mul_u32_u24 v76, s18, v76\n"
    "v_lshrrev_b32 v76, 3, v76\n"
    "s_add_u32 s85, s85, s19\n"
    "v_add_u32 v65, s85, v76\n"
    "v_add_u32 v77, v77, v12\n"
    "v_mul_u32_u24 v77, s18, v77\n"
    "v_lshrrev_b32 v77, 3, v77\n"
    "s_add_u32 s85, s85, s19\n"
    "v_add_u32 v67, s85, v77\n"
    "v_lshl_add_u32 v12, v3, 7, s84\n"
    "s_mov_b64 exec, 0xff\n"
    "s_waitcnt lgkmcnt(0)\n"
    "ds_write_b128 v12, v[56:59] offset:48\n"
    "ds_write_b128 v12, v[60:63] offset:64\n"
    "ds_write_b128 v12, v[64:67] offset:80\n"
    "s_mov_b64 exec, -1\n"
    "s_branch .Ldin_ygate\n"
    ".Ldin_odd:\n"
    "s_cmp_lt_i32 s58, s57\n"
    "s_cbranch_scc1 .Ldin_A0s\n"
    "s_waitcnt vmcnt(0)\n"
    "s_branch .Ldin_exit\n"
    ".p2align 5\n"
    ".Ldin_L2:\n"
    "s_setprio 0\n"
    "s_cmp_ge_i32 s59, s57\n"
    "s_cbranch_scc1 .Ldin_E0\n"
    "s_add_u32 s76, s76, s73\n"
    "v_mov_b32 v253, s63\n"
    "v_and_b32 v184, v254, v184\n"
    "v_and_b32 v185, v255, v185\n"
    "v_and_b32 v186, v254, v186\n"
    "v_and_b32 v187, v255, v187\n"
    "v_and_b32 v188, v254, v188\n"
    "v_and_b32 v189, v255, v189\n"
    "v_and_b32 v190, v254, v190\n"
    "v_and_b32_e64 v191, v255, v191\n"
    "ds_read_b64 v[232:233], v253\n"
    "v_mfma_f32_4x4x4_16b_bf16 v[144:147], v[184:185], v[32:33], v[64:67]\n"
    "ds_read_b128 v[112:115], v252 offset:17408\n"
    "v_mfma_f32_4x4x4_16b_bf16 v[148:151], v[186:187], v[34:35], v[68:71]\n"
    "ds_read_b128 v[116:119], v252 offset:17424\n"
    "v_mfma_f32_4x4x4_16b_bf16 v[152:155], v[188:189], v[36:37], v[72:75]\n"
    "ds_read_b128 v[120:123], v252 offset:17536\n"
    "ds_read_b128 v[124:127], v252 offset:17552\n"
    "v_cvt_pk_bf16_f32 v0, v144, v145\n"
    "v_cvt_pk_bf16_f32 v1, v146, v147\n"
    "v_mfma_f32_4x4x4_16b_bf16 v[156:159], v[190:191], v[38:39], v[76:79]\n"
    "v_cvt_pk_bf16_f32 v2, v148, v149\n"
    "v_cvt_pk_bf16_f32 v3, v150, v151\n"
    "v_mfma_f32_4x4x4_16b_bf16 v[144:147], v[184:185], v[40:41], v[80:83]\n"
    "v_cvt_pk_bf16_f32 v4, v152, v153\n"
    "v_cvt_pk_bf16_f32 v5, v154, v155\n"
    "v_mfma_f32_4x4x4_16b_bf16 v[148:151], v[186:187], v[42:43], v[84:87]\n"
    "v_cvt_pk_bf16_f32 v6, v156, v157\n"
    "v_cvt_pk_bf16_f32 v7, v158, v159\n"
    "v_mfma_f32_4x4x4_16b_bf16 v[152:155], v[188:189], v[44:45], v[88:91]\n"
    "v_cvt_pk_bf16_f32 v8, v144, v145\n"
    "v_cvt_pk_bf16_f32 v9, v146, v147\n"
    "v_mfma_f32_4x4x4_16b_bf16 v[156:159], v[190:191], v[46:47], v[92:95]\n"
    "v_cvt_pk_bf16_f32 v10, v148, v149\n"
    "v_cvt_pk_bf16_f32 v11, v150, v151\n"
    "v_mfma_f32_4x4x4_16b_bf16 v[144:147], v[184:185], v[48:49], v[192:195]\n"
    "v_cvt_pk_bf16_f32 v12, v152, v153\n"
    "v_cvt_pk_bf16_f32 v13, v154, v155\n"
    "v_mfma_f32_4x4x4_16b_bf16 v[148:151], v[186:187], v[50:51], v[196:199]\n"
    "v_cvt_pk_bf16_f32 v14, v156, v157\n"
    "v_cvt_pk_bf16_f32 v15, v158, v159\n"
    "v_mfma_f32_4x4x4_16b_bf16 v[152:155], v[188:189], v[52:53], v[200:203]\n"
    "s_waitcnt lgkmcnt(0)\n"
    "v_readfirstlane_b32 s60, v232\n"
    "v_cvt_pk_bf16_f32 v16, v144, v145\n"
    "v_cvt_pk_bf16_f32 v17, v146, v147\n"
    "v_mfma_f32_4x4x4_16b_bf16 v[156:159], v[190:191], v[54:55], v[204:207]\n"
    "s_cmp_eq_u32 s60, -1\n"
    "s_cbranch_scc1 .Ldin_L2_poll\n"
    ".Ldin_L2_pr:\n"
    "v_readfirstlane_b32 s95, v233\n"
    "s_and_b32 s94, s60, 63\n"
    "s_lshl_b32 s94, s94, 6\n"
    "s_add_u32 s94, s94, 68480\n"
    "s_add_u32 s63, s63, 8\n"
    "v_lshl_add_u32 v252, v249, 1, s94\n"
    "ds_read_b32 v252, v252\n"
    "s_sub_u32 s95, s95, s76\n"
    "s_ashr_i32 s95, s95, 4\n"
    "s_min_i32 s95, s95, s74\n"
    "s_max_i32 s72, s95, s75\n"
    "v_cvt_pk_bf16_f32 v18, v148, v149\n"
    "v_cvt_pk_bf16_f32 v19, v150, v151\n"
    "v_mfma_f32_4x4x4_16b_bf16 v[144:147], v[184:185], v[56:57], v[208:211]\n"
    "v_cvt_pk_bf16_f32 v20, v152, v153\n"
    "v_cvt_pk_bf16_f32 v21, v154, v155\n"
    "v_mfma_f32_4x4x4_16b_bf16 v[148:151], v[186:187], v[58:59], v[212:215]\n"
    "v_cvt_pk_bf16_f32 v22, v156, v157\n"
    "v_cvt_pk_bf16_f32 v23, v158, v159\n"
    "v_mfma_f32_4x4x4_16b_bf16 v[152:155], v[188:189], v[60:61], v[216:219]\n"
    "v_cvt_pk_bf16_f32 v24, v144, v145\n"
    "v_cvt_pk_bf16_f32 v25, v146, v147\n"
    "v_mfma_f32_4x4x4_16b_bf16 v[156:159], v[190:191], v[62:63], v[220:223]\n"
    "v_cvt_pk_bf16_f32 v26, v148, v149\n"
    "v_cvt_pk_bf16_f32 v27, v150, v151\n"
    "v_cvt_pk_bf16_f32 v28, v152, v153\n"
    "v_cvt_pk_bf16_f32 v29, v154, v155\n"
    "v_exp_f32 v136, v136 clamp\n"
    "v_cvt_pk_bf16_f32 v30, v156, v157\n"
    "v_cvt_pk_bf16_f32 v31, v158, v159\n"
    "v_mfma_f32_16x16x32_bf16 v[144:147], v[0:3], v[160:163], v[112:115]\n"
    "v_exp_f32 v128, v128 clamp\n"
    "v_mfma_f32_16x16x32_bf16 v[144:147], v[4:7], v[164:167], v[144:147]\n"
    "v_exp_f32 v129, v129 clamp\n"
    "v_mfma_f32_16x16x32_bf16 v[148:151], v[8:11], v[160:163], v[116:119]\n"
    "v_exp_f32 v130, v130 clamp\n"
    "v_mfma_f32_16x16x32_bf16 v[148:151], v[12:15], v[164:167], v[148:151]\n"
    "v_exp_f32 v131, v131 clamp\n"
    "v_mfma_f32_16x16x32_bf16 v[152:155], v[16:19], v[160:163], v[120:123]\n"
    "v_exp_f32 v132, v132 clamp\n"
    "v_mfma_f32_16x16x32_bf16 v[152:155], v[20:23], v[164:167], v[152:155]\n"
    "v_exp_f32 v133, v133 clamp\n"
    "v_mfma_f32_16x16x32_bf16 v[156:159], v[24:27], v[160:163], v[124:127]\n"
    "v_exp_f32 v134, v134 clamp\n"
    "v_mfma_f32_16x16x32_bf16 v[156:159], v[28:31], v[164:167], v[156:159]\n"
    "v_exp_f32 v135, v135 clamp\n"
    "s_add_u32 m0, s68, 6144\n"
    "v_exp_f32 v137, v137 clamp\n"
    "v_add_u32 v253, s67, v244\n"
    "v_mfma_f32_4x4x4_16b_bf16 v[224:227], v[228:229], v[176:177], v[224:227] cbsz:4 abid:0\n"
    "v_exp_f32 v138, v138 clamp\n"
    "v_exp_f32 v139, v139 clamp\n"
    "v_mfma_f32_4x4x4_16b_bf16 v[224:227], v[228:229], v[178:179], v[224:227] cbsz:4 abid:1\n"
    "v_exp_f32 v140, v140 clamp\n"
    "v_exp_f32 v141, v141 clamp\n"
    "v_mfma_f32_4x4x4_16b_bf16 v[224:227], v[228:229], v[180:181], v[224:227] cbsz:4 abid:2\n"
    "v_exp_f32 v142, v142 clamp\n"
    "v_exp_f32 v143, v143 clamp\n"
    "v_mfma_f32_4x4x4_16b_bf16 v[224:227], v[228:229], v[182:183], v[224:227] cbsz:4 abid:3\n"
    "s_waitcnt vmcnt(4)\n"
    "ds_read_b64_tr_b16 v[176:177], v240 offset:6144\n"
    "ds_read_b64_tr_b16 v[178:179], v241 offset:6400\n"
    "ds_read_b64_tr_b16 v[180:181], v242 offset:6656\n"
    "ds_read_b64_tr_b16 v[182:183], v243 offset:6912\n"
    "ds_read_b32 v236, v253 offset:0\n"
    "ds_read_b32 v238, v253 offset:4\n"
    "ds_read_b128 v[160:163], v234 offset:0\n"
    "ds_read_b128 v[164:167], v235 offset:0\n"
    "s_waitcnt lgkmcnt(8)\n"
    "s_nop 0\n"
    "v_readlane_b32 s20, v252, 0\n"
    "v_readlane_b32 s21, v252, 1\n"
    "v_readlane_b32 s22, v252, 2\n"
    "v_readlane_b32 s23, v252, 3\n"
    "v_readlane_b32 s24, v252, 4\n"
    "v_readlane_b32 s25, v252, 5\n"
    "v_readlane_b32 s26, v252, 6\n"
    "v_readlane_b32 s27, v252, 7\n"
    "v_pk_fma_f32 v[128:129], v[128:129], s[70:71], v[96:97]\n"
    "v_pk_fma_f32 v[130:131], v[130:131], s[70:71], v[98:99]\n"
    "v_pk_add_f32 v[230:231], v[128:129], v[130:131]\n"
    "v_pk_mul_f32 v[232:233], v[128:129], v[130:131]\n"
    "v_pk_fma_f32 v[132:133], v[132:133], s[70:71], v[100:101]\n"
    "v_pk_fma_f32 v[230:231], v[230:231], v[132:133], v[232:233]\n"
    "v_pk_mul_f32 v[232:233], v[232:233], v[132:133]\n"
    "v_pk_fma_f32 v[134:135], v[134:135], s[70:71], v[102:103]\n"
    "v_pk_fma_f32 v[230:231], v[230:231], v[134:135], v[232:233]\n"
    "v_pk_mul_f32 v[232:233], v[232:233], v[134:135]\n"
    "v_pk_fma_f32 v[136:137], v[136:137], s[70:71], v[104:105]\n"
    "v_pk_fma_f32 v[230:231], v[230:231], v[136:137], v[232:233]\n"
    "v_pk_mul_f32 v[232:233], v[232:233], v[136:137]\n"
    "v_pk_fma_f32 v[138:139], v[138:139], s[70:71], v[106:107]\n"
    "v_pk_fma_f32 v[230:231], v[230:231], v[138:139], v[232:233]\n"
    "v_pk_mul_f32 v[232:233], v[232:233], v[138:139]\n"
    "v_pk_fma_f32 v[140:141], v[140:141], s[70:71], v[108:109]\n"
    "v_pk_fma_f32 v[230:231], v[230:231], v[140:141], v[232:233]\n"
    "v_pk_mul_f32 v[232:233], v[232:233], v[140:141]\n"
    "v_pk_fma_f32 v[142:143], v[142:143], s[70:71], v[110:111]\n"
    "v_pk_fma_f32 v[230:231], v[230:231], v[142:143], v[232:233]\n"
    "v_pk_mul_f32 v[232:233], v[232:233], v[142:143]\n"
    "v_rcp_f32 v232, v232\n"
    "v_rcp_f32 v233, v233\n"
    "v_fma_f32 v230, v230, v232, v247\n"
    "v_fmac_f32 v230, v231, v233\n"
    "v_cvt_pk_bf16_f32 v230, v230, v230\n"
    "s_add_u32 s67, s67, 64\n"
    "s_waitcnt lgkmcnt(0)\n"
    "ds_write_b16 v245, v230\n"
    "ds_read_b64 v[228:229], v246\n"
    "s_lshl1_add_u32 s76, s72, s76\n"
    "buffer_load_dwordx4 v[236:237], s[40:43], 0 idxen offen offset:0 nt lds\n"
    "buffer_load_dwordx4 v[238:239], s[40:43], 0 idxen offen offset:1024 nt lds\n"
    "s_and_b32 s62, s58, 63\n"
    "s_mul_i32 s62, s62, 272\n"
    "s_mov_b32 s58, s59\n"
    "s_mov_b32 s59, s60\n"
    "s_mov_b32 s81, s65\n"
    "s_xor_b32 s65, s65, s64\n"
    "s_mov_b32 s80, s83\n"
    "s_add_u32 s82, s65, 256\n"
    "s_cmp_lt_i32 s59, s57\n"
    "s_cselect_b32 s76, s76, -1\n"
    "s_bitcmp1_b32 s58, 30\n"
    "s_cbranch_scc1 .Ldin_A3s\n"
    ".Ldin_A3:\n"
    "v_mfma_f32_16x16x32_bf16 v[128:131], v[0:3], v[160:163], v[112:115]\n"
    "v_exp_f32 v144, v144 clamp\n"
    "v_mfma_f32_16x16x32_bf16 v[128:131], v[4:7], v[164:167], v[128:131]\n"
    "v_exp_f32 v145, v145 clamp\n"
    "v_mfma_f32_16x16x32_bf16 v[132:135], v[8:11], v[160:163], v[116:119]\n"
    "v_exp_f32 v146, v146 clamp\n"
    "v_mfma_f32_16x16x32_bf16 v[132:135], v[12:15], v[164:167], v[132:135]\n"
    "v_exp_f32 v147, v147 clamp\n"
    "v_mfma_f32_16x16x32_bf16 v[136:139], v[16:19], v[160:163], v[120:123]\n"
    "v_exp_f32 v148, v148 clamp\n"
    "v_mfma_f32_16x16x32_bf16 v[136:139], v[20:23], v[164:167], v[136:139]\n"
    "v_exp_f32 v149, v149 clamp\n"
    "v_mfma_f32_16x16x32_bf16 v[140:143], v[24:27], v[160:163], v[124:127]\n"
    "v_exp_f32 v150, v150 clamp\n"
    "v_mfma_f32_16x16x32_bf16 v[140:143], v[28:31], v[164:167], v[140:143]\n"
    "v_exp_f32 v151, v151 clamp\n"
    "v_exp_f32 v152, v152 clamp\n"
    "s_add_u32 m0, s68, 0\n"
    "v_exp_f32 v153, v153 clamp\n"
    "v_add_u32 v253, s67, v244\n"
    "s_waitcnt lgkmcnt(0)\n"
    "v_mfma_f32_4x4x4_16b_bf16 v[224:227], v[228:229], v[168:169], v[224:227] cbsz:4 abid:0\n"
    "v_exp_f32 v154, v154 clamp\n"
    "v_exp_f32 v155, v155 clamp\n"
    "v_mfma_f32_4x4x4_16b_bf16 v[224:227], v[228:229], v[170:171], v[224:227] cbsz:4 abid:1\n"
    "v_exp_f32 v156, v156 clamp\n"
    "v_exp_f32 v157, v157 clamp\n"
    "v_mfma_f32_4x4x4_16b_bf16 v[224:227], v[228:229], v[172:173], v[224:227] cbsz:4 abid:2\n"
    "v_exp_f32 v158, v158 clamp\n"
    "v_exp_f32 v159, v159 clamp\n"
    "v_mfma_f32_4x4x4_16b_bf16 v[224:227], v[228:229], v[174:175], v[224:227] cbsz:4 abid:3\n"
    "s_waitcnt vmcnt(4)\n"
    "ds_read_b64_tr_b16 v[168:169], v240 offset:0\n"
    "ds_read_b64_tr_b16 v[170:171], v241 offset:256\n"
    "ds_read_b64_tr_b16 v[172:173], v242 offset:512\n"
    "ds_read_b64_tr_b16 v[174:175], v243 offset:768\n"
    "ds_read_b32 v236, v253 offset:0\n"
    "ds_read_b32 v238, v253 offset:4\n"
    "ds_read_b128 v[160:163], v234 offset:2048\n"
    "ds_read_b128 v[164:167], v235 offset:2048\n"
    "s_cbranch_vccz .Ldin_A3_np\n"
    "s_setprio 1\n"
    ".Ldin_A3_np:\n"
    "v_pk_fma_f32 v[144:145], v[144:145], s[70:71], v[96:97]\n"
    "v_pk_fma_f32 v[146:147], v[146:147], s[70:71], v[98:99]\n"
    "v_add_f32 v191, v224, v225\n"
    "v_add_f32 v190, v226, v227\n"
    "v_add_f32 v191, v191, v190\n"
    "v_add_u32 v190, s62, v249\n"
    "v_mul_f32 v191, s69, v191\n"
    "v_cvt_pk_bf16_f32 v191, v191, v191\n"
    "ds_write_b16 v190, v191 offset:32768\n"
    "s_bfe_u32 s94, s59, 0x180006\n"
    "s_mul_i32 s86, s94, s56\n"
    "v_mbcnt_lo_u32_b32 v184, s20, 0\n"
    "v_mbcnt_lo_u32_b32 v185, s22, 0\n"
    "v_mbcnt_lo_u32_b32 v186, s24, 0\n"
    "v_mbcnt_lo_u32_b32 v187, s26, 0\n"
    "s_bcnt1_i32_b64 s90, s[20:21]\n"
    "s_bcnt1_i32_b64 s91, s[22:23]\n"
    "s_bcnt1_i32_b64 s92, s[24:25]\n"
    "s_bcnt1_i32_b64 s93, s[26:27]\n"
    "v_mbcnt_hi_u32_b32 v184, s21, v184\n"
    "v_mbcnt_hi_u32_b32 v185, s23, v185\n"
    "v_mbcnt_hi_u32_b32 v186, s25, v186\n"
    "v_mbcnt_hi_u32_b32 v187, s27, v187\n"
    "s_lshl2_add_u32 s90, s90, s65\n"
    "s_lshl2_add_u32 s91, s91, s90\n"
    "s_lshl2_add_u32 s92, s92, s91\n"
    "s_lshl2_add_u32 s93, s93, s92\n"
    "v_add_u32_e64 v188, s86, v251\n"
    "v_pk_add_f32 v[230:231], v[144:145], v[146:147]\n"
    "v_pk_mul_f32 v[232:233], v[144:145], v[146:147]\n"
    "v_pk_fma_f32 v[148:149], v[148:149], s[70:71], v[100:101]\n"
    "v_pk_fma_f32 v[230:231], v[230:231], v[148:149], v[232:233]\n"
    "v_pk_mul_f32 v[232:233], v[232:233], v[148:149]\n"
    "v_lshl_add_u32 v184, v184, 2, s65\n"
    "v_lshl_add_u32 v185, v185, 2, s90\n"
    "v_lshl_add_u32 v186, v186, 2, s91\n"
    "v_lshl_add_u32 v187, v187, 2, s92\n"
    "v_mov_b32 v191, -1\n"
    "v_lshl_add_u32 v190, v249, 1, s93\n"
    "ds_write2_b32 v190, v191, v191 offset1:64\n"
    "s_mov_b64 exec, s[20:21]\n"
    "ds_write_b32 v184, v188\n"
    "s_mov_b64 exec, s[22:23]\n"
    "v_add_u32 v189, 64, v188\n"
    "ds_write_b32 v185, v189\n"
    "s_mov_b64 exec, s[24:25]\n"
    "v_add_u32 v190, 128, v188\n"
    "ds_write_b32 v186, v190\n"
    "s_mov_b64 exec, s[26:27]\n"
    "v_add_u32 v189, 192, v188\n"
    "ds_write_b32 v187, v189\n"
    "s_mov_b64 exec, -1\n"
    "s_add_u32 s95, s93, 60\n"
    "s_add_u32 s94, s82, 128\n"
    "s_andn2_b32 s95, s95, 63\n"
    "s_max_u32 s83, s95, s94\n"
    "v_pk_fma_f32 v[150:151], v[150:151], s[70:71], v[102:103]\n"
    "v_pk_fma_f32 v[230:231], v[230:231], v[150:151], v[232:233]\n"
    "v_pk_mul_f32 v[232:233], v[232:233], v[150:151]\n"
    "v_pk_fma_f32 v[152:153], v[152:153], s[70:71], v[104:105]\n"
    "v_pk_fma_f32 v[230:231], v[230:231], v[152:153], v[232:233]\n"
    "v_pk_mul_f32 v[232:233], v[232:233], v[152:153]\n"
    "v_pk_fma_f32 v[154:155], v[154:155], s[70:71], v[106:107]\n"
    "v_pk_fma_f32 v[230:231], v[230:231], v[154:155], v[232:233]\n"
    "v_pk_mul_f32 v[232:233], v[232:233], v[154:155]\n"
    "v_pk_fma_f32 v[156:157], v[156:157], s[70:71], v[108:109]\n"
    "v_pk_fma_f32 v[230:231], v[230:231], v[156:157], v[232:233]\n"
    "v_pk_mul_f32 v[232:233], v[232:233], v[156:157]\n"
    "v_pk_fma_f32 v[158:159], v[158:159], s[70:71], v[110:111]\n"
    "v_pk_fma_f32 v[230:231], v[230:231], v[158:159], v[232:233]\n"
    "v_pk_mul_f32 v[232:233], v[232:233], v[158:159]\n"
    "v_rcp_f32 v232, v232\n"
    "v_rcp_f32 v233, v233\n"
    "v_fma_f32 v230, v230, v232, v247\n"
    "v_fmac_f32 v230, v231, v233\n"
    "v_cvt_pk_bf16_f32 v230, v230, v230\n"
    "s_add_u32 s67, s67, 64\n"
    "s_waitcnt lgkmcnt(0)\n"
    "ds_write_b16 v245, v230\n"
    "ds_read_b64 v[228:229], v246\n"
    "buffer_load_dwordx4 v[236:237], s[40:43], 0 idxen offen offset:0 nt lds\n"
    "buffer_load_dwordx4 v[238:239], s[40:43], 0 idxen offen offset:1024 nt lds\n"
    ".Ldin_B0:\n"
    "v_mfma_f32_16x16x32_bf16 v[144:147], v[0:3], v[160:163], v[112:115]\n"
    "v_exp_f32 v128, v128 clamp\n"
    "v_mfma_f32_16x16x32_bf16 v[144:147], v[4:7], v[164:167], v[144:147]\n"
    "v_exp_f32 v129, v129 clamp\n"
    "v_mfma_f32_16x16x32_bf16 v[148:151], v[8:11], v[160:163], v[116:119]\n"
    "v_exp_f32 v130, v130 clamp\n"
    "v_mfma_f32_16x16x32_bf16 v[148:151], v[12:15], v[164:167], v[148:151]\n"
    "v_exp_f32 v131, v131 clamp\n"
    "v_mfma_f32_16x16x32_bf16 v[152:155], v[16:19], v[160:163], v[120:123]\n"
    "v_exp_f32 v132, v132 clamp\n"
    "v_mfma_f32_16x16x32_bf16 v[152:155], v[20:23], v[164:167], v[152:155]\n"
    "v_exp_f32 v133, v133 clamp\n"
    "v_mfma_f32_16x16x32_bf16 v[156:159], v[24:27], v[160:163], v[124:127]\n"
    "v_exp_f32 v134, v134 clamp\n"
    "v_mfma_f32_16x16x32_bf16 v[156:159], v[28:31], v[164:167], v[156:159]\n"
    "v_exp_f32 v135, v135 clamp\n"
    "v_exp_f32 v136, v136 clamp\n"
    "s_cmp_eq_u32 s67, s80\n"
    "s_cselect_b32 s67, s65, s67\n"
    "s_and_b32 s94, s59, 63\n"
    "s_mul_i32 s94, s94, 272\n"
    "v_add_u32 v252, s94, v250\n"
    "s_add_u32 m0, s68, 2048\n"
    "v_exp_f32 v137, v137 clamp\n"
    "v_add_u32 v253, s67, v244\n"
    "s_waitcnt lgkmcnt(0)\n"
    "v_mfma_f32_4x4x4_16b_bf16 v[224:227], v[228:229], v[176:177], 0 cbsz:4 abid:0\n"
    "v_exp_f32 v138, v138 clamp\n"
    "v_exp_f32 v139, v139 clamp\n"
    "v_mfma_f32_4x4x4_16b_bf16 v[224:227], v[228:229], v[178:179], v[224:227] cbsz:4 abid:1\n"
    "v_exp_f32 v140, v140 clamp\n"
    "v_exp_f32 v141, v141 clamp\n"
    "v_mfma_f32_4x4x4_16b_bf16 v[224:227], v[228:229], v[180:181], v[224:227] cbsz:4 abid:2\n"
    "v_exp_f32 v142, v142 clamp\n"
    "v_exp_f32 v143, v143 clamp\n"
    "v_mfma_f32_4x4x4_16b_bf16 v[224:227], v[228:229], v[182:183], v[224:227] cbsz:4 abid:3\n"
    "s_waitcnt vmcnt(4)\n"
    "s_memrealtime s[34:35]\n"
    "ds_read_b64_tr_b16 v[176:177], v240 offset:2048\n"
    "ds_read_b64_tr_b16 v[178:179], v241 offset:2304\n"
    "ds_read_b64_tr_b16 v[180:181], v242 offset:2560\n"
    "ds_read_b64_tr_b16 v[182:183], v243 offset:2816\n"
    "ds_read_b32 v236, v253 offset:0\n"
    "ds_read_b32 v238, v253 offset:4\n"
    "ds_read_b128 v[160:163], v234 offset:4096\n"
    "ds_read_b128 v[164:167], v235 offset:4096\n"
    "ds_read_b128 v[184:187], v252 offset:0\n"
    "ds_read_b128 v[188:191], v252 offset:16\n"
    "v_pk_fma_f32 v[128:129], v[128:129], s[70:71], v[96:97]\n"
    "v_pk_fma_f32 v[130:131], v[130:131], s[70:71], v[98:99]\n"
    "v_pk_add_f32 v[230:231], v[128:129], v[130:131]\n"
    "v_pk_mul_f32 v[232:233], v[128:129], v[130:131]\n"
    "v_pk_fma_f32 v[132:133], v[132:133], s[70:71], v[100:101]\n"
    "v_pk_fma_f32 v[230:231], v[230:231], v[132:133], v[232:233]\n"
    "v_pk_mul_f32 v[232:233], v[232:233], v[132:133]\n"
    "v_pk_fma_f32 v[134:135], v[134:135], s[70:71], v[102:103]\n"
    "v_pk_fma_f32 v[230:231], v[230:231], v[134:135], v[232:233]\n"
    "v_pk_mul_f32 v[232:233], v[232:233], v[134:135]\n"
    "v_pk_fma_f32 v[136:137], v[136:137], s[70:71], v[104:105]\n"
    "v_pk_fma_f32 v[230:231], v[230:231], v[136:137], v[232:233]\n"
    "v_pk_mul_f32 v[232:233], v[232:233], v[136:137]\n"
    "v_pk_fma_f32 v[138:139], v[138:139], s[70:71], v[106:107]\n"
    "v_pk_fma_f32 v[230:231], v[230:231], v[138:139], v[232:233]\n"
    "v_pk_mul_f32 v[232:233], v[232:233], v[138:139]\n"
    "v_pk_fma_f32 v[140:141], v[140:141], s[70:71], v[108:109]\n"
    "v_pk_fma_f32 v[230:231], v[230:231], v[140:141], v[232:233]\n"
    "v_pk_mul_f32 v[232:233], v[232:233], v[140:141]\n"
    "v_pk_fma_f32 v[142:143], v[142:143], s[70:71], v[110:111]\n"
    "v_pk_fma_f32 v[230:231], v[230:231], v[142:143], v[232:233]\n"
    "v_pk_mul_f32 v[232:233], v[232:233], v[142:143]\n"
    "v_rcp_f32 v232, v232\n"
    "v_rcp_f32 v233, v233\n"
    "v_fma_f32 v230, v230, v232, v247\n"
    "v_fmac_f32 v230, v231, v233\n"
    "v_cvt_pk_bf16_f32 v230, v230, v230\n"
    "s_add_u32 s67, s67, 64\n"
    "s_waitcnt lgkmcnt(0)\n"
    "ds_write_b16 v245, v230\n"
    "ds_read_b64 v[228:229], v246\n"
    "s_cmp_le_u32 s34, s76\n"
    "s_cbranch_scc1 .Ldin_B0_wait\n"
    ".Ldin_B0_go:\n"
    "s_sub_u32 s94, s34, 60\n"
    "s_max_u32 s76, s76, s94\n"
    "s_add_u32 s76, s76, s72\n"
    ".Ldin_B0_dma:\n"
    "buffer_load_dwordx4 v[236:237], s[40:43], 0 idxen offen offset:0 nt lds\n"
    "buffer_load_dwordx4 v[238:239], s[40:43], 0 idxen offen offset:1024 nt lds\n"
    ".Ldin_B0_nd:\n"
    "s_branch .Ldin_G1\n"
    ".Ldin_L2_poll:\n"
    "s_sleep 1\n"
    "ds_read_b64 v[232:233], v253\n"
    "s_waitcnt lgkmcnt(0)\n"
    "v_readfirstlane_b32 s60, v232\n"
    "s_cmp_eq_u32 s60, -1\n"
    "s_cbranch_scc1 .Ldin_L2_poll\n"
    "s_branch .Ldin_L2_pr\n"
    ".Ldin_B0_wait:\n"
    "s_cmp_lt_i32 s59, s57\n"
    "s_cbranch_scc1 .Ldin_B0_w2\n"
    "s_sub_u32 s94, s67, s65\n"
    "s_cmp_gt_u32 s94, 256\n"
    "s_cbranch_scc1 .Ldin_B0_dma\n"
    "s_cmp_eq_u32 s94, 64\n"
    "s_waitcnt vmcnt(2)\n"
    "s_cbranch_scc1 .Ldin_B0_nd\n"
    "s_waitcnt vmcnt(0)\n"
    "s_branch .Ldin_B0_nd\n"
    ".Ldin_B0_slp:\n"
    "s_sleep 1\n"
    ".Ldin_B0_w2:\n"
    "s_memrealtime s[34:35]\n"
    "s_waitcnt lgkmcnt(0)\n"
    "s_sub_u32 s94, s34, s76\n"
    "s_cmp_lt_i32 s94, 0\n"
    "s_cbranch_scc1 .Ldin_B0_slp\n"
    "s_branch .Ldin_B0_go\n"
    ".p2align 5\n"
    ".Ldin_L1:\n"
    "s_setprio 0\n"
    "s_cmp_ge_i32 s59, s57\n"
    "s_cbranch_scc1 .Ldin_E1\n"
    "s_add_u32 s76, s76, s73\n"
    "v_mov_b32 v253, s63\n"
    "v_and_b32 v184, v254, v184\n"
    "v_and_b32 v185, v255, v185\n"
    "v_and_b32 v186, v254, v186\n"
    "v_and_b32 v187, v255, v187\n"
    "v_and_b32 v188, v254, v188\n"
    "v_and_b32 v189, v255, v189\n"
    "v_and_b32 v190, v254, v190\n"
    "v_and_b32_e64 v191, v255, v191\n"
    "ds_read_b64 v[232:233], v253\n"
    "v_mfma_f32_4x4x4_16b_bf16 v[128:131], v[184:185], v[32:33], v[64:67]\n"
    "ds_read_b128 v[112:115], v252 offset:17408\n"
    "v_mfma_f32_4x4x4_16b_bf16 v[132:135], v[186:187], v[34:35], v[68:71]\n"
    "ds_read_b128 v[116:119], v252 offset:17424\n"
    "v_mfma_f32_4x4x4_16b_bf16 v[136:139], v[188:189], v[36:37], v[72:75]\n"
    "ds_read_b128 v[120:123], v252 offset:17536\n"
    "ds_read_b128 v[124:127], v252 offset:17552\n"
    "v_cvt_pk_bf16_f32 v0, v128, v129\n"
    "v_cvt_pk_bf16_f32 v1, v130, v131\n"
    "v_mfma_f32_4x4x4_16b_bf16 v[140:143], v[190:191], v[38:39], v[76:79]\n"
    "v_cvt_pk_bf16_f32 v2, v132, v133\n"
    "v_cvt_pk_bf16_f32 v3, v134, v135\n"
    "v_mfma_f32_4x4x4_16b_bf16 v[128:131], v[184:185], v[40:41], v[80:83]\n"
    "v_cvt_pk_bf16_f32 v4, v136, v137\n"
    "v_cvt_pk_bf16_f32 v5, v138, v139\n"
    "v_mfma_f32_4x4x4_16b_bf16 v[132:135], v[186:187], v[42:43], v[84:87]\n"
    "v_cvt_pk_bf16_f32 v6, v140, v141\n"
    "v_cvt_pk_bf16_f32 v7, v142, v143\n"
    "v_mfma_f32_4x4x4_16b_bf16 v[136:139], v[188:189], v[44:45], v[88:91]\n"
    "v_cvt_pk_bf16_f32 v8, v128, v129\n"
    "v_cvt_pk_bf16_f32 v9, v130, v131\n"
    "v_mfma_f32_4x4x4_16b_bf16 v[140:143], v[190:191], v[46:47], v[92:95]\n"
    "v_cvt_pk_bf16_f32 v10, v132, v133\n"
    "v_cvt_pk_bf16_f32 v11, v134, v135\n"
    "v_mfma_f32_4x4x4_16b_bf16 v[128:131], v[184:185], v[48:49], v[192:195]\n"
    "v_cvt_pk_bf16_f32 v12, v136, v137\n"
    "v_cvt_pk_bf16_f32 v13, v138, v139\n"
    "v_mfma_f32_4x4x4_16b_bf16 v[132:135], v[186:187], v[50:51], v[196:199]\n"
    "v_cvt_pk_bf16_f32 v14, v140, v141\n"
    "v_cvt_pk_bf16_f32 v15, v142, v143\n"
    "v_mfma_f32_4x4x4_16b_bf16 v[136:139], v[188:189], v[52:53], v[200:203]\n"
    "s_waitcnt lgkmcnt(0)\n"
    "v_readfirstlane_b32 s60, v232\n"
    "v_cvt_pk_bf16_f32 v16, v128, v129\n"
    "v_cvt_pk_bf16_f32 v17, v130, v131\n"
    "v_mfma_f32_4x4x4_16b_bf16 v[140:143], v[190:191], v[54:55], v[204:207]\n"
    "s_cmp_eq_u32 s60, -1\n"
    "s_cbranch_scc1 .Ldin_L1_poll\n"
    ".Ldin_L1_pr:\n"
    "v_readfirstlane_b32 s95, v233\n"
    "s_and_b32 s94, s60, 63\n"
    "s_lshl_b32 s94, s94, 6\n"
    "s_add_u32 s94, s94, 68480\n"
    "s_add_u32 s63, s63, 8\n"
    "v_lshl_add_u32 v252, v249, 1, s94\n"
    "ds_read_b32 v252, v252\n"
    "s_sub_u32 s95, s95, s76\n"
    "s_ashr_i32 s95, s95, 4\n"
    "s_min_i32 s95, s95, s74\n"
    "s_max_i32 s72, s95, s75\n"
    "v_cvt_pk_bf16_f32 v18, v132, v133\n"
    "v_cvt_pk_bf16_f32 v19, v134, v135\n"
    "v_mfma_f32_4x4x4_16b_bf16 v[128:131], v[184:185], v[56:57], v[208:211]\n"
    "v_cvt_pk_bf16_f32 v20, v136, v137\n"
    "v_cvt_pk_bf16_f32 v21, v138, v139\n"
    "v_mfma_f32_4x4x4_16b_bf16 v[132:135], v[186:187], v[58:59], v[212:215]\n"
    "v_cvt_pk_bf16_f32 v22, v140, v141\n"
    "v_cvt_pk_bf16_f32 v23, v142, v143\n"
    "v_mfma_f32_4x4x4_16b_bf16 v[136:139], v[188:189], v[60:61], v[216:219]\n"
    "v_cvt_pk_bf16_f32 v24, v128, v129\n"
    "v_cvt_pk_bf16_f32 v25, v130, v131\n"
    "v_mfma_f32_4x4x4_16b_bf16 v[140:143], v[190:191], v[62:63], v[220:223]\n"
    "v_cvt_pk_bf16_f32 v26, v132, v133\n"
    "v_cvt_pk_bf16_f32 v27, v134, v135\n"
    "v_cvt_pk_bf16_f32 v28, v136, v137\n"
    "v_cvt_pk_bf16_f32 v29, v138, v139\n"
    "v_exp_f32 v152, v152 clamp\n"
    "v_cvt_pk_bf16_f32 v30, v140, v141\n"
    "v_cvt_pk_bf16_f32 v31, v142, v143\n"
    "v_mfma_f32_16x16x32_bf16 v[128:131], v[0:3], v[160:163], v[112:115]\n"
    "v_exp_f32 v144, v144 clamp\n"
    "v_mfma_f32_16x16x32_bf16 v[128:131], v[4:7], v[164:167], v[128:131]\n"
    "v_exp_f32 v145, v145 clamp\n"
    "v_mfma_f32_16x16x32_bf16 v[132:135], v[8:11], v[160:163], v[116:119]\n"
    "v_exp_f32 v146, v146 clamp\n"
    "v_mfma_f32_16x16x32_bf16 v[132:135], v[12:15], v[164:167], v[132:135]\n"
    "v_exp_f32 v147, v147 clamp\n"
    "v_mfma_f32_16x16x32_bf16 v[136:139], v[16:19], v[160:163], v[120:123]\n"
    "v_exp_f32 v148, v148 clamp\n"
    "v_mfma_f32_16x16x32_bf16 v[136:139], v[20:23], v[164:167], v[136:139]\n"
    "v_exp_f32 v149, v149 clamp\n"
    "v_mfma_f32_16x16x32_bf16 v[140:143], v[24:27], v[160:163], v[124:127]\n"
    "v_exp_f32 v150, v150 clamp\n"
    "v_mfma_f32_16x16x32_bf16 v[140:143], v[28:31], v[164:167], v[140:143]\n"
    "v_exp_f32 v151, v151 clamp\n"
    "s_add_u32 m0, s68, 4096\n"
    "v_exp_f32 v153, v153 clamp\n"
    "v_add_u32 v253, s67, v244\n"
    "v_mfma_f32_4x4x4_16b_bf16 v[224:227], v[228:229], v[168:169], v[224:227] cbsz:4 abid:0\n"
    "v_exp_f32 v154, v154 clamp\n"
    "v_exp_f32 v155, v155 clamp\n"
    "v_mfma_f32_4x4x4_16b_bf16 v[224:227], v[228:229], v[170:171], v[224:227] cbsz:4 abid:1\n"
    "v_exp_f32 v156, v156 clamp\n"
    "v_exp_f32 v157, v157 clamp\n"
    "v_mfma_f32_4x4x4_16b_bf16 v[224:227], v[228:229], v[172:173], v[224:227] cbsz:4 abid:2\n"
    "v_exp_f32 v158, v158 clamp\n"
    "v_exp_f32 v159, v159 clamp\n"
    "v_mfma_f32_4x4x4_16b_bf16 v[224:227], v[228:229], v[174:175], v[224:227] cbsz:4 abid:3\n"
    "s_waitcnt vmcnt(4)\n"
    "ds_read_b64_tr_b16 v[168:169], v240 offset:4096\n"
    "ds_read_b64_tr_b16 v[170:171], v241 offset:4352\n"
    "ds_read_b64_tr_b16 v[172:173], v242 offset:4608\n"
    "ds_read_b64_tr_b16 v[174:175], v243 offset:4864\n"
    "ds_read_b32 v236, v253 offset:0\n"
    "ds_read_b32 v238, v253 offset:4\n"
    "ds_read_b128 v[160:163], v234 offset:6144\n"
    "ds_read_b128 v[164:167], v235 offset:6144\n"
    "s_waitcnt lgkmcnt(8)\n"
    "s_nop 0\n"
    "v_readlane_b32 s20, v252, 0\n"
    "v_readlane_b32 s21, v252, 1\n"
    "v_readlane_b32 s22, v252, 2\n"
    "v_readlane_b32 s23, v252, 3\n"
    "v_readlane_b32 s24, v252, 4\n"
    "v_readlane_b32 s25, v252, 5\n"
    "v_readlane_b32 s26, v252, 6\n"
    "v_readlane_b32 s27, v252, 7\n"
    "v_pk_fma_f32 v[144:145], v[144:145], s[70:71], v[96:97]\n"
    "v_pk_fma_f32 v[146:147], v[146:147], s[70:71], v[98:99]\n"
    "v_pk_add_f32 v[230:231], v[144:145], v[146:147]\n"
    "v_pk_mul_f32 v[232:233], v[144:145], v[146:147]\n"
    "v_pk_fma_f32 v[148:149], v[148:149], s[70:71], v[100:101]\n"
    "v_pk_fma_f32 v[230:231], v[230:231], v[148:149], v[232:233]\n"
    "v_pk_mul_f32 v[232:233], v[232:233], v[148:149]\n"
    "v_pk_fma_f32 v[150:151], v[150:151], s[70:71], v[102:103]\n"
    "v_pk_fma_f32 v[230:231], v[230:231], v[150:151], v[232:233]\n"
    "v_pk_mul_f32 v[232:233], v[232:233], v[150:151]\n"
    "v_pk_fma_f32 v[152:153], v[152:153], s[70:71], v[104:105]\n"
    "v_pk_fma_f32 v[230:231], v[230:231], v[152:153], v[232:233]\n"
    "v_pk_mul_f32 v[232:233], v[232:233], v[152:153]\n"
    "v_pk_fma_f32 v[154:155], v[154:155], s[70:71], v[106:107]\n"
    "v_pk_fma_f32 v[230:231], v[230:231], v[154:155], v[232:233]\n"
    "v_pk_mul_f32 v[232:233], v[232:233], v[154:155]\n"
    "v_pk_fma_f32 v[156:157], v[156:157], s[70:71], v[108:109]\n"
    "v_pk_fma_f32 v[230:231], v[230:231], v[156:157], v[232:233]\n"
    "v_pk_mul_f32 v[232:233], v[232:233], v[156:157]\n"
    "v_pk_fma_f32 v[158:159], v[158:159], s[70:71], v[110:111]\n"
    "v_pk_fma_f32 v[230:231], v[230:231], v[158:159], v[232:233]\n"
    "v_pk_mul_f32 v[232:233], v[232:233], v[158:159]\n"
    "v_rcp_f32 v232, v232\n"
    "v_rcp_f32 v233, v233\n"
    "v_fma_f32 v230, v230, v232, v247\n"
    "v_fmac_f32 v230, v231, v233\n"
    "v_cvt_pk_bf16_f32 v230, v230, v230\n"
    "s_add_u32 s67, s67, 64\n"
    "s_waitcnt lgkmcnt(0)\n"
    "ds_write_b16 v245, v230\n"
    "ds_read_b64 v[228:229], v246\n"
    "s_lshl1_add_u32 s76, s72, s76\n"
    "buffer_load_dwordx4 v[236:237], s[40:43], 0 idxen offen offset:0 nt lds\n"
    "buffer_load_dwordx4 v[238:239], s[40:43], 0 idxen offen offset:1024 nt lds\n"
    "s_and_b32 s62, s58, 63\n"
    "s_mul_i32 s62, s62, 272\n"
    "s_mov_b32 s58, s59\n"
    "s_mov_b32 s59, s60\n"
    "s_mov_b32 s81, s65\n"
    "s_xor_b32 s65, s65, s64\n"
    "s_mov_b32 s80, s83\n"
    "s_add_u32 s82, s65, 256\n"
    "s_cmp_lt_i32 s59, s57\n"
    "s_cselect_b32 s76, s76, -1\n"
    "s_bitcmp1_b32 s58, 30\n"
    "s_cbranch_scc1 .Ldin_A2s\n"
    ".Ldin_A2:\n"
    "v_mfma_f32_16x16x32_bf16 v[144:147], v[0:3], v[160:163], v[112:115]\n"
    "v_exp_f32 v128, v128 clamp\n"
    "v_mfma_f32_16x16x32_bf16 v[144:147], v[4:7], v[164:167], v[144:147]\n"
    "v_exp_f32 v129, v129 clamp\n"
    "v_mfma_f32_16x16x32_bf16 v[148:151], v[8:11], v[160:163], v[116:119]\n"
    "v_exp_f32 v130, v130 clamp\n"
    "v_mfma_f32_16x16x32_bf16 v[148:151], v[12:15], v[164:167], v[148:151]\n"
    "v_exp_f32 v131, v131 clamp\n"
    "v_mfma_f32_16x16x32_bf16 v[152:155], v[16:19], v[160:163], v[120:123]\n"
    "v_exp_f32 v132, v132 clamp\n"
    "v_mfma_f32_16x16x32_bf16 v[152:155], v[20:23], v[164:167], v[152:155]\n"
    "v_exp_f32 v133, v133 clamp\n"
    "v_mfma_f32_16x16x32_bf16 v[156:159], v[24:27], v[160:163], v[124:127]\n"
    "v_exp_f32 v134, v134 clamp\n"
    "v_mfma_f32_16x16x32_bf16 v[156:159], v[28:31], v[164:167], v[156:159]\n"
    "v_exp_f32 v135, v135 clamp\n"
    "v_exp_f32 v136, v136 clamp\n"
    "s_add_u32 m0, s68, 6144\n"
    "v_exp_f32 v137, v137 clamp\n"
    "v_add_u32_e64 v253, s67, v244\n"
    "s_waitcnt lgkmcnt(0)\n"
    "v_mfma_f32_4x4x4_16b_bf16 v[224:227], v[228:229], v[176:177], v[224:227] cbsz:4 abid:0\n"
    "v_exp_f32 v138, v138 clamp\n"
    "v_exp_f32 v139, v139 clamp\n"
    "v_mfma_f32_4x4x4_16b_bf16 v[224:227], v[228:229], v[178:179], v[224:227] cbsz:4 abid:1\n"
    "v_exp_f32 v140, v140 clamp\n"
    "v_exp_f32 v141, v141 clamp\n"
    "v_mfma_f32_4x4x4_16b_bf16 v[224:227], v[228:229], v[180:181], v[224:227] cbsz:4 abid:2\n"
    "v_exp_f32 v142, v142 clamp\n"
    "v_exp_f32 v143, v143 clamp\n"
    "v_mfma_f32_4x4x4_16b_bf16 v[224:227], v[228:229], v[182:183], v[224:227] cbsz:4 abid:3\n"
    "s_waitcnt vmcnt(4)\n"
    "ds_read_b64_tr_b16 v[176:177], v240 offset:6144\n"
    "ds_read_b64_tr_b16 v[178:179], v241 offset:6400\n"
    "ds_read_b64_tr_b16 v[180:181], v242 offset:6656\n"
    "ds_read_b64_tr_b16 v[182:183], v243 offset:6912\n"
    "ds_read_b32 v236, v253 offset:0\n"
    "ds_read_b32 v238, v253 offset:4\n"
    "ds_read_b128 v[160:163], v234 offset:0\n"
    "ds_read_b128 v[164:167], v235 offset:0\n"
    "s_cbranch_vccz .Ldin_A2_np\n"
    "s_setprio 1\n"
    ".Ldin_A2_np:\n"
    "v_pk_fma_f32 v[128:129], v[128:129], s[70:71], v[96:97]\n"
    "v_pk_fma_f32 v[130:131], v[130:131], s[70:71], v[98:99]\n"
    "v_add_f32 v191, v224, v225\n"
    "v_add_f32 v190, v226, v227\n"
    "v_add_f32 v191, v191, v190\n"
    "v_add_u32 v190, s62, v249\n"
    "v_mul_f32 v191, s69, v191\n"
    "v_cvt_pk_bf16_f32 v191, v191, v191\n"
    "ds_write_b16 v190, v191 offset:32768\n"
    "s_bfe_u32 s94, s59, 0x180006\n"
    "s_mul_i32 s86, s94, s56\n"
    "v_mbcnt_lo_u32_b32 v184, s20, 0\n"
    "v_mbcnt_lo_u32_b32 v185, s22, 0\n"
    "v_mbcnt_lo_u32_b32 v186, s24, 0\n"
    "v_mbcnt_lo_u32_b32 v187, s26, 0\n"
    "s_bcnt1_i32_b64 s90, s[20:21]\n"
    "s_bcnt1_i32_b64 s91, s[22:23]\n"
    "s_bcnt1_i32_b64 s92, s[24:25]\n"
    "s_bcnt1_i32_b64 s93, s[26:27]\n"
    "v_mbcnt_hi_u32_b32 v184, s21, v184\n"
    "v_mbcnt_hi_u32_b32 v185, s23, v185\n"
    "v_mbcnt_hi_u32_b32 v186, s25, v186\n"
    "v_mbcnt_hi_u32_b32 v187, s27, v187\n"
    "s_lshl2_add_u32 s90, s90, s65\n"
    "s_lshl2_add_u32 s91, s91, s90\n"
    "s_lshl2_add_u32 s92, s92, s91\n"
    "s_lshl2_add_u32 s93, s93, s92\n"
    "v_add_u32_e64 v188, s86, v251\n"
    "v_pk_add_f32 v[230:231], v[128:129], v[130:131]\n"
    "v_pk_mul_f32 v[232:233], v[128:129], v[130:131]\n"
    "v_pk_fma_f32 v[132:133], v[132:133], s[70:71], v[100:101]\n"
    "v_pk_fma_f32 v[230:231], v[230:231], v[132:133], v[232:233]\n"
    "v_pk_mul_f32 v[232:233], v[232:233], v[132:133]\n"
    "v_lshl_add_u32 v184, v184, 2, s65\n"
    "v_lshl_add_u32 v185, v185, 2, s90\n"
    "v_lshl_add_u32 v186, v186, 2, s91\n"
    "v_lshl_add_u32 v187, v187, 2, s92\n"
    "v_mov_b32 v191, -1\n"
    "v_lshl_add_u32 v190, v249, 1, s93\n"
    "ds_write2_b32 v190, v191, v191 offset1:64\n"
    "s_mov_b64 exec, s[20:21]\n"
    "ds_write_b32 v184, v188\n"
    "s_mov_b64 exec, s[22:23]\n"
    "v_add_u32 v189, 64, v188\n"
    "ds_write_b32 v185, v189\n"
    "s_mov_b64 exec, s[24:25]\n"
    "v_add_u32 v190, 128, v188\n"
    "ds_write_b32 v186, v190\n"
    "s_mov_b64 exec, s[26:27]\n"
    "v_add_u32 v189, 192, v188\n"
    "ds_write_b32 v187, v189\n"
    "s_mov_b64 exec, -1\n"
    "s_add_u32 s95, s93, 60\n"
    "s_add_u32 s94, s82, 128\n"
    "s_andn2_b32 s95, s95, 63\n"
    "s_max_u32 s83, s95, s94\n"
    "v_pk_fma_f32 v[134:135], v[134:135], s[70:71], v[102:103]\n"
    "v_pk_fma_f32 v[230:231], v[230:231], v[134:135], v[232:233]\n"
    "v_pk_mul_f32 v[232:233], v[232:233], v[134:135]\n"
    "v_pk_fma_f32 v[136:137], v[136:137], s[70:71], v[104:105]\n"
    "v_pk_fma_f32 v[230:231], v[230:231], v[136:137], v[232:233]\n"
    "v_pk_mul_f32 v[232:233], v[232:233], v[136:137]\n"
    "v_pk_fma_f32 v[138:139], v[138:139], s[70:71], v[106:107]\n"
    "v_pk_fma_f32 v[230:231], v[230:231], v[138:139], v[232:233]\n"
    "v_pk_mul_f32 v[232:233], v[232:233], v[138:139]\n"
    "v_pk_fma_f32 v[140:141], v[140:141], s[70:71], v[108:109]\n"
    "v_pk_fma_f32 v[230:231], v[230:231], v[140:141], v[232:233]\n"
    "v_pk_mul_f32 v[232:233], v[232:233], v[140:141]\n"
    "v_pk_fma_f32 v[142:143], v[142:143], s[70:71], v[110:111]\n"
    "v_pk_fma_f32 v[230:231], v[230:231], v[142:143], v[232:233]\n"
    "v_pk_mul_f32 v[232:233], v[232:233], v[142:143]\n"
    "v_rcp_f32 v232, v232\n"
    "v_rcp_f32 v233, v233\n"
    "v_fma_f32 v230, v230, v232, v247\n"
    "v_fmac_f32 v230, v231, v233\n"
    "v_cvt_pk_bf16_f32 v230, v230, v230\n"
    "s_add_u32 s67, s67, 64\n"
    "s_waitcnt lgkmcnt(0)\n"
    "ds_write_b16 v245, v230\n"
    "ds_read_b64 v[228:229], v246\n"
    "buffer_load_dwordx4 v[236:237], s[40:43], 0 idxen offen offset:0 nt lds\n"
    "buffer_load_dwordx4 v[238:239], s[40:43], 0 idxen offen offset:1024 nt lds\n"
    ".Ldin_B3:\n"
    "v_mfma_f32_16x16x32_bf16 v[128:131], v[0:3], v[160:163], v[112:115]\n"
    "v_exp_f32 v144, v144 clamp\n"
    "v_mfma_f32_16x16x32_bf16 v[128:131], v[4:7], v[164:167], v[128:131]\n"
    "v_exp_f32 v145, v145 clamp\n"
    "v_mfma_f32_16x16x32_bf16 v[132:135], v[8:11], v[160:163], v[116:119]\n"
    "v_exp_f32 v146, v146 clamp\n"
    "v_mfma_f32_16x16x32_bf16 v[132:135], v[12:15], v[164:167], v[132:135]\n"
    "v_exp_f32 v147, v147 clamp\n"
    "v_mfma_f32_16x16x32_bf16 v[136:139], v[16:19], v[160:163], v[120:123]\n"
    "v_exp_f32 v148, v148 clamp\n"
    "v_mfma_f32_16x16x32_bf16 v[136:139], v[20:23], v[164:167], v[136:139]\n"
    "v_exp_f32 v149, v149 clamp\n"
    "v_mfma_f32_16x16x32_bf16 v[140:143], v[24:27], v[160:163], v[124:127]\n"
    "v_exp_f32 v150, v150 clamp\n"
    "v_mfma_f32_16x16x32_bf16 v[140:143], v[28:31], v[164:167], v[140:143]\n"
    "v_exp_f32 v151, v151 clamp\n"
    "v_exp_f32 v152, v152 clamp\n"
    "s_cmp_eq_u32 s67, s80\n"
    "s_cselect_b32 s67, s65, s67\n"
    "s_and_b32 s94, s59, 63\n"
    "s_mul_i32 s94, s94, 272\n"
    "v_add_u32 v252, s94, v250\n"
    "s_add_u32 m0, s68, 0\n"
    "v_exp_f32 v153, v153 clamp\n"
    "v_add_u32_e64 v253, s67, v244\n"
    "s_waitcnt lgkmcnt(0)\n"
    "v_mfma_f32_4x4x4_16b_bf16 v[224:227], v[228:229], v[168:169], 0 cbsz:4 abid:0\n"
    "v_exp_f32 v154, v154 clamp\n"
    "v_exp_f32 v155, v155 clamp\n"
    "v_mfma_f32_4x4x4_16b_bf16 v[224:227], v[228:229], v[170:171], v[224:227] cbsz:4 abid:1\n"
    "v_exp_f32 v156, v156 clamp\n"
    "v_exp_f32 v157, v157 clamp\n"
    "v_mfma_f32_4x4x4_16b_bf16 v[224:227], v[228:229], v[172:173], v[224:227] cbsz:4 abid:2\n"
    "v_exp_f32 v158, v158 clamp\n"
    "v_exp_f32 v159, v159 clamp\n"
    "v_mfma_f32_4x4x4_16b_bf16 v[224:227], v[228:229], v[174:175], v[224:227] cbsz:4 abid:3\n"
    "s_waitcnt vmcnt(4)\n"
    "s_memrealtime s[34:35]\n"
    "ds_read_b64_tr_b16 v[168:169], v240 offset:0\n"
    "ds_read_b64_tr_b16 v[170:171], v241 offset:256\n"
    "ds_read_b64_tr_b16 v[172:173], v242 offset:512\n"
    "ds_read_b64_tr_b16 v[174:175], v243 offset:768\n"
    "ds_read_b32 v236, v253 offset:0\n"
    "ds_read_b32 v238, v253 offset:4\n"
    "ds_read_b128 v[160:163], v234 offset:2048\n"
    "ds_read_b128 v[164:167], v235 offset:2048\n"
    "ds_read_b128 v[184:187], v252 offset:0\n"
    "ds_read_b128 v[188:191], v252 offset:16\n"
    "v_pk_fma_f32 v[144:145], v[144:145], s[70:71], v[96:97]\n"
    "v_pk_fma_f32 v[146:147], v[146:147], s[70:71], v[98:99]\n"
    "v_pk_add_f32 v[230:231], v[144:145], v[146:147]\n"
    "v_pk_mul_f32 v[232:233], v[144:145], v[146:147]\n"
    "v_pk_fma_f32 v[148:149], v[148:149], s[70:71], v[100:101]\n"
    "v_pk_fma_f32 v[230:231], v[230:231], v[148:149], v[232:233]\n"
    "v_pk_mul_f32 v[232:233], v[232:233], v[148:149]\n"
    "v_pk_fma_f32 v[150:151], v[150:151], s[70:71], v[102:103]\n"
    "v_pk_fma_f32 v[230:231], v[230:231], v[150:151], v[232:233]\n"
    "v_pk_mul_f32 v[232:233], v[232:233], v[150:151]\n"
    "v_pk_fma_f32 v[152:153], v[152:153], s[70:71], v[104:105]\n"
    "v_pk_fma_f32 v[230:231], v[230:231], v[152:153], v[232:233]\n"
    "v_pk_mul_f32 v[232:233], v[232:233], v[152:153]\n"
    "v_pk_fma_f32 v[154:155], v[154:155], s[70:71], v[106:107]\n"
    "v_pk_fma_f32 v[230:231], v[230:231], v[154:155], v[232:233]\n"
    "v_pk_mul_f32 v[232:233], v[232:233], v[154:155]\n"
    "v_pk_fma_f32 v[156:157], v[156:157], s[70:71], v[108:109]\n"
    "v_pk_fma_f32 v[230:231], v[230:231], v[156:157], v[232:233]\n"
    "v_pk_mul_f32 v[232:233], v[232:233], v[156:157]\n"
    "v_pk_fma_f32 v[158:159], v[158:159], s[70:71], v[110:111]\n"
    "v_pk_fma_f32 v[230:231], v[230:231], v[158:159], v[232:233]\n"
    "v_pk_mul_f32 v[232:233], v[232:233], v[158:159]\n"
    "v_rcp_f32 v232, v232\n"
    "v_rcp_f32 v233, v233\n"
    "v_fma_f32 v230, v230, v232, v247\n"
    "v_fmac_f32 v230, v231, v233\n"
    "v_cvt_pk_bf16_f32 v230, v230, v230\n"
    "s_add_u32 s67, s67, 64\n"
    "s_waitcnt lgkmcnt(0)\n"
    "ds_write_b16 v245, v230\n"
    "ds_read_b64 v[228:229], v246\n"
    "s_cmp_le_u32 s34, s76\n"
    "s_cbranch_scc1 .Ldin_B3_wait\n"
    ".Ldin_B3_go:\n"
    "s_sub_u32 s94, s34, 60\n"
    "s_max_u32 s76, s76, s94\n"
    "s_add_u32 s76, s76, s72\n"
    ".Ldin_B3_dma:\n"
    "buffer_load_dwordx4 v[236:237], s[40:43], 0 idxen offen offset:0 nt lds\n"
    "buffer_load_dwordx4 v[238:239], s[40:43], 0 idxen offen offset:1024 nt lds\n"
    ".Ldin_B3_nd:\n"
    "s_branch .Ldin_G0\n"
    ".Ldin_L1_poll:\n"
    "s_sleep 1\n"
    "ds_read_b64 v[232:233], v253\n"
    "s_waitcnt lgkmcnt(0)\n"
    "v_readfirstlane_b32 s60, v232\n"
    "s_cmp_eq_u32 s60, -1\n"
    "s_cbranch_scc1 .Ldin_L1_poll\n"
    "s_branch .Ldin_L1_pr\n"
    ".Ldin_B3_wait:\n"
    "s_cmp_lt_i32 s59, s57\n"
    "s_cbranch_scc1 .Ldin_B3_w2\n"
    "s_sub_u32 s94, s67, s65\n"
    "s_cmp_gt_u32 s94, 256\n"
    "s_cbranch_scc1 .Ldin_B3_dma\n"
    "s_cmp_eq_u32 s94, 64\n"
    "s_waitcnt vmcnt(2)\n"
    "s_cbranch_scc1 .Ldin_B3_nd\n"
    "s_waitcnt vmcnt(0)\n"
    "s_branch .Ldin_B3_nd\n"
    ".Ldin_B3_slp:\n"
    "s_sleep 1\n"
    ".Ldin_B3_w2:\n"
    "s_memrealtime s[34:35]\n"
    "s_waitcnt lgkmcnt(0)\n"
    "s_sub_u32 s94, s34, s76\n"
    "s_cmp_lt_i32 s94, 0\n"
    "s_cbranch_scc1 .Ldin_B3_slp\n"
    "s_branch .Ldin_B3_go\n"
    ".p2align 5\n"
    ".Ldin_L0:\n"
    "s_setprio 0\n"
    "s_cmp_ge_i32 s59, s57\n"
    "s_cbranch_scc1 .Ldin_E0\n"
    "s_add_u32 s76, s76, s73\n"
    "v_mov_b32 v253, s63\n"
    "v_and_b32 v184, v254, v184\n"
    "v_and_b32 v185, v255, v185\n"
    "v_and_b32 v186, v254, v186\n"
    "v_and_b32 v187, v255, v187\n"
    "v_and_b32 v188, v254, v188\n"
    "v_and_b32 v189, v255, v189\n"
    "v_and_b32 v190, v254, v190\n"
    "v_and_b32_e64 v191, v255, v191\n"
    "ds_read_b64 v[232:233], v253\n"
    "v_mfma_f32_4x4x4_16b_bf16 v[144:147], v[184:185], v[32:33], v[64:67]\n"
    "ds_read_b128 v[112:115], v252 offset:17408\n"
    "v_mfma_f32_4x4x4_16b_bf16 v[148:151], v[186:187], v[34:35], v[68:71]\n"
    "ds_read_b128 v[116:119], v252 offset:17424\n"
    "v_mfma_f32_4x4x4_16b_bf16 v[152:155], v[188:189], v[36:37], v[72:75]\n"
    "ds_read_b128 v[120:123], v252 offset:17536\n"
    "ds_read_b128 v[124:127], v252 offset:17552\n"
    "v_cvt_pk_bf16_f32 v0, v144, v145\n"
    "v_cvt_pk_bf16_f32 v1, v146, v147\n"
    "v_mfma_f32_4x4x4_16b_bf16 v[156:159], v[190:191], v[38:39], v[76:79]\n"
    "v_cvt_pk_bf16_f32 v2, v148, v149\n"
    "v_cvt_pk_bf16_f32 v3, v150, v151\n"
    "v_mfma_f32_4x4x4_16b_bf16 v[144:147], v[184:185], v[40:41], v[80:83]\n"
    "v_cvt_pk_bf16_f32 v4, v152, v153\n"
    "v_cvt_pk_bf16_f32 v5, v154, v155\n"
    "v_mfma_f32_4x4x4_16b_bf16 v[148:151], v[186:187], v[42:43], v[84:87]\n"
    "v_cvt_pk_bf16_f32 v6, v156, v157\n"
    "v_cvt_pk_bf16_f32 v7, v158, v159\n"
    "v_mfma_f32_4x4x4_16b_bf16 v[152:155], v[188:189], v[44:45], v[88:91]\n"
    "v_cvt_pk_bf16_f32 v8, v144, v145\n"
    "v_cvt_pk_bf16_f32 v9, v146, v147\n"
    "v_mfma_f32_4x4x4_16b_bf16 v[156:159], v[190:191], v[46:47], v[92:95]\n"
    "v_cvt_pk_bf16_f32 v10, v148, v149\n"
    "v_cvt_pk_bf16_f32 v11, v150, v151\n"
    "v_mfma_f32_4x4x4_16b_bf16 v[144:147], v[184:185], v[48:49], v[192:195]\n"
    "v_cvt_pk_bf16_f32 v12, v152, v153\n"
    "v_cvt_pk_bf16_f32 v13, v154, v155\n"
    "v_mfma_f32_4x4x4_16b_bf16 v[148:151], v[186:187], v[50:51], v[196:199]\n"
    "v_cvt_pk_bf16_f32 v14, v156, v157\n"
    "v_cvt_pk_bf16_f32 v15, v158, v159\n"
    "v_mfma_f32_4x4x4_16b_bf16 v[152:155], v[188:189], v[52:53], v[200:203]\n"
    "s_waitcnt lgkmcnt(0)\n"
    "v_readfirstlane_b32 s60, v232\n"
    "v_cvt_pk_bf16_f32 v16, v144, v145\n"
    "v_cvt_pk_bf16_f32 v17, v146, v147\n"
    "v_mfma_f32_4x4x4_16b_bf16 v[156:159], v[190:191], v[54:55], v[204:207]\n"
    "s_cmp_eq_u32 s60, -1\n"
    "s_cbranch_scc1 .Ldin_L0_poll\n"
    ".Ldin_L0_pr:\n"
    "v_readfirstlane_b32 s95, v233\n"
    "s_and_b32 s94, s60, 63\n"
    "s_lshl_b32 s94, s94, 6\n"
    "s_add_u32 s94, s94, 68480\n"
    "s_add_u32 s63, s63, 8\n"
    "v_lshl_add_u32 v252, v249, 1, s94\n"
    "ds_read_b32 v252, v252\n"
    "s_sub_u32 s95, s95, s76\n"
    "s_ashr_i32 s95, s95, 4\n"
    "s_min_i32 s95, s95, s74\n"
    "s_max_i32 s72, s95, s75\n"
    "v_cvt_pk_bf16_f32 v18, v148, v149\n"
    "v_cvt_pk_bf16_f32 v19, v150, v151\n"
    "v_mfma_f32_4x4x4_16b_bf16 v[144:147], v[184:185], v[56:57], v[208:211]\n"
    "v_cvt_pk_bf16_f32 v20, v152, v153\n"
    "v_cvt_pk_bf16_f32 v21, v154, v155\n"
    "v_mfma_f32_4x4x4_16b_bf16 v[148:151], v[186:187], v[58:59], v[212:215]\n"
    "v_cvt_pk_bf16_f32 v22, v156, v157\n"
    "v_cvt_pk_bf16_f32 v23, v158, v159\n"
    "v_mfma_f32_4x4x4_16b_bf16 v[152:155], v[188:189], v[60:61], v[216:219]\n"
    "v_cvt_pk_bf16_f32 v24, v144, v145\n"
    "v_cvt_pk_bf16_f32 v25, v146, v147\n"
    "v_mfma_f32_4x4x4_16b_bf16 v[156:159], v[190:191], v[62:63], v[220:223]\n"
    "v_cvt_pk_bf16_f32 v26, v148, v149\n"
    "v_cvt_pk_bf16_f32 v27, v150, v151\n"
    "v_cvt_pk_bf16_f32 v28, v152, v153\n"
    "v_cvt_pk_bf16_f32 v29, v154, v155\n"
    "v_exp_f32 v136, v136 clamp\n"
    "v_cvt_pk_bf16_f32 v30, v156, v157\n"
    "v_cvt_pk_bf16_f32 v31, v158, v159\n"
    "v_mfma_f32_16x16x32_bf16 v[144:147], v[0:3], v[160:163], v[112:115]\n"
    "v_exp_f32 v128, v128 clamp\n"
    "v_mfma_f32_16x16x32_bf16 v[144:147], v[4:7], v[164:167], v[144:147]\n"
    "v_exp_f32 v129, v129 clamp\n"
    "v_mfma_f32_16x16x32_bf16 v[148:151], v[8:11], v[160:163], v[116:119]\n"
    "v_exp_f32 v130, v130 clamp\n"
    "v_mfma_f32_16x16x32_bf16 v[148:151], v[12:15], v[164:167], v[148:151]\n"
    "v_exp_f32 v131, v131 clamp\n"
    "v_mfma_f32_16x16x32_bf16 v[152:155], v[16:19], v[160:163], v[120:123]\n"
    "v_exp_f32 v132, v132 clamp\n"
    "v_mfma_f32_16x16x32_bf16 v[152:155], v[20:23], v[164:167], v[152:155]\n"
    "v_exp_f32 v133, v133 clamp\n"
    "v_mfma_f32_16x16x32_bf16 v[156:159], v[24:27], v[160:163], v[124:127]\n"
    "v_exp_f32 v134, v134 clamp\n"
    "v_mfma_f32_16x16x32_bf16 v[156:159], v[28:31], v[164:167], v[156:159]\n"
    "v_exp_f32 v135, v135 clamp\n"
    "s_add_u32 m0, s68, 2048\n"
    "v_exp_f32 v137, v137 clamp\n"
    "v_add_u32 v253, s67, v244\n"
    "v_mfma_f32_4x4x4_16b_bf16 v[224:227], v[228:229], v[176:177], v[224:227] cbsz:4 abid:0\n"
    "v_exp_f32 v138, v138 clamp\n"
    "v_exp_f32 v139, v139 clamp\n"
    "v_mfma_f32_4x4x4_16b_bf16 v[224:227], v[228:229], v[178:179], v[224:227] cbsz:4 abid:1\n"
    "v_exp_f32 v140, v140 clamp\n"
    "v_exp_f32 v141, v141 clamp\n"
    "v_mfma_f32_4x4x4_16b_bf16 v[224:227], v[228:229], v[180:181], v[224:227] cbsz:4 abid:2\n"
    "v_exp_f32 v142, v142 clamp\n"
    "v_exp_f32 v143, v143 clamp\n"
    "v_mfma_f32_4x4x4_16b_bf16 v[224:227], v[228:229], v[182:183], v[224:227] cbsz:4 abid:3\n"
    "s_waitcnt vmcnt(4)\n"
    "ds_read_b64_tr_b16 v[176:177], v240 offset:2048\n"
    "ds_read_b64_tr_b16 v[178:179], v241 offset:2304\n"
    "ds_read_b64_tr_b16 v[180:181], v242 offset:2560\n"
    "ds_read_b64_tr_b16 v[182:183], v243 offset:2816\n"
    "ds_read_b32 v236, v253 offset:0\n"
    "ds_read_b32 v238, v253 offset:4\n"
    "ds_read_b128 v[160:163], v234 offset:4096\n"
    "ds_read_b128 v[164:167], v235 offset:4096\n"
    "s_waitcnt lgkmcnt(8)\n"
    "s_nop 0\n"
    "v_readlane_b32 s20, v252, 0\n"
    "v_readlane_b32 s21, v252, 1\n"
    "v_readlane_b32 s22, v252, 2\n"
    "v_readlane_b32 s23, v252, 3\n"
    "v_readlane_b32 s24, v252, 4\n"
    "v_readlane_b32 s25, v252, 5\n"
    "v_readlane_b32 s26, v252, 6\n"
    "v_readlane_b32 s27, v252, 7\n"
    "v_pk_fma_f32 v[128:129], v[128:129], s[70:71], v[96:97]\n"
    "v_pk_fma_f32 v[130:131], v[130:131], s[70:71], v[98:99]\n"
    "v_pk_add_f32 v[230:231], v[128:129], v[130:131]\n"
    "v_pk_mul_f32 v[232:233], v[128:129], v[130:131]\n"
    "v_pk_fma_f32 v[132:133], v[132:133], s[70:71], v[100:101]\n"
    "v_pk_fma_f32 v[230:231], v[230:231], v[132:133], v[232:233]\n"
    "v_pk_mul_f32 v[232:233], v[232:233], v[132:133]\n"
    "v_pk_fma_f32 v[134:135], v[134:135], s[70:71], v[102:103]\n"
    "v_pk_fma_f32 v[230:231], v[230:231], v[134:135], v[232:233]\n"
    "v_pk_mul_f32 v[232:233], v[232:233], v[134:135]\n"
    "v_pk_fma_f32 v[136:137], v[136:137], s[70:71], v[104:105]\n"
    "v_pk_fma_f32 v[230:231], v[230:231], v[136:137], v[232:233]\n"
    "v_pk_mul_f32 v[232:233], v[232:233], v[136:137]\n"
    "v_pk_fma_f32 v[138:139], v[138:139], s[70:71], v[106:107]\n"
    "v_pk_fma_f32 v[230:231], v[230:231], v[138:139], v[232:233]\n"
    "v_pk_mul_f32 v[232:233], v[232:233], v[138:139]\n"
    "v_pk_fma_f32 v[140:141], v[140:141], s[70:71], v[108:109]\n"
    "v_pk_fma_f32 v[230:231], v[230:231], v[140:141], v[232:233]\n"
    "v_pk_mul_f32 v[232:233], v[232:233], v[140:141]\n"
    "v_pk_fma_f32 v[142:143], v[142:143], s[70:71], v[110:111]\n"
    "v_pk_fma_f32 v[230:231], v[230:231], v[142:143], v[232:233]\n"
    "v_pk_mul_f32 v[232:233], v[232:233], v[142:143]\n"
    "v_rcp_f32 v232, v232\n"
    "v_rcp_f32 v233, v233\n"
    "v_fma_f32 v230, v230, v232, v247\n"
    "v_fmac_f32 v230, v231, v233\n"
    "v_cvt_pk_bf16_f32 v230, v230, v230\n"
    "s_add_u32 s67, s67, 64\n"
    "s_waitcnt lgkmcnt(0)\n"
    "ds_write_b16 v245, v230\n"
    "ds_read_b64 v[228:229], v246\n"
    "s_lshl1_add_u32 s76, s72, s76\n"
    "buffer_load_dwordx4 v[236:237], s[40:43], 0 idxen offen offset:0 nt lds\n"
    "buffer_load_dwordx4 v[238:239], s[40:43], 0 idxen offen offset:1024 nt lds\n"
    "s_and_b32 s62, s58, 63\n"
    "s_mul_i32 s62, s62, 272\n"
    "s_mov_b32 s58, s59\n"
    "s_mov_b32 s59, s60\n"
    "s_mov_b32 s81, s65\n"
    "s_xor_b32 s65, s65, s64\n"
    "s_mov_b32 s80, s83\n"
    "s_add_u32 s82, s65, 256\n"
    "s_cmp_lt_i32 s59, s57\n"
    "s_cselect_b32 s76, s76, -1\n"
    "s_bitcmp1_b32 s58, 30\n"
    "s_cbranch_scc1 .Ldin_A1s\n"
    ".Ldin_A1:\n"
    "v_mfma_f32_16x16x32_bf16 v[128:131], v[0:3], v[160:163], v[112:115]\n"
    "v_exp_f32 v144, v144 clamp\n"
    "v_mfma_f32_16x16x32_bf16 v[128:131], v[4:7], v[164:167], v[128:131]\n"
    "v_exp_f32 v145, v145 clamp\n"
    "v_mfma_f32_16x16x32_bf16 v[132:135], v[8:11], v[160:163], v[116:119]\n"
    "v_exp_f32 v146, v146 clamp\n"
    "v_mfma_f32_16x16x32_bf16 v[132:135], v[12:15], v[164:167], v[132:135]\n"
    "v_exp_f32 v147, v147 clamp\n"
    "v_mfma_f32_16x16x32_bf16 v[136:139], v[16:19], v[160:163], v[120:123]\n"
    "v_exp_f32 v148, v148 clamp\n"
    "v_mfma_f32_16x16x32_bf16 v[136:139], v[20:23], v[164:167], v[136:139]\n"
    "v_exp_f32 v149, v149 clamp\n"
    "v_mfma_f32_16x16x32_bf16 v[140:143], v[24:27], v[160:163], v[124:127]\n"
    "v_exp_f32 v150, v150 clamp\n"
    "v_mfma_f32_16x16x32_bf16 v[140:143], v[28:31], v[164:167], v[140:143]\n"
    "v_exp_f32 v151, v151 clamp\n"
    "v_exp_f32 v152, v152 clamp\n"
    "s_add_u32 m0, s68, 4096\n"
    "v_exp_f32 v153, v153 clamp\n"
    "v_add_u32_e64 v253, s67, v244\n"
    "s_waitcnt lgkmcnt(0)\n"
    "v_mfma_f32_4x4x4_16b_bf16 v[224:227], v[228:229], v[168:169], v[224:227] cbsz:4 abid:0\n"
    "v_exp_f32 v154, v154 clamp\n"
    "v_exp_f32 v155, v155 clamp\n"
    "v_mfma_f32_4x4x4_16b_bf16 v[224:227], v[228:229], v[170:171], v[224:227] cbsz:4 abid:1\n"
    "v_exp_f32 v156, v156 clamp\n"
    "v_exp_f32 v157, v157 clamp\n"
    "v_mfma_f32_4x4x4_16b_bf16 v[224:227], v[228:229], v[172:173], v[224:227] cbsz:4 abid:2\n"
    "v_exp_f32 v158, v158 clamp\n"
    "v_exp_f32 v159, v159 clamp\n"
    "v_mfma_f32_4x4x4_16b_bf16 v[224:227], v[228:229], v[174:175], v[224:227] cbsz:4 abid:3\n"
    "s_waitcnt vmcnt(4)\n"
    "ds_read_b64_tr_b16 v[168:169], v240 offset:4096\n"
    "ds_read_b64_tr_b16 v[170:171], v241 offset:4352\n"
    "ds_read_b64_tr_b16 v[172:173], v242 offset:4608\n"
    "ds_read_b64_tr_b16 v[174:175], v243 offset:4864\n"
    "ds_read_b32 v236, v253 offset:0\n"
    "ds_read_b32 v238, v253 offset:4\n"
    "ds_read_b128 v[160:163], v234 offset:6144\n"
    "ds_read_b128 v[164:167], v235 offset:6144\n"
    "s_cbranch_vccz .Ldin_A1_np\n"
    "s_setprio 1\n"
    ".Ldin_A1_np:\n"
    "v_pk_fma_f32 v[144:145], v[144:145], s[70:71], v[96:97]\n"
    "v_pk_fma_f32 v[146:147], v[146:147], s[70:71], v[98:99]\n"
    "v_add_f32 v191, v224, v225\n"
    "v_add_f32 v190, v226, v227\n"
    "v_add_f32 v191, v191, v190\n"
    "v_add_u32 v190, s62, v249\n"
    "v_mul_f32 v191, s69, v191\n"
    "v_cvt_pk_bf16_f32 v191, v191, v191\n"
    "ds_write_b16 v190, v191 offset:32768\n"
    "s_bfe_u32 s94, s59, 0x180006\n"
    "s_mul_i32 s86, s94, s56\n"
    "v_mbcnt_lo_u32_b32 v184, s20, 0\n"
    "v_mbcnt_lo_u32_b32 v185, s22, 0\n"
    "v_mbcnt_lo_u32_b32 v186, s24, 0\n"
    "v_mbcnt_lo_u32_b32 v187, s26, 0\n"
    "s_bcnt1_i32_b64 s90, s[20:21]\n"
    "s_bcnt1_i32_b64 s91, s[22:23]\n"
    "s_bcnt1_i32_b64 s92, s[24:25]\n"
    "s_bcnt1_i32_b64 s93, s[26:27]\n"
    "v_mbcnt_hi_u32_b32 v184, s21, v184\n"
    "v_mbcnt_hi_u32_b32 v185, s23, v185\n"
    "v_mbcnt_hi_u32_b32 v186, s25, v186\n"
    "v_mbcnt_hi_u32_b32 v187, s27, v187\n"
    "s_lshl2_add_u32 s90, s90, s65\n"
    "s_lshl2_add_u32 s91, s91, s90\n"
    "s_lshl2_add_u32 s92, s92, s91\n"
    "s_lshl2_add_u32 s93, s93, s92\n"
    "v_add_u32_e64 v188, s86, v251\n"
    "v_pk_add_f32 v[230:231], v[144:145], v[146:147]\n"
    "v_pk_mul_f32 v[232:233], v[144:145], v[146:147]\n"
    "v_pk_fma_f32 v[148:149], v[148:149], s[70:71], v[100:101]\n"
    "v_pk_fma_f32 v[230:231], v[230:231], v[148:149], v[232:233]\n"
    "v_pk_mul_f32 v[232:233], v[232:233], v[148:149]\n"
    "v_lshl_add_u32 v184, v184, 2, s65\n"
    "v_lshl_add_u32 v185, v185, 2, s90\n"
    "v_lshl_add_u32 v186, v186, 2, s91\n"
    "v_lshl_add_u32 v187, v187, 2, s92\n"
    "v_mov_b32 v191, -1\n"
    "v_lshl_add_u32 v190, v249, 1, s93\n"
    "ds_write2_b32 v190, v191, v191 offset1:64\n"
    "s_mov_b64 exec, s[20:21]\n"
    "ds_write_b32 v184, v188\n"
    "s_mov_b64 exec, s[22:23]\n"
    "v_add_u32 v189, 64, v188\n"
    "ds_write_b32 v185, v189\n"
    "s_mov_b64 exec, s[24:25]\n"
    "v_add_u32 v190, 128, v188\n"
    "ds_write_b32 v186, v190\n"
    "s_mov_b64 exec, s[26:27]\n"
    "v_add_u32 v189, 192, v188\n"
    "ds_write_b32 v187, v189\n"
    "s_mov_b64 exec, -1\n"
    "s_add_u32 s95, s93, 60\n"
    "s_add_u32 s94, s82, 128\n"
    "s_andn2_b32 s95, s95, 63\n"
    "s_max_u32 s83, s95, s94\n"
    "v_pk_fma_f32 v[150:151], v[150:151], s[70:71], v[102:103]\n"
    "v_pk_fma_f32 v[230:231], v[230:231], v[150:151], v[232:233]\n"
    "v_pk_mul_f32 v[232:233], v[232:233], v[150:151]\n"
    "v_pk_fma_f32 v[152:153], v[152:153], s[70:71], v[104:105]\n"
    "v_pk_fma_f32 v[230:231], v[230:231], v[152:153], v[232:233]\n"
    "v_pk_mul_f32 v[232:233], v[232:233], v[152:153]\n"
    "v_pk_fma_f32 v[154:155], v[154:155], s[70:71], v[106:107]\n"
    "v_pk_fma_f32 v[230:231], v[230:231], v[154:155], v[232:233]\n"
    "v_pk_mul_f32 v[232:233], v[232:233], v[154:155]\n"
    "v_pk_fma_f32 v[156:157], v[156:157], s[70:71], v[108:109]\n"
    "v_pk_fma_f32 v[230:231], v[230:231], v[156:157], v[232:233]\n"
    "v_pk_mul_f32 v[232:233], v[232:233], v[156:157]\n"
    "v_pk_fma_f32 v[158:159], v[158:159], s[70:71], v[110:111]\n"
    "v_pk_fma_f32 v[230:231], v[230:231], v[158:159], v[232:233]\n"
    "v_pk_mul_f32 v[232:233], v[232:233], v[158:159]\n"
    "v_rcp_f32 v232, v232\n"
    "v_rcp_f32 v233, v233\n"
    "v_fma_f32 v230, v230, v232, v247\n"
    "v_fmac_f32 v230, v231, v233\n"
    "v_cvt_pk_bf16_f32 v230, v230, v230\n"
    "s_add_u32 s67, s67, 64\n"
    "s_waitcnt lgkmcnt(0)\n"
    "ds_write_b16 v245, v230\n"
    "ds_read_b64 v[228:229], v246\n"
    "buffer_load_dwordx4 v[236:237], s[40:43], 0 idxen offen offset:0 nt lds\n"
    "buffer_load_dwordx4 v[238:239], s[40:43], 0 idxen offen offset:1024 nt lds\n"
    ".Ldin_B2:\n"
    "v_mfma_f32_16x16x32_bf16 v[144:147], v[0:3], v[160:163], v[112:115]\n"
    "v_exp_f32 v128, v128 clamp\n"
    "v_mfma_f32_16x16x32_bf16 v[144:147], v[4:7], v[164:167], v[144:147]\n"
    "v_exp_f32 v129, v129 clamp\n"
    "v_mfma_f32_16x16x32_bf16 v[148:151], v[8:11], v[160:163], v[116:119]\n"
    "v_exp_f32 v130, v130 clamp\n"
    "v_mfma_f32_16x16x32_bf16 v[148:151], v[12:15], v[164:167], v[148:151]\n"
    "v_exp_f32 v131, v131 clamp\n"
    "v_mfma_f32_16x16x32_bf16 v[152:155], v[16:19], v[160:163], v[120:123]\n"
    "v_exp_f32 v132, v132 clamp\n"
    "v_mfma_f32_16x16x32_bf16 v[152:155], v[20:23], v[164:167], v[152:155]\n"
    "v_exp_f32 v133, v133 clamp\n"
    "v_mfma_f32_16x16x32_bf16 v[156:159], v[24:27], v[160:163], v[124:127]\n"
    "v_exp_f32 v134, v134 clamp\n"
    "v_mfma_f32_16x16x32_bf16 v[156:159], v[28:31], v[164:167], v[156:159]\n"
    "v_exp_f32 v135, v135 clamp\n"
    "v_exp_f32 v136, v136 clamp\n"
    "s_cmp_eq_u32 s67, s80\n"
    "s_cselect_b32 s67, s65, s67\n"
    "s_and_b32 s94, s59, 63\n"
    "s_mul_i32 s94, s94, 272\n"
    "v_add_u32 v252, s94, v250\n"
    "s_add_u32 m0, s68, 6144\n"
    "v_exp_f32 v137, v137 clamp\n"
    "v_add_u32 v253, s67, v244\n"
    "s_waitcnt lgkmcnt(0)\n"
    "v_mfma_f32_4x4x4_16b_bf16 v[224:227], v[228:229], v[176:177], 0 cbsz:4 abid:0\n"
    "v_exp_f32 v138, v138 clamp\n"
    "v_exp_f32 v139, v139 clamp\n"
    "v_mfma_f32_4x4x4_16b_bf16 v[224:227], v[228:229], v[178:179], v[224:227] cbsz:4 abid:1\n"
    "v_exp_f32 v140, v140 clamp\n"
    "v_exp_f32 v141, v141 clamp\n"
    "v_mfma_f32_4x4x4_16b_bf16 v[224:227], v[228:229], v[180:181], v[224:227] cbsz:4 abid:2\n"
    "v_exp_f32 v142, v142 clamp\n"
    "v_exp_f32 v143, v143 clamp\n"
    "v_mfma_f32_4x4x4_16b_bf16 v[224:227], v[228:229], v[182:183], v[224:227] cbsz:4 abid:3\n"
    "s_waitcnt vmcnt(4)\n"
    "s_memrealtime s[34:35]\n"
    "ds_read_b64_tr_b16 v[176:177], v240 offset:6144\n"
    "ds_read_b64_tr_b16 v[178:179], v241 offset:6400\n"
    "ds_read_b64_tr_b16 v[180:181], v242 offset:6656\n"
    "ds_read_b64_tr_b16 v[182:183], v243 offset:6912\n"
    "ds_read_b32 v236, v253 offset:0\n"
    "ds_read_b32 v238, v253 offset:4\n"
    "ds_read_b128 v[160:163], v234 offset:0\n"
    "ds_read_b128 v[164:167], v235 offset:0\n"
    "ds_read_b128 v[184:187], v252 offset:0\n"
    "ds_read_b128 v[188:191], v252 offset:16\n"
    "v_pk_fma_f32 v[128:129], v[128:129], s[70:71], v[96:97]\n"
    "v_pk_fma_f32 v[130:131], v[130:131], s[70:71], v[98:99]\n"
    "v_pk_add_f32 v[230:231], v[128:129], v[130:131]\n"
    "v_pk_mul_f32 v[232:233], v[128:129], v[130:131]\n"
    "v_pk_fma_f32 v[132:133], v[132:133], s[70:71], v[100:101]\n"
    "v_pk_fma_f32 v[230:231], v[230:231], v[132:133], v[232:233]\n"
    "v_pk_mul_f32 v[232:233], v[232:233], v[132:133]\n"
    "v_pk_fma_f32 v[134:135], v[134:135], s[70:71], v[102:103]\n"
    "v_pk_fma_f32 v[230:231], v[230:231], v[134:135], v[232:233]\n"
    "v_pk_mul_f32 v[232:233], v[232:233], v[134:135]\n"
    "v_pk_fma_f32 v[136:137], v[136:137], s[70:71], v[104:105]\n"
    "v_pk_fma_f32 v[230:231], v[230:231], v[136:137], v[232:233]\n"
    "v_pk_mul_f32 v[232:233], v[232:233], v[136:137]\n"
    "v_pk_fma_f32 v[138:139], v[138:139], s[70:71], v[106:107]\n"
    "v_pk_fma_f32 v[230:231], v[230:231], v[138:139], v[232:233]\n"
    "v_pk_mul_f32 v[232:233], v[232:233], v[138:139]\n"
    "v_pk_fma_f32 v[140:141], v[140:141], s[70:71], v[108:109]\n"
    "v_pk_fma_f32 v[230:231], v[230:231], v[140:141], v[232:233]\n"
    "v_pk_mul_f32 v[232:233], v[232:233], v[140:141]\n"
    "v_pk_fma_f32 v[142:143], v[142:143], s[70:71], v[110:111]\n"
    "v_pk_fma_f32 v[230:231], v[230:231], v[142:143], v[232:233]\n"
    "v_pk_mul_f32 v[232:233], v[232:233], v[142:143]\n"
    "v_rcp_f32 v232, v232\n"
    "v_rcp_f32 v233, v233\n"
    "v_fma_f32 v230, v230, v232, v247\n"
    "v_fmac_f32 v230, v231, v233\n"
    "v_cvt_pk_bf16_f32 v230, v230, v230\n"
    "s_add_u32 s67, s67, 64\n"
    "s_waitcnt lgkmcnt(0)\n"
    "ds_write_b16 v245, v230\n"
    "ds_read_b64 v[228:229], v246\n"
    "s_cmp_le_u32 s34, s76\n"
    "s_cbranch_scc1 .Ldin_B2_wait\n"
    ".Ldin_B2_go:\n"
    "s_sub_u32 s94, s34, 60\n"
    "s_max_u32 s76, s76, s94\n"
    "s_add_u32 s76, s76, s72\n"
    ".Ldin_B2_dma:\n"
    "buffer_load_dwordx4 v[236:237], s[40:43], 0 idxen offen offset:0 nt lds\n"
    "buffer_load_dwordx4 v[238:239], s[40:43], 0 idxen offen offset:1024 nt lds\n"
    ".Ldin_B2_nd:\n"
    "s_branch .Ldin_G3\n"
    ".Ldin_L0_poll:\n"
    "s_sleep 1\n"
    "ds_read_b64 v[232:233], v253\n"
    "s_waitcnt lgkmcnt(0)\n"
    "v_readfirstlane_b32 s60, v232\n"
    "s_cmp_eq_u32 s60, -1\n"
    "s_cbranch_scc1 .Ldin_L0_poll\n"
    "s_branch .Ldin_L0_pr\n"
    ".Ldin_B2_wait:\n"
    "s_cmp_lt_i32 s59, s57\n"
    "s_cbranch_scc1 .Ldin_B2_w2\n"
    "s_sub_u32 s94, s67, s65\n"
    "s_cmp_gt_u32 s94, 256\n"
    "s_cbranch_scc1 .Ldin_B2_dma\n"
    "s_cmp_eq_u32 s94, 64\n"
    "s_waitcnt vmcnt(2)\n"
    "s_cbranch_scc1 .Ldin_B2_nd\n"
    "s_waitcnt vmcnt(0)\n"
    "s_branch .Ldin_B2_nd\n"
    ".Ldin_B2_slp:\n"
    "s_sleep 1\n"
    ".Ldin_B2_w2:\n"
    "s_memrealtime s[34:35]\n"
    "s_waitcnt lgkmcnt(0)\n"
    "s_sub_u32 s94, s34, s76\n"
    "s_cmp_lt_i32 s94, 0\n"
    "s_cbranch_scc1 .Ldin_B2_slp\n"
    "s_branch .Ldin_B2_go\n"
    ".p2align 5\n"
    ".Ldin_L3:\n"
    "s_setprio 0\n"
    "s_cmp_ge_i32 s59, s57\n"
    "s_cbranch_scc1 .Ldin_E1\n"
    "s_add_u32 s76, s76, s73\n"
    "v_mov_b32 v253, s63\n"
    "v_and_b32 v184, v254, v184\n"
    "v_and_b32 v185, v255, v185\n"
    "v_and_b32 v186, v254, v186\n"
    "v_and_b32 v187, v255, v187\n"
    "v_and_b32 v188, v254, v188\n"
    "v_and_b32 v189, v255, v189\n"
    "v_and_b32 v190, v254, v190\n"
    "v_and_b32_e64 v191, v255, v191\n"
    "ds_read_b64 v[232:233], v253\n"
    "v_mfma_f32_4x4x4_16b_bf16 v[128:131], v[184:185], v[32:33], v[64:67]\n"
    "ds_read_b128 v[112:115], v252 offset:17408\n"
    "v_mfma_f32_4x4x4_16b_bf16 v[132:135], v[186:187], v[34:35], v[68:71]\n"
    "ds_read_b128 v[116:119], v252 offset:17424\n"
    "v_mfma_f32_4x4x4_16b_bf16 v[136:139], v[188:189], v[36:37], v[72:75]\n"
    "ds_read_b128 v[120:123], v252 offset:17536\n"
    "ds_read_b128 v[124:127], v252 offset:17552\n"
    "v_cvt_pk_bf16_f32 v0, v128, v129\n"
    "v_cvt_pk_bf16_f32 v1, v130, v131\n"
    "v_mfma_f32_4x4x4_16b_bf16 v[140:143], v[190:191], v[38:39], v[76:79]\n"
    "v_cvt_pk_bf16_f32 v2, v132, v133\n"
    "v_cvt_pk_bf16_f32 v3, v134, v135\n"
    "v_mfma_f32_4x4x4_16b_bf16 v[128:131], v[184:185], v[40:41], v[80:83]\n"
    "v_cvt_pk_bf16_f32 v4, v136, v137\n"
    "v_cvt_pk_bf16_f32 v5, v138, v139\n"
    "v_mfma_f32_4x4x4_16b_bf16 v[132:135], v[186:187], v[42:43], v[84:87]\n"
    "v_cvt_pk_bf16_f32 v6, v140, v141\n"
    "v_cvt_pk_bf16_f32 v7, v142, v143\n"
    "v_mfma_f32_4x4x4_16b_bf16 v[136:139], v[188:189], v[44:45], v[88:91]\n"
    "v_cvt_pk_bf16_f32 v8, v128, v129\n"
    "v_cvt_pk_bf16_f32 v9, v130, v131\n"
    "v_mfma_f32_4x4x4_16b_bf16 v[140:143], v[190:191], v[46:47], v[92:95]\n"
    "v_cvt_pk_bf16_f32 v10, v132, v133\n"
    "v_cvt_pk_bf16_f32 v11, v134, v135\n"
    "v_mfma_f32_4x4x4_16b_bf16 v[128:131], v[184:185], v[48:49], v[192:195]\n"
    "v_cvt_pk_bf16_f32 v12, v136, v137\n"
    "v_cvt_pk_bf16_f32 v13, v138, v139\n"
    "v_mfma_f32_4x4x4_16b_bf16 v[132:135], v[186:187], v[50:51], v[196:199]\n"
    "v_cvt_pk_bf16_f32 v14, v140, v141\n"
    "v_cvt_pk_bf16_f32 v15, v142, v143\n"
    "v_mfma_f32_4x4x4_16b_bf16 v[136:139], v[188:189], v[52:53], v[200:203]\n"
    "s_waitcnt lgkmcnt(0)\n"
    "v_readfirstlane_b32 s60, v232\n"
    "v_cvt_pk_bf16_f32 v16, v128, v129\n"
    "v_cvt_pk_bf16_f32 v17, v130, v131\n"
    "v_mfma_f32_4x4x4_16b_bf16 v[140:143], v[190:191], v[54:55], v[204:207]\n"
    "s_cmp_eq_u32 s60, -1\n"
    "s_cbranch_scc1 .Ldin_L3_poll\n"
    ".Ldin_L3_pr:\n"
    "v_readfirstlane_b32 s95, v233\n"
    "s_and_b32 s94, s60, 63\n"
    "s_lshl_b32 s94, s94, 6\n"
    "s_add_u32 s94, s94, 68480\n"
    "s_add_u32 s63, s63, 8\n"
    "v_lshl_add_u32 v252, v249, 1, s94\n"
    "ds_read_b32 v252, v252\n"
    "s_sub_u32 s95, s95, s76\n"
    "s_ashr_i32 s95, s95, 4\n"
    "s_min_i32 s95, s95, s74\n"
    "s_max_i32 s72, s95, s75\n"
    "v_cvt_pk_bf16_f32 v18, v132, v133\n"
    "v_cvt_pk_bf16_f32 v19, v134, v135\n"
    "v_mfma_f32_4x4x4_16b_bf16 v[128:131], v[184:185], v[56:57], v[208:211]\n"
    "v_cvt_pk_bf16_f32 v20, v136, v137\n"
    "v_cvt_pk_bf16_f32 v21, v138, v139\n"
    "v_mfma_f32_4x4x4_16b_bf16 v[132:135], v[186:187], v[58:59], v[212:215]\n"
    "v_cvt_pk_bf16_f32 v22, v140, v141\n"
    "v_cvt_pk_bf16_f32 v23, v142, v143\n"
    "v_mfma_f32_4x4x4_16b_bf16 v[136:139], v[188:189], v[60:61], v[216:219]\n"
    "v_cvt_pk_bf16_f32 v24, v128, v129\n"
    "v_cvt_pk_bf16_f32 v25, v130, v131\n"
    "v_mfma_f32_4x4x4_16b_bf16 v[140:143], v[190:191], v[62:63], v[220:223]\n"
    "v_cvt_pk_bf16_f32 v26, v132, v133\n"
    "v_cvt_pk_bf16_f32 v27, v134, v135\n"
    "v_cvt_pk_bf16_f32 v28, v136, v137\n"
    "v_cvt_pk_bf16_f32 v29, v138, v139\n"
    "v_exp_f32 v152, v152 clamp\n"
    "v_cvt_pk_bf16_f32 v30, v140, v141\n"
    "v_cvt_pk_bf16_f32 v31, v142, v143\n"
    "v_mfma_f32_16x16x32_bf16 v[128:131], v[0:3], v[160:163], v[112:115]\n"
    "v_exp_f32 v144, v144 clamp\n"
    "v_mfma_f32_16x16x32_bf16 v[128:131], v[4:7], v[164:167], v[128:131]\n"
    "v_exp_f32 v145, v145 clamp\n"
    "v_mfma_f32_16x16x32_bf16 v[132:135], v[8:11], v[160:163], v[116:119]\n"
    "v_exp_f32 v146, v146 clamp\n"
    "v_mfma_f32_16x16x32_bf16 v[132:135], v[12:15], v[164:167], v[132:135]\n"
    "v_exp_f32 v147, v147 clamp\n"
    "v_mfma_f32_16x16x32_bf16 v[136:139], v[16:19], v[160:163], v[120:123]\n"
    "v_exp_f32 v148, v148 clamp\n"
    "v_mfma_f32_16x16x32_bf16 v[136:139], v[20:23], v[164:167], v[136:139]\n"
    "v_exp_f32 v149, v149 clamp\n"
    "v_mfma_f32_16x16x32_bf16 v[140:143], v[24:27], v[160:163], v[124:127]\n"
    "v_exp_f32 v150, v150 clamp\n"
    "v_mfma_f32_16x16x32_bf16 v[140:143], v[28:31], v[164:167], v[140:143]\n"
    "v_exp_f32 v151, v151 clamp\n"
    "s_add_u32 m0, s68, 0\n"
    "v_exp_f32 v153, v153 clamp\n"
    "v_add_u32_e64 v253, s67, v244\n"
    "v_mfma_f32_4x4x4_16b_bf16 v[224:227], v[228:229], v[168:169], v[224:227] cbsz:4 abid:0\n"
    "v_exp_f32 v154, v154 clamp\n"
    "v_exp_f32 v155, v155 clamp\n"
    "v_mfma_f32_4x4x4_16b_bf16 v[224:227], v[228:229], v[170:171], v[224:227] cbsz:4 abid:1\n"
    "v_exp_f32 v156, v156 clamp\n"
    "v_exp_f32 v157, v157 clamp\n"
    "v_mfma_f32_4x4x4_16b_bf16 v[224:227], v[228:229], v[172:173], v[224:227] cbsz:4 abid:2\n"
    "v_exp_f32 v158, v158 clamp\n"
    "v_exp_f32 v159, v159 clamp\n"
    "v_mfma_f32_4x4x4_16b_bf16 v[224:227], v[228:229], v[174:175], v[224:227] cbsz:4 abid:3\n"
    "s_waitcnt vmcnt(4)\n"
    "ds_read_b64_tr_b16 v[168:169], v240 offset:0\n"
    "ds_read_b64_tr_b16 v[170:171], v241 offset:256\n"
    "ds_read_b64_tr_b16 v[172:173], v242 offset:512\n"
    "ds_read_b64_tr_b16 v[174:175], v243 offset:768\n"
    "ds_read_b32 v236, v253 offset:0\n"
    "ds_read_b32 v238, v253 offset:4\n"
    "ds_read_b128 v[160:163], v234 offset:2048\n"
    "ds_read_b128 v[164:167], v235 offset:2048\n"
    "s_waitcnt lgkmcnt(8)\n"
    "s_nop 0\n"
    "v_readlane_b32 s20, v252, 0\n"
    "v_readlane_b32 s21, v252, 1\n"
    "v_readlane_b32 s22, v252, 2\n"
    "v_readlane_b32 s23, v252, 3\n"
    "v_readlane_b32 s24, v252, 4\n"
    "v_readlane_b32 s25, v252, 5\n"
    "v_readlane_b32 s26, v252, 6\n"
    "v_readlane_b32 s27, v252, 7\n"
    "v_pk_fma_f32 v[144:145], v[144:145], s[70:71], v[96:97]\n"
    "v_pk_fma_f32 v[146:147], v[146:147], s[70:71], v[98:99]\n"
    "v_pk_add_f32 v[230:231], v[144:145], v[146:147]\n"
    "v_pk_mul_f32 v[232:233], v[144:145], v[146:147]\n"
    "v_pk_fma_f32 v[148:149], v[148:149], s[70:71], v[100:101]\n"
    "v_pk_fma_f32 v[230:231], v[230:231], v[148:149], v[232:233]\n"
    "v_pk_mul_f32 v[232:233], v[232:233], v[148:149]\n"
    "v_pk_fma_f32 v[150:151], v[150:151], s[70:71], v[102:103]\n"
    "v_pk_fma_f32 v[230:231], v[230:231], v[150:151], v[232:233]\n"
    "v_pk_mul_f32 v[232:233], v[232:233], v[150:151]\n"
    "v_pk_fma_f32 v[152:153], v[152:153], s[70:71], v[104:105]\n"
    "v_pk_fma_f32 v[230:231], v[230:231], v[152:153], v[232:233]\n"
    "v_pk_mul_f32 v[232:233], v[232:233], v[152:153]\n"
    "v_pk_fma_f32 v[154:155], v[154:155], s[70:71], v[106:107]\n"
    "v_pk_fma_f32 v[230:231], v[230:231], v[154:155], v[232:233]\n"
    "v_pk_mul_f32 v[232:233], v[232:233], v[154:155]\n"
    "v_pk_fma_f32 v[156:157], v[156:157], s[70:71], v[108:109]\n"
    "v_pk_fma_f32 v[230:231], v[230:231], v[156:157], v[232:233]\n"
    "v_pk_mul_f32 v[232:233], v[232:233], v[156:157]\n"
    "v_pk_fma_f32 v[158:159], v[158:159], s[70:71], v[110:111]\n"
    "v_pk_fma_f32 v[230:231], v[230:231], v[158:159], v[232:233]\n"
    "v_pk_mul_f32 v[232:233], v[232:233], v[158:159]\n"
    "v_rcp_f32 v232, v232\n"
    "v_rcp_f32 v233, v233\n"
    "v_fma_f32 v230, v230, v232, v247\n"
    "v_fmac_f32 v230, v231, v233\n"
    "v_cvt_pk_bf16_f32 v230, v230, v230\n"
    "s_add_u32 s67, s67, 64\n"
    "s_waitcnt lgkmcnt(0)\n"
    "ds_write_b16 v245, v230\n"
    "ds_read_b64 v[228:229], v246\n"
    "s_lshl1_add_u32 s76, s72, s76\n"
    "buffer_load_dwordx4 v[236:237], s[40:43], 0 idxen offen offset:0 nt lds\n"
    "buffer_load_dwordx4 v[238:239], s[40:43], 0 idxen offen offset:1024 nt lds\n"
    "s_and_b32 s62, s58, 63\n"
    "s_mul_i32 s62, s62, 272\n"
    "s_mov_b32 s58, s59\n"
    "s_mov_b32 s59, s60\n"
    "s_mov_b32 s81, s65\n"
    "s_xor_b32 s65, s65, s64\n"
    "s_mov_b32 s80, s83\n"
    "s_add_u32 s82, s65, 256\n"
    "s_cmp_lt_i32 s59, s57\n"
    "s_cselect_b32 s76, s76, -1\n"
    "s_bitcmp1_b32 s58, 30\n"
    "s_cbranch_scc1 .Ldin_A0s\n"
    "s_branch .Ldin_A0\n"
    ".Ldin_L3_poll:\n"
    "s_sleep 1\n"
    "ds_read_b64 v[232:233], v253\n"
    "s_waitcnt lgkmcnt(0)\n"
    "v_readfirstlane_b32 s60, v232\n"
    "s_cmp_eq_u32 s60, -1\n"
    "s_cbranch_scc1 .Ldin_L3_poll\n"
    "s_branch .Ldin_L3_pr\n"
    ".p2align 5\n"
    ".Ldin_E0:\n"
    "v_exp_f32 v128, v128 clamp\n"
    "v_exp_f32 v129, v129 clamp\n"
    "v_exp_f32 v130, v130 clamp\n"
    "v_exp_f32 v131, v131 clamp\n"
    "v_exp_f32 v132, v132 clamp\n"
    "v_exp_f32 v133, v133 clamp\n"
    "v_exp_f32 v134, v134 clamp\n"
    "v_exp_f32 v135, v135 clamp\n"
    "s_and_b32 s62, s58, 63\n"
    "s_mul_i32 s62, s62, 272\n"
    "s_waitcnt lgkmcnt(0)\n"
    "v_mfma_f32_4x4x4_16b_bf16 v[224:227], v[228:229], v[176:177], v[224:227] cbsz:4 abid:0\n"
    "v_exp_f32 v136, v136 clamp\n"
    "v_exp_f32 v137, v137 clamp\n"
    "v_mfma_f32_4x4x4_16b_bf16 v[224:227], v[228:229], v[178:179], v[224:227] cbsz:4 abid:1\n"
    "v_exp_f32 v138, v138 clamp\n"
    "v_exp_f32 v139, v139 clamp\n"
    "v_mfma_f32_4x4x4_16b_bf16 v[224:227], v[228:229], v[180:181], v[224:227] cbsz:4 abid:2\n"
    "v_exp_f32 v140, v140 clamp\n"
    "v_exp_f32 v141, v141 clamp\n"
    "v_mfma_f32_4x4x4_16b_bf16 v[224:227], v[228:229], v[182:183], v[224:227] cbsz:4 abid:3\n"
    "v_exp_f32 v142, v142 clamp\n"
    "v_exp_f32 v143, v143 clamp\n"
    "v_pk_fma_f32 v[128:129], v[128:129], s[70:71], v[96:97]\n"
    "v_pk_fma_f32 v[130:131], v[130:131], s[70:71], v[98:99]\n"
    "v_pk_add_f32 v[230:231], v[128:129], v[130:131]\n"
    "v_pk_mul_f32 v[232:233], v[128:129], v[130:131]\n"
    "v_pk_fma_f32 v[132:133], v[132:133], s[70:71], v[100:101]\n"
    "v_pk_fma_f32 v[230:231], v[230:231], v[132:133], v[232:233]\n"
    "v_pk_mul_f32 v[232:233], v[232:233], v[132:133]\n"
    "v_pk_fma_f32 v[134:135], v[134:135], s[70:71], v[102:103]\n"
    "v_pk_fma_f32 v[230:231], v[230:231], v[134:135], v[232:233]\n"
    "v_pk_mul_f32 v[232:233], v[232:233], v[134:135]\n"
    "v_pk_fma_f32 v[136:137], v[136:137], s[70:71], v[104:105]\n"
    "v_pk_fma_f32 v[230:231], v[230:231], v[136:137], v[232:233]\n"
    "v_pk_mul_f32 v[232:233], v[232:233], v[136:137]\n"
    "v_pk_fma_f32 v[138:139], v[138:139], s[70:71], v[106:107]\n"
    "v_pk_fma_f32 v[230:231], v[230:231], v[138:139], v[232:233]\n"
    "v_pk_mul_f32 v[232:233], v[232:233], v[138:139]\n"
    "v_pk_fma_f32 v[140:141], v[140:141], s[70:71], v[108:109]\n"
    "v_pk_fma_f32 v[230:231], v[230:231], v[140:141], v[232:233]\n"
    "v_pk_mul_f32 v[232:233], v[232:233], v[140:141]\n"
    "v_pk_fma_f32 v[142:143], v[142:143], s[70:71], v[110:111]\n"
    "v_pk_fma_f32 v[230:231], v[230:231], v[142:143], v[232:233]\n"
    "v_pk_mul_f32 v[232:233], v[232:233], v[142:143]\n"
    "v_rcp_f32 v232, v232\n"
    "v_rcp_f32 v233, v233\n"
    "v_fma_f32 v230, v230, v232, v247\n"
    "v_fmac_f32 v230, v231, v233\n"
    "v_cvt_pk_bf16_f32 v230, v230, v230\n"
    "ds_write_b16 v245, v230\n"
    "ds_read_b64 v[228:229], v246\n"
    ".Ldin_drain0:\n"
    "v_add_u32 v253, s66, v244\n"
    "ds_read_b32 v253, v253\n"
    "v_lshlrev_b32 v202, 3, v249\n"
    "s_waitcnt lgkmcnt(0)\n"
    "v_mfma_f32_4x4x4_16b_bf16 v[224:227], v[228:229], v[168:169], v[224:227] cbsz:4 abid:0\n"
    "v_and_b32_e64 v201, 63, v253\n"
    "v_and_b32 v202, 0x70, v202\n"
    "v_mfma_f32_4x4x4_16b_bf16 v[224:227], v[228:229], v[170:171], v[224:227] cbsz:4 abid:1\n"
    "v_and_b32 v200, 0x3fffffc0, v253\n"
    "v_mul_u32_u24 v201, 272, v201\n"
    "v_mfma_f32_4x4x4_16b_bf16 v[224:227], v[228:229], v[172:173], v[224:227] cbsz:4 abid:2\n"
    "v_lshl_add_u32 v200, v200, 1, v202\n"
    "v_add_u32 v201, v201, v202\n"
    "v_mfma_f32_4x4x4_16b_bf16 v[224:227], v[228:229], v[174:175], v[224:227] cbsz:4 abid:3\n"
    "v_cmp_gt_u32 vcc, s57, v253\n"
    "v_bfrev_b32 v202, 1\n"
    "s_nop 0\n"
    "v_cndmask_b32 v200, v202, v200, vcc\n"
    "s_nop 0\n"
    "v_add_f32 v232, v224, v225\n"
    "v_add_f32 v233, v226, v227\n"
    "v_add_f32 v232, v232, v233\n"
    "v_add_u32 v233, s62, v249\n"
    "v_mul_f32 v232, s69, v232\n"
    "v_cvt_pk_bf16_f32 v232, v232, v232\n"
    "ds_write_b16 v233, v232 offset:32768\n"
    "ds_read_b128 v[216:219], v201 offset:32768\n"
    "s_waitcnt vmcnt(0) lgkmcnt(0)\n"
    "buffer_store_dwordx4 v[216:219], v200, s[52:55], 0 offen sc0 sc1 nt\n"
    "s_branch .Ldin_exit\n"
    ".p2align 5\n"
    ".Ldin_E1:\n"
    "v_exp_f32 v144, v144 clamp\n"
    "v_exp_f32 v145, v145 clamp\n"
    "v_exp_f32 v146, v146 clamp\n"
    "v_exp_f32 v147, v147 clamp\n"
    "v_exp_f32 v148, v148 clamp\n"
    "v_exp_f32 v149, v149 clamp\n"
    "v_exp_f32 v150, v150 clamp\n"
    "v_exp_f32 v151, v151 clamp\n"
    "s_and_b32 s62, s58, 63\n"
    "s_mul_i32 s62, s62, 272\n"
    "s_waitcnt lgkmcnt(0)\n"
    "v_mfma_f32_4x4x4_16b_bf16 v[224:227], v[228:229], v[168:169], v[224:227] cbsz:4 abid:0\n"
    "v_exp_f32 v152, v152 clamp\n"
    "v_exp_f32 v153, v153 clamp\n"
    "v_mfma_f32_4x4x4_16b_bf16 v[224:227], v[228:229], v[170:171], v[224:227] cbsz:4 abid:1\n"
    "v_exp_f32 v154, v154 clamp\n"
    "v_exp_f32 v155, v155 clamp\n"
    "v_mfma_f32_4x4x4_16b_bf16 v[224:227], v[228:229], v[172:173], v[224:227] cbsz:4 abid:2\n"
    "v_exp_f32 v156, v156 clamp\n"
    "v_exp_f32 v157, v157 clamp\n"
    "v_mfma_f32_4x4x4_16b_bf16 v[224:227], v[228:229], v[174:175], v[224:227] cbsz:4 abid:3\n"
    "v_exp_f32 v158, v158 clamp\n"
    "v_exp_f32 v159, v159 clamp\n"
    "v_pk_fma_f32 v[144:145], v[144:145], s[70:71], v[96:97]\n"
    "v_pk_fma_f32 v[146:147], v[146:147], s[70:71], v[98:99]\n"
    "v_pk_add_f32 v[230:231], v[144:145], v[146:147]\n"
    "v_pk_mul_f32 v[232:233], v[144:145], v[146:147]\n"
    "v_pk_fma_f32 v[148:149], v[148:149], s[70:71], v[100:101]\n"
    "v_pk_fma_f32 v[230:231], v[230:231], v[148:149], v[232:233]\n"
    "v_pk_mul_f32 v[232:233], v[232:233], v[148:149]\n"
    "v_pk_fma_f32 v[150:151], v[150:151], s[70:71], v[102:103]\n"
    "v_pk_fma_f32 v[230:231], v[230:231], v[150:151], v[232:233]\n"
    "v_pk_mul_f32 v[232:233], v[232:233], v[150:151]\n"
    "v_pk_fma_f32 v[152:153], v[152:153], s[70:71], v[104:105]\n"
    "v_pk_fma_f32 v[230:231], v[230:231], v[152:153], v[232:233]\n"
    "v_pk_mul_f32 v[232:233], v[232:233], v[152:153]\n"
    "v_pk_fma_f32 v[154:155], v[154:155], s[70:71], v[106:107]\n"
    "v_pk_fma_f32 v[230:231], v[230:231], v[154:155], v[232:233]\n"
    "v_pk_mul_f32 v[232:233], v[232:233], v[154:155]\n"
    "v_pk_fma_f32 v[156:157], v[156:157], s[70:71], v[108:109]\n"
    "v_pk_fma_f32 v[230:231], v[230:231], v[156:157], v[232:233]\n"
    "v_pk_mul_f32 v[232:233], v[232:233], v[156:157]\n"
    "v_pk_fma_f32 v[158:159], v[158:159], s[70:71], v[110:111]\n"
    "v_pk_fma_f32 v[230:231], v[230:231], v[158:159], v[232:233]\n"
    "v_pk_mul_f32 v[232:233], v[232:233], v[158:159]\n"
    "v_rcp_f32 v232, v232\n"
    "v_rcp_f32 v233, v233\n"
    "v_fma_f32 v230, v230, v232, v247\n"
    "v_fmac_f32 v230, v231, v233\n"
    "v_cvt_pk_bf16_f32 v230, v230, v230\n"
    "ds_write_b16 v245, v230\n"
    "ds_read_b64 v[228:229], v246\n"
    ".Ldin_drain1:\n"
    "v_add_u32 v253, s66, v244\n"
    "ds_read_b32 v253, v253\n"
    "v_lshlrev_b32 v202, 3, v249\n"
    "s_waitcnt lgkmcnt(0)\n"
    "v_mfma_f32_4x4x4_16b_bf16 v[224:227], v[228:229], v[176:177], v[224:227] cbsz:4 abid:0\n"
    "v_and_b32_e64 v201, 63, v253\n"
    "v_and_b32 v202, 0x70, v202\n"
    "v_mfma_f32_4x4x4_16b_bf16 v[224:227], v[228:229], v[178:179], v[224:227] cbsz:4 abid:1\n"
    "v_and_b32 v200, 0x3fffffc0, v253\n"
    "v_mul_u32_u24 v201, 272, v201\n"
    "v_mfma_f32_4x4x4_16b_bf16 v[224:227], v[228:229], v[180:181], v[224:227] cbsz:4 abid:2\n"
    "v_lshl_add_u32 v200, v200, 1, v202\n"
    "v_add_u32 v201, v201, v202\n"
    "v_mfma_f32_4x4x4_16b_bf16 v[224:227], v[228:229], v[182:183], v[224:227] cbsz:4 abid:3\n"
    "v_cmp_gt_u32 vcc, s57, v253\n"
    "v_bfrev_b32 v202, 1\n"
    "s_nop 0\n"
    "v_cndmask_b32 v200, v202, v200, vcc\n"
    "s_nop 0\n"
    "v_add_f32 v232, v224, v225\n"
    "v_add_f32 v233, v226, v227\n"
    "v_add_f32 v232, v232, v233\n"
    "v_add_u32 v233, s62, v249\n"
    "v_mul_f32 v232, s69, v232\n"
    "v_cvt_pk_bf16_f32 v232, v232, v232\n"
    "ds_write_b16 v233, v232 offset:32768\n"
    "ds_read_b128 v[216:219], v201 offset:32768\n"
    "s_waitcnt vmcnt(0) lgkmcnt(0)\n"
    "buffer_store_dwordx4 v[216:219], v200, s[52:55], 0 offen sc0 sc1 nt\n"
    "s_branch .Ldin_exit\n"
    ".Ldin_A0s:\n"
    "v_mfma_f32_16x16x32_bf16 v[144:147], v[0:3], v[160:163], v[112:115]\n"
    "v_exp_f32 v128, v128 clamp\n"
    "v_mfma_f32_16x16x32_bf16 v[144:147], v[4:7], v[164:167], v[144:147]\n"
    "v_exp_f32 v129, v129 clamp\n"
    "v_mfma_f32_16x16x32_bf16 v[148:151], v[8:11], v[160:163], v[116:119]\n"
    "v_exp_f32 v130, v130 clamp\n"
    "v_mfma_f32_16x16x32_bf16 v[148:151], v[12:15], v[164:167], v[148:151]\n"
    "v_exp_f32 v131, v131 clamp\n"
    "v_mfma_f32_16x16x32_bf16 v[152:155], v[16:19], v[160:163], v[120:123]\n"
    "v_exp_f32 v132, v132 clamp\n"
    "v_mfma_f32_16x16x32_bf16 v[152:155], v[20:23], v[164:167], v[152:155]\n"
    "v_exp_f32 v133, v133 clamp\n"
    "v_mfma_f32_16x16x32_bf16 v[156:159], v[24:27], v[160:163], v[124:127]\n"
    "v_exp_f32 v134, v134 clamp\n"
    "v_mfma_f32_16x16x32_bf16 v[156:159], v[28:31], v[164:167], v[156:159]\n"
    "v_exp_f32 v135, v135 clamp\n"
    "v_exp_f32 v136, v136 clamp\n"
    "s_add_u32 m0, s68, 2048\n"
    "v_exp_f32 v137, v137 clamp\n"
    "v_add_u32 v253, s67, v244\n"
    "s_waitcnt lgkmcnt(0)\n"
    "v_mfma_f32_4x4x4_16b_bf16 v[224:227], v[228:229], v[176:177], v[224:227] cbsz:4 abid:0\n"
    "v_exp_f32 v138, v138 clamp\n"
    "v_exp_f32 v139, v139 clamp\n"
    "v_mfma_f32_4x4x4_16b_bf16 v[224:227], v[228:229], v[178:179], v[224:227] cbsz:4 abid:1\n"
    "v_exp_f32 v140, v140 clamp\n"
    "v_exp_f32 v141, v141 clamp\n"
    "v_mfma_f32_4x4x4_16b_bf16 v[224:227], v[228:229], v[180:181], v[224:227] cbsz:4 abid:2\n"
    "v_exp_f32 v142, v142 clamp\n"
    "v_exp_f32 v143, v143 clamp\n"
    "v_mfma_f32_4x4x4_16b_bf16 v[224:227], v[228:229], v[182:183], v[224:227] cbsz:4 abid:3\n"
    "s_waitcnt vmcnt(4)\n"
    "ds_read_b64_tr_b16 v[176:177], v240 offset:2048\n"
    "ds_read_b64_tr_b16 v[178:179], v241 offset:2304\n"
    "ds_read_b64_tr_b16 v[180:181], v242 offset:2560\n"
    "ds_read_b64_tr_b16 v[182:183], v243 offset:2816\n"
    "ds_read_b32 v236, v253 offset:0\n"
    "ds_read_b32 v238, v253 offset:4\n"
    "ds_read_b128 v[160:163], v234 offset:4096\n"
    "ds_read_b128 v[164:167], v235 offset:4096\n"
    "s_cbranch_vccz .Ldin_A0s_np\n"
    "s_setprio 1\n"
    ".Ldin_A0s_np:\n"
    "v_pk_fma_f32 v[128:129], v[128:129], s[70:71], v[96:97]\n"
    "v_pk_fma_f32 v[130:131], v[130:131], s[70:71], v[98:99]\n"
    "v_add_f32 v191, v224, v225\n"
    "v_add_f32 v190, v226, v227\n"
    "v_add_f32 v191, v191, v190\n"
    "v_add_u32 v190, s62, v249\n"
    "v_mul_f32 v191, s69, v191\n"
    "v_cvt_pk_bf16_f32 v191, v191, v191\n"
    "ds_write_b16 v190, v191 offset:32768\n"
    "s_bfe_u32 s94, s59, 0x180006\n"
    "s_mul_i32 s86, s94, s56\n"
    "v_mbcnt_lo_u32_b32 v184, s20, 0\n"
    "v_mbcnt_lo_u32_b32 v185, s22, 0\n"
    "v_mbcnt_lo_u32_b32 v186, s24, 0\n"
    "v_mbcnt_lo_u32_b32 v187, s26, 0\n"
    "s_bcnt1_i32_b64 s90, s[20:21]\n"
    "s_bcnt1_i32_b64 s91, s[22:23]\n"
    "s_bcnt1_i32_b64 s92, s[24:25]\n"
    "s_bcnt1_i32_b64 s93, s[26:27]\n"
    "v_mbcnt_hi_u32_b32 v184, s21, v184\n"
    "v_mbcnt_hi_u32_b32 v185, s23, v185\n"
    "v_mbcnt_hi_u32_b32 v186, s25, v186\n"
    "v_mbcnt_hi_u32_b32 v187, s27, v187\n"
    "s_lshl2_add_u32 s90, s90, s65\n"
    "s_lshl2_add_u32 s91, s91, s90\n"
    "s_lshl2_add_u32 s92, s92, s91\n"
    "s_lshl2_add_u32 s93, s93, s92\n"
    "v_add_u32_e64 v188, s86, v251\n"
    "v_pk_add_f32 v[230:231], v[128:129], v[130:131]\n"
    "v_pk_mul_f32 v[232:233], v[128:129], v[130:131]\n"
    "v_pk_fma_f32 v[132:133], v[132:133], s[70:71], v[100:101]\n"
    "v_pk_fma_f32 v[230:231], v[230:231], v[132:133], v[232:233]\n"
    "v_pk_mul_f32 v[232:233], v[232:233], v[132:133]\n"
    "v_lshl_add_u32 v184, v184, 2, s65\n"
    "v_lshl_add_u32 v185, v185, 2, s90\n"
    "v_lshl_add_u32 v186, v186, 2, s91\n"
    "v_lshl_add_u32 v187, v187, 2, s92\n"
    "v_mov_b32 v191, -1\n"
    "v_lshl_add_u32 v190, v249, 1, s93\n"
    "ds_write2_b32 v190, v191, v191 offset1:64\n"
    "s_mov_b64 exec, s[20:21]\n"
    "ds_write_b32 v184, v188\n"
    "s_mov_b64 exec, s[22:23]\n"
    "v_add_u32 v189, 64, v188\n"
    "ds_write_b32 v185, v189\n"
    "s_mov_b64 exec, s[24:25]\n"
    "v_add_u32 v190, 128, v188\n"
    "ds_write_b32 v186, v190\n"
    "s_mov_b64 exec, s[26:27]\n"
    "v_add_u32 v189, 192, v188\n"
    "ds_write_b32 v187, v189\n"
    "s_mov_b64 exec, -1\n"
    "s_add_u32 s95, s93, 60\n"
    "s_add_u32 s94, s82, 128\n"
    "s_andn2_b32 s95, s95, 63\n"
    "s_max_u32 s83, s95, s94\n"
    "v_pk_fma_f32 v[134:135], v[134:135], s[70:71], v[102:103]\n"
    "v_pk_fma_f32 v[230:231], v[230:231], v[134:135], v[232:233]\n"
    "v_pk_mul_f32 v[232:233], v[232:233], v[134:135]\n"
    "v_pk_fma_f32 v[136:137], v[136:137], s[70:71], v[104:105]\n"
    "v_pk_fma_f32 v[230:231], v[230:231], v[136:137], v[232:233]\n"
    "v_pk_mul_f32 v[232:233], v[232:233], v[136:137]\n"
    "v_pk_fma_f32 v[138:139], v[138:139], s[70:71], v[106:107]\n"
    "v_pk_fma_f32 v[230:231], v[230:231], v[138:139], v[232:233]\n"
    "v_pk_mul_f32 v[232:233], v[232:233], v[138:139]\n"
    "v_pk_fma_f32 v[140:141], v[140:141], s[70:71], v[108:109]\n"
    "v_pk_fma_f32 v[230:231], v[230:231], v[140:141], v[232:233]\n"
    "v_pk_mul_f32 v[232:233], v[232:233], v[140:141]\n"
    "v_pk_fma_f32 v[142:143], v[142:143], s[70:71], v[110:111]\n"
    "v_pk_fma_f32 v[230:231], v[230:231], v[142:143], v[232:233]\n"
    "v_pk_mul_f32 v[232:233], v[232:233], v[142:143]\n"
    "v_rcp_f32 v232, v232\n"
    "v_rcp_f32 v233, v233\n"
    "v_fma_f32 v230, v230, v232, v247\n"
    "v_fmac_f32 v230, v231, v233\n"
    "v_lshlrev_b32 v232, 2, v249\n"
    "v_lshrrev_b32 v233, 2, v249\n"
    "v_and_b32 v232, 56, v232\n"
    "v_and_b32 v233, 4, v233\n"
    "v_add3_u32 v232, v232, v233, s81\n"
    "ds_read_b32 v232, v232\n"
    "s_add_u32 s81, s81, 64\n"
    "s_waitcnt lgkmcnt(0)\n"
    "v_lshlrev_b32 v232, 1, v232\n"
    "buffer_load_ushort v232, v232, s[44:47], 0 offen\n"
    "s_waitcnt vmcnt(0)\n"
    "v_lshlrev_b32 v232, 16, v232\n"
    "v_mul_f32 v230, v230, v232\n"
    "v_cvt_pk_bf16_f32 v230, v230, v230\n"
    "s_add_u32 s67, s67, 64\n"
    "s_waitcnt lgkmcnt(0)\n"
    "ds_write_b16 v245, v230\n"
    "ds_read_b64 v[228:229], v246\n"
    "buffer_load_dwordx4 v[236:237], s[40:43], 0 idxen offen offset:0 nt lds\n"
    "buffer_load_dwordx4 v[238:239], s[40:43], 0 idxen offen offset:1024 nt lds\n"
    ".Ldin_B1s:\n"
    "v_mfma_f32_16x16x32_bf16 v[128:131], v[0:3], v[160:163], v[112:115]\n"
    "v_exp_f32 v144, v144 clamp\n"
    "v_mfma_f32_16x16x32_bf16 v[128:131], v[4:7], v[164:167], v[128:131]\n"
    "v_exp_f32 v145, v145 clamp\n"
    "v_mfma_f32_16x16x32_bf16 v[132:135], v[8:11], v[160:163], v[116:119]\n"
    "v_exp_f32 v146, v146 clamp\n"
    "v_mfma_f32_16x16x32_bf16 v[132:135], v[12:15], v[164:167], v[132:135]\n"
    "v_exp_f32 v147, v147 clamp\n"
    "v_mfma_f32_16x16x32_bf16 v[136:139], v[16:19], v[160:163], v[120:123]\n"
    "v_exp_f32 v148, v148 clamp\n"
    "v_mfma_f32_16x16x32_bf16 v[136:139], v[20:23], v[164:167], v[136:139]\n"
    "v_exp_f32 v149, v149 clamp\n"
    "v_mfma_f32_16x16x32_bf16 v[140:143], v[24:27], v[160:163], v[124:127]\n"
    "v_exp_f32 v150, v150 clamp\n"
    "v_mfma_f32_16x16x32_bf16 v[140:143], v[28:31], v[164:167], v[140:143]\n"
    "v_exp_f32 v151, v151 clamp\n"
    "v_exp_f32 v152, v152 clamp\n"
    "s_cmp_eq_u32 s67, s80\n"
    "s_cselect_b32 s67, s65, s67\n"
    "s_and_b32 s94, s59, 63\n"
    "s_mul_i32 s94, s94, 272\n"
    "v_add_u32 v252, s94, v250\n"
    "s_add_u32 m0, s68, 4096\n"
    "v_exp_f32 v153, v153 clamp\n"
    "v_add_u32 v253, s67, v244\n"
    "s_waitcnt lgkmcnt(0)\n"
    "v_mfma_f32_4x4x4_16b_bf16 v[224:227], v[228:229], v[168:169], 0 cbsz:4 abid:0\n"
    "v_exp_f32 v154, v154 clamp\n"
    "v_exp_f32 v155, v155 clamp\n"
    "v_mfma_f32_4x4x4_16b_bf16 v[224:227], v[228:229], v[170:171], v[224:227] cbsz:4 abid:1\n"
    "v_exp_f32 v156, v156 clamp\n"
    "v_exp_f32 v157, v157 clamp\n"
    "v_mfma_f32_4x4x4_16b_bf16 v[224:227], v[228:229], v[172:173], v[224:227] cbsz:4 abid:2\n"
    "v_exp_f32 v158, v158 clamp\n"
    "v_exp_f32 v159, v159 clamp\n"
    "v_mfma_f32_4x4x4_16b_bf16 v[224:227], v[228:229], v[174:175], v[224:227] cbsz:4 abid:3\n"
    "s_waitcnt vmcnt(4)\n"
    "s_memrealtime s[34:35]\n"
    "ds_read_b64_tr_b16 v[168:169], v240 offset:4096\n"
    "ds_read_b64_tr_b16 v[170:171], v241 offset:4352\n"
    "ds_read_b64_tr_b16 v[172:173], v242 offset:4608\n"
    "ds_read_b64_tr_b16 v[174:175], v243 offset:4864\n"
    "ds_read_b32 v236, v253 offset:0\n"
    "ds_read_b32 v238, v253 offset:4\n"
    "ds_read_b128 v[160:163], v234 offset:6144\n"
    "ds_read_b128 v[164:167], v235 offset:6144\n"
    "ds_read_b128 v[184:187], v252 offset:0\n"
    "ds_read_b128 v[188:191], v252 offset:16\n"
    "v_pk_fma_f32 v[144:145], v[144:145], s[70:71], v[96:97]\n"
    "v_pk_fma_f32 v[146:147], v[146:147], s[70:71], v[98:99]\n"
    "v_pk_add_f32 v[230:231], v[144:145], v[146:147]\n"
    "v_pk_mul_f32 v[232:233], v[144:145], v[146:147]\n"
    "v_pk_fma_f32 v[148:149], v[148:149], s[70:71], v[100:101]\n"
    "v_pk_fma_f32 v[230:231], v[230:231], v[148:149], v[232:233]\n"
    "v_pk_mul_f32 v[232:233], v[232:233], v[148:149]\n"
    "v_pk_fma_f32 v[150:151], v[150:151], s[70:71], v[102:103]\n"
    "v_pk_fma_f32 v[230:231], v[230:231], v[150:151], v[232:233]\n"
    "v_pk_mul_f32 v[232:233], v[232:233], v[150:151]\n"
    "v_pk_fma_f32 v[152:153], v[152:153], s[70:71], v[104:105]\n"
    "v_pk_fma_f32 v[230:231], v[230:231], v[152:153], v[232:233]\n"
    "v_pk_mul_f32 v[232:233], v[232:233], v[152:153]\n"
    "v_pk_fma_f32 v[154:155], v[154:155], s[70:71], v[106:107]\n"
    "v_pk_fma_f32 v[230:231], v[230:231], v[154:155], v[232:233]\n"
    "v_pk_mul_f32 v[232:233], v[232:233], v[154:155]\n"
    "v_pk_fma_f32 v[156:157], v[156:157], s[70:71], v[108:109]\n"
    "v_pk_fma_f32 v[230:231], v[230:231], v[156:157], v[232:233]\n"
    "v_pk_mul_f32 v[232:233], v[232:233], v[156:157]\n"
    "v_pk_fma_f32 v[158:159], v[158:159], s[70:71], v[110:111]\n"
    "v_pk_fma_f32 v[230:231], v[230:231], v[158:159], v[232:233]\n"
    "v_pk_mul_f32 v[232:233], v[232:233], v[158:159]\n"
    "v_rcp_f32 v232, v232\n"
    "v_rcp_f32 v233, v233\n"
    "v_fma_f32 v230, v230, v232, v247\n"
    "v_fmac_f32 v230, v231, v233\n"
    "v_lshlrev_b32 v232, 2, v249\n"
    "v_lshrrev_b32 v233, 2, v249\n"
    "v_and_b32 v232, 56, v232\n"
    "v_and_b32 v233, 4, v233\n"
    "v_add3_u32 v232, v232, v233, s81\n"
    "ds_read_b32 v232, v232\n"
    "s_add_u32 s81, s81, 64\n"
    "s_waitcnt lgkmcnt(0)\n"
    "v_lshlrev_b32 v232, 1, v232\n"
    "buffer_load_ushort v232, v232, s[44:47], 0 offen\n"
    "s_waitcnt vmcnt(0)\n"
    "v_lshlrev_b32 v232, 16, v232\n"
    "v_mul_f32 v230, v230, v232\n"
    "v_cvt_pk_bf16_f32 v230, v230, v230\n"
    "s_add_u32 s67, s67, 64\n"
    "s_waitcnt lgkmcnt(0)\n"
    "ds_write_b16 v245, v230\n"
    "ds_read_b64 v[228:229], v246\n"
    "s_cmp_le_u32 s34, s76\n"
    "s_cbranch_scc1 .Ldin_B1s_wait\n"
    ".Ldin_B1s_go:\n"
    "s_sub_u32 s94, s34, 60\n"
    "s_max_u32 s76, s76, s94\n"
    "s_add_u32 s76, s76, s72\n"
    ".Ldin_B1s_dma:\n"
    "buffer_load_dwordx4 v[236:237], s[40:43], 0 idxen offen offset:0 nt lds\n"
    "buffer_load_dwordx4 v[238:239], s[40:43], 0 idxen offen offset:1024 nt lds\n"
    ".Ldin_B1s_nd:\n"
    ".Ldin_G2s:\n"
    "v_mfma_f32_16x16x32_bf16 v[144:147], v[0:3], v[160:163], v[112:115]\n"
    "v_exp_f32 v128, v128 clamp\n"
    "v_mfma_f32_16x16x32_bf16 v[144:147], v[4:7], v[164:167], v[144:147]\n"
    "v_exp_f32 v129, v129 clamp\n"
    "v_mfma_f32_16x16x32_bf16 v[148:151], v[8:11], v[160:163], v[116:119]\n"
    "v_exp_f32 v130, v130 clamp\n"
    "v_mfma_f32_16x16x32_bf16 v[148:151], v[12:15], v[164:167], v[148:151]\n"
    "v_exp_f32 v131, v131 clamp\n"
    "v_mfma_f32_16x16x32_bf16 v[152:155], v[16:19], v[160:163], v[120:123]\n"
    "v_exp_f32 v132, v132 clamp\n"
    "v_mfma_f32_16x16x32_bf16 v[152:155], v[20:23], v[164:167], v[152:155]\n"
    "v_exp_f32 v133, v133 clamp\n"
    "v_mfma_f32_16x16x32_bf16 v[156:159], v[24:27], v[160:163], v[124:127]\n"
    "v_exp_f32 v134, v134 clamp\n"
    "v_mfma_f32_16x16x32_bf16 v[156:159], v[28:31], v[164:167], v[156:159]\n"
    "v_exp_f32 v135, v135 clamp\n"
    "v_exp_f32 v136, v136 clamp\n"
    "s_cmp_eq_u32 s67, s80\n"
    "s_cselect_b32 s67, s65, s67\n"
    "s_add_u32 m0, s68, 6144\n"
    "v_exp_f32 v137, v137 clamp\n"
    "v_add_u32_e64 v253, s67, v244\n"
    "s_waitcnt lgkmcnt(0)\n"
    "v_mfma_f32_4x4x4_16b_bf16 v[224:227], v[228:229], v[176:177], v[224:227] cbsz:4 abid:0\n"
    "v_exp_f32 v138, v138 clamp\n"
    "v_exp_f32 v139, v139 clamp\n"
    "v_mfma_f32_4x4x4_16b_bf16 v[224:227], v[228:229], v[178:179], v[224:227] cbsz:4 abid:1\n"
    "v_exp_f32 v140, v140 clamp\n"
    "v_exp_f32 v141, v141 clamp\n"
    "v_mfma_f32_4x4x4_16b_bf16 v[224:227], v[228:229], v[180:181], v[224:227] cbsz:4 abid:2\n"
    "v_exp_f32 v142, v142 clamp\n"
    "v_exp_f32 v143, v143 clamp\n"
    "v_mfma_f32_4x4x4_16b_bf16 v[224:227], v[228:229], v[182:183], v[224:227] cbsz:4 abid:3\n"
    "s_waitcnt vmcnt(4)\n"
    "s_memrealtime s[34:35]\n"
    "ds_read_b64_tr_b16 v[176:177], v240 offset:6144\n"
    "ds_read_b64_tr_b16 v[178:179], v241 offset:6400\n"
    "ds_read_b64_tr_b16 v[180:181], v242 offset:6656\n"
    "ds_read_b64_tr_b16 v[182:183], v243 offset:6912\n"
    "ds_read_b32 v236, v253 offset:0\n"
    "ds_read_b32 v238, v253 offset:4\n"
    "ds_read_b128 v[160:163], v234 offset:0\n"
    "ds_read_b128 v[164:167], v235 offset:0\n"
    "v_pk_fma_f32 v[128:129], v[128:129], s[70:71], v[96:97]\n"
    "v_pk_fma_f32 v[130:131], v[130:131], s[70:71], v[98:99]\n"
    "v_pk_add_f32 v[230:231], v[128:129], v[130:131]\n"
    "v_pk_mul_f32 v[232:233], v[128:129], v[130:131]\n"
    "v_pk_fma_f32 v[132:133], v[132:133], s[70:71], v[100:101]\n"
    "v_pk_fma_f32 v[230:231], v[230:231], v[132:133], v[232:233]\n"
    "v_pk_mul_f32 v[232:233], v[232:233], v[132:133]\n"
    "v_pk_fma_f32 v[134:135], v[134:135], s[70:71], v[102:103]\n"
    "v_pk_fma_f32 v[230:231], v[230:231], v[134:135], v[232:233]\n"
    "v_pk_mul_f32 v[232:233], v[232:233], v[134:135]\n"
    "v_pk_fma_f32 v[136:137], v[136:137], s[70:71], v[104:105]\n"
    "v_pk_fma_f32 v[230:231], v[230:231], v[136:137], v[232:233]\n"
    "v_pk_mul_f32 v[232:233], v[232:233], v[136:137]\n"
    "v_pk_fma_f32 v[138:139], v[138:139], s[70:71], v[106:107]\n"
    "v_pk_fma_f32 v[230:231], v[230:231], v[138:139], v[232:233]\n"
    "v_pk_mul_f32 v[232:233], v[232:233], v[138:139]\n"
    "v_pk_fma_f32 v[140:141], v[140:141], s[70:71], v[108:109]\n"
    "v_pk_fma_f32 v[230:231], v[230:231], v[140:141], v[232:233]\n"
    "v_pk_mul_f32 v[232:233], v[232:233], v[140:141]\n"
    "v_pk_fma_f32 v[142:143], v[142:143], s[70:71], v[110:111]\n"
    "v_pk_fma_f32 v[230:231], v[230:231], v[142:143], v[232:233]\n"
    "v_pk_mul_f32 v[232:233], v[232:233], v[142:143]\n"
    "v_rcp_f32 v232, v232\n"
    "v_rcp_f32 v233, v233\n"
    "v_fma_f32 v230, v230, v232, v247\n"
    "v_fmac_f32 v230, v231, v233\n"
    "v_lshlrev_b32 v232, 2, v249\n"
    "v_lshrrev_b32 v233, 2, v249\n"
    "v_and_b32 v232, 56, v232\n"
    "v_and_b32 v233, 4, v233\n"
    "v_add3_u32 v232, v232, v233, s81\n"
    "ds_read_b32 v232, v232\n"
    "s_add_u32 s81, s81, 64\n"
    "s_waitcnt lgkmcnt(0)\n"
    "v_lshlrev_b32 v232, 1, v232\n"
    "buffer_load_ushort v232, v232, s[44:47], 0 offen\n"
    "s_waitcnt vmcnt(0)\n"
    "v_lshlrev_b32 v232, 16, v232\n"
    "v_mul_f32 v230, v230, v232\n"
    "v_cvt_pk_bf16_f32 v230, v230, v230\n"
    "s_add_u32 s67, s67, 64\n"
    "s_waitcnt lgkmcnt(0)\n"
    "ds_write_b16 v245, v230\n"
    "ds_read_b64 v[228:229], v246\n"
    "s_cmp_le_u32 s34, s76\n"
    "s_cbranch_scc1 .Ldin_G2s_wait\n"
    ".Ldin_G2s_go:\n"
    "s_sub_u32 s94, s34, 60\n"
    "s_max_u32 s76, s76, s94\n"
    "s_add_u32 s76, s76, s72\n"
    ".Ldin_G2s_dma:\n"
    "buffer_load_dwordx4 v[236:237], s[40:43], 0 idxen offen offset:0 nt lds\n"
    "buffer_load_dwordx4 v[238:239], s[40:43], 0 idxen offen offset:1024 nt lds\n"
    ".Ldin_G2s_nd:\n"
    "s_cmp_eq_u32 s67, s82\n"
    "s_cbranch_scc1 .Ldin_L3s\n"
    ".Ldin_G3s:\n"
    "v_mfma_f32_16x16x32_bf16 v[128:131], v[0:3], v[160:163], v[112:115]\n"
    "v_exp_f32 v144, v144 clamp\n"
    "v_mfma_f32_16x16x32_bf16 v[128:131], v[4:7], v[164:167], v[128:131]\n"
    "v_exp_f32 v145, v145 clamp\n"
    "v_mfma_f32_16x16x32_bf16 v[132:135], v[8:11], v[160:163], v[116:119]\n"
    "v_exp_f32 v146, v146 clamp\n"
    "v_mfma_f32_16x16x32_bf16 v[132:135], v[12:15], v[164:167], v[132:135]\n"
    "v_exp_f32 v147, v147 clamp\n"
    "v_mfma_f32_16x16x32_bf16 v[136:139], v[16:19], v[160:163], v[120:123]\n"
    "v_exp_f32 v148, v148 clamp\n"
    "v_mfma_f32_16x16x32_bf16 v[136:139], v[20:23], v[164:167], v[136:139]\n"
    "v_exp_f32 v149, v149 clamp\n"
    "v_mfma_f32_16x16x32_bf16 v[140:143], v[24:27], v[160:163], v[124:127]\n"
    "v_exp_f32 v150, v150 clamp\n"
    "v_mfma_f32_16x16x32_bf16 v[140:143], v[28:31], v[164:167], v[140:143]\n"
    "v_exp_f32 v151, v151 clamp\n"
    "v_exp_f32 v152, v152 clamp\n"
    "s_cmp_eq_u32 s67, s80\n"
    "s_cselect_b32 s67, s65, s67\n"
    "s_add_u32 m0, s68, 0\n"
    "v_exp_f32 v153, v153 clamp\n"
    "v_add_u32 v253, s67, v244\n"
    "s_waitcnt lgkmcnt(0)\n"
    "v_mfma_f32_4x4x4_16b_bf16 v[224:227], v[228:229], v[168:169], v[224:227] cbsz:4 abid:0\n"
    "v_exp_f32 v154, v154 clamp\n"
    "v_exp_f32 v155, v155 clamp\n"
    "v_mfma_f32_4x4x4_16b_bf16 v[224:227], v[228:229], v[170:171], v[224:227] cbsz:4 abid:1\n"
    "v_exp_f32 v156, v156 clamp\n"
    "v_exp_f32 v157, v157 clamp\n"
    "v_mfma_f32_4x4x4_16b_bf16 v[224:227], v[228:229], v[172:173], v[224:227] cbsz:4 abid:2\n"
    "v_exp_f32 v158, v158 clamp\n"
    "v_exp_f32 v159, v159 clamp\n"
    "v_mfma_f32_4x4x4_16b_bf16 v[224:227], v[228:229], v[174:175], v[224:227] cbsz:4 abid:3\n"
    "s_waitcnt vmcnt(4)\n"
    "s_memrealtime s[34:35]\n"
    "ds_read_b64_tr_b16 v[168:169], v240 offset:0\n"
    "ds_read_b64_tr_b16 v[170:171], v241 offset:256\n"
    "ds_read_b64_tr_b16 v[172:173], v242 offset:512\n"
    "ds_read_b64_tr_b16 v[174:175], v243 offset:768\n"
    "ds_read_b32 v236, v253 offset:0\n"
    "ds_read_b32 v238, v253 offset:4\n"
    "ds_read_b128 v[160:163], v234 offset:2048\n"
    "ds_read_b128 v[164:167], v235 offset:2048\n"
    "v_pk_fma_f32 v[144:145], v[144:145], s[70:71], v[96:97]\n"
    "v_pk_fma_f32 v[146:147], v[146:147], s[70:71], v[98:99]\n"
    "v_pk_add_f32 v[230:231], v[144:145], v[146:147]\n"
    "v_pk_mul_f32 v[232:233], v[144:145], v[146:147]\n"
    "v_pk_fma_f32 v[148:149], v[148:149], s[70:71], v[100:101]\n"
    "v_pk_fma_f32 v[230:231], v[230:231], v[148:149], v[232:233]\n"
    "v_pk_mul_f32 v[232:233], v[232:233], v[148:149]\n"
    "v_pk_fma_f32 v[150:151], v[150:151], s[70:71], v[102:103]\n"
    "v_pk_fma_f32 v[230:231], v[230:231], v[150:151], v[232:233]\n"
    "v_pk_mul_f32 v[232:233], v[232:233], v[150:151]\n"
    "v_pk_fma_f32 v[152:153], v[152:153], s[70:71], v[104:105]\n"
    "v_pk_fma_f32 v[230:231], v[230:231], v[152:153], v[232:233]\n"
    "v_pk_mul_f32 v[232:233], v[232:233], v[152:153]\n"
    "v_pk_fma_f32 v[154:155], v[154:155], s[70:71], v[106:107]\n"
    "v_pk_fma_f32 v[230:231], v[230:231], v[154:155], v[232:233]\n"
    "v_pk_mul_f32 v[232:233], v[232:233], v[154:155]\n"
    "v_pk_fma_f32 v[156:157], v[156:157], s[70:71], v[108:109]\n"
    "v_pk_fma_f32 v[230:231], v[230:231], v[156:157], v[232:233]\n"
    "v_pk_mul_f32 v[232:233], v[232:233], v[156:157]\n"
    "v_pk_fma_f32 v[158:159], v[158:159], s[70:71], v[110:111]\n"
    "v_pk_fma_f32 v[230:231], v[230:231], v[158:159], v[232:233]\n"
    "v_pk_mul_f32 v[232:233], v[232:233], v[158:159]\n"
    "v_rcp_f32 v232, v232\n"
    "v_rcp_f32 v233, v233\n"
    "v_fma_f32 v230, v230, v232, v247\n"
    "v_fmac_f32 v230, v231, v233\n"
    "v_lshlrev_b32 v232, 2, v249\n"
    "v_lshrrev_b32 v233, 2, v249\n"
    "v_and_b32 v232, 56, v232\n"
    "v_and_b32 v233, 4, v233\n"
    "v_add3_u32 v232, v232, v233, s81\n"
    "ds_read_b32 v232, v232\n"
    "s_add_u32 s81, s81, 64\n"
    "s_waitcnt lgkmcnt(0)\n"
    "v_lshlrev_b32 v232, 1, v232\n"
    "buffer_load_ushort v232, v232, s[44:47], 0 offen\n"
    "s_waitcnt vmcnt(0)\n"
    "v_lshlrev_b32 v232, 16, v232\n"
    "v_mul_f32 v230, v230, v232\n"
    "v_cvt_pk_bf16_f32 v230, v230, v230\n"
    "s_add_u32 s67, s67, 64\n"
    "s_waitcnt lgkmcnt(0)\n"
    "ds_write_b16 v245, v230\n"
    "ds_read_b64 v[228:229], v246\n"
    "s_cmp_le_u32 s34, s76\n"
    "s_cbranch_scc1 .Ldin_G3s_wait\n"
    ".Ldin_G3s_go:\n"
    "s_add_u32 s76, s76, s72\n"
    ".Ldin_G3s_dma:\n"
    "buffer_load_dwordx4 v[236:237], s[40:43], 0 idxen offen offset:0 nt lds\n"
    "buffer_load_dwordx4 v[238:239], s[40:43], 0 idxen offen offset:1024 nt lds\n"
    ".Ldin_G3s_nd:\n"
    "s_cmp_eq_u32 s67, s82\n"
    "s_cbranch_scc1 .Ldin_L0s\n"
    ".Ldin_G0s:\n"
    "v_mfma_f32_16x16x32_bf16 v[144:147], v[0:3], v[160:163], v[112:115]\n"
    "v_exp_f32 v128, v128 clamp\n"
    "v_mfma_f32_16x16x32_bf16 v[144:147], v[4:7], v[164:167], v[144:147]\n"
    "v_exp_f32 v129, v129 clamp\n"
    "v_mfma_f32_16x16x32_bf16 v[148:151], v[8:11], v[160:163], v[116:119]\n"
    "v_exp_f32 v130, v130 clamp\n"
    "v_mfma_f32_16x16x32_bf16 v[148:151], v[12:15], v[164:167], v[148:151]\n"
    "v_exp_f32 v131, v131 clamp\n"
    "v_mfma_f32_16x16x32_bf16 v[152:155], v[16:19], v[160:163], v[120:123]\n"
    "v_exp_f32 v132, v132 clamp\n"
    "v_mfma_f32_16x16x32_bf16 v[152:155], v[20:23], v[164:167], v[152:155]\n"
    "v_exp_f32 v133, v133 clamp\n"
    "v_mfma_f32_16x16x32_bf16 v[156:159], v[24:27], v[160:163], v[124:127]\n"
    "v_exp_f32 v134, v134 clamp\n"
    "v_mfma_f32_16x16x32_bf16 v[156:159], v[28:31], v[164:167], v[156:159]\n"
    "v_exp_f32 v135, v135 clamp\n"
    "v_exp_f32 v136, v136 clamp\n"
    "s_cmp_eq_u32 s67, s80\n"
    "s_cselect_b32 s67, s65, s67\n"
    "s_add_u32 m0, s68, 2048\n"
    "v_exp_f32 v137, v137 clamp\n"
    "v_add_u32_e64 v253, s67, v244\n"
    "s_waitcnt lgkmcnt(0)\n"
    "v_mfma_f32_4x4x4_16b_bf16 v[224:227], v[228:229], v[176:177], v[224:227] cbsz:4 abid:0\n"
    "v_exp_f32 v138, v138 clamp\n"
    "v_exp_f32 v139, v139 clamp\n"
    "v_mfma_f32_4x4x4_16b_bf16 v[224:227], v[228:229], v[178:179], v[224:227] cbsz:4 abid:1\n"
    "v_exp_f32 v140, v140 clamp\n"
    "v_exp_f32 v141, v141 clamp\n"
    "v_mfma_f32_4x4x4_16b_bf16 v[224:227], v[228:229], v[180:181], v[224:227] cbsz:4 abid:2\n"
    "v_exp_f32 v142, v142 clamp\n"
    "v_exp_f32 v143, v143 clamp\n"
    "v_mfma_f32_4x4x4_16b_bf16 v[224:227], v[228:229], v[182:183], v[224:227] cbsz:4 abid:3\n"
    "s_waitcnt vmcnt(4)\n"
    "s_memrealtime s[34:35]\n"
    "ds_read_b64_tr_b16 v[176:177], v240 offset:2048\n"
    "ds_read_b64_tr_b16 v[178:179], v241 offset:2304\n"
    "ds_read_b64_tr_b16 v[180:181], v242 offset:2560\n"
    "ds_read_b64_tr_b16 v[182:183], v243 offset:2816\n"
    "ds_read_b32 v236, v253 offset:0\n"
    "ds_read_b32 v238, v253 offset:4\n"
    "ds_read_b128 v[160:163], v234 offset:4096\n"
    "ds_read_b128 v[164:167], v235 offset:4096\n"
    "v_pk_fma_f32 v[128:129], v[128:129], s[70:71], v[96:97]\n"
    "v_pk_fma_f32 v[130:131], v[130:131], s[70:71], v[98:99]\n"
    "v_pk_add_f32 v[230:231], v[128:129], v[130:131]\n"
    "v_pk_mul_f32 v[232:233], v[128:129], v[130:131]\n"
    "v_pk_fma_f32 v[132:133], v[132:133], s[70:71], v[100:101]\n"
    "v_pk_fma_f32 v[230:231], v[230:231], v[132:133], v[232:233]\n"
    "v_pk_mul_f32 v[232:233], v[232:233], v[132:133]\n"
    "v_pk_fma_f32 v[134:135], v[134:135], s[70:71], v[102:103]\n"
    "v_pk_fma_f32 v[230:231], v[230:231], v[134:135], v[232:233]\n"
    "v_pk_mul_f32 v[232:233], v[232:233], v[134:135]\n"
    "v_pk_fma_f32 v[136:137], v[136:137], s[70:71], v[104:105]\n"
    "v_pk_fma_f32 v[230:231], v[230:231], v[136:137], v[232:233]\n"
    "v_pk_mul_f32 v[232:233], v[232:233], v[136:137]\n"
    "v_pk_fma_f32 v[138:139], v[138:139], s[70:71], v[106:107]\n"
    "v_pk_fma_f32 v[230:231], v[230:231], v[138:139], v[232:233]\n"
    "v_pk_mul_f32 v[232:233], v[232:233], v[138:139]\n"
    "v_pk_fma_f32 v[140:141], v[140:141], s[70:71], v[108:109]\n"
    "v_pk_fma_f32 v[230:231], v[230:231], v[140:141], v[232:233]\n"
    "v_pk_mul_f32 v[232:233], v[232:233], v[140:141]\n"
    "v_pk_fma_f32 v[142:143], v[142:143], s[70:71], v[110:111]\n"
    "v_pk_fma_f32 v[230:231], v[230:231], v[142:143], v[232:233]\n"
    "v_pk_mul_f32 v[232:233], v[232:233], v[142:143]\n"
    "v_rcp_f32 v232, v232\n"
    "v_rcp_f32 v233, v233\n"
    "v_fma_f32 v230, v230, v232, v247\n"
    "v_fmac_f32 v230, v231, v233\n"
    "v_lshlrev_b32 v232, 2, v249\n"
    "v_lshrrev_b32 v233, 2, v249\n"
    "v_and_b32 v232, 56, v232\n"
    "v_and_b32 v233, 4, v233\n"
    "v_add3_u32 v232, v232, v233, s81\n"
    "ds_read_b32 v232, v232\n"
    "s_add_u32 s81, s81, 64\n"
    "s_waitcnt lgkmcnt(0)\n"
    "v_lshlrev_b32 v232, 1, v232\n"
    "buffer_load_ushort v232, v232, s[44:47], 0 offen\n"
    "s_waitcnt vmcnt(0)\n"
    "v_lshlrev_b32 v232, 16, v232\n"
    "v_mul_f32 v230, v230, v232\n"
    "v_cvt_pk_bf16_f32 v230, v230, v230\n"
    "s_add_u32 s67, s67, 64\n"
    "s_waitcnt lgkmcnt(0)\n"
    "ds_write_b16 v245, v230\n"
    "ds_read_b64 v[228:229], v246\n"
    "s_cmp_le_u32 s34, s76\n"
    "s_cbranch_scc1 .Ldin_G0s_wait\n"
    ".Ldin_G0s_go:\n"
    "s_sub_u32 s94, s34, 60\n"
    "s_max_u32 s76, s76, s94\n"
    "s_add_u32 s76, s76, s72\n"
    ".Ldin_G0s_dma:\n"
    "buffer_load_dwordx4 v[236:237], s[40:43], 0 idxen offen offset:0 nt lds\n"
    "buffer_load_dwordx4 v[238:239], s[40:43], 0 idxen offen offset:1024 nt lds\n"
    ".Ldin_G0s_nd:\n"
    "s_cmp_eq_u32 s67, s82\n"
    "s_cbranch_scc1 .Ldin_L1s\n"
    ".Ldin_G1s:\n"
    "v_mfma_f32_16x16x32_bf16 v[128:131], v[0:3], v[160:163], v[112:115]\n"
    "v_exp_f32 v144, v144 clamp\n"
    "v_mfma_f32_16x16x32_bf16 v[128:131], v[4:7], v[164:167], v[128:131]\n"
    "v_exp_f32 v145, v145 clamp\n"
    "v_mfma_f32_16x16x32_bf16 v[132:135], v[8:11], v[160:163], v[116:119]\n"
    "v_exp_f32 v146, v146 clamp\n"
    "v_mfma_f32_16x16x32_bf16 v[132:135], v[12:15], v[164:167], v[132:135]\n"
    "v_exp_f32 v147, v147 clamp\n"
    "v_mfma_f32_16x16x32_bf16 v[136:139], v[16:19], v[160:163], v[120:123]\n"
    "v_exp_f32 v148, v148 clamp\n"
    "v_mfma_f32_16x16x32_bf16 v[136:139], v[20:23], v[164:167], v[136:139]\n"
    "v_exp_f32 v149, v149 clamp\n"
    "v_mfma_f32_16x16x32_bf16 v[140:143], v[24:27], v[160:163], v[124:127]\n"
    "v_exp_f32 v150, v150 clamp\n"
    "v_mfma_f32_16x16x32_bf16 v[140:143], v[28:31], v[164:167], v[140:143]\n"
    "v_exp_f32 v151, v151 clamp\n"
    "v_exp_f32 v152, v152 clamp\n"
    "s_cmp_eq_u32 s67, s80\n"
    "s_cselect_b32 s67, s65, s67\n"
    "s_add_u32 m0, s68, 4096\n"
    "v_exp_f32 v153, v153 clamp\n"
    "v_add_u32_e64 v253, s67, v244\n"
    "s_waitcnt lgkmcnt(0)\n"
    "v_mfma_f32_4x4x4_16b_bf16 v[224:227], v[228:229], v[168:169], v[224:227] cbsz:4 abid:0\n"
    "v_exp_f32 v154, v154 clamp\n"
    "v_exp_f32 v155, v155 clamp\n"
    "v_mfma_f32_4x4x4_16b_bf16 v[224:227], v[228:229], v[170:171], v[224:227] cbsz:4 abid:1\n"
    "v_exp_f32 v156, v156 clamp\n"
    "v_exp_f32 v157, v157 clamp\n"
    "v_mfma_f32_4x4x4_16b_bf16 v[224:227], v[228:229], v[172:173], v[224:227] cbsz:4 abid:2\n"
    "v_exp_f32 v158, v158 clamp\n"
    "v_exp_f32 v159, v159 clamp\n"
    "v_mfma_f32_4x4x4_16b_bf16 v[224:227], v[228:229], v[174:175], v[224:227] cbsz:4 abid:3\n"
    "s_waitcnt vmcnt(4)\n"
    "s_memrealtime s[34:35]\n"
    "ds_read_b64_tr_b16 v[168:169], v240 offset:4096\n"
    "ds_read_b64_tr_b16 v[170:171], v241 offset:4352\n"
    "ds_read_b64_tr_b16 v[172:173], v242 offset:4608\n"
    "ds_read_b64_tr_b16 v[174:175], v243 offset:4864\n"
    "ds_read_b32 v236, v253 offset:0\n"
    "ds_read_b32 v238, v253 offset:4\n"
    "ds_read_b128 v[160:163], v234 offset:6144\n"
    "ds_read_b128 v[164:167], v235 offset:6144\n"
    "v_pk_fma_f32 v[144:145], v[144:145], s[70:71], v[96:97]\n"
    "v_pk_fma_f32 v[146:147], v[146:147], s[70:71], v[98:99]\n"
    "v_pk_add_f32 v[230:231], v[144:145], v[146:147]\n"
    "v_pk_mul_f32 v[232:233], v[144:145], v[146:147]\n"
    "v_pk_fma_f32 v[148:149], v[148:149], s[70:71], v[100:101]\n"
    "v_pk_fma_f32 v[230:231], v[230:231], v[148:149], v[232:233]\n"
    "v_pk_mul_f32 v[232:233], v[232:233], v[148:149]\n"
    "v_pk_fma_f32 v[150:151], v[150:151], s[70:71], v[102:103]\n"
    "v_pk_fma_f32 v[230:231], v[230:231], v[150:151], v[232:233]\n"
    "v_pk_mul_f32 v[232:233], v[232:233], v[150:151]\n"
    "v_pk_fma_f32 v[152:153], v[152:153], s[70:71], v[104:105]\n"
    "v_pk_fma_f32 v[230:231], v[230:231], v[152:153], v[232:233]\n"
    "v_pk_mul_f32 v[232:233], v[232:233], v[152:153]\n"
    "v_pk_fma_f32 v[154:155], v[154:155], s[70:71], v[106:107]\n"
    "v_pk_fma_f32 v[230:231], v[230:231], v[154:155], v[232:233]\n"
    "v_pk_mul_f32 v[232:233], v[232:233], v[154:155]\n"
    "v_pk_fma_f32 v[156:157], v[156:157], s[70:71], v[108:109]\n"
    "v_pk_fma_f32 v[230:231], v[230:231], v[156:157], v[232:233]\n"
    "v_pk_mul_f32 v[232:233], v[232:233], v[156:157]\n"
    "v_pk_fma_f32 v[158:159], v[158:159], s[70:71], v[110:111]\n"
    "v_pk_fma_f32 v[230:231], v[230:231], v[158:159], v[232:233]\n"
    "v_pk_mul_f32 v[232:233], v[232:233], v[158:159]\n"
    "v_rcp_f32 v232, v232\n"
    "v_rcp_f32 v233, v233\n"
    "v_fma_f32 v230, v230, v232, v247\n"
    "v_fmac_f32 v230, v231, v233\n"
    "v_lshlrev_b32 v232, 2, v249\n"
    "v_lshrrev_b32 v233, 2, v249\n"
    "v_and_b32 v232, 56, v232\n"
    "v_and_b32 v233, 4, v233\n"
    "v_add3_u32 v232, v232, v233, s81\n"
    "ds_read_b32 v232, v232\n"
    "s_add_u32 s81, s81, 64\n"
    "s_waitcnt lgkmcnt(0)\n"
    "v_lshlrev_b32 v232, 1, v232\n"
    "buffer_load_ushort v232, v232, s[44:47], 0 offen\n"
    "s_waitcnt vmcnt(0)\n"
    "v_lshlrev_b32 v232, 16, v232\n"
    "v_mul_f32 v230, v230, v232\n"
    "v_cvt_pk_bf16_f32 v230, v230, v230\n"
    "s_add_u32 s67, s67, 64\n"
    "s_waitcnt lgkmcnt(0)\n"
    "ds_write_b16 v245, v230\n"
    "ds_read_b64 v[228:229], v246\n"
    "s_cmp_le_u32 s34, s76\n"
    "s_cbranch_scc1 .Ldin_G1s_wait\n"
    ".Ldin_G1s_go:\n"
    "s_add_u32 s76, s76, s72\n"
    ".Ldin_G1s_dma:\n"
    "buffer_load_dwordx4 v[236:237], s[40:43], 0 idxen offen offset:0 nt lds\n"
    "buffer_load_dwordx4 v[238:239], s[40:43], 0 idxen offen offset:1024 nt lds\n"
    ".Ldin_G1s_nd:\n"
    "s_cmp_eq_u32 s67, s82\n"
    "s_cbranch_scc1 .Ldin_L2s\n"
    "s_branch .Ldin_G2s\n"
    ".Ldin_B1s_wait:\n"
    "s_cmp_lt_i32 s59, s57\n"
    "s_cbranch_scc1 .Ldin_B1s_w2\n"
    "s_sub_u32 s94, s67, s65\n"
    "s_cmp_gt_u32 s94, 256\n"
    "s_cbranch_scc1 .Ldin_B1s_dma\n"
    "s_cmp_eq_u32 s94, 64\n"
    "s_waitcnt vmcnt(2)\n"
    "s_cbranch_scc1 .Ldin_B1s_nd\n"
    "s_waitcnt vmcnt(0)\n"
    "s_branch .Ldin_B1s_nd\n"
    ".Ldin_B1s_slp:\n"
    "s_sleep 1\n"
    ".Ldin_B1s_w2:\n"
    "s_memrealtime s[34:35]\n"
    "s_waitcnt lgkmcnt(0)\n"
    "s_sub_u32 s94, s34, s76\n"
    "s_cmp_lt_i32 s94, 0\n"
    "s_cbranch_scc1 .Ldin_B1s_slp\n"
    "s_branch .Ldin_B1s_go\n"
    ".Ldin_G2s_wait:\n"
    "s_cmp_lt_i32 s59, s57\n"
    "s_cbranch_scc1 .Ldin_G2s_w2\n"
    "s_sub_u32 s94, s67, s65\n"
    "s_cmp_gt_u32 s94, 256\n"
    "s_cbranch_scc1 .Ldin_G2s_dma\n"
    "s_cmp_eq_u32 s94, 64\n"
    "s_waitcnt vmcnt(2)\n"
    "s_cbranch_scc1 .Ldin_G2s_nd\n"
    "s_waitcnt vmcnt(0)\n"
    "s_branch .Ldin_G2s_nd\n"
    ".Ldin_G2s_slp:\n"
    "s_sleep 1\n"
    ".Ldin_G2s_w2:\n"
    "s_memrealtime s[34:35]\n"
    "s_waitcnt lgkmcnt(0)\n"
    "s_sub_u32 s94, s34, s76\n"
    "s_cmp_lt_i32 s94, 0\n"
    "s_cbranch_scc1 .Ldin_G2s_slp\n"
    "s_branch .Ldin_G2s_go\n"
    ".Ldin_G3s_wait:\n"
    "s_cmp_lt_i32 s59, s57\n"
    "s_cbranch_scc1 .Ldin_G3s_w2\n"
    "s_sub_u32 s94, s67, s65\n"
    "s_cmp_gt_u32 s94, 256\n"
    "s_cbranch_scc1 .Ldin_G3s_dma\n"
    "s_cmp_eq_u32 s94, 64\n"
    "s_waitcnt vmcnt(2)\n"
    "s_cbranch_scc1 .Ldin_G3s_nd\n"
    "s_waitcnt vmcnt(0)\n"
    "s_branch .Ldin_G3s_nd\n"
    ".Ldin_G3s_slp:\n"
    "s_sleep 1\n"
    ".Ldin_G3s_w2:\n"
    "s_memrealtime s[34:35]\n"
    "s_waitcnt lgkmcnt(0)\n"
    "s_sub_u32 s94, s34, s76\n"
    "s_cmp_lt_i32 s94, 0\n"
    "s_cbranch_scc1 .Ldin_G3s_slp\n"
    "s_branch .Ldin_G3s_go\n"
    ".Ldin_G0s_wait:\n"
    "s_cmp_lt_i32 s59, s57\n"
    "s_cbranch_scc1 .Ldin_G0s_w2\n"
    "s_sub_u32 s94, s67, s65\n"
    "s_cmp_gt_u32 s94, 256\n"
    "s_cbranch_scc1 .Ldin_G0s_dma\n"
    "s_cmp_eq_u32 s94, 64\n"
    "s_waitcnt vmcnt(2)\n"
    "s_cbranch_scc1 .Ldin_G0s_nd\n"
    "s_waitcnt vmcnt(0)\n"
    "s_branch .Ldin_G0s_nd\n"
    ".Ldin_G0s_slp:\n"
    "s_sleep 1\n"
    ".Ldin_G0s_w2:\n"
    "s_memrealtime s[34:35]\n"
    "s_waitcnt lgkmcnt(0)\n"
    "s_sub_u32 s94, s34, s76\n"
    "s_cmp_lt_i32 s94, 0\n"
    "s_cbranch_scc1 .Ldin_G0s_slp\n"
    "s_branch .Ldin_G0s_go\n"
    ".Ldin_G1s_wait:\n"
    "s_cmp_lt_i32 s59, s57\n"
    "s_cbranch_scc1 .Ldin_G1s_w2\n"
    "s_sub_u32 s94, s67, s65\n"
    "s_cmp_gt_u32 s94, 256\n"
    "s_cbranch_scc1 .Ldin_G1s_dma\n"
    "s_cmp_eq_u32 s94, 64\n"
    "s_waitcnt vmcnt(2)\n"
    "s_cbranch_scc1 .Ldin_G1s_nd\n"
    "s_waitcnt vmcnt(0)\n"
    "s_branch .Ldin_G1s_nd\n"
    ".Ldin_G1s_slp:\n"
    "s_sleep 1\n"
    ".Ldin_G1s_w2:\n"
    "s_memrealtime s[34:35]\n"
    "s_waitcnt lgkmcnt(0)\n"
    "s_sub_u32 s94, s34, s76\n"
    "s_cmp_lt_i32 s94, 0\n"
    "s_cbranch_scc1 .Ldin_G1s_slp\n"
    "s_branch .Ldin_G1s_go\n"
    ".p2align 5\n"
    ".Ldin_L2s:\n"
    "s_setprio 0\n"
    "s_cmp_ge_i32 s59, s57\n"
    "s_cbranch_scc1 .Ldin_E0s\n"
    "s_add_u32 s76, s76, s73\n"
    "v_mov_b32 v253, s63\n"
    "v_and_b32 v184, v254, v184\n"
    "v_and_b32 v185, v255, v185\n"
    "v_and_b32 v186, v254, v186\n"
    "v_and_b32 v187, v255, v187\n"
    "v_and_b32 v188, v254, v188\n"
    "v_and_b32 v189, v255, v189\n"
    "v_and_b32 v190, v254, v190\n"
    "v_and_b32_e64 v191, v255, v191\n"
    "ds_read_b64 v[232:233], v253\n"
    "v_mfma_f32_4x4x4_16b_bf16 v[144:147], v[184:185], v[32:33], v[64:67]\n"
    "ds_read_b128 v[112:115], v252 offset:17408\n"
    "v_mfma_f32_4x4x4_16b_bf16 v[148:151], v[186:187], v[34:35], v[68:71]\n"
    "ds_read_b128 v[116:119], v252 offset:17424\n"
    "v_mfma_f32_4x4x4_16b_bf16 v[152:155], v[188:189], v[36:37], v[72:75]\n"
    "ds_read_b128 v[120:123], v252 offset:17536\n"
    "ds_read_b128 v[124:127], v252 offset:17552\n"
    "v_cvt_pk_bf16_f32 v0, v144, v145\n"
    "v_cvt_pk_bf16_f32 v1, v146, v147\n"
    "v_mfma_f32_4x4x4_16b_bf16 v[156:159], v[190:191], v[38:39], v[76:79]\n"
    "v_cvt_pk_bf16_f32 v2, v148, v149\n"
    "v_cvt_pk_bf16_f32 v3, v150, v151\n"
    "v_mfma_f32_4x4x4_16b_bf16 v[144:147], v[184:185], v[40:41], v[80:83]\n"
    "v_cvt_pk_bf16_f32 v4, v152, v153\n"
    "v_cvt_pk_bf16_f32 v5, v154, v155\n"
    "v_mfma_f32_4x4x4_16b_bf16 v[148:151], v[186:187], v[42:43], v[84:87]\n"
    "v_cvt_pk_bf16_f32 v6, v156, v157\n"
    "v_cvt_pk_bf16_f32 v7, v158, v159\n"
    "v_mfma_f32_4x4x4_16b_bf16 v[152:155], v[188:189], v[44:45], v[88:91]\n"
    "v_cvt_pk_bf16_f32 v8, v144, v145\n"
    "v_cvt_pk_bf16_f32 v9, v146, v147\n"
    "v_mfma_f32_4x4x4_16b_bf16 v[156:159], v[190:191], v[46:47], v[92:95]\n"
    "v_cvt_pk_bf16_f32 v10, v148, v149\n"
    "v_cvt_pk_bf16_f32 v11, v150, v151\n"
    "v_mfma_f32_4x4x4_16b_bf16 v[144:147], v[184:185], v[48:49], v[192:195]\n"
    "v_cvt_pk_bf16_f32 v12, v152, v153\n"
    "v_cvt_pk_bf16_f32 v13, v154, v155\n"
    "v_mfma_f32_4x4x4_16b_bf16 v[148:151], v[186:187], v[50:51], v[196:199]\n"
    "v_cvt_pk_bf16_f32 v14, v156, v157\n"
    "v_cvt_pk_bf16_f32 v15, v158, v159\n"
    "v_mfma_f32_4x4x4_16b_bf16 v[152:155], v[188:189], v[52:53], v[200:203]\n"
    "s_waitcnt lgkmcnt(0)\n"
    "v_readfirstlane_b32 s60, v232\n"
    "v_cvt_pk_bf16_f32 v16, v144, v145\n"
    "v_cvt_pk_bf16_f32 v17, v146, v147\n"
    "v_mfma_f32_4x4x4_16b_bf16 v[156:159], v[190:191], v[54:55], v[204:207]\n"
    "s_cmp_eq_u32 s60, -1\n"
    "s_cbranch_scc1 .Ldin_L2s_poll\n"
    ".Ldin_L2s_pr:\n"
    "v_readfirstlane_b32 s95, v233\n"
    "s_and_b32 s94, s60, 63\n"
    "s_lshl_b32 s94, s94, 6\n"
    "s_add_u32 s94, s94, 68480\n"
    "s_add_u32 s63, s63, 8\n"
    "v_lshl_add_u32 v252, v249, 1, s94\n"
    "ds_read_b32 v252, v252\n"
    "s_sub_u32 s95, s95, s76\n"
    "s_ashr_i32 s95, s95, 4\n"
    "s_min_i32 s95, s95, s74\n"
    "s_max_i32 s72, s95, s75\n"
    "v_cvt_pk_bf16_f32 v18, v148, v149\n"
    "v_cvt_pk_bf16_f32 v19, v150, v151\n"
    "v_mfma_f32_4x4x4_16b_bf16 v[144:147], v[184:185], v[56:57], v[208:211]\n"
    "v_cvt_pk_bf16_f32 v20, v152, v153\n"
    "v_cvt_pk_bf16_f32 v21, v154, v155\n"
    "v_mfma_f32_4x4x4_16b_bf16 v[148:151], v[186:187], v[58:59], v[212:215]\n"
    "v_cvt_pk_bf16_f32 v22, v156, v157\n"
    "v_cvt_pk_bf16_f32 v23, v158, v159\n"
    "v_mfma_f32_4x4x4_16b_bf16 v[152:155], v[188:189], v[60:61], v[216:219]\n"
    "v_cvt_pk_bf16_f32 v24, v144, v145\n"
    "v_cvt_pk_bf16_f32 v25, v146, v147\n"
    "v_mfma_f32_4x4x4_16b_bf16 v[156:159], v[190:191], v[62:63], v[220:223]\n"
    "v_cvt_pk_bf16_f32 v26, v148, v149\n"
    "v_cvt_pk_bf16_f32 v27, v150, v151\n"
    "v_cvt_pk_bf16_f32 v28, v152, v153\n"
    "v_cvt_pk_bf16_f32 v29, v154, v155\n"
    "v_exp_f32 v136, v136 clamp\n"
    "v_cvt_pk_bf16_f32 v30, v156, v157\n"
    "v_cvt_pk_bf16_f32 v31, v158, v159\n"
    "v_mfma_f32_16x16x32_bf16 v[144:147], v[0:3], v[160:163], v[112:115]\n"
    "v_exp_f32 v128, v128 clamp\n"
    "v_mfma_f32_16x16x32_bf16 v[144:147], v[4:7], v[164:167], v[144:147]\n"
    "v_exp_f32 v129, v129 clamp\n"
    "v_mfma_f32_16x16x32_bf16 v[148:151], v[8:11], v[160:163], v[116:119]\n"
    "v_exp_f32 v130, v130 clamp\n"
    "v_mfma_f32_16x16x32_bf16 v[148:151], v[12:15], v[164:167], v[148:151]\n"
    "v_exp_f32 v131, v131 clamp\n"
    "v_mfma_f32_16x16x32_bf16 v[152:155], v[16:19], v[160:163], v[120:123]\n"
    "v_exp_f32 v132, v132 clamp\n"
    "v_mfma_f32_16x16x32_bf16 v[152:155], v[20:23], v[164:167], v[152:155]\n"
    "v_exp_f32 v133, v133 clamp\n"
    "v_mfma_f32_16x16x32_bf16 v[156:159], v[24:27], v[160:163], v[124:127]\n"
    "v_exp_f32 v134, v134 clamp\n"
    "v_mfma_f32_16x16x32_bf16 v[156:159], v[28:31], v[164:167], v[156:159]\n"
    "v_exp_f32 v135, v135 clamp\n"
    "s_add_u32 m0, s68, 6144\n"
    "v_exp_f32 v137, v137 clamp\n"
    "v_add_u32 v253, s67, v244\n"
    "v_mfma_f32_4x4x4_16b_bf16 v[224:227], v[228:229], v[176:177], v[224:227] cbsz:4 abid:0\n"
    "v_exp_f32 v138, v138 clamp\n"
    "v_exp_f32 v139, v139 clamp\n"
    "v_mfma_f32_4x4x4_16b_bf16 v[224:227], v[228:229], v[178:179], v[224:227] cbsz:4 abid:1\n"
    "v_exp_f32 v140, v140 clamp\n"
    "v_exp_f32 v141, v141 clamp\n"
    "v_mfma_f32_4x4x4_16b_bf16 v[224:227], v[228:229], v[180:181], v[224:227] cbsz:4 abid:2\n"
    "v_exp_f32 v142, v142 clamp\n"
    "v_exp_f32 v143, v143 clamp\n"
    "v_mfma_f32_4x4x4_16b_bf16 v[224:227], v[228:229], v[182:183], v[224:227] cbsz:4 abid:3\n"
    "s_waitcnt vmcnt(4)\n"
    "ds_read_b64_tr_b16 v[176:177], v240 offset:6144\n"
    "ds_read_b64_tr_b16 v[178:179], v241 offset:6400\n"
    "ds_read_b64_tr_b16 v[180:181], v242 offset:6656\n"
    "ds_read_b64_tr_b16 v[182:183], v243 offset:6912\n"
    "ds_read_b32 v236, v253 offset:0\n"
    "ds_read_b32 v238, v253 offset:4\n"
    "ds_read_b128 v[160:163], v234 offset:0\n"
    "ds_read_b128 v[164:167], v235 offset:0\n"
    "s_waitcnt lgkmcnt(8)\n"
    "s_nop 0\n"
    "v_readlane_b32 s20, v252, 0\n"
    "v_readlane_b32 s21, v252, 1\n"
    "v_readlane_b32 s22, v252, 2\n"
    "v_readlane_b32 s23, v252, 3\n"
    "v_readlane_b32 s24, v252, 4\n"
    "v_readlane_b32 s25, v252, 5\n"
    "v_readlane_b32 s26, v252, 6\n"
    "v_readlane_b32 s27, v252, 7\n"
    "v_pk_fma_f32 v[128:129], v[128:129], s[70:71], v[96:97]\n"
    "v_pk_fma_f32 v[130:131], v[130:131], s[70:71], v[98:99]\n"
    "v_pk_add_f32 v[230:231], v[128:129], v[130:131]\n"
    "v_pk_mul_f32 v[232:233], v[128:129], v[130:131]\n"
    "v_pk_fma_f32 v[132:133], v[132:133], s[70:71], v[100:101]\n"
    "v_pk_fma_f32 v[230:231], v[230:231], v[132:133], v[232:233]\n"
    "v_pk_mul_f32 v[232:233], v[232:233], v[132:133]\n"
    "v_pk_fma_f32 v[134:135], v[134:135], s[70:71], v[102:103]\n"
    "v_pk_fma_f32 v[230:231], v[230:231], v[134:135], v[232:233]\n"
    "v_pk_mul_f32 v[232:233], v[232:233], v[134:135]\n"
    "v_pk_fma_f32 v[136:137], v[136:137], s[70:71], v[104:105]\n"
    "v_pk_fma_f32 v[230:231], v[230:231], v[136:137], v[232:233]\n"
    "v_pk_mul_f32 v[232:233], v[232:233], v[136:137]\n"
    "v_pk_fma_f32 v[138:139], v[138:139], s[70:71], v[106:107]\n"
    "v_pk_fma_f32 v[230:231], v[230:231], v[138:139], v[232:233]\n"
    "v_pk_mul_f32 v[232:233], v[232:233], v[138:139]\n"
    "v_pk_fma_f32 v[140:141], v[140:141], s[70:71], v[108:109]\n"
    "v_pk_fma_f32 v[230:231], v[230:231], v[140:141], v[232:233]\n"
    "v_pk_mul_f32 v[232:233], v[232:233], v[140:141]\n"
    "v_pk_fma_f32 v[142:143], v[142:143], s[70:71], v[110:111]\n"
    "v_pk_fma_f32 v[230:231], v[230:231], v[142:143], v[232:233]\n"
    "v_pk_mul_f32 v[232:233], v[232:233], v[142:143]\n"
    "v_rcp_f32 v232, v232\n"
    "v_rcp_f32 v233, v233\n"
    "v_fma_f32 v230, v230, v232, v247\n"
    "v_fmac_f32 v230, v231, v233\n"
    "v_lshlrev_b32 v232, 2, v249\n"
    "v_lshrrev_b32 v233, 2, v249\n"
    "v_and_b32 v232, 56, v232\n"
    "v_and_b32 v233, 4, v233\n"
    "v_add3_u32 v232, v232, v233, s81\n"
    "ds_read_b32 v232, v232\n"
    "s_add_u32 s81, s81, 64\n"
    "s_waitcnt lgkmcnt(0)\n"
    "v_lshlrev_b32 v232, 1, v232\n"
    "buffer_load_ushort v232, v232, s[44:47], 0 offen\n"
    "s_waitcnt vmcnt(0)\n"
    "v_lshlrev_b32 v232, 16, v232\n"
    "v_mul_f32 v230, v230, v232\n"
    "v_cvt_pk_bf16_f32 v230, v230, v230\n"
    "s_add_u32 s67, s67, 64\n"
    "s_waitcnt lgkmcnt(0)\n"
    "ds_write_b16 v245, v230\n"
    "ds_read_b64 v[228:229], v246\n"
    "s_lshl1_add_u32 s76, s72, s76\n"
    "buffer_load_dwordx4 v[236:237], s[40:43], 0 idxen offen offset:0 nt lds\n"
    "buffer_load_dwordx4 v[238:239], s[40:43], 0 idxen offen offset:1024 nt lds\n"
    "s_and_b32 s62, s58, 63\n"
    "s_mul_i32 s62, s62, 272\n"
    "s_mov_b32 s58, s59\n"
    "s_mov_b32 s59, s60\n"
    "s_mov_b32 s81, s65\n"
    "s_xor_b32 s65, s65, s64\n"
    "s_mov_b32 s80, s83\n"
    "s_add_u32 s82, s65, 256\n"
    "s_cmp_lt_i32 s59, s57\n"
    "s_cselect_b32 s76, s76, -1\n"
    "s_bitcmp1_b32 s58, 30\n"
    "s_cbranch_scc0 .Ldin_A3\n"
    ".Ldin_A3s:\n"
    "v_mfma_f32_16x16x32_bf16 v[128:131], v[0:3], v[160:163], v[112:115]\n"
    "v_exp_f32 v144, v144 clamp\n"
    "v_mfma_f32_16x16x32_bf16 v[128:131], v[4:7], v[164:167], v[128:131]\n"
    "v_exp_f32 v145, v145 clamp\n"
    "v_mfma_f32_16x16x32_bf16 v[132:135], v[8:11], v[160:163], v[116:119]\n"
    "v_exp_f32 v146, v146 clamp\n"
    "v_mfma_f32_16x16x32_bf16 v[132:135], v[12:15], v[164:167], v[132:135]\n"
    "v_exp_f32 v147, v147 clamp\n"
    "v_mfma_f32_16x16x32_bf16 v[136:139], v[16:19], v[160:163], v[120:123]\n"
    "v_exp_f32 v148, v148 clamp\n"
    "v_mfma_f32_16x16x32_bf16 v[136:139], v[20:23], v[164:167], v[136:139]\n"
    "v_exp_f32 v149, v149 clamp\n"
    "v_mfma_f32_16x16x32_bf16 v[140:143], v[24:27], v[160:163], v[124:127]\n"
    "v_exp_f32 v150, v150 clamp\n"
    "v_mfma_f32_16x16x32_bf16 v[140:143], v[28:31], v[164:167], v[140:143]\n"
    "v_exp_f32 v151, v151 clamp\n"
    "v_exp_f32 v152, v152 clamp\n"
    "s_add_u32 m0, s68, 0\n"
    "v_exp_f32 v153, v153 clamp\n"
    "v_add_u32 v253, s67, v244\n"
    "s_waitcnt lgkmcnt(0)\n"
    "v_mfma_f32_4x4x4_16b_bf16 v[224:227], v[228:229], v[168:169], v[224:227] cbsz:4 abid:0\n"
    "v_exp_f32 v154, v154 clamp\n"
    "v_exp_f32 v155, v155 clamp\n"
    "v_mfma_f32_4x4x4_16b_bf16 v[224:227], v[228:229], v[170:171], v[224:227] cbsz:4 abid:1\n"
    "v_exp_f32 v156, v156 clamp\n"
    "v_exp_f32 v157, v157 clamp\n"
    "v_mfma_f32_4x4x4_16b_bf16 v[224:227], v[228:229], v[172:173], v[224:227] cbsz:4 abid:2\n"
    "v_exp_f32 v158, v158 clamp\n"
    "v_exp_f32 v159, v159 clamp\n"
    "v_mfma_f32_4x4x4_16b_bf16 v[224:227], v[228:229], v[174:175], v[224:227] cbsz:4 abid:3\n"
    "s_waitcnt vmcnt(4)\n"
    "ds_read_b64_tr_b16 v[168:169], v240 offset:0\n"
    "ds_read_b64_tr_b16 v[170:171], v241 offset:256\n"
    "ds_read_b64_tr_b16 v[172:173], v242 offset:512\n"
    "ds_read_b64_tr_b16 v[174:175], v243 offset:768\n"
    "ds_read_b32 v236, v253 offset:0\n"
    "ds_read_b32 v238, v253 offset:4\n"
    "ds_read_b128 v[160:163], v234 offset:2048\n"
    "ds_read_b128 v[164:167], v235 offset:2048\n"
    "s_cbranch_vccz .Ldin_A3s_np\n"
    "s_setprio 1\n"
    ".Ldin_A3s_np:\n"
    "v_pk_fma_f32 v[144:145], v[144:145], s[70:71], v[96:97]\n"
    "v_pk_fma_f32 v[146:147], v[146:147], s[70:71], v[98:99]\n"
    "v_add_f32 v191, v224, v225\n"
    "v_add_f32 v190, v226, v227\n"
    "v_add_f32 v191, v191, v190\n"
    "v_add_u32 v190, s62, v249\n"
    "v_mul_f32 v191, s69, v191\n"
    "v_cvt_pk_bf16_f32 v191, v191, v191\n"
    "ds_write_b16 v190, v191 offset:32768\n"
    "s_bfe_u32 s94, s59, 0x180006\n"
    "s_mul_i32 s86, s94, s56\n"
    "v_mbcnt_lo_u32_b32 v184, s20, 0\n"
    "v_mbcnt_lo_u32_b32 v185, s22, 0\n"
    "v_mbcnt_lo_u32_b32 v186, s24, 0\n"
    "v_mbcnt_lo_u32_b32 v187, s26, 0\n"
    "s_bcnt1_i32_b64 s90, s[20:21]\n"
    "s_bcnt1_i32_b64 s91, s[22:23]\n"
    "s_bcnt1_i32_b64 s92, s[24:25]\n"
    "s_bcnt1_i32_b64 s93, s[26:27]\n"
    "v_mbcnt_hi_u32_b32 v184, s21, v184\n"
    "v_mbcnt_hi_u32_b32 v185, s23, v185\n"
    "v_mbcnt_hi_u32_b32 v186, s25, v186\n"
    "v_mbcnt_hi_u32_b32 v187, s27, v187\n"
    "s_lshl2_add_u32 s90, s90, s65\n"
    "s_lshl2_add_u32 s91, s91, s90\n"
    "s_lshl2_add_u32 s92, s92, s91\n"
    "s_lshl2_add_u32 s93, s93, s92\n"
    "v_add_u32_e64 v188, s86, v251\n"
    "v_pk_add_f32 v[230:231], v[144:145], v[146:147]\n"
    "v_pk_mul_f32 v[232:233], v[144:145], v[146:147]\n"
    "v_pk_fma_f32 v[148:149], v[148:149], s[70:71], v[100:101]\n"
    "v_pk_fma_f32 v[230:231], v[230:231], v[148:149], v[232:233]\n"
    "v_pk_mul_f32 v[232:233], v[232:233], v[148:149]\n"
    "v_lshl_add_u32 v184, v184, 2, s65\n"
    "v_lshl_add_u32 v185, v185, 2, s90\n"
    "v_lshl_add_u32 v186, v186, 2, s91\n"
    "v_lshl_add_u32 v187, v187, 2, s92\n"
    "v_mov_b32 v191, -1\n"
    "v_lshl_add_u32 v190, v249, 1, s93\n"
    "ds_write2_b32 v190, v191, v191 offset1:64\n"
    "s_mov_b64 exec, s[20:21]\n"
    "ds_write_b32 v184, v188\n"
    "s_mov_b64 exec, s[22:23]\n"
    "v_add_u32 v189, 64, v188\n"
    "ds_write_b32 v185, v189\n"
    "s_mov_b64 exec, s[24:25]\n"
    "v_add_u32 v190, 128, v188\n"
    "ds_write_b32 v186, v190\n"
    "s_mov_b64 exec, s[26:27]\n"
    "v_add_u32 v189, 192, v188\n"
    "ds_write_b32 v187, v189\n"
    "s_mov_b64 exec, -1\n"
    "s_add_u32 s95, s93, 60\n"
    "s_add_u32 s94, s82, 128\n"
    "s_andn2_b32 s95, s95, 63\n"
    "s_max_u32 s83, s95, s94\n"
    "v_pk_fma_f32 v[150:151], v[150:151], s[70:71], v[102:103]\n"
    "v_pk_fma_f32 v[230:231], v[230:231], v[150:151], v[232:233]\n"
    "v_pk_mul_f32 v[232:233], v[232:233], v[150:151]\n"
    "v_pk_fma_f32 v[152:153], v[152:153], s[70:71], v[104:105]\n"
    "v_pk_fma_f32 v[230:231], v[230:231], v[152:153], v[232:233]\n"
    "v_pk_mul_f32 v[232:233], v[232:233], v[152:153]\n"
    "v_pk_fma_f32 v[154:155], v[154:155], s[70:71], v[106:107]\n"
    "v_pk_fma_f32 v[230:231], v[230:231], v[154:155], v[232:233]\n"
    "v_pk_mul_f32 v[232:233], v[232:233], v[154:155]\n"
    "v_pk_fma_f32 v[156:157], v[156:157], s[70:71], v[108:109]\n"
    "v_pk_fma_f32 v[230:231], v[230:231], v[156:157], v[232:233]\n"
    "v_pk_mul_f32 v[232:233], v[232:233], v[156:157]\n"
    "v_pk_fma_f32 v[158:159], v[158:159], s[70:71], v[110:111]\n"
    "v_pk_fma_f32 v[230:231], v[230:231], v[158:159], v[232:233]\n"
    "v_pk_mul_f32 v[232:233], v[232:233], v[158:159]\n"
    "v_rcp_f32 v232, v232\n"
    "v_rcp_f32 v233, v233\n"
    "v_fma_f32 v230, v230, v232, v247\n"
    "v_fmac_f32 v230, v231, v233\n"
    "v_lshlrev_b32 v232, 2, v249\n"
    "v_lshrrev_b32 v233, 2, v249\n"
    "v_and_b32 v232, 56, v232\n"
    "v_and_b32 v233, 4, v233\n"
    "v_add3_u32 v232, v232, v233, s81\n"
    "ds_read_b32 v232, v232\n"
    "s_add_u32 s81, s81, 64\n"
    "s_waitcnt lgkmcnt(0)\n"
    "v_lshlrev_b32 v232, 1, v232\n"
    "buffer_load_ushort v232, v232, s[44:47], 0 offen\n"
    "s_waitcnt vmcnt(0)\n"
    "v_lshlrev_b32 v232, 16, v232\n"
    "v_mul_f32 v230, v230, v232\n"
    "v_cvt_pk_bf16_f32 v230, v230, v230\n"
    "s_add_u32 s67, s67, 64\n"
    "s_waitcnt lgkmcnt(0)\n"
    "ds_write_b16 v245, v230\n"
    "ds_read_b64 v[228:229], v246\n"
    "buffer_load_dwordx4 v[236:237], s[40:43], 0 idxen offen offset:0 nt lds\n"
    "buffer_load_dwordx4 v[238:239], s[40:43], 0 idxen offen offset:1024 nt lds\n"
    ".Ldin_B0s:\n"
    "v_mfma_f32_16x16x32_bf16 v[144:147], v[0:3], v[160:163], v[112:115]\n"
    "v_exp_f32 v128, v128 clamp\n"
    "v_mfma_f32_16x16x32_bf16 v[144:147], v[4:7], v[164:167], v[144:147]\n"
    "v_exp_f32 v129, v129 clamp\n"
    "v_mfma_f32_16x16x32_bf16 v[148:151], v[8:11], v[160:163], v[116:119]\n"
    "v_exp_f32 v130, v130 clamp\n"
    "v_mfma_f32_16x16x32_bf16 v[148:151], v[12:15], v[164:167], v[148:151]\n"
    "v_exp_f32 v131, v131 clamp\n"
    "v_mfma_f32_16x16x32_bf16 v[152:155], v[16:19], v[160:163], v[120:123]\n"
    "v_exp_f32 v132, v132 clamp\n"
    "v_mfma_f32_16x16x32_bf16 v[152:155], v[20:23], v[164:167], v[152:155]\n"
    "v_exp_f32 v133, v133 clamp\n"
    "v_mfma_f32_16x16x32_bf16 v[156:159], v[24:27], v[160:163], v[124:127]\n"
    "v_exp_f32 v134, v134 clamp\n"
    "v_mfma_f32_16x16x32_bf16 v[156:159], v[28:31], v[164:167], v[156:159]\n"
    "v_exp_f32 v135, v135 clamp\n"
    "v_exp_f32 v136, v136 clamp\n"
    "s_cmp_eq_u32 s67, s80\n"
    "s_cselect_b32 s67, s65, s67\n"
    "s_and_b32 s94, s59, 63\n"
    "s_mul_i32 s94, s94, 272\n"
    "v_add_u32 v252, s94, v250\n"
    "s_add_u32 m0, s68, 2048\n"
    "v_exp_f32 v137, v137 clamp\n"
    "v_add_u32 v253, s67, v244\n"
    "s_waitcnt lgkmcnt(0)\n"
    "v_mfma_f32_4x4x4_16b_bf16 v[224:227], v[228:229], v[176:177], 0 cbsz:4 abid:0\n"
    "v_exp_f32 v138, v138 clamp\n"
    "v_exp_f32 v139, v139 clamp\n"
    "v_mfma_f32_4x4x4_16b_bf16 v[224:227], v[228:229], v[178:179], v[224:227] cbsz:4 abid:1\n"
    "v_exp_f32 v140, v140 clamp\n"
    "v_exp_f32 v141, v141 clamp\n"
    "v_mfma_f32_4x4x4_16b_bf16 v[224:227], v[228:229], v[180:181], v[224:227] cbsz:4 abid:2\n"
    "v_exp_f32 v142, v142 clamp\n"
    "v_exp_f32 v143, v143 clamp\n"
    "v_mfma_f32_4x4x4_16b_bf16 v[224:227], v[228:229], v[182:183], v[224:227] cbsz:4 abid:3\n"
    "s_waitcnt vmcnt(4)\n"
    "s_memrealtime s[34:35]\n"
    "ds_read_b64_tr_b16 v[176:177], v240 offset:2048\n"
    "ds_read_b64_tr_b16 v[178:179], v241 offset:2304\n"
    "ds_read_b64_tr_b16 v[180:181], v242 offset:2560\n"
    "ds_read_b64_tr_b16 v[182:183], v243 offset:2816\n"
    "ds_read_b32 v236, v253 offset:0\n"
    "ds_read_b32 v238, v253 offset:4\n"
    "ds_read_b128 v[160:163], v234 offset:4096\n"
    "ds_read_b128 v[164:167], v235 offset:4096\n"
    "ds_read_b128 v[184:187], v252 offset:0\n"
    "ds_read_b128 v[188:191], v252 offset:16\n"
    "v_pk_fma_f32 v[128:129], v[128:129], s[70:71], v[96:97]\n"
    "v_pk_fma_f32 v[130:131], v[130:131], s[70:71], v[98:99]\n"
    "v_pk_add_f32 v[230:231], v[128:129], v[130:131]\n"
    "v_pk_mul_f32 v[232:233], v[128:129], v[130:131]\n"
    "v_pk_fma_f32 v[132:133], v[132:133], s[70:71], v[100:101]\n"
    "v_pk_fma_f32 v[230:231], v[230:231], v[132:133], v[232:233]\n"
    "v_pk_mul_f32 v[232:233], v[232:233], v[132:133]\n"
    "v_pk_fma_f32 v[134:135], v[134:135], s[70:71], v[102:103]\n"
    "v_pk_fma_f32 v[230:231], v[230:231], v[134:135], v[232:233]\n"
    "v_pk_mul_f32 v[232:233], v[232:233], v[134:135]\n"
    "v_pk_fma_f32 v[136:137], v[136:137], s[70:71], v[104:105]\n"
    "v_pk_fma_f32 v[230:231], v[230:231], v[136:137], v[232:233]\n"
    "v_pk_mul_f32 v[232:233], v[232:233], v[136:137]\n"
    "v_pk_fma_f32 v[138:139], v[138:139], s[70:71], v[106:107]\n"
    "v_pk_fma_f32 v[230:231], v[230:231], v[138:139], v[232:233]\n"
    "v_pk_mul_f32 v[232:233], v[232:233], v[138:139]\n"
    "v_pk_fma_f32 v[140:141], v[140:141], s[70:71], v[108:109]\n"
    "v_pk_fma_f32 v[230:231], v[230:231], v[140:141], v[232:233]\n"
    "v_pk_mul_f32 v[232:233], v[232:233], v[140:141]\n"
    "v_pk_fma_f32 v[142:143], v[142:143], s[70:71], v[110:111]\n"
    "v_pk_fma_f32 v[230:231], v[230:231], v[142:143], v[232:233]\n"
    "v_pk_mul_f32 v[232:233], v[232:233], v[142:143]\n"
    "v_rcp_f32 v232, v232\n"
    "v_rcp_f32 v233, v233\n"
    "v_fma_f32 v230, v230, v232, v247\n"
    "v_fmac_f32 v230, v231, v233\n"
    "v_lshlrev_b32 v232, 2, v249\n"
    "v_lshrrev_b32 v233, 2, v249\n"
    "v_and_b32 v232, 56, v232\n"
    "v_and_b32 v233, 4, v233\n"
    "v_add3_u32 v232, v232, v233, s81\n"
    "ds_read_b32 v232, v232\n"
    "s_add_u32 s81, s81, 64\n"
    "s_waitcnt lgkmcnt(0)\n"
    "v_lshlrev_b32 v232, 1, v232\n"
    "buffer_load_ushort v232, v232, s[44:47], 0 offen\n"
    "s_waitcnt vmcnt(0)\n"
    "v_lshlrev_b32 v232, 16, v232\n"
    "v_mul_f32 v230, v230, v232\n"
    "v_cvt_pk_bf16_f32 v230, v230, v230\n"
    "s_add_u32 s67, s67, 64\n"
    "s_waitcnt lgkmcnt(0)\n"
    "ds_write_b16 v245, v230\n"
    "ds_read_b64 v[228:229], v246\n"
    "s_cmp_le_u32 s34, s76\n"
    "s_cbranch_scc1 .Ldin_B0s_wait\n"
    ".Ldin_B0s_go:\n"
    "s_sub_u32 s94, s34, 60\n"
    "s_max_u32 s76, s76, s94\n"
    "s_add_u32 s76, s76, s72\n"
    ".Ldin_B0s_dma:\n"
    "buffer_load_dwordx4 v[236:237], s[40:43], 0 idxen offen offset:0 nt lds\n"
    "buffer_load_dwordx4 v[238:239], s[40:43], 0 idxen offen offset:1024 nt lds\n"
    ".Ldin_B0s_nd:\n"
    "s_branch .Ldin_G1s\n"
    ".Ldin_L2s_poll:\n"
    "s_sleep 1\n"
    "ds_read_b64 v[232:233], v253\n"
    "s_waitcnt lgkmcnt(0)\n"
    "v_readfirstlane_b32 s60, v232\n"
    "s_cmp_eq_u32 s60, -1\n"
    "s_cbranch_scc1 .Ldin_L2s_poll\n"
    "s_branch .Ldin_L2s_pr\n"
    ".Ldin_B0s_wait:\n"
    "s_cmp_lt_i32 s59, s57\n"
    "s_cbranch_scc1 .Ldin_B0s_w2\n"
    "s_sub_u32 s94, s67, s65\n"
    "s_cmp_gt_u32 s94, 256\n"
    "s_cbranch_scc1 .Ldin_B0s_dma\n"
    "s_cmp_eq_u32 s94, 64\n"
    "s_waitcnt vmcnt(2)\n"
    "s_cbranch_scc1 .Ldin_B0s_nd\n"
    "s_waitcnt vmcnt(0)\n"
    "s_branch .Ldin_B0s_nd\n"
    ".Ldin_B0s_slp:\n"
    "s_sleep 1\n"
    ".Ldin_B0s_w2:\n"
    "s_memrealtime s[34:35]\n"
    "s_waitcnt lgkmcnt(0)\n"
    "s_sub_u32 s94, s34, s76\n"
    "s_cmp_lt_i32 s94, 0\n"
    "s_cbranch_scc1 .Ldin_B0s_slp\n"
    "s_branch .Ldin_B0s_go\n"
    ".p2align 5\n"
    ".Ldin_L1s:\n"
    "s_setprio 0\n"
    "s_cmp_ge_i32 s59, s57\n"
    "s_cbranch_scc1 .Ldin_E1s\n"
    "s_add_u32 s76, s76, s73\n"
    "v_mov_b32 v253, s63\n"
    "v_and_b32 v184, v254, v184\n"
    "v_and_b32 v185, v255, v185\n"
    "v_and_b32 v186, v254, v186\n"
    "v_and_b32 v187, v255, v187\n"
    "v_and_b32 v188, v254, v188\n"
    "v_and_b32 v189, v255, v189\n"
    "v_and_b32 v190, v254, v190\n"
    "v_and_b32_e64 v191, v255, v191\n"
    "ds_read_b64 v[232:233], v253\n"
    "v_mfma_f32_4x4x4_16b_bf16 v[128:131], v[184:185], v[32:33], v[64:67]\n"
    "ds_read_b128 v[112:115], v252 offset:17408\n"
    "v_mfma_f32_4x4x4_16b_bf16 v[132:135], v[186:187], v[34:35], v[68:71]\n"
    "ds_read_b128 v[116:119], v252 offset:17424\n"
    "v_mfma_f32_4x4x4_16b_bf16 v[136:139], v[188:189], v[36:37], v[72:75]\n"
    "ds_read_b128 v[120:123], v252 offset:17536\n"
    "ds_read_b128 v[124:127], v252 offset:17552\n"
    "v_cvt_pk_bf16_f32 v0, v128, v129\n"
    "v_cvt_pk_bf16_f32 v1, v130, v131\n"
    "v_mfma_f32_4x4x4_16b_bf16 v[140:143], v[190:191], v[38:39], v[76:79]\n"
    "v_cvt_pk_bf16_f32 v2, v132, v133\n"
    "v_cvt_pk_bf16_f32 v3, v134, v135\n"
    "v_mfma_f32_4x4x4_16b_bf16 v[128:131], v[184:185], v[40:41], v[80:83]\n"
    "v_cvt_pk_bf16_f32 v4, v136, v137\n"
    "v_cvt_pk_bf16_f32 v5, v138, v139\n"
    "v_mfma_f32_4x4x4_16b_bf16 v[132:135], v[186:187], v[42:43], v[84:87]\n"
    "v_cvt_pk_bf16_f32 v6, v140, v141\n"
    "v_cvt_pk_bf16_f32 v7, v142, v143\n"
    "v_mfma_f32_4x4x4_16b_bf16 v[136:139], v[188:189], v[44:45], v[88:91]\n"
    "v_cvt_pk_bf16_f32 v8, v128, v129\n"
    "v_cvt_pk_bf16_f32 v9, v130, v131\n"
    "v_mfma_f32_4x4x4_16b_bf16 v[140:143], v[190:191], v[46:47], v[92:95]\n"
    "v_cvt_pk_bf16_f32 v10, v132, v133\n"
    "v_cvt_pk_bf16_f32 v11, v134, v135\n"
    "v_mfma_f32_4x4x4_16b_bf16 v[128:131], v[184:185], v[48:49], v[192:195]\n"
    "v_cvt_pk_bf16_f32 v12, v136, v137\n"
    "v_cvt_pk_bf16_f32 v13, v138, v139\n"
    "v_mfma_f32_4x4x4_16b_bf16 v[132:135], v[186:187], v[50:51], v[196:199]\n"
    "v_cvt_pk_bf16_f32 v14, v140, v141\n"
    "v_cvt_pk_bf16_f32 v15, v142, v143\n"
    "v_mfma_f32_4x4x4_16b_bf16 v[136:139], v[188:189], v[52:53], v[200:203]\n"
    "s_waitcnt lgkmcnt(0)\n"
    "v_readfirstlane_b32 s60, v232\n"
    "v_cvt_pk_bf16_f32 v16, v128, v129\n"
    "v_cvt_pk_bf16_f32 v17, v130, v131\n"
    "v_mfma_f32_4x4x4_16b_bf16 v[140:143], v[190:191], v[54:55], v[204:207]\n"
    "s_cmp_eq_u32 s60, -1\n"
    "s_cbranch_scc1 .Ldin_L1s_poll\n"
    ".Ldin_L1s_pr:\n"
    "v_readfirstlane_b32 s95, v233\n"
    "s_and_b32 s94, s60, 63\n"
    "s_lshl_b32 s94, s94, 6\n"
    "s_add_u32 s94, s94, 68480\n"
    "s_add_u32 s63, s63, 8\n"
    "v_lshl_add_u32 v252, v249, 1, s94\n"
    "ds_read_b32 v252, v252\n"
    "s_sub_u32 s95, s95, s76\n"
    "s_ashr_i32 s95, s95, 4\n"
    "s_min_i32 s95, s95, s74\n"
    "s_max_i32 s72, s95, s75\n"
    "v_cvt_pk_bf16_f32 v18, v132, v133\n"
    "v_cvt_pk_bf16_f32 v19, v134, v135\n"
    "v_mfma_f32_4x4x4_16b_bf16 v[128:131], v[184:185], v[56:57], v[208:211]\n"
    "v_cvt_pk_bf16_f32 v20, v136, v137\n"
    "v_cvt_pk_bf16_f32 v21, v138, v139\n"
    "v_mfma_f32_4x4x4_16b_bf16 v[132:135], v[186:187], v[58:59], v[212:215]\n"
    "v_cvt_pk_bf16_f32 v22, v140, v141\n"
    "v_cvt_pk_bf16_f32 v23, v142, v143\n"
    "v_mfma_f32_4x4x4_16b_bf16 v[136:139], v[188:189], v[60:61], v[216:219]\n"
    "v_cvt_pk_bf16_f32 v24, v128, v129\n"
    "v_cvt_pk_bf16_f32 v25, v130, v131\n"
    "v_mfma_f32_4x4x4_16b_bf16 v[140:143], v[190:191], v[62:63], v[220:223]\n"
    "v_cvt_pk_bf16_f32 v26, v132, v133\n"
    "v_cvt_pk_bf16_f32 v27, v134, v135\n"
    "v_cvt_pk_bf16_f32 v28, v136, v137\n"
    "v_cvt_pk_bf16_f32 v29, v138, v139\n"
    "v_exp_f32 v152, v152 clamp\n"
    "v_cvt_pk_bf16_f32 v30, v140, v141\n"
    "v_cvt_pk_bf16_f32 v31, v142, v143\n"
    "v_mfma_f32_16x16x32_bf16 v[128:131], v[0:3], v[160:163], v[112:115]\n"
    "v_exp_f32 v144, v144 clamp\n"
    "v_mfma_f32_16x16x32_bf16 v[128:131], v[4:7], v[164:167], v[128:131]\n"
    "v_exp_f32 v145, v145 clamp\n"
    "v_mfma_f32_16x16x32_bf16 v[132:135], v[8:11], v[160:163], v[116:119]\n"
    "v_exp_f32 v146, v146 clamp\n"
    "v_mfma_f32_16x16x32_bf16 v[132:135], v[12:15], v[164:167], v[132:135]\n"
    "v_exp_f32 v147, v147 clamp\n"
    "v_mfma_f32_16x16x32_bf16 v[136:139], v[16:19], v[160:163], v[120:123]\n"
    "v_exp_f32 v148, v148 clamp\n"
    "v_mfma_f32_16x16x32_bf16 v[136:139], v[20:23], v[164:167], v[136:139]\n"
    "v_exp_f32 v149, v149 clamp\n"
    "v_mfma_f32_16x16x32_bf16 v[140:143], v[24:27], v[160:163], v[124:127]\n"
    "v_exp_f32 v150, v150 clamp\n"
    "v_mfma_f32_16x16x32_bf16 v[140:143], v[28:31], v[164:167], v[140:143]\n"
    "v_exp_f32 v151, v151 clamp\n"
    "s_add_u32 m0, s68, 4096\n"
    "v_exp_f32 v153, v153 clamp\n"
    "v_add_u32 v253, s67, v244\n"
    "v_mfma_f32_4x4x4_16b_bf16 v[224:227], v[228:229], v[168:169], v[224:227] cbsz:4 abid:0\n"
    "v_exp_f32 v154, v154 clamp\n"
    "v_exp_f32 v155, v155 clamp\n"
    "v_mfma_f32_4x4x4_16b_bf16 v[224:227], v[228:229], v[170:171], v[224:227] cbsz:4 abid:1\n"
    "v_exp_f32 v156, v156 clamp\n"
    "v_exp_f32 v157, v157 clamp\n"
    "v_mfma_f32_4x4x4_16b_bf16 v[224:227], v[228:229], v[172:173], v[224:227] cbsz:4 abid:2\n"
    "v_exp_f32 v158, v158 clamp\n"
    "v_exp_f32 v159, v159 clamp\n"
    "v_mfma_f32_4x4x4_16b_bf16 v[224:227], v[228:229], v[174:175], v[224:227] cbsz:4 abid:3\n"
    "s_waitcnt vmcnt(4)\n"
    "ds_read_b64_tr_b16 v[168:169], v240 offset:4096\n"
    "ds_read_b64_tr_b16 v[170:171], v241 offset:4352\n"
    "ds_read_b64_tr_b16 v[172:173], v242 offset:4608\n"
    "ds_read_b64_tr_b16 v[174:175], v243 offset:4864\n"
    "ds_read_b32 v236, v253 offset:0\n"
    "ds_read_b32 v238, v253 offset:4\n"
    "ds_read_b128 v[160:163], v234 offset:6144\n"
    "ds_read_b128 v[164:167], v235 offset:6144\n"
    "s_waitcnt lgkmcnt(8)\n"
    "s_nop 0\n"
    "v_readlane_b32 s20, v252, 0\n"
    "v_readlane_b32 s21, v252, 1\n"
    "v_readlane_b32 s22, v252, 2\n"
    "v_readlane_b32 s23, v252, 3\n"
    "v_readlane_b32 s24, v252, 4\n"
    "v_readlane_b32 s25, v252, 5\n"
    "v_readlane_b32 s26, v252, 6\n"
    "v_readlane_b32 s27, v252, 7\n"
    "v_pk_fma_f32 v[144:145], v[144:145], s[70:71], v[96:97]\n"
    "v_pk_fma_f32 v[146:147], v[146:147], s[70:71], v[98:99]\n"
    "v_pk_add_f32 v[230:231], v[144:145], v[146:147]\n"
    "v_pk_mul_f32 v[232:233], v[144:145], v[146:147]\n"
    "v_pk_fma_f32 v[148:149], v[148:149], s[70:71], v[100:101]\n"
    "v_pk_fma_f32 v[230:231], v[230:231], v[148:149], v[232:233]\n"
    "v_pk_mul_f32 v[232:233], v[232:233], v[148:149]\n"
    "v_pk_fma_f32 v[150:151], v[150:151], s[70:71], v[102:103]\n"
    "v_pk_fma_f32 v[230:231], v[230:231], v[150:151], v[232:233]\n"
    "v_pk_mul_f32 v[232:233], v[232:233], v[150:151]\n"
    "v_pk_fma_f32 v[152:153], v[152:153], s[70:71], v[104:105]\n"
    "v_pk_fma_f32 v[230:231], v[230:231], v[152:153], v[232:233]\n"
    "v_pk_mul_f32 v[232:233], v[232:233], v[152:153]\n"
    "v_pk_fma_f32 v[154:155], v[154:155], s[70:71], v[106:107]\n"
    "v_pk_fma_f32 v[230:231], v[230:231], v[154:155], v[232:233]\n"
    "v_pk_mul_f32 v[232:233], v[232:233], v[154:155]\n"
    "v_pk_fma_f32 v[156:157], v[156:157], s[70:71], v[108:109]\n"
    "v_pk_fma_f32 v[230:231], v[230:231], v[156:157], v[232:233]\n"
    "v_pk_mul_f32 v[232:233], v[232:233], v[156:157]\n"
    "v_pk_fma_f32 v[158:159], v[158:159], s[70:71], v[110:111]\n"
    "v_pk_fma_f32 v[230:231], v[230:231], v[158:159], v[232:233]\n"
    "v_pk_mul_f32 v[232:233], v[232:233], v[158:159]\n"
    "v_rcp_f32 v232, v232\n"
    "v_rcp_f32 v233, v233\n"
    "v_fma_f32 v230, v230, v232, v247\n"
    "v_fmac_f32 v230, v231, v233\n"
    "v_lshlrev_b32 v232, 2, v249\n"
    "v_lshrrev_b32 v233, 2, v249\n"
    "v_and_b32 v232, 56, v232\n"
    "v_and_b32 v233, 4, v233\n"
    "v_add3_u32 v232, v232, v233, s81\n"
    "ds_read_b32 v232, v232\n"
    "s_add_u32 s81, s81, 64\n"
    "s_waitcnt lgkmcnt(0)\n"
    "v_lshlrev_b32 v232, 1, v232\n"
    "buffer_load_ushort v232, v232, s[44:47], 0 offen\n"
    "s_waitcnt vmcnt(0)\n"
    "v_lshlrev_b32 v232, 16, v232\n"
    "v_mul_f32 v230, v230, v232\n"
    "v_cvt_pk_bf16_f32 v230, v230, v230\n"
    "s_add_u32 s67, s67, 64\n"
    "s_waitcnt lgkmcnt(0)\n"
    "ds_write_b16 v245, v230\n"
    "ds_read_b64 v[228:229], v246\n"
    "s_lshl1_add_u32 s76, s72, s76\n"
    "buffer_load_dwordx4 v[236:237], s[40:43], 0 idxen offen offset:0 nt lds\n"
    "buffer_load_dwordx4 v[238:239], s[40:43], 0 idxen offen offset:1024 nt lds\n"
    "s_and_b32 s62, s58, 63\n"
    "s_mul_i32 s62, s62, 272\n"
    "s_mov_b32 s58, s59\n"
    "s_mov_b32 s59, s60\n"
    "s_mov_b32 s81, s65\n"
    "s_xor_b32 s65, s65, s64\n"
    "s_mov_b32 s80, s83\n"
    "s_add_u32 s82, s65, 256\n"
    "s_cmp_lt_i32 s59, s57\n"
    "s_cselect_b32 s76, s76, -1\n"
    "s_bitcmp1_b32 s58, 30\n"
    "s_cbranch_scc0 .Ldin_A2\n"
    ".Ldin_A2s:\n"
    "v_mfma_f32_16x16x32_bf16 v[144:147], v[0:3], v[160:163], v[112:115]\n"
    "v_exp_f32 v128, v128 clamp\n"
    "v_mfma_f32_16x16x32_bf16 v[144:147], v[4:7], v[164:167], v[144:147]\n"
    "v_exp_f32 v129, v129 clamp\n"
    "v_mfma_f32_16x16x32_bf16 v[148:151], v[8:11], v[160:163], v[116:119]\n"
    "v_exp_f32 v130, v130 clamp\n"
    "v_mfma_f32_16x16x32_bf16 v[148:151], v[12:15], v[164:167], v[148:151]\n"
    "v_exp_f32 v131, v131 clamp\n"
    "v_mfma_f32_16x16x32_bf16 v[152:155], v[16:19], v[160:163], v[120:123]\n"
    "v_exp_f32 v132, v132 clamp\n"
    "v_mfma_f32_16x16x32_bf16 v[152:155], v[20:23], v[164:167], v[152:155]\n"
    "v_exp_f32 v133, v133 clamp\n"
    "v_mfma_f32_16x16x32_bf16 v[156:159], v[24:27], v[160:163], v[124:127]\n"
    "v_exp_f32 v134, v134 clamp\n"
    "v_mfma_f32_16x16x32_bf16 v[156:159], v[28:31], v[164:167], v[156:159]\n"
    "v_exp_f32 v135, v135 clamp\n"
    "v_exp_f32 v136, v136 clamp\n"
    "s_add_u32 m0, s68, 6144\n"
    "v_exp_f32 v137, v137 clamp\n"
    "v_add_u32_e64 v253, s67, v244\n"
    "s_waitcnt lgkmcnt(0)\n"
    "v_mfma_f32_4x4x4_16b_bf16 v[224:227], v[228:229], v[176:177], v[224:227] cbsz:4 abid:0\n"
    "v_exp_f32 v138, v138 clamp\n"
    "v_exp_f32 v139, v139 clamp\n"
    "v_mfma_f32_4x4x4_16b_bf16 v[224:227], v[228:229], v[178:179], v[224:227] cbsz:4 abid:1\n"
    "v_exp_f32 v140, v140 clamp\n"
    "v_exp_f32 v141, v141 clamp\n"
    "v_mfma_f32_4x4x4_16b_bf16 v[224:227], v[228:229], v[180:181], v[224:227] cbsz:4 abid:2\n"
    "v_exp_f32 v142, v142 clamp\n"
    "v_exp_f32 v143, v143 clamp\n"
    "v_mfma_f32_4x4x4_16b_bf16 v[224:227], v[228:229], v[182:183], v[224:227] cbsz:4 abid:3\n"
    "s_waitcnt vmcnt(4)\n"
    "ds_read_b64_tr_b16 v[176:177], v240 offset:6144\n"
    "ds_read_b64_tr_b16 v[178:179], v241 offset:6400\n"
    "ds_read_b64_tr_b16 v[180:181], v242 offset:6656\n"
    "ds_read_b64_tr_b16 v[182:183], v243 offset:6912\n"
    "ds_read_b32 v236, v253 offset:0\n"
    "ds_read_b32 v238, v253 offset:4\n"
    "ds_read_b128 v[160:163], v234 offset:0\n"
    "ds_read_b128 v[164:167], v235 offset:0\n"
    "s_cbranch_vccz .Ldin_A2s_np\n"
    "s_setprio 1\n"
    ".Ldin_A2s_np:\n"
    "v_pk_fma_f32 v[128:129], v[128:129], s[70:71], v[96:97]\n"
    "v_pk_fma_f32 v[130:131], v[130:131], s[70:71], v[98:99]\n"
    "v_add_f32 v191, v224, v225\n"
    "v_add_f32 v190, v226, v227\n"
    "v_add_f32 v191, v191, v190\n"
    "v_add_u32 v190, s62, v249\n"
    "v_mul_f32 v191, s69, v191\n"
    "v_cvt_pk_bf16_f32 v191, v191, v191\n"
    "ds_write_b16 v190, v191 offset:32768\n"
    "s_bfe_u32 s94, s59, 0x180006\n"
    "s_mul_i32 s86, s94, s56\n"
    "v_mbcnt_lo_u32_b32 v184, s20, 0\n"
    "v_mbcnt_lo_u32_b32 v185, s22, 0\n"
    "v_mbcnt_lo_u32_b32 v186, s24, 0\n"
    "v_mbcnt_lo_u32_b32 v187, s26, 0\n"
    "s_bcnt1_i32_b64 s90, s[20:21]\n"
    "s_bcnt1_i32_b64 s91, s[22:23]\n"
    "s_bcnt1_i32_b64 s92, s[24:25]\n"
    "s_bcnt1_i32_b64 s93, s[26:27]\n"
    "v_mbcnt_hi_u32_b32 v184, s21, v184\n"
    "v_mbcnt_hi_u32_b32 v185, s23, v185\n"
    "v_mbcnt_hi_u32_b32 v186, s25, v186\n"
    "v_mbcnt_hi_u32_b32 v187, s27, v187\n"
    "s_lshl2_add_u32 s90, s90, s65\n"
    "s_lshl2_add_u32 s91, s91, s90\n"
    "s_lshl2_add_u32 s92, s92, s91\n"
    "s_lshl2_add_u32 s93, s93, s92\n"
    "v_add_u32_e64 v188, s86, v251\n"
    "v_pk_add_f32 v[230:231], v[128:129], v[130:131]\n"
    "v_pk_mul_f32 v[232:233], v[128:129], v[130:131]\n"
    "v_pk_fma_f32 v[132:133], v[132:133], s[70:71], v[100:101]\n"
    "v_pk_fma_f32 v[230:231], v[230:231], v[132:133], v[232:233]\n"
    "v_pk_mul_f32 v[232:233], v[232:233], v[132:133]\n"
    "v_lshl_add_u32 v184, v184, 2, s65\n"
    "v_lshl_add_u32 v185, v185, 2, s90\n"
    "v_lshl_add_u32 v186, v186, 2, s91\n"
    "v_lshl_add_u32 v187, v187, 2, s92\n"
    "v_mov_b32 v191, -1\n"
    "v_lshl_add_u32 v190, v249, 1, s93\n"
    "ds_write2_b32 v190, v191, v191 offset1:64\n"
    "s_mov_b64 exec, s[20:21]\n"
    "ds_write_b32 v184, v188\n"
    "s_mov_b64 exec, s[22:23]\n"
    "v_add_u32 v189, 64, v188\n"
    "ds_write_b32 v185, v189\n"
    "s_mov_b64 exec, s[24:25]\n"
    "v_add_u32 v190, 128, v188\n"
    "ds_write_b32 v186, v190\n"
    "s_mov_b64 exec, s[26:27]\n"
    "v_add_u32 v189, 192, v188\n"
    "ds_write_b32 v187, v189\n"
    "s_mov_b64 exec, -1\n"
    "s_add_u32 s95, s93, 60\n"
    "s_add_u32 s94, s82, 128\n"
    "s_andn2_b32 s95, s95, 63\n"
    "s_max_u32 s83, s95, s94\n"
    "v_pk_fma_f32 v[134:135], v[134:135], s[70:71], v[102:103]\n"
    "v_pk_fma_f32 v[230:231], v[230:231], v[134:135], v[232:233]\n"
    "v_pk_mul_f32 v[232:233], v[232:233], v[134:135]\n"
    "v_pk_fma_f32 v[136:137], v[136:137], s[70:71], v[104:105]\n"
    "v_pk_fma_f32 v[230:231], v[230:231], v[136:137], v[232:233]\n"
    "v_pk_mul_f32 v[232:233], v[232:233], v[136:137]\n"
    "v_pk_fma_f32 v[138:139], v[138:139], s[70:71], v[106:107]\n"
    "v_pk_fma_f32 v[230:231], v[230:231], v[138:139], v[232:233]\n"
    "v_pk_mul_f32 v[232:233], v[232:233], v[138:139]\n"
    "v_pk_fma_f32 v[140:141], v[140:141], s[70:71], v[108:109]\n"
    "v_pk_fma_f32 v[230:231], v[230:231], v[140:141], v[232:233]\n"
    "v_pk_mul_f32 v[232:233], v[232:233], v[140:141]\n"
    "v_pk_fma_f32 v[142:143], v[142:143], s[70:71], v[110:111]\n"
    "v_pk_fma_f32 v[230:231], v[230:231], v[142:143], v[232:233]\n"
    "v_pk_mul_f32 v[232:233], v[232:233], v[142:143]\n"
    "v_rcp_f32 v232, v232\n"
    "v_rcp_f32 v233, v233\n"
    "v_fma_f32 v230, v230, v232, v247\n"
    "v_fmac_f32 v230, v231, v233\n"
    "v_lshlrev_b32 v232, 2, v249\n"
    "v_lshrrev_b32 v233, 2, v249\n"
    "v_and_b32 v232, 56, v232\n"
    "v_and_b32 v233, 4, v233\n"
    "v_add3_u32 v232, v232, v233, s81\n"
    "ds_read_b32 v232, v232\n"
    "s_add_u32 s81, s81, 64\n"
    "s_waitcnt lgkmcnt(0)\n"
    "v_lshlrev_b32 v232, 1, v232\n"
    "buffer_load_ushort v232, v232, s[44:47], 0 offen\n"
    "s_waitcnt vmcnt(0)\n"
    "v_lshlrev_b32 v232, 16, v232\n"
    "v_mul_f32 v230, v230, v232\n"
    "v_cvt_pk_bf16_f32 v230, v230, v230\n"
    "s_add_u32 s67, s67, 64\n"
    "s_waitcnt lgkmcnt(0)\n"
    "ds_write_b16 v245, v230\n"
    "ds_read_b64 v[228:229], v246\n"
    "buffer_load_dwordx4 v[236:237], s[40:43], 0 idxen offen offset:0 nt lds\n"
    "buffer_load_dwordx4 v[238:239], s[40:43], 0 idxen offen offset:1024 nt lds\n"
    ".Ldin_B3s:\n"
    "v_mfma_f32_16x16x32_bf16 v[128:131], v[0:3], v[160:163], v[112:115]\n"
    "v_exp_f32 v144, v144 clamp\n"
    "v_mfma_f32_16x16x32_bf16 v[128:131], v[4:7], v[164:167], v[128:131]\n"
    "v_exp_f32 v145, v145 clamp\n"
    "v_mfma_f32_16x16x32_bf16 v[132:135], v[8:11], v[160:163], v[116:119]\n"
    "v_exp_f32 v146, v146 clamp\n"
    "v_mfma_f32_16x16x32_bf16 v[132:135], v[12:15], v[164:167], v[132:135]\n"
    "v_exp_f32 v147, v147 clamp\n"
    "v_mfma_f32_16x16x32_bf16 v[136:139], v[16:19], v[160:163], v[120:123]\n"
    "v_exp_f32 v148, v148 clamp\n"
    "v_mfma_f32_16x16x32_bf16 v[136:139], v[20:23], v[164:167], v[136:139]\n"
    "v_exp_f32 v149, v149 clamp\n"
    "v_mfma_f32_16x16x32_bf16 v[140:143], v[24:27], v[160:163], v[124:127]\n"
    "v_exp_f32 v150, v150 clamp\n"
    "v_mfma_f32_16x16x32_bf16 v[140:143], v[28:31], v[164:167], v[140:143]\n"
    "v_exp_f32 v151, v151 clamp\n"
    "v_exp_f32 v152, v152 clamp\n"
    "s_cmp_eq_u32 s67, s80\n"
    "s_cselect_b32 s67, s65, s67\n"
    "s_and_b32 s94, s59, 63\n"
    "s_mul_i32 s94, s94, 272\n"
    "v_add_u32 v252, s94, v250\n"
    "s_add_u32 m0, s68, 0\n"
    "v_exp_f32 v153, v153 clamp\n"
    "v_add_u32_e64 v253, s67, v244\n"
    "s_waitcnt lgkmcnt(0)\n"
    "v_mfma_f32_4x4x4_16b_bf16 v[224:227], v[228:229], v[168:169], 0 cbsz:4 abid:0\n"
    "v_exp_f32 v154, v154 clamp\n"
    "v_exp_f32 v155, v155 clamp\n"
    "v_mfma_f32_4x4x4_16b_bf16 v[224:227], v[228:229], v[170:171], v[224:227] cbsz:4 abid:1\n"
    "v_exp_f32 v156, v156 clamp\n"
    "v_exp_f32 v157, v157 clamp\n"
    "v_mfma_f32_4x4x4_16b_bf16 v[224:227], v[228:229], v[172:173], v[224:227] cbsz:4 abid:2\n"
    "v_exp_f32 v158, v158 clamp\n"
    "v_exp_f32 v159, v159 clamp\n"
    "v_mfma_f32_4x4x4_16b_bf16 v[224:227], v[228:229], v[174:175], v[224:227] cbsz:4 abid:3\n"
    "s_waitcnt vmcnt(4)\n"
    "s_memrealtime s[34:35]\n"
    "ds_read_b64_tr_b16 v[168:169], v240 offset:0\n"
    "ds_read_b64_tr_b16 v[170:171], v241 offset:256\n"
    "ds_read_b64_tr_b16 v[172:173], v242 offset:512\n"
    "ds_read_b64_tr_b16 v[174:175], v243 offset:768\n"
    "ds_read_b32 v236, v253 offset:0\n"
    "ds_read_b32 v238, v253 offset:4\n"
    "ds_read_b128 v[160:163], v234 offset:2048\n"
    "ds_read_b128 v[164:167], v235 offset:2048\n"
    "ds_read_b128 v[184:187], v252 offset:0\n"
    "ds_read_b128 v[188:191], v252 offset:16\n"
    "v_pk_fma_f32 v[144:145], v[144:145], s[70:71], v[96:97]\n"
    "v_pk_fma_f32 v[146:147], v[146:147], s[70:71], v[98:99]\n"
    "v_pk_add_f32 v[230:231], v[144:145], v[146:147]\n"
    "v_pk_mul_f32 v[232:233], v[144:145], v[146:147]\n"
    "v_pk_fma_f32 v[148:149], v[148:149], s[70:71], v[100:101]\n"
    "v_pk_fma_f32 v[230:231], v[230:231], v[148:149], v[232:233]\n"
    "v_pk_mul_f32 v[232:233], v[232:233], v[148:149]\n"
    "v_pk_fma_f32 v[150:151], v[150:151], s[70:71], v[102:103]\n"
    "v_pk_fma_f32 v[230:231], v[230:231], v[150:151], v[232:233]\n"
    "v_pk_mul_f32 v[232:233], v[232:233], v[150:151]\n"
    "v_pk_fma_f32 v[152:153], v[152:153], s[70:71], v[104:105]\n"
    "v_pk_fma_f32 v[230:231], v[230:231], v[152:153], v[232:233]\n"
    "v_pk_mul_f32 v[232:233], v[232:233], v[152:153]\n"
    "v_pk_fma_f32 v[154:155], v[154:155], s[70:71], v[106:107]\n"
    "v_pk_fma_f32 v[230:231], v[230:231], v[154:155], v[232:233]\n"
    "v_pk_mul_f32 v[232:233], v[232:233], v[154:155]\n"
    "v_pk_fma_f32 v[156:157], v[156:157], s[70:71], v[108:109]\n"
    "v_pk_fma_f32 v[230:231], v[230:231], v[156:157], v[232:233]\n"
    "v_pk_mul_f32 v[232:233], v[232:233], v[156:157]\n"
    "v_pk_fma_f32 v[158:159], v[158:159], s[70:71], v[110:111]\n"
    "v_pk_fma_f32 v[230:231], v[230:231], v[158:159], v[232:233]\n"
    "v_pk_mul_f32 v[232:233], v[232:233], v[158:159]\n"
    "v_rcp_f32 v232, v232\n"
    "v_rcp_f32 v233, v233\n"
    "v_fma_f32 v230, v230, v232, v247\n"
    "v_fmac_f32 v230, v231, v233\n"
    "v_lshlrev_b32 v232, 2, v249\n"
    "v_lshrrev_b32 v233, 2, v249\n"
    "v_and_b32 v232, 56, v232\n"
    "v_and_b32 v233, 4, v233\n"
    "v_add3_u32 v232, v232, v233, s81\n"
    "ds_read_b32 v232, v232\n"
    "s_add_u32 s81, s81, 64\n"
    "s_waitcnt lgkmcnt(0)\n"
    "v_lshlrev_b32 v232, 1, v232\n"
    "buffer_load_ushort v232, v232, s[44:47], 0 offen\n"
    "s_waitcnt vmcnt(0)\n"
    "v_lshlrev_b32 v232, 16, v232\n"
    "v_mul_f32 v230, v230, v232\n"
    "v_cvt_pk_bf16_f32 v230, v230, v230\n"
    "s_add_u32 s67, s67, 64\n"
    "s_waitcnt lgkmcnt(0)\n"
    "ds_write_b16 v245, v230\n"
    "ds_read_b64 v[228:229], v246\n"
    "s_cmp_le_u32 s34, s76\n"
    "s_cbranch_scc1 .Ldin_B3s_wait\n"
    ".Ldin_B3s_go:\n"
    "s_sub_u32 s94, s34, 60\n"
    "s_max_u32 s76, s76, s94\n"
    "s_add_u32 s76, s76, s72\n"
    ".Ldin_B3s_dma:\n"
    "buffer_load_dwordx4 v[236:237], s[40:43], 0 idxen offen offset:0 nt lds\n"
    "buffer_load_dwordx4 v[238:239], s[40:43], 0 idxen offen offset:1024 nt lds\n"
    ".Ldin_B3s_nd:\n"
    "s_branch .Ldin_G0s\n"
    ".Ldin_L1s_poll:\n"
    "s_sleep 1\n"
    "ds_read_b64 v[232:233], v253\n"
    "s_waitcnt lgkmcnt(0)\n"
    "v_readfirstlane_b32 s60, v232\n"
    "s_cmp_eq_u32 s60, -1\n"
    "s_cbranch_scc1 .Ldin_L1s_poll\n"
    "s_branch .Ldin_L1s_pr\n"
    ".Ldin_B3s_wait:\n"
    "s_cmp_lt_i32 s59, s57\n"
    "s_cbranch_scc1 .Ldin_B3s_w2\n"
    "s_sub_u32 s94, s67, s65\n"
    "s_cmp_gt_u32 s94, 256\n"
    "s_cbranch_scc1 .Ldin_B3s_dma\n"
    "s_cmp_eq_u32 s94, 64\n"
    "s_waitcnt vmcnt(2)\n"
    "s_cbranch_scc1 .Ldin_B3s_nd\n"
    "s_waitcnt vmcnt(0)\n"
    "s_branch .Ldin_B3s_nd\n"
    ".Ldin_B3s_slp:\n"
    "s_sleep 1\n"
    ".Ldin_B3s_w2:\n"
    "s_memrealtime s[34:35]\n"
    "s_waitcnt lgkmcnt(0)\n"
    "s_sub_u32 s94, s34, s76\n"
    "s_cmp_lt_i32 s94, 0\n"
    "s_cbranch_scc1 .Ldin_B3s_slp\n"
    "s_branch .Ldin_B3s_go\n"
    ".p2align 5\n"
    ".Ldin_L0s:\n"
    "s_setprio 0\n"
    "s_cmp_ge_i32 s59, s57\n"
    "s_cbranch_scc1 .Ldin_E0s\n"
    "s_add_u32 s76, s76, s73\n"
    "v_mov_b32 v253, s63\n"
    "v_and_b32 v184, v254, v184\n"
    "v_and_b32 v185, v255, v185\n"
    "v_and_b32 v186, v254, v186\n"
    "v_and_b32 v187, v255, v187\n"
    "v_and_b32 v188, v254, v188\n"
    "v_and_b32 v189, v255, v189\n"
    "v_and_b32 v190, v254, v190\n"
    "v_and_b32_e64 v191, v255, v191\n"
    "ds_read_b64 v[232:233], v253\n"
    "v_mfma_f32_4x4x4_16b_bf16 v[144:147], v[184:185], v[32:33], v[64:67]\n"
    "ds_read_b128 v[112:115], v252 offset:17408\n"
    "v_mfma_f32_4x4x4_16b_bf16 v[148:151], v[186:187], v[34:35], v[68:71]\n"
    "ds_read_b128 v[116:119], v252 offset:17424\n"
    "v_mfma_f32_4x4x4_16b_bf16 v[152:155], v[188:189], v[36:37], v[72:75]\n"
    "ds_read_b128 v[120:123], v252 offset:17536\n"
    "ds_read_b128 v[124:127], v252 offset:17552\n"
    "v_cvt_pk_bf16_f32 v0, v144, v145\n"
    "v_cvt_pk_bf16_f32 v1, v146, v147\n"
    "v_mfma_f32_4x4x4_16b_bf16 v[156:159], v[190:191], v[38:39], v[76:79]\n"
    "v_cvt_pk_bf16_f32 v2, v148, v149\n"
    "v_cvt_pk_bf16_f32 v3, v150, v151\n"
    "v_mfma_f32_4x4x4_16b_bf16 v[144:147], v[184:185], v[40:41], v[80:83]\n"
    "v_cvt_pk_bf16_f32 v4, v152, v153\n"
    "v_cvt_pk_bf16_f32 v5, v154, v155\n"
    "v_mfma_f32_4x4x4_16b_bf16 v[148:151], v[186:187], v[42:43], v[84:87]\n"
    "v_cvt_pk_bf16_f32 v6, v156, v157\n"
    "v_cvt_pk_bf16_f32 v7, v158, v159\n"
    "v_mfma_f32_4x4x4_16b_bf16 v[152:155], v[188:189], v[44:45], v[88:91]\n"
    "v_cvt_pk_bf16_f32 v8, v144, v145\n"
    "v_cvt_pk_bf16_f32 v9, v146, v147\n"
    "v_mfma_f32_4x4x4_16b_bf16 v[156:159], v[190:191], v[46:47], v[92:95]\n"
    "v_cvt_pk_bf16_f32 v10, v148, v149\n"
    "v_cvt_pk_bf16_f32 v11, v150, v151\n"
    "v_mfma_f32_4x4x4_16b_bf16 v[144:147], v[184:185], v[48:49], v[192:195]\n"
    "v_cvt_pk_bf16_f32 v12, v152, v153\n"
    "v_cvt_pk_bf16_f32 v13, v154, v155\n"
    "v_mfma_f32_4x4x4_16b_bf16 v[148:151], v[186:187], v[50:51], v[196:199]\n"
    "v_cvt_pk_bf16_f32 v14, v156, v157\n"
    "v_cvt_pk_bf16_f32 v15, v158, v159\n"
    "v_mfma_f32_4x4x4_16b_bf16 v[152:155], v[188:189], v[52:53], v[200:203]\n"
    "s_waitcnt lgkmcnt(0)\n"
    "v_readfirstlane_b32 s60, v232\n"
    "v_cvt_pk_bf16_f32 v16, v144, v145\n"
    "v_cvt_pk_bf16_f32 v17, v146, v147\n"
    "v_mfma_f32_4x4x4_16b_bf16 v[156:159], v[190:191], v[54:55], v[204:207]\n"
    "s_cmp_eq_u32 s60, -1\n"
    "s_cbranch_scc1 .Ldin_L0s_poll\n"
    ".Ldin_L0s_pr:\n"
    "v_readfirstlane_b32 s95, v233\n"
    "s_and_b32 s94, s60, 63\n"
    "s_lshl_b32 s94, s94, 6\n"
    "s_add_u32 s94, s94, 68480\n"
    "s_add_u32 s63, s63, 8\n"
    "v_lshl_add_u32 v252, v249, 1, s94\n"
    "ds_read_b32 v252, v252\n"
    "s_sub_u32 s95, s95, s76\n"
    "s_ashr_i32 s95, s95, 4\n"
    "s_min_i32 s95, s95, s74\n"
    "s_max_i32 s72, s95, s75\n"
    "v_cvt_pk_bf16_f32 v18, v148, v149\n"
    "v_cvt_pk_bf16_f32 v19, v150, v151\n"
    "v_mfma_f32_4x4x4_16b_bf16 v[144:147], v[184:185], v[56:57], v[208:211]\n"
    "v_cvt_pk_bf16_f32 v20, v152, v153\n"
    "v_cvt_pk_bf16_f32 v21, v154, v155\n"
    "v_mfma_f32_4x4x4_16b_bf16 v[148:151], v[186:187], v[58:59], v[212:215]\n"
    "v_cvt_pk_bf16_f32 v22, v156, v157\n"
    "v_cvt_pk_bf16_f32 v23, v158, v159\n"
    "v_mfma_f32_4x4x4_16b_bf16 v[152:155], v[188:189], v[60:61], v[216:219]\n"
    "v_cvt_pk_bf16_f32 v24, v144, v145\n"
    "v_cvt_pk_bf16_f32 v25, v146, v147\n"
    "v_mfma_f32_4x4x4_16b_bf16 v[156:159], v[190:191], v[62:63], v[220:223]\n"
    "v_cvt_pk_bf16_f32 v26, v148, v149\n"
    "v_cvt_pk_bf16_f32 v27, v150, v151\n"
    "v_cvt_pk_bf16_f32 v28, v152, v153\n"
    "v_cvt_pk_bf16_f32 v29, v154, v155\n"
    "v_exp_f32 v136, v136 clamp\n"
    "v_cvt_pk_bf16_f32 v30, v156, v157\n"
    "v_cvt_pk_bf16_f32 v31, v158, v159\n"
    "v_mfma_f32_16x16x32_bf16 v[144:147], v[0:3], v[160:163], v[112:115]\n"
    "v_exp_f32 v128, v128 clamp\n"
    "v_mfma_f32_16x16x32_bf16 v[144:147], v[4:7], v[164:167], v[144:147]\n"
    "v_exp_f32 v129, v129 clamp\n"
    "v_mfma_f32_16x16x32_bf16 v[148:151], v[8:11], v[160:163], v[116:119]\n"
    "v_exp_f32 v130, v130 clamp\n"
    "v_mfma_f32_16x16x32_bf16 v[148:151], v[12:15], v[164:167], v[148:151]\n"
    "v_exp_f32 v131, v131 clamp\n"
    "v_mfma_f32_16x16x32_bf16 v[152:155], v[16:19], v[160:163], v[120:123]\n"
    "v_exp_f32 v132, v132 clamp\n"
    "v_mfma_f32_16x16x32_bf16 v[152:155], v[20:23], v[164:167], v[152:155]\n"
    "v_exp_f32 v133, v133 clamp\n"
    "v_mfma_f32_16x16x32_bf16 v[156:159], v[24:27], v[160:163], v[124:127]\n"
    "v_exp_f32 v134, v134 clamp\n"
    "v_mfma_f32_16x16x32_bf16 v[156:159], v[28:31], v[164:167], v[156:159]\n"
    "v_exp_f32 v135, v135 clamp\n"
    "s_add_u32 m0, s68, 2048\n"
    "v_exp_f32 v137, v137 clamp\n"
    "v_add_u32 v253, s67, v244\n"
    "v_mfma_f32_4x4x4_16b_bf16 v[224:227], v[228:229], v[176:177], v[224:227] cbsz:4 abid:0\n"
    "v_exp_f32 v138, v138 clamp\n"
    "v_exp_f32 v139, v139 clamp\n"
    "v_mfma_f32_4x4x4_16b_bf16 v[224:227], v[228:229], v[178:179], v[224:227] cbsz:4 abid:1\n"
    "v_exp_f32 v140, v140 clamp\n"
    "v_exp_f32 v141, v141 clamp\n"
    "v_mfma_f32_4x4x4_16b_bf16 v[224:227], v[228:229], v[180:181], v[224:227] cbsz:4 abid:2\n"
    "v_exp_f32 v142, v142 clamp\n"
    "v_exp_f32 v143, v143 clamp\n"
    "v_mfma_f32_4x4x4_16b_bf16 v[224:227], v[228:229], v[182:183], v[224:227] cbsz:4 abid:3\n"
    "s_waitcnt vmcnt(4)\n"
    "ds_read_b64_tr_b16 v[176:177], v240 offset:2048\n"
    "ds_read_b64_tr_b16 v[178:179], v241 offset:2304\n"
    "ds_read_b64_tr_b16 v[180:181], v242 offset:2560\n"
    "ds_read_b64_tr_b16 v[182:183], v243 offset:2816\n"
    "ds_read_b32 v236, v253 offset:0\n"
    "ds_read_b32 v238, v253 offset:4\n"
    "ds_read_b128 v[160:163], v234 offset:4096\n"
    "ds_read_b128 v[164:167], v235 offset:4096\n"
    "s_waitcnt lgkmcnt(8)\n"
    "s_nop 0\n"
    "v_readlane_b32 s20, v252, 0\n"
    "v_readlane_b32 s21, v252, 1\n"
    "v_readlane_b32 s22, v252, 2\n"
    "v_readlane_b32 s23, v252, 3\n"
    "v_readlane_b32 s24, v252, 4\n"
    "v_readlane_b32 s25, v252, 5\n"
    "v_readlane_b32 s26, v252, 6\n"
    "v_readlane_b32 s27, v252, 7\n"
    "v_pk_fma_f32 v[128:129], v[128:129], s[70:71], v[96:97]\n"
    "v_pk_fma_f32 v[130:131], v[130:131], s[70:71], v[98:99]\n"
    "v_pk_add_f32 v[230:231], v[128:129], v[130:131]\n"
    "v_pk_mul_f32 v[232:233], v[128:129], v[130:131]\n"
    "v_pk_fma_f32 v[132:133], v[132:133], s[70:71], v[100:101]\n"
    "v_pk_fma_f32 v[230:231], v[230:231], v[132:133], v[232:233]\n"
    "v_pk_mul_f32 v[232:233], v[232:233], v[132:133]\n"
    "v_pk_fma_f32 v[134:135], v[134:135], s[70:71], v[102:103]\n"
    "v_pk_fma_f32 v[230:231], v[230:231], v[134:135], v[232:233]\n"
    "v_pk_mul_f32 v[232:233], v[232:233], v[134:135]\n"
    "v_pk_fma_f32 v[136:137], v[136:137], s[70:71], v[104:105]\n"
    "v_pk_fma_f32 v[230:231], v[230:231], v[136:137], v[232:233]\n"
    "v_pk_mul_f32 v[232:233], v[232:233], v[136:137]\n"
    "v_pk_fma_f32 v[138:139], v[138:139], s[70:71], v[106:107]\n"
    "v_pk_fma_f32 v[230:231], v[230:231], v[138:139], v[232:233]\n"
    "v_pk_mul_f32 v[232:233], v[232:233], v[138:139]\n"
    "v_pk_fma_f32 v[140:141], v[140:141], s[70:71], v[108:109]\n"
    "v_pk_fma_f32 v[230:231], v[230:231], v[140:141], v[232:233]\n"
    "v_pk_mul_f32 v[232:233], v[232:233], v[140:141]\n"
    "v_pk_fma_f32 v[142:143], v[142:143], s[70:71], v[110:111]\n"
    "v_pk_fma_f32 v[230:231], v[230:231], v[142:143], v[232:233]\n"
    "v_pk_mul_f32 v[232:233], v[232:233], v[142:143]\n"
    "v_rcp_f32 v232, v232\n"
    "v_rcp_f32 v233, v233\n"
    "v_fma_f32 v230, v230, v232, v247\n"
    "v_fmac_f32 v230, v231, v233\n"
    "v_lshlrev_b32 v232, 2, v249\n"
    "v_lshrrev_b32 v233, 2, v249\n"
    "v_and_b32 v232, 56, v232\n"
    "v_and_b32 v233, 4, v233\n"
    "v_add3_u32 v232, v232, v233, s81\n"
    "ds_read_b32 v232, v232\n"
    "s_add_u32 s81, s81, 64\n"
    "s_waitcnt lgkmcnt(0)\n"
    "v_lshlrev_b32 v232, 1, v232\n"
    "buffer_load_ushort v232, v232, s[44:47], 0 offen\n"
    "s_waitcnt vmcnt(0)\n"
    "v_lshlrev_b32 v232, 16, v232\n"
    "v_mul_f32 v230, v230, v232\n"
    "v_cvt_pk_bf16_f32 v230, v230, v230\n"
    "s_add_u32 s67, s67, 64\n"
    "s_waitcnt lgkmcnt(0)\n"
    "ds_write_b16 v245, v230\n"
    "ds_read_b64 v[228:229], v246\n"
    "s_lshl1_add_u32 s76, s72, s76\n"
    "buffer_load_dwordx4 v[236:237], s[40:43], 0 idxen offen offset:0 nt lds\n"
    "buffer_load_dwordx4 v[238:239], s[40:43], 0 idxen offen offset:1024 nt lds\n"
    "s_and_b32 s62, s58, 63\n"
    "s_mul_i32 s62, s62, 272\n"
    "s_mov_b32 s58, s59\n"
    "s_mov_b32 s59, s60\n"
    "s_mov_b32 s81, s65\n"
    "s_xor_b32 s65, s65, s64\n"
    "s_mov_b32 s80, s83\n"
    "s_add_u32 s82, s65, 256\n"
    "s_cmp_lt_i32 s59, s57\n"
    "s_cselect_b32 s76, s76, -1\n"
    "s_bitcmp1_b32 s58, 30\n"
    "s_cbranch_scc0 .Ldin_A1\n"
    ".Ldin_A1s:\n"
    "v_mfma_f32_16x16x32_bf16 v[128:131], v[0:3], v[160:163], v[112:115]\n"
    "v_exp_f32 v144, v144 clamp\n"
    "v_mfma_f32_16x16x32_bf16 v[128:131], v[4:7], v[164:167], v[128:131]\n"
    "v_exp_f32 v145, v145 clamp\n"
    "v_mfma_f32_16x16x32_bf16 v[132:135], v[8:11], v[160:163], v[116:119]\n"
    "v_exp_f32 v146, v146 clamp\n"
    "v_mfma_f32_16x16x32_bf16 v[132:135], v[12:15], v[164:167], v[132:135]\n"
    "v_exp_f32 v147, v147 clamp\n"
    "v_mfma_f32_16x16x32_bf16 v[136:139], v[16:19], v[160:163], v[120:123]\n"
    "v_exp_f32 v148, v148 clamp\n"
    "v_mfma_f32_16x16x32_bf16 v[136:139], v[20:23], v[164:167], v[136:139]\n"
    "v_exp_f32 v149, v149 clamp\n"
    "v_mfma_f32_16x16x32_bf16 v[140:143], v[24:27], v[160:163], v[124:127]\n"
    "v_exp_f32 v150, v150 clamp\n"
    "v_mfma_f32_16x16x32_bf16 v[140:143], v[28:31], v[164:167], v[140:143]\n"
    "v_exp_f32 v151, v151 clamp\n"
    "v_exp_f32 v152, v152 clamp\n"
    "s_add_u32 m0, s68, 4096\n"
    "v_exp_f32 v153, v153 clamp\n"
    "v_add_u32_e64 v253, s67, v244\n"
    "s_waitcnt lgkmcnt(0)\n"
    "v_mfma_f32_4x4x4_16b_bf16 v[224:227], v[228:229], v[168:169], v[224:227] cbsz:4 abid:0\n"
    "v_exp_f32 v154, v154 clamp\n"
    "v_exp_f32 v155, v155 clamp\n"
    "v_mfma_f32_4x4x4_16b_bf16 v[224:227], v[228:229], v[170:171], v[224:227] cbsz:4 abid:1\n"
    "v_exp_f32 v156, v156 clamp\n"
    "v_exp_f32 v157, v157 clamp\n"
    "v_mfma_f32_4x4x4_16b_bf16 v[224:227], v[228:229], v[172:173], v[224:227] cbsz:4 abid:2\n"
    "v_exp_f32 v158, v158 clamp\n"
    "v_exp_f32 v159, v159 clamp\n"
    "v_mfma_f32_4x4x4_16b_bf16 v[224:227], v[228:229], v[174:175], v[224:227] cbsz:4 abid:3\n"
    "s_waitcnt vmcnt(4)\n"
    "ds_read_b64_tr_b16 v[168:169], v240 offset:4096\n"
    "ds_read_b64_tr_b16 v[170:171], v241 offset:4352\n"
    "ds_read_b64_tr_b16 v[172:173], v242 offset:4608\n"
    "ds_read_b64_tr_b16 v[174:175], v243 offset:4864\n"
    "ds_read_b32 v236, v253 offset:0\n"
    "ds_read_b32 v238, v253 offset:4\n"
    "ds_read_b128 v[160:163], v234 offset:6144\n"
    "ds_read_b128 v[164:167], v235 offset:6144\n"
    "s_cbranch_vccz .Ldin_A1s_np\n"
    "s_setprio 1\n"
    ".Ldin_A1s_np:\n"
    "v_pk_fma_f32 v[144:145], v[144:145], s[70:71], v[96:97]\n"
    "v_pk_fma_f32 v[146:147], v[146:147], s[70:71], v[98:99]\n"
    "v_add_f32 v191, v224, v225\n"
    "v_add_f32 v190, v226, v227\n"
    "v_add_f32 v191, v191, v190\n"
    "v_add_u32 v190, s62, v249\n"
    "v_mul_f32 v191, s69, v191\n"
    "v_cvt_pk_bf16_f32 v191, v191, v191\n"
    "ds_write_b16 v190, v191 offset:32768\n"
    "s_bfe_u32 s94, s59, 0x180006\n"
    "s_mul_i32 s86, s94, s56\n"
    "v_mbcnt_lo_u32_b32 v184, s20, 0\n"
    "v_mbcnt_lo_u32_b32 v185, s22, 0\n"
    "v_mbcnt_lo_u32_b32 v186, s24, 0\n"
    "v_mbcnt_lo_u32_b32 v187, s26, 0\n"
    "s_bcnt1_i32_b64 s90, s[20:21]\n"
    "s_bcnt1_i32_b64 s91, s[22:23]\n"
    "s_bcnt1_i32_b64 s92, s[24:25]\n"
    "s_bcnt1_i32_b64 s93, s[26:27]\n"
    "v_mbcnt_hi_u32_b32 v184, s21, v184\n"
    "v_mbcnt_hi_u32_b32 v185, s23, v185\n"
    "v_mbcnt_hi_u32_b32 v186, s25, v186\n"
    "v_mbcnt_hi_u32_b32 v187, s27, v187\n"
    "s_lshl2_add_u32 s90, s90, s65\n"
    "s_lshl2_add_u32 s91, s91, s90\n"
    "s_lshl2_add_u32 s92, s92, s91\n"
    "s_lshl2_add_u32 s93, s93, s92\n"
    "v_add_u32_e64 v188, s86, v251\n"
    "v_pk_add_f32 v[230:231], v[144:145], v[146:147]\n"
    "v_pk_mul_f32 v[232:233], v[144:145], v[146:147]\n"
    "v_pk_fma_f32 v[148:149], v[148:149], s[70:71], v[100:101]\n"
    "v_pk_fma_f32 v[230:231], v[230:231], v[148:149], v[232:233]\n"
    "v_pk_mul_f32 v[232:233], v[232:233], v[148:149]\n"
    "v_lshl_add_u32 v184, v184, 2, s65\n"
    "v_lshl_add_u32 v185, v185, 2, s90\n"
    "v_lshl_add_u32 v186, v186, 2, s91\n"
    "v_lshl_add_u32 v187, v187, 2, s92\n"
    "v_mov_b32 v191, -1\n"
    "v_lshl_add_u32 v190, v249, 1, s93\n"
    "ds_write2_b32 v190, v191, v191 offset1:64\n"
    "s_mov_b64 exec, s[20:21]\n"
    "ds_write_b32 v184, v188\n"
    "s_mov_b64 exec, s[22:23]\n"
    "v_add_u32 v189, 64, v188\n"
    "ds_write_b32 v185, v189\n"
    "s_mov_b64 exec, s[24:25]\n"
    "v_add_u32 v190, 128, v188\n"
    "ds_write_b32 v186, v190\n"
    "s_mov_b64 exec, s[26:27]\n"
    "v_add_u32 v189, 192, v188\n"
    "ds_write_b32 v187, v189\n"
    "s_mov_b64 exec, -1\n"
    "s_add_u32 s95, s93, 60\n"
    "s_add_u32 s94, s82, 128\n"
    "s_andn2_b32 s95, s95, 63\n"
    "s_max_u32 s83, s95, s94\n"
    "v_pk_fma_f32 v[150:151], v[150:151], s[70:71], v[102:103]\n"
    "v_pk_fma_f32 v[230:231], v[230:231], v[150:151], v[232:233]\n"
    "v_pk_mul_f32 v[232:233], v[232:233], v[150:151]\n"
    "v_pk_fma_f32 v[152:153], v[152:153], s[70:71], v[104:105]\n"
    "v_pk_fma_f32 v[230:231], v[230:231], v[152:153], v[232:233]\n"
    "v_pk_mul_f32 v[232:233], v[232:233], v[152:153]\n"
    "v_pk_fma_f32 v[154:155], v[154:155], s[70:71], v[106:107]\n"
    "v_pk_fma_f32 v[230:231], v[230:231], v[154:155], v[232:233]\n"
    "v_pk_mul_f32 v[232:233], v[232:233], v[154:155]\n"
    "v_pk_fma_f32 v[156:157], v[156:157], s[70:71], v[108:109]\n"
    "v_pk_fma_f32 v[230:231], v[230:231], v[156:157], v[232:233]\n"
    "v_pk_mul_f32 v[232:233], v[232:233], v[156:157]\n"
    "v_pk_fma_f32 v[158:159], v[158:159], s[70:71], v[110:111]\n"
    "v_pk_fma_f32 v[230:231], v[230:231], v[158:159], v[232:233]\n"
    "v_pk_mul_f32 v[232:233], v[232:233], v[158:159]\n"
    "v_rcp_f32 v232, v232\n"
    "v_rcp_f32 v233, v233\n"
    "v_fma_f32 v230, v230, v232, v247\n"
    "v_fmac_f32 v230, v231, v233\n"
    "v_lshlrev_b32 v232, 2, v249\n"
    "v_lshrrev_b32 v233, 2, v249\n"
    "v_and_b32 v232, 56, v232\n"
    "v_and_b32 v233, 4, v233\n"
    "v_add3_u32 v232, v232, v233, s81\n"
    "ds_read_b32 v232, v232\n"
    "s_add_u32 s81, s81, 64\n"
    "s_waitcnt lgkmcnt(0)\n"
    "v_lshlrev_b32 v232, 1, v232\n"
    "buffer_load_ushort v232, v232, s[44:47], 0 offen\n"
    "s_waitcnt vmcnt(0)\n"
    "v_lshlrev_b32 v232, 16, v232\n"
    "v_mul_f32 v230, v230, v232\n"
    "v_cvt_pk_bf16_f32 v230, v230, v230\n"
    "s_add_u32 s67, s67, 64\n"
    "s_waitcnt lgkmcnt(0)\n"
    "ds_write_b16 v245, v230\n"
    "ds_read_b64 v[228:229], v246\n"
    "buffer_load_dwordx4 v[236:237], s[40:43], 0 idxen offen offset:0 nt lds\n"
    "buffer_load_dwordx4 v[238:239], s[40:43], 0 idxen offen offset:1024 nt lds\n"
    ".Ldin_B2s:\n"
    "v_mfma_f32_16x16x32_bf16 v[144:147], v[0:3], v[160:163], v[112:115]\n"
    "v_exp_f32 v128, v128 clamp\n"
    "v_mfma_f32_16x16x32_bf16 v[144:147], v[4:7], v[164:167], v[144:147]\n"
    "v_exp_f32 v129, v129 clamp\n"
    "v_mfma_f32_16x16x32_bf16 v[148:151], v[8:11], v[160:163], v[116:119]\n"
    "v_exp_f32 v130, v130 clamp\n"
    "v_mfma_f32_16x16x32_bf16 v[148:151], v[12:15], v[164:167], v[148:151]\n"
    "v_exp_f32 v131, v131 clamp\n"
    "v_mfma_f32_16x16x32_bf16 v[152:155], v[16:19], v[160:163], v[120:123]\n"
    "v_exp_f32 v132, v132 clamp\n"
    "v_mfma_f32_16x16x32_bf16 v[152:155], v[20:23], v[164:167], v[152:155]\n"
    "v_exp_f32 v133, v133 clamp\n"
    "v_mfma_f32_16x16x32_bf16 v[156:159], v[24:27], v[160:163], v[124:127]\n"
    "v_exp_f32 v134, v134 clamp\n"
    "v_mfma_f32_16x16x32_bf16 v[156:159], v[28:31], v[164:167], v[156:159]\n"
    "v_exp_f32 v135, v135 clamp\n"
    "v_exp_f32 v136, v136 clamp\n"
    "s_cmp_eq_u32 s67, s80\n"
    "s_cselect_b32 s67, s65, s67\n"
    "s_and_b32 s94, s59, 63\n"
    "s_mul_i32 s94, s94, 272\n"
    "v_add_u32 v252, s94, v250\n"
    "s_add_u32 m0, s68, 6144\n"
    "v_exp_f32 v137, v137 clamp\n"
    "v_add_u32 v253, s67, v244\n"
    "s_waitcnt lgkmcnt(0)\n"
    "v_mfma_f32_4x4x4_16b_bf16 v[224:227], v[228:229], v[176:177], 0 cbsz:4 abid:0\n"
    "v_exp_f32 v138, v138 clamp\n"
    "v_exp_f32 v139, v139 clamp\n"
    "v_mfma_f32_4x4x4_16b_bf16 v[224:227], v[228:229], v[178:179], v[224:227] cbsz:4 abid:1\n"
    "v_exp_f32 v140, v140 clamp\n"
    "v_exp_f32 v141, v141 clamp\n"
    "v_mfma_f32_4x4x4_16b_bf16 v[224:227], v[228:229], v[180:181], v[224:227] cbsz:4 abid:2\n"
    "v_exp_f32 v142, v142 clamp\n"
    "v_exp_f32 v143, v143 clamp\n"
    "v_mfma_f32_4x4x4_16b_bf16 v[224:227], v[228:229], v[182:183], v[224:227] cbsz:4 abid:3\n"
    "s_waitcnt vmcnt(4)\n"
    "s_memrealtime s[34:35]\n"
    "ds_read_b64_tr_b16 v[176:177], v240 offset:6144\n"
    "ds_read_b64_tr_b16 v[178:179], v241 offset:6400\n"
    "ds_read_b64_tr_b16 v[180:181], v242 offset:6656\n"
    "ds_read_b64_tr_b16 v[182:183], v243 offset:6912\n"
    "ds_read_b32 v236, v253 offset:0\n"
    "ds_read_b32 v238, v253 offset:4\n"
    "ds_read_b128 v[160:163], v234 offset:0\n"
    "ds_read_b128 v[164:167], v235 offset:0\n"
    "ds_read_b128 v[184:187], v252 offset:0\n"
    "ds_read_b128 v[188:191], v252 offset:16\n"
    "v_pk_fma_f32 v[128:129], v[128:129], s[70:71], v[96:97]\n"
    "v_pk_fma_f32 v[130:131], v[130:131], s[70:71], v[98:99]\n"
    "v_pk_add_f32 v[230:231], v[128:129], v[130:131]\n"
    "v_pk_mul_f32 v[232:233], v[128:129], v[130:131]\n"
    "v_pk_fma_f32 v[132:133], v[132:133], s[70:71], v[100:101]\n"
    "v_pk_fma_f32 v[230:231], v[230:231], v[132:133], v[232:233]\n"
    "v_pk_mul_f32 v[232:233], v[232:233], v[132:133]\n"
    "v_pk_fma_f32 v[134:135], v[134:135], s[70:71], v[102:103]\n"
    "v_pk_fma_f32 v[230:231], v[230:231], v[134:135], v[232:233]\n"
    "v_pk_mul_f32 v[232:233], v[232:233], v[134:135]\n"
    "v_pk_fma_f32 v[136:137], v[136:137], s[70:71], v[104:105]\n"
    "v_pk_fma_f32 v[230:231], v[230:231], v[136:137], v[232:233]\n"
    "v_pk_mul_f32 v[232:233], v[232:233], v[136:137]\n"
    "v_pk_fma_f32 v[138:139], v[138:139], s[70:71], v[106:107]\n"
    "v_pk_fma_f32 v[230:231], v[230:231], v[138:139], v[232:233]\n"
    "v_pk_mul_f32 v[232:233], v[232:233], v[138:139]\n"
    "v_pk_fma_f32 v[140:141], v[140:141], s[70:71], v[108:109]\n"
    "v_pk_fma_f32 v[230:231], v[230:231], v[140:141], v[232:233]\n"
    "v_pk_mul_f32 v[232:233], v[232:233], v[140:141]\n"
    "v_pk_fma_f32 v[142:143], v[142:143], s[70:71], v[110:111]\n"
    "v_pk_fma_f32 v[230:231], v[230:231], v[142:143], v[232:233]\n"
    "v_pk_mul_f32 v[232:233], v[232:233], v[142:143]\n"
    "v_rcp_f32 v232, v232\n"
    "v_rcp_f32 v233, v233\n"
    "v_fma_f32 v230, v230, v232, v247\n"
    "v_fmac_f32 v230, v231, v233\n"
    "v_lshlrev_b32 v232, 2, v249\n"
    "v_lshrrev_b32 v233, 2, v249\n"
    "v_and_b32 v232, 56, v232\n"
    "v_and_b32 v233, 4, v233\n"
    "v_add3_u32 v232, v232, v233, s81\n"
    "ds_read_b32 v232, v232\n"
    "s_add_u32 s81, s81, 64\n"
    "s_waitcnt lgkmcnt(0)\n"
    "v_lshlrev_b32 v232, 1, v232\n"
    "buffer_load_ushort v232, v232, s[44:47], 0 offen\n"
    "s_waitcnt vmcnt(0)\n"
    "v_lshlrev_b32 v232, 16, v232\n"
    "v_mul_f32 v230, v230, v232\n"
    "v_cvt_pk_bf16_f32 v230, v230, v230\n"
    "s_add_u32 s67, s67, 64\n"
    "s_waitcnt lgkmcnt(0)\n"
    "ds_write_b16 v245, v230\n"
    "ds_read_b64 v[228:229], v246\n"
    "s_cmp_le_u32 s34, s76\n"
    "s_cbranch_scc1 .Ldin_B2s_wait\n"
    ".Ldin_B2s_go:\n"
    "s_sub_u32 s94, s34, 60\n"
    "s_max_u32 s76, s76, s94\n"
    "s_add_u32 s76, s76, s72\n"
    ".Ldin_B2s_dma:\n"
    "buffer_load_dwordx4 v[236:237], s[40:43], 0 idxen offen offset:0 nt lds\n"
    "buffer_load_dwordx4 v[238:239], s[40:43], 0 idxen offen offset:1024 nt lds\n"
    ".Ldin_B2s_nd:\n"
    "s_branch .Ldin_G3s\n"
    ".Ldin_L0s_poll:\n"
    "s_sleep 1\n"
    "ds_read_b64 v[232:233], v253\n"
    "s_waitcnt lgkmcnt(0)\n"
    "v_readfirstlane_b32 s60, v232\n"
    "s_cmp_eq_u32 s60, -1\n"
    "s_cbranch_scc1 .Ldin_L0s_poll\n"
    "s_branch .Ldin_L0s_pr\n"
    ".Ldin_B2s_wait:\n"
    "s_cmp_lt_i32 s59, s57\n"
    "s_cbranch_scc1 .Ldin_B2s_w2\n"
    "s_sub_u32 s94, s67, s65\n"
    "s_cmp_gt_u32 s94, 256\n"
    "s_cbranch_scc1 .Ldin_B2s_dma\n"
    "s_cmp_eq_u32 s94, 64\n"
    "s_waitcnt vmcnt(2)\n"
    "s_cbranch_scc1 .Ldin_B2s_nd\n"
    "s_waitcnt vmcnt(0)\n"
    "s_branch .Ldin_B2s_nd\n"
    ".Ldin_B2s_slp:\n"
    "s_sleep 1\n"
    ".Ldin_B2s_w2:\n"
    "s_memrealtime s[34:35]\n"
    "s_waitcnt lgkmcnt(0)\n"
    "s_sub_u32 s94, s34, s76\n"
    "s_cmp_lt_i32 s94, 0\n"
    "s_cbranch_scc1 .Ldin_B2s_slp\n"
    "s_branch .Ldin_B2s_go\n"
    ".p2align 5\n"
    ".Ldin_L3s:\n"
    "s_setprio 0\n"
    "s_cmp_ge_i32 s59, s57\n"
    "s_cbranch_scc1 .Ldin_E1s\n"
    "s_add_u32 s76, s76, s73\n"
    "v_mov_b32 v253, s63\n"
    "v_and_b32 v184, v254, v184\n"
    "v_and_b32 v185, v255, v185\n"
    "v_and_b32 v186, v254, v186\n"
    "v_and_b32 v187, v255, v187\n"
    "v_and_b32 v188, v254, v188\n"
    "v_and_b32 v189, v255, v189\n"
    "v_and_b32 v190, v254, v190\n"
    "v_and_b32_e64 v191, v255, v191\n"
    "ds_read_b64 v[232:233], v253\n"
    "v_mfma_f32_4x4x4_16b_bf16 v[128:131], v[184:185], v[32:33], v[64:67]\n"
    "ds_read_b128 v[112:115], v252 offset:17408\n"
    "v_mfma_f32_4x4x4_16b_bf16 v[132:135], v[186:187], v[34:35], v[68:71]\n"
    "ds_read_b128 v[116:119], v252 offset:17424\n"
    "v_mfma_f32_4x4x4_16b_bf16 v[136:139], v[188:189], v[36:37], v[72:75]\n"
    "ds_read_b128 v[120:123], v252 offset:17536\n"
    "ds_read_b128 v[124:127], v252 offset:17552\n"
    "v_cvt_pk_bf16_f32 v0, v128, v129\n"
    "v_cvt_pk_bf16_f32 v1, v130, v131\n"
    "v_mfma_f32_4x4x4_16b_bf16 v[140:143], v[190:191], v[38:39], v[76:79]\n"
    "v_cvt_pk_bf16_f32 v2, v132, v133\n"
    "v_cvt_pk_bf16_f32 v3, v134, v135\n"
    "v_mfma_f32_4x4x4_16b_bf16 v[128:131], v[184:185], v[40:41], v[80:83]\n"
    "v_cvt_pk_bf16_f32 v4, v136, v137\n"
    "v_cvt_pk_bf16_f32 v5, v138, v139\n"
    "v_mfma_f32_4x4x4_16b_bf16 v[132:135], v[186:187], v[42:43], v[84:87]\n"
    "v_cvt_pk_bf16_f32 v6, v140, v141\n"
    "v_cvt_pk_bf16_f32 v7, v142, v143\n"
    "v_mfma_f32_4x4x4_16b_bf16 v[136:139], v[188:189], v[44:45], v[88:91]\n"
    "v_cvt_pk_bf16_f32 v8, v128, v129\n"
    "v_cvt_pk_bf16_f32 v9, v130, v131\n"
    "v_mfma_f32_4x4x4_16b_bf16 v[140:143], v[190:191], v[46:47], v[92:95]\n"
    "v_cvt_pk_bf16_f32 v10, v132, v133\n"
    "v_cvt_pk_bf16_f32 v11, v134, v135\n"
    "v_mfma_f32_4x4x4_16b_bf16 v[128:131], v[184:185], v[48:49], v[192:195]\n"
    "v_cvt_pk_bf16_f32 v12, v136, v137\n"
    "v_cvt_pk_bf16_f32 v13, v138, v139\n"
    "v_mfma_f32_4x4x4_16b_bf16 v[132:135], v[186:187], v[50:51], v[196:199]\n"
    "v_cvt_pk_bf16_f32 v14, v140, v141\n"
    "v_cvt_pk_bf16_f32 v15, v142, v143\n"
    "v_mfma_f32_4x4x4_16b_bf16 v[136:139], v[188:189], v[52:53], v[200:203]\n"
    "s_waitcnt lgkmcnt(0)\n"
    "v_readfirstlane_b32 s60, v232\n"
    "v_cvt_pk_bf16_f32 v16, v128, v129\n"
    "v_cvt_pk_bf16_f32 v17, v130, v131\n"
    "v_mfma_f32_4x4x4_16b_bf16 v[140:143], v[190:191], v[54:55], v[204:207]\n"
    "s_cmp_eq_u32 s60, -1\n"
    "s_cbranch_scc1 .Ldin_L3s_poll\n"
    ".Ldin_L3s_pr:\n"
    "v_readfirstlane_b32 s95, v233\n"
    "s_and_b32 s94, s60, 63\n"
    "s_lshl_b32 s94, s94, 6\n"
    "s_add_u32 s94, s94, 68480\n"
    "s_add_u32 s63, s63, 8\n"
    "v_lshl_add_u32 v252, v249, 1, s94\n"
    "ds_read_b32 v252, v252\n"
    "s_sub_u32 s95, s95, s76\n"
    "s_ashr_i32 s95, s95, 4\n"
    "s_min_i32 s95, s95, s74\n"
    "s_max_i32 s72, s95, s75\n"
    "v_cvt_pk_bf16_f32 v18, v132, v133\n"
    "v_cvt_pk_bf16_f32 v19, v134, v135\n"
    "v_mfma_f32_4x4x4_16b_bf16 v[128:131], v[184:185], v[56:57], v[208:211]\n"
    "v_cvt_pk_bf16_f32 v20, v136, v137\n"
    "v_cvt_pk_bf16_f32 v21, v138, v139\n"
    "v_mfma_f32_4x4x4_16b_bf16 v[132:135], v[186:187], v[58:59], v[212:215]\n"
    "v_cvt_pk_bf16_f32 v22, v140, v141\n"
    "v_cvt_pk_bf16_f32 v23, v142, v143\n"
    "v_mfma_f32_4x4x4_16b_bf16 v[136:139], v[188:189], v[60:61], v[216:219]\n"
    "v_cvt_pk_bf16_f32 v24, v128, v129\n"
    "v_cvt_pk_bf16_f32 v25, v130, v131\n"
    "v_mfma_f32_4x4x4_16b_bf16 v[140:143], v[190:191], v[62:63], v[220:223]\n"
    "v_cvt_pk_bf16_f32 v26, v132, v133\n"
    "v_cvt_pk_bf16_f32 v27, v134, v135\n"
    "v_cvt_pk_bf16_f32 v28, v136, v137\n"
    "v_cvt_pk_bf16_f32 v29, v138, v139\n"
    "v_exp_f32 v152, v152 clamp\n"
    "v_cvt_pk_bf16_f32 v30, v140, v141\n"
    "v_cvt_pk_bf16_f32 v31, v142, v143\n"
    "v_mfma_f32_16x16x32_bf16 v[128:131], v[0:3], v[160:163], v[112:115]\n"
    "v_exp_f32 v144, v144 clamp\n"
    "v_mfma_f32_16x16x32_bf16 v[128:131], v[4:7], v[164:167], v[128:131]\n"
    "v_exp_f32 v145, v145 clamp\n"
    "v_mfma_f32_16x16x32_bf16 v[132:135], v[8:11], v[160:163], v[116:119]\n"
    "v_exp_f32 v146, v146 clamp\n"
    "v_mfma_f32_16x16x32_bf16 v[132:135], v[12:15], v[164:167], v[132:135]\n"
    "v_exp_f32 v147, v147 clamp\n"
    "v_mfma_f32_16x16x32_bf16 v[136:139], v[16:19], v[160:163], v[120:123]\n"
    "v_exp_f32 v148, v148 clamp\n"
    "v_mfma_f32_16x16x32_bf16 v[136:139], v[20:23], v[164:167], v[136:139]\n"
    "v_exp_f32 v149, v149 clamp\n"
    "v_mfma_f32_16x16x32_bf16 v[140:143], v[24:27], v[160:163], v[124:127]\n"
    "v_exp_f32 v150, v150 clamp\n"
    "v_mfma_f32_16x16x32_bf16 v[140:143], v[28:31], v[164:167], v[140:143]\n"
    "v_exp_f32 v151, v151 clamp\n"
    "s_add_u32 m0, s68, 0\n"
    "v_exp_f32 v153, v153 clamp\n"
    "v_add_u32_e64 v253, s67, v244\n"
    "v_mfma_f32_4x4x4_16b_bf16 v[224:227], v[228:229], v[168:169], v[224:227] cbsz:4 abid:0\n"
    "v_exp_f32 v154, v154 clamp\n"
    "v_exp_f32 v155, v155 clamp\n"
    "v_mfma_f32_4x4x4_16b_bf16 v[224:227], v[228:229], v[170:171], v[224:227] cbsz:4 abid:1\n"
    "v_exp_f32 v156, v156 clamp\n"
    "v_exp_f32 v157, v157 clamp\n"
    "v_mfma_f32_4x4x4_16b_bf16 v[224:227], v[228:229], v[172:173], v[224:227] cbsz:4 abid:2\n"
    "v_exp_f32 v158, v158 clamp\n"
    "v_exp_f32 v159, v159 clamp\n"
    "v_mfma_f32_4x4x4_16b_bf16 v[224:227], v[228:229], v[174:175], v[224:227] cbsz:4 abid:3\n"
    "s_waitcnt vmcnt(4)\n"
    "ds_read_b64_tr_b16 v[168:169], v240 offset:0\n"
    "ds_read_b64_tr_b16 v[170:171], v241 offset:256\n"
    "ds_read_b64_tr_b16 v[172:173], v242 offset:512\n"
    "ds_read_b64_tr_b16 v[174:175], v243 offset:768\n"
    "ds_read_b32 v236, v253 offset:0\n"
    "ds_read_b32 v238, v253 offset:4\n"
    "ds_read_b128 v[160:163], v234 offset:2048\n"
    "ds_read_b128 v[164:167], v235 offset:2048\n"
    "s_waitcnt lgkmcnt(8)\n"
    "s_nop 0\n"
    "v_readlane_b32 s20, v252, 0\n"
    "v_readlane_b32 s21, v252, 1\n"
    "v_readlane_b32 s22, v252, 2\n"
    "v_readlane_b32 s23, v252, 3\n"
    "v_readlane_b32 s24, v252, 4\n"
    "v_readlane_b32 s25, v252, 5\n"
    "v_readlane_b32 s26, v252, 6\n"
    "v_readlane_b32 s27, v252, 7\n"
    "v_pk_fma_f32 v[144:145], v[144:145], s[70:71], v[96:97]\n"
    "v_pk_fma_f32 v[146:147], v[146:147], s[70:71], v[98:99]\n"
    "v_pk_add_f32 v[230:231], v[144:145], v[146:147]\n"
    "v_pk_mul_f32 v[232:233], v[144:145], v[146:147]\n"
    "v_pk_fma_f32 v[148:149], v[148:149], s[70:71], v[100:101]\n"
    "v_pk_fma_f32 v[230:231], v[230:231], v[148:149], v[232:233]\n"
    "v_pk_mul_f32 v[232:233], v[232:233], v[148:149]\n"
    "v_pk_fma_f32 v[150:151], v[150:151], s[70:71], v[102:103]\n"
    "v_pk_fma_f32 v[230:231], v[230:231], v[150:151], v[232:233]\n"
    "v_pk_mul_f32 v[232:233], v[232:233], v[150:151]\n"
    "v_pk_fma_f32 v[152:153], v[152:153], s[70:71], v[104:105]\n"
    "v_pk_fma_f32 v[230:231], v[230:231], v[152:153], v[232:233]\n"
    "v_pk_mul_f32 v[232:233], v[232:233], v[152:153]\n"
    "v_pk_fma_f32 v[154:155], v[154:155], s[70:71], v[106:107]\n"
    "v_pk_fma_f32 v[230:231], v[230:231], v[154:155], v[232:233]\n"
    "v_pk_mul_f32 v[232:233], v[232:233], v[154:155]\n"
    "v_pk_fma_f32 v[156:157], v[156:157], s[70:71], v[108:109]\n"
    "v_pk_fma_f32 v[230:231], v[230:231], v[156:157], v[232:233]\n"
    "v_pk_mul_f32 v[232:233], v[232:233], v[156:157]\n"
    "v_pk_fma_f32 v[158:159], v[158:159], s[70:71], v[110:111]\n"
    "v_pk_fma_f32 v[230:231], v[230:231], v[158:159], v[232:233]\n"
    "v_pk_mul_f32 v[232:233], v[232:233], v[158:159]\n"
    "v_rcp_f32 v232, v232\n"
    "v_rcp_f32 v233, v233\n"
    "v_fma_f32 v230, v230, v232, v247\n"
    "v_fmac_f32 v230, v231, v233\n"
    "v_lshlrev_b32 v232, 2, v249\n"
    "v_lshrrev_b32 v233, 2, v249\n"
    "v_and_b32 v232, 56, v232\n"
    "v_and_b32 v233, 4, v233\n"
    "v_add3_u32 v232, v232, v233, s81\n"
    "ds_read_b32 v232, v232\n"
    "s_add_u32 s81, s81, 64\n"
    "s_waitcnt lgkmcnt(0)\n"
    "v_lshlrev_b32 v232, 1, v232\n"
    "buffer_load_ushort v232, v232, s[44:47], 0 offen\n"
    "s_waitcnt vmcnt(0)\n"
    "v_lshlrev_b32 v232, 16, v232\n"
    "v_mul_f32 v230, v230, v232\n"
    "v_cvt_pk_bf16_f32 v230, v230, v230\n"
    "s_sub_u32 s67, s67, -64\n"
    "s_waitcnt lgkmcnt(0)\n"
    "ds_write_b16 v245, v230\n"
    "ds_read_b64 v[228:229], v246\n"
    "s_lshl1_add_u32 s76, s72, s76\n"
    "buffer_load_dwordx4 v[236:237], s[40:43], 0 idxen offen offset:0 nt lds\n"
    "buffer_load_dwordx4 v[238:239], s[40:43], 0 idxen offen offset:1024 nt lds\n"
    "s_and_b32 s62, s58, 63\n"
    "s_mul_i32 s62, s62, 272\n"
    "s_mov_b32 s58, s59\n"
    "s_mov_b32 s59, s60\n"
    "s_mov_b32 s81, s65\n"
    "s_xor_b32 s65, s65, s64\n"
    "s_mov_b32 s80, s83\n"
    "s_add_u32 s82, s65, 256\n"
    "s_cmp_lt_i32 s59, s57\n"
    "s_cselect_b32 s76, s76, -1\n"
    "s_bitcmp1_b32 s58, 30\n"
    "s_cbranch_scc0 .Ldin_A0\n"
    "s_branch .Ldin_A0s\n"
    ".Ldin_L3s_poll:\n"
    "s_sleep 1\n"
    "ds_read_b64 v[232:233], v253\n"
    "s_waitcnt lgkmcnt(0)\n"
    "v_readfirstlane_b32 s60, v232\n"
    "s_cmp_eq_u32 s60, -1\n"
    "s_cbranch_scc1 .Ldin_L3s_poll\n"
    "s_branch .Ldin_L3s_pr\n"
    ".Ldin_E0s:\n"
    "v_exp_f32 v128, v128 clamp\n"
    "v_exp_f32 v129, v129 clamp\n"
    "v_exp_f32 v130, v130 clamp\n"
    "v_exp_f32 v131, v131 clamp\n"
    "v_exp_f32 v132, v132 clamp\n"
    "v_exp_f32 v133, v133 clamp\n"
    "v_exp_f32 v134, v134 clamp\n"
    "v_exp_f32 v135, v135 clamp\n"
    "s_and_b32 s62, s58, 63\n"
    "s_mul_i32 s62, s62, 272\n"
    "s_waitcnt lgkmcnt(0)\n"
    "v_mfma_f32_4x4x4_16b_bf16 v[224:227], v[228:229], v[176:177], v[224:227] cbsz:4 abid:0\n"
    "v_exp_f32 v136, v136 clamp\n"
    "v_exp_f32 v137, v137 clamp\n"
    "v_mfma_f32_4x4x4_16b_bf16 v[224:227], v[228:229], v[178:179], v[224:227] cbsz:4 abid:1\n"
    "v_exp_f32 v138, v138 clamp\n"
    "v_exp_f32 v139, v139 clamp\n"
    "v_mfma_f32_4x4x4_16b_bf16 v[224:227], v[228:229], v[180:181], v[224:227] cbsz:4 abid:2\n"
    "v_exp_f32 v140, v140 clamp\n"
    "v_exp_f32 v141, v141 clamp\n"
    "v_mfma_f32_4x4x4_16b_bf16 v[224:227], v[228:229], v[182:183], v[224:227] cbsz:4 abid:3\n"
    "v_exp_f32 v142, v142 clamp\n"
    "v_exp_f32 v143, v143 clamp\n"
    "v_pk_fma_f32 v[128:129], v[128:129], s[70:71], v[96:97]\n"
    "v_pk_fma_f32 v[130:131], v[130:131], s[70:71], v[98:99]\n"
    "v_pk_add_f32 v[230:231], v[128:129], v[130:131]\n"
    "v_pk_mul_f32 v[232:233], v[128:129], v[130:131]\n"
    "v_pk_fma_f32 v[132:133], v[132:133], s[70:71], v[100:101]\n"
    "v_pk_fma_f32 v[230:231], v[230:231], v[132:133], v[232:233]\n"
    "v_pk_mul_f32 v[232:233], v[232:233], v[132:133]\n"
    "v_pk_fma_f32 v[134:135], v[134:135], s[70:71], v[102:103]\n"
    "v_pk_fma_f32 v[230:231], v[230:231], v[134:135], v[232:233]\n"
    "v_pk_mul_f32 v[232:233], v[232:233], v[134:135]\n"
    "v_pk_fma_f32 v[136:137], v[136:137], s[70:71], v[104:105]\n"
    "v_pk_fma_f32 v[230:231], v[230:231], v[136:137], v[232:233]\n"
    "v_pk_mul_f32 v[232:233], v[232:233], v[136:137]\n"
    "v_pk_fma_f32 v[138:139], v[138:139], s[70:71], v[106:107]\n"
    "v_pk_fma_f32 v[230:231], v[230:231], v[138:139], v[232:233]\n"
    "v_pk_mul_f32 v[232:233], v[232:233], v[138:139]\n"
    "v_pk_fma_f32 v[140:141], v[140:141], s[70:71], v[108:109]\n"
    "v_pk_fma_f32 v[230:231], v[230:231], v[140:141], v[232:233]\n"
    "v_pk_mul_f32 v[232:233], v[232:233], v[140:141]\n"
    "v_pk_fma_f32 v[142:143], v[142:143], s[70:71], v[110:111]\n"
    "v_pk_fma_f32 v[230:231], v[230:231], v[142:143], v[232:233]\n"
    "v_pk_mul_f32 v[232:233], v[232:233], v[142:143]\n"
    "v_rcp_f32 v232, v232\n"
    "v_rcp_f32 v233, v233\n"
    "v_fma_f32 v230, v230, v232, v247\n"
    "v_fmac_f32 v230, v231, v233\n"
    "v_lshlrev_b32 v232, 2, v249\n"
    "v_lshrrev_b32 v233, 2, v249\n"
    "v_and_b32 v232, 56, v232\n"
    "v_and_b32 v233, 4, v233\n"
    "v_add3_u32 v232, v232, v233, s81\n"
    "ds_read_b32 v232, v232\n"
    "s_add_u32 s81, s81, 64\n"
    "s_waitcnt lgkmcnt(0)\n"
    "v_lshlrev_b32 v232, 1, v232\n"
    "buffer_load_ushort v232, v232, s[44:47], 0 offen\n"
    "s_waitcnt vmcnt(0)\n"
    "v_lshlrev_b32 v232, 16, v232\n"
    "v_mul_f32 v230, v230, v232\n"
    "v_cvt_pk_bf16_f32 v230, v230, v230\n"
    "ds_write_b16 v245, v230\n"
    "ds_read_b64 v[228:229], v246\n"
    "s_branch .Ldin_drain0\n"
    ".Ldin_E1s:\n"
    "v_exp_f32 v144, v144 clamp\n"
    "v_exp_f32 v145, v145 clamp\n"
    "v_exp_f32 v146, v146 clamp\n"
    "v_exp_f32 v147, v147 clamp\n"
    "v_exp_f32 v148, v148 clamp\n"
    "v_exp_f32 v149, v149 clamp\n"
    "v_exp_f32 v150, v150 clamp\n"
    "v_exp_f32 v151, v151 clamp\n"
    "s_and_b32 s62, s58, 63\n"
    "s_mul_i32 s62, s62, 272\n"
    "s_waitcnt lgkmcnt(0)\n"
    "v_mfma_f32_4x4x4_16b_bf16 v[224:227], v[228:229], v[168:169], v[224:227] cbsz:4 abid:0\n"
    "v_exp_f32 v152, v152 clamp\n"
    "v_exp_f32 v153, v153 clamp\n"
    "v_mfma_f32_4x4x4_16b_bf16 v[224:227], v[228:229], v[170:171], v[224:227] cbsz:4 abid:1\n"
    "v_exp_f32 v154, v154 clamp\n"
    "v_exp_f32 v155, v155 clamp\n"
    "v_mfma_f32_4x4x4_16b_bf16 v[224:227], v[228:229], v[172:173], v[224:227] cbsz:4 abid:2\n"
    "v_exp_f32 v156, v156 clamp\n"
    "v_exp_f32 v157, v157 clamp\n"
    "v_mfma_f32_4x4x4_16b_bf16 v[224:227], v[228:229], v[174:175], v[224:227] cbsz:4 abid:3\n"
    "v_exp_f32 v158, v158 clamp\n"
    "v_exp_f32 v159, v159 clamp\n"
    "v_pk_fma_f32 v[144:145], v[144:145], s[70:71], v[96:97]\n"
    "v_pk_fma_f32 v[146:147], v[146:147], s[70:71], v[98:99]\n"
    "v_pk_add_f32 v[230:231], v[144:145], v[146:147]\n"
    "v_pk_mul_f32 v[232:233], v[144:145], v[146:147]\n"
    "v_pk_fma_f32 v[148:149], v[148:149], s[70:71], v[100:101]\n"
    "v_pk_fma_f32 v[230:231], v[230:231], v[148:149], v[232:233]\n"
    "v_pk_mul_f32 v[232:233], v[232:233], v[148:149]\n"
    "v_pk_fma_f32 v[150:151], v[150:151], s[70:71], v[102:103]\n"
    "v_pk_fma_f32 v[230:231], v[230:231], v[150:151], v[232:233]\n"
    "v_pk_mul_f32 v[232:233], v[232:233], v[150:151]\n"
    "v_pk_fma_f32 v[152:153], v[152:153], s[70:71], v[104:105]\n"
    "v_pk_fma_f32 v[230:231], v[230:231], v[152:153], v[232:233]\n"
    "v_pk_mul_f32 v[232:233], v[232:233], v[152:153]\n"
    "v_pk_fma_f32 v[154:155], v[154:155], s[70:71], v[106:107]\n"
    "v_pk_fma_f32 v[230:231], v[230:231], v[154:155], v[232:233]\n"
    "v_pk_mul_f32 v[232:233], v[232:233], v[154:155]\n"
    "v_pk_fma_f32 v[156:157], v[156:157], s[70:71], v[108:109]\n"
    "v_pk_fma_f32 v[230:231], v[230:231], v[156:157], v[232:233]\n"
    "v_pk_mul_f32 v[232:233], v[232:233], v[156:157]\n"
    "v_pk_fma_f32 v[158:159], v[158:159], s[70:71], v[110:111]\n"
    "v_pk_fma_f32 v[230:231], v[230:231], v[158:159], v[232:233]\n"
    "v_pk_mul_f32 v[232:233], v[232:233], v[158:159]\n"
    "v_rcp_f32 v232, v232\n"
    "v_rcp_f32 v233, v233\n"
    "v_fma_f32 v230, v230, v232, v247\n"
    "v_fmac_f32 v230, v231, v233\n"
    "v_lshlrev_b32 v232, 2, v249\n"
    "v_lshrrev_b32 v233, 2, v249\n"
    "v_and_b32 v232, 56, v232\n"
    "v_and_b32 v233, 4, v233\n"
    "v_add3_u32 v232, v232, v233, s81\n"
    "ds_read_b32 v232, v232\n"
    "s_add_u32 s81, s81, 64\n"
    "s_waitcnt lgkmcnt(0)\n"
    "v_lshlrev_b32 v232, 1, v232\n"
    "buffer_load_ushort v232, v232, s[44:47], 0 offen\n"
    "s_waitcnt vmcnt(0)\n"
    "v_lshlrev_b32 v232, 16, v232\n"
    "v_mul_f32 v230, v230, v232\n"
    "v_cvt_pk_bf16_f32 v230, v230, v230\n"
    "ds_write_b16 v245, v230\n"
    "ds_read_b64 v[228:229], v246\n"
    "s_branch .Ldin_drain1\n"
    ".Ldin_exit:\n"

      : "+{s[0:1]}"(kp), "+{v0}"(tid)
      : "{s2}"(blockIdx.x)
      : "memory", "vcc", "scc", "v1", "v2", "v3", "v4", "v5", "v6", "v7", "v8", "v9", "v10", "v11", "v12", "v13", "v14", "v15", "v16", "v17", "v18", "v19", "v20", "v21", "v22", "v23", "v24", "v25", "v26", "v27", "v28", "v29", "v30", "v31", "v32", "v33", "v34", "v35", "v36", "v37", "v38", "v39", "v40", "v41", "v42", "v43", "v44", "v45", "v46", "v47", "v48", "v49", "v50", "v51", "v52", "v53", "v54", "v55", "v56", "v57", "v58", "v59", "v60", "v61", "v62", "v63", "v64", "v65", "v66", "v67", "v68", "v69", "v70", "v71", "v72", "v73", "v74", "v75", "v76", "v77", "v78", "v79", "v80", "v81", "v82", "v83", "v84", "v85", "v86", "v87", "v88", "v89", "v90", "v91", "v92", "v93", "v94", "v95", "v96", "v97", "v98", "v99", "v100", "v101", "v102", "v103", "v104", "v105", "v106", "v107", "v108", "v109", "v110", "v111", "v112", "v113", "v114", "v115", "v116", "v117", "v118", "v119", "v120", "v121", "v122", "v123", "v124", "v125", "v126", "v127", "v128", "v129", "v130", "v131", "v132", "v133", "v134", "v135", "v136", "v137", "v138", "v139", "v140", "v141", "v142", "v143", "v144", "v145", "v146", "v147", "v148", "v149", "v150", "v151", "v152", "v153", "v154", "v155", "v156", "v157", "v158", "v159", "v160", "v161", "v162", "v163", "v164", "v165", "v166", "v167", "v168", "v169", "v170", "v171", "v172", "v173", "v174", "v175", "v176", "v177", "v178", "v179", "v180", "v181", "v182", "v183", "v184", "v185", "v186", "v187", "v188", "v189", "v190", "v191", "v192", "v193", "v194", "v195", "v196", "v197", "v198", "v199", "v200", "v201", "v202", "v203", "v204", "v205", "v206", "v207", "v208", "v209", "v210", "v211", "v212", "v213", "v214", "v215", "v216", "v217", "v218", "v219", "v220", "v221", "v222", "v223", "v224", "v225", "v226", "v227", "v228", "v229", "v230", "v231", "v232", "v233", "v234", "v235", "v236", "v237", "v238", "v239", "v240", "v241", "v242", "v243", "v244", "v245", "v246", "v247", "v248", "v249", "v250", "v251", "v252", "v253", "v254", "v255", "s3", "s4", "s5", "s6", "s7", "s8", "s9", "s10", "s11", "s12", "s13", "s14", "s15", "s16", "s17", "s18", "s19", "s20", "s21", "s22", "s23", "s24", "s25", "s26", "s27", "s28", "s29", "s30", "s31", "s33", "s34", "s35", "s36", "s37", "s38", "s39", "s40", "s41", "s42", "s43", "s44", "s45", "s46", "s47", "s48", "s49", "s50", "s51", "s52", "s53", "s54", "s55", "s56", "s57", "s58", "s59", "s60", "s61", "s62", "s63", "s64", "s65", "s66", "s67", "s68", "s69", "s70", "s71", "s72", "s73", "s74", "s75", "s76", "s77", "s78", "s79", "s80", "s81", "s82", "s83", "s84", "s85", "s86", "s87", "s88", "s89", "s90", "s91", "s92", "s93", "s94", "s95", "s96");
  asm volatile("" ::"s"((uint32_t)(uintptr_t)smem));
}

__global__ __launch_bounds__(64) void din_fallback_kernel(
    const unsigned short* __restrict__ target, const unsigned short* __restrict__ hist,
    const unsigned short* __restrict__ mask, const unsigned short* __restrict__ w1,
    const unsigned short* __restrict__ b1, const unsigned short* __restrict__ w2,
    const unsigned short* __restrict__ b2, unsigned short* __restrict__ out, int B, int L) {
  __shared__ float s_w[64 * 64];
  __shared__ float s_x[64];
  const int b = blockIdx.x, h = threadIdx.x;
  float bias = bf2f(b1[h]);
  for (int d = 0; d < 64; ++d) {
    const float t = bf2f(target[(size_t)b * 64 + d]);
    const float wc = bf2f(w1[(128 + d) * 64 + h]);
    bias += t * (bf2f(w1[(64 + d) * 64 + h]) - wc);
    s_w[d * 64 + h] = bf2f(w1[d * 64 + h]) + wc + t * bf2f(w1[(192 + d) * 64 + h]);
  }
  const float w2h = bf2f(w2[h]), b2v = bf2f(b2[0]);
  float acc = 0.f;
  for (int l = 0; l < L; ++l) {
    const float m = bf2f(mask[(size_t)b * L + l]);
    if (m == 0.f) continue;
    const float x = bf2f(hist[((size_t)b * L + l) * 64 + h]);
    __syncthreads();
    s_x[h] = x;
    __syncthreads();
    float z = bias;
    for (int d = 0; d < 64; ++d) z += s_x[d] * s_w[d * 64 + h];
    float s = w2h / (1.0f + __expf(-z));
    for (int o = 32; o > 0; o >>= 1) s += __shfl_xor(s, o);
    acc += (s + b2v) * m * x;
  }
  const uint32_t u = __float_as_uint(acc);
  out[(size_t)b * 64 + h] = (unsigned short)((u + 0x7fffu + ((u >> 16) & 1u)) >> 16);
}

extern "C" void kernel_launch(void* const* d_in, const int* in_sizes, int n_in, void* d_out,
                              int out_size, void* d_ws, size_t ws_size, hipStream_t stream) {
  const unsigned short* target = (const unsigned short*)d_in[0];
  const unsigned short* hist = (const unsigned short*)d_in[1];
  const unsigned short* mask = (const unsigned short*)d_in[2];
  const unsigned short* w1 = (const unsigned short*)d_in[3];
  const unsigned short* b1 = (const unsigned short*)d_in[4];
  const unsigned short* w2 = (const unsigned short*)d_in[5];
  const unsigned short* b2 = (const unsigned short*)d_in[6];
  const int B = in_sizes[0] / 64;
  const int L = in_sizes[2] / B;
  if ((L & 1) || L > 256 || B >= (1 << 24) - 64) {
    din_fallback_kernel<<<B, 64, 0, stream>>>(target, hist, mask, w1, b1, w2, b2, (unsigned short*)d_out, B, L);
    return;
  }
  const uint32_t grid = (B + 63) / 64, fl = 0x20000u;
  const bool paced = grid >= 224;
  din_kernel<<<grid, 512, 0, stream>>>(hist, (uint32_t)B * L, fl, mask, (uint32_t)B * L * 2u, fl, target, (uint32_t)B * 128u, fl,
                                       (unsigned short*)d_out, (uint32_t)B * 128u, fl, w1, w2, b1, b2, (uint32_t)L, NO_BATCH, (uint32_t)B,
                                       grid * 8u, 2u * L, grid * 16u * L, paced ? 52u : 0u, paced ? 15u : 0u);
}
```

```cpp
#include <hip/hip_runtime.h>
#include <stdint.h>

constexpr int SMEM_B = 163712;
constexpr uint32_t NO_BATCH = 0x7fffffc0u;

__device__ __forceinline__ float bf2f(unsigned short u) { return __uint_as_float(((uint32_t)u) << 16); }

__global__ __launch_bounds__(512, 2) __attribute__((aligned(4096))) void din_kernel(
    const unsigned short* hist, uint32_t rows, uint32_t f0, const unsigned short* mask, uint32_t mask_bytes, uint32_t f1,
    const unsigned short* target, uint32_t target_bytes, uint32_t f2, unsigned short* out, uint32_t out_bytes, uint32_t f3,
    const unsigned short* w1, const unsigned short* w2, const unsigned short* b1, const unsigned short* b2,
    uint32_t L, uint32_t no_batch, uint32_t B, uint32_t round_rows, uint32_t row_bytes, uint32_t round_bytes, uint32_t period, uint32_t extra) {
  __shared__ __attribute__((aligned(128))) char smem[SMEM_B];
  unsigned long long kp = (unsigned long long)(uintptr_t)__builtin_amdgcn_kernarg_segment_ptr();
  uint32_t tid = threadIdx.x;
  asm volatile(
    "s_load_dwordx16 s[40:55], s[0:1], 0x0\n"
    "s_load_dwordx16 s[4:19], s[0:1], 0x40\n"
    "s_memrealtime s[76:77]\n"
    "v_readfirstlane_b32 s3, v0\n"
    "v_and_b32 v251, 63, v0\n"
    "s_lshr_b32 s3, s3, 6\n"
    "s_lshl3_add_u32 s61, s2, s3\n"
    "s_getpc_b64 s[92:93]\n"
    "s_and_b32 s94, s2, 0xf8\n"
    "s_add_u32 s94, s94, s3\n"
    "s_min_u32 s94, s94, 199\n"
    "s_lshl_b32 s94, s94, 7\n"
    "v_mov_b32 v1, s94\n"
    "global_load_dword v1, v1, s[92:93] offset:1024\n"
    "s_cmp_lt_u32 s3, 4\n"
    "s_cbranch_scc0 .Ldin_young\n"
    "v_lshlrev_b32 v105, 3, v251\n"
    "v_mov_b32 v8, 0\n"
    "v_mov_b32 v9, 0\n"
    "v_mov_b32 v10, 0\n"
    "v_and_b32 v2, 48, v251\n"
    "v_and_b32 v3, 1, v251\n"
    "v_lshl_or_b32 v2, v3, 6, v2\n"
    "s_lshl_b32 s39, s61, 7\n"
    "v_lshlrev_b32 v249, 1, v251\n"
    "s_mul_i32 s68, s3, 11392\n"
    "v_lshlrev_b32_e64 v248, 4, v251\n"
    "s_add_u32 s68, s68, 72576\n"
    "v_and_b32 v24, 0x60, v249\n"
    "s_add_u32 s67, s68, 8192\n"
    "v_or_b32 v250, 0x8000, v24\n"
    "s_add_u32 s65, s68, 9728\n"
    "v_lshrrev_b32 v20, 4, v251\n"
    "s_xor_b32 s64, s67, s65\n"
    "v_and_b32 v244, 56, v251\n"
    "s_add_u32 s96, s68, 11264\n"
    "v_bitop3_b32 v21, v20, v251, 7 bitop3:0x78\n"
    "s_lshl_b32 s66, s3, 7\n"
    "v_lshlrev_b32 v21, 4, v21\n"
    "s_add_u32 s66, s66, 68512\n"
    "v_or_b32 v237, 0x400, v21\n"
    "s_lshr_b32 s95, s67, 2\n"
    "v_xor_b32 v239, 64, v21\n"
    "s_mov_b32 s38, 0xffff\n"
    "v_add_u32 v253, s67, v244\n"
    "v_lshlrev_b32 v105, 3, v251\n"
    "v_mov_b32 v201, v237\n"
    "v_mov_b32 v203, v239\n"
    "v_mov_b32 v205, v237\n"
    "v_mov_b32 v207, v239\n"
    "v_mov_b32 v209, v237\n"
    "v_mov_b32 v211, v239\n"
    "v_mov_b32 v213, v237\n"
    "v_mov_b32 v215, v239\n"
    "v_mov_b32 v220, s95\n"
    "v_mov_b32 v224, 0\n"
    "v_mov_b32 v225, 0\n"
    "v_mov_b32 v226, 0\n"
    "v_mov_b32 v227, 0\n"
    "v_bfe_u32 v21, v251, 1, 3\n"
    "v_xor_b32 v21, v20, v21\n"
    "v_and_b32 v22, 0xf0, v248\n"
    "s_mov_b32 s70, 0x47000000\n"
    "v_lshl_add_u32 v22, v22, 3, s68\n"
    "s_mov_b32 s71, s70\n"
    "v_lshl_add_u32 v234, v21, 4, v22\n"
    "s_add_u32 s63, s66, 16\n"
    "v_xor_b32 v235, 64, v234\n"
    "s_mov_b32 s81, s67\n"
    "v_and_b32 v20, 3, v251\n"
    "s_mov_b64 s[92:93], 0xffff\n"
    "v_lshlrev_b64 v[254:255], v248, s[92:93]\n"
    "s_add_u32 s82, s65, 256\n"
    "v_lshl_or_b32 v21, v20, 3, v24\n"
    "s_mul_i32 s62, s3, 272\n"
    "v_and_b32 v22, 8, v251\n"
    "v_mul_u32_u24 v23, 136, v22\n"
    "v_xor_b32 v21, v21, v23\n"
    "v_and_b32 v23, 4, v251\n"
    "v_lshl_or_b32 v21, v23, 5, v21\n"
    "v_add_u32 v240, s68, v21\n"
    "v_xor_b32 v241, 16, v240\n"
    "v_xor_b32 v242, 32, v240\n"
    "v_xor_b32 v243, 48, v240\n"
    "v_add_u32 v252, s62, v250\n"
    "v_and_b32 v21, 24, v249\n"
    "v_add_u32 v21, s96, v21\n"
    "v_lshl_add_u32 v246, v20, 5, v21\n"
    "v_and_b32 v21, 6, v251\n"
    "v_lshl_or_b32 v21, v21, 2, v24\n"
    "v_and_b32 v23, 2, v249\n"
    "v_lshrrev_b32 v22, 1, v22\n"
    "v_or3_b32 v21, v21, v22, v23\n"
    "v_add_u32 v245, s96, v21\n"
    "s_setprio 2\n"
    "s_waitcnt lgkmcnt(0)\n"
    "s_mul_i32 s36, s61, s16\n"
    "s_add_u32 s94, s36, s16\n"
    "s_cmp_lt_u32 s61, s14\n"
    "s_cselect_b32 s46, s94, 0\n"
    "buffer_load_dwordx2 v[228:229], v105, s[44:47], s36 offen nt\n"
    "s_load_dword s10, s[10:11], 0x0\n"
    "global_load_short_d16_hi v8, v249, s[6:7]\n"
    "global_load_short_d16_hi v9, v249, s[8:9]\n"
    "s_setprio 0\n"
    "s_branch .Ldin_old2\n"
    ".Ldin_young:\n"
    "s_setprio 1\n"
    "v_and_b32 v16, 7, v251\n"
    "s_lshl_b32 s94, s3, 11\n"
    "v_lshlrev_b32 v2, 4, v16\n"
    "v_and_b32 v3, 56, v251\n"
    "v_lshl_or_b32 v3, v3, 5, v2\n"
    "v_add_u32 v6, s94, v3\n"
    "v_add_u32 v3, 0x2000, v6\n"
    "v_add_u32 v4, 0xffffe000, v6\n"
    "v_add_u32 v5, 0x4000, v6\n"
    "v_and_b32 v17, 8, v251\n"
    "s_and_b32 s88, s3, 1\n"
    "v_and_b32 v18, 32, v251\n"
    "s_and_b32 s89, s3, 2\n"
    "v_lshl_or_b32 v18, v16, 2, v18\n"
    "s_lshl_b32 s84, s88, 9\n"
    "v_lshl_add_u32 v18, v18, 3, s84\n"
    "s_lshl_b32 s84, s89, 10\n"
    "v_and_b32 v19, 16, v251\n"
    "s_lshl_b32 s85, s89, 9\n"
    "v_lshl_or_b32 v19, v19, 6, v17\n"
    "s_add_u32 s85, s85, 0x4000\n"
    "v_add3_u32 v12, v18, v19, s84\n"
    "s_lshl2_add_u32 s86, s89, s88\n"
    "v_add_u32 v13, 16, v12\n"
    "s_lshl_b32 s86, s86, 4\n"
    "v_and_b32 v19, 24, v251\n"
    "s_add_u32 s87, s94, 0x4000\n"
    "v_lshrrev_b32 v19, 1, v19\n"
    "v_add3_u32 v14, v18, v19, s85\n"
    "v_add_u32 v15, 16, v14\n"
    "v_bfrev_b32 v7, 1\n"
    "v_lshlrev_b32 v26, 4, v251\n"
    "v_and_b32 v26, 0x1f0, v26\n"
    "v_lshrrev_b32 v27, 5, v251\n"
    "s_add_u32 s39, s61, s3\n"
    "s_sub_u32 s39, s39, 8\n"
    "v_lshlrev_b32 v249, 1, v251\n"
    "s_mul_i32 s68, s3, 11392\n"
    "v_lshlrev_b32_e64 v248, 4, v251\n"
    "s_add_u32 s68, s68, 72576\n"
    "v_and_b32 v24, 0x60, v249\n"
    "s_add_u32 s67, s68, 8192\n"
    "v_or_b32 v250, 0x8000, v24\n"
    "s_add_u32 s65, s68, 9728\n"
    "v_lshrrev_b32 v20, 4, v251\n"
    "s_xor_b32 s64, s67, s65\n"
    "v_and_b32 v244, 56, v251\n"
    "s_add_u32 s96, s68, 11264\n"
    "v_bitop3_b32 v21, v20, v251, 7 bitop3:0x78\n"
    "s_lshl_b32 s66, s3, 7\n"
    "v_lshlrev_b32 v21, 4, v21\n"
    "s_add_u32 s66, s66, 68512\n"
    "v_or_b32 v237, 0x400, v21\n"
    "s_lshr_b32 s95, s67, 2\n"
    "v_xor_b32 v239, 64, v21\n"
    "s_mov_b32 s38, 0xffff\n"
    "v_add_u32 v253, s67, v244\n"
    "v_lshlrev_b32 v105, 3, v251\n"
    "v_mov_b32 v201, v237\n"
    "v_mov_b32 v203, v239\n"
    "v_mov_b32 v205, v237\n"
    "v_mov_b32 v207, v239\n"
    "v_mov_b32 v209, v237\n"
    "v_mov_b32 v211, v239\n"
    "v_mov_b32 v213, v237\n"
    "v_mov_b32 v215, v239\n"
    "v_mov_b32 v220, s95\n"
    "v_mov_b32 v224, 0\n"
    "v_mov_b32 v225, 0\n"
    "s_waitcnt lgkmcnt(0)\n"
    "global_load_dwordx4 v[8:11], v2, s[6:7]\n"
    "global_load_dwordx4 v[128:131], v3, s[4:5] offset:0\n"
    "global_load_dwordx4 v[132:135], v3, s[4:5] offset:128\n"
    "v_mov_b32 v226, 0\n"
    "v_mov_b32 v227, 0\n"
    "v_bfe_u32 v21, v251, 1, 3\n"
    "v_xor_b32 v21, v20, v21\n"
    "v_and_b32 v22, 0xf0, v248\n"
    "s_mov_b32 s70, 0x47000000\n"
    "v_lshl_add_u32 v22, v22, 3, s68\n"
    "s_mov_b32 s71, s70\n"
    "v_lshl_add_u32 v234, v21, 4, v22\n"
    "s_add_u32 s63, s66, 16\n"
    "v_xor_b32 v235, 64, v234\n"
    "s_mov_b32 s81, s67\n"
    "global_load_dwordx4 v[136:139], v4, s[4:5] offset:0\n"
    "v_and_b32_e64 v20, 3, v251\n"
    "s_mov_b64 s[92:93], 0xffff\n"
    "v_lshlrev_b64 v[254:255], v248, s[92:93]\n"
    "s_add_u32 s82, s65, 256\n"
    "v_lshl_or_b32 v21, v20, 3, v24\n"
    "s_mul_i32 s62, s3, 272\n"
    "v_and_b32 v22, 8, v251\n"
    "v_mul_u32_u24 v23, 136, v22\n"
    "v_xor_b32 v21, v21, v23\n"
    "v_and_b32 v23, 4, v251\n"
    "v_lshl_or_b32 v21, v23, 5, v21\n"
    "v_add_u32 v240, s68, v21\n"
    "global_load_dwordx4 v[140:143], v4, s[4:5] offset:128\n"
    "v_xor_b32 v241, 16, v240\n"
    "v_xor_b32 v242, 32, v240\n"
    "v_xor_b32 v243, 48, v240\n"
    "v_add_u32 v252, s62, v250\n"
    "v_and_b32 v21, 24, v249\n"
    "v_add_u32 v21, s96, v21\n"
    "v_lshl_add_u32 v246, v20, 5, v21\n"
    "v_and_b32 v21, 6, v251\n"
    "v_lshl_or_b32 v21, v21, 2, v24\n"
    "v_and_b32 v23, 2, v249\n"
    "v_lshrrev_b32 v22, 1, v22\n"
    "v_or3_b32 v21, v21, v22, v23\n"
    "global_load_dwordx4 v[144:147], v5, s[4:5] offset:0\n"
    "v_add_u32 v245, s96, v21\n"
    "v_cmp_gt_u32 vcc, s16, v26\n"
    "v_mad_u32_u24 v27, v27, s16, v26\n"
    "s_mul_i32 s39, s39, s16\n"
    "s_lshl1_add_u32 s39, s17, s39\n"
    "v_cndmask_b32 v27, v7, v27, vcc\n"
    "s_add_u32 s40, s40, 0xfffffc00\n"
    "s_addc_u32 s41, s41, -1\n"
    "s_mul_i32 s94, s61, s12\n"
    "s_bitset1_b32 s41, 23\n"
    "v_lshl_add_u32 v216, v251, 2, s94\n"
    "s_mov_b64 s[56:57], s[12:13]\n"
    "global_load_dwordx4 v[148:151], v5, s[4:5] offset:128\n"
    "v_add_u32 v217, 1, v216\n"
    "v_add_u32 v218, 2, v216\n"
    "v_add_u32 v219, 3, v216\n"
    "s_mov_b64 s[72:73], s[18:19]\n"
    "s_cmp_lg_u32 s72, 0\n"
    "s_cselect_b32 s94, 6, 0\n"
    "s_cselect_b32 s95, 4, 0\n"
    "s_add_u32 s74, s72, s94\n"
    "s_sub_u32 s75, s72, s95\n"
    "v_mov_b32 v20, s13\n"
    "s_mov_b64 exec, 0xffffff\n"
    "v_mov_b32 v20, -1\n"
    "s_mov_b64 exec, 0xff\n"
    "v_mov_b32 v20, 0\n"
    "s_mov_b32 exec_lo, -1\n"
    "v_lshl_add_u32 v21, v249, 1, s66\n"
    "v_add_u32 v21, -32, v21\n"
    "ds_write_b32 v21, v20\n"
    "s_mov_b32 exec_hi, -1\n"
    "global_load_dwordx4 v[152:155], v6, s[4:5] offset:0\n"
    "s_mov_b32 s37, 0xffff0000\n"
    "v_and_b32 v19, 15, v251\n"
    "v_mul_u32_u24 v19, 272, v19\n"
    "v_add_u32 v97, v19, v250\n"
    "v_add_u32 v98, s87, v248\n"
    "v_add_u32 v99, s86, v250\n"
    "v_add_u32 v100, s86, v97\n"
    "s_mov_b32 s84, 0xbfb8aa3b\n"
    "global_load_dwordx4 v[156:159], v6, s[4:5] offset:128\n"
    "s_branch .Ldin_y2\n"
    ".Ldin_old2:\n"
    "s_add_u32 s40, s40, 0xfffffc00\n"
    "s_addc_u32 s41, s41, -1\n"
    "s_mul_i32 s94, s61, s12\n"
    "s_bitset1_b32 s41, 23\n"
    "v_lshl_add_u32 v216, v251, 2, s94\n"
    "s_mov_b64 s[56:57], s[12:13]\n"
    "v_add_u32 v217, 1, v216\n"
    "v_add_u32 v218, 2, v216\n"
    "v_add_u32 v219, 3, v216\n"
    "s_mov_b64 s[72:73], s[18:19]\n"
    "s_cmp_lg_u32 s72, 0\n"
    "s_cselect_b32 s94, 6, 0\n"
    "s_cselect_b32 s95, 4, 0\n"
    "s_add_u32 s74, s72, s94\n"
    "s_sub_u32 s75, s72, s95\n"
    "v_mov_b32 v20, s13\n"
    "s_mov_b64 exec, 0xffffff\n"
    "v_mov_b32 v20, -1\n"
    "s_mov_b64 exec, 0xff\n"
    "v_mov_b32 v20, 0\n"
    "s_mov_b32 exec_lo, -1\n"
    "v_lshl_add_u32 v21, v249, 1, s66\n"
    "v_add_u32 v21, -32, v21\n"
    "ds_write_b32 v21, v20\n"
    "s_mov_b32 exec_hi, -1\n"
    "v_and_b32 v4, 2, v251\n"
    "v_lshl_add_u32 v4, v4, 6, v250\n"
    "v_and_b32 v5, 16, v248\n"
    "v_and_b32 v6, 12, v251\n"
    "v_or3_b32 v104, v4, v5, v6\n"
    "v_mul_u32_u24 v4, 34, v244\n"
    "v_and_b32 v5, 3, v251\n"
    "v_lshl_add_u32 v4, v5, 5, v4\n"
    "v_and_b32 v5, 4, v251\n"
    "v_lshl_add_u32 v4, v5, 2, v4\n"
    "s_lshl_b32 s86, s62, 3\n"
    "v_add_u32_e64 v18, s86, v4\n"
    "s_mul_i32 s84, s3, s15\n"
    "s_lshl3_add_u32 s84, s2, s84\n"
    "s_lshl_b32 s84, s84, 7\n"
    "s_lshl_b32 s85, s15, 9\n"
    "s_add_u32 s85, s85, s84\n"
    "s_lshl_b32 s94, s61, 6\n"
    "s_add_u32 s94, s3, s94\n"
    "s_cmp_lt_u32 s61, s14\n"
    "s_cselect_b32 s58, s94, s13\n"
    "s_add_u32 s95, s61, s15\n"
    "s_add_u32 s36, s36, s17\n"
    "s_lshl_b32 s94, s95, 6\n"
    "s_add_u32 s94, s3, s94\n"
    "s_add_u32 s96, s36, s16\n"
    "s_add_u32 s94, s94, 8\n"
    "s_cmp_lt_u32 s95, s14\n"
    "s_cselect_b32 s46, s96, 0\n"
    "s_cselect_b32 s59, s94, s13\n"
    "s_setprio 2\n"
    "buffer_load_dwordx4 v[160:163], v2, s[48:51], s39 offen nt\n"
    "buffer_load_dwordx4 v[128:131], v248, s[48:51], s84 offen nt\n"
    "buffer_load_dwordx4 v[132:135], v248, s[48:51], s85 offen nt\n"
    "buffer_load_short_d16_hi v224, v249, s[44:47], s36 offen offset:0 nt\n"
    "buffer_load_short_d16_hi v225, v249, s[44:47], s36 offen offset:128 nt\n"
    "buffer_load_short_d16_hi v226, v249, s[44:47], s36 offen offset:256 nt\n"
    "buffer_load_short_d16_hi v227, v249, s[44:47], s36 offen offset:384 nt\n"
    "s_mul_i32 s46, s14, s16\n"
    "s_nop 0\n"
    "v_mul_u32_u24 v32, 0x9e3779, v251\n"
    "s_mov_b32 s90, 0x807f807f\n"
    "v_mov_b32 v47, 0x3f003f00\n"
    "v_add_u32 v32, 0x7f4a7c15, v32\n"
    "v_mul_lo_u32 v33, v32, v32\n"
    "v_mul_lo_u32 v34, v33, v32\n"
    "v_mul_lo_u32 v35, v34, v32\n"
    "v_mul_lo_u32 v36, v35, v32\n"
    "v_mul_lo_u32 v37, v36, v32\n"
    "v_mul_lo_u32 v38, v37, v32\n"
    "v_mul_lo_u32 v39, v38, v32\n"
    "v_and_or_b32 v32, v32, s90, v47\n"
    "v_and_or_b32 v33, v33, s90, v47\n"
    "v_and_or_b32 v34, v34, s90, v47\n"
    "v_and_or_b32 v35, v35, s90, v47\n"
    "v_and_or_b32 v36, v36, s90, v47\n"
    "v_and_or_b32 v37, v37, s90, v47\n"
    "v_and_or_b32 v38, v38, s90, v47\n"
    "v_and_or_b32 v39, v39, s90, v47\n"
    "s_nop 1\n"
    "s_nop 0\n"
    "v_mfma_f32_32x32x16_bf16 v[64:79], v[36:39], v[32:35], 0\n"
    "v_mfma_f32_32x32x16_bf16 v[48:63], v[32:35], v[36:39], 0\n"
    "v_mfma_f32_32x32x16_bf16 v[64:79], v[36:39], v[32:35], 0\n"
    "v_mfma_f32_32x32x16_bf16 v[48:63], v[32:35], v[36:39], 0\n"
    "v_mfma_f32_32x32x16_bf16 v[64:79], v[36:39], v[32:35], 0\n"
    "v_mfma_f32_32x32x16_bf16 v[48:63], v[32:35], v[36:39], 0\n"
    "v_mfma_f32_32x32x16_bf16 v[64:79], v[36:39], v[32:35], 0\n"
    "v_mfma_f32_32x32x16_bf16 v[48:63], v[32:35], v[36:39], 0\n"
    "v_mfma_f32_32x32x16_bf16 v[64:79], v[36:39], v[32:35], 0\n"
    "v_mfma_f32_32x32x16_bf16 v[48:63], v[32:35], v[36:39], 0\n"
    "s_setprio 2\n"
    "s_waitcnt vmcnt(9)\n"
    "v_cmp_ne_u16_e64 s[84:85], 0, v228\n"
    "v_cmp_lt_u32_e64 s[86:87], s38, v228\n"
    "v_cmp_ne_u16_e64 s[88:89], 0, v229\n"
    "v_cmp_lt_u32_e64 s[90:91], s38, v229\n"
    "v_mbcnt_lo_u32_b32 v220, s84, v220\n"
    "s_bcnt1_i32_b64 s92, s[84:85]\n"
    "v_mbcnt_hi_u32_b32 v220, s85, v220\n"
    "s_lshl2_add_u32 s92, s92, s67\n"
    "v_mbcnt_lo_u32_b32 v220, s86, v220\n"
    "s_bcnt1_i32_b64 s93, s[86:87]\n"
    "v_mbcnt_hi_u32_b32 v220, s87, v220\n"
    "s_lshl2_add_u32 s93, s93, s92\n"
    "v_mbcnt_lo_u32_b32 v220, s88, v220\n"
    "s_bcnt1_i32_b64 s94, s[88:89]\n"
    "v_mbcnt_hi_u32_b32 v220, s89, v220\n"
    "s_lshl2_add_u32 s94, s94, s93\n"
    "v_mbcnt_lo_u32_b32 v220, s90, v220\n"
    "s_bcnt1_i32_b64 s95, s[90:91]\n"
    "v_mbcnt_hi_u32_b32 v220, s91, v220\n"
    "s_lshl2_add_u32 s95, s95, s94\n"
    "v_mov_b32_e64 v21, -1\n"
    "v_lshl_add_u32 v20, v249, 1, s95\n"
    "v_addc_co_u32_e64 v221, vcc, v220, 0, s[84:85]\n"
    "v_addc_co_u32_e64 v222, vcc, v221, 0, s[86:87]\n"
    "v_addc_co_u32_e64 v223, vcc, v222, 0, s[88:89]\n"
    "ds_write2_b32 v20, v21, v21 offset1:64\n"
    "v_lshlrev_b32 v220, 2, v220\n"
    "v_lshlrev_b32 v221, 2, v221\n"
    "v_lshlrev_b32 v222, 2, v222\n"
    "v_lshlrev_b32_e64 v223, 2, v223\n"
    "s_mov_b64 exec, s[84:85]\n"
    "ds_write_b32 v220, v216\n"
    "s_mov_b64 exec, s[86:87]\n"
    "ds_write_b32 v221, v217\n"
    "s_mov_b64 exec, s[88:89]\n"
    "ds_write_b32 v222, v218\n"
    "s_mov_b64 exec, s[90:91]\n"
    "ds_write_b32 v223, v219\n"
    "s_mov_b64 exec, -1\n"
    "ds_read_b32 v200, v253 offset:0\n"
    "ds_read_b32 v202, v253 offset:4\n"
    "ds_read_b32 v204, v253 offset:64\n"
    "ds_read_b32 v206, v253 offset:68\n"
    "ds_read_b32 v208, v253 offset:128\n"
    "ds_read_b32 v210, v253 offset:132\n"
    "ds_read_b32 v212, v253 offset:192\n"
    "ds_read_b32 v214, v253 offset:196\n"
    "s_add_u32 m0, s68, 0\n"
    "s_waitcnt lgkmcnt(6)\n"
    "buffer_load_dwordx4 v[200:201], s[40:43], 0 idxen offen offset:0 nt lds\n"
    "buffer_load_dwordx4 v[202:203], s[40:43], 0 idxen offen offset:1024 nt lds\n"
    "s_add_u32 s95, s95, 60\n"
    "s_add_u32 s94, s67, 384\n"
    "s_andn2_b32 s95, s95, 63\n"
    "s_max_u32 s80, s95, s94\n"
    "s_add_u32 m0, s68, 2048\n"
    "s_waitcnt lgkmcnt(0)\n"
    "buffer_load_dwordx4 v[204:205], s[40:43], 0 idxen offen offset:0 nt lds\n"
    "s_lshl_b32 s10, s10, 16\n"
    "v_writelane_b32 v10, s10, 0\n"
    "s_waitcnt vmcnt(10)\n"
    "v_and_b32 v11, 0x7fffffff, v8\n"
    "v_mfma_f32_32x32x16_bf16 v[64:79], v[36:39], v[32:35], 0\n"
    "v_and_b32 v12, 0x7fffffff, v10\n"
    "v_min_f32 v14, 0, v8\n"
    "v_add_u32 v12, 0xf6000000, v12\n"
    "v_add_f32 v14, v14, v10\n"
    "v_max_i32 v12, v11, v12\n"
    "v_mfma_f32_32x32x16_bf16 v[48:63], v[32:35], v[36:39], 0\n"
    "v_rcp_f32 v11, v11\n"
    "v_add_f32_dpp v14, v14, v14 row_ror:8 row_mask:0xf bank_mask:0xf\n"
    "v_max_u32_dpp v12, v12, v12 row_ror:8 row_mask:0xf bank_mask:0xf\n"
    "s_mov_b32 s84, 0xbfb8aa3b\n"
    "v_add_f32_dpp v14, v14, v14 row_ror:4 row_mask:0xf bank_mask:0xf\n"
    "v_mfma_f32_32x32x16_bf16 v[64:79], v[36:39], v[32:35], 0\n"
    "v_max_u32_dpp v12, v12, v12 row_ror:4 row_mask:0xf bank_mask:0xf\n"
    "v_bfrev_b32 v13, 1\n"
    "v_add_f32_dpp v14, v14, v14 row_ror:2 row_mask:0xf bank_mask:0xf\n"
    "v_max_u32_dpp v12, v12, v12 row_ror:2 row_mask:0xf bank_mask:0xf\n"
    "v_mfma_f32_32x32x16_bf16 v[48:63], v[32:35], v[36:39], 0\n"
    "v_bitop3_b32 v8, v8, s84, v13 bitop3:0x6c\n"
    "v_add_f32_dpp v14, v14, v14 row_ror:1 row_mask:0xf bank_mask:0xf\n"
    "v_max_u32_dpp v12, v12, v12 row_ror:1 row_mask:0xf bank_mask:0xf\n"
    "s_nop 0\n"
    "v_readlane_b32 s84, v12, 0\n"
    "v_mfma_f32_32x32x16_bf16 v[64:79], v[36:39], v[32:35], 0\n"
    "v_readlane_b32 s85, v12, 16\n"
    "v_readlane_b32 s86, v12, 32\n"
    "buffer_load_dwordx4 v[206:207], s[40:43], 0 idxen offen offset:1024 nt lds\n"
    "v_readlane_b32 s87, v12, 48\n"
    "s_max_u32 s84, s84, s85\n"
    "s_max_u32 s86, s86, s87\n"
    "v_mfma_f32_32x32x16_bf16 v[48:63], v[32:35], v[36:39], 0\n"
    "s_max_u32 s84, s84, s86\n"
    "s_lshr_b32 s84, s84, 23\n"
    "s_max_u32 s84, s84, 15\n"
    "s_min_u32 s84, s84, 253\n"
    "s_lshl_b32 s84, s84, 23\n"
    "v_mfma_f32_32x32x16_bf16 v[64:79], v[36:39], v[32:35], 0\n"
    "s_add_u32 s85, s84, 0xf9000000\n"
    "s_sub_u32 s86, 0x86000000, s84\n"
    "v_mul_f32 v11, s85, v11\n"
    "v_min_f32 v11, 0x46800000, v11\n"
    "v_mfma_f32_32x32x16_bf16 v[48:63], v[32:35], v[36:39], 0\n"
    "v_log_f32 v13, v11\n"
    "v_mul_f32 v14, s86, v14\n"
    "v_mov_b32 v15, s85\n"
    "v_add_f32 v13, 0xc1700000, v13\n"
    "v_fmac_f32 v13, v9, v8\n"
    "v_mfma_f32_32x32x16_bf16 v[64:79], v[36:39], v[32:35], 0\n"
    "ds_write_b32 v104, v13 offset:35200\n"
    "ds_write_b32 v104, v11 offset:34816\n"
    "v_mov_b32_e64 v247, v14\n"
    "s_mov_b32 s69, s85\n"
    "ds_write_b64 v250, v[14:15] offset:35072\n"
    "v_mfma_f32_32x32x16_bf16 v[48:63], v[32:35], v[36:39], 0\n"
    "s_mov_b32 s94, 0x10001\n"
    "s_mov_b32 s95, 0xc080c080\n"
    "v_pk_min_u16 v22, v228, s94\n"
    "v_pk_min_u16 v23, v229, s94\n"
    "v_pk_mad_u16 v228, v22, s95, v228\n"
    "v_mfma_f32_32x32x16_bf16 v[64:79], v[36:39], v[32:35], 0\n"
    "v_pk_mad_u16 v229, v23, s95, v229\n"
    "s_sub_u32 s87, s80, s81\n"
    "v_or_b32 v228, v228, v229\n"
    "s_lshr_b32 s87, s87, 6\n"
    "v_mfma_f32_32x32x16_bf16 v[48:63], v[32:35], v[36:39], 0\n"
    "s_lshl2_add_u32 s86, s3, 68224\n"
    "s_waitcnt vmcnt(8)\n"
    "ds_write_b128 v18, v[128:131] offset:32768\n"
    "ds_write_b128 v18, v[132:135] offset:41472\n"
    "v_and_b32_dpp v184, v160, v254 quad_perm:[0,0,0,0] row_mask:0xf bank_mask:0xf\n"
    "v_mfma_f32_32x32x16_bf16 v[64:79], v[36:39], v[32:35], 0\n"
    "v_and_b32_dpp v185, v161, v255 quad_perm:[0,0,0,0] row_mask:0xf bank_mask:0xf\n"
    "v_and_b32_dpp v186, v162, v254 quad_perm:[0,0,0,0] row_mask:0xf bank_mask:0xf\n"
    "v_and_b32_dpp v187, v163, v255 quad_perm:[0,0,0,0] row_mask:0xf bank_mask:0xf\n"
    "v_and_b32_dpp v188, v160, v254 quad_perm:[1,1,1,1] row_mask:0xf bank_mask:0xf\n"
    "v_and_b32_dpp v189, v161, v255 quad_perm:[1,1,1,1] row_mask:0xf bank_mask:0xf\n"
    "v_mfma_f32_32x32x16_bf16 v[48:63], v[32:35], v[36:39], 0\n"
    "v_and_b32_dpp v190, v162, v254 quad_perm:[1,1,1,1] row_mask:0xf bank_mask:0xf\n"
    "v_and_b32_dpp v191, v163, v255 quad_perm:[1,1,1,1] row_mask:0xf bank_mask:0xf\n"
    "v_mov_b32 v106, s86\n"
    "v_mov_b32 v108, s66\n"
    "v_mov_b32 v109, s58\n"
    "v_mfma_f32_32x32x16_bf16 v[64:79], v[36:39], v[32:35], 0\n"
    "v_mov_b32_e64 v110, s59\n"
    "s_waitcnt vmcnt(4)\n"
    "v_cmp_neq_f32_e64 s[20:21], 0, v224\n"
    "v_cmp_neq_f32_e64 s[22:23], 0, v225\n"
    "v_mfma_f32_32x32x16_bf16 v[48:63], v[32:35], v[36:39], 0\n"
    "v_cmp_neq_f32_e64 s[24:25], 0, v226\n"
    "v_cmp_neq_f32_e64 s[26:27], 0, v227\n"
    "v_fma_f32 v224, v224, v224, -v224\n"
    "v_fma_f32 v225, v225, v225, -v225\n"
    "v_fma_f32 v226, v226, v226, -v226\n"
    "v_mfma_f32_32x32x16_bf16 v[64:79], v[36:39], v[32:35], 0\n"
    "v_fma_f32 v227, v227, v227, -v227\n"
    "s_bcnt1_i32_b64 s88, s[20:21]\n"
    "s_bcnt1_i32_b64 s89, s[22:23]\n"
    "s_bcnt1_i32_b64 s90, s[24:25]\n"
    "s_bcnt1_i32_b64 s91, s[26:27]\n"
    "v_mfma_f32_32x32x16_bf16 v[48:63], v[32:35], v[36:39], 0\n"
    "v_or3_b32 v228, v228, v224, v225\n"
    "s_add_u32 s88, s88, s89\n"
    "v_or3_b32 v228, v228, v226, v227\n"
    "s_add_u32 s90, s90, s91\n"
    "v_cmp_ne_u32_e64 s[84:85], 0, v228\n"
    "v_mfma_f32_32x32x16_bf16 v[64:79], v[36:39], v[32:35], 0\n"
    "s_add_u32 s88, s88, s90\n"
    "s_pack_ll_b32_b16 s88, s87, s88\n"
    "s_mov_b64 exec, 1\n"
    "v_mov_b32 v107, s88\n"
    "ds_write2_b32 v108, v109, v110 offset1:2\n"
    "s_cmp_lg_u64 s[84:85], 0\n"
    "ds_write_b32 v106, v107\n"
    "s_cselect_b32 s91, 2.0, 0\n"
    "s_mov_b64 exec, -1\n"
    "s_or_b32 s58, s58, s91\n"
    "v_mfma_f32_32x32x16_bf16 v[48:63], v[32:35], v[36:39], 0\n"
    "s_or_b32 s59, s59, s91\n"
    "s_nop 11\n"
    "s_mov_b64 vcc, 0\n"
    "s_waitcnt lgkmcnt(0)\n"
    "s_barrier\n"
    "s_setprio 0\n"
    ".Ldin_entry:\n"
    "ds_read_b128 v[32:35], v248 offset:16384\n"
    "ds_read_b128 v[64:67], v248 offset:0\n"
    "ds_read_b128 v[68:71], v248 offset:1024\n"
    "ds_read_b128 v[36:39], v248 offset:17408\n"
    "ds_read_b128 v[72:75], v248 offset:2048\n"
    "ds_read_b128 v[76:79], v248 offset:3072\n"
    "ds_read_b128 v[40:43], v248 offset:18432\n"
    "ds_read_b128 v[80:83], v248 offset:4096\n"
    "ds_read_b128 v[84:87], v248 offset:5120\n"
    "ds_read_b128 v[44:47], v248 offset:19456\n"
    "ds_read_b128 v[88:91], v248 offset:6144\n"
    "ds_read_b128 v[92:95], v248 offset:7168\n"
    "s_add_u32 s67, s67, 320\n"
    "s_waitcnt lgkmcnt(9)\n"
    "ds_read_b128 v[48:51], v248 offset:20480\n"
    "ds_read_b128 v[192:195], v248 offset:8192\n"
    "ds_read_b128 v[196:199], v248 offset:9216\n"
    "v_mfma_f32_4x4x4_16b_bf16 v[144:147], v[184:185], v[32:33], v[64:67]\n"
    "s_nop 0\n"
    "v_mfma_f32_4x4x4_16b_bf16 v[148:151], v[186:187], v[34:35], v[68:71]\n"
    "s_add_u32 m0, s68, 4096\n"
    "s_cbranch_vccnz .Ldin_e1\n"
    "buffer_load_dwordx4 v[208:209], s[40:43], 0 idxen offen offset:0 nt lds\n"
    "buffer_load_dwordx4 v[210:211], s[40:43], 0 idxen offen offset:1024 nt lds\n"
    ".Ldin_e1:\n"
    "s_waitcnt lgkmcnt(9)\n"
    "ds_read_b128 v[52:55], v248 offset:21504\n"
    "ds_read_b128 v[200:203], v248 offset:10240\n"
    "ds_read_b128 v[204:207], v248 offset:11264\n"
    "v_mfma_f32_4x4x4_16b_bf16 v[152:155], v[188:189], v[36:37], v[72:75]\n"
    "s_nop 1\n"
    "v_cvt_pk_bf16_f32 v0, v144, v145\n"
    "v_cvt_pk_bf16_f32 v1, v146, v147\n"
    "v_mfma_f32_4x4x4_16b_bf16 v[156:159], v[190:191], v[38:39], v[76:79]\n"
    "s_add_u32 m0, s68, 6144\n"
    "s_cbranch_vccnz .Ldin_e2\n"
    "buffer_load_dwordx4 v[212:213], s[40:43], 0 idxen offen offset:0 nt lds\n"
    "buffer_load_dwordx4 v[214:215], s[40:43], 0 idxen offen offset:1024 nt lds\n"
    ".Ldin_e2:\n"
    "s_waitcnt lgkmcnt(9)\n"
    "s_nop 0\n"
    "ds_read_b128 v[56:59], v248 offset:22528\n"
    "ds_read_b128 v[208:211], v248 offset:12288\n"
    "ds_read_b128 v[212:215], v248 offset:13312\n"
    "v_cvt_pk_bf16_f32 v2, v148, v149\n"
    "v_cvt_pk_bf16_f32 v3, v150, v151\n"
    "v_mfma_f32_4x4x4_16b_bf16 v[144:147], v[184:185], v[40:41], v[80:83]\n"
    "v_cvt_pk_bf16_f32 v4, v152, v153\n"
    "v_cvt_pk_bf16_f32 v5, v154, v155\n"
    "v_mfma_f32_4x4x4_16b_bf16 v[148:151], v[186:187], v[42:43], v[84:87]\n"
    "s_cbranch_vccnz .Ldin_e3\n"
    "s_barrier\n"
    ".Ldin_e3:\n"
    "s_waitcnt lgkmcnt(9)\n"
    "s_nop 0\n"
    "ds_read_b128 v[60:63], v248 offset:23552\n"
    "ds_read_b128 v[216:219], v248 offset:14336\n"
    "ds_read_b128 v[220:223], v248 offset:15360\n"
    "v_cvt_pk_bf16_f32 v6, v156, v157\n"
    "v_cvt_pk_bf16_f32 v7, v158, v159\n"
    "v_mfma_f32_4x4x4_16b_bf16 v[152:155], v[188:189], v[44:45], v[88:91]\n"
    "v_cvt_pk_bf16_f32 v8, v144, v145\n"
    "v_cvt_pk_bf16_f32 v9, v146, v147\n"
    "v_mfma_f32_4x4x4_16b_bf16 v[156:159], v[190:191], v[46:47], v[92:95]\n"
    "s_waitcnt lgkmcnt(9)\n"
    "v_cvt_pk_bf16_f32 v10, v148, v149\n"
    "v_cvt_pk_bf16_f32 v11, v150, v151\n"
    "v_mfma_f32_4x4x4_16b_bf16 v[144:147], v[184:185], v[48:49], v[192:195]\n"
    "v_cvt_pk_bf16_f32 v12, v152, v153\n"
    "v_cvt_pk_bf16_f32 v13, v154, v155\n"
    "v_mfma_f32_4x4x4_16b_bf16 v[148:151], v[186:187], v[50:51], v[196:199]\n"
    "s_waitcnt lgkmcnt(6)\n"
    "v_cvt_pk_bf16_f32 v14, v156, v157\n"
    "v_cvt_pk_bf16_f32 v15, v158, v159\n"
    "v_mfma_f32_4x4x4_16b_bf16 v[152:155], v[188:189], v[52:53], v[200:203]\n"
    "v_cvt_pk_bf16_f32 v16, v144, v145\n"
    "v_cvt_pk_bf16_f32 v17, v146, v147\n"
    "v_mfma_f32_4x4x4_16b_bf16 v[156:159], v[190:191], v[54:55], v[204:207]\n"
    "s_waitcnt lgkmcnt(3)\n"
    "s_waitcnt vmcnt(6)\n"
    "ds_read_b128 v[160:163], v234 offset:0\n"
    "ds_read_b128 v[164:167], v235 offset:0\n"
    "ds_read_b32 v236, v253 offset:256\n"
    "ds_read_b32 v238, v253 offset:260\n"
    "ds_read_b64_tr_b16 v[168:169], v240 offset:0\n"
    "ds_read_b64_tr_b16 v[170:171], v241 offset:256\n"
    "ds_read_b64_tr_b16 v[172:173], v242 offset:512\n"
    "ds_read_b64_tr_b16 v[174:175], v243 offset:768\n"
    "v_cvt_pk_bf16_f32 v18, v148, v149\n"
    "v_cvt_pk_bf16_f32 v19, v150, v151\n"
    "v_mfma_f32_4x4x4_16b_bf16 v[144:147], v[184:185], v[56:57], v[208:211]\n"
    "v_cvt_pk_bf16_f32 v20, v152, v153\n"
    "v_cvt_pk_bf16_f32 v21, v154, v155\n"
    "v_mfma_f32_4x4x4_16b_bf16 v[148:151], v[186:187], v[58:59], v[212:215]\n"
    "s_waitcnt lgkmcnt(8)\n"
    "v_cvt_pk_bf16_f32 v22, v156, v157\n"
    "v_cvt_pk_bf16_f32 v23, v158, v159\n"
    "v_mfma_f32_4x4x4_16b_bf16 v[152:155], v[188:189], v[60:61], v[216:219]\n"
    "v_cvt_pk_bf16_f32 v24, v144, v145\n"
    "v_cvt_pk_bf16_f32 v25, v146, v147\n"
    "v_mfma_f32_4x4x4_16b_bf16 v[156:159], v[190:191], v[62:63], v[220:223]\n"
    "s_cbranch_vccnz .Ldin_e8\n"
    "s_barrier\n"
    ".Ldin_e8:\n"
    "s_nop 0\n"
    "ds_read_b128 v[112:115], v252 offset:17408\n"
    "ds_read_b128 v[116:119], v252 offset:17424\n"
    "ds_read_b128 v[120:123], v252 offset:17536\n"
    "ds_read_b128 v[124:127], v252 offset:17552\n"
    "v_cvt_pk_bf16_f32 v26, v148, v149\n"
    "v_cvt_pk_bf16_f32 v27, v150, v151\n"
    "v_cvt_pk_bf16_f32 v28, v152, v153\n"
    "v_cvt_pk_bf16_f32 v29, v154, v155\n"
    "s_nop 0\n"
    "v_cvt_pk_bf16_f32 v30, v156, v157\n"
    "v_cvt_pk_bf16_f32 v31, v158, v159\n"
    "s_add_u32 m0, s68, 0\n"
    "s_waitcnt lgkmcnt(0)\n"
    "v_mfma_f32_16x16x32_bf16 v[128:131], v[0:3], v[160:163], v[112:115]\n"
    "ds_read_b128 v[96:99], v250 offset:34816\n"
    "v_mfma_f32_16x16x32_bf16 v[128:131], v[4:7], v[164:167], v[128:131]\n"
    "ds_read_b128 v[100:103], v250 offset:34832\n"
    "v_mfma_f32_16x16x32_bf16 v[132:135], v[8:11], v[160:163], v[116:119]\n"
    "ds_read_b128 v[104:107], v250 offset:34944\n"
    "v_mfma_f32_16x16x32_bf16 v[132:135], v[12:15], v[164:167], v[132:135]\n"
    "ds_read_b128 v[108:111], v250 offset:34960\n"
    "v_mfma_f32_16x16x32_bf16 v[136:139], v[16:19], v[160:163], v[120:123]\n"
    "v_mfma_f32_16x16x32_bf16 v[136:139], v[20:23], v[164:167], v[136:139]\n"
    "v_mfma_f32_16x16x32_bf16 v[140:143], v[24:27], v[160:163], v[124:127]\n"
    "v_mfma_f32_16x16x32_bf16 v[140:143], v[28:31], v[164:167], v[140:143]\n"
    "s_waitcnt vmcnt(4)\n"
    "ds_read_b128 v[160:163], v234 offset:2048\n"
    "ds_read_b128 v[164:167], v235 offset:2048\n"
    "buffer_load_dwordx4 v[236:237], s[40:43], 0 idxen offen offset:0 nt lds\n"
    "buffer_load_dwordx4 v[238:239], s[40:43], 0 idxen offen offset:1024 nt lds\n"
    "s_memrealtime s[34:35]\n"
    "s_cmp_lg_u64 vcc, 0\n"
    "s_cselect_b32 s94, 58, 52\n"
    "s_cselect_b32 s95, 35, 20\n"
    "s_cmp_lg_u32 s72, 0\n"
    "s_cselect_b32 s72, s94, 0\n"
    "s_cselect_b32 s95, s95, 0\n"
    "s_waitcnt lgkmcnt(0)\n"
    "s_lshl1_add_u32 s76, s95, s34\n"
    "s_cmp_lt_i32 s59, s57\n"
    "s_cselect_b32 s76, s76, -1\n"
    "s_bitcmp1_b32 s58, 30\n"
    "s_cbranch_scc1 .Ldin_odd\n"
    "s_nop 0\n"
    ".Ldin_A0:\n"
    "v_mfma_f32_16x16x32_bf16 v[144:147], v[0:3], v[160:163], v[112:115]\n"
    "v_exp_f32 v128, v128 clamp\n"
    "v_mfma_f32_16x16x32_bf16 v[144:147], v[4:7], v[164:167], v[144:147]\n"
    "v_exp_f32 v129, v129 clamp\n"
    "v_mfma_f32_16x16x32_bf16 v[148:151], v[8:11], v[160:163], v[116:119]\n"
    "v_exp_f32 v130, v130 clamp\n"
    "v_mfma_f32_16x16x32_bf16 v[148:151], v[12:15], v[164:167], v[148:151]\n"
    "v_exp_f32 v131, v131 clamp\n"
    "v_mfma_f32_16x16x32_bf16 v[152:155], v[16:19], v[160:163], v[120:123]\n"
    "v_exp_f32 v132, v132 clamp\n"
    "v_mfma_f32_16x16x32_bf16 v[152:155], v[20:23], v[164:167], v[152:155]\n"
    "v_exp_f32 v133, v133 clamp\n"
    "v_mfma_f32_16x16x32_bf16 v[156:159], v[24:27], v[160:163], v[124:127]\n"
    "v_exp_f32 v134, v134 clamp\n"
    "v_mfma_f32_16x16x32_bf16 v[156:159], v[28:31], v[164:167], v[156:159]\n"
    "v_exp_f32 v135, v135 clamp\n"
    "v_exp_f32 v136, v136 clamp\n"
    "s_add_u32 m0, s68, 2048\n"
    "v_exp_f32 v137, v137 clamp\n"
    "v_add_u32 v253, s67, v244\n"
    "s_waitcnt lgkmcnt(0)\n"
    "v_mfma_f32_4x4x4_16b_bf16 v[224:227], v[228:229], v[176:177], v[224:227] cbsz:4 abid:0\n"
    "v_exp_f32 v138, v138 clamp\n"
    "v_exp_f32 v139, v139 clamp\n"
    "v_mfma_f32_4x4x4_16b_bf16 v[224:227], v[228:229], v[178:179], v[224:227] cbsz:4 abid:1\n"
    "v_exp_f32 v140, v140 clamp\n"
    "v_exp_f32 v141, v141 clamp\n"
    "v_mfma_f32_4x4x4_16b_bf16 v[224:227], v[228:229], v[180:181], v[224:227] cbsz:4 abid:2\n"
    "v_exp_f32 v142, v142 clamp\n"
    "v_exp_f32 v143, v143 clamp\n"
    "v_mfma_f32_4x4x4_16b_bf16 v[224:227], v[228:229], v[182:183], v[224:227] cbsz:4 abid:3\n"
    "s_waitcnt vmcnt(4)\n"
    "ds_read_b64_tr_b16 v[176:177], v240 offset:2048\n"
    "ds_read_b64_tr_b16 v[178:179], v241 offset:2304\n"
    "ds_read_b64_tr_b16 v[180:181], v242 offset:2560\n"
    "ds_read_b64_tr_b16 v[182:183], v243 offset:2816\n"
    "ds_read_b32 v236, v253 offset:0\n"
    "ds_read_b32 v238, v253 offset:4\n"
    "ds_read_b128 v[160:163], v234 offset:4096\n"
    "ds_read_b128 v[164:167], v235 offset:4096\n"
    "s_cbranch_vccz .Ldin_A0_np\n"
    "s_setprio 1\n"
    ".Ldin_A0_np:\n"
    "v_pk_fma_f32 v[128:129], v[128:129], s[70:71], v[96:97]\n"
    "v_pk_fma_f32 v[130:131], v[130:131], s[70:71], v[98:99]\n"
    "v_add_f32 v191, v224, v225\n"
    "v_add_f32 v190, v226, v227\n"
    "v_add_f32 v191, v191, v190\n"
    "v_add_u32 v190, s62, v249\n"
    "v_mul_f32 v191, s69, v191\n"
    "v_cvt_pk_bf16_f32 v191, v191, v191\n"
    "ds_write_b16 v190, v191 offset:32768\n"
    "s_bfe_u32 s94, s59, 0x180006\n"
    "s_mul_i32 s86, s94, s56\n"
    "v_mbcnt_lo_u32_b32 v184, s20, 0\n"
    "v_mbcnt_lo_u32_b32 v185, s22, 0\n"
    "v_mbcnt_lo_u32_b32 v186, s24, 0\n"
    "v_mbcnt_lo_u32_b32 v187, s26, 0\n"
    "s_bcnt1_i32_b64 s90, s[20:21]\n"
    "s_bcnt1_i32_b64 s91, s[22:23]\n"
    "s_bcnt1_i32_b64 s92, s[24:25]\n"
    "s_bcnt1_i32_b64 s93, s[26:27]\n"
    "v_mbcnt_hi_u32_b32 v184, s21, v184\n"
    "v_mbcnt_hi_u32_b32 v185, s23, v185\n"
    "v_mbcnt_hi_u32_b32 v186, s25, v186\n"
    "v_mbcnt_hi_u32_b32 v187, s27, v187\n"
    "s_lshl2_add_u32 s90, s90, s65\n"
    "s_lshl2_add_u32 s91, s91, s90\n"
    "s_lshl2_add_u32 s92, s92, s91\n"
    "s_lshl2_add_u32 s93, s93, s92\n"
    "v_add_u32_e64 v188, s86, v251\n"
    "v_pk_add_f32 v[230:231], v[128:129], v[130:131]\n"
    "v_pk_mul_f32 v[232:233], v[128:129], v[130:131]\n"
    "v_pk_fma_f32 v[132:133], v[132:133], s[70:71], v[100:101]\n"
    "v_pk_fma_f32 v[230:231], v[230:231], v[132:133], v[232:233]\n"
    "v_pk_mul_f32 v[232:233], v[232:233], v[132:133]\n"
    "v_lshl_add_u32 v184, v184, 2, s65\n"
    "v_lshl_add_u32 v185, v185, 2, s90\n"
    "v_lshl_add_u32 v186, v186, 2, s91\n"
    "v_lshl_add_u32 v187, v187, 2, s92\n"
    "v_mov_b32 v191, -1\n"
    "v_lshl_add_u32 v190, v249, 1, s93\n"
    "ds_write2_b32 v190, v191, v191 offset1:64\n"
    "s_mov_b64 exec, s[20:21]\n"
    "ds_write_b32 v184, v188\n"
    "s_mov_b64 exec, s[22:23]\n"
    "v_add_u32 v189, 64, v188\n"
    "ds_write_b32 v185, v189\n"
    "s_mov_b64 exec, s[24:25]\n"
    "v_add_u32 v190, 128, v188\n"
    "ds_write_b32 v186, v190\n"
    "s_mov_b64 exec, s[26:27]\n"
    "v_add_u32 v189, 192, v188\n"
    "ds_write_b32 v187, v189\n"
    "s_mov_b64 exec, -1\n"
    "s_add_u32 s95, s93, 60\n"
    "s_add_u32 s94, s82, 128\n"
    "s_andn2_b32 s95, s95, 63\n"
    "s_max_u32 s83, s95, s94\n"
    "v_pk_fma_f32 v[134:135], v[134:135], s[70:71], v[102:103]\n"
    "v_pk_fma_f32 v[230:231], v[230:231], v[134:135], v[232:233]\n"
    "v_pk_mul_f32 v[232:233], v[232:233], v[134:135]\n"
    "v_pk_fma_f32 v[136:137], v[136:137], s[70:71], v[104:105]\n"
    "v_pk_fma_f32 v[230:231], v[230:231], v[136:137], v[232:233]\n"
    "v_pk_mul_f32 v[232:233], v[232:233], v[136:137]\n"
    "v_pk_fma_f32 v[138:139], v[138:139], s[70:71], v[106:107]\n"
    "v_pk_fma_f32 v[230:231], v[230:231], v[138:139], v[232:233]\n"
    "v_pk_mul_f32 v[232:233], v[232:233], v[138:139]\n"
    "v_pk_fma_f32 v[140:141], v[140:141], s[70:71], v[108:109]\n"
    "v_pk_fma_f32 v[230:231], v[230:231], v[140:141], v[232:233]\n"
    "v_pk_mul_f32 v[232:233], v[232:233], v[140:141]\n"
    "v_pk_fma_f32 v[142:143], v[142:143], s[70:71], v[110:111]\n"
    "v_pk_fma_f32 v[230:231], v[230:231], v[142:143], v[232:233]\n"
    "v_pk_mul_f32 v[232:233], v[232:233], v[142:143]\n"
    "v_rcp_f32 v232, v232\n"
    "v_rcp_f32 v233, v233\n"
    "v_fma_f32 v230, v230, v232, v247\n"
    "v_fmac_f32 v230, v231, v233\n"
    "v_cvt_pk_bf16_f32 v230, v230, v230\n"
    "s_add_u32 s67, s67, 64\n"
    "s_waitcnt lgkmcnt(0)\n"
    "ds_write_b16 v245, v230\n"
    "ds_read_b64 v[228:229], v246\n"
    "buffer_load_dwordx4 v[236:237], s[40:43], 0 idxen offen offset:0 nt lds\n"
    "buffer_load_dwordx4 v[238:239], s[40:43], 0 idxen offen offset:1024 nt lds\n"
    ".Ldin_B1:\n"
    "v_mfma_f32_16x16x32_bf16 v[128:131], v[0:3], v[160:163], v[112:115]\n"
    "v_exp_f32 v144, v144 clamp\n"
    "v_mfma_f32_16x16x32_bf16 v[128:131], v[4:7], v[164:167], v[128:131]\n"
    "v_exp_f32 v145, v145 clamp\n"
    "v_mfma_f32_16x16x32_bf16 v[132:135], v[8:11], v[160:163], v[116:119]\n"
    "v_exp_f32 v146, v146 clamp\n"
    "v_mfma_f32_16x16x32_bf16 v[132:135], v[12:15], v[164:167], v[132:135]\n"
    "v_exp_f32 v147, v147 clamp\n"
    "v_mfma_f32_16x16x32_bf16 v[136:139], v[16:19], v[160:163], v[120:123]\n"
    "v_exp_f32 v148, v148 clamp\n"
    "v_mfma_f32_16x16x32_bf16 v[136:139], v[20:23], v[164:167], v[136:139]\n"
    "v_exp_f32 v149, v149 clamp\n"
    "v_mfma_f32_16x16x32_bf16 v[140:143], v[24:27], v[160:163], v[124:127]\n"
    "v_exp_f32 v150, v150 clamp\n"
    "v_mfma_f32_16x16x32_bf16 v[140:143], v[28:31], v[164:167], v[140:143]\n"
    "v_exp_f32 v151, v151 clamp\n"
    "v_exp_f32 v152, v152 clamp\n"
    "s_cmp_eq_u32 s67, s80\n"
    "s_cselect_b32 s67, s65, s67\n"
    "s_and_b32 s94, s59, 63\n"
    "s_mul_i32 s94, s94, 272\n"
    "v_add_u32 v252, s94, v250\n"
    "s_add_u32 m0, s68, 4096\n"
    "v_exp_f32 v153, v153 clamp\n"
    "v_add_u32 v253, s67, v244\n"
    "s_waitcnt lgkmcnt(0)\n"
    "v_mfma_f32_4x4x4_16b_bf16 v[224:227], v[228:229], v[168:169], 0 cbsz:4 abid:0\n"
    "v_exp_f32 v154, v154 clamp\n"
    "v_exp_f32 v155, v155 clamp\n"
    "v_mfma_f32_4x4x4_16b_bf16 v[224:227], v[228:229], v[170:171], v[224:227] cbsz:4 abid:1\n"
    "v_exp_f32 v156, v156 clamp\n"
    "v_exp_f32 v157, v157 clamp\n"
    "v_mfma_f32_4x4x4_16b_bf16 v[224:227], v[228:229], v[172:173], v[224:227] cbsz:4 abid:2\n"
    "v_exp_f32 v158, v158 clamp\n"
    "v_exp_f32 v159, v159 clamp\n"
    "v_mfma_f32_4x4x4_16b_bf16 v[224:227], v[228:229], v[174:175], v[224:227] cbsz:4 abid:3\n"
    "s_waitcnt vmcnt(4)\n"
    "s_memrealtime s[34:35]\n"
    "ds_read_b64_tr_b16 v[168:169], v240 offset:4096\n"
    "ds_read_b64_tr_b16 v[170:171], v241 offset:4352\n"
    "ds_read_b64_tr_b16 v[172:173], v242 offset:4608\n"
    "ds_read_b64_tr_b16 v[174:175], v243 offset:4864\n"
    "ds_read_b32 v236, v253 offset:0\n"
    "ds_read_b32 v238, v253 offset:4\n"
    "ds_read_b128 v[160:163], v234 offset:6144\n"
    "ds_read_b128 v[164:167], v235 offset:6144\n"
    "ds_read_b128 v[184:187], v252 offset:0\n"
    "ds_read_b128 v[188:191], v252 offset:16\n"
    "v_pk_fma_f32 v[144:145], v[144:145], s[70:71], v[96:97]\n"
    "v_pk_fma_f32 v[146:147], v[146:147], s[70:71], v[98:99]\n"
    "v_pk_add_f32 v[230:231], v[144:145], v[146:147]\n"
    "v_pk_mul_f32 v[232:233], v[144:145], v[146:147]\n"
    "v_pk_fma_f32 v[148:149], v[148:149], s[70:71], v[100:101]\n"
    "v_pk_fma_f32 v[230:231], v[230:231], v[148:149], v[232:233]\n"
    "v_pk_mul_f32 v[232:233], v[232:233], v[148:149]\n"
    "v_pk_fma_f32 v[150:151], v[150:151], s[70:71], v[102:103]\n"
    "v_pk_fma_f32 v[230:231], v[230:231], v[150:151], v[232:233]\n"
    "v_pk_mul_f32 v[232:233], v[232:233], v[150:151]\n"
    "v_pk_fma_f32 v[152:153], v[152:153], s[70:71], v[104:105]\n"
    "v_pk_fma_f32 v[230:231], v[230:231], v[152:153], v[232:233]\n"
    "v_pk_mul_f32 v[232:233], v[232:233], v[152:153]\n"
    "v_pk_fma_f32 v[154:155], v[154:155], s[70:71], v[106:107]\n"
    "v_pk_fma_f32 v[230:231], v[230:231], v[154:155], v[232:233]\n"
    "v_pk_mul_f32 v[232:233], v[232:233], v[154:155]\n"
    "v_pk_fma_f32 v[156:157], v[156:157], s[70:71], v[108:109]\n"
    "v_pk_fma_f32 v[230:231], v[230:231], v[156:157], v[232:233]\n"
    "v_pk_mul_f32 v[232:233], v[232:233], v[156:157]\n"
    "v_pk_fma_f32 v[158:159], v[158:159], s[70:71], v[110:111]\n"
    "v_pk_fma_f32 v[230:231], v[230:231], v[158:159], v[232:233]\n"
    "v_pk_mul_f32 v[232:233], v[232:233], v[158:159]\n"
    "v_rcp_f32 v232, v232\n"
    "v_rcp_f32 v233, v233\n"
    "v_fma_f32 v230, v230, v232, v247\n"
    "v_fmac_f32 v230, v231, v233\n"
    "v_cvt_pk_bf16_f32 v230, v230, v230\n"
    "s_add_u32 s67, s67, 64\n"
    "s_waitcnt lgkmcnt(0)\n"
    "ds_write_b16 v245, v230\n"
    "ds_read_b64 v[228:229], v246\n"
    "s_cmp_le_u32 s34, s76\n"
    "s_cbranch_scc1 .Ldin_B1_wait\n"
    ".Ldin_B1_go:\n"
    "s_sub_u32 s94, s34, 60\n"
    "s_max_u32 s76, s76, s94\n"
    "s_add_u32 s76, s76, s72\n"
    ".Ldin_B1_dma:\n"
    "buffer_load_dwordx4 v[236:237], s[40:43], 0 idxen offen offset:0 nt lds\n"
    "buffer_load_dwordx4 v[238:239], s[40:43], 0 idxen offen offset:1024 nt lds\n"
    ".Ldin_B1_nd:\n"
    ".Ldin_G2:\n"
    "v_mfma_f32_16x16x32_bf16 v[144:147], v[0:3], v[160:163], v[112:115]\n"
    "v_exp_f32 v128, v128 clamp\n"
    "v_mfma_f32_16x16x32_bf16 v[144:147], v[4:7], v[164:167], v[144:147]\n"
    "v_exp_f32 v129, v129 clamp\n"
    "v_mfma_f32_16x16x32_bf16 v[148:151], v[8:11], v[160:163], v[116:119]\n"
    "v_exp_f32 v130, v130 clamp\n"
    "v_mfma_f32_16x16x32_bf16 v[148:151], v[12:15], v[164:167], v[148:151]\n"
    "v_exp_f32 v131, v131 clamp\n"
    "v_mfma_f32_16x16x32_bf16 v[152:155], v[16:19], v[160:163], v[120:123]\n"
    "v_exp_f32 v132, v132 clamp\n"
    "v_mfma_f32_16x16x32_bf16 v[152:155], v[20:23], v[164:167], v[152:155]\n"
    "v_exp_f32 v133, v133 clamp\n"
    "v_mfma_f32_16x16x32_bf16 v[156:159], v[24:27], v[160:163], v[124:127]\n"
    "v_exp_f32 v134, v134 clamp\n"
    "v_mfma_f32_16x16x32_bf16 v[156:159], v[28:31], v[164:167], v[156:159]\n"
    "v_exp_f32 v135, v135 clamp\n"
    "v_exp_f32 v136, v136 clamp\n"
    "s_cmp_eq_u32 s67, s80\n"
    "s_cselect_b32 s67, s65, s67\n"
    "s_add_u32 m0, s68, 6144\n"
    "v_exp_f32 v137, v137 clamp\n"
    "v_add_u32_e64 v253, s67, v244\n"
    "s_waitcnt lgkmcnt(0)\n"
    "v_mfma_f32_4x4x4_16b_bf16 v[224:227], v[228:229], v[176:177], v[224:227] cbsz:4 abid:0\n"
    "v_exp_f32 v138, v138 clamp\n"
    "v_exp_f32 v139, v139 clamp\n"
    "v_mfma_f32_4x4x4_16b_bf16 v[224:227], v[228:229], v[178:179], v[224:227] cbsz:4 abid:1\n"
    "v_exp_f32 v140, v140 clamp\n"
    "v_exp_f32 v141, v141 clamp\n"
    "v_mfma_f32_4x4x4_16b_bf16 v[224:227], v[228:229], v[180:181], v[224:227] cbsz:4 abid:2\n"
    "v_exp_f32 v142, v142 clamp\n"
    "v_exp_f32 v143, v143 clamp\n"
    "v_mfma_f32_4x4x4_16b_bf16 v[224:227], v[228:229], v[182:183], v[224:227] cbsz:4 abid:3\n"
    "s_waitcnt vmcnt(4)\n"
    "s_memrealtime s[34:35]\n"
    "ds_read_b64_tr_b16 v[176:177], v240 offset:6144\n"
    "ds_read_b64_tr_b16 v[178:179], v241 offset:6400\n"
    "ds_read_b64_tr_b16 v[180:181], v242 offset:6656\n"
    "ds_read_b64_tr_b16 v[182:183], v243 offset:6912\n"
    "ds_read_b32 v236, v253 offset:0\n"
    "ds_read_b32 v238, v253 offset:4\n"
    "ds_read_b128 v[160:163], v234 offset:0\n"
    "ds_read_b128 v[164:167], v235 offset:0\n"
    "v_pk_fma_f32 v[128:129], v[128:129], s[70:71], v[96:97]\n"
    "v_pk_fma_f32 v[130:131], v[130:131], s[70:71], v[98:99]\n"
    "v_pk_add_f32 v[230:231], v[128:129], v[130:131]\n"
    "v_pk_mul_f32 v[232:233], v[128:129], v[130:131]\n"
    "v_pk_fma_f32 v[132:133], v[132:133], s[70:71], v[100:101]\n"
    "v_pk_fma_f32 v[230:231], v[230:231], v[132:133], v[232:233]\n"
    "v_pk_mul_f32 v[232:233], v[232:233], v[132:133]\n"
    "v_pk_fma_f32 v[134:135], v[134:135], s[70:71], v[102:103]\n"
    "v_pk_fma_f32 v[230:231], v[230:231], v[134:135], v[232:233]\n"
    "v_pk_mul_f32 v[232:233], v[232:233], v[134:135]\n"
    "v_pk_fma_f32 v[136:137], v[136:137], s[70:71], v[104:105]\n"
    "v_pk_fma_f32 v[230:231], v[230:231], v[136:137], v[232:233]\n"
    "v_pk_mul_f32 v[232:233], v[232:233], v[136:137]\n"
    "v_pk_fma_f32 v[138:139], v[138:139], s[70:71], v[106:107]\n"
    "v_pk_fma_f32 v[230:231], v[230:231], v[138:139], v[232:233]\n"
    "v_pk_mul_f32 v[232:233], v[232:233], v[138:139]\n"
    "v_pk_fma_f32 v[140:141], v[140:141], s[70:71], v[108:109]\n"
    "v_pk_fma_f32 v[230:231], v[230:231], v[140:141], v[232:233]\n"
    "v_pk_mul_f32 v[232:233], v[232:233], v[140:141]\n"
    "v_pk_fma_f32 v[142:143], v[142:143], s[70:71], v[110:111]\n"
    "v_pk_fma_f32 v[230:231], v[230:231], v[142:143], v[232:233]\n"
    "v_pk_mul_f32 v[232:233], v[232:233], v[142:143]\n"
    "v_rcp_f32 v232, v232\n"
    "v_rcp_f32 v233, v233\n"
    "v_fma_f32 v230, v230, v232, v247\n"
    "v_fmac_f32 v230, v231, v233\n"
    "v_cvt_pk_bf16_f32 v230, v230, v230\n"
    "s_add_u32 s67, s67, 64\n"
    "s_waitcnt lgkmcnt(0)\n"
    "ds_write_b16 v245, v230\n"
    "ds_read_b64 v[228:229], v246\n"
    "s_cmp_le_u32 s34, s76\n"
    "s_cbranch_scc1 .Ldin_G2_wait\n"
    ".Ldin_G2_go:\n"
    "s_sub_u32 s94, s34, 60\n"
    "s_max_u32 s76, s76, s94\n"
    "s_add_u32 s76, s76, s72\n"
    ".Ldin_G2_dma:\n"
    "buffer_load_dwordx4 v[236:237], s[40:43], 0 idxen offen offset:0 nt lds\n"
    "buffer_load_dwordx4 v[238:239], s[40:43], 0 idxen offen offset:1024 nt lds\n"
    ".Ldin_G2_nd:\n"
    "s_cmp_eq_u32 s67, s82\n"
    "s_cbranch_scc1 .Ldin_L3\n"
    ".Ldin_G3:\n"
    "v_mfma_f32_16x16x32_bf16 v[128:131], v[0:3], v[160:163], v[112:115]\n"
    "v_exp_f32 v144, v144 clamp\n"
    "v_mfma_f32_16x16x32_bf16 v[128:131], v[4:7], v[164:167], v[128:131]\n"
    "v_exp_f32 v145, v145 clamp\n"
    "v_mfma_f32_16x16x32_bf16 v[132:135], v[8:11], v[160:163], v[116:119]\n"
    "v_exp_f32 v146, v146 clamp\n"
    "v_mfma_f32_16x16x32_bf16 v[132:135], v[12:15], v[164:167], v[132:135]\n"
    "v_exp_f32 v147, v147 clamp\n"
    "v_mfma_f32_16x16x32_bf16 v[136:139], v[16:19], v[160:163], v[120:123]\n"
    "v_exp_f32 v148, v148 clamp\n"
    "v_mfma_f32_16x16x32_bf16 v[136:139], v[20:23], v[164:167], v[136:139]\n"
    "v_exp_f32 v149, v149 clamp\n"
    "v_mfma_f32_16x16x32_bf16 v[140:143], v[24:27], v[160:163], v[124:127]\n"
    "v_exp_f32 v150, v150 clamp\n"
    "v_mfma_f32_16x16x32_bf16 v[140:143], v[28:31], v[164:167], v[140:143]\n"
    "v_exp_f32 v151, v151 clamp\n"
    "v_exp_f32 v152, v152 clamp\n"
    "s_cmp_eq_u32 s67, s80\n"
    "s_cselect_b32 s67, s65, s67\n"
    "s_add_u32 m0, s68, 0\n"
    "v_exp_f32 v153, v153 clamp\n"
    "v_add_u32 v253, s67, v244\n"
    "s_waitcnt lgkmcnt(0)\n"
    "v_mfma_f32_4x4x4_16b_bf16 v[224:227], v[228:229], v[168:169], v[224:227] cbsz:4 abid:0\n"
    "v_exp_f32 v154, v154 clamp\n"
    "v_exp_f32 v155, v155 clamp\n"
    "v_mfma_f32_4x4x4_16b_bf16 v[224:227], v[228:229], v[170:171], v[224:227] cbsz:4 abid:1\n"
    "v_exp_f32 v156, v156 clamp\n"
    "v_exp_f32 v157, v157 clamp\n"
    "v_mfma_f32_4x4x4_16b_bf16 v[224:227], v[228:229], v[172:173], v[224:227] cbsz:4 abid:2\n"
    "v_exp_f32 v158, v158 clamp\n"
    "v_exp_f32 v159, v159 clamp\n"
    "v_mfma_f32_4x4x4_16b_bf16 v[224:227], v[228:229], v[174:175], v[224:227] cbsz:4 abid:3\n"
    "s_waitcnt vmcnt(4)\n"
    "s_memrealtime s[34:35]\n"
    "ds_read_b64_tr_b16 v[168:169], v240 offset:0\n"
    "ds_read_b64_tr_b16 v[170:171], v241 offset:256\n"
    "ds_read_b64_tr_b16 v[172:173], v242 offset:512\n"
    "ds_read_b64_tr_b16 v[174:175], v243 offset:768\n"
    "ds_read_b32 v236, v253 offset:0\n"
    "ds_read_b32 v238, v253 offset:4\n"
    "ds_read_b128 v[160:163], v234 offset:2048\n"
    "ds_read_b128 v[164:167], v235 offset:2048\n"
    "v_pk_fma_f32 v[144:145], v[144:145], s[70:71], v[96:97]\n"
    "v_pk_fma_f32 v[146:147], v[146:147], s[70:71], v[98:99]\n"
    "v_pk_add_f32 v[230:231], v[144:145], v[146:147]\n"
    "v_pk_mul_f32 v[232:233], v[144:145], v[146:147]\n"
    "v_pk_fma_f32 v[148:149], v[148:149], s[70:71], v[100:101]\n"
    "v_pk_fma_f32 v[230:231], v[230:231], v[148:149], v[232:233]\n"
    "v_pk_mul_f32 v[232:233], v[232:233], v[148:149]\n"
    "v_pk_fma_f32 v[150:151], v[150:151], s[70:71], v[102:103]\n"
    "v_pk_fma_f32 v[230:231], v[230:231], v[150:151], v[232:233]\n"
    "v_pk_mul_f32 v[232:233], v[232:233], v[150:151]\n"
    "v_pk_fma_f32 v[152:153], v[152:153], s[70:71], v[104:105]\n"
    "v_pk_fma_f32 v[230:231], v[230:231], v[152:153], v[232:233]\n"
    "v_pk_mul_f32 v[232:233], v[232:233], v[152:153]\n"
    "v_pk_fma_f32 v[154:155], v[154:155], s[70:71], v[106:107]\n"
    "v_pk_fma_f32 v[230:231], v[230:231], v[154:155], v[232:233]\n"
    "v_pk_mul_f32 v[232:233], v[232:233], v[154:155]\n"
    "v_pk_fma_f32 v[156:157], v[156:157], s[70:71], v[108:109]\n"
    "v_pk_fma_f32 v[230:231], v[230:231], v[156:157], v[232:233]\n"
    "v_pk_mul_f32 v[232:233], v[232:233], v[156:157]\n"
    "v_pk_fma_f32 v[158:159], v[158:159], s[70:71], v[110:111]\n"
    "v_pk_fma_f32 v[230:231], v[230:231], v[158:159], v[232:233]\n"
    "v_pk_mul_f32 v[232:233], v[232:233], v[158:159]\n"
    "v_rcp_f32 v232, v232\n"
    "v_rcp_f32 v233, v233\n"
    "v_fma_f32 v230, v230, v232, v247\n"
    "v_fmac_f32 v230, v231, v233\n"
    "v_cvt_pk_bf16_f32 v230, v230, v230\n"
    "s_add_u32 s67, s67, 64\n"
    "s_waitcnt lgkmcnt(0)\n"
    "ds_write_b16 v245, v230\n"
    "ds_read_b64 v[228:229], v246\n"
    "s_cmp_le_u32 s34, s76\n"
    "s_cbranch_scc1 .Ldin_G3_wait\n"
    ".Ldin_G3_go:\n"
    "s_add_u32 s76, s76, s72\n"
    ".Ldin_G3_dma:\n"
    "buffer_load_dwordx4 v[236:237], s[40:43], 0 idxen offen offset:0 nt lds\n"
    "buffer_load_dwordx4 v[238:239], s[40:43], 0 idxen offen offset:1024 nt lds\n"
    ".Ldin_G3_nd:\n"
    "s_cmp_eq_u32 s67, s82\n"
    "s_cbranch_scc1 .Ldin_L0\n"
    ".Ldin_G0:\n"
    "v_mfma_f32_16x16x32_bf16 v[144:147], v[0:3], v[160:163], v[112:115]\n"
    "v_exp_f32 v128, v128 clamp\n"
    "v_mfma_f32_16x16x32_bf16 v[144:147], v[4:7], v[164:167], v[144:147]\n"
    "v_exp_f32 v129, v129 clamp\n"
    "v_mfma_f32_16x16x32_bf16 v[148:151], v[8:11], v[160:163], v[116:119]\n"
    "v_exp_f32 v130, v130 clamp\n"
    "v_mfma_f32_16x16x32_bf16 v[148:151], v[12:15], v[164:167], v[148:151]\n"
    "v_exp_f32 v131, v131 clamp\n"
    "v_mfma_f32_16x16x32_bf16 v[152:155], v[16:19], v[160:163], v[120:123]\n"
    "v_exp_f32 v132, v132 clamp\n"
    "v_mfma_f32_16x16x32_bf16 v[152:155], v[20:23], v[164:167], v[152:155]\n"
    "v_exp_f32 v133, v133 clamp\n"
    "v_mfma_f32_16x16x32_bf16 v[156:159], v[24:27], v[160:163], v[124:127]\n"
    "v_exp_f32 v134, v134 clamp\n"
    "v_mfma_f32_16x16x32_bf16 v[156:159], v[28:31], v[164:167], v[156:159]\n"
    "v_exp_f32 v135, v135 clamp\n"
    "v_exp_f32 v136, v136 clamp\n"
    "s_cmp_eq_u32 s67, s80\n"
    "s_cselect_b32 s67, s65, s67\n"
    "s_add_u32 m0, s68, 2048\n"
    "v_exp_f32 v137, v137 clamp\n"
    "v_add_u32_e64 v253, s67, v244\n"
    "s_waitcnt lgkmcnt(0)\n"
    "v_mfma_f32_4x4x4_16b_bf16 v[224:227], v[228:229], v[176:177], v[224:227] cbsz:4 abid:0\n"
    "v_exp_f32 v138, v138 clamp\n"
    "v_exp_f32 v139, v139 clamp\n"
    "v_mfma_f32_4x4x4_16b_bf16 v[224:227], v[228:229], v[178:179], v[224:227] cbsz:4 abid:1\n"
    "v_exp_f32 v140, v140 clamp\n"
    "v_exp_f32 v141, v141 clamp\n"
    "v_mfma_f32_4x4x4_16b_bf16 v[224:227], v[228:229], v[180:181], v[224:227] cbsz:4 abid:2\n"
    "v_exp_f32 v142, v142 clamp\n"
    "v_exp_f32 v143, v143 clamp\n"
    "v_mfma_f32_4x4x4_16b_bf16 v[224:227], v[228:229], v[182:183], v[224:227] cbsz:4 abid:3\n"
    "s_waitcnt vmcnt(4)\n"
    "s_memrealtime s[34:35]\n"
    "ds_read_b64_tr_b16 v[176:177], v240 offset:2048\n"
    "ds_read_b64_tr_b16 v[178:179], v241 offset:2304\n"
    "ds_read_b64_tr_b16 v[180:181], v242 offset:2560\n"
    "ds_read_b64_tr_b16 v[182:183], v243 offset:2816\n"
    "ds_read_b32 v236, v253 offset:0\n"
    "ds_read_b32 v238, v253 offset:4\n"
    "ds_read_b128 v[160:163], v234 offset:4096\n"
    "ds_read_b128 v[164:167], v235 offset:4096\n"
    "v_pk_fma_f32 v[128:129], v[128:129], s[70:71], v[96:97]\n"
    "v_pk_fma_f32 v[130:131], v[130:131], s[70:71], v[98:99]\n"
    "v_pk_add_f32 v[230:231], v[128:129], v[130:131]\n"
    "v_pk_mul_f32 v[232:233], v[128:129], v[130:131]\n"
    "v_pk_fma_f32 v[132:133], v[132:133], s[70:71], v[100:101]\n"
    "v_pk_fma_f32 v[230:231], v[230:231], v[132:133], v[232:233]\n"
    "v_pk_mul_f32 v[232:233], v[232:233], v[132:133]\n"
    "v_pk_fma_f32 v[134:135], v[134:135], s[70:71], v[102:103]\n"
    "v_pk_fma_f32 v[230:231], v[230:231], v[134:135], v[232:233]\n"
    "v_pk_mul_f32 v[232:233], v[232:233], v[134:135]\n"
    "v_pk_fma_f32 v[136:137], v[136:137], s[70:71], v[104:105]\n"
    "v_pk_fma_f32 v[230:231], v[230:231], v[136:137], v[232:233]\n"
    "v_pk_mul_f32 v[232:233], v[232:233], v[136:137]\n"
    "v_pk_fma_f32 v[138:139], v[138:139], s[70:71], v[106:107]\n"
    "v_pk_fma_f32 v[230:231], v[230:231], v[138:139], v[232:233]\n"
    "v_pk_mul_f32 v[232:233], v[232:233], v[138:139]\n"
    "v_pk_fma_f32 v[140:141], v[140:141], s[70:71], v[108:109]\n"
    "v_pk_fma_f32 v[230:231], v[230:231], v[140:141], v[232:233]\n"
    "v_pk_mul_f32 v[232:233], v[232:233], v[140:141]\n"
    "v_pk_fma_f32 v[142:143], v[142:143], s[70:71], v[110:111]\n"
    "v_pk_fma_f32 v[230:231], v[230:231], v[142:143], v[232:233]\n"
    "v_pk_mul_f32 v[232:233], v[232:233], v[142:143]\n"
    "v_rcp_f32 v232, v232\n"
    "v_rcp_f32 v233, v233\n"
    "v_fma_f32 v230, v230, v232, v247\n"
    "v_fmac_f32 v230, v231, v233\n"
    "v_cvt_pk_bf16_f32 v230, v230, v230\n"
    "s_add_u32 s67, s67, 64\n"
    "s_waitcnt lgkmcnt(0)\n"
    "ds_write_b16 v245, v230\n"
    "ds_read_b64 v[228:229], v246\n"
    "s_cmp_le_u32 s34, s76\n"
    "s_cbranch_scc1 .Ldin_G0_wait\n"
    ".Ldin_G0_go:\n"
    "s_sub_u32 s94, s34, 60\n"
    "s_max_u32 s76, s76, s94\n"
    "s_add_u32 s76, s76, s72\n"
    ".Ldin_G0_dma:\n"
    "buffer_load_dwordx4 v[236:237], s[40:43], 0 idxen offen offset:0 nt lds\n"
    "buffer_load_dwordx4 v[238:239], s[40:43], 0 idxen offen offset:1024 nt lds\n"
    ".Ldin_G0_nd:\n"
    "s_cmp_eq_u32 s67, s82\n"
    "s_cbranch_scc1 .Ldin_L1\n"
    ".Ldin_G1:\n"
    "v_mfma_f32_16x16x32_bf16 v[128:131], v[0:3], v[160:163], v[112:115]\n"
    "v_exp_f32 v144, v144 clamp\n"
    "v_mfma_f32_16x16x32_bf16 v[128:131], v[4:7], v[164:167], v[128:131]\n"
    "v_exp_f32 v145, v145 clamp\n"
    "v_mfma_f32_16x16x32_bf16 v[132:135], v[8:11], v[160:163], v[116:119]\n"
    "v_exp_f32 v146, v146 clamp\n"
    "v_mfma_f32_16x16x32_bf16 v[132:135], v[12:15], v[164:167], v[132:135]\n"
    "v_exp_f32 v147, v147 clamp\n"
    "v_mfma_f32_16x16x32_bf16 v[136:139], v[16:19], v[160:163], v[120:123]\n"
    "v_exp_f32 v148, v148 clamp\n"
    "v_mfma_f32_16x16x32_bf16 v[136:139], v[20:23], v[164:167], v[136:139]\n"
    "v_exp_f32 v149, v149 clamp\n"
    "v_mfma_f32_16x16x32_bf16 v[140:143], v[24:27], v[160:163], v[124:127]\n"
    "v_exp_f32 v150, v150 clamp\n"
    "v_mfma_f32_16x16x32_bf16 v[140:143], v[28:31], v[164:167], v[140:143]\n"
    "v_exp_f32 v151, v151 clamp\n"
    "v_exp_f32 v152, v152 clamp\n"
    "s_cmp_eq_u32 s67, s80\n"
    "s_cselect_b32 s67, s65, s67\n"
    "s_add_u32 m0, s68, 4096\n"
    "v_exp_f32 v153, v153 clamp\n"
    "v_add_u32_e64 v253, s67, v244\n"
    "s_waitcnt lgkmcnt(0)\n"
    "v_mfma_f32_4x4x4_16b_bf16 v[224:227], v[228:229], v[168:169], v[224:227] cbsz:4 abid:0\n"
    "v_exp_f32 v154, v154 clamp\n"
    "v_exp_f32 v155, v155 clamp\n"
    "v_mfma_f32_4x4x4_16b_bf16 v[224:227], v[228:229], v[170:171], v[224:227] cbsz:4 abid:1\n"
    "v_exp_f32 v156, v156 clamp\n"
    "v_exp_f32 v157, v157 clamp\n"
    "v_mfma_f32_4x4x4_16b_bf16 v[224:227], v[228:229], v[172:173], v[224:227] cbsz:4 abid:2\n"
    "v_exp_f32 v158, v158 clamp\n"
    "v_exp_f32 v159, v159 clamp\n"
    "v_mfma_f32_4x4x4_16b_bf16 v[224:227], v[228:229], v[174:175], v[224:227] cbsz:4 abid:3\n"
    "s_waitcnt vmcnt(4)\n"
    "s_memrealtime s[34:35]\n"
    "ds_read_b64_tr_b16 v[168:169], v240 offset:4096\n"
    "ds_read_b64_tr_b16 v[170:171], v241 offset:4352\n"
    "ds_read_b64_tr_b16 v[172:173], v242 offset:4608\n"
    "ds_read_b64_tr_b16 v[174:175], v243 offset:4864\n"
    "ds_read_b32 v236, v253 offset:0\n"
    "ds_read_b32 v238, v253 offset:4\n"
    "ds_read_b128 v[160:163], v234 offset:6144\n"
    "ds_read_b128 v[164:167], v235 offset:6144\n"
    "v_pk_fma_f32 v[144:145], v[144:145], s[70:71], v[96:97]\n"
    "v_pk_fma_f32 v[146:147], v[146:147], s[70:71], v[98:99]\n"
    "v_pk_add_f32 v[230:231], v[144:145], v[146:147]\n"
    "v_pk_mul_f32 v[232:233], v[144:145], v[146:147]\n"
    "v_pk_fma_f32 v[148:149], v[148:149], s[70:71], v[100:101]\n"
    "v_pk_fma_f32 v[230:231], v[230:231], v[148:149], v[232:233]\n"
    "v_pk_mul_f32 v[232:233], v[232:233], v[148:149]\n"
    "v_pk_fma_f32 v[150:151], v[150:151], s[70:71], v[102:103]\n"
    "v_pk_fma_f32 v[230:231], v[230:231], v[150:151], v[232:233]\n"
    "v_pk_mul_f32 v[232:233], v[232:233], v[150:151]\n"
    "v_pk_fma_f32 v[152:153], v[152:153], s[70:71], v[104:105]\n"
    "v_pk_fma_f32 v[230:231], v[230:231], v[152:153], v[232:233]\n"
    "v_pk_mul_f32 v[232:233], v[232:233], v[152:153]\n"
    "v_pk_fma_f32 v[154:155], v[154:155], s[70:71], v[106:107]\n"
    "v_pk_fma_f32 v[230:231], v[230:231], v[154:155], v[232:233]\n"
    "v_pk_mul_f32 v[232:233], v[232:233], v[154:155]\n"
    "v_pk_fma_f32 v[156:157], v[156:157], s[70:71], v[108:109]\n"
    "v_pk_fma_f32 v[230:231], v[230:231], v[156:157], v[232:233]\n"
    "v_pk_mul_f32 v[232:233], v[232:233], v[156:157]\n"
    "v_pk_fma_f32 v[158:159], v[158:159], s[70:71], v[110:111]\n"
    "v_pk_fma_f32 v[230:231], v[230:231], v[158:159], v[232:233]\n"
    "v_pk_mul_f32 v[232:233], v[232:233], v[158:159]\n"
    "v_rcp_f32 v232, v232\n"
    "v_rcp_f32 v233, v233\n"
    "v_fma_f32 v230, v230, v232, v247\n"
    "v_fmac_f32 v230, v231, v233\n"
    "v_cvt_pk_bf16_f32 v230, v230, v230\n"
    "s_add_u32 s67, s67, 64\n"
    "s_waitcnt lgkmcnt(0)\n"
    "ds_write_b16 v245, v230\n"
    "ds_read_b64 v[228:229], v246\n"
    "s_cmp_le_u32 s34, s76\n"
    "s_cbranch_scc1 .Ldin_G1_wait\n"
    ".Ldin_G1_go:\n"
    "s_add_u32 s76, s76, s72\n"
    ".Ldin_G1_dma:\n"
    "buffer_load_dwordx4 v[236:237], s[40:43], 0 idxen offen offset:0 nt lds\n"
    "buffer_load_dwordx4 v[238:239], s[40:43], 0 idxen offen offset:1024 nt lds\n"
    ".Ldin_G1_nd:\n"
    "s_cmp_eq_u32 s67, s82\n"
    "s_cbranch_scc1 .Ldin_L2\n"
    "s_branch .Ldin_G2\n"
    ".Ldin_B1_wait:\n"
    "s_cmp_lt_i32 s59, s57\n"
    "s_cbranch_scc1 .Ldin_B1_w2\n"
    "s_sub_u32 s94, s67, s65\n"
    "s_cmp_gt_u32 s94, 256\n"
    "s_cbranch_scc1 .Ldin_B1_dma\n"
    "s_cmp_eq_u32 s94, 64\n"
    "s_waitcnt vmcnt(2)\n"
    "s_cbranch_scc1 .Ldin_B1_nd\n"
    "s_waitcnt vmcnt(0)\n"
    "s_branch .Ldin_B1_nd\n"
    ".Ldin_B1_slp:\n"
    "s_sleep 1\n"
    ".Ldin_B1_w2:\n"
    "s_memrealtime s[34:35]\n"
    "s_waitcnt lgkmcnt(0)\n"
    "s_sub_u32 s94, s34, s76\n"
    "s_cmp_lt_i32 s94, 0\n"
    "s_cbranch_scc1 .Ldin_B1_slp\n"
    "s_branch .Ldin_B1_go\n"
    ".Ldin_G2_wait:\n"
    "s_cmp_lt_i32 s59, s57\n"
    "s_cbranch_scc1 .Ldin_G2_w2\n"
    "s_sub_u32 s94, s67, s65\n"
    "s_cmp_gt_u32 s94, 256\n"
    "s_cbranch_scc1 .Ldin_G2_dma\n"
    "s_cmp_eq_u32 s94, 64\n"
    "s_waitcnt vmcnt(2)\n"
    "s_cbranch_scc1 .Ldin_G2_nd\n"
    "s_waitcnt vmcnt(0)\n"
    "s_branch .Ldin_G2_nd\n"
    ".Ldin_G2_slp:\n"
    "s_sleep 1\n"
    ".Ldin_G2_w2:\n"
    "s_memrealtime s[34:35]\n"
    "s_waitcnt lgkmcnt(0)\n"
    "s_sub_u32 s94, s34, s76\n"
    "s_cmp_lt_i32 s94, 0\n"
    "s_cbranch_scc1 .Ldin_G2_slp\n"
    "s_branch .Ldin_G2_go\n"
    ".Ldin_G3_wait:\n"
    "s_cmp_lt_i32 s59, s57\n"
    "s_cbranch_scc1 .Ldin_G3_w2\n"
    "s_sub_u32 s94, s67, s65\n"
    "s_cmp_gt_u32 s94, 256\n"
    "s_cbranch_scc1 .Ldin_G3_dma\n"
    "s_cmp_eq_u32 s94, 64\n"
    "s_waitcnt vmcnt(2)\n"
    "s_cbranch_scc1 .Ldin_G3_nd\n"
    "s_waitcnt vmcnt(0)\n"
    "s_branch .Ldin_G3_nd\n"
    ".Ldin_G3_slp:\n"
    "s_sleep 1\n"
    ".Ldin_G3_w2:\n"
    "s_memrealtime s[34:35]\n"
    "s_waitcnt lgkmcnt(0)\n"
    "s_sub_u32 s94, s34, s76\n"
    "s_cmp_lt_i32 s94, 0\n"
    "s_cbranch_scc1 .Ldin_G3_slp\n"
    "s_branch .Ldin_G3_go\n"
    ".Ldin_G0_wait:\n"
    "s_cmp_lt_i32 s59, s57\n"
    "s_cbranch_scc1 .Ldin_G0_w2\n"
    "s_sub_u32 s94, s67, s65\n"
    "s_cmp_gt_u32 s94, 256\n"
    "s_cbranch_scc1 .Ldin_G0_dma\n"
    "s_cmp_eq_u32 s94, 64\n"
    "s_waitcnt vmcnt(2)\n"
    "s_cbranch_scc1 .Ldin_G0_nd\n"
    "s_waitcnt vmcnt(0)\n"
    "s_branch .Ldin_G0_nd\n"
    ".Ldin_G0_slp:\n"
    "s_sleep 1\n"
    ".Ldin_G0_w2:\n"
    "s_memrealtime s[34:35]\n"
    "s_waitcnt lgkmcnt(0)\n"
    "s_sub_u32 s94, s34, s76\n"
    "s_cmp_lt_i32 s94, 0\n"
    "s_cbranch_scc1 .Ldin_G0_slp\n"
    "s_branch .Ldin_G0_go\n"
    ".Ldin_G1_wait:\n"
    "s_cmp_lt_i32 s59, s57\n"
    "s_cbranch_scc1 .Ldin_G1_w2\n"
    "s_sub_u32 s94, s67, s65\n"
    "s_cmp_gt_u32 s94, 256\n"
    "s_cbranch_scc1 .Ldin_G1_dma\n"
    "s_cmp_eq_u32 s94, 64\n"
    "s_waitcnt vmcnt(2)\n"
    "s_cbranch_scc1 .Ldin_G1_nd\n"
    "s_waitcnt vmcnt(0)\n"
    "s_branch .Ldin_G1_nd\n"
    ".Ldin_G1_slp:\n"
    "s_sleep 1\n"
    ".Ldin_G1_w2:\n"
    "s_memrealtime s[34:35]\n"
    "s_waitcnt lgkmcnt(0)\n"
    "s_sub_u32 s94, s34, s76\n"
    "s_cmp_lt_i32 s94, 0\n"
    "s_cbranch_scc1 .Ldin_G1_slp\n"
    "s_branch .Ldin_G1_go\n"
    ".Ldin_odd:\n"
    "s_cmp_lt_i32 s58, s57\n"
    "s_cbranch_scc1 .Ldin_A0s\n"
    "s_waitcnt vmcnt(0)\n"
    "s_branch .Ldin_exit\n"
    ".p2align 5\n"
    ".Ldin_y2:\n"
    "s_waitcnt vmcnt(8)\n"
    "v_lshlrev_b32 v56, 16, v8\n"
    "v_lshlrev_b32 v60, 16, v9\n"
    "v_lshlrev_b32 v64, 16, v10\n"
    "v_lshlrev_b32_e64 v68, 16, v11\n"
    "v_bitop3_b32 v56, v56, s84, v7 bitop3:0x6c\n"
    "v_bitop3_b32 v58, v8, s84, v7 bitop3:0x6c\n"
    "v_bitop3_b32 v60, v60, s84, v7 bitop3:0x6c\n"
    "v_bitop3_b32 v62, v9, s84, v7 bitop3:0x6c\n"
    "v_bitop3_b32 v64, v64, s84, v7 bitop3:0x6c\n"
    "v_bitop3_b32 v66, v10, s84, v7 bitop3:0x6c\n"
    "v_bitop3_b32 v68, v68, s84, v7 bitop3:0x6c\n"
    "v_bitop3_b32 v70, v11, s84, v7 bitop3:0x6c\n"
    "s_waitcnt vmcnt(6)\n"
    "buffer_load_dwordx4 v[32:35], v27, s[44:47], s39 offen nt\n"
    "s_add_u32 s39, s39, s17\n"
    "v_lshlrev_b32 v112, 16, v128\n"
    "v_and_b32 v114, s37, v128\n"
    "v_lshlrev_b32 v116, 16, v129\n"
    "v_and_b32 v118, s37, v129\n"
    "v_lshlrev_b32 v120, 16, v130\n"
    "v_and_b32 v122, s37, v130\n"
    "v_lshlrev_b32 v124, 16, v131\n"
    "v_and_b32 v126, s37, v131\n"
    "buffer_load_dwordx4 v[36:39], v27, s[44:47], s39 offen nt\n"
    "s_add_u32 s39, s39, s17\n"
    "v_lshlrev_b32 v113, 16, v132\n"
    "v_and_b32 v115, s37, v132\n"
    "v_lshlrev_b32 v117, 16, v133\n"
    "v_and_b32 v119, s37, v133\n"
    "v_lshlrev_b32 v121, 16, v134\n"
    "v_and_b32 v123, s37, v134\n"
    "v_lshlrev_b32 v125, 16, v135\n"
    "v_and_b32_e64 v127, s37, v135\n"
    "v_pk_mul_f32 v[112:113], v[112:113], v[56:57] op_sel_hi:[1,0]\n"
    "v_pk_mul_f32 v[114:115], v[114:115], v[58:59] op_sel_hi:[1,0]\n"
    "v_pk_mul_f32 v[116:117], v[116:117], v[60:61] op_sel_hi:[1,0]\n"
    "v_pk_mul_f32 v[118:119], v[118:119], v[62:63] op_sel_hi:[1,0]\n"
    "v_pk_mul_f32 v[120:121], v[120:121], v[64:65] op_sel_hi:[1,0]\n"
    "v_pk_mul_f32 v[122:123], v[122:123], v[66:67] op_sel_hi:[1,0]\n"
    "v_pk_mul_f32 v[124:125], v[124:125], v[68:69] op_sel_hi:[1,0]\n"
    "v_pk_mul_f32 v[126:127], v[126:127], v[70:71] op_sel_hi:[1,0]\n"
    "s_waitcnt vmcnt(6)\n"
    "buffer_load_dwordx4 v[40:43], v27, s[44:47], s39 offen nt\n"
    "s_add_u32 s39, s39, s17\n"
    "v_lshlrev_b32 v168, 16, v136\n"
    "v_and_b32 v170, s37, v136\n"
    "v_lshlrev_b32 v172, 16, v137\n"
    "v_and_b32 v174, s37, v137\n"
    "v_lshlrev_b32 v176, 16, v138\n"
    "v_and_b32 v178, s37, v138\n"
    "v_lshlrev_b32 v180, 16, v139\n"
    "v_and_b32 v182, s37, v139\n"
    "buffer_load_dwordx4 v[44:47], v27, s[44:47], s39 offen nt\n"
    "s_add_u32 s39, s39, s17\n"
    "v_lshlrev_b32 v169, 16, v140\n"
    "v_and_b32 v171, s37, v140\n"
    "v_lshlrev_b32 v173, 16, v141\n"
    "v_and_b32 v175, s37, v141\n"
    "v_lshlrev_b32 v177, 16, v142\n"
    "v_and_b32 v179, s37, v142\n"
    "v_lshlrev_b32 v181, 16, v143\n"
    "v_and_b32_e64 v183, s37, v143\n"
    "v_pk_fma_f32 v[168:169], v[168:169], v[56:57], v[112:113] op_sel_hi:[1,0,1]\n"
    "v_pk_fma_f32 v[170:171], v[170:171], v[58:59], v[114:115] op_sel_hi:[1,0,1]\n"
    "ds_write2st64_b64 v12, v[168:169], v[170:171] offset0:0 offset1:8\n"
    "v_pk_fma_f32 v[172:173], v[172:173], v[60:61], v[116:117] op_sel_hi:[1,0,1]\n"
    "v_pk_fma_f32 v[174:175], v[174:175], v[62:63], v[118:119] op_sel_hi:[1,0,1]\n"
    "ds_write2st64_b64 v12, v[172:173], v[174:175] offset0:16 offset1:24\n"
    "v_pk_fma_f32 v[176:177], v[176:177], v[64:65], v[120:121] op_sel_hi:[1,0,1]\n"
    "v_pk_fma_f32 v[178:179], v[178:179], v[66:67], v[122:123] op_sel_hi:[1,0,1]\n"
    "ds_write2st64_b64 v13, v[176:177], v[178:179] offset0:0 offset1:8\n"
    "v_pk_fma_f32 v[180:181], v[180:181], v[68:69], v[124:125] op_sel_hi:[1,0,1]\n"
    "v_pk_fma_f32 v[182:183], v[182:183], v[70:71], v[126:127] op_sel_hi:[1,0,1]\n"
    "ds_write2st64_b64 v13, v[180:181], v[182:183] offset0:16 offset1:24\n"
    "s_waitcnt vmcnt(6)\n"
    "buffer_load_dwordx4 v[48:51], v27, s[44:47], s39 offen nt\n"
    "s_add_u32 s39, s39, s17\n"
    "v_lshlrev_b32 v80, 16, v144\n"
    "v_and_b32 v82, s37, v144\n"
    "v_lshlrev_b32 v84, 16, v145\n"
    "v_and_b32 v86, s37, v145\n"
    "v_lshlrev_b32 v88, 16, v146\n"
    "v_and_b32 v90, s37, v146\n"
    "v_lshlrev_b32 v92, 16, v147\n"
    "v_and_b32 v94, s37, v147\n"
    "buffer_load_dwordx4 v[52:55], v27, s[44:47], s39 offen nt\n"
    "v_lshlrev_b32 v81, 16, v148\n"
    "v_and_b32 v83, s37, v148\n"
    "v_lshlrev_b32 v85, 16, v149\n"
    "v_and_b32 v87, s37, v149\n"
    "v_lshlrev_b32 v89, 16, v150\n"
    "v_and_b32 v91, s37, v150\n"
    "v_lshlrev_b32 v93, 16, v151\n"
    "v_and_b32 v95, s37, v151\n"
    "v_pk_mul_f32 v[80:81], v[80:81], v[56:57] op_sel_hi:[1,0]\n"
    "v_cvt_pk_bf16_f32 v80, v80, v81\n"
    "v_pk_mul_f32 v[82:83], v[82:83], v[58:59] op_sel_hi:[1,0]\n"
    "v_cvt_pk_bf16_f32 v82, v82, v83\n"
    "ds_write2st64_b32 v14, v80, v82 offset0:0 offset1:8\n"
    "v_pk_mul_f32 v[84:85], v[84:85], v[60:61] op_sel_hi:[1,0]\n"
    "v_cvt_pk_bf16_f32 v84, v84, v85\n"
    "v_pk_mul_f32 v[86:87], v[86:87], v[62:63] op_sel_hi:[1,0]\n"
    "v_cvt_pk_bf16_f32 v86, v86, v87\n"
    "ds_write2st64_b32 v14, v84, v86 offset0:16 offset1:24\n"
    "v_pk_mul_f32 v[88:89], v[88:89], v[64:65] op_sel_hi:[1,0]\n"
    "v_cvt_pk_bf16_f32 v88, v88, v89\n"
    "v_pk_mul_f32 v[90:91], v[90:91], v[66:67] op_sel_hi:[1,0]\n"
    "v_cvt_pk_bf16_f32 v90, v90, v91\n"
    "ds_write2st64_b32 v15, v88, v90 offset0:0 offset1:8\n"
    "v_pk_mul_f32 v[92:93], v[92:93], v[68:69] op_sel_hi:[1,0]\n"
    "v_cvt_pk_bf16_f32 v92, v92, v93\n"
    "v_pk_mul_f32 v[94:95], v[94:95], v[70:71] op_sel_hi:[1,0]\n"
    "v_cvt_pk_bf16_f32 v94, v94, v95\n"
    "ds_write2st64_b32 v15, v92, v94 offset0:16 offset1:24\n"
    "s_waitcnt vmcnt(6)\n"
    "v_lshlrev_b32 v184, 16, v152\n"
    "v_and_b32 v186, s37, v152\n"
    "v_lshlrev_b32 v188, 16, v153\n"
    "v_and_b32 v190, s37, v153\n"
    "v_lshlrev_b32 v192, 16, v154\n"
    "v_and_b32 v194, s37, v154\n"
    "v_lshlrev_b32 v196, 16, v155\n"
    "v_and_b32 v198, s37, v155\n"
    "v_lshlrev_b32 v185, 16, v156\n"
    "v_and_b32 v187, s37, v156\n"
    "v_lshlrev_b32 v189, 16, v157\n"
    "v_and_b32 v191, s37, v157\n"
    "v_lshlrev_b32 v193, 16, v158\n"
    "v_and_b32 v195, s37, v158\n"
    "v_lshlrev_b32 v197, 16, v159\n"
    "v_and_b32_e64 v199, s37, v159\n"
    "s_waitcnt lgkmcnt(0)\n"
    "s_barrier\n"
    "ds_read_b128 v[128:131], v97 offset:0\n"
    "ds_read_b128 v[132:135], v97 offset:16\n"
    "ds_read_b128 v[28:31], v99 offset:35200\n"
    "v_pk_fma_f32 v[184:185], v[184:185], v[56:57], v[112:113] op_sel_hi:[1,0,1] neg_lo:[0,0,1] neg_hi:[0,0,1]\n"
    "v_cvt_pk_bf16_f32 v184, v184, v185\n"
    "v_pk_fma_f32 v[186:187], v[186:187], v[58:59], v[114:115] op_sel_hi:[1,0,1] neg_lo:[0,0,1] neg_hi:[0,0,1]\n"
    "v_cvt_pk_bf16_f32 v186, v186, v187\n"
    "ds_write2st64_b32 v14, v184, v186 offset0:32 offset1:40\n"
    "v_pk_fma_f32 v[188:189], v[188:189], v[60:61], v[116:117] op_sel_hi:[1,0,1] neg_lo:[0,0,1] neg_hi:[0,0,1]\n"
    "v_cvt_pk_bf16_f32 v188, v188, v189\n"
    "v_pk_fma_f32 v[190:191], v[190:191], v[62:63], v[118:119] op_sel_hi:[1,0,1] neg_lo:[0,0,1] neg_hi:[0,0,1]\n"
    "v_cvt_pk_bf16_f32 v190, v190, v191\n"
    "ds_write2st64_b32 v14, v188, v190 offset0:48 offset1:56\n"
    "v_pk_fma_f32 v[192:193], v[192:193], v[64:65], v[120:121] op_sel_hi:[1,0,1] neg_lo:[0,0,1] neg_hi:[0,0,1]\n"
    "v_cvt_pk_bf16_f32 v192, v192, v193\n"
    "v_pk_fma_f32 v[194:195], v[194:195], v[66:67], v[122:123] op_sel_hi:[1,0,1] neg_lo:[0,0,1] neg_hi:[0,0,1]\n"
    "v_cvt_pk_bf16_f32 v194, v194, v195\n"
    "ds_write2st64_b32 v15, v192, v194 offset0:32 offset1:40\n"
    "v_pk_fma_f32 v[196:197], v[196:197], v[68:69], v[124:125] op_sel_hi:[1,0,1] neg_lo:[0,0,1] neg_hi:[0,0,1]\n"
    "v_cvt_pk_bf16_f32 v196, v196, v197\n"
    "v_pk_fma_f32 v[198:199], v[198:199], v[70:71], v[126:127] op_sel_hi:[1,0,1] neg_lo:[0,0,1] neg_hi:[0,0,1]\n"
    "v_cvt_pk_bf16_f32 v198, v198, v199\n"
    "ds_write2st64_b32 v15, v196, v198 offset0:48 offset1:56\n"
    "s_mul_i32 s36, s61, s16\n"
    "s_add_u32 s94, s36, s16\n"
    "s_cmp_lt_u32 s61, s14\n"
    "s_cselect_b32 s46, s94, 0\n"
    "buffer_load_dwordx2 v[228:229], v105, s[44:47], s36 offen nt\n"
    "s_lshl_b32 s94, s61, 6\n"
    "s_add_u32 s94, s3, s94\n"
    "s_cmp_lt_u32 s61, s14\n"
    "s_cselect_b32 s58, s94, s13\n"
    "s_add_u32 s95, s61, s15\n"
    "s_add_u32 s36, s36, s17\n"
    "s_lshl_b32 s94, s95, 6\n"
    "s_add_u32 s94, s3, s94\n"
    "s_add_u32 s96, s36, s16\n"
    "s_add_u32 s94, s94, 8\n"
    "s_cmp_lt_u32 s95, s14\n"
    "s_cselect_b32 s46, s96, 0\n"
    "s_cselect_b32 s59, s94, s13\n"
    "buffer_load_short_d16_hi v224, v249, s[44:47], s36 offen offset:0 nt\n"
    "buffer_load_short_d16_hi v225, v249, s[44:47], s36 offen offset:128 nt\n"
    "buffer_load_short_d16_hi v226, v249, s[44:47], s36 offen offset:256 nt\n"
    "buffer_load_short_d16_hi v227, v249, s[44:47], s36 offen offset:384 nt\n"
    "s_mul_i32 s46, s14, s16\n"
    "s_waitcnt lgkmcnt(0)\n"
    "s_barrier\n"
    "ds_read_b128 v[8:11], v98 offset:0\n"
    "ds_read_b128 v[12:15], v98 offset:1024\n"
    "s_waitcnt lgkmcnt(1)\n"
    "v_mfma_f32_16x16x32_bf16 v[112:115], v[8:11], v[128:131], v[28:31]\n"
    "s_waitcnt lgkmcnt(0)\n"
    "v_mfma_f32_16x16x32_bf16 v[112:115], v[12:15], v[132:135], v[112:115]\n"
    "s_nop 7\n"
    "ds_write_b128 v100, v[112:115] offset:17408\n"
    "s_waitcnt lgkmcnt(0)\n"
    "s_barrier\n"
    "s_setprio 0\n"
    "ds_read_b128 v[184:187], v252 offset:0\n"
    "ds_read_b128 v[188:191], v252 offset:16\n"
    "ds_read_b32 v247, v250 offset:35072\n"
    "ds_read_b32 v232, v250 offset:35076\n"
    "s_and_b32 s84, s12, 7\n"
    "v_sub_u32 v4, s16, v26\n"
    "s_lshl_b32 s84, -1, s84\n"
    "v_cmp_gt_i32 vcc, 16, v4\n"
    "s_not_b32 s84, s84\n"
    "v_mov_b32 v6, s84\n"
    "s_add_u32 s85, s66, 480\n"
    "v_cndmask_b32 v6, -1, v6, vcc\n"
    "v_and_b32 v4, 32, v251\n"
    "v_add3_u32 v7, v4, v251, s85\n"
    "v_mov_b32 v5, 0\n"
    "s_mov_b32 s4, 0x10001\n"
    "s_mov_b32 s5, 0xc080c080\n"
    "s_waitcnt lgkmcnt(0)\n"
    "v_readfirstlane_b32 s69, v232\n"
    "v_and_b32 v184, v254, v184\n"
    "v_and_b32 v185, v255, v185\n"
    "v_and_b32 v186, v254, v186\n"
    "v_and_b32 v187, v255, v187\n"
    "v_and_b32 v188, v254, v188\n"
    "v_and_b32 v189, v255, v189\n"
    "v_and_b32 v190, v254, v190\n"
    "v_and_b32 v191, v255, v191\n"
    "s_waitcnt vmcnt(4)\n"
    "v_cmp_ne_u16_e64 s[84:85], 0, v228\n"
    "v_cmp_lt_u32_e64 s[86:87], s38, v228\n"
    "v_cmp_ne_u16_e64 s[88:89], 0, v229\n"
    "v_cmp_lt_u32_e64 s[90:91], s38, v229\n"
    "v_mbcnt_lo_u32_b32 v220, s84, v220\n"
    "s_bcnt1_i32_b64 s92, s[84:85]\n"
    "v_mbcnt_hi_u32_b32 v220, s85, v220\n"
    "s_lshl2_add_u32 s92, s92, s67\n"
    "v_mbcnt_lo_u32_b32 v220, s86, v220\n"
    "s_bcnt1_i32_b64 s93, s[86:87]\n"
    "v_mbcnt_hi_u32_b32 v220, s87, v220\n"
    "s_lshl2_add_u32 s93, s93, s92\n"
    "v_mbcnt_lo_u32_b32 v220, s88, v220\n"
    "s_bcnt1_i32_b64 s94, s[88:89]\n"
    "v_mbcnt_hi_u32_b32 v220, s89, v220\n"
    "s_lshl2_add_u32 s94, s94, s93\n"
    "v_mbcnt_lo_u32_b32 v220, s90, v220\n"
    "s_bcnt1_i32_b64 s95, s[90:91]\n"
    "v_mbcnt_hi_u32_b32 v220, s91, v220\n"
    "s_lshl2_add_u32 s95, s95, s94\n"
    "v_mov_b32_e64 v21, -1\n"
    "v_lshl_add_u32 v20, v249, 1, s95\n"
    "v_addc_co_u32_e64 v221, vcc, v220, 0, s[84:85]\n"
    "v_addc_co_u32_e64 v222, vcc, v221, 0, s[86:87]\n"
    "v_addc_co_u32_e64 v223, vcc, v222, 0, s[88:89]\n"
    "ds_write2_b32 v20, v21, v21 offset1:64\n"
    "v_lshlrev_b32 v220, 2, v220\n"
    "v_lshlrev_b32 v221, 2, v221\n"
    "v_lshlrev_b32 v222, 2, v222\n"
    "v_lshlrev_b32_e64 v223, 2, v223\n"
    "s_mov_b64 exec, s[84:85]\n"
    "ds_write_b32 v220, v216\n"
    "s_mov_b64 exec, s[86:87]\n"
    "ds_write_b32 v221, v217\n"
    "s_mov_b64 exec, s[88:89]\n"
    "ds_write_b32 v222, v218\n"
    "s_mov_b64 exec, s[90:91]\n"
    "ds_write_b32 v223, v219\n"
    "s_mov_b64 exec, -1\n"
    "ds_read_b32 v200, v253 offset:0\n"
    "ds_read_b32 v202, v253 offset:4\n"
    "ds_read_b32 v204, v253 offset:64\n"
    "ds_read_b32 v206, v253 offset:68\n"
    "ds_read_b32 v208, v253 offset:128\n"
    "ds_read_b32 v210, v253 offset:132\n"
    "ds_read_b32 v212, v253 offset:192\n"
    "ds_read_b32 v214, v253 offset:196\n"
    "s_add_u32 m0, s68, 0\n"
    "s_waitcnt lgkmcnt(6)\n"
    "buffer_load_dwordx4 v[200:201], s[40:43], 0 idxen offen offset:0 nt lds\n"
    "buffer_load_dwordx4 v[202:203], s[40:43], 0 idxen offen offset:1024 nt lds\n"
    "s_add_u32 s95, s95, 60\n"
    "s_add_u32 s94, s67, 384\n"
    "s_andn2_b32 s95, s95, 63\n"
    "s_max_u32 s80, s95, s94\n"
    "s_add_u32 m0, s68, 2048\n"
    "s_waitcnt lgkmcnt(0)\n"
    "buffer_load_dwordx4 v[204:205], s[40:43], 0 idxen offen offset:0 nt lds\n"
    "s_mov_b32 s94, 0x10001\n"
    "s_mov_b32 s95, 0xc080c080\n"
    "v_pk_min_u16 v22, v228, s94\n"
    "v_pk_min_u16 v23, v229, s94\n"
    "v_pk_mad_u16 v228, v22, s95, v228\n"
    "v_pk_mad_u16 v229, v23, s95, v229\n"
    "s_sub_u32 s87, s80, s81\n"
    "v_or_b32 v228, v228, v229\n"
    "s_lshr_b32 s87, s87, 6\n"
    "s_lshl2_add_u32 s86, s3, 68224\n"
    "v_mov_b32 v106, s86\n"
    "v_mov_b32 v108, s66\n"
    "v_mov_b32 v109, s58\n"
    "v_mov_b32 v110, s59\n"
    "s_waitcnt vmcnt(3)\n"
    "v_cmp_neq_f32_e64 s[20:21], 0, v224\n"
    "v_cmp_neq_f32_e64 s[22:23], 0, v225\n"
    "v_cmp_neq_f32_e64 s[24:25], 0, v226\n"
    "v_cmp_neq_f32_e64 s[26:27], 0, v227\n"
    "v_fma_f32 v224, v224, v224, -v224\n"
    "v_fma_f32 v225, v225, v225, -v225\n"
    "v_fma_f32 v226, v226, v226, -v226\n"
    "v_fma_f32 v227, v227, v227, -v227\n"
    "s_bcnt1_i32_b64 s88, s[20:21]\n"
    "s_bcnt1_i32_b64 s89, s[22:23]\n"
    "buffer_load_dwordx4 v[206:207], s[40:43], 0 idxen offen offset:1024 nt lds\n"
    "s_bcnt1_i32_b64 s90, s[24:25]\n"
    "s_bcnt1_i32_b64 s91, s[26:27]\n"
    "v_or3_b32 v228, v228, v224, v225\n"
    "s_add_u32 s88, s88, s89\n"
    "v_or3_b32 v228, v228, v226, v227\n"
    "s_add_u32 s90, s90, s91\n"
    "v_cmp_ne_u32_e64 s[84:85], 0, v228\n"
    "s_add_u32 s88, s88, s90\n"
    "s_pack_ll_b32_b16 s88, s87, s88\n"
    "s_mov_b64 exec, 1\n"
    "v_mov_b32_e64 v107, s88\n"
    "ds_write2_b32 v108, v109, v110 offset1:2\n"
    "s_cmp_lg_u64 s[84:85], 0\n"
    "ds_write_b32 v106, v107\n"
    "s_cselect_b32 s91, 2.0, 0\n"
    "s_mov_b64 exec, -1\n"
    "s_or_b32 s58, s58, s91\n"
    "s_or_b32 s59, s59, s91\n"
    "v_pk_min_u16 v0, v32, s4\n"
    "v_pk_min_u16 v1, v33, s4\n"
    "v_pk_min_u16 v2, v34, s4\n"
    "v_pk_min_u16 v3, v35, s4\n"
    "v_pk_mad_u16 v32, v0, s5, v32\n"
    "v_pk_mad_u16 v33, v1, s5, v33\n"
    "s_add_u32 m0, s68, 4096\n"
    "s_nop 0\n"
    "buffer_load_dwordx4 v[208:209], s[40:43], 0 idxen offen offset:0 nt lds\n"
    "v_pk_mad_u16 v34, v2, s5, v34\n"
    "v_pk_mad_u16 v35, v3, s5, v35\n"
    "v_lshl_or_b32 v0, v1, 2, v0\n"
    "v_lshl_or_b32 v0, v2, 4, v0\n"
    "v_lshl_or_b32 v0, v3, 6, v0\n"
    "v_or3_b32 v5, v5, v32, v33\n"
    "v_lshrrev_b32 v1, 15, v0\n"
    "v_or3_b32 v5, v5, v34, v35\n"
    "v_bitop3_b32 v16, v1, v6, v0 bitop3:0xc8\n"
    "ds_write_b8 v7, v16 offset:0\n"
    "v_pk_min_u16 v0, v36, s4\n"
    "v_pk_min_u16 v1, v37, s4\n"
    "v_pk_min_u16 v2, v38, s4\n"
    "v_pk_min_u16 v3, v39, s4\n"
    "v_pk_mad_u16 v36, v0, s5, v36\n"
    "v_pk_mad_u16 v37, v1, s5, v37\n"
    "v_pk_mad_u16 v38, v2, s5, v38\n"
    "v_pk_mad_u16 v39, v3, s5, v39\n"
    "v_lshl_or_b32 v0, v1, 2, v0\n"
    "v_lshl_or_b32 v0, v2, 4, v0\n"
    "v_lshl_or_b32 v0, v3, 6, v0\n"
    "v_or3_b32 v5, v5, v36, v37\n"
    "v_lshrrev_b32_e64 v1, 15, v0\n"
    "v_or3_b32 v5, v5, v38, v39\n"
    "v_bitop3_b32 v17, v1, v6, v0 bitop3:0xc8\n"
    "buffer_load_dwordx4 v[210:211], s[40:43], 0 idxen offen offset:1024 nt lds\n"
    "ds_write_b8 v7, v17 offset:512\n"
    "v_pk_min_u16 v0, v40, s4\n"
    "v_pk_min_u16 v1, v41, s4\n"
    "v_pk_min_u16 v2, v42, s4\n"
    "v_pk_min_u16 v3, v43, s4\n"
    "v_pk_mad_u16 v40, v0, s5, v40\n"
    "v_pk_mad_u16 v41, v1, s5, v41\n"
    "v_pk_mad_u16 v42, v2, s5, v42\n"
    "v_pk_mad_u16 v43, v3, s5, v43\n"
    "v_lshl_or_b32 v0, v1, 2, v0\n"
    "v_lshl_or_b32 v0, v2, 4, v0\n"
    "v_lshl_or_b32 v0, v3, 6, v0\n"
    "v_or3_b32 v5, v5, v40, v41\n"
    "v_lshrrev_b32_e64 v1, 15, v0\n"
    "v_or3_b32 v5, v5, v42, v43\n"
    "v_bitop3_b32 v18, v1, v6, v0 bitop3:0xc8\n"
    "ds_write_b8 v7, v18 offset:1024\n"
    "v_pk_min_u16 v0, v44, s4\n"
    "v_pk_min_u16 v1, v45, s4\n"
    "v_pk_min_u16 v2, v46, s4\n"
    "v_pk_min_u16 v3, v47, s4\n"
    "v_pk_mad_u16 v44, v0, s5, v44\n"
    "v_pk_mad_u16 v45, v1, s5, v45\n"
    "v_pk_mad_u16 v46, v2, s5, v46\n"
    "v_pk_mad_u16 v47, v3, s5, v47\n"
    "s_add_u32 m0, s68, 6144\n"
    "s_nop 0\n"
    "buffer_load_dwordx4 v[212:213], s[40:43], 0 idxen offen offset:0 nt lds\n"
    "v_lshl_or_b32 v0, v1, 2, v0\n"
    "v_lshl_or_b32 v0, v2, 4, v0\n"
    "v_lshl_or_b32 v0, v3, 6, v0\n"
    "v_or3_b32 v5, v5, v44, v45\n"
    "v_lshrrev_b32 v1, 15, v0\n"
    "v_or3_b32 v5, v5, v46, v47\n"
    "v_bitop3_b32 v19, v1, v6, v0 bitop3:0xc8\n"
    "ds_write_b8 v7, v19 offset:1536\n"
    "v_pk_min_u16 v0, v48, s4\n"
    "v_pk_min_u16 v1, v49, s4\n"
    "v_pk_min_u16 v2, v50, s4\n"
    "v_pk_min_u16 v3, v51, s4\n"
    "v_pk_mad_u16 v48, v0, s5, v48\n"
    "v_pk_mad_u16 v49, v1, s5, v49\n"
    "v_pk_mad_u16 v50, v2, s5, v50\n"
    "v_pk_mad_u16 v51, v3, s5, v51\n"
    "v_lshl_or_b32 v0, v1, 2, v0\n"
    "v_lshl_or_b32 v0, v2, 4, v0\n"
    "v_lshl_or_b32 v0, v3, 6, v0\n"
    "v_or3_b32 v5, v5, v48, v49\n"
    "v_lshrrev_b32_e64 v1, 15, v0\n"
    "v_or3_b32 v5, v5, v50, v51\n"
    "v_bitop3_b32 v16, v1, v6, v0 bitop3:0xc8\n"
    "ds_write_b8 v7, v16 offset:2048\n"
    "v_pk_min_u16 v0, v52, s4\n"
    "buffer_load_dwordx4 v[214:215], s[40:43], 0 idxen offen offset:1024 nt lds\n"
    "v_pk_min_u16 v1, v53, s4\n"
    "v_pk_min_u16 v2, v54, s4\n"
    "v_pk_min_u16 v3, v55, s4\n"
    "v_pk_mad_u16 v52, v0, s5, v52\n"
    "v_pk_mad_u16 v53, v1, s5, v53\n"
    "v_pk_mad_u16 v54, v2, s5, v54\n"
    "v_pk_mad_u16 v55, v3, s5, v55\n"
    "v_lshl_or_b32 v0, v1, 2, v0\n"
    "v_lshl_or_b32 v0, v2, 4, v0\n"
    "v_lshl_or_b32 v0, v3, 6, v0\n"
    "v_or3_b32 v5, v5, v52, v53\n"
    "v_lshrrev_b32_e64 v1, 15, v0\n"
    "v_or3_b32 v5, v5, v54, v55\n"
    "v_bitop3_b32 v17, v1, v6, v0 bitop3:0xc8\n"
    "ds_write_b8 v7, v17 offset:2560\n"
    "ds_read_b128 v[128:131], v97 offset:4352\n"
    "ds_read_b128 v[132:135], v97 offset:4368\n"
    "ds_read_b128 v[144:147], v97 offset:8704\n"
    "ds_read_b128 v[148:151], v97 offset:8720\n"
    "ds_read_b128 v[160:163], v97 offset:13056\n"
    "ds_read_b128 v[164:167], v97 offset:13072\n"
    "s_waitcnt lgkmcnt(4)\n"
    "v_mfma_f32_16x16x32_bf16 v[116:119], v[8:11], v[128:131], v[28:31]\n"
    "v_mfma_f32_16x16x32_bf16 v[116:119], v[12:15], v[132:135], v[116:119]\n"
    "s_waitcnt lgkmcnt(2)\n"
    "v_mfma_f32_16x16x32_bf16 v[120:123], v[8:11], v[144:147], v[28:31]\n"
    "v_mfma_f32_16x16x32_bf16 v[120:123], v[12:15], v[148:151], v[120:123]\n"
    "s_waitcnt lgkmcnt(0)\n"
    "v_mfma_f32_16x16x32_bf16 v[124:127], v[8:11], v[160:163], v[28:31]\n"
    "v_mfma_f32_16x16x32_bf16 v[124:127], v[12:15], v[164:167], v[124:127]\n"
    "s_nop 7\n"
    "ds_write_b128 v100, v[116:119] offset:21760\n"
    "ds_write_b128 v100, v[120:123] offset:26112\n"
    "ds_write_b128 v100, v[124:127] offset:30464\n"
    "v_cmp_ne_u32 vcc, 0, v5\n"
    "s_cmp_lg_u64 vcc, 0\n"
    "s_cselect_b32 s94, 0x101, 1\n"
    "s_mov_b64 exec, 1\n"
    "v_mov_b32 v10, s94\n"
    "ds_add_rtn_u32 v10, v250, v10 offset:35840\n"
    "s_mov_b64 exec, -1\n"
    "s_waitcnt lgkmcnt(0)\n"
    "v_readfirstlane_b32 s95, v10\n"
    "s_add_u32 s96, s95, s94\n"
    "s_bitcmp1_b32 s96, 2\n"
    "s_cbranch_scc1 .Ldin_sched\n"
    ".Ldin_ygate:\n"
    ".Ldin_yentry:\n"
    "s_mov_b64 vcc, -1\n"
    "s_branch .Ldin_entry\n"
    ".Ldin_swait:\n"
    "s_sleep 1\n"
    "ds_read_b32 v10, v250 offset:35840\n"
    "s_waitcnt lgkmcnt(0)\n"
    "v_readfirstlane_b32 s96, v10\n"
    ".Ldin_sched:\n"
    "s_setprio 1\n"
    "v_lshrrev_b32 v2, 3, v251\n"
    "v_and_b32 v3, 7, v251\n"
    "v_lshlrev_b32 v6, 3, v3\n"
    "v_add_u32 v7, 16, v251\n"
    "s_mov_b32 s84, 0x10b80\n"
    "v_lshl_add_u32 v12, v7, 6, s84\n"
    "ds_read_b128 v[32:35], v12\n"
    "ds_read_b128 v[36:39], v12 offset:16\n"
    "s_mov_b32 s85, 0x10a80\n"
    "v_lshl_add_u32 v4, v2, 2, s85\n"
    "v_lshl_add_u32 v5, v3, 2, s85\n"
    "ds_read_u16 v17, v4 offset:2\n"
    "ds_read_u16 v4, v4\n"
    "ds_read_u16 v16, v5 offset:2\n"
    "ds_read_u16 v5, v5\n"
    "s_lshl_b32 s85, s2, 3\n"
    "s_lshl1_add_u32 s85, s15, s85\n"
    "v_mad_u32_u24 v8, v2, s15, v3\n"
    "v_add_u32 v8, s85, v8\n"
    "s_cmp_ge_u32 s96, 256\n"
    "v_cmp_gt_u32_e64 s[86:87], s14, v8\n"
    "s_cselect_b32 s85, 2.0, 0\n"
    "v_lshlrev_b32 v8, 6, v8\n"
    "s_and_b32 s87, s87, 0xffff\n"
    "v_or3_b32 v8, v8, v7, s85\n"
    "v_mov_b32 v12, s13\n"
    "v_cndmask_b32_e64 v8, v12, v8, s[86:87]\n"
    "v_cmp_eq_u32 vcc, s3, v2\n"
    "v_lshrrev_b32 v14, 2, v2\n"
    "v_mul_u32_u24 v14, 2, v14\n"
    "v_addc_co_u32_e64 v14, s[88:89], v14, 0, vcc\n"
    "v_addc_co_u32_e64 v14, s[88:89], v14, 0, vcc\n"
    "v_cmp_eq_u32 vcc, s3, v3\n"
    "v_lshrrev_b32_e64 v15, 2, v3\n"
    "v_mul_u32_u24 v15, 2, v15\n"
    "v_addc_co_u32_e64 v15, s[88:89], v15, 0, vcc\n"
    "v_addc_co_u32_e64 v15, s[88:89], v15, 0, vcc\n"
    "s_waitcnt lgkmcnt(4)\n"
    "v_bcnt_u32_b32 v9, v32, 15\n"
    "v_bcnt_u32_b32 v9, v33, v9\n"
    "v_bcnt_u32_b32 v9, v34, v9\n"
    "v_bcnt_u32_b32 v9, v35, v9\n"
    "v_bcnt_u32_b32 v9, v36, v9\n"
    "v_bcnt_u32_b32 v9, v37, v9\n"
    "v_bcnt_u32_b32 v9, v38, v9\n"
    "v_bcnt_u32_b32 v9, v39, v9\n"
    "v_lshrrev_b32 v9, 4, v9\n"
    "v_max_u32 v9, 6, v9\n"
    "v_lshlrev_b32 v9, 3, v9\n"
    "v_cndmask_b32_e64 v9, 0, v9, s[86:87]\n"
    "v_sub_u32 v10, v3, v9\n"
    "s_waitcnt lgkmcnt(0)\n"
    "v_add_u32 v16, 15, v16\n"
    "v_lshrrev_b32 v16, 4, v16\n"
    "v_max_u32 v16, 6, v16\n"
    "v_add_u32 v17, 15, v17\n"
    "v_lshrrev_b32 v17, 4, v17\n"
    "v_max_u32 v17, 6, v17\n"
    "v_add3_u32 v4, v4, v14, v17\n"
    "v_add3_u32 v5, v5, v15, v16\n"
    "v_mov_b32_dpp v12, v10 row_half_mirror row_mask:0xf bank_mask:0xf\n"
    "v_lshrrev_b32 v14, 2, v2\n"
    "v_mul_u32_u24 v14, 3, v14\n"
    "v_xor_b32 v14, v14, v2\n"
    "v_lshl_or_b32 v4, v4, 3, v14\n"
    "v_lshrrev_b32_e64 v14, 2, v3\n"
    "v_mul_u32_u24 v14, 3, v14\n"
    "v_xor_b32 v14, v14, v3\n"
    "v_lshl_or_b32 v5, v5, 3, v14\n"
    "v_sub_u32_dpp v32, v10, v10 quad_perm:[1,0,3,2] row_mask:0xf bank_mask:0xf\n"
    "v_sub_u32_dpp v33, v10, v10 quad_perm:[2,3,0,1] row_mask:0xf bank_mask:0xf\n"
    "v_sub_u32_dpp v34, v10, v10 quad_perm:[3,2,1,0] row_mask:0xf bank_mask:0xf\n"
    "v_sub_u32_dpp v35, v12, v10 quad_perm:[0,1,2,3] row_mask:0xf bank_mask:0xf\n"
    "v_sub_u32_dpp v36, v12, v10 quad_perm:[1,0,3,2] row_mask:0xf bank_mask:0xf\n"
    "v_sub_u32_dpp v37, v12, v10 quad_perm:[2,3,0,1] row_mask:0xf bank_mask:0xf\n"
    "v_sub_u32_dpp v38, v12, v10 quad_perm:[3,2,1,0] row_mask:0xf bank_mask:0xf\n"
    "v_lshrrev_b32 v32, 31, v32\n"
    "v_lshrrev_b32 v33, 31, v33\n"
    "v_lshrrev_b32 v34, 31, v34\n"
    "v_lshrrev_b32 v35, 31, v35\n"
    "v_lshrrev_b32 v36, 31, v36\n"
    "v_lshrrev_b32 v37, 31, v37\n"
    "v_lshrrev_b32 v38, 31, v38\n"
    "v_add3_u32 v11, v32, v33, v244\n"
    "v_add3_u32 v11, v34, v35, v11\n"
    "v_add3_u32 v11, v36, v37, v11\n"
    "v_add_u32 v11, v38, v11\n"
    "v_add_u32 v12, s65, v11\n"
    "v_lshl_add_u32 v14, v11, 3, s65\n"
    "ds_write_b8 v12, v9\n"
    "ds_write_b32 v14, v8 offset:64\n"
    "v_mov_b32 v12, s65\n"
    "ds_read_b128 v[40:43], v12 offset:0\n"
    "ds_read_b128 v[44:47], v12 offset:16\n"
    "ds_read_b128 v[48:51], v12 offset:32\n"
    "v_lshlrev_b32 v16, 3, v16\n"
    "v_sub_u32 v72, v5, v16\n"
    "v_cmp_lt_i32 vcc, v5, v4\n"
    "v_mov_b32_e64 v73, v5\n"
    "s_waitcnt lgkmcnt(0)\n"
    "v_lshrrev_b64 v[20:21], v6, vcc\n"
    "v_lshrrev_b64 v[18:19], v244, vcc\n"
    "v_and_b32 v20, 0xff, v20\n"
    "v_and_b32 v18, 0xff, v18\n"
    "v_bcnt_u32_b32 v20, v20, 0\n"
    "v_bcnt_u32_b32 v18, v18, 0\n"
    "v_lshl_add_u32 v20, v20, 3, s65\n"
    "v_lshlrev_b32_e64 v18, 3, v18\n"
    "ds_read_b32 v56, v20 offset:64\n"
    "v_lshrrev_b64 v[22:23], v20, v[40:41]\n"
    "v_lshrrev_b64 v[18:19], v18, v[40:41]\n"
    "v_and_b32 v22, 0xff, v22\n"
    "v_and_b32 v18, 0xff, v18\n"
    "v_add_u32 v5, v5, v22\n"
    "v_add_u32 v4, v4, v18\n"
    "v_cmp_lt_i32 vcc, v5, v4\n"
    "v_mov_b32_e64 v74, v5\n"
    "s_nop 0\n"
    "v_lshrrev_b64 v[20:21], v6, vcc\n"
    "v_lshrrev_b64 v[18:19], v244, vcc\n"
    "v_and_b32 v20, 0xff, v20\n"
    "v_and_b32 v18, 0xff, v18\n"
    "v_bcnt_u32_b32 v20, v20, 0\n"
    "v_bcnt_u32_b32 v18, v18, 0\n"
    "v_lshl_add_u32 v20, v20, 3, s65\n"
    "v_lshlrev_b32_e64 v18, 3, v18\n"
    "ds_read_b32 v58, v20 offset:128\n"
    "v_lshrrev_b64 v[22:23], v20, v[42:43]\n"
    "v_lshrrev_b64 v[18:19], v18, v[42:43]\n"
    "v_and_b32 v22, 0xff, v22\n"
    "v_and_b32 v18, 0xff, v18\n"
    "v_add_u32 v5, v5, v22\n"
    "v_add_u32 v4, v4, v18\n"
    "v_cmp_lt_i32 vcc, v5, v4\n"
    "v_mov_b32_e64 v75, v5\n"
    "s_nop 0\n"
    "v_lshrrev_b64 v[20:21], v6, vcc\n"
    "v_lshrrev_b64 v[18:19], v244, vcc\n"
    "v_and_b32 v20, 0xff, v20\n"
    "v_and_b32 v18, 0xff, v18\n"
    "v_bcnt_u32_b32 v20, v20, 0\n"
    "v_bcnt_u32_b32 v18, v18, 0\n"
    "v_lshl_add_u32 v20, v20, 3, s65\n"
    "v_lshlrev_b32_e64 v18, 3, v18\n"
    "ds_read_b32 v60, v20 offset:192\n"
    "v_lshrrev_b64 v[22:23], v20, v[44:45]\n"
    "v_lshrrev_b64 v[18:19], v18, v[44:45]\n"
    "v_and_b32 v22, 0xff, v22\n"
    "v_and_b32 v18, 0xff, v18\n"
    "v_add_u32 v5, v5, v22\n"
    "v_add_u32 v4, v4, v18\n"
    "v_cmp_lt_i32 vcc, v5, v4\n"
    "v_mov_b32_e64 v76, v5\n"
    "s_nop 0\n"
    "v_lshrrev_b64 v[20:21], v6, vcc\n"
    "v_lshrrev_b64 v[18:19], v244, vcc\n"
    "v_and_b32 v20, 0xff, v20\n"
    "v_and_b32 v18, 0xff, v18\n"
    "v_bcnt_u32_b32 v20, v20, 0\n"
    "v_bcnt_u32_b32 v18, v18, 0\n"
    "v_lshl_add_u32 v20, v20, 3, s65\n"
    "v_lshlrev_b32_e64 v18, 3, v18\n"
    "ds_read_b32 v62, v20 offset:256\n"
    "v_lshrrev_b64 v[22:23], v20, v[46:47]\n"
    "v_lshrrev_b64 v[18:19], v18, v[46:47]\n"
    "v_and_b32 v22, 0xff, v22\n"
    "v_and_b32 v18, 0xff, v18\n"
    "v_add_u32 v5, v5, v22\n"
    "v_add_u32 v4, v4, v18\n"
    "v_cmp_lt_i32 vcc, v5, v4\n"
    "v_mov_b32_e64 v77, v5\n"
    "s_nop 0\n"
    "v_lshrrev_b64 v[20:21], v6, vcc\n"
    "v_lshrrev_b64 v[18:19], v244, vcc\n"
    "v_and_b32 v20, 0xff, v20\n"
    "v_and_b32 v18, 0xff, v18\n"
    "v_bcnt_u32_b32 v20, v20, 0\n"
    "v_bcnt_u32_b32 v18, v18, 0\n"
    "v_lshl_add_u32 v20, v20, 3, s65\n"
    "v_lshlrev_b32_e64 v18, 3, v18\n"
    "ds_read_b32 v64, v20 offset:320\n"
    "v_lshrrev_b64 v[22:23], v20, v[48:49]\n"
    "v_lshrrev_b64 v[18:19], v18, v[48:49]\n"
    "v_and_b32 v22, 0xff, v22\n"
    "v_and_b32 v18, 0xff, v18\n"
    "v_add_u32 v5, v5, v22\n"
    "v_add_u32 v4, v4, v18\n"
    "v_cmp_lt_i32 vcc, v5, v4\n"
    "v_mov_b32_e64 v78, v5\n"
    "s_nop 0\n"
    "v_lshrrev_b64 v[20:21], v6, vcc\n"
    "v_and_b32 v20, 0xff, v20\n"
    "v_bcnt_u32_b32 v20, v20, 0\n"
    "v_lshl_add_u32 v20, v20, 3, s65\n"
    "ds_read_b32 v66, v20 offset:384\n"
    "v_lshrrev_b64 v[22:23], v20, v[50:51]\n"
    "v_and_b32 v22, 0xff, v22\n"
    "v_add_u32 v5, v5, v22\n"
    "s_add_u32 s85, s76, 280\n"
    "s_lshl_b32 s86, s18, 4\n"
    "v_add_u32_dpp v12, v5, v5 quad_perm:[1,0,3,2] row_mask:0xf bank_mask:0xf\n"
    "s_add_u32 s85, s85, s86\n"
    "s_nop 0\n"
    "v_add_u32_dpp v12, v12, v12 quad_perm:[2,3,0,1] row_mask:0xf bank_mask:0xf\n"
    "s_nop 1\n"
    "v_add_u32_dpp v12, v12, v12 row_half_mirror row_mask:0xf bank_mask:0xf\n"
    "v_add_u32 v12, 158, v12\n"
    "v_and_b32 v12, -256, v12\n"
    "v_lshrrev_b32 v12, 3, v12\n"
    "v_sub_u32 v12, v12, v5\n"
    "v_add_u32 v72, v72, v12\n"
    "v_mul_u32_u24 v72, s18, v72\n"
    "v_lshrrev_b32 v72, 3, v72\n"
    "s_add_u32 s85, s85, s19\n"
    "v_add_u32 v57, s85, v72\n"
    "v_add_u32 v73, v73, v12\n"
    "v_mul_u32_u24 v73, s18, v73\n"
    "v_lshrrev_b32 v73, 3, v73\n"
    "s_add_u32 s85, s85, s19\n"
    "v_add_u32 v59, s85, v73\n"
    "v_add_u32 v74, v74, v12\n"
    "v_mul_u32_u24 v74, s18, v74\n"
    "v_lshrrev_b32 v74, 3, v74\n"
    "s_add_u32 s85, s85, s19\n"
    "v_add_u32 v61, s85, v74\n"
    "v_add_u32 v75, v75, v12\n"
    "v_mul_u32_u24 v75, s18, v75\n"
    "v_lshrrev_b32 v75, 3, v75\n"
    "s_add_u32 s85, s85, s19\n"
    "v_add_u32 v63, s85, v75\n"
    "v_add_u32 v76, v76, v12\n"
    "v_mul_u32_u24 v76, s18, v76\n"
    "v_lshrrev_b32 v76, 3, v76\n"
    "s_add_u32 s85, s85, s19\n"
    "v_add_u32 v65, s85, v76\n"
    "v_add_u32 v77, v77, v12\n"
    "v_mul_u32_u24 v77, s18, v77\n"
    "v_lshrrev_b32 v77, 3, v77\n"
    "s_add_u32 s85, s85, s19\n"
    "v_add_u32 v67, s85, v77\n"
    "v_lshl_add_u32 v12, v3, 7, s84\n"
    "s_mov_b64 exec, 0xff\n"
    "s_waitcnt lgkmcnt(0)\n"
    "ds_write_b128 v12, v[56:59] offset:48\n"
    "ds_write_b128 v12, v[60:63] offset:64\n"
    "ds_write_b128 v12, v[64:67] offset:80\n"
    "s_mov_b64 exec, -1\n"
    "s_branch .Ldin_ygate\n"
    ".p2align 5\n"
    ".Ldin_L2:\n"
    "s_setprio 0\n"
    "s_cmp_ge_i32 s59, s57\n"
    "s_cbranch_scc1 .Ldin_E0\n"
    "s_add_u32 s76, s76, s73\n"
    "v_mov_b32 v253, s63\n"
    "v_and_b32 v184, v254, v184\n"
    "v_and_b32 v185, v255, v185\n"
    "v_and_b32 v186, v254, v186\n"
    "v_and_b32 v187, v255, v187\n"
    "v_and_b32 v188, v254, v188\n"
    "v_and_b32 v189, v255, v189\n"
    "v_and_b32 v190, v254, v190\n"
    "v_and_b32_e64 v191, v255, v191\n"
    "ds_read_b64 v[232:233], v253\n"
    "v_mfma_f32_4x4x4_16b_bf16 v[144:147], v[184:185], v[32:33], v[64:67]\n"
    "ds_read_b128 v[112:115], v252 offset:17408\n"
    "v_mfma_f32_4x4x4_16b_bf16 v[148:151], v[186:187], v[34:35], v[68:71]\n"
    "ds_read_b128 v[116:119], v252 offset:17424\n"
    "v_mfma_f32_4x4x4_16b_bf16 v[152:155], v[188:189], v[36:37], v[72:75]\n"
    "ds_read_b128 v[120:123], v252 offset:17536\n"
    "ds_read_b128 v[124:127], v252 offset:17552\n"
    "v_cvt_pk_bf16_f32 v0, v144, v145\n"
    "v_cvt_pk_bf16_f32 v1, v146, v147\n"
    "v_mfma_f32_4x4x4_16b_bf16 v[156:159], v[190:191], v[38:39], v[76:79]\n"
    "v_cvt_pk_bf16_f32 v2, v148, v149\n"
    "v_cvt_pk_bf16_f32 v3, v150, v151\n"
    "v_mfma_f32_4x4x4_16b_bf16 v[144:147], v[184:185], v[40:41], v[80:83]\n"
    "v_cvt_pk_bf16_f32 v4, v152, v153\n"
    "v_cvt_pk_bf16_f32 v5, v154, v155\n"
    "v_mfma_f32_4x4x4_16b_bf16 v[148:151], v[186:187], v[42:43], v[84:87]\n"
    "v_cvt_pk_bf16_f32 v6, v156, v157\n"
    "v_cvt_pk_bf16_f32 v7, v158, v159\n"
    "v_mfma_f32_4x4x4_16b_bf16 v[152:155], v[188:189], v[44:45], v[88:91]\n"
    "v_cvt_pk_bf16_f32 v8, v144, v145\n"
    "v_cvt_pk_bf16_f32 v9, v146, v147\n"
    "v_mfma_f32_4x4x4_16b_bf16 v[156:159], v[190:191], v[46:47], v[92:95]\n"
    "v_cvt_pk_bf16_f32 v10, v148, v149\n"
    "v_cvt_pk_bf16_f32 v11, v150, v151\n"
    "v_mfma_f32_4x4x4_16b_bf16 v[144:147], v[184:185], v[48:49], v[192:195]\n"
    "v_cvt_pk_bf16_f32 v12, v152, v153\n"
    "v_cvt_pk_bf16_f32 v13, v154, v155\n"
    "v_mfma_f32_4x4x4_16b_bf16 v[148:151], v[186:187], v[50:51], v[196:199]\n"
    "v_cvt_pk_bf16_f32 v14, v156, v157\n"
    "v_cvt_pk_bf16_f32 v15, v158, v159\n"
    "v_mfma_f32_4x4x4_16b_bf16 v[152:155], v[188:189], v[52:53], v[200:203]\n"
    "s_waitcnt lgkmcnt(0)\n"
    "v_readfirstlane_b32 s60, v232\n"
    "v_cvt_pk_bf16_f32 v16, v144, v145\n"
    "v_cvt_pk_bf16_f32 v17, v146, v147\n"
    "v_mfma_f32_4x4x4_16b_bf16 v[156:159], v[190:191], v[54:55], v[204:207]\n"
    "s_cmp_eq_u32 s60, -1\n"
    "s_cbranch_scc1 .Ldin_L2_poll\n"
    ".Ldin_L2_pr:\n"
    "v_readfirstlane_b32 s95, v233\n"
    "s_and_b32 s94, s60, 63\n"
    "s_lshl_b32 s94, s94, 6\n"
    "s_add_u32 s94, s94, 68480\n"
    "s_add_u32 s63, s63, 8\n"
    "v_lshl_add_u32 v252, v249, 1, s94\n"
    "ds_read_b32 v252, v252\n"
    "s_sub_u32 s95, s95, s76\n"
    "s_ashr_i32 s95, s95, 4\n"
    "s_min_i32 s95, s95, s74\n"
    "s_max_i32 s72, s95, s75\n"
    "v_cvt_pk_bf16_f32 v18, v148, v149\n"
    "v_cvt_pk_bf16_f32 v19, v150, v151\n"
    "v_mfma_f32_4x4x4_16b_bf16 v[144:147], v[184:185], v[56:57], v[208:211]\n"
    "v_cvt_pk_bf16_f32 v20, v152, v153\n"
    "v_cvt_pk_bf16_f32 v21, v154, v155\n"
    "v_mfma_f32_4x4x4_16b_bf16 v[148:151], v[186:187], v[58:59], v[212:215]\n"
    "v_cvt_pk_bf16_f32 v22, v156, v157\n"
    "v_cvt_pk_bf16_f32 v23, v158, v159\n"
    "v_mfma_f32_4x4x4_16b_bf16 v[152:155], v[188:189], v[60:61], v[216:219]\n"
    "v_cvt_pk_bf16_f32 v24, v144, v145\n"
    "v_cvt_pk_bf16_f32 v25, v146, v147\n"
    "v_mfma_f32_4x4x4_16b_bf16 v[156:159], v[190:191], v[62:63], v[220:223]\n"
    "v_cvt_pk_bf16_f32 v26, v148, v149\n"
    "v_cvt_pk_bf16_f32 v27, v150, v151\n"
    "v_cvt_pk_bf16_f32 v28, v152, v153\n"
    "v_cvt_pk_bf16_f32 v29, v154, v155\n"
    "v_exp_f32 v136, v136 clamp\n"
    "v_cvt_pk_bf16_f32 v30, v156, v157\n"
    "v_cvt_pk_bf16_f32 v31, v158, v159\n"
    "v_mfma_f32_16x16x32_bf16 v[144:147], v[0:3], v[160:163], v[112:115]\n"
    "v_exp_f32 v128, v128 clamp\n"
    "v_mfma_f32_16x16x32_bf16 v[144:147], v[4:7], v[164:167], v[144:147]\n"
    "v_exp_f32 v129, v129 clamp\n"
    "v_mfma_f32_16x16x32_bf16 v[148:151], v[8:11], v[160:163], v[116:119]\n"
    "v_exp_f32 v130, v130 clamp\n"
    "v_mfma_f32_16x16x32_bf16 v[148:151], v[12:15], v[164:167], v[148:151]\n"
    "v_exp_f32 v131, v131 clamp\n"
    "v_mfma_f32_16x16x32_bf16 v[152:155], v[16:19], v[160:163], v[120:123]\n"
    "v_exp_f32 v132, v132 clamp\n"
    "v_mfma_f32_16x16x32_bf16 v[152:155], v[20:23], v[164:167], v[152:155]\n"
    "v_exp_f32 v133, v133 clamp\n"
    "v_mfma_f32_16x16x32_bf16 v[156:159], v[24:27], v[160:163], v[124:127]\n"
    "v_exp_f32 v134, v134 clamp\n"
    "v_mfma_f32_16x16x32_bf16 v[156:159], v[28:31], v[164:167], v[156:159]\n"
    "v_exp_f32 v135, v135 clamp\n"
    "s_add_u32 m0, s68, 6144\n"
    "v_exp_f32 v137, v137 clamp\n"
    "v_add_u32 v253, s67, v244\n"
    "v_mfma_f32_4x4x4_16b_bf16 v[224:227], v[228:229], v[176:177], v[224:227] cbsz:4 abid:0\n"
    "v_exp_f32 v138, v138 clamp\n"
    "v_exp_f32 v139, v139 clamp\n"
    "v_mfma_f32_4x4x4_16b_bf16 v[224:227], v[228:229], v[178:179], v[224:227] cbsz:4 abid:1\n"
    "v_exp_f32 v140, v140 clamp\n"
    "v_exp_f32 v141, v141 clamp\n"
    "v_mfma_f32_4x4x4_16b_bf16 v[224:227], v[228:229], v[180:181], v[224:227] cbsz:4 abid:2\n"
    "v_exp_f32 v142, v142 clamp\n"
    "v_exp_f32 v143, v143 clamp\n"
    "v_mfma_f32_4x4x4_16b_bf16 v[224:227], v[228:229], v[182:183], v[224:227] cbsz:4 abid:3\n"
    "s_waitcnt vmcnt(4)\n"
    "ds_read_b64_tr_b16 v[176:177], v240 offset:6144\n"
    "ds_read_b64_tr_b16 v[178:179], v241 offset:6400\n"
    "ds_read_b64_tr_b16 v[180:181], v242 offset:6656\n"
    "ds_read_b64_tr_b16 v[182:183], v243 offset:6912\n"
    "ds_read_b32 v236, v253 offset:0\n"
    "ds_read_b32 v238, v253 offset:4\n"
    "ds_read_b128 v[160:163], v234 offset:0\n"
    "ds_read_b128 v[164:167], v235 offset:0\n"
    "s_waitcnt lgkmcnt(8)\n"
    "s_nop 0\n"
    "v_readlane_b32 s20, v252, 0\n"
    "v_readlane_b32 s21, v252, 1\n"
    "v_readlane_b32 s22, v252, 2\n"
    "v_readlane_b32 s23, v252, 3\n"
    "v_readlane_b32 s24, v252, 4\n"
    "v_readlane_b32 s25, v252, 5\n"
    "v_readlane_b32 s26, v252, 6\n"
    "v_readlane_b32 s27, v252, 7\n"
    "v_pk_fma_f32 v[128:129], v[128:129], s[70:71], v[96:97]\n"
    "v_pk_fma_f32 v[130:131], v[130:131], s[70:71], v[98:99]\n"
    "v_pk_add_f32 v[230:231], v[128:129], v[130:131]\n"
    "v_pk_mul_f32 v[232:233], v[128:129], v[130:131]\n"
    "v_pk_fma_f32 v[132:133], v[132:133], s[70:71], v[100:101]\n"
    "v_pk_fma_f32 v[230:231], v[230:231], v[132:133], v[232:233]\n"
    "v_pk_mul_f32 v[232:233], v[232:233], v[132:133]\n"
    "v_pk_fma_f32 v[134:135], v[134:135], s[70:71], v[102:103]\n"
    "v_pk_fma_f32 v[230:231], v[230:231], v[134:135], v[232:233]\n"
    "v_pk_mul_f32 v[232:233], v[232:233], v[134:135]\n"
    "v_pk_fma_f32 v[136:137], v[136:137], s[70:71], v[104:105]\n"
    "v_pk_fma_f32 v[230:231], v[230:231], v[136:137], v[232:233]\n"
    "v_pk_mul_f32 v[232:233], v[232:233], v[136:137]\n"
    "v_pk_fma_f32 v[138:139], v[138:139], s[70:71], v[106:107]\n"
    "v_pk_fma_f32 v[230:231], v[230:231], v[138:139], v[232:233]\n"
    "v_pk_mul_f32 v[232:233], v[232:233], v[138:139]\n"
    "v_pk_fma_f32 v[140:141], v[140:141], s[70:71], v[108:109]\n"
    "v_pk_fma_f32 v[230:231], v[230:231], v[140:141], v[232:233]\n"
    "v_pk_mul_f32 v[232:233], v[232:233], v[140:141]\n"
    "v_pk_fma_f32 v[142:143], v[142:143], s[70:71], v[110:111]\n"
    "v_pk_fma_f32 v[230:231], v[230:231], v[142:143], v[232:233]\n"
    "v_pk_mul_f32 v[232:233], v[232:233], v[142:143]\n"
    "v_rcp_f32 v232, v232\n"
    "v_rcp_f32 v233, v233\n"
    "v_fma_f32 v230, v230, v232, v247\n"
    "v_fmac_f32 v230, v231, v233\n"
    "v_cvt_pk_bf16_f32 v230, v230, v230\n"
    "s_add_u32 s67, s67, 64\n"
    "s_waitcnt lgkmcnt(0)\n"
    "ds_write_b16 v245, v230\n"
    "ds_read_b64 v[228:229], v246\n"
    "s_lshl1_add_u32 s76, s72, s76\n"
    "buffer_load_dwordx4 v[236:237], s[40:43], 0 idxen offen offset:0 nt lds\n"
    "buffer_load_dwordx4 v[238:239], s[40:43], 0 idxen offen offset:1024 nt lds\n"
    "s_and_b32 s62, s58, 63\n"
    "s_mul_i32 s62, s62, 272\n"
    "s_mov_b32 s58, s59\n"
    "s_mov_b32 s59, s60\n"
    "s_mov_b32 s81, s65\n"
    "s_xor_b32 s65, s65, s64\n"
    "s_mov_b32 s80, s83\n"
    "s_add_u32 s82, s65, 256\n"
    "s_cmp_lt_i32 s59, s57\n"
    "s_cselect_b32 s76, s76, -1\n"
    "s_bitcmp1_b32 s58, 30\n"
    "s_cbranch_scc1 .Ldin_A3s\n"
    ".Ldin_A3:\n"
    "v_mfma_f32_16x16x32_bf16 v[128:131], v[0:3], v[160:163], v[112:115]\n"
    "v_exp_f32 v144, v144 clamp\n"
    "v_mfma_f32_16x16x32_bf16 v[128:131], v[4:7], v[164:167], v[128:131]\n"
    "v_exp_f32 v145, v145 clamp\n"
    "v_mfma_f32_16x16x32_bf16 v[132:135], v[8:11], v[160:163], v[116:119]\n"
    "v_exp_f32 v146, v146 clamp\n"
    "v_mfma_f32_16x16x32_bf16 v[132:135], v[12:15], v[164:167], v[132:135]\n"
    "v_exp_f32 v147, v147 clamp\n"
    "v_mfma_f32_16x16x32_bf16 v[136:139], v[16:19], v[160:163], v[120:123]\n"
    "v_exp_f32 v148, v148 clamp\n"
    "v_mfma_f32_16x16x32_bf16 v[136:139], v[20:23], v[164:167], v[136:139]\n"
    "v_exp_f32 v149, v149 clamp\n"
    "v_mfma_f32_16x16x32_bf16 v[140:143], v[24:27], v[160:163], v[124:127]\n"
    "v_exp_f32 v150, v150 clamp\n"
    "v_mfma_f32_16x16x32_bf16 v[140:143], v[28:31], v[164:167], v[140:143]\n"
    "v_exp_f32 v151, v151 clamp\n"
    "v_exp_f32 v152, v152 clamp\n"
    "s_add_u32 m0, s68, 0\n"
    "v_exp_f32 v153, v153 clamp\n"
    "v_add_u32 v253, s67, v244\n"
    "s_waitcnt lgkmcnt(0)\n"
    "v_mfma_f32_4x4x4_16b_bf16 v[224:227], v[228:229], v[168:169], v[224:227] cbsz:4 abid:0\n"
    "v_exp_f32 v154, v154 clamp\n"
    "v_exp_f32 v155, v155 clamp\n"
    "v_mfma_f32_4x4x4_16b_bf16 v[224:227], v[228:229], v[170:171], v[224:227] cbsz:4 abid:1\n"
    "v_exp_f32 v156, v156 clamp\n"
    "v_exp_f32 v157, v157 clamp\n"
    "v_mfma_f32_4x4x4_16b_bf16 v[224:227], v[228:229], v[172:173], v[224:227] cbsz:4 abid:2\n"
    "v_exp_f32 v158, v158 clamp\n"
    "v_exp_f32 v159, v159 clamp\n"
    "v_mfma_f32_4x4x4_16b_bf16 v[224:227], v[228:229], v[174:175], v[224:227] cbsz:4 abid:3\n"
    "s_waitcnt vmcnt(4)\n"
    "ds_read_b64_tr_b16 v[168:169], v240 offset:0\n"
    "ds_read_b64_tr_b16 v[170:171], v241 offset:256\n"
    "ds_read_b64_tr_b16 v[172:173], v242 offset:512\n"
    "ds_read_b64_tr_b16 v[174:175], v243 offset:768\n"
    "ds_read_b32 v236, v253 offset:0\n"
    "ds_read_b32 v238, v253 offset:4\n"
    "ds_read_b128 v[160:163], v234 offset:2048\n"
    "ds_read_b128 v[164:167], v235 offset:2048\n"
    "s_cbranch_vccz .Ldin_A3_np\n"
    "s_setprio 1\n"
    ".Ldin_A3_np:\n"
    "v_pk_fma_f32 v[144:145], v[144:145], s[70:71], v[96:97]\n"
    "v_pk_fma_f32 v[146:147], v[146:147], s[70:71], v[98:99]\n"
    "v_add_f32 v191, v224, v225\n"
    "v_add_f32 v190, v226, v227\n"
    "v_add_f32 v191, v191, v190\n"
    "v_add_u32 v190, s62, v249\n"
    "v_mul_f32 v191, s69, v191\n"
    "v_cvt_pk_bf16_f32 v191, v191, v191\n"
    "ds_write_b16 v190, v191 offset:32768\n"
    "s_bfe_u32 s94, s59, 0x180006\n"
    "s_mul_i32 s86, s94, s56\n"
    "v_mbcnt_lo_u32_b32 v184, s20, 0\n"
    "v_mbcnt_lo_u32_b32 v185, s22, 0\n"
    "v_mbcnt_lo_u32_b32 v186, s24, 0\n"
    "v_mbcnt_lo_u32_b32 v187, s26, 0\n"
    "s_bcnt1_i32_b64 s90, s[20:21]\n"
    "s_bcnt1_i32_b64 s91, s[22:23]\n"
    "s_bcnt1_i32_b64 s92, s[24:25]\n"
    "s_bcnt1_i32_b64 s93, s[26:27]\n"
    "v_mbcnt_hi_u32_b32 v184, s21, v184\n"
    "v_mbcnt_hi_u32_b32 v185, s23, v185\n"
    "v_mbcnt_hi_u32_b32 v186, s25, v186\n"
    "v_mbcnt_hi_u32_b32 v187, s27, v187\n"
    "s_lshl2_add_u32 s90, s90, s65\n"
    "s_lshl2_add_u32 s91, s91, s90\n"
    "s_lshl2_add_u32 s92, s92, s91\n"
    "s_lshl2_add_u32 s93, s93, s92\n"
    "v_add_u32_e64 v188, s86, v251\n"
    "v_pk_add_f32 v[230:231], v[144:145], v[146:147]\n"
    "v_pk_mul_f32 v[232:233], v[144:145], v[146:147]\n"
    "v_pk_fma_f32 v[148:149], v[148:149], s[70:71], v[100:101]\n"
    "v_pk_fma_f32 v[230:231], v[230:231], v[148:149], v[232:233]\n"
    "v_pk_mul_f32 v[232:233], v[232:233], v[148:149]\n"
    "v_lshl_add_u32 v184, v184, 2, s65\n"
    "v_lshl_add_u32 v185, v185, 2, s90\n"
    "v_lshl_add_u32 v186, v186, 2, s91\n"
    "v_lshl_add_u32 v187, v187, 2, s92\n"
    "v_mov_b32 v191, -1\n"
    "v_lshl_add_u32 v190, v249, 1, s93\n"
    "ds_write2_b32 v190, v191, v191 offset1:64\n"
    "s_mov_b64 exec, s[20:21]\n"
    "ds_write_b32 v184, v188\n"
    "s_mov_b64 exec, s[22:23]\n"
    "v_add_u32 v189, 64, v188\n"
    "ds_write_b32 v185, v189\n"
    "s_mov_b64 exec, s[24:25]\n"
    "v_add_u32 v190, 128, v188\n"
    "ds_write_b32 v186, v190\n"
    "s_mov_b64 exec, s[26:27]\n"
    "v_add_u32 v189, 192, v188\n"
    "ds_write_b32 v187, v189\n"
    "s_mov_b64 exec, -1\n"
    "s_add_u32 s95, s93, 60\n"
    "s_add_u32 s94, s82, 128\n"
    "s_andn2_b32 s95, s95, 63\n"
    "s_max_u32 s83, s95, s94\n"
    "v_pk_fma_f32 v[150:151], v[150:151], s[70:71], v[102:103]\n"
    "v_pk_fma_f32 v[230:231], v[230:231], v[150:151], v[232:233]\n"
    "v_pk_mul_f32 v[232:233], v[232:233], v[150:151]\n"
    "v_pk_fma_f32 v[152:153], v[152:153], s[70:71], v[104:105]\n"
    "v_pk_fma_f32 v[230:231], v[230:231], v[152:153], v[232:233]\n"
    "v_pk_mul_f32 v[232:233], v[232:233], v[152:153]\n"
    "v_pk_fma_f32 v[154:155], v[154:155], s[70:71], v[106:107]\n"
    "v_pk_fma_f32 v[230:231], v[230:231], v[154:155], v[232:233]\n"
    "v_pk_mul_f32 v[232:233], v[232:233], v[154:155]\n"
    "v_pk_fma_f32 v[156:157], v[156:157], s[70:71], v[108:109]\n"
    "v_pk_fma_f32 v[230:231], v[230:231], v[156:157], v[232:233]\n"
    "v_pk_mul_f32 v[232:233], v[232:233], v[156:157]\n"
    "v_pk_fma_f32 v[158:159], v[158:159], s[70:71], v[110:111]\n"
    "v_pk_fma_f32 v[230:231], v[230:231], v[158:159], v[232:233]\n"
    "v_pk_mul_f32 v[232:233], v[232:233], v[158:159]\n"
    "v_rcp_f32 v232, v232\n"
    "v_rcp_f32 v233, v233\n"
    "v_fma_f32 v230, v230, v232, v247\n"
    "v_fmac_f32 v230, v231, v233\n"
    "v_cvt_pk_bf16_f32 v230, v230, v230\n"
    "s_add_u32 s67, s67, 64\n"
    "s_waitcnt lgkmcnt(0)\n"
    "ds_write_b16 v245, v230\n"
    "ds_read_b64 v[228:229], v246\n"
    "buffer_load_dwordx4 v[236:237], s[40:43], 0 idxen offen offset:0 nt lds\n"
    "buffer_load_dwordx4 v[238:239], s[40:43], 0 idxen offen offset:1024 nt lds\n"
    ".Ldin_B0:\n"
    "v_mfma_f32_16x16x32_bf16 v[144:147], v[0:3], v[160:163], v[112:115]\n"
    "v_exp_f32 v128, v128 clamp\n"
    "v_mfma_f32_16x16x32_bf16 v[144:147], v[4:7], v[164:167], v[144:147]\n"
    "v_exp_f32 v129, v129 clamp\n"
    "v_mfma_f32_16x16x32_bf16 v[148:151], v[8:11], v[160:163], v[116:119]\n"
    "v_exp_f32 v130, v130 clamp\n"
    "v_mfma_f32_16x16x32_bf16 v[148:151], v[12:15], v[164:167], v[148:151]\n"
    "v_exp_f32 v131, v131 clamp\n"
    "v_mfma_f32_16x16x32_bf16 v[152:155], v[16:19], v[160:163], v[120:123]\n"
    "v_exp_f32 v132, v132 clamp\n"
    "v_mfma_f32_16x16x32_bf16 v[152:155], v[20:23], v[164:167], v[152:155]\n"
    "v_exp_f32 v133, v133 clamp\n"
    "v_mfma_f32_16x16x32_bf16 v[156:159], v[24:27], v[160:163], v[124:127]\n"
    "v_exp_f32 v134, v134 clamp\n"
    "v_mfma_f32_16x16x32_bf16 v[156:159], v[28:31], v[164:167], v[156:159]\n"
    "v_exp_f32 v135, v135 clamp\n"
    "v_exp_f32 v136, v136 clamp\n"
    "s_cmp_eq_u32 s67, s80\n"
    "s_cselect_b32 s67, s65, s67\n"
    "s_and_b32 s94, s59, 63\n"
    "s_mul_i32 s94, s94, 272\n"
    "v_add_u32 v252, s94, v250\n"
    "s_add_u32 m0, s68, 2048\n"
    "v_exp_f32 v137, v137 clamp\n"
    "v_add_u32 v253, s67, v244\n"
    "s_waitcnt lgkmcnt(0)\n"
    "v_mfma_f32_4x4x4_16b_bf16 v[224:227], v[228:229], v[176:177], 0 cbsz:4 abid:0\n"
    "v_exp_f32 v138, v138 clamp\n"
    "v_exp_f32 v139, v139 clamp\n"
    "v_mfma_f32_4x4x4_16b_bf16 v[224:227], v[228:229], v[178:179], v[224:227] cbsz:4 abid:1\n"
    "v_exp_f32 v140, v140 clamp\n"
    "v_exp_f32 v141, v141 clamp\n"
    "v_mfma_f32_4x4x4_16b_bf16 v[224:227], v[228:229], v[180:181], v[224:227] cbsz:4 abid:2\n"
    "v_exp_f32 v142, v142 clamp\n"
    "v_exp_f32 v143, v143 clamp\n"
    "v_mfma_f32_4x4x4_16b_bf16 v[224:227], v[228:229], v[182:183], v[224:227] cbsz:4 abid:3\n"
    "s_waitcnt vmcnt(4)\n"
    "s_memrealtime s[34:35]\n"
    "ds_read_b64_tr_b16 v[176:177], v240 offset:2048\n"
    "ds_read_b64_tr_b16 v[178:179], v241 offset:2304\n"
    "ds_read_b64_tr_b16 v[180:181], v242 offset:2560\n"
    "ds_read_b64_tr_b16 v[182:183], v243 offset:2816\n"
    "ds_read_b32 v236, v253 offset:0\n"
    "ds_read_b32 v238, v253 offset:4\n"
    "ds_read_b128 v[160:163], v234 offset:4096\n"
    "ds_read_b128 v[164:167], v235 offset:4096\n"
    "ds_read_b128 v[184:187], v252 offset:0\n"
    "ds_read_b128 v[188:191], v252 offset:16\n"
    "v_pk_fma_f32 v[128:129], v[128:129], s[70:71], v[96:97]\n"
    "v_pk_fma_f32 v[130:131], v[130:131], s[70:71], v[98:99]\n"
    "v_pk_add_f32 v[230:231], v[128:129], v[130:131]\n"
    "v_pk_mul_f32 v[232:233], v[128:129], v[130:131]\n"
    "v_pk_fma_f32 v[132:133], v[132:133], s[70:71], v[100:101]\n"
    "v_pk_fma_f32 v[230:231], v[230:231], v[132:133], v[232:233]\n"
    "v_pk_mul_f32 v[232:233], v[232:233], v[132:133]\n"
    "v_pk_fma_f32 v[134:135], v[134:135], s[70:71], v[102:103]\n"
    "v_pk_fma_f32 v[230:231], v[230:231], v[134:135], v[232:233]\n"
    "v_pk_mul_f32 v[232:233], v[232:233], v[134:135]\n"
    "v_pk_fma_f32 v[136:137], v[136:137], s[70:71], v[104:105]\n"
    "v_pk_fma_f32 v[230:231], v[230:231], v[136:137], v[232:233]\n"
    "v_pk_mul_f32 v[232:233], v[232:233], v[136:137]\n"
    "v_pk_fma_f32 v[138:139], v[138:139], s[70:71], v[106:107]\n"
    "v_pk_fma_f32 v[230:231], v[230:231], v[138:139], v[232:233]\n"
    "v_pk_mul_f32 v[232:233], v[232:233], v[138:139]\n"
    "v_pk_fma_f32 v[140:141], v[140:141], s[70:71], v[108:109]\n"
    "v_pk_fma_f32 v[230:231], v[230:231], v[140:141], v[232:233]\n"
    "v_pk_mul_f32 v[232:233], v[232:233], v[140:141]\n"
    "v_pk_fma_f32 v[142:143], v[142:143], s[70:71], v[110:111]\n"
    "v_pk_fma_f32 v[230:231], v[230:231], v[142:143], v[232:233]\n"
    "v_pk_mul_f32 v[232:233], v[232:233], v[142:143]\n"
    "v_rcp_f32 v232, v232\n"
    "v_rcp_f32 v233, v233\n"
    "v_fma_f32 v230, v230, v232, v247\n"
    "v_fmac_f32 v230, v231, v233\n"
    "v_cvt_pk_bf16_f32 v230, v230, v230\n"
    "s_add_u32 s67, s67, 64\n"
    "s_waitcnt lgkmcnt(0)\n"
    "ds_write_b16 v245, v230\n"
    "ds_read_b64 v[228:229], v246\n"
    "s_cmp_le_u32 s34, s76\n"
    "s_cbranch_scc1 .Ldin_B0_wait\n"
    ".Ldin_B0_go:\n"
    "s_sub_u32 s94, s34, 60\n"
    "s_max_u32 s76, s76, s94\n"
    "s_add_u32 s76, s76, s72\n"
    ".Ldin_B0_dma:\n"
    "buffer_load_dwordx4 v[236:237], s[40:43], 0 idxen offen offset:0 nt lds\n"
    "buffer_load_dwordx4 v[238:239], s[40:43], 0 idxen offen offset:1024 nt lds\n"
    ".Ldin_B0_nd:\n"
    "s_branch .Ldin_G1\n"
    ".Ldin_L2_poll:\n"
    "s_sleep 1\n"
    "ds_read_b64 v[232:233], v253\n"
    "s_waitcnt lgkmcnt(0)\n"
    "v_readfirstlane_b32 s60, v232\n"
    "s_cmp_eq_u32 s60, -1\n"
    "s_cbranch_scc1 .Ldin_L2_poll\n"
    "s_branch .Ldin_L2_pr\n"
    ".Ldin_B0_wait:\n"
    "s_cmp_lt_i32 s59, s57\n"
    "s_cbranch_scc1 .Ldin_B0_w2\n"
    "s_sub_u32 s94, s67, s65\n"
    "s_cmp_gt_u32 s94, 256\n"
    "s_cbranch_scc1 .Ldin_B0_dma\n"
    "s_cmp_eq_u32 s94, 64\n"
    "s_waitcnt vmcnt(2)\n"
    "s_cbranch_scc1 .Ldin_B0_nd\n"
    "s_waitcnt vmcnt(0)\n"
    "s_branch .Ldin_B0_nd\n"
    ".Ldin_B0_slp:\n"
    "s_sleep 1\n"
    ".Ldin_B0_w2:\n"
    "s_memrealtime s[34:35]\n"
    "s_waitcnt lgkmcnt(0)\n"
    "s_sub_u32 s94, s34, s76\n"
    "s_cmp_lt_i32 s94, 0\n"
    "s_cbranch_scc1 .Ldin_B0_slp\n"
    "s_branch .Ldin_B0_go\n"
    ".p2align 5\n"
    ".Ldin_L1:\n"
    "s_setprio 0\n"
    "s_cmp_ge_i32 s59, s57\n"
    "s_cbranch_scc1 .Ldin_E1\n"
    "s_add_u32 s76, s76, s73\n"
    "v_mov_b32 v253, s63\n"
    "v_and_b32 v184, v254, v184\n"
    "v_and_b32 v185, v255, v185\n"
    "v_and_b32 v186, v254, v186\n"
    "v_and_b32 v187, v255, v187\n"
    "v_and_b32 v188, v254, v188\n"
    "v_and_b32 v189, v255, v189\n"
    "v_and_b32 v190, v254, v190\n"
    "v_and_b32_e64 v191, v255, v191\n"
    "ds_read_b64 v[232:233], v253\n"
    "v_mfma_f32_4x4x4_16b_bf16 v[128:131], v[184:185], v[32:33], v[64:67]\n"
    "ds_read_b128 v[112:115], v252 offset:17408\n"
    "v_mfma_f32_4x4x4_16b_bf16 v[132:135], v[186:187], v[34:35], v[68:71]\n"
    "ds_read_b128 v[116:119], v252 offset:17424\n"
    "v_mfma_f32_4x4x4_16b_bf16 v[136:139], v[188:189], v[36:37], v[72:75]\n"
    "ds_read_b128 v[120:123], v252 offset:17536\n"
    "ds_read_b128 v[124:127], v252 offset:17552\n"
    "v_cvt_pk_bf16_f32 v0, v128, v129\n"
    "v_cvt_pk_bf16_f32 v1, v130, v131\n"
    "v_mfma_f32_4x4x4_16b_bf16 v[140:143], v[190:191], v[38:39], v[76:79]\n"
    "v_cvt_pk_bf16_f32 v2, v132, v133\n"
    "v_cvt_pk_bf16_f32 v3, v134, v135\n"
    "v_mfma_f32_4x4x4_16b_bf16 v[128:131], v[184:185], v[40:41], v[80:83]\n"
    "v_cvt_pk_bf16_f32 v4, v136, v137\n"
    "v_cvt_pk_bf16_f32 v5, v138, v139\n"
    "v_mfma_f32_4x4x4_16b_bf16 v[132:135], v[186:187], v[42:43], v[84:87]\n"
    "v_cvt_pk_bf16_f32 v6, v140, v141\n"
    "v_cvt_pk_bf16_f32 v7, v142, v143\n"
    "v_mfma_f32_4x4x4_16b_bf16 v[136:139], v[188:189], v[44:45], v[88:91]\n"
    "v_cvt_pk_bf16_f32 v8, v128, v129\n"
    "v_cvt_pk_bf16_f32 v9, v130, v131\n"
    "v_mfma_f32_4x4x4_16b_bf16 v[140:143], v[190:191], v[46:47], v[92:95]\n"
    "v_cvt_pk_bf16_f32 v10, v132, v133\n"
    "v_cvt_pk_bf16_f32 v11, v134, v135\n"
    "v_mfma_f32_4x4x4_16b_bf16 v[128:131], v[184:185], v[48:49], v[192:195]\n"
    "v_cvt_pk_bf16_f32 v12, v136, v137\n"
    "v_cvt_pk_bf16_f32 v13, v138, v139\n"
    "v_mfma_f32_4x4x4_16b_bf16 v[132:135], v[186:187], v[50:51], v[196:199]\n"
    "v_cvt_pk_bf16_f32 v14, v140, v141\n"
    "v_cvt_pk_bf16_f32 v15, v142, v143\n"
    "v_mfma_f32_4x4x4_16b_bf16 v[136:139], v[188:189], v[52:53], v[200:203]\n"
    "s_waitcnt lgkmcnt(0)\n"
    "v_readfirstlane_b32 s60, v232\n"
    "v_cvt_pk_bf16_f32 v16, v128, v129\n"
    "v_cvt_pk_bf16_f32 v17, v130, v131\n"
    "v_mfma_f32_4x4x4_16b_bf16 v[140:143], v[190:191], v[54:55], v[204:207]\n"
    "s_cmp_eq_u32 s60, -1\n"
    "s_cbranch_scc1 .Ldin_L1_poll\n"
    ".Ldin_L1_pr:\n"
    "v_readfirstlane_b32 s95, v233\n"
    "s_and_b32 s94, s60, 63\n"
    "s_lshl_b32 s94, s94, 6\n"
    "s_add_u32 s94, s94, 68480\n"
    "s_add_u32 s63, s63, 8\n"
    "v_lshl_add_u32 v252, v249, 1, s94\n"
    "ds_read_b32 v252, v252\n"
    "s_sub_u32 s95, s95, s76\n"
    "s_ashr_i32 s95, s95, 4\n"
    "s_min_i32 s95, s95, s74\n"
    "s_max_i32 s72, s95, s75\n"
    "v_cvt_pk_bf16_f32 v18, v132, v133\n"
    "v_cvt_pk_bf16_f32 v19, v134, v135\n"
    "v_mfma_f32_4x4x4_16b_bf16 v[128:131], v[184:185], v[56:57], v[208:211]\n"
    "v_cvt_pk_bf16_f32 v20, v136, v137\n"
    "v_cvt_pk_bf16_f32 v21, v138, v139\n"
    "v_mfma_f32_4x4x4_16b_bf16 v[132:135], v[186:187], v[58:59], v[212:215]\n"
    "v_cvt_pk_bf16_f32 v22, v140, v141\n"
    "v_cvt_pk_bf16_f32 v23, v142, v143\n"
    "v_mfma_f32_4x4x4_16b_bf16 v[136:139], v[188:189], v[60:61], v[216:219]\n"
    "v_cvt_pk_bf16_f32 v24, v128, v129\n"
    "v_cvt_pk_bf16_f32 v25, v130, v131\n"
    "v_mfma_f32_4x4x4_16b_bf16 v[140:143], v[190:191], v[62:63], v[220:223]\n"
    "v_cvt_pk_bf16_f32 v26, v132, v133\n"
    "v_cvt_pk_bf16_f32 v27, v134, v135\n"
    "v_cvt_pk_bf16_f32 v28, v136, v137\n"
    "v_cvt_pk_bf16_f32 v29, v138, v139\n"
    "v_exp_f32 v152, v152 clamp\n"
    "v_cvt_pk_bf16_f32 v30, v140, v141\n"
    "v_cvt_pk_bf16_f32 v31, v142, v143\n"
    "v_mfma_f32_16x16x32_bf16 v[128:131], v[0:3], v[160:163], v[112:115]\n"
    "v_exp_f32 v144, v144 clamp\n"
    "v_mfma_f32_16x16x32_bf16 v[128:131], v[4:7], v[164:167], v[128:131]\n"
    "v_exp_f32 v145, v145 clamp\n"
    "v_mfma_f32_16x16x32_bf16 v[132:135], v[8:11], v[160:163], v[116:119]\n"
    "v_exp_f32 v146, v146 clamp\n"
    "v_mfma_f32_16x16x32_bf16 v[132:135], v[12:15], v[164:167], v[132:135]\n"
    "v_exp_f32 v147, v147 clamp\n"
    "v_mfma_f32_16x16x32_bf16 v[136:139], v[16:19], v[160:163], v[120:123]\n"
    "v_exp_f32 v148, v148 clamp\n"
    "v_mfma_f32_16x16x32_bf16 v[136:139], v[20:23], v[164:167], v[136:139]\n"
    "v_exp_f32 v149, v149 clamp\n"
    "v_mfma_f32_16x16x32_bf16 v[140:143], v[24:27], v[160:163], v[124:127]\n"
    "v_exp_f32 v150, v150 clamp\n"
    "v_mfma_f32_16x16x32_bf16 v[140:143], v[28:31], v[164:167], v[140:143]\n"
    "v_exp_f32 v151, v151 clamp\n"
    "s_add_u32 m0, s68, 4096\n"
    "v_exp_f32 v153, v153 clamp\n"
    "v_add_u32 v253, s67, v244\n"
    "v_mfma_f32_4x4x4_16b_bf16 v[224:227], v[228:229], v[168:169], v[224:227] cbsz:4 abid:0\n"
    "v_exp_f32 v154, v154 clamp\n"
    "v_exp_f32 v155, v155 clamp\n"
    "v_mfma_f32_4x4x4_16b_bf16 v[224:227], v[228:229], v[170:171], v[224:227] cbsz:4 abid:1\n"
    "v_exp_f32 v156, v156 clamp\n"
    "v_exp_f32 v157, v157 clamp\n"
    "v_mfma_f32_4x4x4_16b_bf16 v[224:227], v[228:229], v[172:173], v[224:227] cbsz:4 abid:2\n"
    "v_exp_f32 v158, v158 clamp\n"
    "v_exp_f32 v159, v159 clamp\n"
    "v_mfma_f32_4x4x4_16b_bf16 v[224:227], v[228:229], v[174:175], v[224:227] cbsz:4 abid:3\n"
    "s_waitcnt vmcnt(4)\n"
    "ds_read_b64_tr_b16 v[168:169], v240 offset:4096\n"
    "ds_read_b64_tr_b16 v[170:171], v241 offset:4352\n"
    "ds_read_b64_tr_b16 v[172:173], v242 offset:4608\n"
    "ds_read_b64_tr_b16 v[174:175], v243 offset:4864\n"
    "ds_read_b32 v236, v253 offset:0\n"
    "ds_read_b32 v238, v253 offset:4\n"
    "ds_read_b128 v[160:163], v234 offset:6144\n"
    "ds_read_b128 v[164:167], v235 offset:6144\n"
    "s_waitcnt lgkmcnt(8)\n"
    "s_nop 0\n"
    "v_readlane_b32 s20, v252, 0\n"
    "v_readlane_b32 s21, v252, 1\n"
    "v_readlane_b32 s22, v252, 2\n"
    "v_readlane_b32 s23, v252, 3\n"
    "v_readlane_b32 s24, v252, 4\n"
    "v_readlane_b32 s25, v252, 5\n"
    "v_readlane_b32 s26, v252, 6\n"
    "v_readlane_b32 s27, v252, 7\n"
    "v_pk_fma_f32 v[144:145], v[144:145], s[70:71], v[96:97]\n"
    "v_pk_fma_f32 v[146:147], v[146:147], s[70:71], v[98:99]\n"
    "v_pk_add_f32 v[230:231], v[144:145], v[146:147]\n"
    "v_pk_mul_f32 v[232:233], v[144:145], v[146:147]\n"
    "v_pk_fma_f32 v[148:149], v[148:149], s[70:71], v[100:101]\n"
    "v_pk_fma_f32 v[230:231], v[230:231], v[148:149], v[232:233]\n"
    "v_pk_mul_f32 v[232:233], v[232:233], v[148:149]\n"
    "v_pk_fma_f32 v[150:151], v[150:151], s[70:71], v[102:103]\n"
    "v_pk_fma_f32 v[230:231], v[230:231], v[150:151], v[232:233]\n"
    "v_pk_mul_f32 v[232:233], v[232:233], v[150:151]\n"
    "v_pk_fma_f32 v[152:153], v[152:153], s[70:71], v[104:105]\n"
    "v_pk_fma_f32 v[230:231], v[230:231], v[152:153], v[232:233]\n"
    "v_pk_mul_f32 v[232:233], v[232:233], v[152:153]\n"
    "v_pk_fma_f32 v[154:155], v[154:155], s[70:71], v[106:107]\n"
    "v_pk_fma_f32 v[230:231], v[230:231], v[154:155], v[232:233]\n"
    "v_pk_mul_f32 v[232:233], v[232:233], v[154:155]\n"
    "v_pk_fma_f32 v[156:157], v[156:157], s[70:71], v[108:109]\n"
    "v_pk_fma_f32 v[230:231], v[230:231], v[156:157], v[232:233]\n"
    "v_pk_mul_f32 v[232:233], v[232:233], v[156:157]\n"
    "v_pk_fma_f32 v[158:159], v[158:159], s[70:71], v[110:111]\n"
    "v_pk_fma_f32 v[230:231], v[230:231], v[158:159], v[232:233]\n"
    "v_pk_mul_f32 v[232:233], v[232:233], v[158:159]\n"
    "v_rcp_f32 v232, v232\n"
    "v_rcp_f32 v233, v233\n"
    "v_fma_f32 v230, v230, v232, v247\n"
    "v_fmac_f32 v230, v231, v233\n"
    "v_cvt_pk_bf16_f32 v230, v230, v230\n"
    "s_add_u32 s67, s67, 64\n"
    "s_waitcnt lgkmcnt(0)\n"
    "ds_write_b16 v245, v230\n"
    "ds_read_b64 v[228:229], v246\n"
    "s_lshl1_add_u32 s76, s72, s76\n"
    "buffer_load_dwordx4 v[236:237], s[40:43], 0 idxen offen offset:0 nt lds\n"
    "buffer_load_dwordx4 v[238:239], s[40:43], 0 idxen offen offset:1024 nt lds\n"
    "s_and_b32 s62, s58, 63\n"
    "s_mul_i32 s62, s62, 272\n"
    "s_mov_b32 s58, s59\n"
    "s_mov_b32 s59, s60\n"
    "s_mov_b32 s81, s65\n"
    "s_xor_b32 s65, s65, s64\n"
    "s_mov_b32 s80, s83\n"
    "s_add_u32 s82, s65, 256\n"
    "s_cmp_lt_i32 s59, s57\n"
    "s_cselect_b32 s76, s76, -1\n"
    "s_bitcmp1_b32 s58, 30\n"
    "s_cbranch_scc1 .Ldin_A2s\n"
    ".Ldin_A2:\n"
    "v_mfma_f32_16x16x32_bf16 v[144:147], v[0:3], v[160:163], v[112:115]\n"
    "v_exp_f32 v128, v128 clamp\n"
    "v_mfma_f32_16x16x32_bf16 v[144:147], v[4:7], v[164:167], v[144:147]\n"
    "v_exp_f32 v129, v129 clamp\n"
    "v_mfma_f32_16x16x32_bf16 v[148:151], v[8:11], v[160:163], v[116:119]\n"
    "v_exp_f32 v130, v130 clamp\n"
    "v_mfma_f32_16x16x32_bf16 v[148:151], v[12:15], v[164:167], v[148:151]\n"
    "v_exp_f32 v131, v131 clamp\n"
    "v_mfma_f32_16x16x32_bf16 v[152:155], v[16:19], v[160:163], v[120:123]\n"
    "v_exp_f32 v132, v132 clamp\n"
    "v_mfma_f32_16x16x32_bf16 v[152:155], v[20:23], v[164:167], v[152:155]\n"
    "v_exp_f32 v133, v133 clamp\n"
    "v_mfma_f32_16x16x32_bf16 v[156:159], v[24:27], v[160:163], v[124:127]\n"
    "v_exp_f32 v134, v134 clamp\n"
    "v_mfma_f32_16x16x32_bf16 v[156:159], v[28:31], v[164:167], v[156:159]\n"
    "v_exp_f32 v135, v135 clamp\n"
    "v_exp_f32 v136, v136 clamp\n"
    "s_add_u32 m0, s68, 6144\n"
    "v_exp_f32 v137, v137 clamp\n"
    "v_add_u32_e64 v253, s67, v244\n"
    "s_waitcnt lgkmcnt(0)\n"
    "v_mfma_f32_4x4x4_16b_bf16 v[224:227], v[228:229], v[176:177], v[224:227] cbsz:4 abid:0\n"
    "v_exp_f32 v138, v138 clamp\n"
    "v_exp_f32 v139, v139 clamp\n"
    "v_mfma_f32_4x4x4_16b_bf16 v[224:227], v[228:229], v[178:179], v[224:227] cbsz:4 abid:1\n"
    "v_exp_f32 v140, v140 clamp\n"
    "v_exp_f32 v141, v141 clamp\n"
    "v_mfma_f32_4x4x4_16b_bf16 v[224:227], v[228:229], v[180:181], v[224:227] cbsz:4 abid:2\n"
    "v_exp_f32 v142, v142 clamp\n"
    "v_exp_f32 v143, v143 clamp\n"
    "v_mfma_f32_4x4x4_16b_bf16 v[224:227], v[228:229], v[182:183], v[224:227] cbsz:4 abid:3\n"
    "s_waitcnt vmcnt(4)\n"
    "ds_read_b64_tr_b16 v[176:177], v240 offset:6144\n"
    "ds_read_b64_tr_b16 v[178:179], v241 offset:6400\n"
    "ds_read_b64_tr_b16 v[180:181], v242 offset:6656\n"
    "ds_read_b64_tr_b16 v[182:183], v243 offset:6912\n"
    "ds_read_b32 v236, v253 offset:0\n"
    "ds_read_b32 v238, v253 offset:4\n"
    "ds_read_b128 v[160:163], v234 offset:0\n"
    "ds_read_b128 v[164:167], v235 offset:0\n"
    "s_cbranch_vccz .Ldin_A2_np\n"
    "s_setprio 1\n"
    ".Ldin_A2_np:\n"
    "v_pk_fma_f32 v[128:129], v[128:129], s[70:71], v[96:97]\n"
    "v_pk_fma_f32 v[130:131], v[130:131], s[70:71], v[98:99]\n"
    "v_add_f32 v191, v224, v225\n"
    "v_add_f32 v190, v226, v227\n"
    "v_add_f32 v191, v191, v190\n"
    "v_add_u32 v190, s62, v249\n"
    "v_mul_f32 v191, s69, v191\n"
    "v_cvt_pk_bf16_f32 v191, v191, v191\n"
    "ds_write_b16 v190, v191 offset:32768\n"
    "s_bfe_u32 s94, s59, 0x180006\n"
    "s_mul_i32 s86, s94, s56\n"
    "v_mbcnt_lo_u32_b32 v184, s20, 0\n"
    "v_mbcnt_lo_u32_b32 v185, s22, 0\n"
    "v_mbcnt_lo_u32_b32 v186, s24, 0\n"
    "v_mbcnt_lo_u32_b32 v187, s26, 0\n"
    "s_bcnt1_i32_b64 s90, s[20:21]\n"
    "s_bcnt1_i32_b64 s91, s[22:23]\n"
    "s_bcnt1_i32_b64 s92, s[24:25]\n"
    "s_bcnt1_i32_b64 s93, s[26:27]\n"
    "v_mbcnt_hi_u32_b32 v184, s21, v184\n"
    "v_mbcnt_hi_u32_b32 v185, s23, v185\n"
    "v_mbcnt_hi_u32_b32 v186, s25, v186\n"
    "v_mbcnt_hi_u32_b32 v187, s27, v187\n"
    "s_lshl2_add_u32 s90, s90, s65\n"
    "s_lshl2_add_u32 s91, s91, s90\n"
    "s_lshl2_add_u32 s92, s92, s91\n"
    "s_lshl2_add_u32 s93, s93, s92\n"
    "v_add_u32_e64 v188, s86, v251\n"
    "v_pk_add_f32 v[230:231], v[128:129], v[130:131]\n"
    "v_pk_mul_f32 v[232:233], v[128:129], v[130:131]\n"
    "v_pk_fma_f32 v[132:133], v[132:133], s[70:71], v[100:101]\n"
    "v_pk_fma_f32 v[230:231], v[230:231], v[132:133], v[232:233]\n"
    "v_pk_mul_f32 v[232:233], v[232:233], v[132:133]\n"
    "v_lshl_add_u32 v184, v184, 2, s65\n"
    "v_lshl_add_u32 v185, v185, 2, s90\n"
    "v_lshl_add_u32 v186, v186, 2, s91\n"
    "v_lshl_add_u32 v187, v187, 2, s92\n"
    "v_mov_b32 v191, -1\n"
    "v_lshl_add_u32 v190, v249, 1, s93\n"
    "ds_write2_b32 v190, v191, v191 offset1:64\n"
    "s_mov_b64 exec, s[20:21]\n"
    "ds_write_b32 v184, v188\n"
    "s_mov_b64 exec, s[22:23]\n"
    "v_add_u32 v189, 64, v188\n"
    "ds_write_b32 v185, v189\n"
    "s_mov_b64 exec, s[24:25]\n"
    "v_add_u32 v190, 128, v188\n"
    "ds_write_b32 v186, v190\n"
    "s_mov_b64 exec, s[26:27]\n"
    "v_add_u32 v189, 192, v188\n"
    "ds_write_b32 v187, v189\n"
    "s_mov_b64 exec, -1\n"
    "s_add_u32 s95, s93, 60\n"
    "s_add_u32 s94, s82, 128\n"
    "s_andn2_b32 s95, s95, 63\n"
    "s_max_u32 s83, s95, s94\n"
    "v_pk_fma_f32 v[134:135], v[134:135], s[70:71], v[102:103]\n"
    "v_pk_fma_f32 v[230:231], v[230:231], v[134:135], v[232:233]\n"
    "v_pk_mul_f32 v[232:233], v[232:233], v[134:135]\n"
    "v_pk_fma_f32 v[136:137], v[136:137], s[70:71], v[104:105]\n"
    "v_pk_fma_f32 v[230:231], v[230:231], v[136:137], v[232:233]\n"
    "v_pk_mul_f32 v[232:233], v[232:233], v[136:137]\n"
    "v_pk_fma_f32 v[138:139], v[138:139], s[70:71], v[106:107]\n"
    "v_pk_fma_f32 v[230:231], v[230:231], v[138:139], v[232:233]\n"
    "v_pk_mul_f32 v[232:233], v[232:233], v[138:139]\n"
    "v_pk_fma_f32 v[140:141], v[140:141], s[70:71], v[108:109]\n"
    "v_pk_fma_f32 v[230:231], v[230:231], v[140:141], v[232:233]\n"
    "v_pk_mul_f32 v[232:233], v[232:233], v[140:141]\n"
    "v_pk_fma_f32 v[142:143], v[142:143], s[70:71], v[110:111]\n"
    "v_pk_fma_f32 v[230:231], v[230:231], v[142:143], v[232:233]\n"
    "v_pk_mul_f32 v[232:233], v[232:233], v[142:143]\n"
    "v_rcp_f32 v232, v232\n"
    "v_rcp_f32 v233, v233\n"
    "v_fma_f32 v230, v230, v232, v247\n"
    "v_fmac_f32 v230, v231, v233\n"
    "v_cvt_pk_bf16_f32 v230, v230, v230\n"
    "s_add_u32 s67, s67, 64\n"
    "s_waitcnt lgkmcnt(0)\n"
    "ds_write_b16 v245, v230\n"
    "ds_read_b64 v[228:229], v246\n"
    "buffer_load_dwordx4 v[236:237], s[40:43], 0 idxen offen offset:0 nt lds\n"
    "buffer_load_dwordx4 v[238:239], s[40:43], 0 idxen offen offset:1024 nt lds\n"
    ".Ldin_B3:\n"
    "v_mfma_f32_16x16x32_bf16 v[128:131], v[0:3], v[160:163], v[112:115]\n"
    "v_exp_f32 v144, v144 clamp\n"
    "v_mfma_f32_16x16x32_bf16 v[128:131], v[4:7], v[164:167], v[128:131]\n"
    "v_exp_f32 v145, v145 clamp\n"
    "v_mfma_f32_16x16x32_bf16 v[132:135], v[8:11], v[160:163], v[116:119]\n"
    "v_exp_f32 v146, v146 clamp\n"
    "v_mfma_f32_16x16x32_bf16 v[132:135], v[12:15], v[164:167], v[132:135]\n"
    "v_exp_f32 v147, v147 clamp\n"
    "v_mfma_f32_16x16x32_bf16 v[136:139], v[16:19], v[160:163], v[120:123]\n"
    "v_exp_f32 v148, v148 clamp\n"
    "v_mfma_f32_16x16x32_bf16 v[136:139], v[20:23], v[164:167], v[136:139]\n"
    "v_exp_f32 v149, v149 clamp\n"
    "v_mfma_f32_16x16x32_bf16 v[140:143], v[24:27], v[160:163], v[124:127]\n"
    "v_exp_f32 v150, v150 clamp\n"
    "v_mfma_f32_16x16x32_bf16 v[140:143], v[28:31], v[164:167], v[140:143]\n"
    "v_exp_f32 v151, v151 clamp\n"
    "v_exp_f32 v152, v152 clamp\n"
    "s_cmp_eq_u32 s67, s80\n"
    "s_cselect_b32 s67, s65, s67\n"
    "s_and_b32 s94, s59, 63\n"
    "s_mul_i32 s94, s94, 272\n"
    "v_add_u32 v252, s94, v250\n"
    "s_add_u32 m0, s68, 0\n"
    "v_exp_f32 v153, v153 clamp\n"
    "v_add_u32_e64 v253, s67, v244\n"
    "s_waitcnt lgkmcnt(0)\n"
    "v_mfma_f32_4x4x4_16b_bf16 v[224:227], v[228:229], v[168:169], 0 cbsz:4 abid:0\n"
    "v_exp_f32 v154, v154 clamp\n"
    "v_exp_f32 v155, v155 clamp\n"
    "v_mfma_f32_4x4x4_16b_bf16 v[224:227], v[228:229], v[170:171], v[224:227] cbsz:4 abid:1\n"
    "v_exp_f32 v156, v156 clamp\n"
    "v_exp_f32 v157, v157 clamp\n"
    "v_mfma_f32_4x4x4_16b_bf16 v[224:227], v[228:229], v[172:173], v[224:227] cbsz:4 abid:2\n"
    "v_exp_f32 v158, v158 clamp\n"
    "v_exp_f32 v159, v159 clamp\n"
    "v_mfma_f32_4x4x4_16b_bf16 v[224:227], v[228:229], v[174:175], v[224:227] cbsz:4 abid:3\n"
    "s_waitcnt vmcnt(4)\n"
    "s_memrealtime s[34:35]\n"
    "ds_read_b64_tr_b16 v[168:169], v240 offset:0\n"
    "ds_read_b64_tr_b16 v[170:171], v241 offset:256\n"
    "ds_read_b64_tr_b16 v[172:173], v242 offset:512\n"
    "ds_read_b64_tr_b16 v[174:175], v243 offset:768\n"
    "ds_read_b32 v236, v253 offset:0\n"
    "ds_read_b32 v238, v253 offset:4\n"
    "ds_read_b128 v[160:163], v234 offset:2048\n"
    "ds_read_b128 v[164:167], v235 offset:2048\n"
    "ds_read_b128 v[184:187], v252 offset:0\n"
    "ds_read_b128 v[188:191], v252 offset:16\n"
    "v_pk_fma_f32 v[144:145], v[144:145], s[70:71], v[96:97]\n"
    "v_pk_fma_f32 v[146:147], v[146:147], s[70:71], v[98:99]\n"
    "v_pk_add_f32 v[230:231], v[144:145], v[146:147]\n"
    "v_pk_mul_f32 v[232:233], v[144:145], v[146:147]\n"
    "v_pk_fma_f32 v[148:149], v[148:149], s[70:71], v[100:101]\n"
    "v_pk_fma_f32 v[230:231], v[230:231], v[148:149], v[232:233]\n"
    "v_pk_mul_f32 v[232:233], v[232:233], v[148:149]\n"
    "v_pk_fma_f32 v[150:151], v[150:151], s[70:71], v[102:103]\n"
    "v_pk_fma_f32 v[230:231], v[230:231], v[150:151], v[232:233]\n"
    "v_pk_mul_f32 v[232:233], v[232:233], v[150:151]\n"
    "v_pk_fma_f32 v[152:153], v[152:153], s[70:71], v[104:105]\n"
    "v_pk_fma_f32 v[230:231], v[230:231], v[152:153], v[232:233]\n"
    "v_pk_mul_f32 v[232:233], v[232:233], v[152:153]\n"
    "v_pk_fma_f32 v[154:155], v[154:155], s[70:71], v[106:107]\n"
    "v_pk_fma_f32 v[230:231], v[230:231], v[154:155], v[232:233]\n"
    "v_pk_mul_f32 v[232:233], v[232:233], v[154:155]\n"
    "v_pk_fma_f32 v[156:157], v[156:157], s[70:71], v[108:109]\n"
    "v_pk_fma_f32 v[230:231], v[230:231], v[156:157], v[232:233]\n"
    "v_pk_mul_f32 v[232:233], v[232:233], v[156:157]\n"
    "v_pk_fma_f32 v[158:159], v[158:159], s[70:71], v[110:111]\n"
    "v_pk_fma_f32 v[230:231], v[230:231], v[158:159], v[232:233]\n"
    "v_pk_mul_f32 v[232:233], v[232:233], v[158:159]\n"
    "v_rcp_f32 v232, v232\n"
    "v_rcp_f32 v233, v233\n"
    "v_fma_f32 v230, v230, v232, v247\n"
    "v_fmac_f32 v230, v231, v233\n"
    "v_cvt_pk_bf16_f32 v230, v230, v230\n"
    "s_add_u32 s67, s67, 64\n"
    "s_waitcnt lgkmcnt(0)\n"
    "ds_write_b16 v245, v230\n"
    "ds_read_b64 v[228:229], v246\n"
    "s_cmp_le_u32 s34, s76\n"
    "s_cbranch_scc1 .Ldin_B3_wait\n"
    ".Ldin_B3_go:\n"
    "s_sub_u32 s94, s34, 60\n"
    "s_max_u32 s76, s76, s94\n"
    "s_add_u32 s76, s76, s72\n"
    ".Ldin_B3_dma:\n"
    "buffer_load_dwordx4 v[236:237], s[40:43], 0 idxen offen offset:0 nt lds\n"
    "buffer_load_dwordx4 v[238:239], s[40:43], 0 idxen offen offset:1024 nt lds\n"
    ".Ldin_B3_nd:\n"
    "s_branch .Ldin_G0\n"
    ".Ldin_L1_poll:\n"
    "s_sleep 1\n"
    "ds_read_b64 v[232:233], v253\n"
    "s_waitcnt lgkmcnt(0)\n"
    "v_readfirstlane_b32 s60, v232\n"
    "s_cmp_eq_u32 s60, -1\n"
    "s_cbranch_scc1 .Ldin_L1_poll\n"
    "s_branch .Ldin_L1_pr\n"
    ".Ldin_B3_wait:\n"
    "s_cmp_lt_i32 s59, s57\n"
    "s_cbranch_scc1 .Ldin_B3_w2\n"
    "s_sub_u32 s94, s67, s65\n"
    "s_cmp_gt_u32 s94, 256\n"
    "s_cbranch_scc1 .Ldin_B3_dma\n"
    "s_cmp_eq_u32 s94, 64\n"
    "s_waitcnt vmcnt(2)\n"
    "s_cbranch_scc1 .Ldin_B3_nd\n"
    "s_waitcnt vmcnt(0)\n"
    "s_branch .Ldin_B3_nd\n"
    ".Ldin_B3_slp:\n"
    "s_sleep 1\n"
    ".Ldin_B3_w2:\n"
    "s_memrealtime s[34:35]\n"
    "s_waitcnt lgkmcnt(0)\n"
    "s_sub_u32 s94, s34, s76\n"
    "s_cmp_lt_i32 s94, 0\n"
    "s_cbranch_scc1 .Ldin_B3_slp\n"
    "s_branch .Ldin_B3_go\n"
    ".p2align 5\n"
    ".Ldin_L0:\n"
    "s_setprio 0\n"
    "s_cmp_ge_i32 s59, s57\n"
    "s_cbranch_scc1 .Ldin_E0\n"
    "s_add_u32 s76, s76, s73\n"
    "v_mov_b32 v253, s63\n"
    "v_and_b32 v184, v254, v184\n"
    "v_and_b32 v185, v255, v185\n"
    "v_and_b32 v186, v254, v186\n"
    "v_and_b32 v187, v255, v187\n"
    "v_and_b32 v188, v254, v188\n"
    "v_and_b32 v189, v255, v189\n"
    "v_and_b32 v190, v254, v190\n"
    "v_and_b32_e64 v191, v255, v191\n"
    "ds_read_b64 v[232:233], v253\n"
    "v_mfma_f32_4x4x4_16b_bf16 v[144:147], v[184:185], v[32:33], v[64:67]\n"
    "ds_read_b128 v[112:115], v252 offset:17408\n"
    "v_mfma_f32_4x4x4_16b_bf16 v[148:151], v[186:187], v[34:35], v[68:71]\n"
    "ds_read_b128 v[116:119], v252 offset:17424\n"
    "v_mfma_f32_4x4x4_16b_bf16 v[152:155], v[188:189], v[36:37], v[72:75]\n"
    "ds_read_b128 v[120:123], v252 offset:17536\n"
    "ds_read_b128 v[124:127], v252 offset:17552\n"
    "v_cvt_pk_bf16_f32 v0, v144, v145\n"
    "v_cvt_pk_bf16_f32 v1, v146, v147\n"
    "v_mfma_f32_4x4x4_16b_bf16 v[156:159], v[190:191], v[38:39], v[76:79]\n"
    "v_cvt_pk_bf16_f32 v2, v148, v149\n"
    "v_cvt_pk_bf16_f32 v3, v150, v151\n"
    "v_mfma_f32_4x4x4_16b_bf16 v[144:147], v[184:185], v[40:41], v[80:83]\n"
    "v_cvt_pk_bf16_f32 v4, v152, v153\n"
    "v_cvt_pk_bf16_f32 v5, v154, v155\n"
    "v_mfma_f32_4x4x4_16b_bf16 v[148:151], v[186:187], v[42:43], v[84:87]\n"
    "v_cvt_pk_bf16_f32 v6, v156, v157\n"
    "v_cvt_pk_bf16_f32 v7, v158, v159\n"
    "v_mfma_f32_4x4x4_16b_bf16 v[152:155], v[188:189], v[44:45], v[88:91]\n"
    "v_cvt_pk_bf16_f32 v8, v144, v145\n"
    "v_cvt_pk_bf16_f32 v9, v146, v147\n"
    "v_mfma_f32_4x4x4_16b_bf16 v[156:159], v[190:191], v[46:47], v[92:95]\n"
    "v_cvt_pk_bf16_f32 v10, v148, v149\n"
    "v_cvt_pk_bf16_f32 v11, v150, v151\n"
    "v_mfma_f32_4x4x4_16b_bf16 v[144:147], v[184:185], v[48:49], v[192:195]\n"
    "v_cvt_pk_bf16_f32 v12, v152, v153\n"
    "v_cvt_pk_bf16_f32 v13, v154, v155\n"
    "v_mfma_f32_4x4x4_16b_bf16 v[148:151], v[186:187], v[50:51], v[196:199]\n"
    "v_cvt_pk_bf16_f32 v14, v156, v157\n"
    "v_cvt_pk_bf16_f32 v15, v158, v159\n"
    "v_mfma_f32_4x4x4_16b_bf16 v[152:155], v[188:189], v[52:53], v[200:203]\n"
    "s_waitcnt lgkmcnt(0)\n"
    "v_readfirstlane_b32 s60, v232\n"
    "v_cvt_pk_bf16_f32 v16, v144, v145\n"
    "v_cvt_pk_bf16_f32 v17, v146, v147\n"
    "v_mfma_f32_4x4x4_16b_bf16 v[156:159], v[190:191], v[54:55], v[204:207]\n"
    "s_cmp_eq_u32 s60, -1\n"
    "s_cbranch_scc1 .Ldin_L0_poll\n"
    ".Ldin_L0_pr:\n"
    "v_readfirstlane_b32 s95, v233\n"
    "s_and_b32 s94, s60, 63\n"
    "s_lshl_b32 s94, s94, 6\n"
    "s_add_u32 s94, s94, 68480\n"
    "s_add_u32 s63, s63, 8\n"
    "v_lshl_add_u32 v252, v249, 1, s94\n"
    "ds_read_b32 v252, v252\n"
    "s_sub_u32 s95, s95, s76\n"
    "s_ashr_i32 s95, s95, 4\n"
    "s_min_i32 s95, s95, s74\n"
    "s_max_i32 s72, s95, s75\n"
    "v_cvt_pk_bf16_f32 v18, v148, v149\n"
    "v_cvt_pk_bf16_f32 v19, v150, v151\n"
    "v_mfma_f32_4x4x4_16b_bf16 v[144:147], v[184:185], v[56:57], v[208:211]\n"
    "v_cvt_pk_bf16_f32 v20, v152, v153\n"
    "v_cvt_pk_bf16_f32 v21, v154, v155\n"
    "v_mfma_f32_4x4x4_16b_bf16 v[148:151], v[186:187], v[58:59], v[212:215]\n"
    "v_cvt_pk_bf16_f32 v22, v156, v157\n"
    "v_cvt_pk_bf16_f32 v23, v158, v159\n"
    "v_mfma_f32_4x4x4_16b_bf16 v[152:155], v[188:189], v[60:61], v[216:219]\n"
    "v_cvt_pk_bf16_f32 v24, v144, v145\n"
    "v_cvt_pk_bf16_f32 v25, v146, v147\n"
    "v_mfma_f32_4x4x4_16b_bf16 v[156:159], v[190:191], v[62:63], v[220:223]\n"
    "v_cvt_pk_bf16_f32 v26, v148, v149\n"
    "v_cvt_pk_bf16_f32 v27, v150, v151\n"
    "v_cvt_pk_bf16_f32 v28, v152, v153\n"
    "v_cvt_pk_bf16_f32 v29, v154, v155\n"
    "v_exp_f32 v136, v136 clamp\n"
    "v_cvt_pk_bf16_f32 v30, v156, v157\n"
    "v_cvt_pk_bf16_f32 v31, v158, v159\n"
    "v_mfma_f32_16x16x32_bf16 v[144:147], v[0:3], v[160:163], v[112:115]\n"
    "v_exp_f32 v128, v128 clamp\n"
    "v_mfma_f32_16x16x32_bf16 v[144:147], v[4:7], v[164:167], v[144:147]\n"
    "v_exp_f32 v129, v129 clamp\n"
    "v_mfma_f32_16x16x32_bf16 v[148:151], v[8:11], v[160:163], v[116:119]\n"
    "v_exp_f32 v130, v130 clamp\n"
    "v_mfma_f32_16x16x32_bf16 v[148:151], v[12:15], v[164:167], v[148:151]\n"
    "v_exp_f32 v131, v131 clamp\n"
    "v_mfma_f32_16x16x32_bf16 v[152:155], v[16:19], v[160:163], v[120:123]\n"
    "v_exp_f32 v132, v132 clamp\n"
    "v_mfma_f32_16x16x32_bf16 v[152:155], v[20:23], v[164:167], v[152:155]\n"
    "v_exp_f32 v133, v133 clamp\n"
    "v_mfma_f32_16x16x32_bf16 v[156:159], v[24:27], v[160:163], v[124:127]\n"
    "v_exp_f32 v134, v134 clamp\n"
    "v_mfma_f32_16x16x32_bf16 v[156:159], v[28:31], v[164:167], v[156:159]\n"
    "v_exp_f32 v135, v135 clamp\n"
    "s_add_u32 m0, s68, 2048\n"
    "v_exp_f32 v137, v137 clamp\n"
    "v_add_u32 v253, s67, v244\n"
    "v_mfma_f32_4x4x4_16b_bf16 v[224:227], v[228:229], v[176:177], v[224:227] cbsz:4 abid:0\n"
    "v_exp_f32 v138, v138 clamp\n"
    "v_exp_f32 v139, v139 clamp\n"
    "v_mfma_f32_4x4x4_16b_bf16 v[224:227], v[228:229], v[178:179], v[224:227] cbsz:4 abid:1\n"
    "v_exp_f32 v140, v140 clamp\n"
    "v_exp_f32 v141, v141 clamp\n"
    "v_mfma_f32_4x4x4_16b_bf16 v[224:227], v[228:229], v[180:181], v[224:227] cbsz:4 abid:2\n"
    "v_exp_f32 v142, v142 clamp\n"
    "v_exp_f32 v143, v143 clamp\n"
    "v_mfma_f32_4x4x4_16b_bf16 v[224:227], v[228:229], v[182:183], v[224:227] cbsz:4 abid:3\n"
    "s_waitcnt vmcnt(4)\n"
    "ds_read_b64_tr_b16 v[176:177], v240 offset:2048\n"
    "ds_read_b64_tr_b16 v[178:179], v241 offset:2304\n"
    "ds_read_b64_tr_b16 v[180:181], v242 offset:2560\n"
    "ds_read_b64_tr_b16 v[182:183], v243 offset:2816\n"
    "ds_read_b32 v236, v253 offset:0\n"
    "ds_read_b32 v238, v253 offset:4\n"
    "ds_read_b128 v[160:163], v234 offset:4096\n"
    "ds_read_b128 v[164:167], v235 offset:4096\n"
    "s_waitcnt lgkmcnt(8)\n"
    "s_nop 0\n"
    "v_readlane_b32 s20, v252, 0\n"
    "v_readlane_b32 s21, v252, 1\n"
    "v_readlane_b32 s22, v252, 2\n"
    "v_readlane_b32 s23, v252, 3\n"
    "v_readlane_b32 s24, v252, 4\n"
    "v_readlane_b32 s25, v252, 5\n"
    "v_readlane_b32 s26, v252, 6\n"
    "v_readlane_b32 s27, v252, 7\n"
    "v_pk_fma_f32 v[128:129], v[128:129], s[70:71], v[96:97]\n"
    "v_pk_fma_f32 v[130:131], v[130:131], s[70:71], v[98:99]\n"
    "v_pk_add_f32 v[230:231], v[128:129], v[130:131]\n"
    "v_pk_mul_f32 v[232:233], v[128:129], v[130:131]\n"
    "v_pk_fma_f32 v[132:133], v[132:133], s[70:71], v[100:101]\n"
    "v_pk_fma_f32 v[230:231], v[230:231], v[132:133], v[232:233]\n"
    "v_pk_mul_f32 v[232:233], v[232:233], v[132:133]\n"
    "v_pk_fma_f32 v[134:135], v[134:135], s[70:71], v[102:103]\n"
    "v_pk_fma_f32 v[230:231], v[230:231], v[134:135], v[232:233]\n"
    "v_pk_mul_f32 v[232:233], v[232:233], v[134:135]\n"
    "v_pk_fma_f32 v[136:137], v[136:137], s[70:71], v[104:105]\n"
    "v_pk_fma_f32 v[230:231], v[230:231], v[136:137], v[232:233]\n"
    "v_pk_mul_f32 v[232:233], v[232:233], v[136:137]\n"
    "v_pk_fma_f32 v[138:139], v[138:139], s[70:71], v[106:107]\n"
    "v_pk_fma_f32 v[230:231], v[230:231], v[138:139], v[232:233]\n"
    "v_pk_mul_f32 v[232:233], v[232:233], v[138:139]\n"
    "v_pk_fma_f32 v[140:141], v[140:141], s[70:71], v[108:109]\n"
    "v_pk_fma_f32 v[230:231], v[230:231], v[140:141], v[232:233]\n"
    "v_pk_mul_f32 v[232:233], v[232:233], v[140:141]\n"
    "v_pk_fma_f32 v[142:143], v[142:143], s[70:71], v[110:111]\n"
    "v_pk_fma_f32 v[230:231], v[230:231], v[142:143], v[232:233]\n"
    "v_pk_mul_f32 v[232:233], v[232:233], v[142:143]\n"
    "v_rcp_f32 v232, v232\n"
    "v_rcp_f32 v233, v233\n"
    "v_fma_f32 v230, v230, v232, v247\n"
    "v_fmac_f32 v230, v231, v233\n"
    "v_cvt_pk_bf16_f32 v230, v230, v230\n"
    "s_add_u32 s67, s67, 64\n"
    "s_waitcnt lgkmcnt(0)\n"
    "ds_write_b16 v245, v230\n"
    "ds_read_b64 v[228:229], v246\n"
    "s_lshl1_add_u32 s76, s72, s76\n"
    "buffer_load_dwordx4 v[236:237], s[40:43], 0 idxen offen offset:0 nt lds\n"
    "buffer_load_dwordx4 v[238:239], s[40:43], 0 idxen offen offset:1024 nt lds\n"
    "s_and_b32 s62, s58, 63\n"
    "s_mul_i32 s62, s62, 272\n"
    "s_mov_b32 s58, s59\n"
    "s_mov_b32 s59, s60\n"
    "s_mov_b32 s81, s65\n"
    "s_xor_b32 s65, s65, s64\n"
    "s_mov_b32 s80, s83\n"
    "s_add_u32 s82, s65, 256\n"
    "s_cmp_lt_i32 s59, s57\n"
    "s_cselect_b32 s76, s76, -1\n"
    "s_bitcmp1_b32 s58, 30\n"
    "s_cbranch_scc1 .Ldin_A1s\n"
    ".Ldin_A1:\n"
    "v_mfma_f32_16x16x32_bf16 v[128:131], v[0:3], v[160:163], v[112:115]\n"
    "v_exp_f32 v144, v144 clamp\n"
    "v_mfma_f32_16x16x32_bf16 v[128:131], v[4:7], v[164:167], v[128:131]\n"
    "v_exp_f32 v145, v145 clamp\n"
    "v_mfma_f32_16x16x32_bf16 v[132:135], v[8:11], v[160:163], v[116:119]\n"
    "v_exp_f32 v146, v146 clamp\n"
    "v_mfma_f32_16x16x32_bf16 v[132:135], v[12:15], v[164:167], v[132:135]\n"
    "v_exp_f32 v147, v147 clamp\n"
    "v_mfma_f32_16x16x32_bf16 v[136:139], v[16:19], v[160:163], v[120:123]\n"
    "v_exp_f32 v148, v148 clamp\n"
    "v_mfma_f32_16x16x32_bf16 v[136:139], v[20:23], v[164:167], v[136:139]\n"
    "v_exp_f32 v149, v149 clamp\n"
    "v_mfma_f32_16x16x32_bf16 v[140:143], v[24:27], v[160:163], v[124:127]\n"
    "v_exp_f32 v150, v150 clamp\n"
    "v_mfma_f32_16x16x32_bf16 v[140:143], v[28:31], v[164:167], v[140:143]\n"
    "v_exp_f32 v151, v151 clamp\n"
    "v_exp_f32 v152, v152 clamp\n"
    "s_add_u32 m0, s68, 4096\n"
    "v_exp_f32 v153, v153 clamp\n"
    "v_add_u32_e64 v253, s67, v244\n"
    "s_waitcnt lgkmcnt(0)\n"
    "v_mfma_f32_4x4x4_16b_bf16 v[224:227], v[228:229], v[168:169], v[224:227] cbsz:4 abid:0\n"
    "v_exp_f32 v154, v154 clamp\n"
    "v_exp_f32 v155, v155 clamp\n"
    "v_mfma_f32_4x4x4_16b_bf16 v[224:227], v[228:229], v[170:171], v[224:227] cbsz:4 abid:1\n"
    "v_exp_f32 v156, v156 clamp\n"
    "v_exp_f32 v157, v157 clamp\n"
    "v_mfma_f32_4x4x4_16b_bf16 v[224:227], v[228:229], v[172:173], v[224:227] cbsz:4 abid:2\n"
    "v_exp_f32 v158, v158 clamp\n"
    "v_exp_f32 v159, v159 clamp\n"
    "v_mfma_f32_4x4x4_16b_bf16 v[224:227], v[228:229], v[174:175], v[224:227] cbsz:4 abid:3\n"
    "s_waitcnt vmcnt(4)\n"
    "ds_read_b64_tr_b16 v[168:169], v240 offset:4096\n"
    "ds_read_b64_tr_b16 v[170:171], v241 offset:4352\n"
    "ds_read_b64_tr_b16 v[172:173], v242 offset:4608\n"
    "ds_read_b64_tr_b16 v[174:175], v243 offset:4864\n"
    "ds_read_b32 v236, v253 offset:0\n"
    "ds_read_b32 v238, v253 offset:4\n"
    "ds_read_b128 v[160:163], v234 offset:6144\n"
    "ds_read_b128 v[164:167], v235 offset:6144\n"
    "s_cbranch_vccz .Ldin_A1_np\n"
    "s_setprio 1\n"
    ".Ldin_A1_np:\n"
    "v_pk_fma_f32 v[144:145], v[144:145], s[70:71], v[96:97]\n"
    "v_pk_fma_f32 v[146:147], v[146:147], s[70:71], v[98:99]\n"
    "v_add_f32 v191, v224, v225\n"
    "v_add_f32 v190, v226, v227\n"
    "v_add_f32 v191, v191, v190\n"
    "v_add_u32 v190, s62, v249\n"
    "v_mul_f32 v191, s69, v191\n"
    "v_cvt_pk_bf16_f32 v191, v191, v191\n"
    "ds_write_b16 v190, v191 offset:32768\n"
    "s_bfe_u32 s94, s59, 0x180006\n"
    "s_mul_i32 s86, s94, s56\n"
    "v_mbcnt_lo_u32_b32 v184, s20, 0\n"
    "v_mbcnt_lo_u32_b32 v185, s22, 0\n"
    "v_mbcnt_lo_u32_b32 v186, s24, 0\n"
    "v_mbcnt_lo_u32_b32 v187, s26, 0\n"
    "s_bcnt1_i32_b64 s90, s[20:21]\n"
    "s_bcnt1_i32_b64 s91, s[22:23]\n"
    "s_bcnt1_i32_b64 s92, s[24:25]\n"
    "s_bcnt1_i32_b64 s93, s[26:27]\n"
    "v_mbcnt_hi_u32_b32 v184, s21, v184\n"
    "v_mbcnt_hi_u32_b32 v185, s23, v185\n"
    "v_mbcnt_hi_u32_b32 v186, s25, v186\n"
    "v_mbcnt_hi_u32_b32 v187, s27, v187\n"
    "s_lshl2_add_u32 s90, s90, s65\n"
    "s_lshl2_add_u32 s91, s91, s90\n"
    "s_lshl2_add_u32 s92, s92, s91\n"
    "s_lshl2_add_u32 s93, s93, s92\n"
    "v_add_u32_e64 v188, s86, v251\n"
    "v_pk_add_f32 v[230:231], v[144:145], v[146:147]\n"
    "v_pk_mul_f32 v[232:233], v[144:145], v[146:147]\n"
    "v_pk_fma_f32 v[148:149], v[148:149], s[70:71], v[100:101]\n"
    "v_pk_fma_f32 v[230:231], v[230:231], v[148:149], v[232:233]\n"
    "v_pk_mul_f32 v[232:233], v[232:233], v[148:149]\n"
    "v_lshl_add_u32 v184, v184, 2, s65\n"
    "v_lshl_add_u32 v185, v185, 2, s90\n"
    "v_lshl_add_u32 v186, v186, 2, s91\n"
    "v_lshl_add_u32 v187, v187, 2, s92\n"
    "v_mov_b32 v191, -1\n"
    "v_lshl_add_u32 v190, v249, 1, s93\n"
    "ds_write2_b32 v190, v191, v191 offset1:64\n"
    "s_mov_b64 exec, s[20:21]\n"
    "ds_write_b32 v184, v188\n"
    "s_mov_b64 exec, s[22:23]\n"
    "v_add_u32 v189, 64, v188\n"
    "ds_write_b32 v185, v189\n"
    "s_mov_b64 exec, s[24:25]\n"
    "v_add_u32 v190, 128, v188\n"
    "ds_write_b32 v186, v190\n"
    "s_mov_b64 exec, s[26:27]\n"
    "v_add_u32 v189, 192, v188\n"
    "ds_write_b32 v187, v189\n"
    "s_mov_b64 exec, -1\n"
    "s_add_u32 s95, s93, 60\n"
    "s_add_u32 s94, s82, 128\n"
    "s_andn2_b32 s95, s95, 63\n"
    "s_max_u32 s83, s95, s94\n"
    "v_pk_fma_f32 v[150:151], v[150:151], s[70:71], v[102:103]\n"
    "v_pk_fma_f32 v[230:231], v[230:231], v[150:151], v[232:233]\n"
    "v_pk_mul_f32 v[232:233], v[232:233], v[150:151]\n"
    "v_pk_fma_f32 v[152:153], v[152:153], s[70:71], v[104:105]\n"
    "v_pk_fma_f32 v[230:231], v[230:231], v[152:153], v[232:233]\n"
    "v_pk_mul_f32 v[232:233], v[232:233], v[152:153]\n"
    "v_pk_fma_f32 v[154:155], v[154:155], s[70:71], v[106:107]\n"
    "v_pk_fma_f32 v[230:231], v[230:231], v[154:155], v[232:233]\n"
    "v_pk_mul_f32 v[232:233], v[232:233], v[154:155]\n"
    "v_pk_fma_f32 v[156:157], v[156:157], s[70:71], v[108:109]\n"
    "v_pk_fma_f32 v[230:231], v[230:231], v[156:157], v[232:233]\n"
    "v_pk_mul_f32 v[232:233], v[232:233], v[156:157]\n"
    "v_pk_fma_f32 v[158:159], v[158:159], s[70:71], v[110:111]\n"
    "v_pk_fma_f32 v[230:231], v[230:231], v[158:159], v[232:233]\n"
    "v_pk_mul_f32 v[232:233], v[232:233], v[158:159]\n"
    "v_rcp_f32 v232, v232\n"
    "v_rcp_f32 v233, v233\n"
    "v_fma_f32 v230, v230, v232, v247\n"
    "v_fmac_f32 v230, v231, v233\n"
    "v_cvt_pk_bf16_f32 v230, v230, v230\n"
    "s_add_u32 s67, s67, 64\n"
    "s_waitcnt lgkmcnt(0)\n"
    "ds_write_b16 v245, v230\n"
    "ds_read_b64 v[228:229], v246\n"
    "buffer_load_dwordx4 v[236:237], s[40:43], 0 idxen offen offset:0 nt lds\n"
    "buffer_load_dwordx4 v[238:239], s[40:43], 0 idxen offen offset:1024 nt lds\n"
    ".Ldin_B2:\n"
    "v_mfma_f32_16x16x32_bf16 v[144:147], v[0:3], v[160:163], v[112:115]\n"
    "v_exp_f32 v128, v128 clamp\n"
    "v_mfma_f32_16x16x32_bf16 v[144:147], v[4:7], v[164:167], v[144:147]\n"
    "v_exp_f32 v129, v129 clamp\n"
    "v_mfma_f32_16x16x32_bf16 v[148:151], v[8:11], v[160:163], v[116:119]\n"
    "v_exp_f32 v130, v130 clamp\n"
    "v_mfma_f32_16x16x32_bf16 v[148:151], v[12:15], v[164:167], v[148:151]\n"
    "v_exp_f32 v131, v131 clamp\n"
    "v_mfma_f32_16x16x32_bf16 v[152:155], v[16:19], v[160:163], v[120:123]\n"
    "v_exp_f32 v132, v132 clamp\n"
    "v_mfma_f32_16x16x32_bf16 v[152:155], v[20:23], v[164:167], v[152:155]\n"
    "v_exp_f32 v133, v133 clamp\n"
    "v_mfma_f32_16x16x32_bf16 v[156:159], v[24:27], v[160:163], v[124:127]\n"
    "v_exp_f32 v134, v134 clamp\n"
    "v_mfma_f32_16x16x32_bf16 v[156:159], v[28:31], v[164:167], v[156:159]\n"
    "v_exp_f32 v135, v135 clamp\n"
    "v_exp_f32 v136, v136 clamp\n"
    "s_cmp_eq_u32 s67, s80\n"
    "s_cselect_b32 s67, s65, s67\n"
    "s_and_b32 s94, s59, 63\n"
    "s_mul_i32 s94, s94, 272\n"
    "v_add_u32 v252, s94, v250\n"
    "s_add_u32 m0, s68, 6144\n"
    "v_exp_f32 v137, v137 clamp\n"
    "v_add_u32 v253, s67, v244\n"
    "s_waitcnt lgkmcnt(0)\n"
    "v_mfma_f32_4x4x4_16b_bf16 v[224:227], v[228:229], v[176:177], 0 cbsz:4 abid:0\n"
    "v_exp_f32 v138, v138 clamp\n"
    "v_exp_f32 v139, v139 clamp\n"
    "v_mfma_f32_4x4x4_16b_bf16 v[224:227], v[228:229], v[178:179], v[224:227] cbsz:4 abid:1\n"
    "v_exp_f32 v140, v140 clamp\n"
    "v_exp_f32 v141, v141 clamp\n"
    "v_mfma_f32_4x4x4_16b_bf16 v[224:227], v[228:229], v[180:181], v[224:227] cbsz:4 abid:2\n"
    "v_exp_f32 v142, v142 clamp\n"
    "v_exp_f32 v143, v143 clamp\n"
    "v_mfma_f32_4x4x4_16b_bf16 v[224:227], v[228:229], v[182:183], v[224:227] cbsz:4 abid:3\n"
    "s_waitcnt vmcnt(4)\n"
    "s_memrealtime s[34:35]\n"
    "ds_read_b64_tr_b16 v[176:177], v240 offset:6144\n"
    "ds_read_b64_tr_b16 v[178:179], v241 offset:6400\n"
    "ds_read_b64_tr_b16 v[180:181], v242 offset:6656\n"
    "ds_read_b64_tr_b16 v[182:183], v243 offset:6912\n"
    "ds_read_b32 v236, v253 offset:0\n"
    "ds_read_b32 v238, v253 offset:4\n"
    "ds_read_b128 v[160:163], v234 offset:0\n"
    "ds_read_b128 v[164:167], v235 offset:0\n"
    "ds_read_b128 v[184:187], v252 offset:0\n"
    "ds_read_b128 v[188:191], v252 offset:16\n"
    "v_pk_fma_f32 v[128:129], v[128:129], s[70:71], v[96:97]\n"
    "v_pk_fma_f32 v[130:131], v[130:131], s[70:71], v[98:99]\n"
    "v_pk_add_f32 v[230:231], v[128:129], v[130:131]\n"
    "v_pk_mul_f32 v[232:233], v[128:129], v[130:131]\n"
    "v_pk_fma_f32 v[132:133], v[132:133], s[70:71], v[100:101]\n"
    "v_pk_fma_f32 v[230:231], v[230:231], v[132:133], v[232:233]\n"
    "v_pk_mul_f32 v[232:233], v[232:233], v[132:133]\n"
    "v_pk_fma_f32 v[134:135], v[134:135], s[70:71], v[102:103]\n"
    "v_pk_fma_f32 v[230:231], v[230:231], v[134:135], v[232:233]\n"
    "v_pk_mul_f32 v[232:233], v[232:233], v[134:135]\n"
    "v_pk_fma_f32 v[136:137], v[136:137], s[70:71], v[104:105]\n"
    "v_pk_fma_f32 v[230:231], v[230:231], v[136:137], v[232:233]\n"
    "v_pk_mul_f32 v[232:233], v[232:233], v[136:137]\n"
    "v_pk_fma_f32 v[138:139], v[138:139], s[70:71], v[106:107]\n"
    "v_pk_fma_f32 v[230:231], v[230:231], v[138:139], v[232:233]\n"
    "v_pk_mul_f32 v[232:233], v[232:233], v[138:139]\n"
    "v_pk_fma_f32 v[140:141], v[140:141], s[70:71], v[108:109]\n"
    "v_pk_fma_f32 v[230:231], v[230:231], v[140:141], v[232:233]\n"
    "v_pk_mul_f32 v[232:233], v[232:233], v[140:141]\n"
    "v_pk_fma_f32 v[142:143], v[142:143], s[70:71], v[110:111]\n"
    "v_pk_fma_f32 v[230:231], v[230:231], v[142:143], v[232:233]\n"
    "v_pk_mul_f32 v[232:233], v[232:233], v[142:143]\n"
    "v_rcp_f32 v232, v232\n"
    "v_rcp_f32 v233, v233\n"
    "v_fma_f32 v230, v230, v232, v247\n"
    "v_fmac_f32 v230, v231, v233\n"
    "v_cvt_pk_bf16_f32 v230, v230, v230\n"
    "s_add_u32 s67, s67, 64\n"
    "s_waitcnt lgkmcnt(0)\n"
    "ds_write_b16 v245, v230\n"
    "ds_read_b64 v[228:229], v246\n"
    "s_cmp_le_u32 s34, s76\n"
    "s_cbranch_scc1 .Ldin_B2_wait\n"
    ".Ldin_B2_go:\n"
    "s_sub_u32 s94, s34, 60\n"
    "s_max_u32 s76, s76, s94\n"
    "s_add_u32 s76, s76, s72\n"
    ".Ldin_B2_dma:\n"
    "buffer_load_dwordx4 v[236:237], s[40:43], 0 idxen offen offset:0 nt lds\n"
    "buffer_load_dwordx4 v[238:239], s[40:43], 0 idxen offen offset:1024 nt lds\n"
    ".Ldin_B2_nd:\n"
    "s_branch .Ldin_G3\n"
    ".Ldin_L0_poll:\n"
    "s_sleep 1\n"
    "ds_read_b64 v[232:233], v253\n"
    "s_waitcnt lgkmcnt(0)\n"
    "v_readfirstlane_b32 s60, v232\n"
    "s_cmp_eq_u32 s60, -1\n"
    "s_cbranch_scc1 .Ldin_L0_poll\n"
    "s_branch .Ldin_L0_pr\n"
    ".Ldin_B2_wait:\n"
    "s_cmp_lt_i32 s59, s57\n"
    "s_cbranch_scc1 .Ldin_B2_w2\n"
    "s_sub_u32 s94, s67, s65\n"
    "s_cmp_gt_u32 s94, 256\n"
    "s_cbranch_scc1 .Ldin_B2_dma\n"
    "s_cmp_eq_u32 s94, 64\n"
    "s_waitcnt vmcnt(2)\n"
    "s_cbranch_scc1 .Ldin_B2_nd\n"
    "s_waitcnt vmcnt(0)\n"
    "s_branch .Ldin_B2_nd\n"
    ".Ldin_B2_slp:\n"
    "s_sleep 1\n"
    ".Ldin_B2_w2:\n"
    "s_memrealtime s[34:35]\n"
    "s_waitcnt lgkmcnt(0)\n"
    "s_sub_u32 s94, s34, s76\n"
    "s_cmp_lt_i32 s94, 0\n"
    "s_cbranch_scc1 .Ldin_B2_slp\n"
    "s_branch .Ldin_B2_go\n"
    ".p2align 5\n"
    ".Ldin_L3:\n"
    "s_setprio 0\n"
    "s_cmp_ge_i32 s59, s57\n"
    "s_cbranch_scc1 .Ldin_E1\n"
    "s_add_u32 s76, s76, s73\n"
    "v_mov_b32 v253, s63\n"
    "v_and_b32 v184, v254, v184\n"
    "v_and_b32 v185, v255, v185\n"
    "v_and_b32 v186, v254, v186\n"
    "v_and_b32 v187, v255, v187\n"
    "v_and_b32 v188, v254, v188\n"
    "v_and_b32 v189, v255, v189\n"
    "v_and_b32 v190, v254, v190\n"
    "v_and_b32_e64 v191, v255, v191\n"
    "ds_read_b64 v[232:233], v253\n"
    "v_mfma_f32_4x4x4_16b_bf16 v[128:131], v[184:185], v[32:33], v[64:67]\n"
    "ds_read_b128 v[112:115], v252 offset:17408\n"
    "v_mfma_f32_4x4x4_16b_bf16 v[132:135], v[186:187], v[34:35], v[68:71]\n"
    "ds_read_b128 v[116:119], v252 offset:17424\n"
    "v_mfma_f32_4x4x4_16b_bf16 v[136:139], v[188:189], v[36:37], v[72:75]\n"
    "ds_read_b128 v[120:123], v252 offset:17536\n"
    "ds_read_b128 v[124:127], v252 offset:17552\n"
    "v_cvt_pk_bf16_f32 v0, v128, v129\n"
    "v_cvt_pk_bf16_f32 v1, v130, v131\n"
    "v_mfma_f32_4x4x4_16b_bf16 v[140:143], v[190:191], v[38:39], v[76:79]\n"
    "v_cvt_pk_bf16_f32 v2, v132, v133\n"
    "v_cvt_pk_bf16_f32 v3, v134, v135\n"
    "v_mfma_f32_4x4x4_16b_bf16 v[128:131], v[184:185], v[40:41], v[80:83]\n"
    "v_cvt_pk_bf16_f32 v4, v136, v137\n"
    "v_cvt_pk_bf16_f32 v5, v138, v139\n"
    "v_mfma_f32_4x4x4_16b_bf16 v[132:135], v[186:187], v[42:43], v[84:87]\n"
    "v_cvt_pk_bf16_f32 v6, v140, v141\n"
    "v_cvt_pk_bf16_f32 v7, v142, v143\n"
    "v_mfma_f32_4x4x4_16b_bf16 v[136:139], v[188:189], v[44:45], v[88:91]\n"
    "v_cvt_pk_bf16_f32 v8, v128, v129\n"
    "v_cvt_pk_bf16_f32 v9, v130, v131\n"
    "v_mfma_f32_4x4x4_16b_bf16 v[140:143], v[190:191], v[46:47], v[92:95]\n"
    "v_cvt_pk_bf16_f32 v10, v132, v133\n"
    "v_cvt_pk_bf16_f32 v11, v134, v135\n"
    "v_mfma_f32_4x4x4_16b_bf16 v[128:131], v[184:185], v[48:49], v[192:195]\n"
    "v_cvt_pk_bf16_f32 v12, v136, v137\n"
    "v_cvt_pk_bf16_f32 v13, v138, v139\n"
    "v_mfma_f32_4x4x4_16b_bf16 v[132:135], v[186:187], v[50:51], v[196:199]\n"
    "v_cvt_pk_bf16_f32 v14, v140, v141\n"
    "v_cvt_pk_bf16_f32 v15, v142, v143\n"
    "v_mfma_f32_4x4x4_16b_bf16 v[136:139], v[188:189], v[52:53], v[200:203]\n"
    "s_waitcnt lgkmcnt(0)\n"
    "v_readfirstlane_b32 s60, v232\n"
    "v_cvt_pk_bf16_f32 v16, v128, v129\n"
    "v_cvt_pk_bf16_f32 v17, v130, v131\n"
    "v_mfma_f32_4x4x4_16b_bf16 v[140:143], v[190:191], v[54:55], v[204:207]\n"
    "s_cmp_eq_u32 s60, -1\n"
    "s_cbranch_scc1 .Ldin_L3_poll\n"
    ".Ldin_L3_pr:\n"
    "v_readfirstlane_b32 s95, v233\n"
    "s_and_b32 s94, s60, 63\n"
    "s_lshl_b32 s94, s94, 6\n"
    "s_add_u32 s94, s94, 68480\n"
    "s_add_u32 s63, s63, 8\n"
    "v_lshl_add_u32 v252, v249, 1, s94\n"
    "ds_read_b32 v252, v252\n"
    "s_sub_u32 s95, s95, s76\n"
    "s_ashr_i32 s95, s95, 4\n"
    "s_min_i32 s95, s95, s74\n"
    "s_max_i32 s72, s95, s75\n"
    "v_cvt_pk_bf16_f32 v18, v132, v133\n"
    "v_cvt_pk_bf16_f32 v19, v134, v135\n"
    "v_mfma_f32_4x4x4_16b_bf16 v[128:131], v[184:185], v[56:57], v[208:211]\n"
    "v_cvt_pk_bf16_f32 v20, v136, v137\n"
    "v_cvt_pk_bf16_f32 v21, v138, v139\n"
    "v_mfma_f32_4x4x4_16b_bf16 v[132:135], v[186:187], v[58:59], v[212:215]\n"
    "v_cvt_pk_bf16_f32 v22, v140, v141\n"
    "v_cvt_pk_bf16_f32 v23, v142, v143\n"
    "v_mfma_f32_4x4x4_16b_bf16 v[136:139], v[188:189], v[60:61], v[216:219]\n"
    "v_cvt_pk_bf16_f32 v24, v128, v129\n"
    "v_cvt_pk_bf16_f32 v25, v130, v131\n"
    "v_mfma_f32_4x4x4_16b_bf16 v[140:143], v[190:191], v[62:63], v[220:223]\n"
    "v_cvt_pk_bf16_f32 v26, v132, v133\n"
    "v_cvt_pk_bf16_f32 v27, v134, v135\n"
    "v_cvt_pk_bf16_f32 v28, v136, v137\n"
    "v_cvt_pk_bf16_f32 v29, v138, v139\n"
    "v_exp_f32 v152, v152 clamp\n"
    "v_cvt_pk_bf16_f32 v30, v140, v141\n"
    "v_cvt_pk_bf16_f32 v31, v142, v143\n"
    "v_mfma_f32_16x16x32_bf16 v[128:131], v[0:3], v[160:163], v[112:115]\n"
    "v_exp_f32 v144, v144 clamp\n"
    "v_mfma_f32_16x16x32_bf16 v[128:131], v[4:7], v[164:167], v[128:131]\n"
    "v_exp_f32 v145, v145 clamp\n"
    "v_mfma_f32_16x16x32_bf16 v[132:135], v[8:11], v[160:163], v[116:119]\n"
    "v_exp_f32 v146, v146 clamp\n"
    "v_mfma_f32_16x16x32_bf16 v[132:135], v[12:15], v[164:167], v[132:135]\n"
    "v_exp_f32 v147, v147 clamp\n"
    "v_mfma_f32_16x16x32_bf16 v[136:139], v[16:19], v[160:163], v[120:123]\n"
    "v_exp_f32 v148, v148 clamp\n"
    "v_mfma_f32_16x16x32_bf16 v[136:139], v[20:23], v[164:167], v[136:139]\n"
    "v_exp_f32 v149, v149 clamp\n"
    "v_mfma_f32_16x16x32_bf16 v[140:143], v[24:27], v[160:163], v[124:127]\n"
    "v_exp_f32 v150, v150 clamp\n"
    "v_mfma_f32_16x16x32_bf16 v[140:143], v[28:31], v[164:167], v[140:143]\n"
    "v_exp_f32 v151, v151 clamp\n"
    "s_add_u32 m0, s68, 0\n"
    "v_exp_f32 v153, v153 clamp\n"
    "v_add_u32_e64 v253, s67, v244\n"
    "v_mfma_f32_4x4x4_16b_bf16 v[224:227], v[228:229], v[168:169], v[224:227] cbsz:4 abid:0\n"
    "v_exp_f32 v154, v154 clamp\n"
    "v_exp_f32 v155, v155 clamp\n"
    "v_mfma_f32_4x4x4_16b_bf16 v[224:227], v[228:229], v[170:171], v[224:227] cbsz:4 abid:1\n"
    "v_exp_f32 v156, v156 clamp\n"
    "v_exp_f32 v157, v157 clamp\n"
    "v_mfma_f32_4x4x4_16b_bf16 v[224:227], v[228:229], v[172:173], v[224:227] cbsz:4 abid:2\n"
    "v_exp_f32 v158, v158 clamp\n"
    "v_exp_f32 v159, v159 clamp\n"
    "v_mfma_f32_4x4x4_16b_bf16 v[224:227], v[228:229], v[174:175], v[224:227] cbsz:4 abid:3\n"
    "s_waitcnt vmcnt(4)\n"
    "ds_read_b64_tr_b16 v[168:169], v240 offset:0\n"
    "ds_read_b64_tr_b16 v[170:171], v241 offset:256\n"
    "ds_read_b64_tr_b16 v[172:173], v242 offset:512\n"
    "ds_read_b64_tr_b16 v[174:175], v243 offset:768\n"
    "ds_read_b32 v236, v253 offset:0\n"
    "ds_read_b32 v238, v253 offset:4\n"
    "ds_read_b128 v[160:163], v234 offset:2048\n"
    "ds_read_b128 v[164:167], v235 offset:2048\n"
    "s_waitcnt lgkmcnt(8)\n"
    "s_nop 0\n"
    "v_readlane_b32 s20, v252, 0\n"
    "v_readlane_b32 s21, v252, 1\n"
    "v_readlane_b32 s22, v252, 2\n"
    "v_readlane_b32 s23, v252, 3\n"
    "v_readlane_b32 s24, v252, 4\n"
    "v_readlane_b32 s25, v252, 5\n"
    "v_readlane_b32 s26, v252, 6\n"
    "v_readlane_b32 s27, v252, 7\n"
    "v_pk_fma_f32 v[144:145], v[144:145], s[70:71], v[96:97]\n"
    "v_pk_fma_f32 v[146:147], v[146:147], s[70:71], v[98:99]\n"
    "v_pk_add_f32 v[230:231], v[144:145], v[146:147]\n"
    "v_pk_mul_f32 v[232:233], v[144:145], v[146:147]\n"
    "v_pk_fma_f32 v[148:149], v[148:149], s[70:71], v[100:101]\n"
    "v_pk_fma_f32 v[230:231], v[230:231], v[148:149], v[232:233]\n"
    "v_pk_mul_f32 v[232:233], v[232:233], v[148:149]\n"
    "v_pk_fma_f32 v[150:151], v[150:151], s[70:71], v[102:103]\n"
    "v_pk_fma_f32 v[230:231], v[230:231], v[150:151], v[232:233]\n"
    "v_pk_mul_f32 v[232:233], v[232:233], v[150:151]\n"
    "v_pk_fma_f32 v[152:153], v[152:153], s[70:71], v[104:105]\n"
    "v_pk_fma_f32 v[230:231], v[230:231], v[152:153], v[232:233]\n"
    "v_pk_mul_f32 v[232:233], v[232:233], v[152:153]\n"
    "v_pk_fma_f32 v[154:155], v[154:155], s[70:71], v[106:107]\n"
    "v_pk_fma_f32 v[230:231], v[230:231], v[154:155], v[232:233]\n"
    "v_pk_mul_f32 v[232:233], v[232:233], v[154:155]\n"
    "v_pk_fma_f32 v[156:157], v[156:157], s[70:71], v[108:109]\n"
    "v_pk_fma_f32 v[230:231], v[230:231], v[156:157], v[232:233]\n"
    "v_pk_mul_f32 v[232:233], v[232:233], v[156:157]\n"
    "v_pk_fma_f32 v[158:159], v[158:159], s[70:71], v[110:111]\n"
    "v_pk_fma_f32 v[230:231], v[230:231], v[158:159], v[232:233]\n"
    "v_pk_mul_f32 v[232:233], v[232:233], v[158:159]\n"
    "v_rcp_f32 v232, v232\n"
    "v_rcp_f32 v233, v233\n"
    "v_fma_f32 v230, v230, v232, v247\n"
    "v_fmac_f32 v230, v231, v233\n"
    "v_cvt_pk_bf16_f32 v230, v230, v230\n"
    "s_add_u32 s67, s67, 64\n"
    "s_waitcnt lgkmcnt(0)\n"
    "ds_write_b16 v245, v230\n"
    "ds_read_b64 v[228:229], v246\n"
    "s_lshl1_add_u32 s76, s72, s76\n"
    "buffer_load_dwordx4 v[236:237], s[40:43], 0 idxen offen offset:0 nt lds\n"
    "buffer_load_dwordx4 v[238:239], s[40:43], 0 idxen offen offset:1024 nt lds\n"
    "s_and_b32 s62, s58, 63\n"
    "s_mul_i32 s62, s62, 272\n"
    "s_mov_b32 s58, s59\n"
    "s_mov_b32 s59, s60\n"
    "s_mov_b32 s81, s65\n"
    "s_xor_b32 s65, s65, s64\n"
    "s_mov_b32 s80, s83\n"
    "s_add_u32 s82, s65, 256\n"
    "s_cmp_lt_i32 s59, s57\n"
    "s_cselect_b32 s76, s76, -1\n"
    "s_bitcmp1_b32 s58, 30\n"
    "s_cbranch_scc1 .Ldin_A0s\n"
    "s_branch .Ldin_A0\n"
    ".Ldin_L3_poll:\n"
    "s_sleep 1\n"
    "ds_read_b64 v[232:233], v253\n"
    "s_waitcnt lgkmcnt(0)\n"
    "v_readfirstlane_b32 s60, v232\n"
    "s_cmp_eq_u32 s60, -1\n"
    "s_cbranch_scc1 .Ldin_L3_poll\n"
    "s_branch .Ldin_L3_pr\n"
    ".p2align 5\n"
    ".Ldin_E0:\n"
    "v_exp_f32 v128, v128 clamp\n"
    "v_exp_f32 v129, v129 clamp\n"
    "v_exp_f32 v130, v130 clamp\n"
    "v_exp_f32 v131, v131 clamp\n"
    "v_exp_f32 v132, v132 clamp\n"
    "v_exp_f32 v133, v133 clamp\n"
    "v_exp_f32 v134, v134 clamp\n"
    "v_exp_f32 v135, v135 clamp\n"
    "s_and_b32 s62, s58, 63\n"
    "s_mul_i32 s62, s62, 272\n"
    "s_waitcnt lgkmcnt(0)\n"
    "v_mfma_f32_4x4x4_16b_bf16 v[224:227], v[228:229], v[176:177], v[224:227] cbsz:4 abid:0\n"
    "v_exp_f32 v136, v136 clamp\n"
    "v_exp_f32 v137, v137 clamp\n"
    "v_mfma_f32_4x4x4_16b_bf16 v[224:227], v[228:229], v[178:179], v[224:227] cbsz:4 abid:1\n"
    "v_exp_f32 v138, v138 clamp\n"
    "v_exp_f32 v139, v139 clamp\n"
    "v_mfma_f32_4x4x4_16b_bf16 v[224:227], v[228:229], v[180:181], v[224:227] cbsz:4 abid:2\n"
    "v_exp_f32 v140, v140 clamp\n"
    "v_exp_f32 v141, v141 clamp\n"
    "v_mfma_f32_4x4x4_16b_bf16 v[224:227], v[228:229], v[182:183], v[224:227] cbsz:4 abid:3\n"
    "v_exp_f32 v142, v142 clamp\n"
    "v_exp_f32 v143, v143 clamp\n"
    "v_pk_fma_f32 v[128:129], v[128:129], s[70:71], v[96:97]\n"
    "v_pk_fma_f32 v[130:131], v[130:131], s[70:71], v[98:99]\n"
    "v_pk_add_f32 v[230:231], v[128:129], v[130:131]\n"
    "v_pk_mul_f32 v[232:233], v[128:129], v[130:131]\n"
    "v_pk_fma_f32 v[132:133], v[132:133], s[70:71], v[100:101]\n"
    "v_pk_fma_f32 v[230:231], v[230:231], v[132:133], v[232:233]\n"
    "v_pk_mul_f32 v[232:233], v[232:233], v[132:133]\n"
    "v_pk_fma_f32 v[134:135], v[134:135], s[70:71], v[102:103]\n"
    "v_pk_fma_f32 v[230:231], v[230:231], v[134:135], v[232:233]\n"
    "v_pk_mul_f32 v[232:233], v[232:233], v[134:135]\n"
    "v_pk_fma_f32 v[136:137], v[136:137], s[70:71], v[104:105]\n"
    "v_pk_fma_f32 v[230:231], v[230:231], v[136:137], v[232:233]\n"
    "v_pk_mul_f32 v[232:233], v[232:233], v[136:137]\n"
    "v_pk_fma_f32 v[138:139], v[138:139], s[70:71], v[106:107]\n"
    "v_pk_fma_f32 v[230:231], v[230:231], v[138:139], v[232:233]\n"
    "v_pk_mul_f32 v[232:233], v[232:233], v[138:139]\n"
    "v_pk_fma_f32 v[140:141], v[140:141], s[70:71], v[108:109]\n"
    "v_pk_fma_f32 v[230:231], v[230:231], v[140:141], v[232:233]\n"
    "v_pk_mul_f32 v[232:233], v[232:233], v[140:141]\n"
    "v_pk_fma_f32 v[142:143], v[142:143], s[70:71], v[110:111]\n"
    "v_pk_fma_f32 v[230:231], v[230:231], v[142:143], v[232:233]\n"
    "v_pk_mul_f32 v[232:233], v[232:233], v[142:143]\n"
    "v_rcp_f32 v232, v232\n"
    "v_rcp_f32 v233, v233\n"
    "v_fma_f32 v230, v230, v232, v247\n"
    "v_fmac_f32 v230, v231, v233\n"
    "v_cvt_pk_bf16_f32 v230, v230, v230\n"
    "ds_write_b16 v245, v230\n"
    "ds_read_b64 v[228:229], v246\n"
    ".Ldin_drain0:\n"
    "v_add_u32 v253, s66, v244\n"
    "ds_read_b32 v253, v253\n"
    "v_lshlrev_b32 v202, 3, v249\n"
    "s_waitcnt lgkmcnt(0)\n"
    "v_mfma_f32_4x4x4_16b_bf16 v[224:227], v[228:229], v[168:169], v[224:227] cbsz:4 abid:0\n"
    "v_and_b32_e64 v201, 63, v253\n"
    "v_and_b32 v202, 0x70, v202\n"
    "v_mfma_f32_4x4x4_16b_bf16 v[224:227], v[228:229], v[170:171], v[224:227] cbsz:4 abid:1\n"
    "v_and_b32 v200, 0x3fffffc0, v253\n"
    "v_mul_u32_u24 v201, 272, v201\n"
    "v_mfma_f32_4x4x4_16b_bf16 v[224:227], v[228:229], v[172:173], v[224:227] cbsz:4 abid:2\n"
    "v_lshl_add_u32 v200, v200, 1, v202\n"
    "v_add_u32 v201, v201, v202\n"
    "v_mfma_f32_4x4x4_16b_bf16 v[224:227], v[228:229], v[174:175], v[224:227] cbsz:4 abid:3\n"
    "v_cmp_gt_u32 vcc, s57, v253\n"
    "v_bfrev_b32 v202, 1\n"
    "s_nop 0\n"
    "v_cndmask_b32 v200, v202, v200, vcc\n"
    "s_nop 0\n"
    "v_add_f32 v232, v224, v225\n"
    "v_add_f32 v233, v226, v227\n"
    "v_add_f32 v232, v232, v233\n"
    "v_add_u32 v233, s62, v249\n"
    "v_mul_f32 v232, s69, v232\n"
    "v_cvt_pk_bf16_f32 v232, v232, v232\n"
    "ds_write_b16 v233, v232 offset:32768\n"
    "ds_read_b128 v[216:219], v201 offset:32768\n"
    "s_waitcnt vmcnt(0) lgkmcnt(0)\n"
    "buffer_store_dwordx4 v[216:219], v200, s[52:55], 0 offen sc0 sc1 nt\n"
    "s_branch .Ldin_exit\n"
    ".p2align 5\n"
    ".Ldin_E1:\n"
    "v_exp_f32 v144, v144 clamp\n"
    "v_exp_f32 v145, v145 clamp\n"
    "v_exp_f32 v146, v146 clamp\n"
    "v_exp_f32 v147, v147 clamp\n"
    "v_exp_f32 v148, v148 clamp\n"
    "v_exp_f32 v149, v149 clamp\n"
    "v_exp_f32 v150, v150 clamp\n"
    "v_exp_f32 v151, v151 clamp\n"
    "s_and_b32 s62, s58, 63\n"
    "s_mul_i32 s62, s62, 272\n"
    "s_waitcnt lgkmcnt(0)\n"
    "v_mfma_f32_4x4x4_16b_bf16 v[224:227], v[228:229], v[168:169], v[224:227] cbsz:4 abid:0\n"
    "v_exp_f32 v152, v152 clamp\n"
    "v_exp_f32 v153, v153 clamp\n"
    "v_mfma_f32_4x4x4_16b_bf16 v[224:227], v[228:229], v[170:171], v[224:227] cbsz:4 abid:1\n"
    "v_exp_f32 v154, v154 clamp\n"
    "v_exp_f32 v155, v155 clamp\n"
    "v_mfma_f32_4x4x4_16b_bf16 v[224:227], v[228:229], v[172:173], v[224:227] cbsz:4 abid:2\n"
    "v_exp_f32 v156, v156 clamp\n"
    "v_exp_f32 v157, v157 clamp\n"
    "v_mfma_f32_4x4x4_16b_bf16 v[224:227], v[228:229], v[174:175], v[224:227] cbsz:4 abid:3\n"
    "v_exp_f32 v158, v158 clamp\n"
    "v_exp_f32 v159, v159 clamp\n"
    "v_pk_fma_f32 v[144:145], v[144:145], s[70:71], v[96:97]\n"
    "v_pk_fma_f32 v[146:147], v[146:147], s[70:71], v[98:99]\n"
    "v_pk_add_f32 v[230:231], v[144:145], v[146:147]\n"
    "v_pk_mul_f32 v[232:233], v[144:145], v[146:147]\n"
    "v_pk_fma_f32 v[148:149], v[148:149], s[70:71], v[100:101]\n"
    "v_pk_fma_f32 v[230:231], v[230:231], v[148:149], v[232:233]\n"
    "v_pk_mul_f32 v[232:233], v[232:233], v[148:149]\n"
    "v_pk_fma_f32 v[150:151], v[150:151], s[70:71], v[102:103]\n"
    "v_pk_fma_f32 v[230:231], v[230:231], v[150:151], v[232:233]\n"
    "v_pk_mul_f32 v[232:233], v[232:233], v[150:151]\n"
    "v_pk_fma_f32 v[152:153], v[152:153], s[70:71], v[104:105]\n"
    "v_pk_fma_f32 v[230:231], v[230:231], v[152:153], v[232:233]\n"
    "v_pk_mul_f32 v[232:233], v[232:233], v[152:153]\n"
    "v_pk_fma_f32 v[154:155], v[154:155], s[70:71], v[106:107]\n"
    "v_pk_fma_f32 v[230:231], v[230:231], v[154:155], v[232:233]\n"
    "v_pk_mul_f32 v[232:233], v[232:233], v[154:155]\n"
    "v_pk_fma_f32 v[156:157], v[156:157], s[70:71], v[108:109]\n"
    "v_pk_fma_f32 v[230:231], v[230:231], v[156:157], v[232:233]\n"
    "v_pk_mul_f32 v[232:233], v[232:233], v[156:157]\n"
    "v_pk_fma_f32 v[158:159], v[158:159], s[70:71], v[110:111]\n"
    "v_pk_fma_f32 v[230:231], v[230:231], v[158:159], v[232:233]\n"
    "v_pk_mul_f32 v[232:233], v[232:233], v[158:159]\n"
    "v_rcp_f32 v232, v232\n"
    "v_rcp_f32 v233, v233\n"
    "v_fma_f32 v230, v230, v232, v247\n"
    "v_fmac_f32 v230, v231, v233\n"
    "v_cvt_pk_bf16_f32 v230, v230, v230\n"
    "ds_write_b16 v245, v230\n"
    "ds_read_b64 v[228:229], v246\n"
    ".Ldin_drain1:\n"
    "v_add_u32 v253, s66, v244\n"
    "ds_read_b32 v253, v253\n"
    "v_lshlrev_b32 v202, 3, v249\n"
    "s_waitcnt lgkmcnt(0)\n"
    "v_mfma_f32_4x4x4_16b_bf16 v[224:227], v[228:229], v[176:177], v[224:227] cbsz:4 abid:0\n"
    "v_and_b32_e64 v201, 63, v253\n"
    "v_and_b32 v202, 0x70, v202\n"
    "v_mfma_f32_4x4x4_16b_bf16 v[224:227], v[228:229], v[178:179], v[224:227] cbsz:4 abid:1\n"
    "v_and_b32 v200, 0x3fffffc0, v253\n"
    "v_mul_u32_u24 v201, 272, v201\n"
    "v_mfma_f32_4x4x4_16b_bf16 v[224:227], v[228:229], v[180:181], v[224:227] cbsz:4 abid:2\n"
    "v_lshl_add_u32 v200, v200, 1, v202\n"
    "v_add_u32 v201, v201, v202\n"
    "v_mfma_f32_4x4x4_16b_bf16 v[224:227], v[228:229], v[182:183], v[224:227] cbsz:4 abid:3\n"
    "v_cmp_gt_u32 vcc, s57, v253\n"
    "v_bfrev_b32 v202, 1\n"
    "s_nop 0\n"
    "v_cndmask_b32 v200, v202, v200, vcc\n"
    "s_nop 0\n"
    "v_add_f32 v232, v224, v225\n"
    "v_add_f32 v233, v226, v227\n"
    "v_add_f32 v232, v232, v233\n"
    "v_add_u32 v233, s62, v249\n"
    "v_mul_f32 v232, s69, v232\n"
    "v_cvt_pk_bf16_f32 v232, v232, v232\n"
    "ds_write_b16 v233, v232 offset:32768\n"
    "ds_read_b128 v[216:219], v201 offset:32768\n"
    "s_waitcnt vmcnt(0) lgkmcnt(0)\n"
    "buffer_store_dwordx4 v[216:219], v200, s[52:55], 0 offen sc0 sc1 nt\n"
    "s_branch .Ldin_exit\n"
    ".Ldin_A0s:\n"
    "v_mfma_f32_16x16x32_bf16 v[144:147], v[0:3], v[160:163], v[112:115]\n"
    "v_exp_f32 v128, v128 clamp\n"
    "v_mfma_f32_16x16x32_bf16 v[144:147], v[4:7], v[164:167], v[144:147]\n"
    "v_exp_f32 v129, v129 clamp\n"
    "v_mfma_f32_16x16x32_bf16 v[148:151], v[8:11], v[160:163], v[116:119]\n"
    "v_exp_f32 v130, v130 clamp\n"
    "v_mfma_f32_16x16x32_bf16 v[148:151], v[12:15], v[164:167], v[148:151]\n"
    "v_exp_f32 v131, v131 clamp\n"
    "v_mfma_f32_16x16x32_bf16 v[152:155], v[16:19], v[160:163], v[120:123]\n"
    "v_exp_f32 v132, v132 clamp\n"
    "v_mfma_f32_16x16x32_bf16 v[152:155], v[20:23], v[164:167], v[152:155]\n"
    "v_exp_f32 v133, v133 clamp\n"
    "v_mfma_f32_16x16x32_bf16 v[156:159], v[24:27], v[160:163], v[124:127]\n"
    "v_exp_f32 v134, v134 clamp\n"
    "v_mfma_f32_16x16x32_bf16 v[156:159], v[28:31], v[164:167], v[156:159]\n"
    "v_exp_f32 v135, v135 clamp\n"
    "v_exp_f32 v136, v136 clamp\n"
    "s_add_u32 m0, s68, 2048\n"
    "v_exp_f32 v137, v137 clamp\n"
    "v_add_u32 v253, s67, v244\n"
    "s_waitcnt lgkmcnt(0)\n"
    "v_mfma_f32_4x4x4_16b_bf16 v[224:227], v[228:229], v[176:177], v[224:227] cbsz:4 abid:0\n"
    "v_exp_f32 v138, v138 clamp\n"
    "v_exp_f32 v139, v139 clamp\n"
    "v_mfma_f32_4x4x4_16b_bf16 v[224:227], v[228:229], v[178:179], v[224:227] cbsz:4 abid:1\n"
    "v_exp_f32 v140, v140 clamp\n"
    "v_exp_f32 v141, v141 clamp\n"
    "v_mfma_f32_4x4x4_16b_bf16 v[224:227], v[228:229], v[180:181], v[224:227] cbsz:4 abid:2\n"
    "v_exp_f32 v142, v142 clamp\n"
    "v_exp_f32 v143, v143 clamp\n"
    "v_mfma_f32_4x4x4_16b_bf16 v[224:227], v[228:229], v[182:183], v[224:227] cbsz:4 abid:3\n"
    "s_waitcnt vmcnt(4)\n"
    "ds_read_b64_tr_b16 v[176:177], v240 offset:2048\n"
    "ds_read_b64_tr_b16 v[178:179], v241 offset:2304\n"
    "ds_read_b64_tr_b16 v[180:181], v242 offset:2560\n"
    "ds_read_b64_tr_b16 v[182:183], v243 offset:2816\n"
    "ds_read_b32 v236, v253 offset:0\n"
    "ds_read_b32 v238, v253 offset:4\n"
    "ds_read_b128 v[160:163], v234 offset:4096\n"
    "ds_read_b128 v[164:167], v235 offset:4096\n"
    "s_cbranch_vccz .Ldin_A0s_np\n"
    "s_setprio 1\n"
    ".Ldin_A0s_np:\n"
    "v_pk_fma_f32 v[128:129], v[128:129], s[70:71], v[96:97]\n"
    "v_pk_fma_f32 v[130:131], v[130:131], s[70:71], v[98:99]\n"
    "v_add_f32 v191, v224, v225\n"
    "v_add_f32 v190, v226, v227\n"
    "v_add_f32 v191, v191, v190\n"
    "v_add_u32 v190, s62, v249\n"
    "v_mul_f32 v191, s69, v191\n"
    "v_cvt_pk_bf16_f32 v191, v191, v191\n"
    "ds_write_b16 v190, v191 offset:32768\n"
    "s_bfe_u32 s94, s59, 0x180006\n"
    "s_mul_i32 s86, s94, s56\n"
    "v_mbcnt_lo_u32_b32 v184, s20, 0\n"
    "v_mbcnt_lo_u32_b32 v185, s22, 0\n"
    "v_mbcnt_lo_u32_b32 v186, s24, 0\n"
    "v_mbcnt_lo_u32_b32 v187, s26, 0\n"
    "s_bcnt1_i32_b64 s90, s[20:21]\n"
    "s_bcnt1_i32_b64 s91, s[22:23]\n"
    "s_bcnt1_i32_b64 s92, s[24:25]\n"
    "s_bcnt1_i32_b64 s93, s[26:27]\n"
    "v_mbcnt_hi_u32_b32 v184, s21, v184\n"
    "v_mbcnt_hi_u32_b32 v185, s23, v185\n"
    "v_mbcnt_hi_u32_b32 v186, s25, v186\n"
    "v_mbcnt_hi_u32_b32 v187, s27, v187\n"
    "s_lshl2_add_u32 s90, s90, s65\n"
    "s_lshl2_add_u32 s91, s91, s90\n"
    "s_lshl2_add_u32 s92, s92, s91\n"
    "s_lshl2_add_u32 s93, s93, s92\n"
    "v_add_u32_e64 v188, s86, v251\n"
    "v_pk_add_f32 v[230:231], v[128:129], v[130:131]\n"
    "v_pk_mul_f32 v[232:233], v[128:129], v[130:131]\n"
    "v_pk_fma_f32 v[132:133], v[132:133], s[70:71], v[100:101]\n"
    "v_pk_fma_f32 v[230:231], v[230:231], v[132:133], v[232:233]\n"
    "v_pk_mul_f32 v[232:233], v[232:233], v[132:133]\n"
    "v_lshl_add_u32 v184, v184, 2, s65\n"
    "v_lshl_add_u32 v185, v185, 2, s90\n"
    "v_lshl_add_u32 v186, v186, 2, s91\n"
    "v_lshl_add_u32 v187, v187, 2, s92\n"
    "v_mov_b32 v191, -1\n"
    "v_lshl_add_u32 v190, v249, 1, s93\n"
    "ds_write2_b32 v190, v191, v191 offset1:64\n"
    "s_mov_b64 exec, s[20:21]\n"
    "ds_write_b32 v184, v188\n"
    "s_mov_b64 exec, s[22:23]\n"
    "v_add_u32 v189, 64, v188\n"
    "ds_write_b32 v185, v189\n"
    "s_mov_b64 exec, s[24:25]\n"
    "v_add_u32 v190, 128, v188\n"
    "ds_write_b32 v186, v190\n"
    "s_mov_b64 exec, s[26:27]\n"
    "v_add_u32 v189, 192, v188\n"
    "ds_write_b32 v187, v189\n"
    "s_mov_b64 exec, -1\n"
    "s_add_u32 s95, s93, 60\n"
    "s_add_u32 s94, s82, 128\n"
    "s_andn2_b32 s95, s95, 63\n"
    "s_max_u32 s83, s95, s94\n"
    "v_pk_fma_f32 v[134:135], v[134:135], s[70:71], v[102:103]\n"
    "v_pk_fma_f32 v[230:231], v[230:231], v[134:135], v[232:233]\n"
    "v_pk_mul_f32 v[232:233], v[232:233], v[134:135]\n"
    "v_pk_fma_f32 v[136:137], v[136:137], s[70:71], v[104:105]\n"
    "v_pk_fma_f32 v[230:231], v[230:231], v[136:137], v[232:233]\n"
    "v_pk_mul_f32 v[232:233], v[232:233], v[136:137]\n"
    "v_pk_fma_f32 v[138:139], v[138:139], s[70:71], v[106:107]\n"
    "v_pk_fma_f32 v[230:231], v[230:231], v[138:139], v[232:233]\n"
    "v_pk_mul_f32 v[232:233], v[232:233], v[138:139]\n"
    "v_pk_fma_f32 v[140:141], v[140:141], s[70:71], v[108:109]\n"
    "v_pk_fma_f32 v[230:231], v[230:231], v[140:141], v[232:233]\n"
    "v_pk_mul_f32 v[232:233], v[232:233], v[140:141]\n"
    "v_pk_fma_f32 v[142:143], v[142:143], s[70:71], v[110:111]\n"
    "v_pk_fma_f32 v[230:231], v[230:231], v[142:143], v[232:233]\n"
    "v_pk_mul_f32 v[232:233], v[232:233], v[142:143]\n"
    "v_rcp_f32 v232, v232\n"
    "v_rcp_f32 v233, v233\n"
    "v_fma_f32 v230, v230, v232, v247\n"
    "v_fmac_f32 v230, v231, v233\n"
    "v_lshlrev_b32 v232, 2, v249\n"
    "v_lshrrev_b32 v233, 2, v249\n"
    "v_and_b32 v232, 56, v232\n"
    "v_and_b32 v233, 4, v233\n"
    "v_add3_u32 v232, v232, v233, s81\n"
    "ds_read_b32 v232, v232\n"
    "s_add_u32 s81, s81, 64\n"
    "s_waitcnt lgkmcnt(0)\n"
    "v_lshlrev_b32 v232, 1, v232\n"
    "buffer_load_ushort v232, v232, s[44:47], 0 offen\n"
    "s_waitcnt vmcnt(0)\n"
    "v_lshlrev_b32 v232, 16, v232\n"
    "v_mul_f32 v230, v230, v232\n"
    "v_cvt_pk_bf16_f32 v230, v230, v230\n"
    "s_add_u32 s67, s67, 64\n"
    "s_waitcnt lgkmcnt(0)\n"
    "ds_write_b16 v245, v230\n"
    "ds_read_b64 v[228:229], v246\n"
    "buffer_load_dwordx4 v[236:237], s[40:43], 0 idxen offen offset:0 nt lds\n"
    "buffer_load_dwordx4 v[238:239], s[40:43], 0 idxen offen offset:1024 nt lds\n"
    ".Ldin_B1s:\n"
    "v_mfma_f32_16x16x32_bf16 v[128:131], v[0:3], v[160:163], v[112:115]\n"
    "v_exp_f32 v144, v144 clamp\n"
    "v_mfma_f32_16x16x32_bf16 v[128:131], v[4:7], v[164:167], v[128:131]\n"
    "v_exp_f32 v145, v145 clamp\n"
    "v_mfma_f32_16x16x32_bf16 v[132:135], v[8:11], v[160:163], v[116:119]\n"
    "v_exp_f32 v146, v146 clamp\n"
    "v_mfma_f32_16x16x32_bf16 v[132:135], v[12:15], v[164:167], v[132:135]\n"
    "v_exp_f32 v147, v147 clamp\n"
    "v_mfma_f32_16x16x32_bf16 v[136:139], v[16:19], v[160:163], v[120:123]\n"
    "v_exp_f32 v148, v148 clamp\n"
    "v_mfma_f32_16x16x32_bf16 v[136:139], v[20:23], v[164:167], v[136:139]\n"
    "v_exp_f32 v149, v149 clamp\n"
    "v_mfma_f32_16x16x32_bf16 v[140:143], v[24:27], v[160:163], v[124:127]\n"
    "v_exp_f32 v150, v150 clamp\n"
    "v_mfma_f32_16x16x32_bf16 v[140:143], v[28:31], v[164:167], v[140:143]\n"
    "v_exp_f32 v151, v151 clamp\n"
    "v_exp_f32 v152, v152 clamp\n"
    "s_cmp_eq_u32 s67, s80\n"
    "s_cselect_b32 s67, s65, s67\n"
    "s_and_b32 s94, s59, 63\n"
    "s_mul_i32 s94, s94, 272\n"
    "v_add_u32 v252, s94, v250\n"
    "s_add_u32 m0, s68, 4096\n"
    "v_exp_f32 v153, v153 clamp\n"
    "v_add_u32 v253, s67, v244\n"
    "s_waitcnt lgkmcnt(0)\n"
    "v_mfma_f32_4x4x4_16b_bf16 v[224:227], v[228:229], v[168:169], 0 cbsz:4 abid:0\n"
    "v_exp_f32 v154, v154 clamp\n"
    "v_exp_f32 v155, v155 clamp\n"
    "v_mfma_f32_4x4x4_16b_bf16 v[224:227], v[228:229], v[170:171], v[224:227] cbsz:4 abid:1\n"
    "v_exp_f32 v156, v156 clamp\n"
    "v_exp_f32 v157, v157 clamp\n"
    "v_mfma_f32_4x4x4_16b_bf16 v[224:227], v[228:229], v[172:173], v[224:227] cbsz:4 abid:2\n"
    "v_exp_f32 v158, v158 clamp\n"
    "v_exp_f32 v159, v159 clamp\n"
    "v_mfma_f32_4x4x4_16b_bf16 v[224:227], v[228:229], v[174:175], v[224:227] cbsz:4 abid:3\n"
    "s_waitcnt vmcnt(4)\n"
    "s_memrealtime s[34:35]\n"
    "ds_read_b64_tr_b16 v[168:169], v240 offset:4096\n"
    "ds_read_b64_tr_b16 v[170:171], v241 offset:4352\n"
    "ds_read_b64_tr_b16 v[172:173], v242 offset:4608\n"
    "ds_read_b64_tr_b16 v[174:175], v243 offset:4864\n"
    "ds_read_b32 v236, v253 offset:0\n"
    "ds_read_b32 v238, v253 offset:4\n"
    "ds_read_b128 v[160:163], v234 offset:6144\n"
    "ds_read_b128 v[164:167], v235 offset:6144\n"
    "ds_read_b128 v[184:187], v252 offset:0\n"
    "ds_read_b128 v[188:191], v252 offset:16\n"
    "v_pk_fma_f32 v[144:145], v[144:145], s[70:71], v[96:97]\n"
    "v_pk_fma_f32 v[146:147], v[146:147], s[70:71], v[98:99]\n"
    "v_pk_add_f32 v[230:231], v[144:145], v[146:147]\n"
    "v_pk_mul_f32 v[232:233], v[144:145], v[146:147]\n"
    "v_pk_fma_f32 v[148:149], v[148:149], s[70:71], v[100:101]\n"
    "v_pk_fma_f32 v[230:231], v[230:231], v[148:149], v[232:233]\n"
    "v_pk_mul_f32 v[232:233], v[232:233], v[148:149]\n"
    "v_pk_fma_f32 v[150:151], v[150:151], s[70:71], v[102:103]\n"
    "v_pk_fma_f32 v[230:231], v[230:231], v[150:151], v[232:233]\n"
    "v_pk_mul_f32 v[232:233], v[232:233], v[150:151]\n"
    "v_pk_fma_f32 v[152:153], v[152:153], s[70:71], v[104:105]\n"
    "v_pk_fma_f32 v[230:231], v[230:231], v[152:153], v[232:233]\n"
    "v_pk_mul_f32 v[232:233], v[232:233], v[152:153]\n"
    "v_pk_fma_f32 v[154:155], v[154:155], s[70:71], v[106:107]\n"
    "v_pk_fma_f32 v[230:231], v[230:231], v[154:155], v[232:233]\n"
    "v_pk_mul_f32 v[232:233], v[232:233], v[154:155]\n"
    "v_pk_fma_f32 v[156:157], v[156:157], s[70:71], v[108:109]\n"
    "v_pk_fma_f32 v[230:231], v[230:231], v[156:157], v[232:233]\n"
    "v_pk_mul_f32 v[232:233], v[232:233], v[156:157]\n"
    "v_pk_fma_f32 v[158:159], v[158:159], s[70:71], v[110:111]\n"
    "v_pk_fma_f32 v[230:231], v[230:231], v[158:159], v[232:233]\n"
    "v_pk_mul_f32 v[232:233], v[232:233], v[158:159]\n"
    "v_rcp_f32 v232, v232\n"
    "v_rcp_f32 v233, v233\n"
    "v_fma_f32 v230, v230, v232, v247\n"
    "v_fmac_f32 v230, v231, v233\n"
    "v_lshlrev_b32 v232, 2, v249\n"
    "v_lshrrev_b32 v233, 2, v249\n"
    "v_and_b32 v232, 56, v232\n"
    "v_and_b32 v233, 4, v233\n"
    "v_add3_u32 v232, v232, v233, s81\n"
    "ds_read_b32 v232, v232\n"
    "s_add_u32 s81, s81, 64\n"
    "s_waitcnt lgkmcnt(0)\n"
    "v_lshlrev_b32 v232, 1, v232\n"
    "buffer_load_ushort v232, v232, s[44:47], 0 offen\n"
    "s_waitcnt vmcnt(0)\n"
    "v_lshlrev_b32 v232, 16, v232\n"
    "v_mul_f32 v230, v230, v232\n"
    "v_cvt_pk_bf16_f32 v230, v230, v230\n"
    "s_add_u32 s67, s67, 64\n"
    "s_waitcnt lgkmcnt(0)\n"
    "ds_write_b16 v245, v230\n"
    "ds_read_b64 v[228:229], v246\n"
    "s_cmp_le_u32 s34, s76\n"
    "s_cbranch_scc1 .Ldin_B1s_wait\n"
    ".Ldin_B1s_go:\n"
    "s_sub_u32 s94, s34, 60\n"
    "s_max_u32 s76, s76, s94\n"
    "s_add_u32 s76, s76, s72\n"
    ".Ldin_B1s_dma:\n"
    "buffer_load_dwordx4 v[236:237], s[40:43], 0 idxen offen offset:0 nt lds\n"
    "buffer_load_dwordx4 v[238:239], s[40:43], 0 idxen offen offset:1024 nt lds\n"
    ".Ldin_B1s_nd:\n"
    ".Ldin_G2s:\n"
    "v_mfma_f32_16x16x32_bf16 v[144:147], v[0:3], v[160:163], v[112:115]\n"
    "v_exp_f32 v128, v128 clamp\n"
    "v_mfma_f32_16x16x32_bf16 v[144:147], v[4:7], v[164:167], v[144:147]\n"
    "v_exp_f32 v129, v129 clamp\n"
    "v_mfma_f32_16x16x32_bf16 v[148:151], v[8:11], v[160:163], v[116:119]\n"
    "v_exp_f32 v130, v130 clamp\n"
    "v_mfma_f32_16x16x32_bf16 v[148:151], v[12:15], v[164:167], v[148:151]\n"
    "v_exp_f32 v131, v131 clamp\n"
    "v_mfma_f32_16x16x32_bf16 v[152:155], v[16:19], v[160:163], v[120:123]\n"
    "v_exp_f32 v132, v132 clamp\n"
    "v_mfma_f32_16x16x32_bf16 v[152:155], v[20:23], v[164:167], v[152:155]\n"
    "v_exp_f32 v133, v133 clamp\n"
    "v_mfma_f32_16x16x32_bf16 v[156:159], v[24:27], v[160:163], v[124:127]\n"
    "v_exp_f32 v134, v134 clamp\n"
    "v_mfma_f32_16x16x32_bf16 v[156:159], v[28:31], v[164:167], v[156:159]\n"
    "v_exp_f32 v135, v135 clamp\n"
    "v_exp_f32 v136, v136 clamp\n"
    "s_cmp_eq_u32 s67, s80\n"
    "s_cselect_b32 s67, s65, s67\n"
    "s_add_u32 m0, s68, 6144\n"
    "v_exp_f32 v137, v137 clamp\n"
    "v_add_u32_e64 v253, s67, v244\n"
    "s_waitcnt lgkmcnt(0)\n"
    "v_mfma_f32_4x4x4_16b_bf16 v[224:227], v[228:229], v[176:177], v[224:227] cbsz:4 abid:0\n"
    "v_exp_f32 v138, v138 clamp\n"
    "v_exp_f32 v139, v139 clamp\n"
    "v_mfma_f32_4x4x4_16b_bf16 v[224:227], v[228:229], v[178:179], v[224:227] cbsz:4 abid:1\n"
    "v_exp_f32 v140, v140 clamp\n"
    "v_exp_f32 v141, v141 clamp\n"
    "v_mfma_f32_4x4x4_16b_bf16 v[224:227], v[228:229], v[180:181], v[224:227] cbsz:4 abid:2\n"
    "v_exp_f32 v142, v142 clamp\n"
    "v_exp_f32 v143, v143 clamp\n"
    "v_mfma_f32_4x4x4_16b_bf16 v[224:227], v[228:229], v[182:183], v[224:227] cbsz:4 abid:3\n"
    "s_waitcnt vmcnt(4)\n"
    "s_memrealtime s[34:35]\n"
    "ds_read_b64_tr_b16 v[176:177], v240 offset:6144\n"
    "ds_read_b64_tr_b16 v[178:179], v241 offset:6400\n"
    "ds_read_b64_tr_b16 v[180:181], v242 offset:6656\n"
    "ds_read_b64_tr_b16 v[182:183], v243 offset:6912\n"
    "ds_read_b32 v236, v253 offset:0\n"
    "ds_read_b32 v238, v253 offset:4\n"
    "ds_read_b128 v[160:163], v234 offset:0\n"
    "ds_read_b128 v[164:167], v235 offset:0\n"
    "v_pk_fma_f32 v[128:129], v[128:129], s[70:71], v[96:97]\n"
    "v_pk_fma_f32 v[130:131], v[130:131], s[70:71], v[98:99]\n"
    "v_pk_add_f32 v[230:231], v[128:129], v[130:131]\n"
    "v_pk_mul_f32 v[232:233], v[128:129], v[130:131]\n"
    "v_pk_fma_f32 v[132:133], v[132:133], s[70:71], v[100:101]\n"
    "v_pk_fma_f32 v[230:231], v[230:231], v[132:133], v[232:233]\n"
    "v_pk_mul_f32 v[232:233], v[232:233], v[132:133]\n"
    "v_pk_fma_f32 v[134:135], v[134:135], s[70:71], v[102:103]\n"
    "v_pk_fma_f32 v[230:231], v[230:231], v[134:135], v[232:233]\n"
    "v_pk_mul_f32 v[232:233], v[232:233], v[134:135]\n"
    "v_pk_fma_f32 v[136:137], v[136:137], s[70:71], v[104:105]\n"
    "v_pk_fma_f32 v[230:231], v[230:231], v[136:137], v[232:233]\n"
    "v_pk_mul_f32 v[232:233], v[232:233], v[136:137]\n"
    "v_pk_fma_f32 v[138:139], v[138:139], s[70:71], v[106:107]\n"
    "v_pk_fma_f32 v[230:231], v[230:231], v[138:139], v[232:233]\n"
    "v_pk_mul_f32 v[232:233], v[232:233], v[138:139]\n"
    "v_pk_fma_f32 v[140:141], v[140:141], s[70:71], v[108:109]\n"
    "v_pk_fma_f32 v[230:231], v[230:231], v[140:141], v[232:233]\n"
    "v_pk_mul_f32 v[232:233], v[232:233], v[140:141]\n"
    "v_pk_fma_f32 v[142:143], v[142:143], s[70:71], v[110:111]\n"
    "v_pk_fma_f32 v[230:231], v[230:231], v[142:143], v[232:233]\n"
    "v_pk_mul_f32 v[232:233], v[232:233], v[142:143]\n"
    "v_rcp_f32 v232, v232\n"
    "v_rcp_f32 v233, v233\n"
    "v_fma_f32 v230, v230, v232, v247\n"
    "v_fmac_f32 v230, v231, v233\n"
    "v_lshlrev_b32 v232, 2, v249\n"
    "v_lshrrev_b32 v233, 2, v249\n"
    "v_and_b32 v232, 56, v232\n"
    "v_and_b32 v233, 4, v233\n"
    "v_add3_u32 v232, v232, v233, s81\n"
    "ds_read_b32 v232, v232\n"
    "s_add_u32 s81, s81, 64\n"
    "s_waitcnt lgkmcnt(0)\n"
    "v_lshlrev_b32 v232, 1, v232\n"
    "buffer_load_ushort v232, v232, s[44:47], 0 offen\n"
    "s_waitcnt vmcnt(0)\n"
    "v_lshlrev_b32 v232, 16, v232\n"
    "v_mul_f32 v230, v230, v232\n"
    "v_cvt_pk_bf16_f32 v230, v230, v230\n"
    "s_add_u32 s67, s67, 64\n"
    "s_waitcnt lgkmcnt(0)\n"
    "ds_write_b16 v245, v230\n"
    "ds_read_b64 v[228:229], v246\n"
    "s_cmp_le_u32 s34, s76\n"
    "s_cbranch_scc1 .Ldin_G2s_wait\n"
    ".Ldin_G2s_go:\n"
    "s_sub_u32 s94, s34, 60\n"
    "s_max_u32 s76, s76, s94\n"
    "s_add_u32 s76, s76, s72\n"
    ".Ldin_G2s_dma:\n"
    "buffer_load_dwordx4 v[236:237], s[40:43], 0 idxen offen offset:0 nt lds\n"
    "buffer_load_dwordx4 v[238:239], s[40:43], 0 idxen offen offset:1024 nt lds\n"
    ".Ldin_G2s_nd:\n"
    "s_cmp_eq_u32 s67, s82\n"
    "s_cbranch_scc1 .Ldin_L3s\n"
    ".Ldin_G3s:\n"
    "v_mfma_f32_16x16x32_bf16 v[128:131], v[0:3], v[160:163], v[112:115]\n"
    "v_exp_f32 v144, v144 clamp\n"
    "v_mfma_f32_16x16x32_bf16 v[128:131], v[4:7], v[164:167], v[128:131]\n"
    "v_exp_f32 v145, v145 clamp\n"
    "v_mfma_f32_16x16x32_bf16 v[132:135], v[8:11], v[160:163], v[116:119]\n"
    "v_exp_f32 v146, v146 clamp\n"
    "v_mfma_f32_16x16x32_bf16 v[132:135], v[12:15], v[164:167], v[132:135]\n"
    "v_exp_f32 v147, v147 clamp\n"
    "v_mfma_f32_16x16x32_bf16 v[136:139], v[16:19], v[160:163], v[120:123]\n"
    "v_exp_f32 v148, v148 clamp\n"
    "v_mfma_f32_16x16x32_bf16 v[136:139], v[20:23], v[164:167], v[136:139]\n"
    "v_exp_f32 v149, v149 clamp\n"
    "v_mfma_f32_16x16x32_bf16 v[140:143], v[24:27], v[160:163], v[124:127]\n"
    "v_exp_f32 v150, v150 clamp\n"
    "v_mfma_f32_16x16x32_bf16 v[140:143], v[28:31], v[164:167], v[140:143]\n"
    "v_exp_f32 v151, v151 clamp\n"
    "v_exp_f32 v152, v152 clamp\n"
    "s_cmp_eq_u32 s67, s80\n"
    "s_cselect_b32 s67, s65, s67\n"
    "s_add_u32 m0, s68, 0\n"
    "v_exp_f32 v153, v153 clamp\n"
    "v_add_u32 v253, s67, v244\n"
    "s_waitcnt lgkmcnt(0)\n"
    "v_mfma_f32_4x4x4_16b_bf16 v[224:227], v[228:229], v[168:169], v[224:227] cbsz:4 abid:0\n"
    "v_exp_f32 v154, v154 clamp\n"
    "v_exp_f32 v155, v155 clamp\n"
    "v_mfma_f32_4x4x4_16b_bf16 v[224:227], v[228:229], v[170:171], v[224:227] cbsz:4 abid:1\n"
    "v_exp_f32 v156, v156 clamp\n"
    "v_exp_f32 v157, v157 clamp\n"
    "v_mfma_f32_4x4x4_16b_bf16 v[224:227], v[228:229], v[172:173], v[224:227] cbsz:4 abid:2\n"
    "v_exp_f32 v158, v158 clamp\n"
    "v_exp_f32 v159, v159 clamp\n"
    "v_mfma_f32_4x4x4_16b_bf16 v[224:227], v[228:229], v[174:175], v[224:227] cbsz:4 abid:3\n"
    "s_waitcnt vmcnt(4)\n"
    "s_memrealtime s[34:35]\n"
    "ds_read_b64_tr_b16 v[168:169], v240 offset:0\n"
    "ds_read_b64_tr_b16 v[170:171], v241 offset:256\n"
    "ds_read_b64_tr_b16 v[172:173], v242 offset:512\n"
    "ds_read_b64_tr_b16 v[174:175], v243 offset:768\n"
    "ds_read_b32 v236, v253 offset:0\n"
    "ds_read_b32 v238, v253 offset:4\n"
    "ds_read_b128 v[160:163], v234 offset:2048\n"
    "ds_read_b128 v[164:167], v235 offset:2048\n"
    "v_pk_fma_f32 v[144:145], v[144:145], s[70:71], v[96:97]\n"
    "v_pk_fma_f32 v[146:147], v[146:147], s[70:71], v[98:99]\n"
    "v_pk_add_f32 v[230:231], v[144:145], v[146:147]\n"
    "v_pk_mul_f32 v[232:233], v[144:145], v[146:147]\n"
    "v_pk_fma_f32 v[148:149], v[148:149], s[70:71], v[100:101]\n"
    "v_pk_fma_f32 v[230:231], v[230:231], v[148:149], v[232:233]\n"
    "v_pk_mul_f32 v[232:233], v[232:233], v[148:149]\n"
    "v_pk_fma_f32 v[150:151], v[150:151], s[70:71], v[102:103]\n"
    "v_pk_fma_f32 v[230:231], v[230:231], v[150:151], v[232:233]\n"
    "v_pk_mul_f32 v[232:233], v[232:233], v[150:151]\n"
    "v_pk_fma_f32 v[152:153], v[152:153], s[70:71], v[104:105]\n"
    "v_pk_fma_f32 v[230:231], v[230:231], v[152:153], v[232:233]\n"
    "v_pk_mul_f32 v[232:233], v[232:233], v[152:153]\n"
    "v_pk_fma_f32 v[154:155], v[154:155], s[70:71], v[106:107]\n"
    "v_pk_fma_f32 v[230:231], v[230:231], v[154:155], v[232:233]\n"
    "v_pk_mul_f32 v[232:233], v[232:233], v[154:155]\n"
    "v_pk_fma_f32 v[156:157], v[156:157], s[70:71], v[108:109]\n"
    "v_pk_fma_f32 v[230:231], v[230:231], v[156:157], v[232:233]\n"
    "v_pk_mul_f32 v[232:233], v[232:233], v[156:157]\n"
    "v_pk_fma_f32 v[158:159], v[158:159], s[70:71], v[110:111]\n"
    "v_pk_fma_f32 v[230:231], v[230:231], v[158:159], v[232:233]\n"
    "v_pk_mul_f32 v[232:233], v[232:233], v[158:159]\n"
    "v_rcp_f32 v232, v232\n"
    "v_rcp_f32 v233, v233\n"
    "v_fma_f32 v230, v230, v232, v247\n"
    "v_fmac_f32 v230, v231, v233\n"
    "v_lshlrev_b32 v232, 2, v249\n"
    "v_lshrrev_b32 v233, 2, v249\n"
    "v_and_b32 v232, 56, v232\n"
    "v_and_b32 v233, 4, v233\n"
    "v_add3_u32 v232, v232, v233, s81\n"
    "ds_read_b32 v232, v232\n"
    "s_add_u32 s81, s81, 64\n"
    "s_waitcnt lgkmcnt(0)\n"
    "v_lshlrev_b32 v232, 1, v232\n"
    "buffer_load_ushort v232, v232, s[44:47], 0 offen\n"
    "s_waitcnt vmcnt(0)\n"
    "v_lshlrev_b32 v232, 16, v232\n"
    "v_mul_f32 v230, v230, v232\n"
    "v_cvt_pk_bf16_f32 v230, v230, v230\n"
    "s_add_u32 s67, s67, 64\n"
    "s_waitcnt lgkmcnt(0)\n"
    "ds_write_b16 v245, v230\n"
    "ds_read_b64 v[228:229], v246\n"
    "s_cmp_le_u32 s34, s76\n"
    "s_cbranch_scc1 .Ldin_G3s_wait\n"
    ".Ldin_G3s_go:\n"
    "s_add_u32 s76, s76, s72\n"
    ".Ldin_G3s_dma:\n"
    "buffer_load_dwordx4 v[236:237], s[40:43], 0 idxen offen offset:0 nt lds\n"
    "buffer_load_dwordx4 v[238:239], s[40:43], 0 idxen offen offset:1024 nt lds\n"
    ".Ldin_G3s_nd:\n"
    "s_cmp_eq_u32 s67, s82\n"
    "s_cbranch_scc1 .Ldin_L0s\n"
    ".Ldin_G0s:\n"
    "v_mfma_f32_16x16x32_bf16 v[144:147], v[0:3], v[160:163], v[112:115]\n"
    "v_exp_f32 v128, v128 clamp\n"
    "v_mfma_f32_16x16x32_bf16 v[144:147], v[4:7], v[164:167], v[144:147]\n"
    "v_exp_f32 v129, v129 clamp\n"
    "v_mfma_f32_16x16x32_bf16 v[148:151], v[8:11], v[160:163], v[116:119]\n"
    "v_exp_f32 v130, v130 clamp\n"
    "v_mfma_f32_16x16x32_bf16 v[148:151], v[12:15], v[164:167], v[148:151]\n"
    "v_exp_f32 v131, v131 clamp\n"
    "v_mfma_f32_16x16x32_bf16 v[152:155], v[16:19], v[160:163], v[120:123]\n"
    "v_exp_f32 v132, v132 clamp\n"
    "v_mfma_f32_16x16x32_bf16 v[152:155], v[20:23], v[164:167], v[152:155]\n"
    "v_exp_f32 v133, v133 clamp\n"
    "v_mfma_f32_16x16x32_bf16 v[156:159], v[24:27], v[160:163], v[124:127]\n"
    "v_exp_f32 v134, v134 clamp\n"
    "v_mfma_f32_16x16x32_bf16 v[156:159], v[28:31], v[164:167], v[156:159]\n"
    "v_exp_f32 v135, v135 clamp\n"
    "v_exp_f32 v136, v136 clamp\n"
    "s_cmp_eq_u32 s67, s80\n"
    "s_cselect_b32 s67, s65, s67\n"
    "s_add_u32 m0, s68, 2048\n"
    "v_exp_f32 v137, v137 clamp\n"
    "v_add_u32_e64 v253, s67, v244\n"
    "s_waitcnt lgkmcnt(0)\n"
    "v_mfma_f32_4x4x4_16b_bf16 v[224:227], v[228:229], v[176:177], v[224:227] cbsz:4 abid:0\n"
    "v_exp_f32 v138, v138 clamp\n"
    "v_exp_f32 v139, v139 clamp\n"
    "v_mfma_f32_4x4x4_16b_bf16 v[224:227], v[228:229], v[178:179], v[224:227] cbsz:4 abid:1\n"
    "v_exp_f32 v140, v140 clamp\n"
    "v_exp_f32 v141, v141 clamp\n"
    "v_mfma_f32_4x4x4_16b_bf16 v[224:227], v[228:229], v[180:181], v[224:227] cbsz:4 abid:2\n"
    "v_exp_f32 v142, v142 clamp\n"
    "v_exp_f32 v143, v143 clamp\n"
    "v_mfma_f32_4x4x4_16b_bf16 v[224:227], v[228:229], v[182:183], v[224:227] cbsz:4 abid:3\n"
    "s_waitcnt vmcnt(4)\n"
    "s_memrealtime s[34:35]\n"
    "ds_read_b64_tr_b16 v[176:177], v240 offset:2048\n"
    "ds_read_b64_tr_b16 v[178:179], v241 offset:2304\n"
    "ds_read_b64_tr_b16 v[180:181], v242 offset:2560\n"
    "ds_read_b64_tr_b16 v[182:183], v243 offset:2816\n"
    "ds_read_b32 v236, v253 offset:0\n"
    "ds_read_b32 v238, v253 offset:4\n"
    "ds_read_b128 v[160:163], v234 offset:4096\n"
    "ds_read_b128 v[164:167], v235 offset:4096\n"
    "v_pk_fma_f32 v[128:129], v[128:129], s[70:71], v[96:97]\n"
    "v_pk_fma_f32 v[130:131], v[130:131], s[70:71], v[98:99]\n"
    "v_pk_add_f32 v[230:231], v[128:129], v[130:131]\n"
    "v_pk_mul_f32 v[232:233], v[128:129], v[130:131]\n"
    "v_pk_fma_f32 v[132:133], v[132:133], s[70:71], v[100:101]\n"
    "v_pk_fma_f32 v[230:231], v[230:231], v[132:133], v[232:233]\n"
    "v_pk_mul_f32 v[232:233], v[232:233], v[132:133]\n"
    "v_pk_fma_f32 v[134:135], v[134:135], s[70:71], v[102:103]\n"
    "v_pk_fma_f32 v[230:231], v[230:231], v[134:135], v[232:233]\n"
    "v_pk_mul_f32 v[232:233], v[232:233], v[134:135]\n"
    "v_pk_fma_f32 v[136:137], v[136:137], s[70:71], v[104:105]\n"
    "v_pk_fma_f32 v[230:231], v[230:231], v[136:137], v[232:233]\n"
    "v_pk_mul_f32 v[232:233], v[232:233], v[136:137]\n"
    "v_pk_fma_f32 v[138:139], v[138:139], s[70:71], v[106:107]\n"
    "v_pk_fma_f32 v[230:231], v[230:231], v[138:139], v[232:233]\n"
    "v_pk_mul_f32 v[232:233], v[232:233], v[138:139]\n"
    "v_pk_fma_f32 v[140:141], v[140:141], s[70:71], v[108:109]\n"
    "v_pk_fma_f32 v[230:231], v[230:231], v[140:141], v[232:233]\n"
    "v_pk_mul_f32 v[232:233], v[232:233], v[140:141]\n"
    "v_pk_fma_f32 v[142:143], v[142:143], s[70:71], v[110:111]\n"
    "v_pk_fma_f32 v[230:231], v[230:231], v[142:143], v[232:233]\n"
    "v_pk_mul_f32 v[232:233], v[232:233], v[142:143]\n"
    "v_rcp_f32 v232, v232\n"
    "v_rcp_f32 v233, v233\n"
    "v_fma_f32 v230, v230, v232, v247\n"
    "v_fmac_f32 v230, v231, v233\n"
    "v_lshlrev_b32 v232, 2, v249\n"
    "v_lshrrev_b32 v233, 2, v249\n"
    "v_and_b32 v232, 56, v232\n"
    "v_and_b32 v233, 4, v233\n"
    "v_add3_u32 v232, v232, v233, s81\n"
    "ds_read_b32 v232, v232\n"
    "s_add_u32 s81, s81, 64\n"
    "s_waitcnt lgkmcnt(0)\n"
    "v_lshlrev_b32 v232, 1, v232\n"
    "buffer_load_ushort v232, v232, s[44:47], 0 offen\n"
    "s_waitcnt vmcnt(0)\n"
    "v_lshlrev_b32 v232, 16, v232\n"
    "v_mul_f32 v230, v230, v232\n"
    "v_cvt_pk_bf16_f32 v230, v230, v230\n"
    "s_add_u32 s67, s67, 64\n"
    "s_waitcnt lgkmcnt(0)\n"
    "ds_write_b16 v245, v230\n"
    "ds_read_b64 v[228:229], v246\n"
    "s_cmp_le_u32 s34, s76\n"
    "s_cbranch_scc1 .Ldin_G0s_wait\n"
    ".Ldin_G0s_go:\n"
    "s_sub_u32 s94, s34, 60\n"
    "s_max_u32 s76, s76, s94\n"
    "s_add_u32 s76, s76, s72\n"
    ".Ldin_G0s_dma:\n"
    "buffer_load_dwordx4 v[236:237], s[40:43], 0 idxen offen offset:0 nt lds\n"
    "buffer_load_dwordx4 v[238:239], s[40:43], 0 idxen offen offset:1024 nt lds\n"
    ".Ldin_G0s_nd:\n"
    "s_cmp_eq_u32 s67, s82\n"
    "s_cbranch_scc1 .Ldin_L1s\n"
    ".Ldin_G1s:\n"
    "v_mfma_f32_16x16x32_bf16 v[128:131], v[0:3], v[160:163], v[112:115]\n"
    "v_exp_f32 v144, v144 clamp\n"
    "v_mfma_f32_16x16x32_bf16 v[128:131], v[4:7], v[164:167], v[128:131]\n"
    "v_exp_f32 v145, v145 clamp\n"
    "v_mfma_f32_16x16x32_bf16 v[132:135], v[8:11], v[160:163], v[116:119]\n"
    "v_exp_f32 v146, v146 clamp\n"
    "v_mfma_f32_16x16x32_bf16 v[132:135], v[12:15], v[164:167], v[132:135]\n"
    "v_exp_f32 v147, v147 clamp\n"
    "v_mfma_f32_16x16x32_bf16 v[136:139], v[16:19], v[160:163], v[120:123]\n"
    "v_exp_f32 v148, v148 clamp\n"
    "v_mfma_f32_16x16x32_bf16 v[136:139], v[20:23], v[164:167], v[136:139]\n"
    "v_exp_f32 v149, v149 clamp\n"
    "v_mfma_f32_16x16x32_bf16 v[140:143], v[24:27], v[160:163], v[124:127]\n"
    "v_exp_f32 v150, v150 clamp\n"
    "v_mfma_f32_16x16x32_bf16 v[140:143], v[28:31], v[164:167], v[140:143]\n"
    "v_exp_f32 v151, v151 clamp\n"
    "v_exp_f32 v152, v152 clamp\n"
    "s_cmp_eq_u32 s67, s80\n"
    "s_cselect_b32 s67, s65, s67\n"
    "s_add_u32 m0, s68, 4096\n"
    "v_exp_f32 v153, v153 clamp\n"
    "v_add_u32_e64 v253, s67, v244\n"
    "s_waitcnt lgkmcnt(0)\n"
    "v_mfma_f32_4x4x4_16b_bf16 v[224:227], v[228:229], v[168:169], v[224:227] cbsz:4 abid:0\n"
    "v_exp_f32 v154, v154 clamp\n"
    "v_exp_f32 v155, v155 clamp\n"
    "v_mfma_f32_4x4x4_16b_bf16 v[224:227], v[228:229], v[170:171], v[224:227] cbsz:4 abid:1\n"
    "v_exp_f32 v156, v156 clamp\n"
    "v_exp_f32 v157, v157 clamp\n"
    "v_mfma_f32_4x4x4_16b_bf16 v[224:227], v[228:229], v[172:173], v[224:227] cbsz:4 abid:2\n"
    "v_exp_f32 v158, v158 clamp\n"
    "v_exp_f32 v159, v159 clamp\n"
    "v_mfma_f32_4x4x4_16b_bf16 v[224:227], v[228:229], v[174:175], v[224:227] cbsz:4 abid:3\n"
    "s_waitcnt vmcnt(4)\n"
    "s_memrealtime s[34:35]\n"
    "ds_read_b64_tr_b16 v[168:169], v240 offset:4096\n"
    "ds_read_b64_tr_b16 v[170:171], v241 offset:4352\n"
    "ds_read_b64_tr_b16 v[172:173], v242 offset:4608\n"
    "ds_read_b64_tr_b16 v[174:175], v243 offset:4864\n"
    "ds_read_b32 v236, v253 offset:0\n"
    "ds_read_b32 v238, v253 offset:4\n"
    "ds_read_b128 v[160:163], v234 offset:6144\n"
    "ds_read_b128 v[164:167], v235 offset:6144\n"
    "v_pk_fma_f32 v[144:145], v[144:145], s[70:71], v[96:97]\n"
    "v_pk_fma_f32 v[146:147], v[146:147], s[70:71], v[98:99]\n"
    "v_pk_add_f32 v[230:231], v[144:145], v[146:147]\n"
    "v_pk_mul_f32 v[232:233], v[144:145], v[146:147]\n"
    "v_pk_fma_f32 v[148:149], v[148:149], s[70:71], v[100:101]\n"
    "v_pk_fma_f32 v[230:231], v[230:231], v[148:149], v[232:233]\n"
    "v_pk_mul_f32 v[232:233], v[232:233], v[148:149]\n"
    "v_pk_fma_f32 v[150:151], v[150:151], s[70:71], v[102:103]\n"
    "v_pk_fma_f32 v[230:231], v[230:231], v[150:151], v[232:233]\n"
    "v_pk_mul_f32 v[232:233], v[232:233], v[150:151]\n"
    "v_pk_fma_f32 v[152:153], v[152:153], s[70:71], v[104:105]\n"
    "v_pk_fma_f32 v[230:231], v[230:231], v[152:153], v[232:233]\n"
    "v_pk_mul_f32 v[232:233], v[232:233], v[152:153]\n"
    "v_pk_fma_f32 v[154:155], v[154:155], s[70:71], v[106:107]\n"
    "v_pk_fma_f32 v[230:231], v[230:231], v[154:155], v[232:233]\n"
    "v_pk_mul_f32 v[232:233], v[232:233], v[154:155]\n"
    "v_pk_fma_f32 v[156:157], v[156:157], s[70:71], v[108:109]\n"
    "v_pk_fma_f32 v[230:231], v[230:231], v[156:157], v[232:233]\n"
    "v_pk_mul_f32 v[232:233], v[232:233], v[156:157]\n"
    "v_pk_fma_f32 v[158:159], v[158:159], s[70:71], v[110:111]\n"
    "v_pk_fma_f32 v[230:231], v[230:231], v[158:159], v[232:233]\n"
    "v_pk_mul_f32 v[232:233], v[232:233], v[158:159]\n"
    "v_rcp_f32 v232, v232\n"
    "v_rcp_f32 v233, v233\n"
    "v_fma_f32 v230, v230, v232, v247\n"
    "v_fmac_f32 v230, v231, v233\n"
    "v_lshlrev_b32 v232, 2, v249\n"
    "v_lshrrev_b32 v233, 2, v249\n"
    "v_and_b32 v232, 56, v232\n"
    "v_and_b32 v233, 4, v233\n"
    "v_add3_u32 v232, v232, v233, s81\n"
    "ds_read_b32 v232, v232\n"
    "s_add_u32 s81, s81, 64\n"
    "s_waitcnt lgkmcnt(0)\n"
    "v_lshlrev_b32 v232, 1, v232\n"
    "buffer_load_ushort v232, v232, s[44:47], 0 offen\n"
    "s_waitcnt vmcnt(0)\n"
    "v_lshlrev_b32 v232, 16, v232\n"
    "v_mul_f32 v230, v230, v232\n"
    "v_cvt_pk_bf16_f32 v230, v230, v230\n"
    "s_add_u32 s67, s67, 64\n"
    "s_waitcnt lgkmcnt(0)\n"
    "ds_write_b16 v245, v230\n"
    "ds_read_b64 v[228:229], v246\n"
    "s_cmp_le_u32 s34, s76\n"
    "s_cbranch_scc1 .Ldin_G1s_wait\n"
    ".Ldin_G1s_go:\n"
    "s_add_u32 s76, s76, s72\n"
    ".Ldin_G1s_dma:\n"
    "buffer_load_dwordx4 v[236:237], s[40:43], 0 idxen offen offset:0 nt lds\n"
    "buffer_load_dwordx4 v[238:239], s[40:43], 0 idxen offen offset:1024 nt lds\n"
    ".Ldin_G1s_nd:\n"
    "s_cmp_eq_u32 s67, s82\n"
    "s_cbranch_scc1 .Ldin_L2s\n"
    "s_branch .Ldin_G2s\n"
    ".Ldin_B1s_wait:\n"
    "s_cmp_lt_i32 s59, s57\n"
    "s_cbranch_scc1 .Ldin_B1s_w2\n"
    "s_sub_u32 s94, s67, s65\n"
    "s_cmp_gt_u32 s94, 256\n"
    "s_cbranch_scc1 .Ldin_B1s_dma\n"
    "s_cmp_eq_u32 s94, 64\n"
    "s_waitcnt vmcnt(2)\n"
    "s_cbranch_scc1 .Ldin_B1s_nd\n"
    "s_waitcnt vmcnt(0)\n"
    "s_branch .Ldin_B1s_nd\n"
    ".Ldin_B1s_slp:\n"
    "s_sleep 1\n"
    ".Ldin_B1s_w2:\n"
    "s_memrealtime s[34:35]\n"
    "s_waitcnt lgkmcnt(0)\n"
    "s_sub_u32 s94, s34, s76\n"
    "s_cmp_lt_i32 s94, 0\n"
    "s_cbranch_scc1 .Ldin_B1s_slp\n"
    "s_branch .Ldin_B1s_go\n"
    ".Ldin_G2s_wait:\n"
    "s_cmp_lt_i32 s59, s57\n"
    "s_cbranch_scc1 .Ldin_G2s_w2\n"
    "s_sub_u32 s94, s67, s65\n"
    "s_cmp_gt_u32 s94, 256\n"
    "s_cbranch_scc1 .Ldin_G2s_dma\n"
    "s_cmp_eq_u32 s94, 64\n"
    "s_waitcnt vmcnt(2)\n"
    "s_cbranch_scc1 .Ldin_G2s_nd\n"
    "s_waitcnt vmcnt(0)\n"
    "s_branch .Ldin_G2s_nd\n"
    ".Ldin_G2s_slp:\n"
    "s_sleep 1\n"
    ".Ldin_G2s_w2:\n"
    "s_memrealtime s[34:35]\n"
    "s_waitcnt lgkmcnt(0)\n"
    "s_sub_u32 s94, s34, s76\n"
    "s_cmp_lt_i32 s94, 0\n"
    "s_cbranch_scc1 .Ldin_G2s_slp\n"
    "s_branch .Ldin_G2s_go\n"
    ".Ldin_G3s_wait:\n"
    "s_cmp_lt_i32 s59, s57\n"
    "s_cbranch_scc1 .Ldin_G3s_w2\n"
    "s_sub_u32 s94, s67, s65\n"
    "s_cmp_gt_u32 s94, 256\n"
    "s_cbranch_scc1 .Ldin_G3s_dma\n"
    "s_cmp_eq_u32 s94, 64\n"
    "s_waitcnt vmcnt(2)\n"
    "s_cbranch_scc1 .Ldin_G3s_nd\n"
    "s_waitcnt vmcnt(0)\n"
    "s_branch .Ldin_G3s_nd\n"
    ".Ldin_G3s_slp:\n"
    "s_sleep 1\n"
    ".Ldin_G3s_w2:\n"
    "s_memrealtime s[34:35]\n"
    "s_waitcnt lgkmcnt(0)\n"
    "s_sub_u32 s94, s34, s76\n"
    "s_cmp_lt_i32 s94, 0\n"
    "s_cbranch_scc1 .Ldin_G3s_slp\n"
    "s_branch .Ldin_G3s_go\n"
    ".Ldin_G0s_wait:\n"
    "s_cmp_lt_i32 s59, s57\n"
    "s_cbranch_scc1 .Ldin_G0s_w2\n"
    "s_sub_u32 s94, s67, s65\n"
    "s_cmp_gt_u32 s94, 256\n"
    "s_cbranch_scc1 .Ldin_G0s_dma\n"
    "s_cmp_eq_u32 s94, 64\n"
    "s_waitcnt vmcnt(2)\n"
    "s_cbranch_scc1 .Ldin_G0s_nd\n"
    "s_waitcnt vmcnt(0)\n"
    "s_branch .Ldin_G0s_nd\n"
    ".Ldin_G0s_slp:\n"
    "s_sleep 1\n"
    ".Ldin_G0s_w2:\n"
    "s_memrealtime s[34:35]\n"
    "s_waitcnt lgkmcnt(0)\n"
    "s_sub_u32 s94, s34, s76\n"
    "s_cmp_lt_i32 s94, 0\n"
    "s_cbranch_scc1 .Ldin_G0s_slp\n"
    "s_branch .Ldin_G0s_go\n"
    ".Ldin_G1s_wait:\n"
    "s_cmp_lt_i32 s59, s57\n"
    "s_cbranch_scc1 .Ldin_G1s_w2\n"
    "s_sub_u32 s94, s67, s65\n"
    "s_cmp_gt_u32 s94, 256\n"
    "s_cbranch_scc1 .Ldin_G1s_dma\n"
    "s_cmp_eq_u32 s94, 64\n"
    "s_waitcnt vmcnt(2)\n"
    "s_cbranch_scc1 .Ldin_G1s_nd\n"
    "s_waitcnt vmcnt(0)\n"
    "s_branch .Ldin_G1s_nd\n"
    ".Ldin_G1s_slp:\n"
    "s_sleep 1\n"
    ".Ldin_G1s_w2:\n"
    "s_memrealtime s[34:35]\n"
    "s_waitcnt lgkmcnt(0)\n"
    "s_sub_u32 s94, s34, s76\n"
    "s_cmp_lt_i32 s94, 0\n"
    "s_cbranch_scc1 .Ldin_G1s_slp\n"
    "s_branch .Ldin_G1s_go\n"
    ".p2align 5\n"
    ".Ldin_L2s:\n"
    "s_setprio 0\n"
    "s_cmp_ge_i32 s59, s57\n"
    "s_cbranch_scc1 .Ldin_E0s\n"
    "s_add_u32 s76, s76, s73\n"
    "v_mov_b32 v253, s63\n"
    "v_and_b32 v184, v254, v184\n"
    "v_and_b32 v185, v255, v185\n"
    "v_and_b32 v186, v254, v186\n"
    "v_and_b32 v187, v255, v187\n"
    "v_and_b32 v188, v254, v188\n"
    "v_and_b32 v189, v255, v189\n"
    "v_and_b32 v190, v254, v190\n"
    "v_and_b32_e64 v191, v255, v191\n"
    "ds_read_b64 v[232:233], v253\n"
    "v_mfma_f32_4x4x4_16b_bf16 v[144:147], v[184:185], v[32:33], v[64:67]\n"
    "ds_read_b128 v[112:115], v252 offset:17408\n"
    "v_mfma_f32_4x4x4_16b_bf16 v[148:151], v[186:187], v[34:35], v[68:71]\n"
    "ds_read_b128 v[116:119], v252 offset:17424\n"
    "v_mfma_f32_4x4x4_16b_bf16 v[152:155], v[188:189], v[36:37], v[72:75]\n"
    "ds_read_b128 v[120:123], v252 offset:17536\n"
    "ds_read_b128 v[124:127], v252 offset:17552\n"
    "v_cvt_pk_bf16_f32 v0, v144, v145\n"
    "v_cvt_pk_bf16_f32 v1, v146, v147\n"
    "v_mfma_f32_4x4x4_16b_bf16 v[156:159], v[190:191], v[38:39], v[76:79]\n"
    "v_cvt_pk_bf16_f32 v2, v148, v149\n"
    "v_cvt_pk_bf16_f32 v3, v150, v151\n"
    "v_mfma_f32_4x4x4_16b_bf16 v[144:147], v[184:185], v[40:41], v[80:83]\n"
    "v_cvt_pk_bf16_f32 v4, v152, v153\n"
    "v_cvt_pk_bf16_f32 v5, v154, v155\n"
    "v_mfma_f32_4x4x4_16b_bf16 v[148:151], v[186:187], v[42:43], v[84:87]\n"
    "v_cvt_pk_bf16_f32 v6, v156, v157\n"
    "v_cvt_pk_bf16_f32 v7, v158, v159\n"
    "v_mfma_f32_4x4x4_16b_bf16 v[152:155], v[188:189], v[44:45], v[88:91]\n"
    "v_cvt_pk_bf16_f32 v8, v144, v145\n"
    "v_cvt_pk_bf16_f32 v9, v146, v147\n"
    "v_mfma_f32_4x4x4_16b_bf16 v[156:159], v[190:191], v[46:47], v[92:95]\n"
    "v_cvt_pk_bf16_f32 v10, v148, v149\n"
    "v_cvt_pk_bf16_f32 v11, v150, v151\n"
    "v_mfma_f32_4x4x4_16b_bf16 v[144:147], v[184:185], v[48:49], v[192:195]\n"
    "v_cvt_pk_bf16_f32 v12, v152, v153\n"
    "v_cvt_pk_bf16_f32 v13, v154, v155\n"
    "v_mfma_f32_4x4x4_16b_bf16 v[148:151], v[186:187], v[50:51], v[196:199]\n"
    "v_cvt_pk_bf16_f32 v14, v156, v157\n"
    "v_cvt_pk_bf16_f32 v15, v158, v159\n"
    "v_mfma_f32_4x4x4_16b_bf16 v[152:155], v[188:189], v[52:53], v[200:203]\n"
    "s_waitcnt lgkmcnt(0)\n"
    "v_readfirstlane_b32 s60, v232\n"
    "v_cvt_pk_bf16_f32 v16, v144, v145\n"
    "v_cvt_pk_bf16_f32 v17, v146, v147\n"
    "v_mfma_f32_4x4x4_16b_bf16 v[156:159], v[190:191], v[54:55], v[204:207]\n"
    "s_cmp_eq_u32 s60, -1\n"
    "s_cbranch_scc1 .Ldin_L2s_poll\n"
    ".Ldin_L2s_pr:\n"
    "v_readfirstlane_b32 s95, v233\n"
    "s_and_b32 s94, s60, 63\n"
    "s_lshl_b32 s94, s94, 6\n"
    "s_add_u32 s94, s94, 68480\n"
    "s_add_u32 s63, s63, 8\n"
    "v_lshl_add_u32 v252, v249, 1, s94\n"
    "ds_read_b32 v252, v252\n"
    "s_sub_u32 s95, s95, s76\n"
    "s_ashr_i32 s95, s95, 4\n"
    "s_min_i32 s95, s95, s74\n"
    "s_max_i32 s72, s95, s75\n"
    "v_cvt_pk_bf16_f32 v18, v148, v149\n"
    "v_cvt_pk_bf16_f32 v19, v150, v151\n"
    "v_mfma_f32_4x4x4_16b_bf16 v[144:147], v[184:185], v[56:57], v[208:211]\n"
    "v_cvt_pk_bf16_f32 v20, v152, v153\n"
    "v_cvt_pk_bf16_f32 v21, v154, v155\n"
    "v_mfma_f32_4x4x4_16b_bf16 v[148:151], v[186:187], v[58:59], v[212:215]\n"
    "v_cvt_pk_bf16_f32 v22, v156, v157\n"
    "v_cvt_pk_bf16_f32 v23, v158, v159\n"
    "v_mfma_f32_4x4x4_16b_bf16 v[152:155], v[188:189], v[60:61], v[216:219]\n"
    "v_cvt_pk_bf16_f32 v24, v144, v145\n"
    "v_cvt_pk_bf16_f32 v25, v146, v147\n"
    "v_mfma_f32_4x4x4_16b_bf16 v[156:159], v[190:191], v[62:63], v[220:223]\n"
    "v_cvt_pk_bf16_f32 v26, v148, v149\n"
    "v_cvt_pk_bf16_f32 v27, v150, v151\n"
    "v_cvt_pk_bf16_f32 v28, v152, v153\n"
    "v_cvt_pk_bf16_f32 v29, v154, v155\n"
    "v_exp_f32 v136, v136 clamp\n"
    "v_cvt_pk_bf16_f32 v30, v156, v157\n"
    "v_cvt_pk_bf16_f32 v31, v158, v159\n"
    "v_mfma_f32_16x16x32_bf16 v[144:147], v[0:3], v[160:163], v[112:115]\n"
    "v_exp_f32 v128, v128 clamp\n"
    "v_mfma_f32_16x16x32_bf16 v[144:147], v[4:7], v[164:167], v[144:147]\n"
    "v_exp_f32 v129, v129 clamp\n"
    "v_mfma_f32_16x16x32_bf16 v[148:151], v[8:11], v[160:163], v[116:119]\n"
    "v_exp_f32 v130, v130 clamp\n"
    "v_mfma_f32_16x16x32_bf16 v[148:151], v[12:15], v[164:167], v[148:151]\n"
    "v_exp_f32 v131, v131 clamp\n"
    "v_mfma_f32_16x16x32_bf16 v[152:155], v[16:19], v[160:163], v[120:123]\n"
    "v_exp_f32 v132, v132 clamp\n"
    "v_mfma_f32_16x16x32_bf16 v[152:155], v[20:23], v[164:167], v[152:155]\n"
    "v_exp_f32 v133, v133 clamp\n"
    "v_mfma_f32_16x16x32_bf16 v[156:159], v[24:27], v[160:163], v[124:127]\n"
    "v_exp_f32 v134, v134 clamp\n"
    "v_mfma_f32_16x16x32_bf16 v[156:159], v[28:31], v[164:167], v[156:159]\n"
    "v_exp_f32 v135, v135 clamp\n"
    "s_add_u32 m0, s68, 6144\n"
    "v_exp_f32 v137, v137 clamp\n"
    "v_add_u32 v253, s67, v244\n"
    "v_mfma_f32_4x4x4_16b_bf16 v[224:227], v[228:229], v[176:177], v[224:227] cbsz:4 abid:0\n"
    "v_exp_f32 v138, v138 clamp\n"
    "v_exp_f32 v139, v139 clamp\n"
    "v_mfma_f32_4x4x4_16b_bf16 v[224:227], v[228:229], v[178:179], v[224:227] cbsz:4 abid:1\n"
    "v_exp_f32 v140, v140 clamp\n"
    "v_exp_f32 v141, v141 clamp\n"
    "v_mfma_f32_4x4x4_16b_bf16 v[224:227], v[228:229], v[180:181], v[224:227] cbsz:4 abid:2\n"
    "v_exp_f32 v142, v142 clamp\n"
    "v_exp_f32 v143, v143 clamp\n"
    "v_mfma_f32_4x4x4_16b_bf16 v[224:227], v[228:229], v[182:183], v[224:227] cbsz:4 abid:3\n"
    "s_waitcnt vmcnt(4)\n"
    "ds_read_b64_tr_b16 v[176:177], v240 offset:6144\n"
    "ds_read_b64_tr_b16 v[178:179], v241 offset:6400\n"
    "ds_read_b64_tr_b16 v[180:181], v242 offset:6656\n"
    "ds_read_b64_tr_b16 v[182:183], v243 offset:6912\n"
    "ds_read_b32 v236, v253 offset:0\n"
    "ds_read_b32 v238, v253 offset:4\n"
    "ds_read_b128 v[160:163], v234 offset:0\n"
    "ds_read_b128 v[164:167], v235 offset:0\n"
    "s_waitcnt lgkmcnt(8)\n"
    "s_nop 0\n"
    "v_readlane_b32 s20, v252, 0\n"
    "v_readlane_b32 s21, v252, 1\n"
    "v_readlane_b32 s22, v252, 2\n"
    "v_readlane_b32 s23, v252, 3\n"
    "v_readlane_b32 s24, v252, 4\n"
    "v_readlane_b32 s25, v252, 5\n"
    "v_readlane_b32 s26, v252, 6\n"
    "v_readlane_b32 s27, v252, 7\n"
    "v_pk_fma_f32 v[128:129], v[128:129], s[70:71], v[96:97]\n"
    "v_pk_fma_f32 v[130:131], v[130:131], s[70:71], v[98:99]\n"
    "v_pk_add_f32 v[230:231], v[128:129], v[130:131]\n"
    "v_pk_mul_f32 v[232:233], v[128:129], v[130:131]\n"
    "v_pk_fma_f32 v[132:133], v[132:133], s[70:71], v[100:101]\n"
    "v_pk_fma_f32 v[230:231], v[230:231], v[132:133], v[232:233]\n"
    "v_pk_mul_f32 v[232:233], v[232:233], v[132:133]\n"
    "v_pk_fma_f32 v[134:135], v[134:135], s[70:71], v[102:103]\n"
    "v_pk_fma_f32 v[230:231], v[230:231], v[134:135], v[232:233]\n"
    "v_pk_mul_f32 v[232:233], v[232:233], v[134:135]\n"
    "v_pk_fma_f32 v[136:137], v[136:137], s[70:71], v[104:105]\n"
    "v_pk_fma_f32 v[230:231], v[230:231], v[136:137], v[232:233]\n"
    "v_pk_mul_f32 v[232:233], v[232:233], v[136:137]\n"
    "v_pk_fma_f32 v[138:139], v[138:139], s[70:71], v[106:107]\n"
    "v_pk_fma_f32 v[230:231], v[230:231], v[138:139], v[232:233]\n"
    "v_pk_mul_f32 v[232:233], v[232:233], v[138:139]\n"
    "v_pk_fma_f32 v[140:141], v[140:141], s[70:71], v[108:109]\n"
    "v_pk_fma_f32 v[230:231], v[230:231], v[140:141], v[232:233]\n"
    "v_pk_mul_f32 v[232:233], v[232:233], v[140:141]\n"
    "v_pk_fma_f32 v[142:143], v[142:143], s[70:71], v[110:111]\n"
    "v_pk_fma_f32 v[230:231], v[230:231], v[142:143], v[232:233]\n"
    "v_pk_mul_f32 v[232:233], v[232:233], v[142:143]\n"
    "v_rcp_f32 v232, v232\n"
    "v_rcp_f32 v233, v233\n"
    "v_fma_f32 v230, v230, v232, v247\n"
    "v_fmac_f32 v230, v231, v233\n"
    "v_lshlrev_b32 v232, 2, v249\n"
    "v_lshrrev_b32 v233, 2, v249\n"
    "v_and_b32 v232, 56, v232\n"
    "v_and_b32 v233, 4, v233\n"
    "v_add3_u32 v232, v232, v233, s81\n"
    "ds_read_b32 v232, v232\n"
    "s_add_u32 s81, s81, 64\n"
    "s_waitcnt lgkmcnt(0)\n"
    "v_lshlrev_b32 v232, 1, v232\n"
    "buffer_load_ushort v232, v232, s[44:47], 0 offen\n"
    "s_waitcnt vmcnt(0)\n"
    "v_lshlrev_b32 v232, 16, v232\n"
    "v_mul_f32 v230, v230, v232\n"
    "v_cvt_pk_bf16_f32 v230, v230, v230\n"
    "s_add_u32 s67, s67, 64\n"
    "s_waitcnt lgkmcnt(0)\n"
    "ds_write_b16 v245, v230\n"
    "ds_read_b64 v[228:229], v246\n"
    "s_lshl1_add_u32 s76, s72, s76\n"
    "buffer_load_dwordx4 v[236:237], s[40:43], 0 idxen offen offset:0 nt lds\n"
    "buffer_load_dwordx4 v[238:239], s[40:43], 0 idxen offen offset:1024 nt lds\n"
    "s_and_b32 s62, s58, 63\n"
    "s_mul_i32 s62, s62, 272\n"
    "s_mov_b32 s58, s59\n"
    "s_mov_b32 s59, s60\n"
    "s_mov_b32 s81, s65\n"
    "s_xor_b32 s65, s65, s64\n"
    "s_mov_b32 s80, s83\n"
    "s_add_u32 s82, s65, 256\n"
    "s_cmp_lt_i32 s59, s57\n"
    "s_cselect_b32 s76, s76, -1\n"
    "s_bitcmp1_b32 s58, 30\n"
    "s_cbranch_scc0 .Ldin_A3\n"
    ".Ldin_A3s:\n"
    "v_mfma_f32_16x16x32_bf16 v[128:131], v[0:3], v[160:163], v[112:115]\n"
    "v_exp_f32 v144, v144 clamp\n"
    "v_mfma_f32_16x16x32_bf16 v[128:131], v[4:7], v[164:167], v[128:131]\n"
    "v_exp_f32 v145, v145 clamp\n"
    "v_mfma_f32_16x16x32_bf16 v[132:135], v[8:11], v[160:163], v[116:119]\n"
    "v_exp_f32 v146, v146 clamp\n"
    "v_mfma_f32_16x16x32_bf16 v[132:135], v[12:15], v[164:167], v[132:135]\n"
    "v_exp_f32 v147, v147 clamp\n"
    "v_mfma_f32_16x16x32_bf16 v[136:139], v[16:19], v[160:163], v[120:123]\n"
    "v_exp_f32 v148, v148 clamp\n"
    "v_mfma_f32_16x16x32_bf16 v[136:139], v[20:23], v[164:167], v[136:139]\n"
    "v_exp_f32 v149, v149 clamp\n"
    "v_mfma_f32_16x16x32_bf16 v[140:143], v[24:27], v[160:163], v[124:127]\n"
    "v_exp_f32 v150, v150 clamp\n"
    "v_mfma_f32_16x16x32_bf16 v[140:143], v[28:31], v[164:167], v[140:143]\n"
    "v_exp_f32 v151, v151 clamp\n"
    "v_exp_f32 v152, v152 clamp\n"
    "s_add_u32 m0, s68, 0\n"
    "v_exp_f32 v153, v153 clamp\n"
    "v_add_u32 v253, s67, v244\n"
    "s_waitcnt lgkmcnt(0)\n"
    "v_mfma_f32_4x4x4_16b_bf16 v[224:227], v[228:229], v[168:169], v[224:227] cbsz:4 abid:0\n"
    "v_exp_f32 v154, v154 clamp\n"
    "v_exp_f32 v155, v155 clamp\n"
    "v_mfma_f32_4x4x4_16b_bf16 v[224:227], v[228:229], v[170:171], v[224:227] cbsz:4 abid:1\n"
    "v_exp_f32 v156, v156 clamp\n"
    "v_exp_f32 v157, v157 clamp\n"
    "v_mfma_f32_4x4x4_16b_bf16 v[224:227], v[228:229], v[172:173], v[224:227] cbsz:4 abid:2\n"
    "v_exp_f32 v158, v158 clamp\n"
    "v_exp_f32 v159, v159 clamp\n"
    "v_mfma_f32_4x4x4_16b_bf16 v[224:227], v[228:229], v[174:175], v[224:227] cbsz:4 abid:3\n"
    "s_waitcnt vmcnt(4)\n"
    "ds_read_b64_tr_b16 v[168:169], v240 offset:0\n"
    "ds_read_b64_tr_b16 v[170:171], v241 offset:256\n"
    "ds_read_b64_tr_b16 v[172:173], v242 offset:512\n"
    "ds_read_b64_tr_b16 v[174:175], v243 offset:768\n"
    "ds_read_b32 v236, v253 offset:0\n"
    "ds_read_b32 v238, v253 offset:4\n"
    "ds_read_b128 v[160:163], v234 offset:2048\n"
    "ds_read_b128 v[164:167], v235 offset:2048\n"
    "s_cbranch_vccz .Ldin_A3s_np\n"
    "s_setprio 1\n"
    ".Ldin_A3s_np:\n"
    "v_pk_fma_f32 v[144:145], v[144:145], s[70:71], v[96:97]\n"
    "v_pk_fma_f32 v[146:147], v[146:147], s[70:71], v[98:99]\n"
    "v_add_f32 v191, v224, v225\n"
    "v_add_f32 v190, v226, v227\n"
    "v_add_f32 v191, v191, v190\n"
    "v_add_u32 v190, s62, v249\n"
    "v_mul_f32 v191, s69, v191\n"
    "v_cvt_pk_bf16_f32 v191, v191, v191\n"
    "ds_write_b16 v190, v191 offset:32768\n"
    "s_bfe_u32 s94, s59, 0x180006\n"
    "s_mul_i32 s86, s94, s56\n"
    "v_mbcnt_lo_u32_b32 v184, s20, 0\n"
    "v_mbcnt_lo_u32_b32 v185, s22, 0\n"
    "v_mbcnt_lo_u32_b32 v186, s24, 0\n"
    "v_mbcnt_lo_u32_b32 v187, s26, 0\n"
    "s_bcnt1_i32_b64 s90, s[20:21]\n"
    "s_bcnt1_i32_b64 s91, s[22:23]\n"
    "s_bcnt1_i32_b64 s92, s[24:25]\n"
    "s_bcnt1_i32_b64 s93, s[26:27]\n"
    "v_mbcnt_hi_u32_b32 v184, s21, v184\n"
    "v_mbcnt_hi_u32_b32 v185, s23, v185\n"
    "v_mbcnt_hi_u32_b32 v186, s25, v186\n"
    "v_mbcnt_hi_u32_b32 v187, s27, v187\n"
    "s_lshl2_add_u32 s90, s90, s65\n"
    "s_lshl2_add_u32 s91, s91, s90\n"
    "s_lshl2_add_u32 s92, s92, s91\n"
    "s_lshl2_add_u32 s93, s93, s92\n"
    "v_add_u32_e64 v188, s86, v251\n"
    "v_pk_add_f32 v[230:231], v[144:145], v[146:147]\n"
    "v_pk_mul_f32 v[232:233], v[144:145], v[146:147]\n"
    "v_pk_fma_f32 v[148:149], v[148:149], s[70:71], v[100:101]\n"
    "v_pk_fma_f32 v[230:231], v[230:231], v[148:149], v[232:233]\n"
    "v_pk_mul_f32 v[232:233], v[232:233], v[148:149]\n"
    "v_lshl_add_u32 v184, v184, 2, s65\n"
    "v_lshl_add_u32 v185, v185, 2, s90\n"
    "v_lshl_add_u32 v186, v186, 2, s91\n"
    "v_lshl_add_u32 v187, v187, 2, s92\n"
    "v_mov_b32 v191, -1\n"
    "v_lshl_add_u32 v190, v249, 1, s93\n"
    "ds_write2_b32 v190, v191, v191 offset1:64\n"
    "s_mov_b64 exec, s[20:21]\n"
    "ds_write_b32 v184, v188\n"
    "s_mov_b64 exec, s[22:23]\n"
    "v_add_u32 v189, 64, v188\n"
    "ds_write_b32 v185, v189\n"
    "s_mov_b64 exec, s[24:25]\n"
    "v_add_u32 v190, 128, v188\n"
    "ds_write_b32 v186, v190\n"
    "s_mov_b64 exec, s[26:27]\n"
    "v_add_u32 v189, 192, v188\n"
    "ds_write_b32 v187, v189\n"
    "s_mov_b64 exec, -1\n"
    "s_add_u32 s95, s93, 60\n"
    "s_add_u32 s94, s82, 128\n"
    "s_andn2_b32 s95, s95, 63\n"
    "s_max_u32 s83, s95, s94\n"
    "v_pk_fma_f32 v[150:151], v[150:151], s[70:71], v[102:103]\n"
    "v_pk_fma_f32 v[230:231], v[230:231], v[150:151], v[232:233]\n"
    "v_pk_mul_f32 v[232:233], v[232:233], v[150:151]\n"
    "v_pk_fma_f32 v[152:153], v[152:153], s[70:71], v[104:105]\n"
    "v_pk_fma_f32 v[230:231], v[230:231], v[152:153], v[232:233]\n"
    "v_pk_mul_f32 v[232:233], v[232:233], v[152:153]\n"
    "v_pk_fma_f32 v[154:155], v[154:155], s[70:71], v[106:107]\n"
    "v_pk_fma_f32 v[230:231], v[230:231], v[154:155], v[232:233]\n"
    "v_pk_mul_f32 v[232:233], v[232:233], v[154:155]\n"
    "v_pk_fma_f32 v[156:157], v[156:157], s[70:71], v[108:109]\n"
    "v_pk_fma_f32 v[230:231], v[230:231], v[156:157], v[232:233]\n"
    "v_pk_mul_f32 v[232:233], v[232:233], v[156:157]\n"
    "v_pk_fma_f32 v[158:159], v[158:159], s[70:71], v[110:111]\n"
    "v_pk_fma_f32 v[230:231], v[230:231], v[158:159], v[232:233]\n"
    "v_pk_mul_f32 v[232:233], v[232:233], v[158:159]\n"
    "v_rcp_f32 v232, v232\n"
    "v_rcp_f32 v233, v233\n"
    "v_fma_f32 v230, v230, v232, v247\n"
    "v_fmac_f32 v230, v231, v233\n"
    "v_lshlrev_b32 v232, 2, v249\n"
    "v_lshrrev_b32 v233, 2, v249\n"
    "v_and_b32 v232, 56, v232\n"
    "v_and_b32 v233, 4, v233\n"
    "v_add3_u32 v232, v232, v233, s81\n"
    "ds_read_b32 v232, v232\n"
    "s_add_u32 s81, s81, 64\n"
    "s_waitcnt lgkmcnt(0)\n"
    "v_lshlrev_b32 v232, 1, v232\n"
    "buffer_load_ushort v232, v232, s[44:47], 0 offen\n"
    "s_waitcnt vmcnt(0)\n"
    "v_lshlrev_b32 v232, 16, v232\n"
    "v_mul_f32 v230, v230, v232\n"
    "v_cvt_pk_bf16_f32 v230, v230, v230\n"
    "s_add_u32 s67, s67, 64\n"
    "s_waitcnt lgkmcnt(0)\n"
    "ds_write_b16 v245, v230\n"
    "ds_read_b64 v[228:229], v246\n"
    "buffer_load_dwordx4 v[236:237], s[40:43], 0 idxen offen offset:0 nt lds\n"
    "buffer_load_dwordx4 v[238:239], s[40:43], 0 idxen offen offset:1024 nt lds\n"
    ".Ldin_B0s:\n"
    "v_mfma_f32_16x16x32_bf16 v[144:147], v[0:3], v[160:163], v[112:115]\n"
    "v_exp_f32 v128, v128 clamp\n"
    "v_mfma_f32_16x16x32_bf16 v[144:147], v[4:7], v[164:167], v[144:147]\n"
    "v_exp_f32 v129, v129 clamp\n"
    "v_mfma_f32_16x16x32_bf16 v[148:151], v[8:11], v[160:163], v[116:119]\n"
    "v_exp_f32 v130, v130 clamp\n"
    "v_mfma_f32_16x16x32_bf16 v[148:151], v[12:15], v[164:167], v[148:151]\n"
    "v_exp_f32 v131, v131 clamp\n"
    "v_mfma_f32_16x16x32_bf16 v[152:155], v[16:19], v[160:163], v[120:123]\n"
    "v_exp_f32 v132, v132 clamp\n"
    "v_mfma_f32_16x16x32_bf16 v[152:155], v[20:23], v[164:167], v[152:155]\n"
    "v_exp_f32 v133, v133 clamp\n"
    "v_mfma_f32_16x16x32_bf16 v[156:159], v[24:27], v[160:163], v[124:127]\n"
    "v_exp_f32 v134, v134 clamp\n"
    "v_mfma_f32_16x16x32_bf16 v[156:159], v[28:31], v[164:167], v[156:159]\n"
    "v_exp_f32 v135, v135 clamp\n"
    "v_exp_f32 v136, v136 clamp\n"
    "s_cmp_eq_u32 s67, s80\n"
    "s_cselect_b32 s67, s65, s67\n"
    "s_and_b32 s94, s59, 63\n"
    "s_mul_i32 s94, s94, 272\n"
    "v_add_u32 v252, s94, v250\n"
    "s_add_u32 m0, s68, 2048\n"
    "v_exp_f32 v137, v137 clamp\n"
    "v_add_u32 v253, s67, v244\n"
    "s_waitcnt lgkmcnt(0)\n"
    "v_mfma_f32_4x4x4_16b_bf16 v[224:227], v[228:229], v[176:177], 0 cbsz:4 abid:0\n"
    "v_exp_f32 v138, v138 clamp\n"
    "v_exp_f32 v139, v139 clamp\n"
    "v_mfma_f32_4x4x4_16b_bf16 v[224:227], v[228:229], v[178:179], v[224:227] cbsz:4 abid:1\n"
    "v_exp_f32 v140, v140 clamp\n"
    "v_exp_f32 v141, v141 clamp\n"
    "v_mfma_f32_4x4x4_16b_bf16 v[224:227], v[228:229], v[180:181], v[224:227] cbsz:4 abid:2\n"
    "v_exp_f32 v142, v142 clamp\n"
    "v_exp_f32 v143, v143 clamp\n"
    "v_mfma_f32_4x4x4_16b_bf16 v[224:227], v[228:229], v[182:183], v[224:227] cbsz:4 abid:3\n"
    "s_waitcnt vmcnt(4)\n"
    "s_memrealtime s[34:35]\n"
    "ds_read_b64_tr_b16 v[176:177], v240 offset:2048\n"
    "ds_read_b64_tr_b16 v[178:179], v241 offset:2304\n"
    "ds_read_b64_tr_b16 v[180:181], v242 offset:2560\n"
    "ds_read_b64_tr_b16 v[182:183], v243 offset:2816\n"
    "ds_read_b32 v236, v253 offset:0\n"
    "ds_read_b32 v238, v253 offset:4\n"
    "ds_read_b128 v[160:163], v234 offset:4096\n"
    "ds_read_b128 v[164:167], v235 offset:4096\n"
    "ds_read_b128 v[184:187], v252 offset:0\n"
    "ds_read_b128 v[188:191], v252 offset:16\n"
    "v_pk_fma_f32 v[128:129], v[128:129], s[70:71], v[96:97]\n"
    "v_pk_fma_f32 v[130:131], v[130:131], s[70:71], v[98:99]\n"
    "v_pk_add_f32 v[230:231], v[128:129], v[130:131]\n"
    "v_pk_mul_f32 v[232:233], v[128:129], v[130:131]\n"
    "v_pk_fma_f32 v[132:133], v[132:133], s[70:71], v[100:101]\n"
    "v_pk_fma_f32 v[230:231], v[230:231], v[132:133], v[232:233]\n"
    "v_pk_mul_f32 v[232:233], v[232:233], v[132:133]\n"
    "v_pk_fma_f32 v[134:135], v[134:135], s[70:71], v[102:103]\n"
    "v_pk_fma_f32 v[230:231], v[230:231], v[134:135], v[232:233]\n"
    "v_pk_mul_f32 v[232:233], v[232:233], v[134:135]\n"
    "v_pk_fma_f32 v[136:137], v[136:137], s[70:71], v[104:105]\n"
    "v_pk_fma_f32 v[230:231], v[230:231], v[136:137], v[232:233]\n"
    "v_pk_mul_f32 v[232:233], v[232:233], v[136:137]\n"
    "v_pk_fma_f32 v[138:139], v[138:139], s[70:71], v[106:107]\n"
    "v_pk_fma_f32 v[230:231], v[230:231], v[138:139], v[232:233]\n"
    "v_pk_mul_f32 v[232:233], v[232:233], v[138:139]\n"
    "v_pk_fma_f32 v[140:141], v[140:141], s[70:71], v[108:109]\n"
    "v_pk_fma_f32 v[230:231], v[230:231], v[140:141], v[232:233]\n"
    "v_pk_mul_f32 v[232:233], v[232:233], v[140:141]\n"
    "v_pk_fma_f32 v[142:143], v[142:143], s[70:71], v[110:111]\n"
    "v_pk_fma_f32 v[230:231], v[230:231], v[142:143], v[232:233]\n"
    "v_pk_mul_f32 v[232:233], v[232:233], v[142:143]\n"
    "v_rcp_f32 v232, v232\n"
    "v_rcp_f32 v233, v233\n"
    "v_fma_f32 v230, v230, v232, v247\n"
    "v_fmac_f32 v230, v231, v233\n"
    "v_lshlrev_b32 v232, 2, v249\n"
    "v_lshrrev_b32 v233, 2, v249\n"
    "v_and_b32 v232, 56, v232\n"
    "v_and_b32 v233, 4, v233\n"
    "v_add3_u32 v232, v232, v233, s81\n"
    "ds_read_b32 v232, v232\n"
    "s_add_u32 s81, s81, 64\n"
    "s_waitcnt lgkmcnt(0)\n"
    "v_lshlrev_b32 v232, 1, v232\n"
    "buffer_load_ushort v232, v232, s[44:47], 0 offen\n"
    "s_waitcnt vmcnt(0)\n"
    "v_lshlrev_b32 v232, 16, v232\n"
    "v_mul_f32 v230, v230, v232\n"
    "v_cvt_pk_bf16_f32 v230, v230, v230\n"
    "s_add_u32 s67, s67, 64\n"
    "s_waitcnt lgkmcnt(0)\n"
    "ds_write_b16 v245, v230\n"
    "ds_read_b64 v[228:229], v246\n"
    "s_cmp_le_u32 s34, s76\n"
    "s_cbranch_scc1 .Ldin_B0s_wait\n"
    ".Ldin_B0s_go:\n"
    "s_sub_u32 s94, s34, 60\n"
    "s_max_u32 s76, s76, s94\n"
    "s_add_u32 s76, s76, s72\n"
    ".Ldin_B0s_dma:\n"
    "buffer_load_dwordx4 v[236:237], s[40:43], 0 idxen offen offset:0 nt lds\n"
    "buffer_load_dwordx4 v[238:239], s[40:43], 0 idxen offen offset:1024 nt lds\n"
    ".Ldin_B0s_nd:\n"
    "s_branch .Ldin_G1s\n"
    ".Ldin_L2s_poll:\n"
    "s_sleep 1\n"
    "ds_read_b64 v[232:233], v253\n"
    "s_waitcnt lgkmcnt(0)\n"
    "v_readfirstlane_b32 s60, v232\n"
    "s_cmp_eq_u32 s60, -1\n"
    "s_cbranch_scc1 .Ldin_L2s_poll\n"
    "s_branch .Ldin_L2s_pr\n"
    ".Ldin_B0s_wait:\n"
    "s_cmp_lt_i32 s59, s57\n"
    "s_cbranch_scc1 .Ldin_B0s_w2\n"
    "s_sub_u32 s94, s67, s65\n"
    "s_cmp_gt_u32 s94, 256\n"
    "s_cbranch_scc1 .Ldin_B0s_dma\n"
    "s_cmp_eq_u32 s94, 64\n"
    "s_waitcnt vmcnt(2)\n"
    "s_cbranch_scc1 .Ldin_B0s_nd\n"
    "s_waitcnt vmcnt(0)\n"
    "s_branch .Ldin_B0s_nd\n"
    ".Ldin_B0s_slp:\n"
    "s_sleep 1\n"
    ".Ldin_B0s_w2:\n"
    "s_memrealtime s[34:35]\n"
    "s_waitcnt lgkmcnt(0)\n"
    "s_sub_u32 s94, s34, s76\n"
    "s_cmp_lt_i32 s94, 0\n"
    "s_cbranch_scc1 .Ldin_B0s_slp\n"
    "s_branch .Ldin_B0s_go\n"
    ".p2align 5\n"
    ".Ldin_L1s:\n"
    "s_setprio 0\n"
    "s_cmp_ge_i32 s59, s57\n"
    "s_cbranch_scc1 .Ldin_E1s\n"
    "s_add_u32 s76, s76, s73\n"
    "v_mov_b32 v253, s63\n"
    "v_and_b32 v184, v254, v184\n"
    "v_and_b32 v185, v255, v185\n"
    "v_and_b32 v186, v254, v186\n"
    "v_and_b32 v187, v255, v187\n"
    "v_and_b32 v188, v254, v188\n"
    "v_and_b32 v189, v255, v189\n"
    "v_and_b32 v190, v254, v190\n"
    "v_and_b32_e64 v191, v255, v191\n"
    "ds_read_b64 v[232:233], v253\n"
    "v_mfma_f32_4x4x4_16b_bf16 v[128:131], v[184:185], v[32:33], v[64:67]\n"
    "ds_read_b128 v[112:115], v252 offset:17408\n"
    "v_mfma_f32_4x4x4_16b_bf16 v[132:135], v[186:187], v[34:35], v[68:71]\n"
    "ds_read_b128 v[116:119], v252 offset:17424\n"
    "v_mfma_f32_4x4x4_16b_bf16 v[136:139], v[188:189], v[36:37], v[72:75]\n"
    "ds_read_b128 v[120:123], v252 offset:17536\n"
    "ds_read_b128 v[124:127], v252 offset:17552\n"
    "v_cvt_pk_bf16_f32 v0, v128, v129\n"
    "v_cvt_pk_bf16_f32 v1, v130, v131\n"
    "v_mfma_f32_4x4x4_16b_bf16 v[140:143], v[190:191], v[38:39], v[76:79]\n"
    "v_cvt_pk_bf16_f32 v2, v132, v133\n"
    "v_cvt_pk_bf16_f32 v3, v134, v135\n"
    "v_mfma_f32_4x4x4_16b_bf16 v[128:131], v[184:185], v[40:41], v[80:83]\n"
    "v_cvt_pk_bf16_f32 v4, v136, v137\n"
    "v_cvt_pk_bf16_f32 v5, v138, v139\n"
    "v_mfma_f32_4x4x4_16b_bf16 v[132:135], v[186:187], v[42:43], v[84:87]\n"
    "v_cvt_pk_bf16_f32 v6, v140, v141\n"
    "v_cvt_pk_bf16_f32 v7, v142, v143\n"
    "v_mfma_f32_4x4x4_16b_bf16 v[136:139], v[188:189], v[44:45], v[88:91]\n"
    "v_cvt_pk_bf16_f32 v8, v128, v129\n"
    "v_cvt_pk_bf16_f32 v9, v130, v131\n"
    "v_mfma_f32_4x4x4_16b_bf16 v[140:143], v[190:191], v[46:47], v[92:95]\n"
    "v_cvt_pk_bf16_f32 v10, v132, v133\n"
    "v_cvt_pk_bf16_f32 v11, v134, v135\n"
    "v_mfma_f32_4x4x4_16b_bf16 v[128:131], v[184:185], v[48:49], v[192:195]\n"
    "v_cvt_pk_bf16_f32 v12, v136, v137\n"
    "v_cvt_pk_bf16_f32 v13, v138, v139\n"
    "v_mfma_f32_4x4x4_16b_bf16 v[132:135], v[186:187], v[50:51], v[196:199]\n"
    "v_cvt_pk_bf16_f32 v14, v140, v141\n"
    "v_cvt_pk_bf16_f32 v15, v142, v143\n"
    "v_mfma_f32_4x4x4_16b_bf16 v[136:139], v[188:189], v[52:53], v[200:203]\n"
    "s_waitcnt lgkmcnt(0)\n"
    "v_readfirstlane_b32 s60, v232\n"
    "v_cvt_pk_bf16_f32 v16, v128, v129\n"
    "v_cvt_pk_bf16_f32 v17, v130, v131\n"
    "v_mfma_f32_4x4x4_16b_bf16 v[140:143], v[190:191], v[54:55], v[204:207]\n"
    "s_cmp_eq_u32 s60, -1\n"
    "s_cbranch_scc1 .Ldin_L1s_poll\n"
    ".Ldin_L1s_pr:\n"
    "v_readfirstlane_b32 s95, v233\n"
    "s_and_b32 s94, s60, 63\n"
    "s_lshl_b32 s94, s94, 6\n"
    "s_add_u32 s94, s94, 68480\n"
    "s_add_u32 s63, s63, 8\n"
    "v_lshl_add_u32 v252, v249, 1, s94\n"
    "ds_read_b32 v252, v252\n"
    "s_sub_u32 s95, s95, s76\n"
    "s_ashr_i32 s95, s95, 4\n"
    "s_min_i32 s95, s95, s74\n"
    "s_max_i32 s72, s95, s75\n"
    "v_cvt_pk_bf16_f32 v18, v132, v133\n"
    "v_cvt_pk_bf16_f32 v19, v134, v135\n"
    "v_mfma_f32_4x4x4_16b_bf16 v[128:131], v[184:185], v[56:57], v[208:211]\n"
    "v_cvt_pk_bf16_f32 v20, v136, v137\n"
    "v_cvt_pk_bf16_f32 v21, v138, v139\n"
    "v_mfma_f32_4x4x4_16b_bf16 v[132:135], v[186:187], v[58:59], v[212:215]\n"
    "v_cvt_pk_bf16_f32 v22, v140, v141\n"
    "v_cvt_pk_bf16_f32 v23, v142, v143\n"
    "v_mfma_f32_4x4x4_16b_bf16 v[136:139], v[188:189], v[60:61], v[216:219]\n"
    "v_cvt_pk_bf16_f32 v24, v128, v129\n"
    "v_cvt_pk_bf16_f32 v25, v130, v131\n"
    "v_mfma_f32_4x4x4_16b_bf16 v[140:143], v[190:191], v[62:63], v[220:223]\n"
    "v_cvt_pk_bf16_f32 v26, v132, v133\n"
    "v_cvt_pk_bf16_f32 v27, v134, v135\n"
    "v_cvt_pk_bf16_f32 v28, v136, v137\n"
    "v_cvt_pk_bf16_f32 v29, v138, v139\n"
    "v_exp_f32 v152, v152 clamp\n"
    "v_cvt_pk_bf16_f32 v30, v140, v141\n"
    "v_cvt_pk_bf16_f32 v31, v142, v143\n"
    "v_mfma_f32_16x16x32_bf16 v[128:131], v[0:3], v[160:163], v[112:115]\n"
    "v_exp_f32 v144, v144 clamp\n"
    "v_mfma_f32_16x16x32_bf16 v[128:131], v[4:7], v[164:167], v[128:131]\n"
    "v_exp_f32 v145, v145 clamp\n"
    "v_mfma_f32_16x16x32_bf16 v[132:135], v[8:11], v[160:163], v[116:119]\n"
    "v_exp_f32 v146, v146 clamp\n"
    "v_mfma_f32_16x16x32_bf16 v[132:135], v[12:15], v[164:167], v[132:135]\n"
    "v_exp_f32 v147, v147 clamp\n"
    "v_mfma_f32_16x16x32_bf16 v[136:139], v[16:19], v[160:163], v[120:123]\n"
    "v_exp_f32 v148, v148 clamp\n"
    "v_mfma_f32_16x16x32_bf16 v[136:139], v[20:23], v[164:167], v[136:139]\n"
    "v_exp_f32 v149, v149 clamp\n"
    "v_mfma_f32_16x16x32_bf16 v[140:143], v[24:27], v[160:163], v[124:127]\n"
    "v_exp_f32 v150, v150 clamp\n"
    "v_mfma_f32_16x16x32_bf16 v[140:143], v[28:31], v[164:167], v[140:143]\n"
    "v_exp_f32 v151, v151 clamp\n"
    "s_add_u32 m0, s68, 4096\n"
    "v_exp_f32 v153, v153 clamp\n"
    "v_add_u32 v253, s67, v244\n"
    "v_mfma_f32_4x4x4_16b_bf16 v[224:227], v[228:229], v[168:169], v[224:227] cbsz:4 abid:0\n"
    "v_exp_f32 v154, v154 clamp\n"
    "v_exp_f32 v155, v155 clamp\n"
    "v_mfma_f32_4x4x4_16b_bf16 v[224:227], v[228:229], v[170:171], v[224:227] cbsz:4 abid:1\n"
    "v_exp_f32 v156, v156 clamp\n"
    "v_exp_f32 v157, v157 clamp\n"
    "v_mfma_f32_4x4x4_16b_bf16 v[224:227], v[228:229], v[172:173], v[224:227] cbsz:4 abid:2\n"
    "v_exp_f32 v158, v158 clamp\n"
    "v_exp_f32 v159, v159 clamp\n"
    "v_mfma_f32_4x4x4_16b_bf16 v[224:227], v[228:229], v[174:175], v[224:227] cbsz:4 abid:3\n"
    "s_waitcnt vmcnt(4)\n"
    "ds_read_b64_tr_b16 v[168:169], v240 offset:4096\n"
    "ds_read_b64_tr_b16 v[170:171], v241 offset:4352\n"
    "ds_read_b64_tr_b16 v[172:173], v242 offset:4608\n"
    "ds_read_b64_tr_b16 v[174:175], v243 offset:4864\n"
    "ds_read_b32 v236, v253 offset:0\n"
    "ds_read_b32 v238, v253 offset:4\n"
    "ds_read_b128 v[160:163], v234 offset:6144\n"
    "ds_read_b128 v[164:167], v235 offset:6144\n"
    "s_waitcnt lgkmcnt(8)\n"
    "s_nop 0\n"
    "v_readlane_b32 s20, v252, 0\n"
    "v_readlane_b32 s21, v252, 1\n"
    "v_readlane_b32 s22, v252, 2\n"
    "v_readlane_b32 s23, v252, 3\n"
    "v_readlane_b32 s24, v252, 4\n"
    "v_readlane_b32 s25, v252, 5\n"
    "v_readlane_b32 s26, v252, 6\n"
    "v_readlane_b32 s27, v252, 7\n"
    "v_pk_fma_f32 v[144:145], v[144:145], s[70:71], v[96:97]\n"
    "v_pk_fma_f32 v[146:147], v[146:147], s[70:71], v[98:99]\n"
    "v_pk_add_f32 v[230:231], v[144:145], v[146:147]\n"
    "v_pk_mul_f32 v[232:233], v[144:145], v[146:147]\n"
    "v_pk_fma_f32 v[148:149], v[148:149], s[70:71], v[100:101]\n"
    "v_pk_fma_f32 v[230:231], v[230:231], v[148:149], v[232:233]\n"
    "v_pk_mul_f32 v[232:233], v[232:233], v[148:149]\n"
    "v_pk_fma_f32 v[150:151], v[150:151], s[70:71], v[102:103]\n"
    "v_pk_fma_f32 v[230:231], v[230:231], v[150:151], v[232:233]\n"
    "v_pk_mul_f32 v[232:233], v[232:233], v[150:151]\n"
    "v_pk_fma_f32 v[152:153], v[152:153], s[70:71], v[104:105]\n"
    "v_pk_fma_f32 v[230:231], v[230:231], v[152:153], v[232:233]\n"
    "v_pk_mul_f32 v[232:233], v[232:233], v[152:153]\n"
    "v_pk_fma_f32 v[154:155], v[154:155], s[70:71], v[106:107]\n"
    "v_pk_fma_f32 v[230:231], v[230:231], v[154:155], v[232:233]\n"
    "v_pk_mul_f32 v[232:233], v[232:233], v[154:155]\n"
    "v_pk_fma_f32 v[156:157], v[156:157], s[70:71], v[108:109]\n"
    "v_pk_fma_f32 v[230:231], v[230:231], v[156:157], v[232:233]\n"
    "v_pk_mul_f32 v[232:233], v[232:233], v[156:157]\n"
    "v_pk_fma_f32 v[158:159], v[158:159], s[70:71], v[110:111]\n"
    "v_pk_fma_f32 v[230:231], v[230:231], v[158:159], v[232:233]\n"
    "v_pk_mul_f32 v[232:233], v[232:233], v[158:159]\n"
    "v_rcp_f32 v232, v232\n"
    "v_rcp_f32 v233, v233\n"
    "v_fma_f32 v230, v230, v232, v247\n"
    "v_fmac_f32 v230, v231, v233\n"
    "v_lshlrev_b32 v232, 2, v249\n"
    "v_lshrrev_b32 v233, 2, v249\n"
    "v_and_b32 v232, 56, v232\n"
    "v_and_b32 v233, 4, v233\n"
    "v_add3_u32 v232, v232, v233, s81\n"
    "ds_read_b32 v232, v232\n"
    "s_add_u32 s81, s81, 64\n"
    "s_waitcnt lgkmcnt(0)\n"
    "v_lshlrev_b32 v232, 1, v232\n"
    "buffer_load_ushort v232, v232, s[44:47], 0 offen\n"
    "s_waitcnt vmcnt(0)\n"
    "v_lshlrev_b32 v232, 16, v232\n"
    "v_mul_f32 v230, v230, v232\n"
    "v_cvt_pk_bf16_f32 v230, v230, v230\n"
    "s_add_u32 s67, s67, 64\n"
    "s_waitcnt lgkmcnt(0)\n"
    "ds_write_b16 v245, v230\n"
    "ds_read_b64 v[228:229], v246\n"
    "s_lshl1_add_u32 s76, s72, s76\n"
    "buffer_load_dwordx4 v[236:237], s[40:43], 0 idxen offen offset:0 nt lds\n"
    "buffer_load_dwordx4 v[238:239], s[40:43], 0 idxen offen offset:1024 nt lds\n"
    "s_and_b32 s62, s58, 63\n"
    "s_mul_i32 s62, s62, 272\n"
    "s_mov_b32 s58, s59\n"
    "s_mov_b32 s59, s60\n"
    "s_mov_b32 s81, s65\n"
    "s_xor_b32 s65, s65, s64\n"
    "s_mov_b32 s80, s83\n"
    "s_add_u32 s82, s65, 256\n"
    "s_cmp_lt_i32 s59, s57\n"
    "s_cselect_b32 s76, s76, -1\n"
    "s_bitcmp1_b32 s58, 30\n"
    "s_cbranch_scc0 .Ldin_A2\n"
    ".Ldin_A2s:\n"
    "v_mfma_f32_16x16x32_bf16 v[144:147], v[0:3], v[160:163], v[112:115]\n"
    "v_exp_f32 v128, v128 clamp\n"
    "v_mfma_f32_16x16x32_bf16 v[144:147], v[4:7], v[164:167], v[144:147]\n"
    "v_exp_f32 v129, v129 clamp\n"
    "v_mfma_f32_16x16x32_bf16 v[148:151], v[8:11], v[160:163], v[116:119]\n"
    "v_exp_f32 v130, v130 clamp\n"
    "v_mfma_f32_16x16x32_bf16 v[148:151], v[12:15], v[164:167], v[148:151]\n"
    "v_exp_f32 v131, v131 clamp\n"
    "v_mfma_f32_16x16x32_bf16 v[152:155], v[16:19], v[160:163], v[120:123]\n"
    "v_exp_f32 v132, v132 clamp\n"
    "v_mfma_f32_16x16x32_bf16 v[152:155], v[20:23], v[164:167], v[152:155]\n"
    "v_exp_f32 v133, v133 clamp\n"
    "v_mfma_f32_16x16x32_bf16 v[156:159], v[24:27], v[160:163], v[124:127]\n"
    "v_exp_f32 v134, v134 clamp\n"
    "v_mfma_f32_16x16x32_bf16 v[156:159], v[28:31], v[164:167], v[156:159]\n"
    "v_exp_f32 v135, v135 clamp\n"
    "v_exp_f32 v136, v136 clamp\n"
    "s_add_u32 m0, s68, 6144\n"
    "v_exp_f32 v137, v137 clamp\n"
    "v_add_u32_e64 v253, s67, v244\n"
    "s_waitcnt lgkmcnt(0)\n"
    "v_mfma_f32_4x4x4_16b_bf16 v[224:227], v[228:229], v[176:177], v[224:227] cbsz:4 abid:0\n"
    "v_exp_f32 v138, v138 clamp\n"
    "v_exp_f32 v139, v139 clamp\n"
    "v_mfma_f32_4x4x4_16b_bf16 v[224:227], v[228:229], v[178:179], v[224:227] cbsz:4 abid:1\n"
    "v_exp_f32 v140, v140 clamp\n"
    "v_exp_f32 v141, v141 clamp\n"
    "v_mfma_f32_4x4x4_16b_bf16 v[224:227], v[228:229], v[180:181], v[224:227] cbsz:4 abid:2\n"
    "v_exp_f32 v142, v142 clamp\n"
    "v_exp_f32 v143, v143 clamp\n"
    "v_mfma_f32_4x4x4_16b_bf16 v[224:227], v[228:229], v[182:183], v[224:227] cbsz:4 abid:3\n"
    "s_waitcnt vmcnt(4)\n"
    "ds_read_b64_tr_b16 v[176:177], v240 offset:6144\n"
    "ds_read_b64_tr_b16 v[178:179], v241 offset:6400\n"
    "ds_read_b64_tr_b16 v[180:181], v242 offset:6656\n"
    "ds_read_b64_tr_b16 v[182:183], v243 offset:6912\n"
    "ds_read_b32 v236, v253 offset:0\n"
    "ds_read_b32 v238, v253 offset:4\n"
    "ds_read_b128 v[160:163], v234 offset:0\n"
    "ds_read_b128 v[164:167], v235 offset:0\n"
    "s_cbranch_vccz .Ldin_A2s_np\n"
    "s_setprio 1\n"
    ".Ldin_A2s_np:\n"
    "v_pk_fma_f32 v[128:129], v[128:129], s[70:71], v[96:97]\n"
    "v_pk_fma_f32 v[130:131], v[130:131], s[70:71], v[98:99]\n"
    "v_add_f32 v191, v224, v225\n"
    "v_add_f32 v190, v226, v227\n"
    "v_add_f32 v191, v191, v190\n"
    "v_add_u32 v190, s62, v249\n"
    "v_mul_f32 v191, s69, v191\n"
    "v_cvt_pk_bf16_f32 v191, v191, v191\n"
    "ds_write_b16 v190, v191 offset:32768\n"
    "s_bfe_u32 s94, s59, 0x180006\n"
    "s_mul_i32 s86, s94, s56\n"
    "v_mbcnt_lo_u32_b32 v184, s20, 0\n"
    "v_mbcnt_lo_u32_b32 v185, s22, 0\n"
    "v_mbcnt_lo_u32_b32 v186, s24, 0\n"
    "v_mbcnt_lo_u32_b32 v187, s26, 0\n"
    "s_bcnt1_i32_b64 s90, s[20:21]\n"
    "s_bcnt1_i32_b64 s91, s[22:23]\n"
    "s_bcnt1_i32_b64 s92, s[24:25]\n"
    "s_bcnt1_i32_b64 s93, s[26:27]\n"
    "v_mbcnt_hi_u32_b32 v184, s21, v184\n"
    "v_mbcnt_hi_u32_b32 v185, s23, v185\n"
    "v_mbcnt_hi_u32_b32 v186, s25, v186\n"
    "v_mbcnt_hi_u32_b32 v187, s27, v187\n"
    "s_lshl2_add_u32 s90, s90, s65\n"
    "s_lshl2_add_u32 s91, s91, s90\n"
    "s_lshl2_add_u32 s92, s92, s91\n"
    "s_lshl2_add_u32 s93, s93, s92\n"
    "v_add_u32_e64 v188, s86, v251\n"
    "v_pk_add_f32 v[230:231], v[128:129], v[130:131]\n"
    "v_pk_mul_f32 v[232:233], v[128:129], v[130:131]\n"
    "v_pk_fma_f32 v[132:133], v[132:133], s[70:71], v[100:101]\n"
    "v_pk_fma_f32 v[230:231], v[230:231], v[132:133], v[232:233]\n"
    "v_pk_mul_f32 v[232:233], v[232:233], v[132:133]\n"
    "v_lshl_add_u32 v184, v184, 2, s65\n"
    "v_lshl_add_u32 v185, v185, 2, s90\n"
    "v_lshl_add_u32 v186, v186, 2, s91\n"
    "v_lshl_add_u32 v187, v187, 2, s92\n"
    "v_mov_b32 v191, -1\n"
    "v_lshl_add_u32 v190, v249, 1, s93\n"
    "ds_write2_b32 v190, v191, v191 offset1:64\n"
    "s_mov_b64 exec, s[20:21]\n"
    "ds_write_b32 v184, v188\n"
    "s_mov_b64 exec, s[22:23]\n"
    "v_add_u32 v189, 64, v188\n"
    "ds_write_b32 v185, v189\n"
    "s_mov_b64 exec, s[24:25]\n"
    "v_add_u32 v190, 128, v188\n"
    "ds_write_b32 v186, v190\n"
    "s_mov_b64 exec, s[26:27]\n"
    "v_add_u32 v189, 192, v188\n"
    "ds_write_b32 v187, v189\n"
    "s_mov_b64 exec, -1\n"
    "s_add_u32 s95, s93, 60\n"
    "s_add_u32 s94, s82, 128\n"
    "s_andn2_b32 s95, s95, 63\n"
    "s_max_u32 s83, s95, s94\n"
    "v_pk_fma_f32 v[134:135], v[134:135], s[70:71], v[102:103]\n"
    "v_pk_fma_f32 v[230:231], v[230:231], v[134:135], v[232:233]\n"
    "v_pk_mul_f32 v[232:233], v[232:233], v[134:135]\n"
    "v_pk_fma_f32 v[136:137], v[136:137], s[70:71], v[104:105]\n"
    "v_pk_fma_f32 v[230:231], v[230:231], v[136:137], v[232:233]\n"
    "v_pk_mul_f32 v[232:233], v[232:233], v[136:137]\n"
    "v_pk_fma_f32 v[138:139], v[138:139], s[70:71], v[106:107]\n"
    "v_pk_fma_f32 v[230:231], v[230:231], v[138:139], v[232:233]\n"
    "v_pk_mul_f32 v[232:233], v[232:233], v[138:139]\n"
    "v_pk_fma_f32 v[140:141], v[140:141], s[70:71], v[108:109]\n"
    "v_pk_fma_f32 v[230:231], v[230:231], v[140:141], v[232:233]\n"
    "v_pk_mul_f32 v[232:233], v[232:233], v[140:141]\n"
    "v_pk_fma_f32 v[142:143], v[142:143], s[70:71], v[110:111]\n"
    "v_pk_fma_f32 v[230:231], v[230:231], v[142:143], v[232:233]\n"
    "v_pk_mul_f32 v[232:233], v[232:233], v[142:143]\n"
    "v_rcp_f32 v232, v232\n"
    "v_rcp_f32 v233, v233\n"
    "v_fma_f32 v230, v230, v232, v247\n"
    "v_fmac_f32 v230, v231, v233\n"
    "v_lshlrev_b32 v232, 2, v249\n"
    "v_lshrrev_b32 v233, 2, v249\n"
    "v_and_b32 v232, 56, v232\n"
    "v_and_b32 v233, 4, v233\n"
    "v_add3_u32 v232, v232, v233, s81\n"
    "ds_read_b32 v232, v232\n"
    "s_add_u32 s81, s81, 64\n"
    "s_waitcnt lgkmcnt(0)\n"
    "v_lshlrev_b32 v232, 1, v232\n"
    "buffer_load_ushort v232, v232, s[44:47], 0 offen\n"
    "s_waitcnt vmcnt(0)\n"
    "v_lshlrev_b32 v232, 16, v232\n"
    "v_mul_f32 v230, v230, v232\n"
    "v_cvt_pk_bf16_f32 v230, v230, v230\n"
    "s_add_u32 s67, s67, 64\n"
    "s_waitcnt lgkmcnt(0)\n"
    "ds_write_b16 v245, v230\n"
    "ds_read_b64 v[228:229], v246\n"
    "buffer_load_dwordx4 v[236:237], s[40:43], 0 idxen offen offset:0 nt lds\n"
    "buffer_load_dwordx4 v[238:239], s[40:43], 0 idxen offen offset:1024 nt lds\n"
    ".Ldin_B3s:\n"
    "v_mfma_f32_16x16x32_bf16 v[128:131], v[0:3], v[160:163], v[112:115]\n"
    "v_exp_f32 v144, v144 clamp\n"
    "v_mfma_f32_16x16x32_bf16 v[128:131], v[4:7], v[164:167], v[128:131]\n"
    "v_exp_f32 v145, v145 clamp\n"
    "v_mfma_f32_16x16x32_bf16 v[132:135], v[8:11], v[160:163], v[116:119]\n"
    "v_exp_f32 v146, v146 clamp\n"
    "v_mfma_f32_16x16x32_bf16 v[132:135], v[12:15], v[164:167], v[132:135]\n"
    "v_exp_f32 v147, v147 clamp\n"
    "v_mfma_f32_16x16x32_bf16 v[136:139], v[16:19], v[160:163], v[120:123]\n"
    "v_exp_f32 v148, v148 clamp\n"
    "v_mfma_f32_16x16x32_bf16 v[136:139], v[20:23], v[164:167], v[136:139]\n"
    "v_exp_f32 v149, v149 clamp\n"
    "v_mfma_f32_16x16x32_bf16 v[140:143], v[24:27], v[160:163], v[124:127]\n"
    "v_exp_f32 v150, v150 clamp\n"
    "v_mfma_f32_16x16x32_bf16 v[140:143], v[28:31], v[164:167], v[140:143]\n"
    "v_exp_f32 v151, v151 clamp\n"
    "v_exp_f32 v152, v152 clamp\n"
    "s_cmp_eq_u32 s67, s80\n"
    "s_cselect_b32 s67, s65, s67\n"
    "s_and_b32 s94, s59, 63\n"
    "s_mul_i32 s94, s94, 272\n"
    "v_add_u32 v252, s94, v250\n"
    "s_add_u32 m0, s68, 0\n"
    "v_exp_f32 v153, v153 clamp\n"
    "v_add_u32_e64 v253, s67, v244\n"
    "s_waitcnt lgkmcnt(0)\n"
    "v_mfma_f32_4x4x4_16b_bf16 v[224:227], v[228:229], v[168:169], 0 cbsz:4 abid:0\n"
    "v_exp_f32 v154, v154 clamp\n"
    "v_exp_f32 v155, v155 clamp\n"
    "v_mfma_f32_4x4x4_16b_bf16 v[224:227], v[228:229], v[170:171], v[224:227] cbsz:4 abid:1\n"
    "v_exp_f32 v156, v156 clamp\n"
    "v_exp_f32 v157, v157 clamp\n"
    "v_mfma_f32_4x4x4_16b_bf16 v[224:227], v[228:229], v[172:173], v[224:227] cbsz:4 abid:2\n"
    "v_exp_f32 v158, v158 clamp\n"
    "v_exp_f32 v159, v159 clamp\n"
    "v_mfma_f32_4x4x4_16b_bf16 v[224:227], v[228:229], v[174:175], v[224:227] cbsz:4 abid:3\n"
    "s_waitcnt vmcnt(4)\n"
    "s_memrealtime s[34:35]\n"
    "ds_read_b64_tr_b16 v[168:169], v240 offset:0\n"
    "ds_read_b64_tr_b16 v[170:171], v241 offset:256\n"
    "ds_read_b64_tr_b16 v[172:173], v242 offset:512\n"
    "ds_read_b64_tr_b16 v[174:175], v243 offset:768\n"
    "ds_read_b32 v236, v253 offset:0\n"
    "ds_read_b32 v238, v253 offset:4\n"
    "ds_read_b128 v[160:163], v234 offset:2048\n"
    "ds_read_b128 v[164:167], v235 offset:2048\n"
    "ds_read_b128 v[184:187], v252 offset:0\n"
    "ds_read_b128 v[188:191], v252 offset:16\n"
    "v_pk_fma_f32 v[144:145], v[144:145], s[70:71], v[96:97]\n"
    "v_pk_fma_f32 v[146:147], v[146:147], s[70:71], v[98:99]\n"
    "v_pk_add_f32 v[230:231], v[144:145], v[146:147]\n"
    "v_pk_mul_f32 v[232:233], v[144:145], v[146:147]\n"
    "v_pk_fma_f32 v[148:149], v[148:149], s[70:71], v[100:101]\n"
    "v_pk_fma_f32 v[230:231], v[230:231], v[148:149], v[232:233]\n"
    "v_pk_mul_f32 v[232:233], v[232:233], v[148:149]\n"
    "v_pk_fma_f32 v[150:151], v[150:151], s[70:71], v[102:103]\n"
    "v_pk_fma_f32 v[230:231], v[230:231], v[150:151], v[232:233]\n"
    "v_pk_mul_f32 v[232:233], v[232:233], v[150:151]\n"
    "v_pk_fma_f32 v[152:153], v[152:153], s[70:71], v[104:105]\n"
    "v_pk_fma_f32 v[230:231], v[230:231], v[152:153], v[232:233]\n"
    "v_pk_mul_f32 v[232:233], v[232:233], v[152:153]\n"
    "v_pk_fma_f32 v[154:155], v[154:155], s[70:71], v[106:107]\n"
    "v_pk_fma_f32 v[230:231], v[230:231], v[154:155], v[232:233]\n"
    "v_pk_mul_f32 v[232:233], v[232:233], v[154:155]\n"
    "v_pk_fma_f32 v[156:157], v[156:157], s[70:71], v[108:109]\n"
    "v_pk_fma_f32 v[230:231], v[230:231], v[156:157], v[232:233]\n"
    "v_pk_mul_f32 v[232:233], v[232:233], v[156:157]\n"
    "v_pk_fma_f32 v[158:159], v[158:159], s[70:71], v[110:111]\n"
    "v_pk_fma_f32 v[230:231], v[230:231], v[158:159], v[232:233]\n"
    "v_pk_mul_f32 v[232:233], v[232:233], v[158:159]\n"
    "v_rcp_f32 v232, v232\n"
    "v_rcp_f32 v233, v233\n"
    "v_fma_f32 v230, v230, v232, v247\n"
    "v_fmac_f32 v230, v231, v233\n"
    "v_lshlrev_b32 v232, 2, v249\n"
    "v_lshrrev_b32 v233, 2, v249\n"
    "v_and_b32 v232, 56, v232\n"
    "v_and_b32 v233, 4, v233\n"
    "v_add3_u32 v232, v232, v233, s81\n"
    "ds_read_b32 v232, v232\n"
    "s_add_u32 s81, s81, 64\n"
    "s_waitcnt lgkmcnt(0)\n"
    "v_lshlrev_b32 v232, 1, v232\n"
    "buffer_load_ushort v232, v232, s[44:47], 0 offen\n"
    "s_waitcnt vmcnt(0)\n"
    "v_lshlrev_b32 v232, 16, v232\n"
    "v_mul_f32 v230, v230, v232\n"
    "v_cvt_pk_bf16_f32 v230, v230, v230\n"
    "s_add_u32 s67, s67, 64\n"
    "s_waitcnt lgkmcnt(0)\n"
    "ds_write_b16 v245, v230\n"
    "ds_read_b64 v[228:229], v246\n"
    "s_cmp_le_u32 s34, s76\n"
    "s_cbranch_scc1 .Ldin_B3s_wait\n"
    ".Ldin_B3s_go:\n"
    "s_sub_u32 s94, s34, 60\n"
    "s_max_u32 s76, s76, s94\n"
    "s_add_u32 s76, s76, s72\n"
    ".Ldin_B3s_dma:\n"
    "buffer_load_dwordx4 v[236:237], s[40:43], 0 idxen offen offset:0 nt lds\n"
    "buffer_load_dwordx4 v[238:239], s[40:43], 0 idxen offen offset:1024 nt lds\n"
    ".Ldin_B3s_nd:\n"
    "s_branch .Ldin_G0s\n"
    ".Ldin_L1s_poll:\n"
    "s_sleep 1\n"
    "ds_read_b64 v[232:233], v253\n"
    "s_waitcnt lgkmcnt(0)\n"
    "v_readfirstlane_b32 s60, v232\n"
    "s_cmp_eq_u32 s60, -1\n"
    "s_cbranch_scc1 .Ldin_L1s_poll\n"
    "s_branch .Ldin_L1s_pr\n"
    ".Ldin_B3s_wait:\n"
    "s_cmp_lt_i32 s59, s57\n"
    "s_cbranch_scc1 .Ldin_B3s_w2\n"
    "s_sub_u32 s94, s67, s65\n"
    "s_cmp_gt_u32 s94, 256\n"
    "s_cbranch_scc1 .Ldin_B3s_dma\n"
    "s_cmp_eq_u32 s94, 64\n"
    "s_waitcnt vmcnt(2)\n"
    "s_cbranch_scc1 .Ldin_B3s_nd\n"
    "s_waitcnt vmcnt(0)\n"
    "s_branch .Ldin_B3s_nd\n"
    ".Ldin_B3s_slp:\n"
    "s_sleep 1\n"
    ".Ldin_B3s_w2:\n"
    "s_memrealtime s[34:35]\n"
    "s_waitcnt lgkmcnt(0)\n"
    "s_sub_u32 s94, s34, s76\n"
    "s_cmp_lt_i32 s94, 0\n"
    "s_cbranch_scc1 .Ldin_B3s_slp\n"
    "s_branch .Ldin_B3s_go\n"
    ".p2align 5\n"
    ".Ldin_L0s:\n"
    "s_setprio 0\n"
    "s_cmp_ge_i32 s59, s57\n"
    "s_cbranch_scc1 .Ldin_E0s\n"
    "s_add_u32 s76, s76, s73\n"
    "v_mov_b32 v253, s63\n"
    "v_and_b32 v184, v254, v184\n"
    "v_and_b32 v185, v255, v185\n"
    "v_and_b32 v186, v254, v186\n"
    "v_and_b32 v187, v255, v187\n"
    "v_and_b32 v188, v254, v188\n"
    "v_and_b32 v189, v255, v189\n"
    "v_and_b32 v190, v254, v190\n"
    "v_and_b32_e64 v191, v255, v191\n"
    "ds_read_b64 v[232:233], v253\n"
    "v_mfma_f32_4x4x4_16b_bf16 v[144:147], v[184:185], v[32:33], v[64:67]\n"
    "ds_read_b128 v[112:115], v252 offset:17408\n"
    "v_mfma_f32_4x4x4_16b_bf16 v[148:151], v[186:187], v[34:35], v[68:71]\n"
    "ds_read_b128 v[116:119], v252 offset:17424\n"
    "v_mfma_f32_4x4x4_16b_bf16 v[152:155], v[188:189], v[36:37], v[72:75]\n"
    "ds_read_b128 v[120:123], v252 offset:17536\n"
    "ds_read_b128 v[124:127], v252 offset:17552\n"
    "v_cvt_pk_bf16_f32 v0, v144, v145\n"
    "v_cvt_pk_bf16_f32 v1, v146, v147\n"
    "v_mfma_f32_4x4x4_16b_bf16 v[156:159], v[190:191], v[38:39], v[76:79]\n"
    "v_cvt_pk_bf16_f32 v2, v148, v149\n"
    "v_cvt_pk_bf16_f32 v3, v150, v151\n"
    "v_mfma_f32_4x4x4_16b_bf16 v[144:147], v[184:185], v[40:41], v[80:83]\n"
    "v_cvt_pk_bf16_f32 v4, v152, v153\n"
    "v_cvt_pk_bf16_f32 v5, v154, v155\n"
    "v_mfma_f32_4x4x4_16b_bf16 v[148:151], v[186:187], v[42:43], v[84:87]\n"
    "v_cvt_pk_bf16_f32 v6, v156, v157\n"
    "v_cvt_pk_bf16_f32 v7, v158, v159\n"
    "v_mfma_f32_4x4x4_16b_bf16 v[152:155], v[188:189], v[44:45], v[88:91]\n"
    "v_cvt_pk_bf16_f32 v8, v144, v145\n"
    "v_cvt_pk_bf16_f32 v9, v146, v147\n"
    "v_mfma_f32_4x4x4_16b_bf16 v[156:159], v[190:191], v[46:47], v[92:95]\n"
    "v_cvt_pk_bf16_f32 v10, v148, v149\n"
    "v_cvt_pk_bf16_f32 v11, v150, v151\n"
    "v_mfma_f32_4x4x4_16b_bf16 v[144:147], v[184:185], v[48:49], v[192:195]\n"
    "v_cvt_pk_bf16_f32 v12, v152, v153\n"
    "v_cvt_pk_bf16_f32 v13, v154, v155\n"
    "v_mfma_f32_4x4x4_16b_bf16 v[148:151], v[186:187], v[50:51], v[196:199]\n"
    "v_cvt_pk_bf16_f32 v14, v156, v157\n"
    "v_cvt_pk_bf16_f32 v15, v158, v159\n"
    "v_mfma_f32_4x4x4_16b_bf16 v[152:155], v[188:189], v[52:53], v[200:203]\n"
    "s_waitcnt lgkmcnt(0)\n"
    "v_readfirstlane_b32 s60, v232\n"
    "v_cvt_pk_bf16_f32 v16, v144, v145\n"
    "v_cvt_pk_bf16_f32 v17, v146, v147\n"
    "v_mfma_f32_4x4x4_16b_bf16 v[156:159], v[190:191], v[54:55], v[204:207]\n"
    "s_cmp_eq_u32 s60, -1\n"
    "s_cbranch_scc1 .Ldin_L0s_poll\n"
    ".Ldin_L0s_pr:\n"
    "v_readfirstlane_b32 s95, v233\n"
    "s_and_b32 s94, s60, 63\n"
    "s_lshl_b32 s94, s94, 6\n"
    "s_add_u32 s94, s94, 68480\n"
    "s_add_u32 s63, s63, 8\n"
    "v_lshl_add_u32 v252, v249, 1, s94\n"
    "ds_read_b32 v252, v252\n"
    "s_sub_u32 s95, s95, s76\n"
    "s_ashr_i32 s95, s95, 4\n"
    "s_min_i32 s95, s95, s74\n"
    "s_max_i32 s72, s95, s75\n"
    "v_cvt_pk_bf16_f32 v18, v148, v149\n"
    "v_cvt_pk_bf16_f32 v19, v150, v151\n"
    "v_mfma_f32_4x4x4_16b_bf16 v[144:147], v[184:185], v[56:57], v[208:211]\n"
    "v_cvt_pk_bf16_f32 v20, v152, v153\n"
    "v_cvt_pk_bf16_f32 v21, v154, v155\n"
    "v_mfma_f32_4x4x4_16b_bf16 v[148:151], v[186:187], v[58:59], v[212:215]\n"
    "v_cvt_pk_bf16_f32 v22, v156, v157\n"
    "v_cvt_pk_bf16_f32 v23, v158, v159\n"
    "v_mfma_f32_4x4x4_16b_bf16 v[152:155], v[188:189], v[60:61], v[216:219]\n"
    "v_cvt_pk_bf16_f32 v24, v144, v145\n"
    "v_cvt_pk_bf16_f32 v25, v146, v147\n"
    "v_mfma_f32_4x4x4_16b_bf16 v[156:159], v[190:191], v[62:63], v[220:223]\n"
    "v_cvt_pk_bf16_f32 v26, v148, v149\n"
    "v_cvt_pk_bf16_f32 v27, v150, v151\n"
    "v_cvt_pk_bf16_f32 v28, v152, v153\n"
    "v_cvt_pk_bf16_f32 v29, v154, v155\n"
    "v_exp_f32 v136, v136 clamp\n"
    "v_cvt_pk_bf16_f32 v30, v156, v157\n"
    "v_cvt_pk_bf16_f32 v31, v158, v159\n"
    "v_mfma_f32_16x16x32_bf16 v[144:147], v[0:3], v[160:163], v[112:115]\n"
    "v_exp_f32 v128, v128 clamp\n"
    "v_mfma_f32_16x16x32_bf16 v[144:147], v[4:7], v[164:167], v[144:147]\n"
    "v_exp_f32 v129, v129 clamp\n"
    "v_mfma_f32_16x16x32_bf16 v[148:151], v[8:11], v[160:163], v[116:119]\n"
    "v_exp_f32 v130, v130 clamp\n"
    "v_mfma_f32_16x16x32_bf16 v[148:151], v[12:15], v[164:167], v[148:151]\n"
    "v_exp_f32 v131, v131 clamp\n"
    "v_mfma_f32_16x16x32_bf16 v[152:155], v[16:19], v[160:163], v[120:123]\n"
    "v_exp_f32 v132, v132 clamp\n"
    "v_mfma_f32_16x16x32_bf16 v[152:155], v[20:23], v[164:167], v[152:155]\n"
    "v_exp_f32 v133, v133 clamp\n"
    "v_mfma_f32_16x16x32_bf16 v[156:159], v[24:27], v[160:163], v[124:127]\n"
    "v_exp_f32 v134, v134 clamp\n"
    "v_mfma_f32_16x16x32_bf16 v[156:159], v[28:31], v[164:167], v[156:159]\n"
    "v_exp_f32 v135, v135 clamp\n"
    "s_add_u32 m0, s68, 2048\n"
    "v_exp_f32 v137, v137 clamp\n"
    "v_add_u32 v253, s67, v244\n"
    "v_mfma_f32_4x4x4_16b_bf16 v[224:227], v[228:229], v[176:177], v[224:227] cbsz:4 abid:0\n"
    "v_exp_f32 v138, v138 clamp\n"
    "v_exp_f32 v139, v139 clamp\n"
    "v_mfma_f32_4x4x4_16b_bf16 v[224:227], v[228:229], v[178:179], v[224:227] cbsz:4 abid:1\n"
    "v_exp_f32 v140, v140 clamp\n"
    "v_exp_f32 v141, v141 clamp\n"
    "v_mfma_f32_4x4x4_16b_bf16 v[224:227], v[228:229], v[180:181], v[224:227] cbsz:4 abid:2\n"
    "v_exp_f32 v142, v142 clamp\n"
    "v_exp_f32 v143, v143 clamp\n"
    "v_mfma_f32_4x4x4_16b_bf16 v[224:227], v[228:229], v[182:183], v[224:227] cbsz:4 abid:3\n"
    "s_waitcnt vmcnt(4)\n"
    "ds_read_b64_tr_b16 v[176:177], v240 offset:2048\n"
    "ds_read_b64_tr_b16 v[178:179], v241 offset:2304\n"
    "ds_read_b64_tr_b16 v[180:181], v242 offset:2560\n"
    "ds_read_b64_tr_b16 v[182:183], v243 offset:2816\n"
    "ds_read_b32 v236, v253 offset:0\n"
    "ds_read_b32 v238, v253 offset:4\n"
    "ds_read_b128 v[160:163], v234 offset:4096\n"
    "ds_read_b128 v[164:167], v235 offset:4096\n"
    "s_waitcnt lgkmcnt(8)\n"
    "s_nop 0\n"
    "v_readlane_b32 s20, v252, 0\n"
    "v_readlane_b32 s21, v252, 1\n"
    "v_readlane_b32 s22, v252, 2\n"
    "v_readlane_b32 s23, v252, 3\n"
    "v_readlane_b32 s24, v252, 4\n"
    "v_readlane_b32 s25, v252, 5\n"
    "v_readlane_b32 s26, v252, 6\n"
    "v_readlane_b32 s27, v252, 7\n"
    "v_pk_fma_f32 v[128:129], v[128:129], s[70:71], v[96:97]\n"
    "v_pk_fma_f32 v[130:131], v[130:131], s[70:71], v[98:99]\n"
    "v_pk_add_f32 v[230:231], v[128:129], v[130:131]\n"
    "v_pk_mul_f32 v[232:233], v[128:129], v[130:131]\n"
    "v_pk_fma_f32 v[132:133], v[132:133], s[70:71], v[100:101]\n"
    "v_pk_fma_f32 v[230:231], v[230:231], v[132:133], v[232:233]\n"
    "v_pk_mul_f32 v[232:233], v[232:233], v[132:133]\n"
    "v_pk_fma_f32 v[134:135], v[134:135], s[70:71], v[102:103]\n"
    "v_pk_fma_f32 v[230:231], v[230:231], v[134:135], v[232:233]\n"
    "v_pk_mul_f32 v[232:233], v[232:233], v[134:135]\n"
    "v_pk_fma_f32 v[136:137], v[136:137], s[70:71], v[104:105]\n"
    "v_pk_fma_f32 v[230:231], v[230:231], v[136:137], v[232:233]\n"
    "v_pk_mul_f32 v[232:233], v[232:233], v[136:137]\n"
    "v_pk_fma_f32 v[138:139], v[138:139], s[70:71], v[106:107]\n"
    "v_pk_fma_f32 v[230:231], v[230:231], v[138:139], v[232:233]\n"
    "v_pk_mul_f32 v[232:233], v[232:233], v[138:139]\n"
    "v_pk_fma_f32 v[140:141], v[140:141], s[70:71], v[108:109]\n"
    "v_pk_fma_f32 v[230:231], v[230:231], v[140:141], v[232:233]\n"
    "v_pk_mul_f32 v[232:233], v[232:233], v[140:141]\n"
    "v_pk_fma_f32 v[142:143], v[142:143], s[70:71], v[110:111]\n"
    "v_pk_fma_f32 v[230:231], v[230:231], v[142:143], v[232:233]\n"
    "v_pk_mul_f32 v[232:233], v[232:233], v[142:143]\n"
    "v_rcp_f32 v232, v232\n"
    "v_rcp_f32 v233, v233\n"
    "v_fma_f32 v230, v230, v232, v247\n"
    "v_fmac_f32 v230, v231, v233\n"
    "v_lshlrev_b32 v232, 2, v249\n"
    "v_lshrrev_b32 v233, 2, v249\n"
    "v_and_b32 v232, 56, v232\n"
    "v_and_b32 v233, 4, v233\n"
    "v_add3_u32 v232, v232, v233, s81\n"
    "ds_read_b32 v232, v232\n"
    "s_add_u32 s81, s81, 64\n"
    "s_waitcnt lgkmcnt(0)\n"
    "v_lshlrev_b32 v232, 1, v232\n"
    "buffer_load_ushort v232, v232, s[44:47], 0 offen\n"
    "s_waitcnt vmcnt(0)\n"
    "v_lshlrev_b32 v232, 16, v232\n"
    "v_mul_f32 v230, v230, v232\n"
    "v_cvt_pk_bf16_f32 v230, v230, v230\n"
    "s_add_u32 s67, s67, 64\n"
    "s_waitcnt lgkmcnt(0)\n"
    "ds_write_b16 v245, v230\n"
    "ds_read_b64 v[228:229], v246\n"
    "s_lshl1_add_u32 s76, s72, s76\n"
    "buffer_load_dwordx4 v[236:237], s[40:43], 0 idxen offen offset:0 nt lds\n"
    "buffer_load_dwordx4 v[238:239], s[40:43], 0 idxen offen offset:1024 nt lds\n"
    "s_and_b32 s62, s58, 63\n"
    "s_mul_i32 s62, s62, 272\n"
    "s_mov_b32 s58, s59\n"
    "s_mov_b32 s59, s60\n"
    "s_mov_b32 s81, s65\n"
    "s_xor_b32 s65, s65, s64\n"
    "s_mov_b32 s80, s83\n"
    "s_add_u32 s82, s65, 256\n"
    "s_cmp_lt_i32 s59, s57\n"
    "s_cselect_b32 s76, s76, -1\n"
    "s_bitcmp1_b32 s58, 30\n"
    "s_cbranch_scc0 .Ldin_A1\n"
    ".Ldin_A1s:\n"
    "v_mfma_f32_16x16x32_bf16 v[128:131], v[0:3], v[160:163], v[112:115]\n"
    "v_exp_f32 v144, v144 clamp\n"
    "v_mfma_f32_16x16x32_bf16 v[128:131], v[4:7], v[164:167], v[128:131]\n"
    "v_exp_f32 v145, v145 clamp\n"
    "v_mfma_f32_16x16x32_bf16 v[132:135], v[8:11], v[160:163], v[116:119]\n"
    "v_exp_f32 v146, v146 clamp\n"
    "v_mfma_f32_16x16x32_bf16 v[132:135], v[12:15], v[164:167], v[132:135]\n"
    "v_exp_f32 v147, v147 clamp\n"
    "v_mfma_f32_16x16x32_bf16 v[136:139], v[16:19], v[160:163], v[120:123]\n"
    "v_exp_f32 v148, v148 clamp\n"
    "v_mfma_f32_16x16x32_bf16 v[136:139], v[20:23], v[164:167], v[136:139]\n"
    "v_exp_f32 v149, v149 clamp\n"
    "v_mfma_f32_16x16x32_bf16 v[140:143], v[24:27], v[160:163], v[124:127]\n"
    "v_exp_f32 v150, v150 clamp\n"
    "v_mfma_f32_16x16x32_bf16 v[140:143], v[28:31], v[164:167], v[140:143]\n"
    "v_exp_f32 v151, v151 clamp\n"
    "v_exp_f32 v152, v152 clamp\n"
    "s_add_u32 m0, s68, 4096\n"
    "v_exp_f32 v153, v153 clamp\n"
    "v_add_u32_e64 v253, s67, v244\n"
    "s_waitcnt lgkmcnt(0)\n"
    "v_mfma_f32_4x4x4_16b_bf16 v[224:227], v[228:229], v[168:169], v[224:227] cbsz:4 abid:0\n"
    "v_exp_f32 v154, v154 clamp\n"
    "v_exp_f32 v155, v155 clamp\n"
    "v_mfma_f32_4x4x4_16b_bf16 v[224:227], v[228:229], v[170:171], v[224:227] cbsz:4 abid:1\n"
    "v_exp_f32 v156, v156 clamp\n"
    "v_exp_f32 v157, v157 clamp\n"
    "v_mfma_f32_4x4x4_16b_bf16 v[224:227], v[228:229], v[172:173], v[224:227] cbsz:4 abid:2\n"
    "v_exp_f32 v158, v158 clamp\n"
    "v_exp_f32 v159, v159 clamp\n"
    "v_mfma_f32_4x4x4_16b_bf16 v[224:227], v[228:229], v[174:175], v[224:227] cbsz:4 abid:3\n"
    "s_waitcnt vmcnt(4)\n"
    "ds_read_b64_tr_b16 v[168:169], v240 offset:4096\n"
    "ds_read_b64_tr_b16 v[170:171], v241 offset:4352\n"
    "ds_read_b64_tr_b16 v[172:173], v242 offset:4608\n"
    "ds_read_b64_tr_b16 v[174:175], v243 offset:4864\n"
    "ds_read_b32 v236, v253 offset:0\n"
    "ds_read_b32 v238, v253 offset:4\n"
    "ds_read_b128 v[160:163], v234 offset:6144\n"
    "ds_read_b128 v[164:167], v235 offset:6144\n"
    "s_cbranch_vccz .Ldin_A1s_np\n"
    "s_setprio 1\n"
    ".Ldin_A1s_np:\n"
    "v_pk_fma_f32 v[144:145], v[144:145], s[70:71], v[96:97]\n"
    "v_pk_fma_f32 v[146:147], v[146:147], s[70:71], v[98:99]\n"
    "v_add_f32 v191, v224, v225\n"
    "v_add_f32 v190, v226, v227\n"
    "v_add_f32 v191, v191, v190\n"
    "v_add_u32 v190, s62, v249\n"
    "v_mul_f32 v191, s69, v191\n"
    "v_cvt_pk_bf16_f32 v191, v191, v191\n"
    "ds_write_b16 v190, v191 offset:32768\n"
    "s_bfe_u32 s94, s59, 0x180006\n"
    "s_mul_i32 s86, s94, s56\n"
    "v_mbcnt_lo_u32_b32 v184, s20, 0\n"
    "v_mbcnt_lo_u32_b32 v185, s22, 0\n"
    "v_mbcnt_lo_u32_b32 v186, s24, 0\n"
    "v_mbcnt_lo_u32_b32 v187, s26, 0\n"
    "s_bcnt1_i32_b64 s90, s[20:21]\n"
    "s_bcnt1_i32_b64 s91, s[22:23]\n"
    "s_bcnt1_i32_b64 s92, s[24:25]\n"
    "s_bcnt1_i32_b64 s93, s[26:27]\n"
    "v_mbcnt_hi_u32_b32 v184, s21, v184\n"
    "v_mbcnt_hi_u32_b32 v185, s23, v185\n"
    "v_mbcnt_hi_u32_b32 v186, s25, v186\n"
    "v_mbcnt_hi_u32_b32 v187, s27, v187\n"
    "s_lshl2_add_u32 s90, s90, s65\n"
    "s_lshl2_add_u32 s91, s91, s90\n"
    "s_lshl2_add_u32 s92, s92, s91\n"
    "s_lshl2_add_u32 s93, s93, s92\n"
    "v_add_u32_e64 v188, s86, v251\n"
    "v_pk_add_f32 v[230:231], v[144:145], v[146:147]\n"
    "v_pk_mul_f32 v[232:233], v[144:145], v[146:147]\n"
    "v_pk_fma_f32 v[148:149], v[148:149], s[70:71], v[100:101]\n"
    "v_pk_fma_f32 v[230:231], v[230:231], v[148:149], v[232:233]\n"
    "v_pk_mul_f32 v[232:233], v[232:233], v[148:149]\n"
    "v_lshl_add_u32 v184, v184, 2, s65\n"
    "v_lshl_add_u32 v185, v185, 2, s90\n"
    "v_lshl_add_u32 v186, v186, 2, s91\n"
    "v_lshl_add_u32 v187, v187, 2, s92\n"
    "v_mov_b32 v191, -1\n"
    "v_lshl_add_u32 v190, v249, 1, s93\n"
    "ds_write2_b32 v190, v191, v191 offset1:64\n"
    "s_mov_b64 exec, s[20:21]\n"
    "ds_write_b32 v184, v188\n"
    "s_mov_b64 exec, s[22:23]\n"
    "v_add_u32 v189, 64, v188\n"
    "ds_write_b32 v185, v189\n"
    "s_mov_b64 exec, s[24:25]\n"
    "v_add_u32 v190, 128, v188\n"
    "ds_write_b32 v186, v190\n"
    "s_mov_b64 exec, s[26:27]\n"
    "v_add_u32 v189, 192, v188\n"
    "ds_write_b32 v187, v189\n"
    "s_mov_b64 exec, -1\n"
    "s_add_u32 s95, s93, 60\n"
    "s_add_u32 s94, s82, 128\n"
    "s_andn2_b32 s95, s95, 63\n"
    "s_max_u32 s83, s95, s94\n"
    "v_pk_fma_f32 v[150:151], v[150:151], s[70:71], v[102:103]\n"
    "v_pk_fma_f32 v[230:231], v[230:231], v[150:151], v[232:233]\n"
    "v_pk_mul_f32 v[232:233], v[232:233], v[150:151]\n"
    "v_pk_fma_f32 v[152:153], v[152:153], s[70:71], v[104:105]\n"
    "v_pk_fma_f32 v[230:231], v[230:231], v[152:153], v[232:233]\n"
    "v_pk_mul_f32 v[232:233], v[232:233], v[152:153]\n"
    "v_pk_fma_f32 v[154:155], v[154:155], s[70:71], v[106:107]\n"
    "v_pk_fma_f32 v[230:231], v[230:231], v[154:155], v[232:233]\n"
    "v_pk_mul_f32 v[232:233], v[232:233], v[154:155]\n"
    "v_pk_fma_f32 v[156:157], v[156:157], s[70:71], v[108:109]\n"
    "v_pk_fma_f32 v[230:231], v[230:231], v[156:157], v[232:233]\n"
    "v_pk_mul_f32 v[232:233], v[232:233], v[156:157]\n"
    "v_pk_fma_f32 v[158:159], v[158:159], s[70:71], v[110:111]\n"
    "v_pk_fma_f32 v[230:231], v[230:231], v[158:159], v[232:233]\n"
    "v_pk_mul_f32 v[232:233], v[232:233], v[158:159]\n"
    "v_rcp_f32 v232, v232\n"
    "v_rcp_f32 v233, v233\n"
    "v_fma_f32 v230, v230, v232, v247\n"
    "v_fmac_f32 v230, v231, v233\n"
    "v_lshlrev_b32 v232, 2, v249\n"
    "v_lshrrev_b32 v233, 2, v249\n"
    "v_and_b32 v232, 56, v232\n"
    "v_and_b32 v233, 4, v233\n"
    "v_add3_u32 v232, v232, v233, s81\n"
    "ds_read_b32 v232, v232\n"
    "s_add_u32 s81, s81, 64\n"
    "s_waitcnt lgkmcnt(0)\n"
    "v_lshlrev_b32 v232, 1, v232\n"
    "buffer_load_ushort v232, v232, s[44:47], 0 offen\n"
    "s_waitcnt vmcnt(0)\n"
    "v_lshlrev_b32 v232, 16, v232\n"
    "v_mul_f32 v230, v230, v232\n"
    "v_cvt_pk_bf16_f32 v230, v230, v230\n"
    "s_add_u32 s67, s67, 64\n"
    "s_waitcnt lgkmcnt(0)\n"
    "ds_write_b16 v245, v230\n"
    "ds_read_b64 v[228:229], v246\n"
    "buffer_load_dwordx4 v[236:237], s[40:43], 0 idxen offen offset:0 nt lds\n"
    "buffer_load_dwordx4 v[238:239], s[40:43], 0 idxen offen offset:1024 nt lds\n"
    ".Ldin_B2s:\n"
    "v_mfma_f32_16x16x32_bf16 v[144:147], v[0:3], v[160:163], v[112:115]\n"
    "v_exp_f32 v128, v128 clamp\n"
    "v_mfma_f32_16x16x32_bf16 v[144:147], v[4:7], v[164:167], v[144:147]\n"
    "v_exp_f32 v129, v129 clamp\n"
    "v_mfma_f32_16x16x32_bf16 v[148:151], v[8:11], v[160:163], v[116:119]\n"
    "v_exp_f32 v130, v130 clamp\n"
    "v_mfma_f32_16x16x32_bf16 v[148:151], v[12:15], v[164:167], v[148:151]\n"
    "v_exp_f32 v131, v131 clamp\n"
    "v_mfma_f32_16x16x32_bf16 v[152:155], v[16:19], v[160:163], v[120:123]\n"
    "v_exp_f32 v132, v132 clamp\n"
    "v_mfma_f32_16x16x32_bf16 v[152:155], v[20:23], v[164:167], v[152:155]\n"
    "v_exp_f32 v133, v133 clamp\n"
    "v_mfma_f32_16x16x32_bf16 v[156:159], v[24:27], v[160:163], v[124:127]\n"
    "v_exp_f32 v134, v134 clamp\n"
    "v_mfma_f32_16x16x32_bf16 v[156:159], v[28:31], v[164:167], v[156:159]\n"
    "v_exp_f32 v135, v135 clamp\n"
    "v_exp_f32 v136, v136 clamp\n"
    "s_cmp_eq_u32 s67, s80\n"
    "s_cselect_b32 s67, s65, s67\n"
    "s_and_b32 s94, s59, 63\n"
    "s_mul_i32 s94, s94, 272\n"
    "v_add_u32 v252, s94, v250\n"
    "s_add_u32 m0, s68, 6144\n"
    "v_exp_f32 v137, v137 clamp\n"
    "v_add_u32 v253, s67, v244\n"
    "s_waitcnt lgkmcnt(0)\n"
    "v_mfma_f32_4x4x4_16b_bf16 v[224:227], v[228:229], v[176:177], 0 cbsz:4 abid:0\n"
    "v_exp_f32 v138, v138 clamp\n"
    "v_exp_f32 v139, v139 clamp\n"
    "v_mfma_f32_4x4x4_16b_bf16 v[224:227], v[228:229], v[178:179], v[224:227] cbsz:4 abid:1\n"
    "v_exp_f32 v140, v140 clamp\n"
    "v_exp_f32 v141, v141 clamp\n"
    "v_mfma_f32_4x4x4_16b_bf16 v[224:227], v[228:229], v[180:181], v[224:227] cbsz:4 abid:2\n"
    "v_exp_f32 v142, v142 clamp\n"
    "v_exp_f32 v143, v143 clamp\n"
    "v_mfma_f32_4x4x4_16b_bf16 v[224:227], v[228:229], v[182:183], v[224:227] cbsz:4 abid:3\n"
    "s_waitcnt vmcnt(4)\n"
    "s_memrealtime s[34:35]\n"
    "ds_read_b64_tr_b16 v[176:177], v240 offset:6144\n"
    "ds_read_b64_tr_b16 v[178:179], v241 offset:6400\n"
    "ds_read_b64_tr_b16 v[180:181], v242 offset:6656\n"
    "ds_read_b64_tr_b16 v[182:183], v243 offset:6912\n"
    "ds_read_b32 v236, v253 offset:0\n"
    "ds_read_b32 v238, v253 offset:4\n"
    "ds_read_b128 v[160:163], v234 offset:0\n"
    "ds_read_b128 v[164:167], v235 offset:0\n"
    "ds_read_b128 v[184:187], v252 offset:0\n"
    "ds_read_b128 v[188:191], v252 offset:16\n"
    "v_pk_fma_f32 v[128:129], v[128:129], s[70:71], v[96:97]\n"
    "v_pk_fma_f32 v[130:131], v[130:131], s[70:71], v[98:99]\n"
    "v_pk_add_f32 v[230:231], v[128:129], v[130:131]\n"
    "v_pk_mul_f32 v[232:233], v[128:129], v[130:131]\n"
    "v_pk_fma_f32 v[132:133], v[132:133], s[70:71], v[100:101]\n"
    "v_pk_fma_f32 v[230:231], v[230:231], v[132:133], v[232:233]\n"
    "v_pk_mul_f32 v[232:233], v[232:233], v[132:133]\n"
    "v_pk_fma_f32 v[134:135], v[134:135], s[70:71], v[102:103]\n"
    "v_pk_fma_f32 v[230:231], v[230:231], v[134:135], v[232:233]\n"
    "v_pk_mul_f32 v[232:233], v[232:233], v[134:135]\n"
    "v_pk_fma_f32 v[136:137], v[136:137], s[70:71], v[104:105]\n"
    "v_pk_fma_f32 v[230:231], v[230:231], v[136:137], v[232:233]\n"
    "v_pk_mul_f32 v[232:233], v[232:233], v[136:137]\n"
    "v_pk_fma_f32 v[138:139], v[138:139], s[70:71], v[106:107]\n"
    "v_pk_fma_f32 v[230:231], v[230:231], v[138:139], v[232:233]\n"
    "v_pk_mul_f32 v[232:233], v[232:233], v[138:139]\n"
    "v_pk_fma_f32 v[140:141], v[140:141], s[70:71], v[108:109]\n"
    "v_pk_fma_f32 v[230:231], v[230:231], v[140:141], v[232:233]\n"
    "v_pk_mul_f32 v[232:233], v[232:233], v[140:141]\n"
    "v_pk_fma_f32 v[142:143], v[142:143], s[70:71], v[110:111]\n"
    "v_pk_fma_f32 v[230:231], v[230:231], v[142:143], v[232:233]\n"
    "v_pk_mul_f32 v[232:233], v[232:233], v[142:143]\n"
    "v_rcp_f32 v232, v232\n"
    "v_rcp_f32 v233, v233\n"
    "v_fma_f32 v230, v230, v232, v247\n"
    "v_fmac_f32 v230, v231, v233\n"
    "v_lshlrev_b32 v232, 2, v249\n"
    "v_lshrrev_b32 v233, 2, v249\n"
    "v_and_b32 v232, 56, v232\n"
    "v_and_b32 v233, 4, v233\n"
    "v_add3_u32 v232, v232, v233, s81\n"
    "ds_read_b32 v232, v232\n"
    "s_add_u32 s81, s81, 64\n"
    "s_waitcnt lgkmcnt(0)\n"
    "v_lshlrev_b32 v232, 1, v232\n"
    "buffer_load_ushort v232, v232, s[44:47], 0 offen\n"
    "s_waitcnt vmcnt(0)\n"
    "v_lshlrev_b32 v232, 16, v232\n"
    "v_mul_f32 v230, v230, v232\n"
    "v_cvt_pk_bf16_f32 v230, v230, v230\n"
    "s_add_u32 s67, s67, 64\n"
    "s_waitcnt lgkmcnt(0)\n"
    "ds_write_b16 v245, v230\n"
    "ds_read_b64 v[228:229], v246\n"
    "s_cmp_le_u32 s34, s76\n"
    "s_cbranch_scc1 .Ldin_B2s_wait\n"
    ".Ldin_B2s_go:\n"
    "s_sub_u32 s94, s34, 60\n"
    "s_max_u32 s76, s76, s94\n"
    "s_add_u32 s76, s76, s72\n"
    ".Ldin_B2s_dma:\n"
    "buffer_load_dwordx4 v[236:237], s[40:43], 0 idxen offen offset:0 nt lds\n"
    "buffer_load_dwordx4 v[238:239], s[40:43], 0 idxen offen offset:1024 nt lds\n"
    ".Ldin_B2s_nd:\n"
    "s_branch .Ldin_G3s\n"
    ".Ldin_L0s_poll:\n"
    "s_sleep 1\n"
    "ds_read_b64 v[232:233], v253\n"
    "s_waitcnt lgkmcnt(0)\n"
    "v_readfirstlane_b32 s60, v232\n"
    "s_cmp_eq_u32 s60, -1\n"
    "s_cbranch_scc1 .Ldin_L0s_poll\n"
    "s_branch .Ldin_L0s_pr\n"
    ".Ldin_B2s_wait:\n"
    "s_cmp_lt_i32 s59, s57\n"
    "s_cbranch_scc1 .Ldin_B2s_w2\n"
    "s_sub_u32 s94, s67, s65\n"
    "s_cmp_gt_u32 s94, 256\n"
    "s_cbranch_scc1 .Ldin_B2s_dma\n"
    "s_cmp_eq_u32 s94, 64\n"
    "s_waitcnt vmcnt(2)\n"
    "s_cbranch_scc1 .Ldin_B2s_nd\n"
    "s_waitcnt vmcnt(0)\n"
    "s_branch .Ldin_B2s_nd\n"
    ".Ldin_B2s_slp:\n"
    "s_sleep 1\n"
    ".Ldin_B2s_w2:\n"
    "s_memrealtime s[34:35]\n"
    "s_waitcnt lgkmcnt(0)\n"
    "s_sub_u32 s94, s34, s76\n"
    "s_cmp_lt_i32 s94, 0\n"
    "s_cbranch_scc1 .Ldin_B2s_slp\n"
    "s_branch .Ldin_B2s_go\n"
    ".p2align 5\n"
    ".Ldin_L3s:\n"
    "s_setprio 0\n"
    "s_cmp_ge_i32 s59, s57\n"
    "s_cbranch_scc1 .Ldin_E1s\n"
    "s_add_u32 s76, s76, s73\n"
    "v_mov_b32 v253, s63\n"
    "v_and_b32 v184, v254, v184\n"
    "v_and_b32 v185, v255, v185\n"
    "v_and_b32 v186, v254, v186\n"
    "v_and_b32 v187, v255, v187\n"
    "v_and_b32 v188, v254, v188\n"
    "v_and_b32 v189, v255, v189\n"
    "v_and_b32 v190, v254, v190\n"
    "v_and_b32_e64 v191, v255, v191\n"
    "ds_read_b64 v[232:233], v253\n"
    "v_mfma_f32_4x4x4_16b_bf16 v[128:131], v[184:185], v[32:33], v[64:67]\n"
    "ds_read_b128 v[112:115], v252 offset:17408\n"
    "v_mfma_f32_4x4x4_16b_bf16 v[132:135], v[186:187], v[34:35], v[68:71]\n"
    "ds_read_b128 v[116:119], v252 offset:17424\n"
    "v_mfma_f32_4x4x4_16b_bf16 v[136:139], v[188:189], v[36:37], v[72:75]\n"
    "ds_read_b128 v[120:123], v252 offset:17536\n"
    "ds_read_b128 v[124:127], v252 offset:17552\n"
    "v_cvt_pk_bf16_f32 v0, v128, v129\n"
    "v_cvt_pk_bf16_f32 v1, v130, v131\n"
    "v_mfma_f32_4x4x4_16b_bf16 v[140:143], v[190:191], v[38:39], v[76:79]\n"
    "v_cvt_pk_bf16_f32 v2, v132, v133\n"
    "v_cvt_pk_bf16_f32 v3, v134, v135\n"
    "v_mfma_f32_4x4x4_16b_bf16 v[128:131], v[184:185], v[40:41], v[80:83]\n"
    "v_cvt_pk_bf16_f32 v4, v136, v137\n"
    "v_cvt_pk_bf16_f32 v5, v138, v139\n"
    "v_mfma_f32_4x4x4_16b_bf16 v[132:135], v[186:187], v[42:43], v[84:87]\n"
    "v_cvt_pk_bf16_f32 v6, v140, v141\n"
    "v_cvt_pk_bf16_f32 v7, v142, v143\n"
    "v_mfma_f32_4x4x4_16b_bf16 v[136:139], v[188:189], v[44:45], v[88:91]\n"
    "v_cvt_pk_bf16_f32 v8, v128, v129\n"
    "v_cvt_pk_bf16_f32 v9, v130, v131\n"
    "v_mfma_f32_4x4x4_16b_bf16 v[140:143], v[190:191], v[46:47], v[92:95]\n"
    "v_cvt_pk_bf16_f32 v10, v132, v133\n"
    "v_cvt_pk_bf16_f32 v11, v134, v135\n"
    "v_mfma_f32_4x4x4_16b_bf16 v[128:131], v[184:185], v[48:49], v[192:195]\n"
    "v_cvt_pk_bf16_f32 v12, v136, v137\n"
    "v_cvt_pk_bf16_f32 v13, v138, v139\n"
    "v_mfma_f32_4x4x4_16b_bf16 v[132:135], v[186:187], v[50:51], v[196:199]\n"
    "v_cvt_pk_bf16_f32 v14, v140, v141\n"
    "v_cvt_pk_bf16_f32 v15, v142, v143\n"
    "v_mfma_f32_4x4x4_16b_bf16 v[136:139], v[188:189], v[52:53], v[200:203]\n"
    "s_waitcnt lgkmcnt(0)\n"
    "v_readfirstlane_b32 s60, v232\n"
    "v_cvt_pk_bf16_f32 v16, v128, v129\n"
    "v_cvt_pk_bf16_f32 v17, v130, v131\n"
    "v_mfma_f32_4x4x4_16b_bf16 v[140:143], v[190:191], v[54:55], v[204:207]\n"
    "s_cmp_eq_u32 s60, -1\n"
    "s_cbranch_scc1 .Ldin_L3s_poll\n"
    ".Ldin_L3s_pr:\n"
    "v_readfirstlane_b32 s95, v233\n"
    "s_and_b32 s94, s60, 63\n"
    "s_lshl_b32 s94, s94, 6\n"
    "s_add_u32 s94, s94, 68480\n"
    "s_add_u32 s63, s63, 8\n"
    "v_lshl_add_u32 v252, v249, 1, s94\n"
    "ds_read_b32 v252, v252\n"
    "s_sub_u32 s95, s95, s76\n"
    "s_ashr_i32 s95, s95, 4\n"
    "s_min_i32 s95, s95, s74\n"
    "s_max_i32 s72, s95, s75\n"
    "v_cvt_pk_bf16_f32 v18, v132, v133\n"
    "v_cvt_pk_bf16_f32 v19, v134, v135\n"
    "v_mfma_f32_4x4x4_16b_bf16 v[128:131], v[184:185], v[56:57], v[208:211]\n"
    "v_cvt_pk_bf16_f32 v20, v136, v137\n"
    "v_cvt_pk_bf16_f32 v21, v138, v139\n"
    "v_mfma_f32_4x4x4_16b_bf16 v[132:135], v[186:187], v[58:59], v[212:215]\n"
    "v_cvt_pk_bf16_f32 v22, v140, v141\n"
    "v_cvt_pk_bf16_f32 v23, v142, v143\n"
    "v_mfma_f32_4x4x4_16b_bf16 v[136:139], v[188:189], v[60:61], v[216:219]\n"
    "v_cvt_pk_bf16_f32 v24, v128, v129\n"
    "v_cvt_pk_bf16_f32 v25, v130, v131\n"
    "v_mfma_f32_4x4x4_16b_bf16 v[140:143], v[190:191], v[62:63], v[220:223]\n"
    "v_cvt_pk_bf16_f32 v26, v132, v133\n"
    "v_cvt_pk_bf16_f32 v27, v134, v135\n"
    "v_cvt_pk_bf16_f32 v28, v136, v137\n"
    "v_cvt_pk_bf16_f32 v29, v138, v139\n"
    "v_exp_f32 v152, v152 clamp\n"
    "v_cvt_pk_bf16_f32 v30, v140, v141\n"
    "v_cvt_pk_bf16_f32 v31, v142, v143\n"
    "v_mfma_f32_16x16x32_bf16 v[128:131], v[0:3], v[160:163], v[112:115]\n"
    "v_exp_f32 v144, v144 clamp\n"
    "v_mfma_f32_16x16x32_bf16 v[128:131], v[4:7], v[164:167], v[128:131]\n"
    "v_exp_f32 v145, v145 clamp\n"
    "v_mfma_f32_16x16x32_bf16 v[132:135], v[8:11], v[160:163], v[116:119]\n"
    "v_exp_f32 v146, v146 clamp\n"
    "v_mfma_f32_16x16x32_bf16 v[132:135], v[12:15], v[164:167], v[132:135]\n"
    "v_exp_f32 v147, v147 clamp\n"
    "v_mfma_f32_16x16x32_bf16 v[136:139], v[16:19], v[160:163], v[120:123]\n"
    "v_exp_f32 v148, v148 clamp\n"
    "v_mfma_f32_16x16x32_bf16 v[136:139], v[20:23], v[164:167], v[136:139]\n"
    "v_exp_f32 v149, v149 clamp\n"
    "v_mfma_f32_16x16x32_bf16 v[140:143], v[24:27], v[160:163], v[124:127]\n"
    "v_exp_f32 v150, v150 clamp\n"
    "v_mfma_f32_16x16x32_bf16 v[140:143], v[28:31], v[164:167], v[140:143]\n"
    "v_exp_f32 v151, v151 clamp\n"
    "s_add_u32 m0, s68, 0\n"
    "v_exp_f32 v153, v153 clamp\n"
    "v_add_u32_e64 v253, s67, v244\n"
    "v_mfma_f32_4x4x4_16b_bf16 v[224:227], v[228:229], v[168:169], v[224:227] cbsz:4 abid:0\n"
    "v_exp_f32 v154, v154 clamp\n"
    "v_exp_f32 v155, v155 clamp\n"
    "v_mfma_f32_4x4x4_16b_bf16 v[224:227], v[228:229], v[170:171], v[224:227] cbsz:4 abid:1\n"
    "v_exp_f32 v156, v156 clamp\n"
    "v_exp_f32 v157, v157 clamp\n"
    "v_mfma_f32_4x4x4_16b_bf16 v[224:227], v[228:229], v[172:173], v[224:227] cbsz:4 abid:2\n"
    "v_exp_f32 v158, v158 clamp\n"
    "v_exp_f32 v159, v159 clamp\n"
    "v_mfma_f32_4x4x4_16b_bf16 v[224:227], v[228:229], v[174:175], v[224:227] cbsz:4 abid:3\n"
    "s_waitcnt vmcnt(4)\n"
    "ds_read_b64_tr_b16 v[168:169], v240 offset:0\n"
    "ds_read_b64_tr_b16 v[170:171], v241 offset:256\n"
    "ds_read_b64_tr_b16 v[172:173], v242 offset:512\n"
    "ds_read_b64_tr_b16 v[174:175], v243 offset:768\n"
    "ds_read_b32 v236, v253 offset:0\n"
    "ds_read_b32 v238, v253 offset:4\n"
    "ds_read_b128 v[160:163], v234 offset:2048\n"
    "ds_read_b128 v[164:167], v235 offset:2048\n"
    "s_waitcnt lgkmcnt(8)\n"
    "s_nop 0\n"
    "v_readlane_b32 s20, v252, 0\n"
    "v_readlane_b32 s21, v252, 1\n"
    "v_readlane_b32 s22, v252, 2\n"
    "v_readlane_b32 s23, v252, 3\n"
    "v_readlane_b32 s24, v252, 4\n"
    "v_readlane_b32 s25, v252, 5\n"
    "v_readlane_b32 s26, v252, 6\n"
    "v_readlane_b32 s27, v252, 7\n"
    "v_pk_fma_f32 v[144:145], v[144:145], s[70:71], v[96:97]\n"
    "v_pk_fma_f32 v[146:147], v[146:147], s[70:71], v[98:99]\n"
    "v_pk_add_f32 v[230:231], v[144:145], v[146:147]\n"
    "v_pk_mul_f32 v[232:233], v[144:145], v[146:147]\n"
    "v_pk_fma_f32 v[148:149], v[148:149], s[70:71], v[100:101]\n"
    "v_pk_fma_f32 v[230:231], v[230:231], v[148:149], v[232:233]\n"
    "v_pk_mul_f32 v[232:233], v[232:233], v[148:149]\n"
    "v_pk_fma_f32 v[150:151], v[150:151], s[70:71], v[102:103]\n"
    "v_pk_fma_f32 v[230:231], v[230:231], v[150:151], v[232:233]\n"
    "v_pk_mul_f32 v[232:233], v[232:233], v[150:151]\n"
    "v_pk_fma_f32 v[152:153], v[152:153], s[70:71], v[104:105]\n"
    "v_pk_fma_f32 v[230:231], v[230:231], v[152:153], v[232:233]\n"
    "v_pk_mul_f32 v[232:233], v[232:233], v[152:153]\n"
    "v_pk_fma_f32 v[154:155], v[154:155], s[70:71], v[106:107]\n"
    "v_pk_fma_f32 v[230:231], v[230:231], v[154:155], v[232:233]\n"
    "v_pk_mul_f32 v[232:233], v[232:233], v[154:155]\n"
    "v_pk_fma_f32 v[156:157], v[156:157], s[70:71], v[108:109]\n"
    "v_pk_fma_f32 v[230:231], v[230:231], v[156:157], v[232:233]\n"
    "v_pk_mul_f32 v[232:233], v[232:233], v[156:157]\n"
    "v_pk_fma_f32 v[158:159], v[158:159], s[70:71], v[110:111]\n"
    "v_pk_fma_f32 v[230:231], v[230:231], v[158:159], v[232:233]\n"
    "v_pk_mul_f32 v[232:233], v[232:233], v[158:159]\n"
    "v_rcp_f32 v232, v232\n"
    "v_rcp_f32 v233, v233\n"
    "v_fma_f32 v230, v230, v232, v247\n"
    "v_fmac_f32 v230, v231, v233\n"
    "v_lshlrev_b32 v232, 2, v249\n"
    "v_lshrrev_b32 v233, 2, v249\n"
    "v_and_b32 v232, 56, v232\n"
    "v_and_b32 v233, 4, v233\n"
    "v_add3_u32 v232, v232, v233, s81\n"
    "ds_read_b32 v232, v232\n"
    "s_add_u32 s81, s81, 64\n"
    "s_waitcnt lgkmcnt(0)\n"
    "v_lshlrev_b32 v232, 1, v232\n"
    "buffer_load_ushort v232, v232, s[44:47], 0 offen\n"
    "s_waitcnt vmcnt(0)\n"
    "v_lshlrev_b32 v232, 16, v232\n"
    "v_mul_f32 v230, v230, v232\n"
    "v_cvt_pk_bf16_f32 v230, v230, v230\n"
    "s_sub_u32 s67, s67, -64\n"
    "s_waitcnt lgkmcnt(0)\n"
    "ds_write_b16 v245, v230\n"
    "ds_read_b64 v[228:229], v246\n"
    "s_lshl1_add_u32 s76, s72, s76\n"
    "buffer_load_dwordx4 v[236:237], s[40:43], 0 idxen offen offset:0 nt lds\n"
    "buffer_load_dwordx4 v[238:239], s[40:43], 0 idxen offen offset:1024 nt lds\n"
    "s_and_b32 s62, s58, 63\n"
    "s_mul_i32 s62, s62, 272\n"
    "s_mov_b32 s58, s59\n"
    "s_mov_b32 s59, s60\n"
    "s_mov_b32 s81, s65\n"
    "s_xor_b32 s65, s65, s64\n"
    "s_mov_b32 s80, s83\n"
    "s_add_u32 s82, s65, 256\n"
    "s_cmp_lt_i32 s59, s57\n"
    "s_cselect_b32 s76, s76, -1\n"
    "s_bitcmp1_b32 s58, 30\n"
    "s_cbranch_scc0 .Ldin_A0\n"
    "s_branch .Ldin_A0s\n"
    ".Ldin_L3s_poll:\n"
    "s_sleep 1\n"
    "ds_read_b64 v[232:233], v253\n"
    "s_waitcnt lgkmcnt(0)\n"
    "v_readfirstlane_b32 s60, v232\n"
    "s_cmp_eq_u32 s60, -1\n"
    "s_cbranch_scc1 .Ldin_L3s_poll\n"
    "s_branch .Ldin_L3s_pr\n"
    ".Ldin_E0s:\n"
    "v_exp_f32 v128, v128 clamp\n"
    "v_exp_f32 v129, v129 clamp\n"
    "v_exp_f32 v130, v130 clamp\n"
    "v_exp_f32 v131, v131 clamp\n"
    "v_exp_f32 v132, v132 clamp\n"
    "v_exp_f32 v133, v133 clamp\n"
    "v_exp_f32 v134, v134 clamp\n"
    "v_exp_f32 v135, v135 clamp\n"
    "s_and_b32 s62, s58, 63\n"
    "s_mul_i32 s62, s62, 272\n"
    "s_waitcnt lgkmcnt(0)\n"
    "v_mfma_f32_4x4x4_16b_bf16 v[224:227], v[228:229], v[176:177], v[224:227] cbsz:4 abid:0\n"
    "v_exp_f32 v136, v136 clamp\n"
    "v_exp_f32 v137, v137 clamp\n"
    "v_mfma_f32_4x4x4_16b_bf16 v[224:227], v[228:229], v[178:179], v[224:227] cbsz:4 abid:1\n"
    "v_exp_f32 v138, v138 clamp\n"
    "v_exp_f32 v139, v139 clamp\n"
    "v_mfma_f32_4x4x4_16b_bf16 v[224:227], v[228:229], v[180:181], v[224:227] cbsz:4 abid:2\n"
    "v_exp_f32 v140, v140 clamp\n"
    "v_exp_f32 v141, v141 clamp\n"
    "v_mfma_f32_4x4x4_16b_bf16 v[224:227], v[228:229], v[182:183], v[224:227] cbsz:4 abid:3\n"
    "v_exp_f32 v142, v142 clamp\n"
    "v_exp_f32 v143, v143 clamp\n"
    "v_pk_fma_f32 v[128:129], v[128:129], s[70:71], v[96:97]\n"
    "v_pk_fma_f32 v[130:131], v[130:131], s[70:71], v[98:99]\n"
    "v_pk_add_f32 v[230:231], v[128:129], v[130:131]\n"
    "v_pk_mul_f32 v[232:233], v[128:129], v[130:131]\n"
    "v_pk_fma_f32 v[132:133], v[132:133], s[70:71], v[100:101]\n"
    "v_pk_fma_f32 v[230:231], v[230:231], v[132:133], v[232:233]\n"
    "v_pk_mul_f32 v[232:233], v[232:233], v[132:133]\n"
    "v_pk_fma_f32 v[134:135], v[134:135], s[70:71], v[102:103]\n"
    "v_pk_fma_f32 v[230:231], v[230:231], v[134:135], v[232:233]\n"
    "v_pk_mul_f32 v[232:233], v[232:233], v[134:135]\n"
    "v_pk_fma_f32 v[136:137], v[136:137], s[70:71], v[104:105]\n"
    "v_pk_fma_f32 v[230:231], v[230:231], v[136:137], v[232:233]\n"
    "v_pk_mul_f32 v[232:233], v[232:233], v[136:137]\n"
    "v_pk_fma_f32 v[138:139], v[138:139], s[70:71], v[106:107]\n"
    "v_pk_fma_f32 v[230:231], v[230:231], v[138:139], v[232:233]\n"
    "v_pk_mul_f32 v[232:233], v[232:233], v[138:139]\n"
    "v_pk_fma_f32 v[140:141], v[140:141], s[70:71], v[108:109]\n"
    "v_pk_fma_f32 v[230:231], v[230:231], v[140:141], v[232:233]\n"
    "v_pk_mul_f32 v[232:233], v[232:233], v[140:141]\n"
    "v_pk_fma_f32 v[142:143], v[142:143], s[70:71], v[110:111]\n"
    "v_pk_fma_f32 v[230:231], v[230:231], v[142:143], v[232:233]\n"
    "v_pk_mul_f32 v[232:233], v[232:233], v[142:143]\n"
    "v_rcp_f32 v232, v232\n"
    "v_rcp_f32 v233, v233\n"
    "v_fma_f32 v230, v230, v232, v247\n"
    "v_fmac_f32 v230, v231, v233\n"
    "v_lshlrev_b32 v232, 2, v249\n"
    "v_lshrrev_b32 v233, 2, v249\n"
    "v_and_b32 v232, 56, v232\n"
    "v_and_b32 v233, 4, v233\n"
    "v_add3_u32 v232, v232, v233, s81\n"
    "ds_read_b32 v232, v232\n"
    "s_add_u32 s81, s81, 64\n"
    "s_waitcnt lgkmcnt(0)\n"
    "v_lshlrev_b32 v232, 1, v232\n"
    "buffer_load_ushort v232, v232, s[44:47], 0 offen\n"
    "s_waitcnt vmcnt(0)\n"
    "v_lshlrev_b32 v232, 16, v232\n"
    "v_mul_f32 v230, v230, v232\n"
    "v_cvt_pk_bf16_f32 v230, v230, v230\n"
    "ds_write_b16 v245, v230\n"
    "ds_read_b64 v[228:229], v246\n"
    "s_branch .Ldin_drain0\n"
    ".Ldin_E1s:\n"
    "v_exp_f32 v144, v144 clamp\n"
    "v_exp_f32 v145, v145 clamp\n"
    "v_exp_f32 v146, v146 clamp\n"
    "v_exp_f32 v147, v147 clamp\n"
    "v_exp_f32 v148, v148 clamp\n"
    "v_exp_f32 v149, v149 clamp\n"
    "v_exp_f32 v150, v150 clamp\n"
    "v_exp_f32 v151, v151 clamp\n"
    "s_and_b32 s62, s58, 63\n"
    "s_mul_i32 s62, s62, 272\n"
    "s_waitcnt lgkmcnt(0)\n"
    "v_mfma_f32_4x4x4_16b_bf16 v[224:227], v[228:229], v[168:169], v[224:227] cbsz:4 abid:0\n"
    "v_exp_f32 v152, v152 clamp\n"
    "v_exp_f32 v153, v153 clamp\n"
    "v_mfma_f32_4x4x4_16b_bf16 v[224:227], v[228:229], v[170:171], v[224:227] cbsz:4 abid:1\n"
    "v_exp_f32 v154, v154 clamp\n"
    "v_exp_f32 v155, v155 clamp\n"
    "v_mfma_f32_4x4x4_16b_bf16 v[224:227], v[228:229], v[172:173], v[224:227] cbsz:4 abid:2\n"
    "v_exp_f32 v156, v156 clamp\n"
    "v_exp_f32 v157, v157 clamp\n"
    "v_mfma_f32_4x4x4_16b_bf16 v[224:227], v[228:229], v[174:175], v[224:227] cbsz:4 abid:3\n"
    "v_exp_f32 v158, v158 clamp\n"
    "v_exp_f32 v159, v159 clamp\n"
    "v_pk_fma_f32 v[144:145], v[144:145], s[70:71], v[96:97]\n"
    "v_pk_fma_f32 v[146:147], v[146:147], s[70:71], v[98:99]\n"
    "v_pk_add_f32 v[230:231], v[144:145], v[146:147]\n"
    "v_pk_mul_f32 v[232:233], v[144:145], v[146:147]\n"
    "v_pk_fma_f32 v[148:149], v[148:149], s[70:71], v[100:101]\n"
    "v_pk_fma_f32 v[230:231], v[230:231], v[148:149], v[232:233]\n"
    "v_pk_mul_f32 v[232:233], v[232:233], v[148:149]\n"
    "v_pk_fma_f32 v[150:151], v[150:151], s[70:71], v[102:103]\n"
    "v_pk_fma_f32 v[230:231], v[230:231], v[150:151], v[232:233]\n"
    "v_pk_mul_f32 v[232:233], v[232:233], v[150:151]\n"
    "v_pk_fma_f32 v[152:153], v[152:153], s[70:71], v[104:105]\n"
    "v_pk_fma_f32 v[230:231], v[230:231], v[152:153], v[232:233]\n"
    "v_pk_mul_f32 v[232:233], v[232:233], v[152:153]\n"
    "v_pk_fma_f32 v[154:155], v[154:155], s[70:71], v[106:107]\n"
    "v_pk_fma_f32 v[230:231], v[230:231], v[154:155], v[232:233]\n"
    "v_pk_mul_f32 v[232:233], v[232:233], v[154:155]\n"
    "v_pk_fma_f32 v[156:157], v[156:157], s[70:71], v[108:109]\n"
    "v_pk_fma_f32 v[230:231], v[230:231], v[156:157], v[232:233]\n"
    "v_pk_mul_f32 v[232:233], v[232:233], v[156:157]\n"
    "v_pk_fma_f32 v[158:159], v[158:159], s[70:71], v[110:111]\n"
    "v_pk_fma_f32 v[230:231], v[230:231], v[158:159], v[232:233]\n"
    "v_pk_mul_f32 v[232:233], v[232:233], v[158:159]\n"
    "v_rcp_f32 v232, v232\n"
    "v_rcp_f32 v233, v233\n"
    "v_fma_f32 v230, v230, v232, v247\n"
    "v_fmac_f32 v230, v231, v233\n"
    "v_lshlrev_b32 v232, 2, v249\n"
    "v_lshrrev_b32 v233, 2, v249\n"
    "v_and_b32 v232, 56, v232\n"
    "v_and_b32 v233, 4, v233\n"
    "v_add3_u32 v232, v232, v233, s81\n"
    "ds_read_b32 v232, v232\n"
    "s_add_u32 s81, s81, 64\n"
    "s_waitcnt lgkmcnt(0)\n"
    "v_lshlrev_b32 v232, 1, v232\n"
    "buffer_load_ushort v232, v232, s[44:47], 0 offen\n"
    "s_waitcnt vmcnt(0)\n"
    "v_lshlrev_b32 v232, 16, v232\n"
    "v_mul_f32 v230, v230, v232\n"
    "v_cvt_pk_bf16_f32 v230, v230, v230\n"
    "ds_write_b16 v245, v230\n"
    "ds_read_b64 v[228:229], v246\n"
    "s_branch .Ldin_drain1\n"
    ".Ldin_exit:\n"

      : "+{s[0:1]}"(kp), "+{v0}"(tid)
      : "{s2}"(blockIdx.x)
      : "memory", "vcc", "scc", "v1", "v2", "v3", "v4", "v5", "v6", "v7", "v8", "v9", "v10", "v11", "v12", "v13", "v14", "v15", "v16", "v17", "v18", "v19", "v20", "v21", "v22", "v23", "v24", "v25", "v26", "v27", "v28", "v29", "v30", "v31", "v32", "v33", "v34", "v35", "v36", "v37", "v38", "v39", "v40", "v41", "v42", "v43", "v44", "v45", "v46", "v47", "v48", "v49", "v50", "v51", "v52", "v53", "v54", "v55", "v56", "v57", "v58", "v59", "v60", "v61", "v62", "v63", "v64", "v65", "v66", "v67", "v68", "v69", "v70", "v71", "v72", "v73", "v74", "v75", "v76", "v77", "v78", "v79", "v80", "v81", "v82", "v83", "v84", "v85", "v86", "v87", "v88", "v89", "v90", "v91", "v92", "v93", "v94", "v95", "v96", "v97", "v98", "v99", "v100", "v101", "v102", "v103", "v104", "v105", "v106", "v107", "v108", "v109", "v110", "v111", "v112", "v113", "v114", "v115", "v116", "v117", "v118", "v119", "v120", "v121", "v122", "v123", "v124", "v125", "v126", "v127", "v128", "v129", "v130", "v131", "v132", "v133", "v134", "v135", "v136", "v137", "v138", "v139", "v140", "v141", "v142", "v143", "v144", "v145", "v146", "v147", "v148", "v149", "v150", "v151", "v152", "v153", "v154", "v155", "v156", "v157", "v158", "v159", "v160", "v161", "v162", "v163", "v164", "v165", "v166", "v167", "v168", "v169", "v170", "v171", "v172", "v173", "v174", "v175", "v176", "v177", "v178", "v179", "v180", "v181", "v182", "v183", "v184", "v185", "v186", "v187", "v188", "v189", "v190", "v191", "v192", "v193", "v194", "v195", "v196", "v197", "v198", "v199", "v200", "v201", "v202", "v203", "v204", "v205", "v206", "v207", "v208", "v209", "v210", "v211", "v212", "v213", "v214", "v215", "v216", "v217", "v218", "v219", "v220", "v221", "v222", "v223", "v224", "v225", "v226", "v227", "v228", "v229", "v230", "v231", "v232", "v233", "v234", "v235", "v236", "v237", "v238", "v239", "v240", "v241", "v242", "v243", "v244", "v245", "v246", "v247", "v248", "v249", "v250", "v251", "v252", "v253", "v254", "v255", "s3", "s4", "s5", "s6", "s7", "s8", "s9", "s10", "s11", "s12", "s13", "s14", "s15", "s16", "s17", "s18", "s19", "s20", "s21", "s22", "s23", "s24", "s25", "s26", "s27", "s28", "s29", "s30", "s31", "s33", "s34", "s35", "s36", "s37", "s38", "s39", "s40", "s41", "s42", "s43", "s44", "s45", "s46", "s47", "s48", "s49", "s50", "s51", "s52", "s53", "s54", "s55", "s56", "s57", "s58", "s59", "s60", "s61", "s62", "s63", "s64", "s65", "s66", "s67", "s68", "s69", "s70", "s71", "s72", "s73", "s74", "s75", "s76", "s77", "s78", "s79", "s80", "s81", "s82", "s83", "s84", "s85", "s86", "s87", "s88", "s89", "s90", "s91", "s92", "s93", "s94", "s95", "s96");
  asm volatile("" ::"s"((uint32_t)(uintptr_t)smem));
}

__global__ __launch_bounds__(64) void din_fallback_kernel(
    const unsigned short* __restrict__ target, const unsigned short* __restrict__ hist,
    const unsigned short* __restrict__ mask, const unsigned short* __restrict__ w1,
    const unsigned short* __restrict__ b1, const unsigned short* __restrict__ w2,
    const unsigned short* __restrict__ b2, unsigned short* __restrict__ out, int B, int L) {
  __shared__ float s_w[64 * 64];
  __shared__ float s_x[64];
  const int b = blockIdx.x, h = threadIdx.x;
  float bias = bf2f(b1[h]);
  for (int d = 0; d < 64; ++d) {
    const float t = bf2f(target[(size_t)b * 64 + d]);
    const float wc = bf2f(w1[(128 + d) * 64 + h]);
    bias += t * (bf2f(w1[(64 + d) * 64 + h]) - wc);
    s_w[d * 64 + h] = bf2f(w1[d * 64 + h]) + wc + t * bf2f(w1[(192 + d) * 64 + h]);
  }
  const float w2h = bf2f(w2[h]), b2v = bf2f(b2[0]);
  float acc = 0.f;
  for (int l = 0; l < L; ++l) {
    const float m = bf2f(mask[(size_t)b * L + l]);
    if (m == 0.f) continue;
    const float x = bf2f(hist[((size_t)b * L + l) * 64 + h]);
    __syncthreads();
    s_x[h] = x;
    __syncthreads();
    float z = bias;
    for (int d = 0; d < 64; ++d) z += s_x[d] * s_w[d * 64 + h];
    float s = w2h / (1.0f + __expf(-z));
    for (int o = 32; o > 0; o >>= 1) s += __shfl_xor(s, o);
    acc += (s + b2v) * m * x;
  }
  const uint32_t u = __float_as_uint(acc);
  out[(size_t)b * 64 + h] = (unsigned short)((u + 0x7fffu + ((u >> 16) & 1u)) >> 16);
}

extern "C" void kernel_launch(void* const* d_in, const int* in_sizes, int n_in, void* d_out,
                              int out_size, void* d_ws, size_t ws_size, hipStream_t stream) {
  const unsigned short* target = (const unsigned short*)d_in[0];
  const unsigned short* hist = (const unsigned short*)d_in[1];
  const unsigned short* mask = (const unsigned short*)d_in[2];
  const unsigned short* w1 = (const unsigned short*)d_in[3];
  const unsigned short* b1 = (const unsigned short*)d_in[4];
  const unsigned short* w2 = (const unsigned short*)d_in[5];
  const unsigned short* b2 = (const unsigned short*)d_in[6];
  const int B = in_sizes[0] / 64;
  const int L = in_sizes[2] / B;
  if ((L & 1) || L > 256 || B >= (1 << 24) - 64) {
    din_fallback_kernel<<<B, 64, 0, stream>>>(target, hist, mask, w1, b1, w2, b2, (unsigned short*)d_out, B, L);
    return;
  }
  const uint32_t grid = (B + 63) / 64, fl = 0x20000u;
  const bool paced = grid >= 224;
  din_kernel<<<grid, 512, 0, stream>>>(hist, (uint32_t)B * L, fl, mask, (uint32_t)B * L * 2u, fl, target, (uint32_t)B * 128u, fl,
                                       (unsigned short*)d_out, (uint32_t)B * 128u, fl, w1, w2, b1, b2, (uint32_t)L, NO_BATCH, (uint32_t)B,
                                       grid * 8u, 2u * L, grid * 16u * L, paced ? 52u : 0u, paced ? 15u : 0u);
}
```

```cpp
#include <hip/hip_runtime.h>
#include <stdint.h>

constexpr int SMEM_B = 163712;

__device__ __forceinline__ float bf2f(unsigned short u) { return __uint_as_float(((uint32_t)u) << 16); }

__global__ __launch_bounds__(512, 2) __attribute__((aligned(4096))) void din_kernel(
    const unsigned short* hist, uint32_t rows, uint32_t f0, const unsigned short* mask, uint32_t mask_bytes, uint32_t f1,
    const unsigned short* target, uint32_t target_bytes, uint32_t f2, unsigned short* out, uint32_t out_bytes, uint32_t f3,
    const unsigned short* w1, const unsigned short* w2, const unsigned short* b1, const unsigned short* b2,
    uint32_t L, uint32_t first_row, uint32_t B, uint32_t round_rows, uint32_t row_bytes, uint32_t round_bytes, uint32_t period, uint32_t extra) {
  __shared__ __attribute__((aligned(128))) char smem[SMEM_B];
  unsigned long long kp = (unsigned long long)(uintptr_t)__builtin_amdgcn_kernarg_segment_ptr();
  uint32_t tid = threadIdx.x;
  asm volatile(
    "s_load_dwordx16 s[40:55], s[0:1], 0x0\n"
    "s_load_dwordx16 s[4:19], s[0:1], 0x40\n"
    "s_memrealtime s[76:77]\n"
    "s_memtime s[0:1]\n"
    "v_readfirstlane_b32 s3, v0\n"
    "v_and_b32 v251, 63, v0\n"
    "s_lshr_b32 s3, s3, 6\n"
    "s_lshl3_add_u32 s61, s2, s3\n"
    "s_getpc_b64 s[92:93]\n"
    "s_and_b32 s94, s2, 0xf8\n"
    "s_add_u32 s94, s94, s3\n"
    "s_min_u32 s94, s94, 199\n"
    "s_lshl_b32 s94, s94, 7\n"
    "v_mov_b32 v1, s94\n"
    "global_load_dword v1, v1, s[92:93] offset:1024\n"
    "s_cmp_lt_u32 s3, 4\n"
    "s_cbranch_scc0 .Ldin_young\n"
    "v_lshlrev_b32 v105, 3, v251\n"
    "v_mov_b32 v8, 0\n"
    "v_mov_b32 v9, 0\n"
    "v_mov_b32 v10, 0\n"
    "v_and_b32 v2, 48, v251\n"
    "v_and_b32 v3, 8, v251\n"
    "v_lshl_or_b32 v2, v3, 3, v2\n"
    "v_and_b32 v3, 4, v251\n"
    "v_lshl_or_b32 v2, v3, 1, v2\n"
    "v_lshlrev_b32 v249, 1, v251\n"
    "s_mul_i32 s68, s3, 11392\n"
    "v_lshlrev_b32_e64 v248, 4, v251\n"
    "s_add_u32 s68, s68, 72576\n"
    "v_and_b32 v24, 0x60, v249\n"
    "s_add_u32 s67, s68, 8192\n"
    "v_or_b32 v250, 0x8000, v24\n"
    "s_add_u32 s65, s68, 9728\n"
    "v_lshrrev_b32 v20, 4, v251\n"
    "s_xor_b32 s64, s67, s65\n"
    "v_and_b32 v244, 56, v251\n"
    "s_add_u32 s96, s68, 11264\n"
    "v_bitop3_b32 v21, v20, v251, 7 bitop3:0x78\n"
    "s_lshl_b32 s66, s3, 7\n"
    "v_lshlrev_b32 v21, 4, v21\n"
    "s_add_u32 s66, s66, 68512\n"
    "v_or_b32 v237, 0x400, v21\n"
    "s_lshr_b32 s95, s67, 2\n"
    "v_xor_b32 v239, 64, v21\n"
    "s_mov_b32 s38, 0xffff\n"
    "s_mov_b32 s57, 0x7fffffc0\n"
    "v_add_u32 v253, s67, v244\n"
    "v_lshlrev_b32 v105, 3, v251\n"
    "v_mov_b32 v201, v237\n"
    "v_mov_b32 v203, v239\n"
    "v_mov_b32 v205, v237\n"
    "v_mov_b32 v207, v239\n"
    "v_mov_b32 v209, v237\n"
    "v_mov_b32 v211, v239\n"
    "v_mov_b32 v213, v237\n"
    "v_mov_b32 v215, v239\n"
    "v_mov_b32 v220, s95\n"
    "v_mov_b32 v224, 0\n"
    "v_mov_b32 v225, 0\n"
    "v_mov_b32 v226, 0\n"
    "v_mov_b32 v227, 0\n"
    "v_bfe_u32 v21, v251, 1, 3\n"
    "v_xor_b32 v21, v20, v21\n"
    "v_and_b32 v22, 0xf0, v248\n"
    "s_mov_b32 s70, 0x47000000\n"
    "v_lshl_add_u32 v22, v22, 3, s68\n"
    "s_mov_b32 s71, s70\n"
    "v_lshl_add_u32 v234, v21, 4, v22\n"
    "s_add_u32 s63, s66, 16\n"
    "v_xor_b32 v235, 64, v234\n"
    "s_mov_b32 s81, s67\n"
    "v_and_b32 v20, 3, v251\n"
    "s_mov_b64 s[92:93], 0xffff\n"
    "v_lshlrev_b64 v[254:255], v248, s[92:93]\n"
    "s_add_u32 s82, s65, 256\n"
    "v_lshl_or_b32 v21, v20, 3, v24\n"
    "s_mul_i32 s62, s3, 272\n"
    "v_and_b32 v22, 8, v251\n"
    "v_mul_u32_u24 v23, 136, v22\n"
    "v_xor_b32 v21, v21, v23\n"
    "v_and_b32 v23, 4, v251\n"
    "v_lshl_or_b32 v21, v23, 5, v21\n"
    "v_add_u32 v240, s68, v21\n"
    "v_xor_b32 v241, 16, v240\n"
    "v_xor_b32 v242, 32, v240\n"
    "v_xor_b32 v243, 48, v240\n"
    "v_add_u32 v252, s62, v250\n"
    "v_and_b32 v21, 24, v249\n"
    "v_add_u32 v21, s96, v21\n"
    "v_lshl_add_u32 v246, v20, 5, v21\n"
    "v_and_b32 v21, 6, v251\n"
    "v_lshl_or_b32 v21, v21, 2, v24\n"
    "v_and_b32 v23, 2, v249\n"
    "v_lshrrev_b32 v22, 1, v22\n"
    "v_or3_b32 v21, v21, v22, v23\n"
    "v_add_u32 v245, s96, v21\n"
    "s_setprio 2\n"
    "s_waitcnt lgkmcnt(0)\n"
    "s_add_u32 s61, s61, s13\n"
    "s_mul_i32 s36, s61, s16\n"
    "s_add_u32 s94, s36, s16\n"
    "s_cmp_lt_u32 s61, s14\n"
    "s_cselect_b32 s46, s94, 0\n"
    "buffer_load_dwordx2 v[228:229], v105, s[44:47], s36 offen nt\n"
    "s_load_dword s10, s[10:11], 0x0\n"
    "global_load_short_d16_hi v8, v249, s[6:7]\n"
    "global_load_short_d16_hi v9, v249, s[8:9]\n"
    "s_setprio 0\n"
    "s_branch .Ldin_old2\n"
    ".Ldin_young:\n"
    "s_setprio 1\n"
    "v_and_b32 v16, 7, v251\n"
    "s_lshl_b32 s94, s3, 11\n"
    "v_lshlrev_b32 v2, 4, v16\n"
    "v_and_b32 v3, 56, v251\n"
    "v_lshl_or_b32 v3, v3, 5, v2\n"
    "v_add_u32 v6, s94, v3\n"
    "v_add_u32 v3, 0x2000, v6\n"
    "v_add_u32 v4, 0xffffe000, v6\n"
    "v_add_u32 v5, 0x4000, v6\n"
    "v_and_b32 v17, 8, v251\n"
    "s_and_b32 s88, s3, 1\n"
    "v_and_b32 v18, 32, v251\n"
    "s_and_b32 s89, s3, 2\n"
    "v_lshl_or_b32 v18, v16, 2, v18\n"
    "s_lshl_b32 s84, s88, 9\n"
    "v_lshl_add_u32 v18, v18, 3, s84\n"
    "s_lshl_b32 s84, s89, 10\n"
    "v_and_b32 v19, 16, v251\n"
    "s_lshl_b32 s85, s89, 9\n"
    "v_lshl_or_b32 v19, v19, 6, v17\n"
    "s_add_u32 s85, s85, 0x4000\n"
    "v_add3_u32 v12, v18, v19, s84\n"
    "s_lshl2_add_u32 s86, s89, s88\n"
    "v_add_u32 v13, 16, v12\n"
    "s_lshl_b32 s86, s86, 4\n"
    "v_and_b32 v19, 24, v251\n"
    "s_add_u32 s87, s94, 0x4000\n"
    "v_lshrrev_b32 v19, 1, v19\n"
    "v_add3_u32 v14, v18, v19, s85\n"
    "v_add_u32 v15, 16, v14\n"
    "v_bfrev_b32 v7, 1\n"
    "v_lshlrev_b32 v26, 4, v251\n"
    "v_and_b32 v26, 0x1f0, v26\n"
    "v_lshrrev_b32 v27, 5, v251\n"
    "v_lshlrev_b32 v249, 1, v251\n"
    "s_mul_i32 s68, s3, 11392\n"
    "v_lshlrev_b32 v248, 4, v251\n"
    "s_add_u32 s68, s68, 72576\n"
    "v_and_b32 v24, 0x60, v249\n"
    "s_add_u32 s67, s68, 8192\n"
    "v_or_b32 v250, 0x8000, v24\n"
    "s_add_u32 s65, s68, 9728\n"
    "v_lshrrev_b32 v20, 4, v251\n"
    "s_xor_b32 s64, s67, s65\n"
    "v_and_b32 v244, 56, v251\n"
    "s_add_u32 s96, s68, 11264\n"
    "v_bitop3_b32 v21, v20, v251, 7 bitop3:0x78\n"
    "s_lshl_b32 s66, s3, 7\n"
    "v_lshlrev_b32 v21, 4, v21\n"
    "s_add_u32 s66, s66, 68512\n"
    "v_or_b32 v237, 0x400, v21\n"
    "s_lshr_b32 s95, s67, 2\n"
    "v_xor_b32 v239, 64, v21\n"
    "s_mov_b32 s38, 0xffff\n"
    "s_mov_b32 s57, 0x7fffffc0\n"
    "v_add_u32 v253, s67, v244\n"
    "v_lshlrev_b32 v105, 3, v251\n"
    "v_mov_b32 v201, v237\n"
    "v_mov_b32 v203, v239\n"
    "v_mov_b32 v205, v237\n"
    "v_mov_b32 v207, v239\n"
    "v_mov_b32 v209, v237\n"
    "v_mov_b32 v211, v239\n"
    "v_mov_b32 v213, v237\n"
    "v_mov_b32 v215, v239\n"
    "v_mov_b32 v220, s95\n"
    "v_mov_b32 v224, 0\n"
    "v_mov_b32 v225, 0\n"
    "v_mov_b32 v226, 0\n"
    "s_waitcnt lgkmcnt(0)\n"
    "global_load_dwordx4 v[8:11], v2, s[6:7]\n"
    "global_load_dwordx4 v[128:131], v3, s[4:5] offset:0\n"
    "global_load_dwordx4 v[132:135], v3, s[4:5] offset:128\n"
    "v_mov_b32 v227, 0\n"
    "v_bfe_u32 v21, v251, 1, 3\n"
    "v_xor_b32 v21, v20, v21\n"
    "v_and_b32 v22, 0xf0, v248\n"
    "s_mov_b32 s70, 0x47000000\n"
    "v_lshl_add_u32 v22, v22, 3, s68\n"
    "s_mov_b32 s71, s70\n"
    "v_lshl_add_u32 v234, v21, 4, v22\n"
    "s_add_u32 s63, s66, 16\n"
    "v_xor_b32 v235, 64, v234\n"
    "s_mov_b32 s81, s67\n"
    "v_and_b32_e64 v20, 3, v251\n"
    "global_load_dwordx4 v[136:139], v4, s[4:5] offset:0\n"
    "s_mov_b64 s[92:93], 0xffff\n"
    "v_lshlrev_b64 v[254:255], v248, s[92:93]\n"
    "s_add_u32 s82, s65, 256\n"
    "v_lshl_or_b32 v21, v20, 3, v24\n"
    "s_mul_i32 s62, s3, 272\n"
    "v_and_b32 v22, 8, v251\n"
    "v_mul_u32_u24 v23, 136, v22\n"
    "v_xor_b32 v21, v21, v23\n"
    "v_and_b32 v23, 4, v251\n"
    "v_lshl_or_b32 v21, v23, 5, v21\n"
    "v_add_u32 v240, s68, v21\n"
    "v_xor_b32 v241, 16, v240\n"
    "global_load_dwordx4 v[140:143], v4, s[4:5] offset:128\n"
    "v_xor_b32 v242, 32, v240\n"
    "v_xor_b32 v243, 48, v240\n"
    "v_add_u32 v252, s62, v250\n"
    "v_and_b32 v21, 24, v249\n"
    "v_add_u32 v21, s96, v21\n"
    "v_lshl_add_u32 v246, v20, 5, v21\n"
    "v_and_b32 v21, 6, v251\n"
    "v_lshl_or_b32 v21, v21, 2, v24\n"
    "v_and_b32 v23, 2, v249\n"
    "v_lshrrev_b32 v22, 1, v22\n"
    "v_or3_b32 v21, v21, v22, v23\n"
    "v_add_u32 v245, s96, v21\n"
    "global_load_dwordx4 v[144:147], v5, s[4:5] offset:0\n"
    "s_add_u32 s61, s61, s13\n"
    "s_add_u32 s39, s61, s3\n"
    "s_sub_u32 s39, s39, 8\n"
    "v_cmp_gt_u32 vcc, s16, v26\n"
    "v_mad_u32_u24 v27, v27, s16, v26\n"
    "s_mul_i32 s39, s39, s16\n"
    "s_lshl1_add_u32 s39, s17, s39\n"
    "v_cndmask_b32 v27, v7, v27, vcc\n"
    "s_add_u32 s40, s40, 0xfffffc00\n"
    "s_addc_u32 s41, s41, -1\n"
    "s_mul_i32 s94, s61, s12\n"
    "s_bitset1_b32 s41, 23\n"
    "global_load_dwordx4 v[148:151], v5, s[4:5] offset:128\n"
    "v_lshl_add_u32 v216, v251, 2, s94\n"
    "s_mov_b32 s56, s12\n"
    "v_add_u32 v217, 1, v216\n"
    "v_add_u32 v218, 2, v216\n"
    "v_add_u32 v219, 3, v216\n"
    "s_mov_b64 s[72:73], s[18:19]\n"
    "s_cmp_lg_u32 s72, 0\n"
    "s_cselect_b32 s94, 6, 0\n"
    "s_cselect_b32 s95, 6, 0\n"
    "s_add_u32 s74, s72, s94\n"
    "s_sub_u32 s75, s72, s95\n"
    "v_mov_b32 v20, s57\n"
    "s_mov_b64 exec, 0xffffff\n"
    "v_mov_b32 v20, -1\n"
    "s_mov_b64 exec, 0xff\n"
    "v_mov_b32 v20, 0\n"
    "s_mov_b32 exec_lo, -1\n"
    "v_lshl_add_u32 v21, v249, 1, s66\n"
    "v_add_u32 v21, -32, v21\n"
    "ds_write_b32 v21, v20\n"
    "s_mov_b32 exec_hi, -1\n"
    "global_load_dwordx4 v[152:155], v6, s[4:5] offset:0\n"
    "s_mov_b32 s37, 0xffff0000\n"
    "v_and_b32 v19, 15, v251\n"
    "v_mul_u32_u24 v19, 272, v19\n"
    "v_add_u32 v97, v19, v250\n"
    "v_add_u32 v98, s87, v248\n"
    "v_add_u32 v99, s86, v250\n"
    "v_add_u32 v100, s86, v97\n"
    "s_mov_b32 s84, 0xbfb8aa3b\n"
    "global_load_dwordx4 v[156:159], v6, s[4:5] offset:128\n"
    "s_branch .Ldin_y2\n"
    ".Ldin_old2:\n"
    "s_add_u32 s40, s40, 0xfffffc00\n"
    "s_addc_u32 s41, s41, -1\n"
    "s_mul_i32 s94, s61, s12\n"
    "s_bitset1_b32 s41, 23\n"
    "v_lshl_add_u32 v216, v251, 2, s94\n"
    "s_mov_b32 s56, s12\n"
    "v_add_u32 v217, 1, v216\n"
    "v_add_u32 v218, 2, v216\n"
    "v_add_u32 v219, 3, v216\n"
    "s_mov_b64 s[72:73], s[18:19]\n"
    "s_cmp_lg_u32 s72, 0\n"
    "s_cselect_b32 s94, 6, 0\n"
    "s_cselect_b32 s95, 6, 0\n"
    "s_add_u32 s74, s72, s94\n"
    "s_sub_u32 s75, s72, s95\n"
    "v_mov_b32 v20, s57\n"
    "s_mov_b64 exec, 0xffffff\n"
    "v_mov_b32 v20, -1\n"
    "s_mov_b64 exec, 0xff\n"
    "v_mov_b32 v20, 0\n"
    "s_mov_b32 exec_lo, -1\n"
    "v_lshl_add_u32 v21, v249, 1, s66\n"
    "v_add_u32 v21, -32, v21\n"
    "ds_write_b32 v21, v20\n"
    "s_mov_b32 exec_hi, -1\n"
    "v_and_b32 v4, 2, v251\n"
    "v_lshl_add_u32 v4, v4, 6, v250\n"
    "v_and_b32 v5, 16, v248\n"
    "v_and_b32 v6, 12, v251\n"
    "v_or3_b32 v104, v4, v5, v6\n"
    "v_mul_u32_u24 v4, 34, v244\n"
    "v_and_b32 v5, 3, v251\n"
    "v_lshl_add_u32 v4, v5, 5, v4\n"
    "v_and_b32 v5, 4, v251\n"
    "v_lshl_add_u32 v4, v5, 2, v4\n"
    "s_lshl_b32 s86, s62, 3\n"
    "v_add_u32 v18, s86, v4\n"
    "s_mul_i32 s84, s3, s15\n"
    "s_lshl_b32 s39, s61, 7\n"
    "s_sub_u32 s84, s84, s3\n"
    "s_add_u32 s84, s84, s61\n"
    "s_lshl_b32 s84, s84, 7\n"
    "s_lshl_b32 s85, s15, 9\n"
    "s_add_u32 s85, s85, s84\n"
    "s_lshl_b32 s94, s61, 6\n"
    "s_add_u32 s94, s3, s94\n"
    "s_cmp_lt_u32 s61, s14\n"
    "s_cselect_b32 s58, s94, s57\n"
    "s_add_u32 s95, s61, s15\n"
    "s_add_u32 s36, s36, s17\n"
    "s_lshl_b32 s94, s95, 6\n"
    "s_add_u32 s94, s3, s94\n"
    "s_add_u32 s96, s36, s16\n"
    "s_add_u32 s94, s94, 8\n"
    "s_cmp_lt_u32 s95, s14\n"
    "s_cselect_b32 s46, s96, 0\n"
    "s_cselect_b32 s59, s94, s57\n"
    "s_setprio 2\n"
    "buffer_load_dwordx2 v[184:185], v2, s[48:51], s39 offen nt\n"
    "buffer_load_dwordx4 v[128:131], v248, s[48:51], s84 offen nt\n"
    "buffer_load_dwordx4 v[132:135], v248, s[48:51], s85 offen nt\n"
    "buffer_load_short_d16_hi v224, v249, s[44:47], s36 offen offset:0 nt\n"
    "buffer_load_short_d16_hi v225, v249, s[44:47], s36 offen offset:128 nt\n"
    "buffer_load_short_d16_hi v226, v249, s[44:47], s36 offen offset:256 nt\n"
    "buffer_load_short_d16_hi v227, v249, s[44:47], s36 offen offset:384 nt\n"
    "s_mul_i32 s46, s14, s16\n"
    "s_nop 0\n"
    "v_mul_u32_u24 v32, 0x9e3779, v251\n"
    "s_mov_b32 s90, 0x807f807f\n"
    "v_mov_b32 v47, 0x3f003f00\n"
    "v_add_u32 v32, 0x7f4a7c15, v32\n"
    "v_mul_lo_u32 v33, v32, v32\n"
    "v_mul_lo_u32 v34, v33, v32\n"
    "v_mul_lo_u32 v35, v34, v32\n"
    "v_mul_lo_u32 v36, v35, v32\n"
    "v_mul_lo_u32 v37, v36, v32\n"
    "v_mul_lo_u32 v38, v37, v32\n"
    "v_mul_lo_u32 v39, v38, v32\n"
    "v_and_or_b32 v32, v32, s90, v47\n"
    "v_and_or_b32 v33, v33, s90, v47\n"
    "v_and_or_b32 v34, v34, s90, v47\n"
    "v_and_or_b32 v35, v35, s90, v47\n"
    "v_and_or_b32 v36, v36, s90, v47\n"
    "v_and_or_b32 v37, v37, s90, v47\n"
    "v_and_or_b32 v38, v38, s90, v47\n"
    "v_and_or_b32 v39, v39, s90, v47\n"
    "s_nop 1\n"
    "s_nop 0\n"
    "v_mfma_f32_32x32x16_bf16 v[64:79], v[36:39], v[32:35], 0\n"
    "v_mfma_f32_32x32x16_bf16 v[48:63], v[32:35], v[36:39], 0\n"
    "v_mfma_f32_32x32x16_bf16 v[64:79], v[36:39], v[32:35], 0\n"
    "v_mfma_f32_32x32x16_bf16 v[48:63], v[32:35], v[36:39], 0\n"
    "v_mfma_f32_32x32x16_bf16 v[64:79], v[36:39], v[32:35], 0\n"
    "v_mfma_f32_32x32x16_bf16 v[48:63], v[32:35], v[36:39], 0\n"
    "v_mfma_f32_32x32x16_bf16 v[64:79], v[36:39], v[32:35], 0\n"
    "v_mfma_f32_32x32x16_bf16 v[48:63], v[32:35], v[36:39], 0\n"
    "v_mfma_f32_32x32x16_bf16 v[64:79], v[36:39], v[32:35], 0\n"
    "v_mfma_f32_32x32x16_bf16 v[48:63], v[32:35], v[36:39], 0\n"
    "s_mov_b64 exec, 0\n"
    "s_bitcmp1_b32 s3, 1\n"
    "s_cbranch_scc1 .Ldin_t0_hi\n"
    "s_bitcmp1_b32 s3, 0\n"
    "s_cbranch_scc0 .Ldin_stub_0\n"
    "s_branch .Ldin_stub_1\n"
    ".Ldin_t0_hi:\n"
    "s_bitcmp1_b32 s3, 0\n"
    "s_cbranch_scc0 .Ldin_stub_2\n"
    "s_branch .Ldin_stub_3\n"
    ".Ldin_t0_ret:\n"
    "s_mov_b64 exec, -1\n"
    "s_mov_b64 exec, 0\n"
    "s_bitcmp1_b32 s3, 1\n"
    "s_cbranch_scc1 .Ldin_t3_hi\n"
    "s_bitcmp1_b32 s3, 0\n"
    "s_cbranch_scc0 .Ldin_stub_12\n"
    "s_branch .Ldin_stub_13\n"
    ".Ldin_t3_hi:\n"
    "s_bitcmp1_b32 s3, 0\n"
    "s_cbranch_scc0 .Ldin_stub_14\n"
    "s_branch .Ldin_stub_15\n"
    ".Ldin_t3_ret:\n"
    "s_mov_b64 exec, -1\n"
    "s_setprio 2\n"
    "s_waitcnt vmcnt(9)\n"
    "v_cmp_ne_u16_e64 s[84:85], 0, v228\n"
    "v_cmp_lt_u32_e64 s[86:87], s38, v228\n"
    "v_cmp_ne_u16_e64 s[88:89], 0, v229\n"
    "v_cmp_lt_u32_e64 s[90:91], s38, v229\n"
    "v_mbcnt_lo_u32_b32 v220, s84, v220\n"
    "s_bcnt1_i32_b64 s92, s[84:85]\n"
    "v_mbcnt_hi_u32_b32 v220, s85, v220\n"
    "s_lshl2_add_u32 s92, s92, s67\n"
    "v_mbcnt_lo_u32_b32 v220, s86, v220\n"
    "s_bcnt1_i32_b64 s93, s[86:87]\n"
    "v_mbcnt_hi_u32_b32 v220, s87, v220\n"
    "s_lshl2_add_u32 s93, s93, s92\n"
    "v_mbcnt_lo_u32_b32 v220, s88, v220\n"
    "s_bcnt1_i32_b64 s94, s[88:89]\n"
    "v_mbcnt_hi_u32_b32 v220, s89, v220\n"
    "s_lshl2_add_u32 s94, s94, s93\n"
    "v_mbcnt_lo_u32_b32 v220, s90, v220\n"
    "s_bcnt1_i32_b64 s95, s[90:91]\n"
    "v_mbcnt_hi_u32_b32 v220, s91, v220\n"
    "s_lshl2_add_u32 s95, s95, s94\n"
    "v_mov_b32_e64 v21, -1\n"
    "v_lshl_add_u32 v20, v249, 1, s95\n"
    "v_addc_co_u32_e64 v221, vcc, v220, 0, s[84:85]\n"
    "v_addc_co_u32_e64 v222, vcc, v221, 0, s[86:87]\n"
    "v_addc_co_u32_e64 v223, vcc, v222, 0, s[88:89]\n"
    "ds_write2_b32 v20, v21, v21 offset1:64\n"
    "v_lshlrev_b32 v220, 2, v220\n"
    "v_lshlrev_b32 v221, 2, v221\n"
    "v_lshlrev_b32 v222, 2, v222\n"
    "v_lshlrev_b32_e64 v223, 2, v223\n"
    "s_mov_b64 exec, s[84:85]\n"
    "ds_write_b32 v220, v216\n"
    "s_mov_b64 exec, s[86:87]\n"
    "ds_write_b32 v221, v217\n"
    "s_mov_b64 exec, s[88:89]\n"
    "ds_write_b32 v222, v218\n"
    "s_mov_b64 exec, s[90:91]\n"
    "ds_write_b32 v223, v219\n"
    "s_mov_b64 exec, -1\n"
    "ds_read_b32 v200, v253 offset:0\n"
    "ds_read_b32 v202, v253 offset:4\n"
    "ds_read_b32 v204, v253 offset:64\n"
    "ds_read_b32 v206, v253 offset:68\n"
    "ds_read_b32 v208, v253 offset:128\n"
    "ds_read_b32 v210, v253 offset:132\n"
    "ds_read_b32 v212, v253 offset:192\n"
    "ds_read_b32 v214, v253 offset:196\n"
    "s_add_u32 m0, s68, 0\n"
    "s_waitcnt lgkmcnt(6)\n"
    "buffer_load_dwordx4 v[200:201], s[40:43], 0 idxen offen offset:0 nt lds\n"
    "buffer_load_dwordx4 v[202:203], s[40:43], 0 idxen offen offset:1024 nt lds\n"
    "s_add_u32 s95, s95, 60\n"
    "s_add_u32 s94, s67, 384\n"
    "s_andn2_b32 s95, s95, 63\n"
    "s_max_u32 s80, s95, s94\n"
    "s_add_u32 m0, s68, 2048\n"
    "s_waitcnt lgkmcnt(0)\n"
    "buffer_load_dwordx4 v[204:205], s[40:43], 0 idxen offen offset:0 nt lds\n"
    "s_lshl_b32 s10, s10, 16\n"
    "v_writelane_b32 v10, s10, 0\n"
    "s_waitcnt vmcnt(10)\n"
    "v_and_b32 v11, 0x7fffffff, v8\n"
    "v_mfma_f32_32x32x16_bf16 v[64:79], v[36:39], v[32:35], 0\n"
    "v_and_b32 v12, 0x7fffffff, v10\n"
    "v_min_f32 v14, 0, v8\n"
    "v_add_u32 v12, 0xf6000000, v12\n"
    "v_add_f32 v14, v14, v10\n"
    "v_mfma_f32_32x32x16_bf16 v[48:63], v[32:35], v[36:39], 0\n"
    "v_max_i32 v12, v11, v12\n"
    "v_rcp_f32 v11, v11\n"
    "v_add_f32_dpp v14, v14, v14 row_ror:8 row_mask:0xf bank_mask:0xf\n"
    "v_max_u32_dpp v12, v12, v12 row_ror:8 row_mask:0xf bank_mask:0xf\n"
    "s_mov_b32 s84, 0xbfb8aa3b\n"
    "v_mfma_f32_32x32x16_bf16 v[64:79], v[36:39], v[32:35], 0\n"
    "v_add_f32_dpp v14, v14, v14 row_ror:4 row_mask:0xf bank_mask:0xf\n"
    "v_max_u32_dpp v12, v12, v12 row_ror:4 row_mask:0xf bank_mask:0xf\n"
    "v_bfrev_b32 v13, 1\n"
    "v_add_f32_dpp v14, v14, v14 row_ror:2 row_mask:0xf bank_mask:0xf\n"
    "v_mfma_f32_32x32x16_bf16 v[48:63], v[32:35], v[36:39], 0\n"
    "v_max_u32_dpp v12, v12, v12 row_ror:2 row_mask:0xf bank_mask:0xf\n"
    "v_bitop3_b32 v8, v8, s84, v13 bitop3:0x6c\n"
    "v_add_f32_dpp v14, v14, v14 row_ror:1 row_mask:0xf bank_mask:0xf\n"
    "v_max_u32_dpp v12, v12, v12 row_ror:1 row_mask:0xf bank_mask:0xf\n"
    "s_nop 0\n"
    "v_mfma_f32_32x32x16_bf16 v[64:79], v[36:39], v[32:35], 0\n"
    "v_readlane_b32 s84, v12, 0\n"
    "v_readlane_b32 s85, v12, 16\n"
    "v_readlane_b32 s86, v12, 32\n"
    "buffer_load_dwordx4 v[206:207], s[40:43], 0 idxen offen offset:1024 nt lds\n"
    "v_readlane_b32 s87, v12, 48\n"
    "v_mfma_f32_32x32x16_bf16 v[48:63], v[32:35], v[36:39], 0\n"
    "s_max_u32 s84, s84, s85\n"
    "s_max_u32 s86, s86, s87\n"
    "s_max_u32 s84, s84, s86\n"
    "s_lshr_b32 s84, s84, 23\n"
    "s_max_u32 s84, s84, 15\n"
    "v_mfma_f32_32x32x16_bf16 v[64:79], v[36:39], v[32:35], 0\n"
    "s_min_u32 s84, s84, 253\n"
    "s_lshl_b32 s84, s84, 23\n"
    "s_add_u32 s85, s84, 0xf9000000\n"
    "s_sub_u32 s86, 0x86000000, s84\n"
    "v_mfma_f32_32x32x16_bf16 v[48:63], v[32:35], v[36:39], 0\n"
    "v_mul_f32 v11, s85, v11\n"
    "v_min_f32 v11, 0x46800000, v11\n"
    "v_log_f32 v13, v11\n"
    "v_mul_f32 v14, s86, v14\n"
    "v_mov_b32 v15, s85\n"
    "v_mfma_f32_32x32x16_bf16 v[64:79], v[36:39], v[32:35], 0\n"
    "v_add_f32 v13, 0xc1700000, v13\n"
    "v_fmac_f32 v13, v9, v8\n"
    "ds_write_b32 v104, v13 offset:35200\n"
    "ds_write_b32 v104, v11 offset:34816\n"
    "v_mfma_f32_32x32x16_bf16 v[48:63], v[32:35], v[36:39], 0\n"
    "v_mov_b32_e64 v247, v14\n"
    "s_mov_b32 s69, s85\n"
    "ds_write_b64 v250, v[14:15] offset:35072\n"
    "s_mov_b32 s94, 0x10001\n"
    "v_mfma_f32_32x32x16_bf16 v[64:79], v[36:39], v[32:35], 0\n"
    "s_mov_b32 s95, 0xc080c080\n"
    "v_pk_min_u16 v22, v228, s94\n"
    "v_pk_min_u16 v23, v229, s94\n"
    "v_pk_mad_u16 v228, v22, s95, v228\n"
    "v_pk_mad_u16 v229, v23, s95, v229\n"
    "v_mfma_f32_32x32x16_bf16 v[48:63], v[32:35], v[36:39], 0\n"
    "s_sub_u32 s87, s80, s81\n"
    "v_or_b32 v228, v228, v229\n"
    "s_lshr_b32 s87, s87, 6\n"
    "s_lshl2_add_u32 s86, s3, 68224\n"
    "v_mfma_f32_32x32x16_bf16 v[64:79], v[36:39], v[32:35], 0\n"
    "s_waitcnt vmcnt(8)\n"
    "ds_write_b128 v18, v[128:131] offset:32768\n"
    "ds_write_b128 v18, v[132:135] offset:41472\n"
    "v_and_b32 v184, v254, v184\n"
    "v_and_b32 v185, v255, v185\n"
    "v_mfma_f32_32x32x16_bf16 v[48:63], v[32:35], v[36:39], 0\n"
    "v_mov_b32 v106, s86\n"
    "v_mov_b32 v108, s66\n"
    "v_mov_b32 v109, s58\n"
    "v_mov_b32_e64 v110, s59\n"
    "v_mfma_f32_32x32x16_bf16 v[64:79], v[36:39], v[32:35], 0\n"
    "s_waitcnt vmcnt(4)\n"
    "v_cmp_neq_f32_e64 s[20:21], 0, v224\n"
    "v_cmp_neq_f32_e64 s[22:23], 0, v225\n"
    "v_cmp_neq_f32_e64 s[24:25], 0, v226\n"
    "v_cmp_neq_f32_e64 s[26:27], 0, v227\n"
    "v_mfma_f32_32x32x16_bf16 v[48:63], v[32:35], v[36:39], 0\n"
    "v_fma_f32 v224, v224, v224, -v224\n"
    "v_fma_f32 v225, v225, v225, -v225\n"
    "v_fma_f32 v226, v226, v226, -v226\n"
    "v_fma_f32 v227, v227, v227, -v227\n"
    "v_mfma_f32_32x32x16_bf16 v[64:79], v[36:39], v[32:35], 0\n"
    "s_bcnt1_i32_b64 s88, s[20:21]\n"
    "s_bcnt1_i32_b64 s89, s[22:23]\n"
    "s_bcnt1_i32_b64 s90, s[24:25]\n"
    "s_bcnt1_i32_b64 s91, s[26:27]\n"
    "v_or3_b32 v228, v228, v224, v225\n"
    "v_mfma_f32_32x32x16_bf16 v[48:63], v[32:35], v[36:39], 0\n"
    "s_add_u32 s88, s88, s89\n"
    "v_or3_b32 v228, v228, v226, v227\n"
    "s_add_u32 s90, s90, s91\n"
    "v_cmp_ne_u32_e64 s[84:85], 0, v228\n"
    "v_mfma_f32_32x32x16_bf16 v[64:79], v[36:39], v[32:35], 0\n"
    "s_add_u32 s88, s88, s90\n"
    "s_pack_ll_b32_b16 s88, s87, s88\n"
    "s_mov_b64 exec, 1\n"
    "v_mov_b32 v107, s88\n"
    "ds_write2_b32 v108, v109, v110 offset1:2\n"
    "s_cmp_lg_u64 s[84:85], 0\n"
    "ds_write_b32 v106, v107\n"
    "s_cselect_b32 s91, 2.0, 0\n"
    "s_mov_b64 exec, -1\n"
    "s_or_b32 s58, s58, s91\n"
    "v_mfma_f32_32x32x16_bf16 v[48:63], v[32:35], v[36:39], 0\n"
    "s_or_b32 s59, s59, s91\n"
    "s_nop 11\n"
    "s_mov_b64 vcc, 0\n"
    "s_waitcnt lgkmcnt(0)\n"
    "s_barrier\n"
    "s_setprio 0\n"
    ".Ldin_entry:\n"
    "ds_read_b128 v[32:35], v248 offset:16384\n"
    "ds_read_b128 v[64:67], v248 offset:0\n"
    "ds_read_b128 v[68:71], v248 offset:1024\n"
    "ds_read_b128 v[36:39], v248 offset:17408\n"
    "ds_read_b128 v[72:75], v248 offset:2048\n"
    "ds_read_b128 v[76:79], v248 offset:3072\n"
    "ds_read_b128 v[40:43], v248 offset:18432\n"
    "ds_read_b128 v[80:83], v248 offset:4096\n"
    "ds_read_b128 v[84:87], v248 offset:5120\n"
    "ds_read_b128 v[44:47], v248 offset:19456\n"
    "ds_read_b128 v[88:91], v248 offset:6144\n"
    "ds_read_b128 v[92:95], v248 offset:7168\n"
    "s_add_u32 s67, s67, 320\n"
    "s_waitcnt lgkmcnt(9)\n"
    "ds_read_b128 v[48:51], v248 offset:20480\n"
    "ds_read_b128 v[192:195], v248 offset:8192\n"
    "ds_read_b128 v[196:199], v248 offset:9216\n"
    "v_mfma_f32_4x4x4_16b_bf16 v[144:147], v[184:185], v[32:33], v[64:67] cbsz:2 abid:0\n"
    "s_nop 0\n"
    "v_mfma_f32_4x4x4_16b_bf16 v[148:151], v[184:185], v[34:35], v[68:71] cbsz:2 abid:1\n"
    "s_add_u32 m0, s68, 4096\n"
    "s_cbranch_vccnz .Ldin_e1\n"
    "buffer_load_dwordx4 v[208:209], s[40:43], 0 idxen offen offset:0 nt lds\n"
    "buffer_load_dwordx4 v[210:211], s[40:43], 0 idxen offen offset:1024 nt lds\n"
    ".Ldin_e1:\n"
    "s_waitcnt lgkmcnt(9)\n"
    "ds_read_b128 v[52:55], v248 offset:21504\n"
    "ds_read_b128 v[200:203], v248 offset:10240\n"
    "ds_read_b128 v[204:207], v248 offset:11264\n"
    "v_mfma_f32_4x4x4_16b_bf16 v[152:155], v[184:185], v[36:37], v[72:75] cbsz:2 abid:2\n"
    "s_nop 1\n"
    "v_cvt_pk_bf16_f32 v0, v144, v145\n"
    "v_cvt_pk_bf16_f32 v1, v146, v147\n"
    "v_mfma_f32_4x4x4_16b_bf16 v[156:159], v[184:185], v[38:39], v[76:79] cbsz:2 abid:3\n"
    "s_add_u32 m0, s68, 6144\n"
    "s_cbranch_vccnz .Ldin_e2\n"
    "buffer_load_dwordx4 v[212:213], s[40:43], 0 idxen offen offset:0 nt lds\n"
    "buffer_load_dwordx4 v[214:215], s[40:43], 0 idxen offen offset:1024 nt lds\n"
    ".Ldin_e2:\n"
    "s_waitcnt lgkmcnt(9)\n"
    "s_nop 0\n"
    "ds_read_b128 v[56:59], v248 offset:22528\n"
    "ds_read_b128 v[208:211], v248 offset:12288\n"
    "ds_read_b128 v[212:215], v248 offset:13312\n"
    "v_cvt_pk_bf16_f32 v2, v148, v149\n"
    "v_cvt_pk_bf16_f32 v3, v150, v151\n"
    "v_mfma_f32_4x4x4_16b_bf16 v[144:147], v[184:185], v[40:41], v[80:83] cbsz:2 abid:0\n"
    "v_cvt_pk_bf16_f32 v4, v152, v153\n"
    "v_cvt_pk_bf16_f32 v5, v154, v155\n"
    "v_mfma_f32_4x4x4_16b_bf16 v[148:151], v[184:185], v[42:43], v[84:87] cbsz:2 abid:1\n"
    "s_cbranch_vccnz .Ldin_e3\n"
    "s_barrier\n"
    ".Ldin_e3:\n"
    "s_waitcnt lgkmcnt(9)\n"
    "s_nop 0\n"
    "ds_read_b128 v[60:63], v248 offset:23552\n"
    "ds_read_b128 v[216:219], v248 offset:14336\n"
    "ds_read_b128 v[220:223], v248 offset:15360\n"
    "v_cvt_pk_bf16_f32 v6, v156, v157\n"
    "v_cvt_pk_bf16_f32 v7, v158, v159\n"
    "v_mfma_f32_4x4x4_16b_bf16 v[152:155], v[184:185], v[44:45], v[88:91] cbsz:2 abid:2\n"
    "v_cvt_pk_bf16_f32 v8, v144, v145\n"
    "v_cvt_pk_bf16_f32 v9, v146, v147\n"
    "v_mfma_f32_4x4x4_16b_bf16 v[156:159], v[184:185], v[46:47], v[92:95] cbsz:2 abid:3\n"
    "s_waitcnt lgkmcnt(9)\n"
    "v_cvt_pk_bf16_f32 v10, v148, v149\n"
    "v_cvt_pk_bf16_f32 v11, v150, v151\n"
    "v_mfma_f32_4x4x4_16b_bf16 v[144:147], v[184:185], v[48:49], v[192:195] cbsz:2 abid:0\n"
    "v_cvt_pk_bf16_f32 v12, v152, v153\n"
    "v_cvt_pk_bf16_f32 v13, v154, v155\n"
    "v_mfma_f32_4x4x4_16b_bf16 v[148:151], v[184:185], v[50:51], v[196:199] cbsz:2 abid:1\n"
    "s_waitcnt lgkmcnt(6)\n"
    "v_cvt_pk_bf16_f32 v14, v156, v157\n"
    "v_cvt_pk_bf16_f32 v15, v158, v159\n"
    "v_mfma_f32_4x4x4_16b_bf16 v[152:155], v[184:185], v[52:53], v[200:203] cbsz:2 abid:2\n"
    "v_cvt_pk_bf16_f32 v16, v144, v145\n"
    "v_cvt_pk_bf16_f32 v17, v146, v147\n"
    "v_mfma_f32_4x4x4_16b_bf16 v[156:159], v[184:185], v[54:55], v[204:207] cbsz:2 abid:3\n"
    "s_waitcnt lgkmcnt(3)\n"
    "s_waitcnt vmcnt(6)\n"
    "ds_read_b128 v[160:163], v234 offset:0\n"
    "ds_read_b128 v[164:167], v235 offset:0\n"
    "ds_read_b32 v236, v253 offset:256\n"
    "ds_read_b32 v238, v253 offset:260\n"
    "ds_read_b64_tr_b16 v[168:169], v240 offset:0\n"
    "ds_read_b64_tr_b16 v[170:171], v241 offset:256\n"
    "ds_read_b64_tr_b16 v[172:173], v242 offset:512\n"
    "ds_read_b64_tr_b16 v[174:175], v243 offset:768\n"
    "v_cvt_pk_bf16_f32 v18, v148, v149\n"
    "v_cvt_pk_bf16_f32 v19, v150, v151\n"
    "v_mfma_f32_4x4x4_16b_bf16 v[144:147], v[184:185], v[56:57], v[208:211] cbsz:2 abid:0\n"
    "v_cvt_pk_bf16_f32 v20, v152, v153\n"
    "v_cvt_pk_bf16_f32 v21, v154, v155\n"
    "v_mfma_f32_4x4x4_16b_bf16 v[148:151], v[184:185], v[58:59], v[212:215] cbsz:2 abid:1\n"
    "s_waitcnt lgkmcnt(8)\n"
    "v_cvt_pk_bf16_f32 v22, v156, v157\n"
    "v_cvt_pk_bf16_f32 v23, v158, v159\n"
    "v_mfma_f32_4x4x4_16b_bf16 v[152:155], v[184:185], v[60:61], v[216:219] cbsz:2 abid:2\n"
    "v_cvt_pk_bf16_f32 v24, v144, v145\n"
    "v_cvt_pk_bf16_f32 v25, v146, v147\n"
    "v_mfma_f32_4x4x4_16b_bf16 v[156:159], v[184:185], v[62:63], v[220:223] cbsz:2 abid:3\n"
    "s_cbranch_vccnz .Ldin_e8\n"
    "s_barrier\n"
    ".Ldin_e8:\n"
    "s_nop 0\n"
    "ds_read_b128 v[112:115], v252 offset:17408\n"
    "ds_read_b128 v[116:119], v252 offset:17424\n"
    "ds_read_b128 v[120:123], v252 offset:17536\n"
    "ds_read_b128 v[124:127], v252 offset:17552\n"
    "v_cvt_pk_bf16_f32 v26, v148, v149\n"
    "v_cvt_pk_bf16_f32 v27, v150, v151\n"
    "v_cvt_pk_bf16_f32 v28, v152, v153\n"
    "v_cvt_pk_bf16_f32 v29, v154, v155\n"
    "s_nop 0\n"
    "v_cvt_pk_bf16_f32 v30, v156, v157\n"
    "v_cvt_pk_bf16_f32 v31, v158, v159\n"
    "s_add_u32 m0, s68, 0\n"
    "s_waitcnt lgkmcnt(0)\n"
    "v_mfma_f32_16x16x32_bf16 v[128:131], v[0:3], v[160:163], v[112:115]\n"
    "ds_read_b128 v[96:99], v250 offset:34816\n"
    "v_mfma_f32_16x16x32_bf16 v[128:131], v[4:7], v[164:167], v[128:131]\n"
    "ds_read_b128 v[100:103], v250 offset:34832\n"
    "v_mfma_f32_16x16x32_bf16 v[132:135], v[8:11], v[160:163], v[116:119]\n"
    "ds_read_b128 v[104:107], v250 offset:34944\n"
    "v_mfma_f32_16x16x32_bf16 v[132:135], v[12:15], v[164:167], v[132:135]\n"
    "ds_read_b128 v[108:111], v250 offset:34960\n"
    "v_mfma_f32_16x16x32_bf16 v[136:139], v[16:19], v[160:163], v[120:123]\n"
    "v_mfma_f32_16x16x32_bf16 v[136:139], v[20:23], v[164:167], v[136:139]\n"
    "v_mfma_f32_16x16x32_bf16 v[140:143], v[24:27], v[160:163], v[124:127]\n"
    "v_mfma_f32_16x16x32_bf16 v[140:143], v[28:31], v[164:167], v[140:143]\n"
    "s_waitcnt vmcnt(4)\n"
    "ds_read_b128 v[160:163], v234 offset:2048\n"
    "ds_read_b128 v[164:167], v235 offset:2048\n"
    "buffer_load_dwordx4 v[236:237], s[40:43], 0 idxen offen offset:0 nt lds\n"
    "buffer_load_dwordx4 v[238:239], s[40:43], 0 idxen offen offset:1024 nt lds\n"
    "v_and_b32 v248, 24, v249\n"
    "s_memrealtime s[34:35]\n"
    "s_bitcmp1_b32 s7, 0\n"
    "s_cselect_b32 s94, 52, 58\n"
    "s_cselect_b32 s95, 10, 35\n"
    "s_cmp_lg_u64 vcc, 0\n"
    "s_cselect_b32 s94, s94, 52\n"
    "s_cselect_b32 s95, s95, 20\n"
    "s_cmp_lg_u32 s72, 0\n"
    "s_cselect_b32 s72, s94, 0\n"
    "s_cselect_b32 s95, s95, 0\n"
    "s_waitcnt lgkmcnt(0)\n"
    "s_lshl1_add_u32 s76, s95, s34\n"
    "s_cmp_lt_i32 s59, s57\n"
    "s_cselect_b32 s76, s76, -1\n"
    "s_bitcmp1_b32 s58, 30\n"
    "s_cbranch_scc1 .Ldin_odd\n"
    "s_nop 0\n"
    ".Ldin_A0:\n"
    "v_mfma_f32_16x16x32_bf16 v[144:147], v[0:3], v[160:163], v[112:115]\n"
    "v_exp_f32 v128, v128 clamp\n"
    "v_mfma_f32_16x16x32_bf16 v[144:147], v[4:7], v[164:167], v[144:147]\n"
    "v_exp_f32 v129, v129 clamp\n"
    "v_mfma_f32_16x16x32_bf16 v[148:151], v[8:11], v[160:163], v[116:119]\n"
    "v_exp_f32 v130, v130 clamp\n"
    "v_mfma_f32_16x16x32_bf16 v[148:151], v[12:15], v[164:167], v[148:151]\n"
    "v_exp_f32 v131, v131 clamp\n"
    "v_mfma_f32_16x16x32_bf16 v[152:155], v[16:19], v[160:163], v[120:123]\n"
    "v_exp_f32 v132, v132 clamp\n"
    "v_mfma_f32_16x16x32_bf16 v[152:155], v[20:23], v[164:167], v[152:155]\n"
    "v_exp_f32 v133, v133 clamp\n"
    "v_mfma_f32_16x16x32_bf16 v[156:159], v[24:27], v[160:163], v[124:127]\n"
    "v_exp_f32 v134, v134 clamp\n"
    "v_mfma_f32_16x16x32_bf16 v[156:159], v[28:31], v[164:167], v[156:159]\n"
    "v_exp_f32 v135, v135 clamp\n"
    "v_exp_f32 v136, v136 clamp\n"
    "s_mov_b32 m0, s62\n"
    "v_exp_f32 v137, v137 clamp\n"
    "v_add_u32_e64 v253, s67, v244\n"
    "s_waitcnt lgkmcnt(0)\n"
    "v_mfma_f32_4x4x4_16b_bf16 v[224:227], v[228:229], v[176:177], v[224:227] cbsz:4 abid:0\n"
    "v_exp_f32 v138, v138 clamp\n"
    "v_exp_f32 v139, v139 clamp\n"
    "v_mfma_f32_4x4x4_16b_bf16 v[224:227], v[228:229], v[178:179], v[224:227] cbsz:4 abid:1\n"
    "v_exp_f32 v140, v140 clamp\n"
    "v_exp_f32 v141, v141 clamp\n"
    "v_mfma_f32_4x4x4_16b_bf16 v[224:227], v[228:229], v[180:181], v[224:227] cbsz:4 abid:2\n"
    "v_exp_f32 v142, v142 clamp\n"
    "v_exp_f32 v143, v143 clamp\n"
    "v_mfma_f32_4x4x4_16b_bf16 v[224:227], v[228:229], v[182:183], v[224:227] cbsz:4 abid:3\n"
    "s_waitcnt vmcnt(4)\n"
    "ds_read_b64_tr_b16 v[176:177], v240 offset:2048\n"
    "ds_read_b64_tr_b16 v[178:179], v241 offset:2304\n"
    "ds_read_b64_tr_b16 v[180:181], v242 offset:2560\n"
    "ds_read_b64_tr_b16 v[182:183], v243 offset:2816\n"
    "ds_read_b32 v236, v253 offset:0\n"
    "ds_read_b32 v238, v253 offset:4\n"
    "ds_read_b128 v[160:163], v234 offset:4096\n"
    "ds_read_b128 v[164:167], v235 offset:4096\n"
    "s_cbranch_vccz .Ldin_A0_np\n"
    "s_setprio 1\n"
    ".Ldin_A0_np:\n"
    "v_pk_fma_f32 v[128:129], v[128:129], s[70:71], v[96:97]\n"
    "v_pk_fma_f32 v[130:131], v[130:131], s[70:71], v[98:99]\n"
    "v_pk_add_f32 v[190:191], v[224:225], v[226:227]\n"
    "v_add_f32 v190, v190, v191\n"
    "ds_write_addtid_b32 v190 offset:32768\n"
    "s_add_u32 m0, s68, 2048\n"
    "s_bfe_u32 s94, s59, 0x180006\n"
    "s_mul_i32 s86, s94, s56\n"
    "v_mbcnt_lo_u32_b32 v184, s20, 0\n"
    "v_mbcnt_lo_u32_b32 v185, s22, 0\n"
    "v_mbcnt_lo_u32_b32 v186, s24, 0\n"
    "v_mbcnt_lo_u32_b32 v187, s26, 0\n"
    "s_bcnt1_i32_b64 s90, s[20:21]\n"
    "s_bcnt1_i32_b64 s91, s[22:23]\n"
    "s_bcnt1_i32_b64 s92, s[24:25]\n"
    "s_bcnt1_i32_b64 s93, s[26:27]\n"
    "v_mbcnt_hi_u32_b32 v184, s21, v184\n"
    "v_mbcnt_hi_u32_b32 v185, s23, v185\n"
    "v_mbcnt_hi_u32_b32 v186, s25, v186\n"
    "v_mbcnt_hi_u32_b32 v187, s27, v187\n"
    "s_lshl2_add_u32 s90, s90, s65\n"
    "s_lshl2_add_u32 s91, s91, s90\n"
    "s_lshl2_add_u32 s92, s92, s91\n"
    "s_lshl2_add_u32 s93, s93, s92\n"
    "v_add_u32_e64 v188, s86, v251\n"
    "v_pk_add_f32 v[230:231], v[128:129], v[130:131]\n"
    "v_pk_mul_f32 v[232:233], v[128:129], v[130:131]\n"
    "v_pk_fma_f32 v[132:133], v[132:133], s[70:71], v[100:101]\n"
    "v_pk_fma_f32 v[230:231], v[230:231], v[132:133], v[232:233]\n"
    "v_pk_mul_f32 v[232:233], v[232:233], v[132:133]\n"
    "v_lshl_add_u32 v184, v184, 2, s65\n"
    "v_lshl_add_u32 v185, v185, 2, s90\n"
    "v_lshl_add_u32 v186, v186, 2, s91\n"
    "v_lshl_add_u32 v187, v187, 2, s92\n"
    "v_mov_b32 v191, -1\n"
    "v_lshl_add_u32 v190, v249, 1, s93\n"
    "ds_write2_b32 v190, v191, v191 offset1:64\n"
    "s_mov_b64 exec, s[20:21]\n"
    "ds_write_b32 v184, v188\n"
    "s_mov_b64 exec, s[22:23]\n"
    "v_add_u32 v189, 64, v188\n"
    "ds_write_b32 v185, v189\n"
    "s_mov_b64 exec, s[24:25]\n"
    "v_add_u32 v190, 128, v188\n"
    "ds_write_b32 v186, v190\n"
    "s_mov_b64 exec, s[26:27]\n"
    "v_add_u32 v189, 192, v188\n"
    "ds_write_b32 v187, v189\n"
    "s_mov_b64 exec, -1\n"
    "s_add_u32 s95, s93, 60\n"
    "s_add_u32 s94, s82, 128\n"
    "s_andn2_b32 s95, s95, 63\n"
    "s_max_u32 s83, s95, s94\n"
    "v_pk_fma_f32 v[134:135], v[134:135], s[70:71], v[102:103]\n"
    "v_pk_fma_f32 v[230:231], v[230:231], v[134:135], v[232:233]\n"
    "v_pk_mul_f32 v[232:233], v[232:233], v[134:135]\n"
    "v_pk_fma_f32 v[136:137], v[136:137], s[70:71], v[104:105]\n"
    "v_pk_fma_f32 v[230:231], v[230:231], v[136:137], v[232:233]\n"
    "v_pk_mul_f32 v[232:233], v[232:233], v[136:137]\n"
    "v_pk_fma_f32 v[138:139], v[138:139], s[70:71], v[106:107]\n"
    "v_pk_fma_f32 v[230:231], v[230:231], v[138:139], v[232:233]\n"
    "v_pk_mul_f32 v[232:233], v[232:233], v[138:139]\n"
    "v_pk_fma_f32 v[140:141], v[140:141], s[70:71], v[108:109]\n"
    "v_pk_fma_f32 v[230:231], v[230:231], v[140:141], v[232:233]\n"
    "v_pk_mul_f32 v[232:233], v[232:233], v[140:141]\n"
    "v_pk_fma_f32 v[142:143], v[142:143], s[70:71], v[110:111]\n"
    "v_pk_fma_f32 v[230:231], v[230:231], v[142:143], v[232:233]\n"
    "v_pk_mul_f32 v[232:233], v[232:233], v[142:143]\n"
    "v_rcp_f32 v232, v232\n"
    "v_rcp_f32 v233, v233\n"
    "v_fma_f32 v230, v230, v232, v247\n"
    "v_fmac_f32 v230, v231, v233\n"
    "v_cvt_pk_bf16_f32 v230, v230, v230\n"
    "s_add_u32 s67, s67, 64\n"
    "s_waitcnt lgkmcnt(0)\n"
    "ds_write_b16 v245, v230\n"
    "ds_read_b64 v[228:229], v246\n"
    "buffer_load_dwordx4 v[236:237], s[40:43], 0 idxen offen offset:0 nt lds\n"
    "buffer_load_dwordx4 v[238:239], s[40:43], 0 idxen offen offset:1024 nt lds\n"
    ".Ldin_B1:\n"
    "v_mfma_f32_16x16x32_bf16 v[128:131], v[0:3], v[160:163], v[112:115]\n"
    "v_exp_f32 v144, v144 clamp\n"
    "v_mfma_f32_16x16x32_bf16 v[128:131], v[4:7], v[164:167], v[128:131]\n"
    "v_exp_f32 v145, v145 clamp\n"
    "v_mfma_f32_16x16x32_bf16 v[132:135], v[8:11], v[160:163], v[116:119]\n"
    "v_exp_f32 v146, v146 clamp\n"
    "v_mfma_f32_16x16x32_bf16 v[132:135], v[12:15], v[164:167], v[132:135]\n"
    "v_exp_f32 v147, v147 clamp\n"
    "v_mfma_f32_16x16x32_bf16 v[136:139], v[16:19], v[160:163], v[120:123]\n"
    "v_exp_f32 v148, v148 clamp\n"
    "v_mfma_f32_16x16x32_bf16 v[136:139], v[20:23], v[164:167], v[136:139]\n"
    "v_exp_f32 v149, v149 clamp\n"
    "v_mfma_f32_16x16x32_bf16 v[140:143], v[24:27], v[160:163], v[124:127]\n"
    "v_exp_f32 v150, v150 clamp\n"
    "v_mfma_f32_16x16x32_bf16 v[140:143], v[28:31], v[164:167], v[140:143]\n"
    "v_exp_f32 v151, v151 clamp\n"
    "v_exp_f32 v152, v152 clamp\n"
    "s_cmp_eq_u32 s67, s80\n"
    "s_cselect_b32 s67, s65, s67\n"
    "s_and_b32 s94, s59, 63\n"
    "s_mul_i32 s94, s94, 272\n"
    "v_add_u32 v252, s94, v250\n"
    "s_add_u32 m0, s68, 4096\n"
    "v_exp_f32 v153, v153 clamp\n"
    "v_add_u32 v253, s67, v244\n"
    "s_waitcnt lgkmcnt(0)\n"
    "v_mfma_f32_4x4x4_16b_bf16 v[224:227], v[228:229], v[168:169], 0 cbsz:4 abid:0\n"
    "v_exp_f32 v154, v154 clamp\n"
    "v_exp_f32 v155, v155 clamp\n"
    "v_mfma_f32_4x4x4_16b_bf16 v[224:227], v[228:229], v[170:171], v[224:227] cbsz:4 abid:1\n"
    "v_exp_f32 v156, v156 clamp\n"
    "v_exp_f32 v157, v157 clamp\n"
    "v_mfma_f32_4x4x4_16b_bf16 v[224:227], v[228:229], v[172:173], v[224:227] cbsz:4 abid:2\n"
    "v_exp_f32 v158, v158 clamp\n"
    "v_exp_f32 v159, v159 clamp\n"
    "v_mfma_f32_4x4x4_16b_bf16 v[224:227], v[228:229], v[174:175], v[224:227] cbsz:4 abid:3\n"
    "s_waitcnt vmcnt(4)\n"
    "s_memrealtime s[34:35]\n"
    "ds_read_b64_tr_b16 v[168:169], v240 offset:4096\n"
    "ds_read_b64_tr_b16 v[170:171], v241 offset:4352\n"
    "ds_read_b64_tr_b16 v[172:173], v242 offset:4608\n"
    "ds_read_b64_tr_b16 v[174:175], v243 offset:4864\n"
    "ds_read_b32 v236, v253 offset:0\n"
    "ds_read_b32 v238, v253 offset:4\n"
    "ds_read_b128 v[160:163], v234 offset:6144\n"
    "ds_read_b128 v[164:167], v235 offset:6144\n"
    "v_add_u32_e64 v184, v248, v252\n"
    "ds_read_b64 v[184:185], v184\n"
    "v_pk_fma_f32 v[144:145], v[144:145], s[70:71], v[96:97]\n"
    "v_pk_fma_f32 v[146:147], v[146:147], s[70:71], v[98:99]\n"
    "v_pk_add_f32 v[230:231], v[144:145], v[146:147]\n"
    "v_pk_mul_f32 v[232:233], v[144:145], v[146:147]\n"
    "v_pk_fma_f32 v[148:149], v[148:149], s[70:71], v[100:101]\n"
    "v_pk_fma_f32 v[230:231], v[230:231], v[148:149], v[232:233]\n"
    "v_pk_mul_f32 v[232:233], v[232:233], v[148:149]\n"
    "v_pk_fma_f32 v[150:151], v[150:151], s[70:71], v[102:103]\n"
    "v_pk_fma_f32 v[230:231], v[230:231], v[150:151], v[232:233]\n"
    "v_pk_mul_f32 v[232:233], v[232:233], v[150:151]\n"
    "v_pk_fma_f32 v[152:153], v[152:153], s[70:71], v[104:105]\n"
    "v_pk_fma_f32 v[230:231], v[230:231], v[152:153], v[232:233]\n"
    "v_pk_mul_f32 v[232:233], v[232:233], v[152:153]\n"
    "v_pk_fma_f32 v[154:155], v[154:155], s[70:71], v[106:107]\n"
    "v_pk_fma_f32 v[230:231], v[230:231], v[154:155], v[232:233]\n"
    "v_pk_mul_f32 v[232:233], v[232:233], v[154:155]\n"
    "v_pk_fma_f32 v[156:157], v[156:157], s[70:71], v[108:109]\n"
    "v_pk_fma_f32 v[230:231], v[230:231], v[156:157], v[232:233]\n"
    "v_pk_mul_f32 v[232:233], v[232:233], v[156:157]\n"
    "v_pk_fma_f32 v[158:159], v[158:159], s[70:71], v[110:111]\n"
    "v_pk_fma_f32 v[230:231], v[230:231], v[158:159], v[232:233]\n"
    "v_pk_mul_f32 v[232:233], v[232:233], v[158:159]\n"
    "v_rcp_f32 v232, v232\n"
    "v_rcp_f32 v233, v233\n"
    "v_fma_f32 v230, v230, v232, v247\n"
    "v_fmac_f32 v230, v231, v233\n"
    "v_cvt_pk_bf16_f32 v230, v230, v230\n"
    "s_add_u32 s67, s67, 64\n"
    "s_waitcnt lgkmcnt(0)\n"
    "ds_write_b16 v245, v230\n"
    "ds_read_b64 v[228:229], v246\n"
    "v_and_b32 v184, v254, v184\n"
    "v_and_b32 v185, v255, v185\n"
    "s_cmp_le_u32 s34, s76\n"
    "s_cbranch_scc1 .Ldin_B1_wait\n"
    ".Ldin_B1_go:\n"
    "s_sub_u32 s94, s34, 60\n"
    "s_max_u32 s76, s76, s94\n"
    "s_add_u32 s76, s76, s72\n"
    ".Ldin_B1_dma:\n"
    "buffer_load_dwordx4 v[236:237], s[40:43], 0 idxen offen offset:0 nt lds\n"
    "buffer_load_dwordx4 v[238:239], s[40:43], 0 idxen offen offset:1024 nt lds\n"
    ".Ldin_B1_nd:\n"
    ".Ldin_G2:\n"
    "v_mfma_f32_16x16x32_bf16 v[144:147], v[0:3], v[160:163], v[112:115]\n"
    "v_exp_f32 v128, v128 clamp\n"
    "v_mfma_f32_16x16x32_bf16 v[144:147], v[4:7], v[164:167], v[144:147]\n"
    "v_exp_f32 v129, v129 clamp\n"
    "v_mfma_f32_16x16x32_bf16 v[148:151], v[8:11], v[160:163], v[116:119]\n"
    "v_exp_f32 v130, v130 clamp\n"
    "v_mfma_f32_16x16x32_bf16 v[148:151], v[12:15], v[164:167], v[148:151]\n"
    "v_exp_f32 v131, v131 clamp\n"
    "v_mfma_f32_16x16x32_bf16 v[152:155], v[16:19], v[160:163], v[120:123]\n"
    "v_exp_f32 v132, v132 clamp\n"
    "v_mfma_f32_16x16x32_bf16 v[152:155], v[20:23], v[164:167], v[152:155]\n"
    "v_exp_f32 v133, v133 clamp\n"
    "v_mfma_f32_16x16x32_bf16 v[156:159], v[24:27], v[160:163], v[124:127]\n"
    "v_exp_f32 v134, v134 clamp\n"
    "v_mfma_f32_16x16x32_bf16 v[156:159], v[28:31], v[164:167], v[156:159]\n"
    "v_exp_f32 v135, v135 clamp\n"
    "v_exp_f32 v136, v136 clamp\n"
    "s_cmp_eq_u32 s67, s80\n"
    "s_cselect_b32 s67, s65, s67\n"
    "s_add_u32 m0, s68, 6144\n"
    "v_exp_f32 v137, v137 clamp\n"
    "v_add_u32_e64 v253, s67, v244\n"
    "s_waitcnt lgkmcnt(0)\n"
    "v_mfma_f32_4x4x4_16b_bf16 v[224:227], v[228:229], v[176:177], v[224:227] cbsz:4 abid:0\n"
    "v_exp_f32 v138, v138 clamp\n"
    "v_exp_f32 v139, v139 clamp\n"
    "v_mfma_f32_4x4x4_16b_bf16 v[224:227], v[228:229], v[178:179], v[224:227] cbsz:4 abid:1\n"
    "v_exp_f32 v140, v140 clamp\n"
    "v_exp_f32 v141, v141 clamp\n"
    "v_mfma_f32_4x4x4_16b_bf16 v[224:227], v[228:229], v[180:181], v[224:227] cbsz:4 abid:2\n"
    "v_exp_f32 v142, v142 clamp\n"
    "v_exp_f32 v143, v143 clamp\n"
    "v_mfma_f32_4x4x4_16b_bf16 v[224:227], v[228:229], v[182:183], v[224:227] cbsz:4 abid:3\n"
    "s_waitcnt vmcnt(4)\n"
    "s_memrealtime s[34:35]\n"
    "ds_read_b64_tr_b16 v[176:177], v240 offset:6144\n"
    "ds_read_b64_tr_b16 v[178:179], v241 offset:6400\n"
    "ds_read_b64_tr_b16 v[180:181], v242 offset:6656\n"
    "ds_read_b64_tr_b16 v[182:183], v243 offset:6912\n"
    "ds_read_b32 v236, v253 offset:0\n"
    "ds_read_b32 v238, v253 offset:4\n"
    "ds_read_b128 v[160:163], v234 offset:0\n"
    "ds_read_b128 v[164:167], v235 offset:0\n"
    "v_pk_fma_f32 v[128:129], v[128:129], s[70:71], v[96:97]\n"
    "v_pk_fma_f32 v[130:131], v[130:131], s[70:71], v[98:99]\n"
    "v_pk_add_f32 v[230:231], v[128:129], v[130:131]\n"
    "v_pk_mul_f32 v[232:233], v[128:129], v[130:131]\n"
    "v_pk_fma_f32 v[132:133], v[132:133], s[70:71], v[100:101]\n"
    "v_pk_fma_f32 v[230:231], v[230:231], v[132:133], v[232:233]\n"
    "v_pk_mul_f32 v[232:233], v[232:233], v[132:133]\n"
    "v_pk_fma_f32 v[134:135], v[134:135], s[70:71], v[102:103]\n"
    "v_pk_fma_f32 v[230:231], v[230:231], v[134:135], v[232:233]\n"
    "v_pk_mul_f32 v[232:233], v[232:233], v[134:135]\n"
    "v_pk_fma_f32 v[136:137], v[136:137], s[70:71], v[104:105]\n"
    "v_pk_fma_f32 v[230:231], v[230:231], v[136:137], v[232:233]\n"
    "v_pk_mul_f32 v[232:233], v[232:233], v[136:137]\n"
    "v_pk_fma_f32 v[138:139], v[138:139], s[70:71], v[106:107]\n"
    "v_pk_fma_f32 v[230:231], v[230:231], v[138:139], v[232:233]\n"
    "v_pk_mul_f32 v[232:233], v[232:233], v[138:139]\n"
    "v_pk_fma_f32 v[140:141], v[140:141], s[70:71], v[108:109]\n"
    "v_pk_fma_f32 v[230:231], v[230:231], v[140:141], v[232:233]\n"
    "v_pk_mul_f32 v[232:233], v[232:233], v[140:141]\n"
    "v_pk_fma_f32 v[142:143], v[142:143], s[70:71], v[110:111]\n"
    "v_pk_fma_f32 v[230:231], v[230:231], v[142:143], v[232:233]\n"
    "v_pk_mul_f32 v[232:233], v[232:233], v[142:143]\n"
    "v_rcp_f32 v232, v232\n"
    "v_rcp_f32 v233, v233\n"
    "v_fma_f32 v230, v230, v232, v247\n"
    "v_fmac_f32 v230, v231, v233\n"
    "v_cvt_pk_bf16_f32 v230, v230, v230\n"
    "s_add_u32 s67, s67, 64\n"
    "s_waitcnt lgkmcnt(0)\n"
    "ds_write_b16 v245, v230\n"
    "ds_read_b64 v[228:229], v246\n"
    "s_cmp_le_u32 s34, s76\n"
    "s_cbranch_scc1 .Ldin_G2_wait\n"
    ".Ldin_G2_go:\n"
    "s_sub_u32 s94, s34, 60\n"
    "s_max_u32 s76, s76, s94\n"
    "s_add_u32 s76, s76, s72\n"
    ".Ldin_G2_dma:\n"
    "buffer_load_dwordx4 v[236:237], s[40:43], 0 idxen offen offset:0 nt lds\n"
    "buffer_load_dwordx4 v[238:239], s[40:43], 0 idxen offen offset:1024 nt lds\n"
    ".Ldin_G2_nd:\n"
    "s_cmp_eq_u32 s67, s82\n"
    "s_cbranch_scc1 .Ldin_L3\n"
    ".Ldin_G3:\n"
    "v_mfma_f32_16x16x32_bf16 v[128:131], v[0:3], v[160:163], v[112:115]\n"
    "v_exp_f32 v144, v144 clamp\n"
    "v_mfma_f32_16x16x32_bf16 v[128:131], v[4:7], v[164:167], v[128:131]\n"
    "v_exp_f32 v145, v145 clamp\n"
    "v_mfma_f32_16x16x32_bf16 v[132:135], v[8:11], v[160:163], v[116:119]\n"
    "v_exp_f32 v146, v146 clamp\n"
    "v_mfma_f32_16x16x32_bf16 v[132:135], v[12:15], v[164:167], v[132:135]\n"
    "v_exp_f32 v147, v147 clamp\n"
    "v_mfma_f32_16x16x32_bf16 v[136:139], v[16:19], v[160:163], v[120:123]\n"
    "v_exp_f32 v148, v148 clamp\n"
    "v_mfma_f32_16x16x32_bf16 v[136:139], v[20:23], v[164:167], v[136:139]\n"
    "v_exp_f32 v149, v149 clamp\n"
    "v_mfma_f32_16x16x32_bf16 v[140:143], v[24:27], v[160:163], v[124:127]\n"
    "v_exp_f32 v150, v150 clamp\n"
    "v_mfma_f32_16x16x32_bf16 v[140:143], v[28:31], v[164:167], v[140:143]\n"
    "v_exp_f32 v151, v151 clamp\n"
    "v_exp_f32 v152, v152 clamp\n"
    "s_cmp_eq_u32 s67, s80\n"
    "s_cselect_b32 s67, s65, s67\n"
    "s_add_u32 m0, s68, 0\n"
    "v_exp_f32 v153, v153 clamp\n"
    "v_add_u32 v253, s67, v244\n"
    "s_waitcnt lgkmcnt(0)\n"
    "v_mfma_f32_4x4x4_16b_bf16 v[224:227], v[228:229], v[168:169], v[224:227] cbsz:4 abid:0\n"
    "v_exp_f32 v154, v154 clamp\n"
    "v_exp_f32 v155, v155 clamp\n"
    "v_mfma_f32_4x4x4_16b_bf16 v[224:227], v[228:229], v[170:171], v[224:227] cbsz:4 abid:1\n"
    "v_exp_f32 v156, v156 clamp\n"
    "v_exp_f32 v157, v157 clamp\n"
    "v_mfma_f32_4x4x4_16b_bf16 v[224:227], v[228:229], v[172:173], v[224:227] cbsz:4 abid:2\n"
    "v_exp_f32 v158, v158 clamp\n"
    "v_exp_f32 v159, v159 clamp\n"
    "v_mfma_f32_4x4x4_16b_bf16 v[224:227], v[228:229], v[174:175], v[224:227] cbsz:4 abid:3\n"
    "s_waitcnt vmcnt(4)\n"
    "s_memrealtime s[34:35]\n"
    "ds_read_b64_tr_b16 v[168:169], v240 offset:0\n"
    "ds_read_b64_tr_b16 v[170:171], v241 offset:256\n"
    "ds_read_b64_tr_b16 v[172:173], v242 offset:512\n"
    "ds_read_b64_tr_b16 v[174:175], v243 offset:768\n"
    "ds_read_b32 v236, v253 offset:0\n"
    "ds_read_b32 v238, v253 offset:4\n"
    "ds_read_b128 v[160:163], v234 offset:2048\n"
    "ds_read_b128 v[164:167], v235 offset:2048\n"
    "v_pk_fma_f32 v[144:145], v[144:145], s[70:71], v[96:97]\n"
    "v_pk_fma_f32 v[146:147], v[146:147], s[70:71], v[98:99]\n"
    "v_pk_add_f32 v[230:231], v[144:145], v[146:147]\n"
    "v_pk_mul_f32 v[232:233], v[144:145], v[146:147]\n"
    "v_pk_fma_f32 v[148:149], v[148:149], s[70:71], v[100:101]\n"
    "v_pk_fma_f32 v[230:231], v[230:231], v[148:149], v[232:233]\n"
    "v_pk_mul_f32 v[232:233], v[232:233], v[148:149]\n"
    "v_pk_fma_f32 v[150:151], v[150:151], s[70:71], v[102:103]\n"
    "v_pk_fma_f32 v[230:231], v[230:231], v[150:151], v[232:233]\n"
    "v_pk_mul_f32 v[232:233], v[232:233], v[150:151]\n"
    "v_pk_fma_f32 v[152:153], v[152:153], s[70:71], v[104:105]\n"
    "v_pk_fma_f32 v[230:231], v[230:231], v[152:153], v[232:233]\n"
    "v_pk_mul_f32 v[232:233], v[232:233], v[152:153]\n"
    "v_pk_fma_f32 v[154:155], v[154:155], s[70:71], v[106:107]\n"
    "v_pk_fma_f32 v[230:231], v[230:231], v[154:155], v[232:233]\n"
    "v_pk_mul_f32 v[232:233], v[232:233], v[154:155]\n"
    "v_pk_fma_f32 v[156:157], v[156:157], s[70:71], v[108:109]\n"
    "v_pk_fma_f32 v[230:231], v[230:231], v[156:157], v[232:233]\n"
    "v_pk_mul_f32 v[232:233], v[232:233], v[156:157]\n"
    "v_pk_fma_f32 v[158:159], v[158:159], s[70:71], v[110:111]\n"
    "v_pk_fma_f32 v[230:231], v[230:231], v[158:159], v[232:233]\n"
    "v_pk_mul_f32 v[232:233], v[232:233], v[158:159]\n"
    "v_rcp_f32 v232, v232\n"
    "v_rcp_f32 v233, v233\n"
    "v_fma_f32 v230, v230, v232, v247\n"
    "v_fmac_f32 v230, v231, v233\n"
    "v_cvt_pk_bf16_f32 v230, v230, v230\n"
    "s_add_u32 s67, s67, 64\n"
    "s_waitcnt lgkmcnt(0)\n"
    "ds_write_b16 v245, v230\n"
    "ds_read_b64 v[228:229], v246\n"
    "s_cmp_le_u32 s34, s76\n"
    "s_cbranch_scc1 .Ldin_G3_wait\n"
    ".Ldin_G3_go:\n"
    "s_add_u32 s76, s76, s72\n"
    ".Ldin_G3_dma:\n"
    "buffer_load_dwordx4 v[236:237], s[40:43], 0 idxen offen offset:0 nt lds\n"
    "buffer_load_dwordx4 v[238:239], s[40:43], 0 idxen offen offset:1024 nt lds\n"
    ".Ldin_G3_nd:\n"
    "s_cmp_eq_u32 s67, s82\n"
    "s_cbranch_scc1 .Ldin_L0\n"
    ".Ldin_G0:\n"
    "v_mfma_f32_16x16x32_bf16 v[144:147], v[0:3], v[160:163], v[112:115]\n"
    "v_exp_f32 v128, v128 clamp\n"
    "v_mfma_f32_16x16x32_bf16 v[144:147], v[4:7], v[164:167], v[144:147]\n"
    "v_exp_f32 v129, v129 clamp\n"
    "v_mfma_f32_16x16x32_bf16 v[148:151], v[8:11], v[160:163], v[116:119]\n"
    "v_exp_f32 v130, v130 clamp\n"
    "v_mfma_f32_16x16x32_bf16 v[148:151], v[12:15], v[164:167], v[148:151]\n"
    "v_exp_f32 v131, v131 clamp\n"
    "v_mfma_f32_16x16x32_bf16 v[152:155], v[16:19], v[160:163], v[120:123]\n"
    "v_exp_f32 v132, v132 clamp\n"
    "v_mfma_f32_16x16x32_bf16 v[152:155], v[20:23], v[164:167], v[152:155]\n"
    "v_exp_f32 v133, v133 clamp\n"
    "v_mfma_f32_16x16x32_bf16 v[156:159], v[24:27], v[160:163], v[124:127]\n"
    "v_exp_f32 v134, v134 clamp\n"
    "v_mfma_f32_16x16x32_bf16 v[156:159], v[28:31], v[164:167], v[156:159]\n"
    "v_exp_f32 v135, v135 clamp\n"
    "v_exp_f32 v136, v136 clamp\n"
    "s_cmp_eq_u32 s67, s80\n"
    "s_cselect_b32 s67, s65, s67\n"
    "s_add_u32 m0, s68, 2048\n"
    "v_exp_f32 v137, v137 clamp\n"
    "v_add_u32_e64 v253, s67, v244\n"
    "s_waitcnt lgkmcnt(0)\n"
    "v_mfma_f32_4x4x4_16b_bf16 v[224:227], v[228:229], v[176:177], v[224:227] cbsz:4 abid:0\n"
    "v_exp_f32 v138, v138 clamp\n"
    "v_exp_f32 v139, v139 clamp\n"
    "v_mfma_f32_4x4x4_16b_bf16 v[224:227], v[228:229], v[178:179], v[224:227] cbsz:4 abid:1\n"
    "v_exp_f32 v140, v140 clamp\n"
    "v_exp_f32 v141, v141 clamp\n"
    "v_mfma_f32_4x4x4_16b_bf16 v[224:227], v[228:229], v[180:181], v[224:227] cbsz:4 abid:2\n"
    "v_exp_f32 v142, v142 clamp\n"
    "v_exp_f32 v143, v143 clamp\n"
    "v_mfma_f32_4x4x4_16b_bf16 v[224:227], v[228:229], v[182:183], v[224:227] cbsz:4 abid:3\n"
    "s_waitcnt vmcnt(4)\n"
    "s_memrealtime s[34:35]\n"
    "ds_read_b64_tr_b16 v[176:177], v240 offset:2048\n"
    "ds_read_b64_tr_b16 v[178:179], v241 offset:2304\n"
    "ds_read_b64_tr_b16 v[180:181], v242 offset:2560\n"
    "ds_read_b64_tr_b16 v[182:183], v243 offset:2816\n"
    "ds_read_b32 v236, v253 offset:0\n"
    "ds_read_b32 v238, v253 offset:4\n"
    "ds_read_b128 v[160:163], v234 offset:4096\n"
    "ds_read_b128 v[164:167], v235 offset:4096\n"
    "v_pk_fma_f32 v[128:129], v[128:129], s[70:71], v[96:97]\n"
    "v_pk_fma_f32 v[130:131], v[130:131], s[70:71], v[98:99]\n"
    "v_pk_add_f32 v[230:231], v[128:129], v[130:131]\n"
    "v_pk_mul_f32 v[232:233], v[128:129], v[130:131]\n"
    "v_pk_fma_f32 v[132:133], v[132:133], s[70:71], v[100:101]\n"
    "v_pk_fma_f32 v[230:231], v[230:231], v[132:133], v[232:233]\n"
    "v_pk_mul_f32 v[232:233], v[232:233], v[132:133]\n"
    "v_pk_fma_f32 v[134:135], v[134:135], s[70:71], v[102:103]\n"
    "v_pk_fma_f32 v[230:231], v[230:231], v[134:135], v[232:233]\n"
    "v_pk_mul_f32 v[232:233], v[232:233], v[134:135]\n"
    "v_pk_fma_f32 v[136:137], v[136:137], s[70:71], v[104:105]\n"
    "v_pk_fma_f32 v[230:231], v[230:231], v[136:137], v[232:233]\n"
    "v_pk_mul_f32 v[232:233], v[232:233], v[136:137]\n"
    "v_pk_fma_f32 v[138:139], v[138:139], s[70:71], v[106:107]\n"
    "v_pk_fma_f32 v[230:231], v[230:231], v[138:139], v[232:233]\n"
    "v_pk_mul_f32 v[232:233], v[232:233], v[138:139]\n"
    "v_pk_fma_f32 v[140:141], v[140:141], s[70:71], v[108:109]\n"
    "v_pk_fma_f32 v[230:231], v[230:231], v[140:141], v[232:233]\n"
    "v_pk_mul_f32 v[232:233], v[232:233], v[140:141]\n"
    "v_pk_fma_f32 v[142:143], v[142:143], s[70:71], v[110:111]\n"
    "v_pk_fma_f32 v[230:231], v[230:231], v[142:143], v[232:233]\n"
    "v_pk_mul_f32 v[232:233], v[232:233], v[142:143]\n"
    "v_rcp_f32 v232, v232\n"
    "v_rcp_f32 v233, v233\n"
    "v_fma_f32 v230, v230, v232, v247\n"
    "v_fmac_f32 v230, v231, v233\n"
    "v_cvt_pk_bf16_f32 v230, v230, v230\n"
    "s_add_u32 s67, s67, 64\n"
    "s_waitcnt lgkmcnt(0)\n"
    "ds_write_b16 v245, v230\n"
    "ds_read_b64 v[228:229], v246\n"
    "s_cmp_le_u32 s34, s76\n"
    "s_cbranch_scc1 .Ldin_G0_wait\n"
    ".Ldin_G0_go:\n"
    "s_sub_u32 s94, s34, 60\n"
    "s_max_u32 s76, s76, s94\n"
    "s_add_u32 s76, s76, s72\n"
    ".Ldin_G0_dma:\n"
    "buffer_load_dwordx4 v[236:237], s[40:43], 0 idxen offen offset:0 nt lds\n"
    "buffer_load_dwordx4 v[238:239], s[40:43], 0 idxen offen offset:1024 nt lds\n"
    ".Ldin_G0_nd:\n"
    "s_cmp_eq_u32 s67, s82\n"
    "s_cbranch_scc1 .Ldin_L1\n"
    ".Ldin_G1:\n"
    "v_mfma_f32_16x16x32_bf16 v[128:131], v[0:3], v[160:163], v[112:115]\n"
    "v_exp_f32 v144, v144 clamp\n"
    "v_mfma_f32_16x16x32_bf16 v[128:131], v[4:7], v[164:167], v[128:131]\n"
    "v_exp_f32 v145, v145 clamp\n"
    "v_mfma_f32_16x16x32_bf16 v[132:135], v[8:11], v[160:163], v[116:119]\n"
    "v_exp_f32 v146, v146 clamp\n"
    "v_mfma_f32_16x16x32_bf16 v[132:135], v[12:15], v[164:167], v[132:135]\n"
    "v_exp_f32 v147, v147 clamp\n"
    "v_mfma_f32_16x16x32_bf16 v[136:139], v[16:19], v[160:163], v[120:123]\n"
    "v_exp_f32 v148, v148 clamp\n"
    "v_mfma_f32_16x16x32_bf16 v[136:139], v[20:23], v[164:167], v[136:139]\n"
    "v_exp_f32 v149, v149 clamp\n"
    "v_mfma_f32_16x16x32_bf16 v[140:143], v[24:27], v[160:163], v[124:127]\n"
    "v_exp_f32 v150, v150 clamp\n"
    "v_mfma_f32_16x16x32_bf16 v[140:143], v[28:31], v[164:167], v[140:143]\n"
    "v_exp_f32 v151, v151 clamp\n"
    "v_exp_f32 v152, v152 clamp\n"
    "s_cmp_eq_u32 s67, s80\n"
    "s_cselect_b32 s67, s65, s67\n"
    "s_add_u32 m0, s68, 4096\n"
    "v_exp_f32 v153, v153 clamp\n"
    "v_add_u32_e64 v253, s67, v244\n"
    "s_waitcnt lgkmcnt(0)\n"
    "v_mfma_f32_4x4x4_16b_bf16 v[224:227], v[228:229], v[168:169], v[224:227] cbsz:4 abid:0\n"
    "v_exp_f32 v154, v154 clamp\n"
    "v_exp_f32 v155, v155 clamp\n"
    "v_mfma_f32_4x4x4_16b_bf16 v[224:227], v[228:229], v[170:171], v[224:227] cbsz:4 abid:1\n"
    "v_exp_f32 v156, v156 clamp\n"
    "v_exp_f32 v157, v157 clamp\n"
    "v_mfma_f32_4x4x4_16b_bf16 v[224:227], v[228:229], v[172:173], v[224:227] cbsz:4 abid:2\n"
    "v_exp_f32 v158, v158 clamp\n"
    "v_exp_f32 v159, v159 clamp\n"
    "v_mfma_f32_4x4x4_16b_bf16 v[224:227], v[228:229], v[174:175], v[224:227] cbsz:4 abid:3\n"
    "s_waitcnt vmcnt(4)\n"
    "s_memrealtime s[34:35]\n"
    "ds_read_b64_tr_b16 v[168:169], v240 offset:4096\n"
    "ds_read_b64_tr_b16 v[170:171], v241 offset:4352\n"
    "ds_read_b64_tr_b16 v[172:173], v242 offset:4608\n"
    "ds_read_b64_tr_b16 v[174:175], v243 offset:4864\n"
    "ds_read_b32 v236, v253 offset:0\n"
    "ds_read_b32 v238, v253 offset:4\n"
    "ds_read_b128 v[160:163], v234 offset:6144\n"
    "ds_read_b128 v[164:167], v235 offset:6144\n"
    "v_pk_fma_f32 v[144:145], v[144:145], s[70:71], v[96:97]\n"
    "v_pk_fma_f32 v[146:147], v[146:147], s[70:71], v[98:99]\n"
    "v_pk_add_f32 v[230:231], v[144:145], v[146:147]\n"
    "v_pk_mul_f32 v[232:233], v[144:145], v[146:147]\n"
    "v_pk_fma_f32 v[148:149], v[148:149], s[70:71], v[100:101]\n"
    "v_pk_fma_f32 v[230:231], v[230:231], v[148:149], v[232:233]\n"
    "v_pk_mul_f32 v[232:233], v[232:233], v[148:149]\n"
    "v_pk_fma_f32 v[150:151], v[150:151], s[70:71], v[102:103]\n"
    "v_pk_fma_f32 v[230:231], v[230:231], v[150:151], v[232:233]\n"
    "v_pk_mul_f32 v[232:233], v[232:233], v[150:151]\n"
    "v_pk_fma_f32 v[152:153], v[152:153], s[70:71], v[104:105]\n"
    "v_pk_fma_f32 v[230:231], v[230:231], v[152:153], v[232:233]\n"
    "v_pk_mul_f32 v[232:233], v[232:233], v[152:153]\n"
    "v_pk_fma_f32 v[154:155], v[154:155], s[70:71], v[106:107]\n"
    "v_pk_fma_f32 v[230:231], v[230:231], v[154:155], v[232:233]\n"
    "v_pk_mul_f32 v[232:233], v[232:233], v[154:155]\n"
    "v_pk_fma_f32 v[156:157], v[156:157], s[70:71], v[108:109]\n"
    "v_pk_fma_f32 v[230:231], v[230:231], v[156:157], v[232:233]\n"
    "v_pk_mul_f32 v[232:233], v[232:233], v[156:157]\n"
    "v_pk_fma_f32 v[158:159], v[158:159], s[70:71], v[110:111]\n"
    "v_pk_fma_f32 v[230:231], v[230:231], v[158:159], v[232:233]\n"
    "v_pk_mul_f32 v[232:233], v[232:233], v[158:159]\n"
    "v_rcp_f32 v232, v232\n"
    "v_rcp_f32 v233, v233\n"
    "v_fma_f32 v230, v230, v232, v247\n"
    "v_fmac_f32 v230, v231, v233\n"
    "v_cvt_pk_bf16_f32 v230, v230, v230\n"
    "s_add_u32 s67, s67, 64\n"
    "s_waitcnt lgkmcnt(0)\n"
    "ds_write_b16 v245, v230\n"
    "ds_read_b64 v[228:229], v246\n"
    "s_cmp_le_u32 s34, s76\n"
    "s_cbranch_scc1 .Ldin_G1_wait\n"
    ".Ldin_G1_go:\n"
    "s_add_u32 s76, s76, s72\n"
    ".Ldin_G1_dma:\n"
    "buffer_load_dwordx4 v[236:237], s[40:43], 0 idxen offen offset:0 nt lds\n"
    "buffer_load_dwordx4 v[238:239], s[40:43], 0 idxen offen offset:1024 nt lds\n"
    ".Ldin_G1_nd:\n"
    "s_cmp_eq_u32 s67, s82\n"
    "s_cbranch_scc1 .Ldin_L2\n"
    "s_branch .Ldin_G2\n"
    ".Ldin_B1_wait:\n"
    "s_cmp_lt_i32 s59, s57\n"
    "s_cbranch_scc1 .Ldin_B1_w2\n"
    "s_sub_u32 s94, s67, s65\n"
    "s_cmp_gt_u32 s94, 256\n"
    "s_cbranch_scc1 .Ldin_B1_dma\n"
    "s_cmp_eq_u32 s94, 64\n"
    "s_waitcnt vmcnt(2)\n"
    "s_cbranch_scc1 .Ldin_B1_nd\n"
    "s_waitcnt vmcnt(0)\n"
    "s_branch .Ldin_B1_nd\n"
    ".Ldin_B1_slp:\n"
    "s_sleep 1\n"
    ".Ldin_B1_w2:\n"
    "s_memrealtime s[34:35]\n"
    "s_waitcnt lgkmcnt(0)\n"
    "s_sub_u32 s94, s34, s76\n"
    "s_cmp_lt_i32 s94, 0\n"
    "s_cbranch_scc1 .Ldin_B1_slp\n"
    "s_branch .Ldin_B1_go\n"
    ".Ldin_G2_wait:\n"
    "s_cmp_lt_i32 s59, s57\n"
    "s_cbranch_scc1 .Ldin_G2_w2\n"
    "s_sub_u32 s94, s67, s65\n"
    "s_cmp_gt_u32 s94, 256\n"
    "s_cbranch_scc1 .Ldin_G2_dma\n"
    "s_cmp_eq_u32 s94, 64\n"
    "s_waitcnt vmcnt(2)\n"
    "s_cbranch_scc1 .Ldin_G2_nd\n"
    "s_waitcnt vmcnt(0)\n"
    "s_branch .Ldin_G2_nd\n"
    ".Ldin_G2_slp:\n"
    "s_sleep 1\n"
    ".Ldin_G2_w2:\n"
    "s_memrealtime s[34:35]\n"
    "s_waitcnt lgkmcnt(0)\n"
    "s_sub_u32 s94, s34, s76\n"
    "s_cmp_lt_i32 s94, 0\n"
    "s_cbranch_scc1 .Ldin_G2_slp\n"
    "s_branch .Ldin_G2_go\n"
    ".Ldin_G3_wait:\n"
    "s_cmp_lt_i32 s59, s57\n"
    "s_cbranch_scc1 .Ldin_G3_w2\n"
    "s_sub_u32 s94, s67, s65\n"
    "s_cmp_gt_u32 s94, 256\n"
    "s_cbranch_scc1 .Ldin_G3_dma\n"
    "s_cmp_eq_u32 s94, 64\n"
    "s_waitcnt vmcnt(2)\n"
    "s_cbranch_scc1 .Ldin_G3_nd\n"
    "s_waitcnt vmcnt(0)\n"
    "s_branch .Ldin_G3_nd\n"
    ".Ldin_G3_slp:\n"
    "s_sleep 1\n"
    ".Ldin_G3_w2:\n"
    "s_memrealtime s[34:35]\n"
    "s_waitcnt lgkmcnt(0)\n"
    "s_sub_u32 s94, s34, s76\n"
    "s_cmp_lt_i32 s94, 0\n"
    "s_cbranch_scc1 .Ldin_G3_slp\n"
    "s_branch .Ldin_G3_go\n"
    ".Ldin_G0_wait:\n"
    "s_cmp_lt_i32 s59, s57\n"
    "s_cbranch_scc1 .Ldin_G0_w2\n"
    "s_sub_u32 s94, s67, s65\n"
    "s_cmp_gt_u32 s94, 256\n"
    "s_cbranch_scc1 .Ldin_G0_dma\n"
    "s_cmp_eq_u32 s94, 64\n"
    "s_waitcnt vmcnt(2)\n"
    "s_cbranch_scc1 .Ldin_G0_nd\n"
    "s_waitcnt vmcnt(0)\n"
    "s_branch .Ldin_G0_nd\n"
    ".Ldin_G0_slp:\n"
    "s_sleep 1\n"
    ".Ldin_G0_w2:\n"
    "s_memrealtime s[34:35]\n"
    "s_waitcnt lgkmcnt(0)\n"
    "s_sub_u32 s94, s34, s76\n"
    "s_cmp_lt_i32 s94, 0\n"
    "s_cbranch_scc1 .Ldin_G0_slp\n"
    "s_branch .Ldin_G0_go\n"
    ".Ldin_G1_wait:\n"
    "s_cmp_lt_i32 s59, s57\n"
    "s_cbranch_scc1 .Ldin_G1_w2\n"
    "s_sub_u32 s94, s67, s65\n"
    "s_cmp_gt_u32 s94, 256\n"
    "s_cbranch_scc1 .Ldin_G1_dma\n"
    "s_cmp_eq_u32 s94, 64\n"
    "s_waitcnt vmcnt(2)\n"
    "s_cbranch_scc1 .Ldin_G1_nd\n"
    "s_waitcnt vmcnt(0)\n"
    "s_branch .Ldin_G1_nd\n"
    ".Ldin_G1_slp:\n"
    "s_sleep 1\n"
    ".Ldin_G1_w2:\n"
    "s_memrealtime s[34:35]\n"
    "s_waitcnt lgkmcnt(0)\n"
    "s_sub_u32 s94, s34, s76\n"
    "s_cmp_lt_i32 s94, 0\n"
    "s_cbranch_scc1 .Ldin_G1_slp\n"
    "s_branch .Ldin_G1_go\n"
    ".Ldin_odd:\n"
    "s_cmp_lt_i32 s58, s57\n"
    "s_cbranch_scc1 .Ldin_A0s\n"
    "s_waitcnt vmcnt(0)\n"
    "s_branch .Ldin_exit\n"
    ".p2align 5\n"
    ".Ldin_y2:\n"
    "s_waitcnt vmcnt(8)\n"
    "v_lshlrev_b32 v56, 16, v8\n"
    "v_lshlrev_b32 v60, 16, v9\n"
    "v_lshlrev_b32 v64, 16, v10\n"
    "v_lshlrev_b32_e64 v68, 16, v11\n"
    "v_bitop3_b32 v56, v56, s84, v7 bitop3:0x6c\n"
    "v_bitop3_b32 v58, v8, s84, v7 bitop3:0x6c\n"
    "v_bitop3_b32 v60, v60, s84, v7 bitop3:0x6c\n"
    "v_bitop3_b32 v62, v9, s84, v7 bitop3:0x6c\n"
    "v_bitop3_b32 v64, v64, s84, v7 bitop3:0x6c\n"
    "v_bitop3_b32 v66, v10, s84, v7 bitop3:0x6c\n"
    "v_bitop3_b32 v68, v68, s84, v7 bitop3:0x6c\n"
    "v_bitop3_b32 v70, v11, s84, v7 bitop3:0x6c\n"
    "s_waitcnt vmcnt(6)\n"
    "buffer_load_dwordx4 v[32:35], v27, s[44:47], s39 offen nt\n"
    "s_add_u32 s39, s39, s17\n"
    "v_lshlrev_b32 v112, 16, v128\n"
    "v_and_b32 v114, s37, v128\n"
    "v_lshlrev_b32 v116, 16, v129\n"
    "v_and_b32 v118, s37, v129\n"
    "v_lshlrev_b32 v120, 16, v130\n"
    "v_and_b32 v122, s37, v130\n"
    "v_lshlrev_b32 v124, 16, v131\n"
    "v_and_b32 v126, s37, v131\n"
    "buffer_load_dwordx4 v[36:39], v27, s[44:47], s39 offen nt\n"
    "s_add_u32 s39, s39, s17\n"
    "v_lshlrev_b32 v113, 16, v132\n"
    "v_and_b32 v115, s37, v132\n"
    "v_lshlrev_b32 v117, 16, v133\n"
    "v_and_b32 v119, s37, v133\n"
    "v_lshlrev_b32 v121, 16, v134\n"
    "v_and_b32 v123, s37, v134\n"
    "v_lshlrev_b32 v125, 16, v135\n"
    "v_and_b32_e64 v127, s37, v135\n"
    "v_pk_mul_f32 v[112:113], v[112:113], v[56:57] op_sel_hi:[1,0]\n"
    "v_pk_mul_f32 v[114:115], v[114:115], v[58:59] op_sel_hi:[1,0]\n"
    "v_pk_mul_f32 v[116:117], v[116:117], v[60:61] op_sel_hi:[1,0]\n"
    "v_pk_mul_f32 v[118:119], v[118:119], v[62:63] op_sel_hi:[1,0]\n"
    "v_pk_mul_f32 v[120:121], v[120:121], v[64:65] op_sel_hi:[1,0]\n"
    "v_pk_mul_f32 v[122:123], v[122:123], v[66:67] op_sel_hi:[1,0]\n"
    "v_pk_mul_f32 v[124:125], v[124:125], v[68:69] op_sel_hi:[1,0]\n"
    "v_pk_mul_f32 v[126:127], v[126:127], v[70:71] op_sel_hi:[1,0]\n"
    "s_waitcnt vmcnt(6)\n"
    "buffer_load_dwordx4 v[40:43], v27, s[44:47], s39 offen nt\n"
    "s_add_u32 s39, s39, s17\n"
    "v_lshlrev_b32 v168, 16, v136\n"
    "v_and_b32 v170, s37, v136\n"
    "v_lshlrev_b32 v172, 16, v137\n"
    "v_and_b32 v174, s37, v137\n"
    "v_lshlrev_b32 v176, 16, v138\n"
    "v_and_b32 v178, s37, v138\n"
    "v_lshlrev_b32 v180, 16, v139\n"
    "v_and_b32 v182, s37, v139\n"
    "buffer_load_dwordx4 v[44:47], v27, s[44:47], s39 offen nt\n"
    "s_add_u32 s39, s39, s17\n"
    "v_lshlrev_b32 v169, 16, v140\n"
    "v_and_b32 v171, s37, v140\n"
    "v_lshlrev_b32 v173, 16, v141\n"
    "v_and_b32 v175, s37, v141\n"
    "v_lshlrev_b32 v177, 16, v142\n"
    "v_and_b32 v179, s37, v142\n"
    "v_lshlrev_b32 v181, 16, v143\n"
    "v_and_b32_e64 v183, s37, v143\n"
    "v_pk_fma_f32 v[168:169], v[168:169], v[56:57], v[112:113] op_sel_hi:[1,0,1]\n"
    "v_pk_fma_f32 v[170:171], v[170:171], v[58:59], v[114:115] op_sel_hi:[1,0,1]\n"
    "ds_write2st64_b64 v12, v[168:169], v[170:171] offset0:0 offset1:8\n"
    "v_pk_fma_f32 v[172:173], v[172:173], v[60:61], v[116:117] op_sel_hi:[1,0,1]\n"
    "v_pk_fma_f32 v[174:175], v[174:175], v[62:63], v[118:119] op_sel_hi:[1,0,1]\n"
    "ds_write2st64_b64 v12, v[172:173], v[174:175] offset0:16 offset1:24\n"
    "v_pk_fma_f32 v[176:177], v[176:177], v[64:65], v[120:121] op_sel_hi:[1,0,1]\n"
    "v_pk_fma_f32 v[178:179], v[178:179], v[66:67], v[122:123] op_sel_hi:[1,0,1]\n"
    "ds_write2st64_b64 v13, v[176:177], v[178:179] offset0:0 offset1:8\n"
    "v_pk_fma_f32 v[180:181], v[180:181], v[68:69], v[124:125] op_sel_hi:[1,0,1]\n"
    "v_pk_fma_f32 v[182:183], v[182:183], v[70:71], v[126:127] op_sel_hi:[1,0,1]\n"
    "ds_write2st64_b64 v13, v[180:181], v[182:183] offset0:16 offset1:24\n"
    "s_waitcnt vmcnt(6)\n"
    "buffer_load_dwordx4 v[48:51], v27, s[44:47], s39 offen nt\n"
    "s_add_u32 s39, s39, s17\n"
    "v_lshlrev_b32 v80, 16, v144\n"
    "v_and_b32 v82, s37, v144\n"
    "v_lshlrev_b32 v84, 16, v145\n"
    "v_and_b32 v86, s37, v145\n"
    "v_lshlrev_b32 v88, 16, v146\n"
    "v_and_b32 v90, s37, v146\n"
    "v_lshlrev_b32 v92, 16, v147\n"
    "v_and_b32 v94, s37, v147\n"
    "buffer_load_dwordx4 v[52:55], v27, s[44:47], s39 offen nt\n"
    "v_lshlrev_b32 v81, 16, v148\n"
    "v_and_b32 v83, s37, v148\n"
    "v_lshlrev_b32 v85, 16, v149\n"
    "v_and_b32 v87, s37, v149\n"
    "v_lshlrev_b32 v89, 16, v150\n"
    "v_and_b32 v91, s37, v150\n"
    "v_lshlrev_b32 v93, 16, v151\n"
    "v_and_b32 v95, s37, v151\n"
    "v_pk_mul_f32 v[80:81], v[80:81], v[56:57] op_sel_hi:[1,0]\n"
    "v_cvt_pk_bf16_f32 v80, v80, v81\n"
    "v_pk_mul_f32 v[82:83], v[82:83], v[58:59] op_sel_hi:[1,0]\n"
    "v_cvt_pk_bf16_f32 v82, v82, v83\n"
    "ds_write2st64_b32 v14, v80, v82 offset0:0 offset1:8\n"
    "v_pk_mul_f32 v[84:85], v[84:85], v[60:61] op_sel_hi:[1,0]\n"
    "v_cvt_pk_bf16_f32 v84, v84, v85\n"
    "v_pk_mul_f32 v[86:87], v[86:87], v[62:63] op_sel_hi:[1,0]\n"
    "v_cvt_pk_bf16_f32 v86, v86, v87\n"
    "ds_write2st64_b32 v14, v84, v86 offset0:16 offset1:24\n"
    "v_pk_mul_f32 v[88:89], v[88:89], v[64:65] op_sel_hi:[1,0]\n"
    "v_cvt_pk_bf16_f32 v88, v88, v89\n"
    "v_pk_mul_f32 v[90:91], v[90:91], v[66:67] op_sel_hi:[1,0]\n"
    "v_cvt_pk_bf16_f32 v90, v90, v91\n"
    "ds_write2st64_b32 v15, v88, v90 offset0:0 offset1:8\n"
    "v_pk_mul_f32 v[92:93], v[92:93], v[68:69] op_sel_hi:[1,0]\n"
    "v_cvt_pk_bf16_f32 v92, v92, v93\n"
    "v_pk_mul_f32 v[94:95], v[94:95], v[70:71] op_sel_hi:[1,0]\n"
    "v_cvt_pk_bf16_f32 v94, v94, v95\n"
    "ds_write2st64_b32 v15, v92, v94 offset0:16 offset1:24\n"
    "s_waitcnt vmcnt(6)\n"
    "v_lshlrev_b32 v184, 16, v152\n"
    "v_and_b32 v186, s37, v152\n"
    "v_lshlrev_b32 v188, 16, v153\n"
    "v_and_b32 v190, s37, v153\n"
    "v_lshlrev_b32 v192, 16, v154\n"
    "v_and_b32 v194, s37, v154\n"
    "v_lshlrev_b32 v196, 16, v155\n"
    "v_and_b32 v198, s37, v155\n"
    "v_lshlrev_b32 v185, 16, v156\n"
    "v_and_b32 v187, s37, v156\n"
    "v_lshlrev_b32 v189, 16, v157\n"
    "v_and_b32 v191, s37, v157\n"
    "v_lshlrev_b32 v193, 16, v158\n"
    "v_and_b32 v195, s37, v158\n"
    "v_lshlrev_b32 v197, 16, v159\n"
    "v_and_b32_e64 v199, s37, v159\n"
    "s_waitcnt lgkmcnt(0)\n"
    "s_barrier\n"
    "ds_read_b128 v[128:131], v97 offset:0\n"
    "ds_read_b128 v[132:135], v97 offset:16\n"
    "ds_read_b128 v[28:31], v99 offset:35200\n"
    "v_pk_fma_f32 v[184:185], v[184:185], v[56:57], v[112:113] op_sel_hi:[1,0,1] neg_lo:[0,0,1] neg_hi:[0,0,1]\n"
    "v_cvt_pk_bf16_f32 v184, v184, v185\n"
    "v_pk_fma_f32 v[186:187], v[186:187], v[58:59], v[114:115] op_sel_hi:[1,0,1] neg_lo:[0,0,1] neg_hi:[0,0,1]\n"
    "v_cvt_pk_bf16_f32 v186, v186, v187\n"
    "ds_write2st64_b32 v14, v184, v186 offset0:32 offset1:40\n"
    "v_pk_fma_f32 v[188:189], v[188:189], v[60:61], v[116:117] op_sel_hi:[1,0,1] neg_lo:[0,0,1] neg_hi:[0,0,1]\n"
    "v_cvt_pk_bf16_f32 v188, v188, v189\n"
    "v_pk_fma_f32 v[190:191], v[190:191], v[62:63], v[118:119] op_sel_hi:[1,0,1] neg_lo:[0,0,1] neg_hi:[0,0,1]\n"
    "v_cvt_pk_bf16_f32 v190, v190, v191\n"
    "ds_write2st64_b32 v14, v188, v190 offset0:48 offset1:56\n"
    "v_pk_fma_f32 v[192:193], v[192:193], v[64:65], v[120:121] op_sel_hi:[1,0,1] neg_lo:[0,0,1] neg_hi:[0,0,1]\n"
    "v_cvt_pk_bf16_f32 v192, v192, v193\n"
    "v_pk_fma_f32 v[194:195], v[194:195], v[66:67], v[122:123] op_sel_hi:[1,0,1] neg_lo:[0,0,1] neg_hi:[0,0,1]\n"
    "v_cvt_pk_bf16_f32 v194, v194, v195\n"
    "ds_write2st64_b32 v15, v192, v194 offset0:32 offset1:40\n"
    "v_pk_fma_f32 v[196:197], v[196:197], v[68:69], v[124:125] op_sel_hi:[1,0,1] neg_lo:[0,0,1] neg_hi:[0,0,1]\n"
    "v_cvt_pk_bf16_f32 v196, v196, v197\n"
    "v_pk_fma_f32 v[198:199], v[198:199], v[70:71], v[126:127] op_sel_hi:[1,0,1] neg_lo:[0,0,1] neg_hi:[0,0,1]\n"
    "v_cvt_pk_bf16_f32 v198, v198, v199\n"
    "ds_write2st64_b32 v15, v196, v198 offset0:48 offset1:56\n"
    "s_mul_i32 s36, s61, s16\n"
    "s_add_u32 s94, s36, s16\n"
    "s_cmp_lt_u32 s61, s14\n"
    "s_cselect_b32 s46, s94, 0\n"
    "buffer_load_dwordx2 v[228:229], v105, s[44:47], s36 offen nt\n"
    "s_lshl_b32 s94, s61, 6\n"
    "s_add_u32 s94, s3, s94\n"
    "s_cmp_lt_u32 s61, s14\n"
    "s_cselect_b32 s58, s94, s57\n"
    "s_add_u32 s95, s61, s15\n"
    "s_add_u32 s36, s36, s17\n"
    "s_lshl_b32 s94, s95, 6\n"
    "s_add_u32 s94, s3, s94\n"
    "s_add_u32 s96, s36, s16\n"
    "s_add_u32 s94, s94, 8\n"
    "s_cmp_lt_u32 s95, s14\n"
    "s_cselect_b32 s46, s96, 0\n"
    "s_cselect_b32 s59, s94, s57\n"
    "buffer_load_short_d16_hi v224, v249, s[44:47], s36 offen offset:0 nt\n"
    "buffer_load_short_d16_hi v225, v249, s[44:47], s36 offen offset:128 nt\n"
    "buffer_load_short_d16_hi v226, v249, s[44:47], s36 offen offset:256 nt\n"
    "buffer_load_short_d16_hi v227, v249, s[44:47], s36 offen offset:384 nt\n"
    "s_mul_i32 s46, s14, s16\n"
    "s_waitcnt lgkmcnt(0)\n"
    "s_barrier\n"
    "ds_read_b128 v[8:11], v98 offset:0\n"
    "ds_read_b128 v[12:15], v98 offset:1024\n"
    "s_waitcnt lgkmcnt(1)\n"
    "v_mfma_f32_16x16x32_bf16 v[112:115], v[8:11], v[128:131], v[28:31]\n"
    "s_waitcnt lgkmcnt(0)\n"
    "v_mfma_f32_16x16x32_bf16 v[112:115], v[12:15], v[132:135], v[112:115]\n"
    "s_nop 7\n"
    "ds_write_b128 v100, v[112:115] offset:17408\n"
    "s_memtime s[88:89]\n"
    "s_memrealtime s[90:91]\n"
    "s_waitcnt lgkmcnt(0)\n"
    "s_barrier\n"
    "s_sub_u32 s88, s88, s0\n"
    "s_sub_u32 s90, s90, s76\n"
    "s_lshl3_add_u32 s88, s88, 5360\n"
    "s_mul_i32 s90, s90, 181\n"
    "s_cmp_lt_u32 s88, s90\n"
    "s_cselect_b32 s7, 1, 0\n"
    "s_cbranch_scc1 .Ldin_xo\n"
    "s_setprio 0\n"
    ".Ldin_xo:\n"
    "v_and_b32 v184, 24, v249\n"
    "v_add_u32 v184, v184, v252\n"
    "ds_read_b64 v[184:185], v184\n"
    "ds_read_b32 v247, v250 offset:35072\n"
    "ds_read_b32 v232, v250 offset:35076\n"
    "s_and_b32 s84, s12, 7\n"
    "v_sub_u32 v4, s16, v26\n"
    "s_lshl_b32 s84, -1, s84\n"
    "v_cmp_gt_i32 vcc, 16, v4\n"
    "s_not_b32 s84, s84\n"
    "v_mov_b32 v6, s84\n"
    "s_add_u32 s85, s66, 480\n"
    "v_cndmask_b32 v6, -1, v6, vcc\n"
    "v_and_b32 v4, 32, v251\n"
    "v_add3_u32 v7, v4, v251, s85\n"
    "v_mov_b32 v5, 0\n"
    "s_mov_b32 s4, 0x10001\n"
    "s_mov_b32 s5, 0xc080c080\n"
    "s_waitcnt lgkmcnt(0)\n"
    "v_readfirstlane_b32 s69, v232\n"
    "v_and_b32 v184, v254, v184\n"
    "v_and_b32_e64 v185, v255, v185\n"
    "s_waitcnt vmcnt(4)\n"
    "v_cmp_ne_u16_e64 s[84:85], 0, v228\n"
    "v_cmp_lt_u32_e64 s[86:87], s38, v228\n"
    "v_cmp_ne_u16_e64 s[88:89], 0, v229\n"
    "v_cmp_lt_u32_e64 s[90:91], s38, v229\n"
    "v_mbcnt_lo_u32_b32 v220, s84, v220\n"
    "s_bcnt1_i32_b64 s92, s[84:85]\n"
    "v_mbcnt_hi_u32_b32 v220, s85, v220\n"
    "s_lshl2_add_u32 s92, s92, s67\n"
    "v_mbcnt_lo_u32_b32 v220, s86, v220\n"
    "s_bcnt1_i32_b64 s93, s[86:87]\n"
    "v_mbcnt_hi_u32_b32 v220, s87, v220\n"
    "s_lshl2_add_u32 s93, s93, s92\n"
    "v_mbcnt_lo_u32_b32 v220, s88, v220\n"
    "s_bcnt1_i32_b64 s94, s[88:89]\n"
    "v_mbcnt_hi_u32_b32 v220, s89, v220\n"
    "s_lshl2_add_u32 s94, s94, s93\n"
    "v_mbcnt_lo_u32_b32 v220, s90, v220\n"
    "s_bcnt1_i32_b64 s95, s[90:91]\n"
    "v_mbcnt_hi_u32_b32 v220, s91, v220\n"
    "s_lshl2_add_u32 s95, s95, s94\n"
    "v_mov_b32_e64 v21, -1\n"
    "v_lshl_add_u32 v20, v249, 1, s95\n"
    "v_addc_co_u32_e64 v221, vcc, v220, 0, s[84:85]\n"
    "v_addc_co_u32_e64 v222, vcc, v221, 0, s[86:87]\n"
    "v_addc_co_u32_e64 v223, vcc, v222, 0, s[88:89]\n"
    "ds_write2_b32 v20, v21, v21 offset1:64\n"
    "v_lshlrev_b32 v220, 2, v220\n"
    "v_lshlrev_b32 v221, 2, v221\n"
    "v_lshlrev_b32 v222, 2, v222\n"
    "v_lshlrev_b32_e64 v223, 2, v223\n"
    "s_mov_b64 exec, s[84:85]\n"
    "ds_write_b32 v220, v216\n"
    "s_mov_b64 exec, s[86:87]\n"
    "ds_write_b32 v221, v217\n"
    "s_mov_b64 exec, s[88:89]\n"
    "ds_write_b32 v222, v218\n"
    "s_mov_b64 exec, s[90:91]\n"
    "ds_write_b32 v223, v219\n"
    "s_mov_b64 exec, -1\n"
    "ds_read_b32 v200, v253 offset:0\n"
    "ds_read_b32 v202, v253 offset:4\n"
    "ds_read_b32 v204, v253 offset:64\n"
    "ds_read_b32 v206, v253 offset:68\n"
    "ds_read_b32 v208, v253 offset:128\n"
    "ds_read_b32 v210, v253 offset:132\n"
    "ds_read_b32 v212, v253 offset:192\n"
    "ds_read_b32 v214, v253 offset:196\n"
    "s_add_u32 m0, s68, 0\n"
    "s_waitcnt lgkmcnt(6)\n"
    "buffer_load_dwordx4 v[200:201], s[40:43], 0 idxen offen offset:0 nt lds\n"
    "buffer_load_dwordx4 v[202:203], s[40:43], 0 idxen offen offset:1024 nt lds\n"
    "s_add_u32 s95, s95, 60\n"
    "s_add_u32 s94, s67, 384\n"
    "s_andn2_b32 s95, s95, 63\n"
    "s_max_u32 s80, s95, s94\n"
    "s_add_u32 m0, s68, 2048\n"
    "s_waitcnt lgkmcnt(0)\n"
    "buffer_load_dwordx4 v[204:205], s[40:43], 0 idxen offen offset:0 nt lds\n"
    "s_mov_b32 s94, 0x10001\n"
    "s_mov_b32 s95, 0xc080c080\n"
    "v_pk_min_u16 v22, v228, s94\n"
    "v_pk_min_u16 v23, v229, s94\n"
    "v_pk_mad_u16 v228, v22, s95, v228\n"
    "v_pk_mad_u16 v229, v23, s95, v229\n"
    "s_sub_u32 s87, s80, s81\n"
    "v_or_b32 v228, v228, v229\n"
    "s_lshr_b32 s87, s87, 6\n"
    "s_lshl2_add_u32 s86, s3, 68224\n"
    "v_mov_b32 v106, s86\n"
    "v_mov_b32 v108, s66\n"
    "v_mov_b32 v109, s58\n"
    "v_mov_b32 v110, s59\n"
    "s_waitcnt vmcnt(3)\n"
    "v_cmp_neq_f32_e64 s[20:21], 0, v224\n"
    "v_cmp_neq_f32_e64 s[22:23], 0, v225\n"
    "v_cmp_neq_f32_e64 s[24:25], 0, v226\n"
    "v_cmp_neq_f32_e64 s[26:27], 0, v227\n"
    "v_fma_f32 v224, v224, v224, -v224\n"
    "v_fma_f32 v225, v225, v225, -v225\n"
    "v_fma_f32 v226, v226, v226, -v226\n"
    "v_fma_f32 v227, v227, v227, -v227\n"
    "s_bcnt1_i32_b64 s88, s[20:21]\n"
    "s_bcnt1_i32_b64 s89, s[22:23]\n"
    "buffer_load_dwordx4 v[206:207], s[40:43], 0 idxen offen offset:1024 nt lds\n"
    "s_bcnt1_i32_b64 s90, s[24:25]\n"
    "s_bcnt1_i32_b64 s91, s[26:27]\n"
    "v_or3_b32 v228, v228, v224, v225\n"
    "s_add_u32 s88, s88, s89\n"
    "v_or3_b32 v228, v228, v226, v227\n"
    "s_add_u32 s90, s90, s91\n"
    "v_cmp_ne_u32_e64 s[84:85], 0, v228\n"
    "s_add_u32 s88, s88, s90\n"
    "s_pack_ll_b32_b16 s88, s87, s88\n"
    "s_mov_b64 exec, 1\n"
    "v_mov_b32_e64 v107, s88\n"
    "ds_write2_b32 v108, v109, v110 offset1:2\n"
    "s_cmp_lg_u64 s[84:85], 0\n"
    "ds_write_b32 v106, v107\n"
    "s_cselect_b32 s91, 2.0, 0\n"
    "s_mov_b64 exec, -1\n"
    "s_or_b32 s58, s58, s91\n"
    "s_or_b32 s59, s59, s91\n"
    "v_pk_min_u16 v0, v32, s4\n"
    "v_pk_min_u16 v1, v33, s4\n"
    "v_pk_min_u16 v2, v34, s4\n"
    "v_pk_min_u16 v3, v35, s4\n"
    "v_pk_mad_u16 v32, v0, s5, v32\n"
    "v_pk_mad_u16 v33, v1, s5, v33\n"
    "s_add_u32 m0, s68, 4096\n"
    "s_nop 0\n"
    "buffer_load_dwordx4 v[208:209], s[40:43], 0 idxen offen offset:0 nt lds\n"
    "v_pk_mad_u16 v34, v2, s5, v34\n"
    "v_pk_mad_u16 v35, v3, s5, v35\n"
    "v_lshl_or_b32 v0, v1, 2, v0\n"
    "v_lshl_or_b32 v0, v2, 4, v0\n"
    "v_lshl_or_b32 v0, v3, 6, v0\n"
    "v_or3_b32 v5, v5, v32, v33\n"
    "v_lshrrev_b32 v1, 15, v0\n"
    "v_or3_b32 v5, v5, v34, v35\n"
    "v_bitop3_b32 v16, v1, v6, v0 bitop3:0xc8\n"
    "ds_write_b8 v7, v16 offset:0\n"
    "v_pk_min_u16 v0, v36, s4\n"
    "v_pk_min_u16 v1, v37, s4\n"
    "v_pk_min_u16 v2, v38, s4\n"
    "v_pk_min_u16 v3, v39, s4\n"
    "v_pk_mad_u16 v36, v0, s5, v36\n"
    "v_pk_mad_u16 v37, v1, s5, v37\n"
    "v_pk_mad_u16 v38, v2, s5, v38\n"
    "v_pk_mad_u16 v39, v3, s5, v39\n"
    "v_lshl_or_b32 v0, v1, 2, v0\n"
    "v_lshl_or_b32 v0, v2, 4, v0\n"
    "v_lshl_or_b32 v0, v3, 6, v0\n"
    "v_or3_b32 v5, v5, v36, v37\n"
    "v_lshrrev_b32_e64 v1, 15, v0\n"
    "v_or3_b32 v5, v5, v38, v39\n"
    "v_bitop3_b32 v17, v1, v6, v0 bitop3:0xc8\n"
    "buffer_load_dwordx4 v[210:211], s[40:43], 0 idxen offen offset:1024 nt lds\n"
    "ds_write_b8 v7, v17 offset:512\n"
    "v_pk_min_u16 v0, v40, s4\n"
    "v_pk_min_u16 v1, v41, s4\n"
    "v_pk_min_u16 v2, v42, s4\n"
    "v_pk_min_u16 v3, v43, s4\n"
    "v_pk_mad_u16 v40, v0, s5, v40\n"
    "v_pk_mad_u16 v41, v1, s5, v41\n"
    "v_pk_mad_u16 v42, v2, s5, v42\n"
    "v_pk_mad_u16 v43, v3, s5, v43\n"
    "v_lshl_or_b32 v0, v1, 2, v0\n"
    "v_lshl_or_b32 v0, v2, 4, v0\n"
    "v_lshl_or_b32 v0, v3, 6, v0\n"
    "v_or3_b32 v5, v5, v40, v41\n"
    "v_lshrrev_b32_e64 v1, 15, v0\n"
    "v_or3_b32 v5, v5, v42, v43\n"
    "v_bitop3_b32 v18, v1, v6, v0 bitop3:0xc8\n"
    "ds_write_b8 v7, v18 offset:1024\n"
    "v_pk_min_u16 v0, v44, s4\n"
    "v_pk_min_u16 v1, v45, s4\n"
    "v_pk_min_u16 v2, v46, s4\n"
    "v_pk_min_u16 v3, v47, s4\n"
    "v_pk_mad_u16 v44, v0, s5, v44\n"
    "v_pk_mad_u16 v45, v1, s5, v45\n"
    "v_pk_mad_u16 v46, v2, s5, v46\n"
    "v_pk_mad_u16 v47, v3, s5, v47\n"
    "s_add_u32 m0, s68, 6144\n"
    "s_nop 0\n"
    "buffer_load_dwordx4 v[212:213], s[40:43], 0 idxen offen offset:0 nt lds\n"
    "v_lshl_or_b32 v0, v1, 2, v0\n"
    "v_lshl_or_b32 v0, v2, 4, v0\n"
    "v_lshl_or_b32 v0, v3, 6, v0\n"
    "v_or3_b32 v5, v5, v44, v45\n"
    "v_lshrrev_b32 v1, 15, v0\n"
    "v_or3_b32 v5, v5, v46, v47\n"
    "v_bitop3_b32 v19, v1, v6, v0 bitop3:0xc8\n"
    "ds_write_b8 v7, v19 offset:1536\n"
    "v_pk_min_u16 v0, v48, s4\n"
    "v_pk_min_u16 v1, v49, s4\n"
    "v_pk_min_u16 v2, v50, s4\n"
    "v_pk_min_u16 v3, v51, s4\n"
    "v_pk_mad_u16 v48, v0, s5, v48\n"
    "v_pk_mad_u16 v49, v1, s5, v49\n"
    "v_pk_mad_u16 v50, v2, s5, v50\n"
    "v_pk_mad_u16 v51, v3, s5, v51\n"
    "v_lshl_or_b32 v0, v1, 2, v0\n"
    "v_lshl_or_b32 v0, v2, 4, v0\n"
    "v_lshl_or_b32 v0, v3, 6, v0\n"
    "v_or3_b32 v5, v5, v48, v49\n"
    "v_lshrrev_b32_e64 v1, 15, v0\n"
    "v_or3_b32 v5, v5, v50, v51\n"
    "v_bitop3_b32 v16, v1, v6, v0 bitop3:0xc8\n"
    "ds_write_b8 v7, v16 offset:2048\n"
    "v_pk_min_u16 v0, v52, s4\n"
    "buffer_load_dwordx4 v[214:215], s[40:43], 0 idxen offen offset:1024 nt lds\n"
    "v_pk_min_u16 v1, v53, s4\n"
    "v_pk_min_u16 v2, v54, s4\n"
    "v_pk_min_u16 v3, v55, s4\n"
    "v_pk_mad_u16 v52, v0, s5, v52\n"
    "v_pk_mad_u16 v53, v1, s5, v53\n"
    "v_pk_mad_u16 v54, v2, s5, v54\n"
    "v_pk_mad_u16 v55, v3, s5, v55\n"
    "v_lshl_or_b32 v0, v1, 2, v0\n"
    "v_lshl_or_b32 v0, v2, 4, v0\n"
    "v_lshl_or_b32 v0, v3, 6, v0\n"
    "v_or3_b32 v5, v5, v52, v53\n"
    "v_lshrrev_b32_e64 v1, 15, v0\n"
    "v_or3_b32 v5, v5, v54, v55\n"
    "v_bitop3_b32 v17, v1, v6, v0 bitop3:0xc8\n"
    "ds_write_b8 v7, v17 offset:2560\n"
    "ds_read_b128 v[128:131], v97 offset:4352\n"
    "ds_read_b128 v[132:135], v97 offset:4368\n"
    "ds_read_b128 v[144:147], v97 offset:8704\n"
    "ds_read_b128 v[148:151], v97 offset:8720\n"
    "ds_read_b128 v[160:163], v97 offset:13056\n"
    "ds_read_b128 v[164:167], v97 offset:13072\n"
    "s_waitcnt lgkmcnt(4)\n"
    "v_mfma_f32_16x16x32_bf16 v[116:119], v[8:11], v[128:131], v[28:31]\n"
    "v_mfma_f32_16x16x32_bf16 v[116:119], v[12:15], v[132:135], v[116:119]\n"
    "s_waitcnt lgkmcnt(2)\n"
    "v_mfma_f32_16x16x32_bf16 v[120:123], v[8:11], v[144:147], v[28:31]\n"
    "v_mfma_f32_16x16x32_bf16 v[120:123], v[12:15], v[148:151], v[120:123]\n"
    "s_waitcnt lgkmcnt(0)\n"
    "v_mfma_f32_16x16x32_bf16 v[124:127], v[8:11], v[160:163], v[28:31]\n"
    "v_mfma_f32_16x16x32_bf16 v[124:127], v[12:15], v[164:167], v[124:127]\n"
    "s_nop 7\n"
    "ds_write_b128 v100, v[116:119] offset:21760\n"
    "ds_write_b128 v100, v[120:123] offset:26112\n"
    "ds_write_b128 v100, v[124:127] offset:30464\n"
    "v_cmp_ne_u32 vcc, 0, v5\n"
    "s_cmp_lg_u64 vcc, 0\n"
    "s_cselect_b32 s94, 0x101, 1\n"
    "s_mov_b64 exec, 1\n"
    "v_mov_b32 v10, s94\n"
    "ds_add_rtn_u32 v10, v250, v10 offset:35840\n"
    "s_mov_b64 exec, -1\n"
    "s_waitcnt lgkmcnt(0)\n"
    "v_readfirstlane_b32 s95, v10\n"
    "s_add_u32 s96, s95, s94\n"
    "s_bitcmp1_b32 s96, 2\n"
    "s_cbranch_scc1 .Ldin_sched\n"
    ".Ldin_ygate:\n"
    ".Ldin_yentry:\n"
    "s_mov_b64 vcc, -1\n"
    "s_branch .Ldin_entry\n"
    ".Ldin_swait:\n"
    "s_sleep 1\n"
    "ds_read_b32 v10, v250 offset:35840\n"
    "s_waitcnt lgkmcnt(0)\n"
    "v_readfirstlane_b32 s96, v10\n"
    ".Ldin_sched:\n"
    "s_setprio 1\n"
    "v_lshrrev_b32 v2, 3, v251\n"
    "v_and_b32 v3, 7, v251\n"
    "v_lshlrev_b32 v6, 3, v3\n"
    "v_add_u32 v7, 16, v251\n"
    "s_mov_b32 s84, 0x10b80\n"
    "v_lshl_add_u32 v12, v7, 6, s84\n"
    "ds_read_b128 v[32:35], v12\n"
    "ds_read_b128 v[36:39], v12 offset:16\n"
    "s_mov_b32 s85, 0x10a80\n"
    "v_lshl_add_u32 v4, v2, 2, s85\n"
    "v_lshl_add_u32 v5, v3, 2, s85\n"
    "ds_read_u16 v17, v4 offset:2\n"
    "ds_read_u16 v4, v4\n"
    "ds_read_u16 v16, v5 offset:2\n"
    "ds_read_u16 v5, v5\n"
    "s_sub_u32 s85, s61, s3\n"
    "s_lshl1_add_u32 s85, s15, s85\n"
    "v_mul_lo_u32 v8, v2, s15\n"
    "v_add3_u32 v8, v8, v3, s85\n"
    "s_cmp_ge_u32 s96, 256\n"
    "v_cmp_gt_u32_e64 s[86:87], s14, v8\n"
    "s_cselect_b32 s85, 2.0, 0\n"
    "v_lshlrev_b32 v8, 6, v8\n"
    "s_and_b32 s87, s87, 0xffff\n"
    "v_or3_b32 v8, v8, v7, s85\n"
    "v_mov_b32 v12, s57\n"
    "v_cndmask_b32_e64 v8, v12, v8, s[86:87]\n"
    "v_cmp_eq_u32 vcc, s3, v2\n"
    "v_lshrrev_b32 v14, 2, v2\n"
    "v_mul_u32_u24 v14, 2, v14\n"
    "v_addc_co_u32_e64 v14, s[88:89], v14, 0, vcc\n"
    "v_addc_co_u32_e64 v14, s[88:89], v14, 0, vcc\n"
    "v_cmp_eq_u32 vcc, s3, v3\n"
    "v_lshrrev_b32 v15, 2, v3\n"
    "v_mul_u32_u24 v15, 2, v15\n"
    "v_addc_co_u32_e64 v15, s[88:89], v15, 0, vcc\n"
    "v_addc_co_u32_e64 v15, s[88:89], v15, 0, vcc\n"
    "s_waitcnt lgkmcnt(4)\n"
    "v_bcnt_u32_b32 v9, v32, 15\n"
    "v_bcnt_u32_b32 v9, v33, v9\n"
    "v_bcnt_u32_b32 v9, v34, v9\n"
    "v_bcnt_u32_b32 v9, v35, v9\n"
    "v_bcnt_u32_b32 v9, v36, v9\n"
    "v_bcnt_u32_b32 v9, v37, v9\n"
    "v_bcnt_u32_b32 v9, v38, v9\n"
    "v_bcnt_u32_b32 v9, v39, v9\n"
    "v_lshrrev_b32 v9, 4, v9\n"
    "v_max_u32 v9, 6, v9\n"
    "v_lshlrev_b32 v9, 3, v9\n"
    "v_cndmask_b32_e64 v9, 0, v9, s[86:87]\n"
    "v_sub_u32 v10, v3, v9\n"
    "s_waitcnt lgkmcnt(0)\n"
    "v_add_u32 v16, 15, v16\n"
    "v_lshrrev_b32 v16, 4, v16\n"
    "v_max_u32 v16, 6, v16\n"
    "v_add_u32 v17, 15, v17\n"
    "v_lshrrev_b32 v17, 4, v17\n"
    "v_max_u32 v17, 6, v17\n"
    "v_add3_u32 v4, v4, v14, v17\n"
    "v_add3_u32 v5, v5, v15, v16\n"
    "v_mov_b32_dpp v12, v10 row_half_mirror row_mask:0xf bank_mask:0xf\n"
    "v_lshrrev_b32 v14, 2, v2\n"
    "v_mul_u32_u24 v14, 3, v14\n"
    "v_xor_b32 v14, v14, v2\n"
    "v_lshl_or_b32 v4, v4, 3, v14\n"
    "v_lshrrev_b32_e64 v14, 2, v3\n"
    "v_mul_u32_u24 v14, 3, v14\n"
    "v_xor_b32 v14, v14, v3\n"
    "v_lshl_or_b32 v5, v5, 3, v14\n"
    "v_sub_u32_dpp v32, v10, v10 quad_perm:[1,0,3,2] row_mask:0xf bank_mask:0xf\n"
    "v_sub_u32_dpp v33, v10, v10 quad_perm:[2,3,0,1] row_mask:0xf bank_mask:0xf\n"
    "v_sub_u32_dpp v34, v10, v10 quad_perm:[3,2,1,0] row_mask:0xf bank_mask:0xf\n"
    "v_sub_u32_dpp v35, v12, v10 quad_perm:[0,1,2,3] row_mask:0xf bank_mask:0xf\n"
    "v_sub_u32_dpp v36, v12, v10 quad_perm:[1,0,3,2] row_mask:0xf bank_mask:0xf\n"
    "v_sub_u32_dpp v37, v12, v10 quad_perm:[2,3,0,1] row_mask:0xf bank_mask:0xf\n"
    "v_sub_u32_dpp v38, v12, v10 quad_perm:[3,2,1,0] row_mask:0xf bank_mask:0xf\n"
    "v_lshrrev_b32 v32, 31, v32\n"
    "v_lshrrev_b32 v33, 31, v33\n"
    "v_lshrrev_b32 v34, 31, v34\n"
    "v_lshrrev_b32 v35, 31, v35\n"
    "v_lshrrev_b32 v36, 31, v36\n"
    "v_lshrrev_b32 v37, 31, v37\n"
    "v_lshrrev_b32 v38, 31, v38\n"
    "v_add3_u32 v11, v32, v33, v244\n"
    "v_add3_u32 v11, v34, v35, v11\n"
    "v_add3_u32 v11, v36, v37, v11\n"
    "v_add_u32 v11, v38, v11\n"
    "v_add_u32 v12, s65, v11\n"
    "v_lshl_add_u32 v14, v11, 3, s65\n"
    "ds_write_b8 v12, v9\n"
    "ds_write_b32 v14, v8 offset:64\n"
    "v_mov_b32 v12, s65\n"
    "ds_read_b128 v[40:43], v12 offset:0\n"
    "ds_read_b128 v[44:47], v12 offset:16\n"
    "ds_read_b128 v[48:51], v12 offset:32\n"
    "v_lshlrev_b32 v16, 3, v16\n"
    "v_sub_u32 v72, v5, v16\n"
    "v_cmp_lt_i32 vcc, v5, v4\n"
    "v_mov_b32_e64 v73, v5\n"
    "s_waitcnt lgkmcnt(0)\n"
    "v_lshrrev_b64 v[20:21], v6, vcc\n"
    "v_lshrrev_b64 v[18:19], v244, vcc\n"
    "v_and_b32 v20, 0xff, v20\n"
    "v_and_b32 v18, 0xff, v18\n"
    "v_bcnt_u32_b32 v20, v20, 0\n"
    "v_bcnt_u32_b32 v18, v18, 0\n"
    "v_lshl_add_u32 v20, v20, 3, s65\n"
    "v_lshlrev_b32_e64 v18, 3, v18\n"
    "ds_read_b32 v56, v20 offset:64\n"
    "v_lshrrev_b64 v[22:23], v20, v[40:41]\n"
    "v_lshrrev_b64 v[18:19], v18, v[40:41]\n"
    "v_and_b32 v22, 0xff, v22\n"
    "v_and_b32 v18, 0xff, v18\n"
    "v_add_u32 v5, v5, v22\n"
    "v_add_u32 v4, v4, v18\n"
    "v_cmp_lt_i32 vcc, v5, v4\n"
    "v_mov_b32_e64 v74, v5\n"
    "s_nop 0\n"
    "v_lshrrev_b64 v[20:21], v6, vcc\n"
    "v_lshrrev_b64 v[18:19], v244, vcc\n"
    "v_and_b32 v20, 0xff, v20\n"
    "v_and_b32 v18, 0xff, v18\n"
    "v_bcnt_u32_b32 v20, v20, 0\n"
    "v_bcnt_u32_b32 v18, v18, 0\n"
    "v_lshl_add_u32 v20, v20, 3, s65\n"
    "v_lshlrev_b32_e64 v18, 3, v18\n"
    "ds_read_b32 v58, v20 offset:128\n"
    "v_lshrrev_b64 v[22:23], v20, v[42:43]\n"
    "v_lshrrev_b64 v[18:19], v18, v[42:43]\n"
    "v_and_b32 v22, 0xff, v22\n"
    "v_and_b32 v18, 0xff, v18\n"
    "v_add_u32 v5, v5, v22\n"
    "v_add_u32 v4, v4, v18\n"
    "v_cmp_lt_i32 vcc, v5, v4\n"
    "v_mov_b32_e64 v75, v5\n"
    "s_nop 0\n"
    "v_lshrrev_b64 v[20:21], v6, vcc\n"
    "v_lshrrev_b64 v[18:19], v244, vcc\n"
    "v_and_b32 v20, 0xff, v20\n"
    "v_and_b32 v18, 0xff, v18\n"
    "v_bcnt_u32_b32 v20, v20, 0\n"
    "v_bcnt_u32_b32 v18, v18, 0\n"
    "v_lshl_add_u32 v20, v20, 3, s65\n"
    "v_lshlrev_b32_e64 v18, 3, v18\n"
    "ds_read_b32 v60, v20 offset:192\n"
    "v_lshrrev_b64 v[22:23], v20, v[44:45]\n"
    "v_lshrrev_b64 v[18:19], v18, v[44:45]\n"
    "v_and_b32 v22, 0xff, v22\n"
    "v_and_b32 v18, 0xff, v18\n"
    "v_add_u32 v5, v5, v22\n"
    "v_add_u32 v4, v4, v18\n"
    "v_cmp_lt_i32 vcc, v5, v4\n"
    "v_mov_b32_e64 v76, v5\n"
    "s_nop 0\n"
    "v_lshrrev_b64 v[20:21], v6, vcc\n"
    "v_lshrrev_b64 v[18:19], v244, vcc\n"
    "v_and_b32 v20, 0xff, v20\n"
    "v_and_b32 v18, 0xff, v18\n"
    "v_bcnt_u32_b32 v20, v20, 0\n"
    "v_bcnt_u32_b32 v18, v18, 0\n"
    "v_lshl_add_u32 v20, v20, 3, s65\n"
    "v_lshlrev_b32_e64 v18, 3, v18\n"
    "ds_read_b32 v62, v20 offset:256\n"
    "v_lshrrev_b64 v[22:23], v20, v[46:47]\n"
    "v_lshrrev_b64 v[18:19], v18, v[46:47]\n"
    "v_and_b32 v22, 0xff, v22\n"
    "v_and_b32 v18, 0xff, v18\n"
    "v_add_u32 v5, v5, v22\n"
    "v_add_u32 v4, v4, v18\n"
    "v_cmp_lt_i32 vcc, v5, v4\n"
    "v_mov_b32_e64 v77, v5\n"
    "s_nop 0\n"
    "v_lshrrev_b64 v[20:21], v6, vcc\n"
    "v_lshrrev_b64 v[18:19], v244, vcc\n"
    "v_and_b32 v20, 0xff, v20\n"
    "v_and_b32 v18, 0xff, v18\n"
    "v_bcnt_u32_b32 v20, v20, 0\n"
    "v_bcnt_u32_b32 v18, v18, 0\n"
    "v_lshl_add_u32 v20, v20, 3, s65\n"
    "v_lshlrev_b32_e64 v18, 3, v18\n"
    "ds_read_b32 v64, v20 offset:320\n"
    "v_lshrrev_b64 v[22:23], v20, v[48:49]\n"
    "v_lshrrev_b64 v[18:19], v18, v[48:49]\n"
    "v_and_b32 v22, 0xff, v22\n"
    "v_and_b32 v18, 0xff, v18\n"
    "v_add_u32 v5, v5, v22\n"
    "v_add_u32 v4, v4, v18\n"
    "v_cmp_lt_i32 vcc, v5, v4\n"
    "v_mov_b32_e64 v78, v5\n"
    "s_nop 0\n"
    "v_lshrrev_b64 v[20:21], v6, vcc\n"
    "v_and_b32 v20, 0xff, v20\n"
    "v_bcnt_u32_b32 v20, v20, 0\n"
    "v_lshl_add_u32 v20, v20, 3, s65\n"
    "ds_read_b32 v66, v20 offset:384\n"
    "v_lshrrev_b64 v[22:23], v20, v[50:51]\n"
    "v_and_b32 v22, 0xff, v22\n"
    "v_add_u32 v5, v5, v22\n"
    "s_add_u32 s85, s76, 280\n"
    "s_lshl_b32 s86, s18, 4\n"
    "v_add_u32_dpp v12, v5, v5 quad_perm:[1,0,3,2] row_mask:0xf bank_mask:0xf\n"
    "s_add_u32 s85, s85, s86\n"
    "s_nop 0\n"
    "v_add_u32_dpp v12, v12, v12 quad_perm:[2,3,0,1] row_mask:0xf bank_mask:0xf\n"
    "s_nop 1\n"
    "v_add_u32_dpp v12, v12, v12 row_half_mirror row_mask:0xf bank_mask:0xf\n"
    "v_add_u32 v12, 158, v12\n"
    "v_and_b32 v12, -256, v12\n"
    "v_lshrrev_b32 v12, 3, v12\n"
    "v_sub_u32 v12, v12, v5\n"
    "v_add_u32 v72, v72, v12\n"
    "v_mul_u32_u24 v72, s18, v72\n"
    "v_lshrrev_b32 v72, 3, v72\n"
    "s_add_u32 s85, s85, s19\n"
    "v_add_u32 v57, s85, v72\n"
    "v_add_u32 v73, v73, v12\n"
    "v_mul_u32_u24 v73, s18, v73\n"
    "v_lshrrev_b32 v73, 3, v73\n"
    "s_add_u32 s85, s85, s19\n"
    "v_add_u32 v59, s85, v73\n"
    "v_add_u32 v74, v74, v12\n"
    "v_mul_u32_u24 v74, s18, v74\n"
    "v_lshrrev_b32 v74, 3, v74\n"
    "s_add_u32 s85, s85, s19\n"
    "v_add_u32 v61, s85, v74\n"
    "v_add_u32 v75, v75, v12\n"
    "v_mul_u32_u24 v75, s18, v75\n"
    "v_lshrrev_b32 v75, 3, v75\n"
    "s_add_u32 s85, s85, s19\n"
    "v_add_u32 v63, s85, v75\n"
    "v_add_u32 v76, v76, v12\n"
    "v_mul_u32_u24 v76, s18, v76\n"
    "v_lshrrev_b32 v76, 3, v76\n"
    "s_add_u32 s85, s85, s19\n"
    "v_add_u32 v65, s85, v76\n"
    "v_add_u32 v77, v77, v12\n"
    "v_mul_u32_u24 v77, s18, v77\n"
    "v_lshrrev_b32 v77, 3, v77\n"
    "s_add_u32 s85, s85, s19\n"
    "v_add_u32 v67, s85, v77\n"
    "v_lshl_add_u32 v12, v3, 7, s84\n"
    "s_mov_b64 exec, 0xff\n"
    "s_waitcnt lgkmcnt(0)\n"
    "ds_write_b128 v12, v[56:59] offset:48\n"
    "ds_write_b128 v12, v[60:63] offset:64\n"
    "ds_write_b128 v12, v[64:67] offset:80\n"
    "s_mov_b64 exec, -1\n"
    "s_branch .Ldin_ygate\n"
    ".p2align 5\n"
    ".Ldin_stub_12:\n"
    "s_cbranch_execz .Ldin_t3_ret\n"
    "s_nop 0\n"
    ".Ldin_L2:\n"
    "s_setprio 0\n"
    "s_cmp_ge_i32 s59, s57\n"
    "s_cbranch_scc1 .Ldin_E0\n"
    "s_add_u32 s76, s76, s73\n"
    "v_mov_b32_e64 v253, s63\n"
    "ds_read_b64 v[232:233], v253\n"
    "v_mfma_f32_4x4x4_16b_bf16 v[144:147], v[184:185], v[32:33], v[64:67] cbsz:2 abid:0\n"
    "ds_read_b128 v[112:115], v252 offset:17408\n"
    "v_mfma_f32_4x4x4_16b_bf16 v[148:151], v[184:185], v[34:35], v[68:71] cbsz:2 abid:1\n"
    "ds_read_b128 v[116:119], v252 offset:17424\n"
    "v_mfma_f32_4x4x4_16b_bf16 v[152:155], v[184:185], v[36:37], v[72:75] cbsz:2 abid:2\n"
    "ds_read_b128 v[120:123], v252 offset:17536\n"
    "ds_read_b128 v[124:127], v252 offset:17552\n"
    "v_cvt_pk_bf16_f32 v0, v144, v145\n"
    "v_cvt_pk_bf16_f32 v1, v146, v147\n"
    "v_mfma_f32_4x4x4_16b_bf16 v[156:159], v[184:185], v[38:39], v[76:79] cbsz:2 abid:3\n"
    "v_cvt_pk_bf16_f32 v2, v148, v149\n"
    "v_cvt_pk_bf16_f32 v3, v150, v151\n"
    "v_mfma_f32_4x4x4_16b_bf16 v[144:147], v[184:185], v[40:41], v[80:83] cbsz:2 abid:0\n"
    "v_cvt_pk_bf16_f32 v4, v152, v153\n"
    "v_cvt_pk_bf16_f32 v5, v154, v155\n"
    "v_mfma_f32_4x4x4_16b_bf16 v[148:151], v[184:185], v[42:43], v[84:87] cbsz:2 abid:1\n"
    "v_cvt_pk_bf16_f32 v6, v156, v157\n"
    "v_cvt_pk_bf16_f32 v7, v158, v159\n"
    "v_mfma_f32_4x4x4_16b_bf16 v[152:155], v[184:185], v[44:45], v[88:91] cbsz:2 abid:2\n"
    "v_cvt_pk_bf16_f32 v8, v144, v145\n"
    "v_cvt_pk_bf16_f32 v9, v146, v147\n"
    "v_mfma_f32_4x4x4_16b_bf16 v[156:159], v[184:185], v[46:47], v[92:95] cbsz:2 abid:3\n"
    "v_cvt_pk_bf16_f32 v10, v148, v149\n"
    "v_cvt_pk_bf16_f32 v11, v150, v151\n"
    "v_mfma_f32_4x4x4_16b_bf16 v[144:147], v[184:185], v[48:49], v[192:195] cbsz:2 abid:0\n"
    "v_cvt_pk_bf16_f32 v12, v152, v153\n"
    "v_cvt_pk_bf16_f32 v13, v154, v155\n"
    "v_mfma_f32_4x4x4_16b_bf16 v[148:151], v[184:185], v[50:51], v[196:199] cbsz:2 abid:1\n"
    "v_cvt_pk_bf16_f32 v14, v156, v157\n"
    "v_cvt_pk_bf16_f32 v15, v158, v159\n"
    "v_mfma_f32_4x4x4_16b_bf16 v[152:155], v[184:185], v[52:53], v[200:203] cbsz:2 abid:2\n"
    "s_waitcnt lgkmcnt(0)\n"
    "v_readfirstlane_b32 s60, v232\n"
    "v_cvt_pk_bf16_f32 v16, v144, v145\n"
    "v_cvt_pk_bf16_f32 v17, v146, v147\n"
    "v_mfma_f32_4x4x4_16b_bf16 v[156:159], v[184:185], v[54:55], v[204:207] cbsz:2 abid:3\n"
    "s_cmp_eq_u32 s60, -1\n"
    "s_cbranch_scc1 .Ldin_L2_poll\n"
    ".Ldin_L2_pr:\n"
    "v_readfirstlane_b32 s95, v233\n"
    "s_and_b32 s94, s60, 63\n"
    "s_lshl_b32 s94, s94, 6\n"
    "s_add_u32 s94, s94, 68480\n"
    "s_add_u32 s63, s63, 8\n"
    "v_lshl_add_u32 v252, v249, 1, s94\n"
    "ds_read_b32 v252, v252\n"
    "s_sub_u32 s95, s95, s76\n"
    "s_ashr_i32 s95, s95, 4\n"
    "s_min_i32 s95, s95, s74\n"
    "s_max_i32 s72, s95, s75\n"
    "v_cvt_pk_bf16_f32 v18, v148, v149\n"
    "v_cvt_pk_bf16_f32 v19, v150, v151\n"
    "v_mfma_f32_4x4x4_16b_bf16 v[144:147], v[184:185], v[56:57], v[208:211] cbsz:2 abid:0\n"
    "v_cvt_pk_bf16_f32 v20, v152, v153\n"
    "v_cvt_pk_bf16_f32 v21, v154, v155\n"
    "v_mfma_f32_4x4x4_16b_bf16 v[148:151], v[184:185], v[58:59], v[212:215] cbsz:2 abid:1\n"
    "v_cvt_pk_bf16_f32 v22, v156, v157\n"
    "v_cvt_pk_bf16_f32 v23, v158, v159\n"
    "v_mfma_f32_4x4x4_16b_bf16 v[152:155], v[184:185], v[60:61], v[216:219] cbsz:2 abid:2\n"
    "v_cvt_pk_bf16_f32 v24, v144, v145\n"
    "v_cvt_pk_bf16_f32 v25, v146, v147\n"
    "v_mfma_f32_4x4x4_16b_bf16 v[156:159], v[184:185], v[62:63], v[220:223] cbsz:2 abid:3\n"
    "v_cvt_pk_bf16_f32 v26, v148, v149\n"
    "v_cvt_pk_bf16_f32 v27, v150, v151\n"
    "v_cvt_pk_bf16_f32 v28, v152, v153\n"
    "v_cvt_pk_bf16_f32 v29, v154, v155\n"
    "v_exp_f32 v136, v136 clamp\n"
    "v_cvt_pk_bf16_f32 v30, v156, v157\n"
    "v_cvt_pk_bf16_f32 v31, v158, v159\n"
    "v_mfma_f32_16x16x32_bf16 v[144:147], v[0:3], v[160:163], v[112:115]\n"
    "v_exp_f32 v128, v128 clamp\n"
    "v_mfma_f32_16x16x32_bf16 v[144:147], v[4:7], v[164:167], v[144:147]\n"
    "v_exp_f32 v129, v129 clamp\n"
    "v_mfma_f32_16x16x32_bf16 v[148:151], v[8:11], v[160:163], v[116:119]\n"
    "v_exp_f32 v130, v130 clamp\n"
    "v_mfma_f32_16x16x32_bf16 v[148:151], v[12:15], v[164:167], v[148:151]\n"
    "v_exp_f32 v131, v131 clamp\n"
    "v_mfma_f32_16x16x32_bf16 v[152:155], v[16:19], v[160:163], v[120:123]\n"
    "v_exp_f32 v132, v132 clamp\n"
    "v_mfma_f32_16x16x32_bf16 v[152:155], v[20:23], v[164:167], v[152:155]\n"
    "v_exp_f32 v133, v133 clamp\n"
    "v_mfma_f32_16x16x32_bf16 v[156:159], v[24:27], v[160:163], v[124:127]\n"
    "v_exp_f32 v134, v134 clamp\n"
    "v_mfma_f32_16x16x32_bf16 v[156:159], v[28:31], v[164:167], v[156:159]\n"
    "v_exp_f32 v135, v135 clamp\n"
    "s_add_u32 m0, s68, 6144\n"
    "v_exp_f32 v137, v137 clamp\n"
    "v_add_u32 v253, s67, v244\n"
    "v_mfma_f32_4x4x4_16b_bf16 v[224:227], v[228:229], v[176:177], v[224:227] cbsz:4 abid:0\n"
    "v_exp_f32 v138, v138 clamp\n"
    "v_exp_f32 v139, v139 clamp\n"
    "v_mfma_f32_4x4x4_16b_bf16 v[224:227], v[228:229], v[178:179], v[224:227] cbsz:4 abid:1\n"
    "v_exp_f32 v140, v140 clamp\n"
    "v_exp_f32 v141, v141 clamp\n"
    "v_mfma_f32_4x4x4_16b_bf16 v[224:227], v[228:229], v[180:181], v[224:227] cbsz:4 abid:2\n"
    "v_exp_f32 v142, v142 clamp\n"
    "v_exp_f32 v143, v143 clamp\n"
    "v_mfma_f32_4x4x4_16b_bf16 v[224:227], v[228:229], v[182:183], v[224:227] cbsz:4 abid:3\n"
    "s_waitcnt vmcnt(4)\n"
    "ds_read_b64_tr_b16 v[176:177], v240 offset:6144\n"
    "ds_read_b64_tr_b16 v[178:179], v241 offset:6400\n"
    "ds_read_b64_tr_b16 v[180:181], v242 offset:6656\n"
    "ds_read_b64_tr_b16 v[182:183], v243 offset:6912\n"
    "ds_read_b32 v236, v253 offset:0\n"
    "ds_read_b32 v238, v253 offset:4\n"
    "ds_read_b128 v[160:163], v234 offset:0\n"
    "ds_read_b128 v[164:167], v235 offset:0\n"
    "s_waitcnt lgkmcnt(8)\n"
    "s_nop 0\n"
    "v_readlane_b32 s20, v252, 0\n"
    "v_readlane_b32 s21, v252, 1\n"
    "v_readlane_b32 s22, v252, 2\n"
    "v_readlane_b32 s23, v252, 3\n"
    "v_readlane_b32 s24, v252, 4\n"
    "v_readlane_b32 s25, v252, 5\n"
    "v_readlane_b32 s26, v252, 6\n"
    "v_readlane_b32 s27, v252, 7\n"
    "v_pk_fma_f32 v[128:129], v[128:129], s[70:71], v[96:97]\n"
    "v_pk_fma_f32 v[130:131], v[130:131], s[70:71], v[98:99]\n"
    "v_pk_add_f32 v[230:231], v[128:129], v[130:131]\n"
    "v_pk_mul_f32 v[232:233], v[128:129], v[130:131]\n"
    "v_pk_fma_f32 v[132:133], v[132:133], s[70:71], v[100:101]\n"
    "v_pk_fma_f32 v[230:231], v[230:231], v[132:133], v[232:233]\n"
    "v_pk_mul_f32 v[232:233], v[232:233], v[132:133]\n"
    "v_pk_fma_f32 v[134:135], v[134:135], s[70:71], v[102:103]\n"
    "v_pk_fma_f32 v[230:231], v[230:231], v[134:135], v[232:233]\n"
    "v_pk_mul_f32 v[232:233], v[232:233], v[134:135]\n"
    "v_pk_fma_f32 v[136:137], v[136:137], s[70:71], v[104:105]\n"
    "v_pk_fma_f32 v[230:231], v[230:231], v[136:137], v[232:233]\n"
    "v_pk_mul_f32 v[232:233], v[232:233], v[136:137]\n"
    "v_pk_fma_f32 v[138:139], v[138:139], s[70:71], v[106:107]\n"
    "v_pk_fma_f32 v[230:231], v[230:231], v[138:139], v[232:233]\n"
    "v_pk_mul_f32 v[232:233], v[232:233], v[138:139]\n"
    "v_pk_fma_f32 v[140:141], v[140:141], s[70:71], v[108:109]\n"
    "v_pk_fma_f32 v[230:231], v[230:231], v[140:141], v[232:233]\n"
    "v_pk_mul_f32 v[232:233], v[232:233], v[140:141]\n"
    "v_pk_fma_f32 v[142:143], v[142:143], s[70:71], v[110:111]\n"
    "v_pk_fma_f32 v[230:231], v[230:231], v[142:143], v[232:233]\n"
    "v_pk_mul_f32 v[232:233], v[232:233], v[142:143]\n"
    "v_rcp_f32 v232, v232\n"
    "v_rcp_f32 v233, v233\n"
    "v_fma_f32 v230, v230, v232, v247\n"
    "v_fmac_f32 v230, v231, v233\n"
    "v_cvt_pk_bf16_f32 v230, v230, v230\n"
    "s_add_u32 s67, s67, 64\n"
    "s_waitcnt lgkmcnt(0)\n"
    "ds_write_b16 v245, v230\n"
    "ds_read_b64 v[228:229], v246\n"
    "s_lshl1_add_u32 s76, s72, s76\n"
    "buffer_load_dwordx4 v[236:237], s[40:43], 0 idxen offen offset:0 nt lds\n"
    "buffer_load_dwordx4 v[238:239], s[40:43], 0 idxen offen offset:1024 nt lds\n"
    "s_and_b32 s62, s58, 63\n"
    "s_mul_i32 s62, s62, 272\n"
    "s_mov_b32 s58, s59\n"
    "s_mov_b32 s59, s60\n"
    "s_mov_b32 s81, s65\n"
    "s_xor_b32 s65, s65, s64\n"
    "s_mov_b32 s80, s83\n"
    "s_add_u32 s82, s65, 256\n"
    "s_cmp_lt_i32 s59, s57\n"
    "s_cselect_b32 s76, s76, -1\n"
    "s_bitcmp1_b32 s58, 30\n"
    "s_cbranch_scc1 .Ldin_A3s\n"
    ".Ldin_A3:\n"
    "v_mfma_f32_16x16x32_bf16 v[128:131], v[0:3], v[160:163], v[112:115]\n"
    "v_exp_f32 v144, v144 clamp\n"
    "v_mfma_f32_16x16x32_bf16 v[128:131], v[4:7], v[164:167], v[128:131]\n"
    "v_exp_f32 v145, v145 clamp\n"
    "v_mfma_f32_16x16x32_bf16 v[132:135], v[8:11], v[160:163], v[116:119]\n"
    "v_exp_f32 v146, v146 clamp\n"
    "v_mfma_f32_16x16x32_bf16 v[132:135], v[12:15], v[164:167], v[132:135]\n"
    "v_exp_f32 v147, v147 clamp\n"
    "v_mfma_f32_16x16x32_bf16 v[136:139], v[16:19], v[160:163], v[120:123]\n"
    "v_exp_f32 v148, v148 clamp\n"
    "v_mfma_f32_16x16x32_bf16 v[136:139], v[20:23], v[164:167], v[136:139]\n"
    "v_exp_f32 v149, v149 clamp\n"
    "v_mfma_f32_16x16x32_bf16 v[140:143], v[24:27], v[160:163], v[124:127]\n"
    "v_exp_f32 v150, v150 clamp\n"
    "v_mfma_f32_16x16x32_bf16 v[140:143], v[28:31], v[164:167], v[140:143]\n"
    "v_exp_f32 v151, v151 clamp\n"
    "v_exp_f32 v152, v152 clamp\n"
    "s_mov_b32 m0, s62\n"
    "v_exp_f32 v153, v153 clamp\n"
    "v_add_u32 v253, s67, v244\n"
    "s_waitcnt lgkmcnt(0)\n"
    "v_mfma_f32_4x4x4_16b_bf16 v[224:227], v[228:229], v[168:169], v[224:227] cbsz:4 abid:0\n"
    "v_exp_f32 v154, v154 clamp\n"
    "v_exp_f32 v155, v155 clamp\n"
    "v_mfma_f32_4x4x4_16b_bf16 v[224:227], v[228:229], v[170:171], v[224:227] cbsz:4 abid:1\n"
    "v_exp_f32 v156, v156 clamp\n"
    "v_exp_f32 v157, v157 clamp\n"
    "v_mfma_f32_4x4x4_16b_bf16 v[224:227], v[228:229], v[172:173], v[224:227] cbsz:4 abid:2\n"
    "v_exp_f32 v158, v158 clamp\n"
    "v_exp_f32 v159, v159 clamp\n"
    "v_mfma_f32_4x4x4_16b_bf16 v[224:227], v[228:229], v[174:175], v[224:227] cbsz:4 abid:3\n"
    "s_waitcnt vmcnt(4)\n"
    "ds_read_b64_tr_b16 v[168:169], v240 offset:0\n"
    "ds_read_b64_tr_b16 v[170:171], v241 offset:256\n"
    "ds_read_b64_tr_b16 v[172:173], v242 offset:512\n"
    "ds_read_b64_tr_b16 v[174:175], v243 offset:768\n"
    "ds_read_b32 v236, v253 offset:0\n"
    "ds_read_b32 v238, v253 offset:4\n"
    "ds_read_b128 v[160:163], v234 offset:2048\n"
    "ds_read_b128 v[164:167], v235 offset:2048\n"
    "s_cbranch_vccz .Ldin_A3_np\n"
    "s_setprio 1\n"
    ".Ldin_A3_np:\n"
    "v_pk_fma_f32 v[144:145], v[144:145], s[70:71], v[96:97]\n"
    "v_pk_fma_f32 v[146:147], v[146:147], s[70:71], v[98:99]\n"
    "v_pk_add_f32 v[190:191], v[224:225], v[226:227]\n"
    "v_add_f32 v190, v190, v191\n"
    "ds_write_addtid_b32 v190 offset:32768\n"
    "s_add_u32 m0, s68, 0\n"
    "s_bfe_u32 s94, s59, 0x180006\n"
    "s_mul_i32 s86, s94, s56\n"
    "v_mbcnt_lo_u32_b32 v184, s20, 0\n"
    "v_mbcnt_lo_u32_b32 v185, s22, 0\n"
    "v_mbcnt_lo_u32_b32 v186, s24, 0\n"
    "v_mbcnt_lo_u32_b32 v187, s26, 0\n"
    "s_bcnt1_i32_b64 s90, s[20:21]\n"
    "s_bcnt1_i32_b64 s91, s[22:23]\n"
    "s_bcnt1_i32_b64 s92, s[24:25]\n"
    "s_bcnt1_i32_b64 s93, s[26:27]\n"
    "v_mbcnt_hi_u32_b32 v184, s21, v184\n"
    "v_mbcnt_hi_u32_b32 v185, s23, v185\n"
    "v_mbcnt_hi_u32_b32 v186, s25, v186\n"
    "v_mbcnt_hi_u32_b32 v187, s27, v187\n"
    "s_lshl2_add_u32 s90, s90, s65\n"
    "s_lshl2_add_u32 s91, s91, s90\n"
    "s_lshl2_add_u32 s92, s92, s91\n"
    "s_lshl2_add_u32 s93, s93, s92\n"
    "v_add_u32 v188, s86, v251\n"
    "v_pk_add_f32 v[230:231], v[144:145], v[146:147]\n"
    "v_pk_mul_f32 v[232:233], v[144:145], v[146:147]\n"
    "v_pk_fma_f32 v[148:149], v[148:149], s[70:71], v[100:101]\n"
    "v_pk_fma_f32 v[230:231], v[230:231], v[148:149], v[232:233]\n"
    "v_pk_mul_f32 v[232:233], v[232:233], v[148:149]\n"
    "v_lshl_add_u32 v184, v184, 2, s65\n"
    "v_lshl_add_u32 v185, v185, 2, s90\n"
    "v_lshl_add_u32 v186, v186, 2, s91\n"
    "v_lshl_add_u32 v187, v187, 2, s92\n"
    "v_mov_b32 v191, -1\n"
    "v_lshl_add_u32 v190, v249, 1, s93\n"
    "ds_write2_b32 v190, v191, v191 offset1:64\n"
    "s_mov_b64 exec, s[20:21]\n"
    "ds_write_b32 v184, v188\n"
    "s_mov_b64 exec, s[22:23]\n"
    "v_add_u32 v189, 64, v188\n"
    "ds_write_b32 v185, v189\n"
    "s_mov_b64 exec, s[24:25]\n"
    "v_add_u32 v190, 128, v188\n"
    "ds_write_b32 v186, v190\n"
    "s_mov_b64 exec, s[26:27]\n"
    "v_add_u32 v189, 192, v188\n"
    "ds_write_b32 v187, v189\n"
    "s_mov_b64 exec, -1\n"
    "s_add_u32 s95, s93, 60\n"
    "s_add_u32 s94, s82, 128\n"
    "s_andn2_b32 s95, s95, 63\n"
    "s_max_u32 s83, s95, s94\n"
    "v_pk_fma_f32 v[150:151], v[150:151], s[70:71], v[102:103]\n"
    "v_pk_fma_f32 v[230:231], v[230:231], v[150:151], v[232:233]\n"
    "v_pk_mul_f32 v[232:233], v[232:233], v[150:151]\n"
    "v_pk_fma_f32 v[152:153], v[152:153], s[70:71], v[104:105]\n"
    "v_pk_fma_f32 v[230:231], v[230:231], v[152:153], v[232:233]\n"
    "v_pk_mul_f32 v[232:233], v[232:233], v[152:153]\n"
    "v_pk_fma_f32 v[154:155], v[154:155], s[70:71], v[106:107]\n"
    "v_pk_fma_f32 v[230:231], v[230:231], v[154:155], v[232:233]\n"
    "v_pk_mul_f32 v[232:233], v[232:233], v[154:155]\n"
    "v_pk_fma_f32 v[156:157], v[156:157], s[70:71], v[108:109]\n"
    "v_pk_fma_f32 v[230:231], v[230:231], v[156:157], v[232:233]\n"
    "v_pk_mul_f32 v[232:233], v[232:233], v[156:157]\n"
    "v_pk_fma_f32 v[158:159], v[158:159], s[70:71], v[110:111]\n"
    "v_pk_fma_f32 v[230:231], v[230:231], v[158:159], v[232:233]\n"
    "v_pk_mul_f32 v[232:233], v[232:233], v[158:159]\n"
    "v_rcp_f32 v232, v232\n"
    "v_rcp_f32 v233, v233\n"
    "v_fma_f32 v230, v230, v232, v247\n"
    "v_fmac_f32 v230, v231, v233\n"
    "v_cvt_pk_bf16_f32 v230, v230, v230\n"
    "s_add_u32 s67, s67, 64\n"
    "s_waitcnt lgkmcnt(0)\n"
    "ds_write_b16 v245, v230\n"
    "ds_read_b64 v[228:229], v246\n"
    "buffer_load_dwordx4 v[236:237], s[40:43], 0 idxen offen offset:0 nt lds\n"
    "buffer_load_dwordx4 v[238:239], s[40:43], 0 idxen offen offset:1024 nt lds\n"
    ".Ldin_B0:\n"
    "v_mfma_f32_16x16x32_bf16 v[144:147], v[0:3], v[160:163], v[112:115]\n"
    "v_exp_f32 v128, v128 clamp\n"
    "v_mfma_f32_16x16x32_bf16 v[144:147], v[4:7], v[164:167], v[144:147]\n"
    "v_exp_f32 v129, v129 clamp\n"
    "v_mfma_f32_16x16x32_bf16 v[148:151], v[8:11], v[160:163], v[116:119]\n"
    "v_exp_f32 v130, v130 clamp\n"
    "v_mfma_f32_16x16x32_bf16 v[148:151], v[12:15], v[164:167], v[148:151]\n"
    "v_exp_f32 v131, v131 clamp\n"
    "v_mfma_f32_16x16x32_bf16 v[152:155], v[16:19], v[160:163], v[120:123]\n"
    "v_exp_f32 v132, v132 clamp\n"
    "v_mfma_f32_16x16x32_bf16 v[152:155], v[20:23], v[164:167], v[152:155]\n"
    "v_exp_f32 v133, v133 clamp\n"
    "v_mfma_f32_16x16x32_bf16 v[156:159], v[24:27], v[160:163], v[124:127]\n"
    "v_exp_f32 v134, v134 clamp\n"
    "v_mfma_f32_16x16x32_bf16 v[156:159], v[28:31], v[164:167], v[156:159]\n"
    "v_exp_f32 v135, v135 clamp\n"
    "v_exp_f32 v136, v136 clamp\n"
    "s_cmp_eq_u32 s67, s80\n"
    "s_cselect_b32 s67, s65, s67\n"
    "s_and_b32 s94, s59, 63\n"
    "s_mul_i32 s94, s94, 272\n"
    "v_add_u32 v252, s94, v250\n"
    "s_add_u32 m0, s68, 2048\n"
    "v_exp_f32 v137, v137 clamp\n"
    "v_add_u32 v253, s67, v244\n"
    "s_waitcnt lgkmcnt(0)\n"
    "v_mfma_f32_4x4x4_16b_bf16 v[224:227], v[228:229], v[176:177], 0 cbsz:4 abid:0\n"
    "v_exp_f32 v138, v138 clamp\n"
    "v_exp_f32 v139, v139 clamp\n"
    "v_mfma_f32_4x4x4_16b_bf16 v[224:227], v[228:229], v[178:179], v[224:227] cbsz:4 abid:1\n"
    "v_exp_f32 v140, v140 clamp\n"
    "v_exp_f32 v141, v141 clamp\n"
    "v_mfma_f32_4x4x4_16b_bf16 v[224:227], v[228:229], v[180:181], v[224:227] cbsz:4 abid:2\n"
    "v_exp_f32 v142, v142 clamp\n"
    "v_exp_f32 v143, v143 clamp\n"
    "v_mfma_f32_4x4x4_16b_bf16 v[224:227], v[228:229], v[182:183], v[224:227] cbsz:4 abid:3\n"
    "s_waitcnt vmcnt(4)\n"
    "s_memrealtime s[34:35]\n"
    "ds_read_b64_tr_b16 v[176:177], v240 offset:2048\n"
    "ds_read_b64_tr_b16 v[178:179], v241 offset:2304\n"
    "ds_read_b64_tr_b16 v[180:181], v242 offset:2560\n"
    "ds_read_b64_tr_b16 v[182:183], v243 offset:2816\n"
    "ds_read_b32 v236, v253 offset:0\n"
    "ds_read_b32 v238, v253 offset:4\n"
    "ds_read_b128 v[160:163], v234 offset:4096\n"
    "ds_read_b128 v[164:167], v235 offset:4096\n"
    "v_add_u32_e64 v184, v248, v252\n"
    "ds_read_b64 v[184:185], v184\n"
    "v_pk_fma_f32 v[128:129], v[128:129], s[70:71], v[96:97]\n"
    "v_pk_fma_f32 v[130:131], v[130:131], s[70:71], v[98:99]\n"
    "v_pk_add_f32 v[230:231], v[128:129], v[130:131]\n"
    "v_pk_mul_f32 v[232:233], v[128:129], v[130:131]\n"
    "v_pk_fma_f32 v[132:133], v[132:133], s[70:71], v[100:101]\n"
    "v_pk_fma_f32 v[230:231], v[230:231], v[132:133], v[232:233]\n"
    "v_pk_mul_f32 v[232:233], v[232:233], v[132:133]\n"
    "v_pk_fma_f32 v[134:135], v[134:135], s[70:71], v[102:103]\n"
    "v_pk_fma_f32 v[230:231], v[230:231], v[134:135], v[232:233]\n"
    "v_pk_mul_f32 v[232:233], v[232:233], v[134:135]\n"
    "v_pk_fma_f32 v[136:137], v[136:137], s[70:71], v[104:105]\n"
    "v_pk_fma_f32 v[230:231], v[230:231], v[136:137], v[232:233]\n"
    "v_pk_mul_f32 v[232:233], v[232:233], v[136:137]\n"
    "v_pk_fma_f32 v[138:139], v[138:139], s[70:71], v[106:107]\n"
    "v_pk_fma_f32 v[230:231], v[230:231], v[138:139], v[232:233]\n"
    "v_pk_mul_f32 v[232:233], v[232:233], v[138:139]\n"
    "v_pk_fma_f32 v[140:141], v[140:141], s[70:71], v[108:109]\n"
    "v_pk_fma_f32 v[230:231], v[230:231], v[140:141], v[232:233]\n"
    "v_pk_mul_f32 v[232:233], v[232:233], v[140:141]\n"
    "v_pk_fma_f32 v[142:143], v[142:143], s[70:71], v[110:111]\n"
    "v_pk_fma_f32 v[230:231], v[230:231], v[142:143], v[232:233]\n"
    "v_pk_mul_f32 v[232:233], v[232:233], v[142:143]\n"
    "v_rcp_f32 v232, v232\n"
    "v_rcp_f32 v233, v233\n"
    "v_fma_f32 v230, v230, v232, v247\n"
    "v_fmac_f32 v230, v231, v233\n"
    "v_cvt_pk_bf16_f32 v230, v230, v230\n"
    "s_add_u32 s67, s67, 64\n"
    "s_waitcnt lgkmcnt(0)\n"
    "ds_write_b16 v245, v230\n"
    "ds_read_b64 v[228:229], v246\n"
    "v_and_b32 v184, v254, v184\n"
    "v_and_b32 v185, v255, v185\n"
    "s_cmp_le_u32 s34, s76\n"
    "s_cbranch_scc1 .Ldin_B0_wait\n"
    ".Ldin_B0_go:\n"
    "s_sub_u32 s94, s34, 60\n"
    "s_max_u32 s76, s76, s94\n"
    "s_add_u32 s76, s76, s72\n"
    ".Ldin_B0_dma:\n"
    "buffer_load_dwordx4 v[236:237], s[40:43], 0 idxen offen offset:0 nt lds\n"
    "buffer_load_dwordx4 v[238:239], s[40:43], 0 idxen offen offset:1024 nt lds\n"
    ".Ldin_B0_nd:\n"
    "s_branch .Ldin_G1\n"
    ".Ldin_L2_poll:\n"
    "s_sleep 1\n"
    "ds_read_b64 v[232:233], v253\n"
    "s_waitcnt lgkmcnt(0)\n"
    "v_readfirstlane_b32 s60, v232\n"
    "s_cmp_eq_u32 s60, -1\n"
    "s_cbranch_scc1 .Ldin_L2_poll\n"
    "s_branch .Ldin_L2_pr\n"
    ".Ldin_B0_wait:\n"
    "s_cmp_lt_i32 s59, s57\n"
    "s_cbranch_scc1 .Ldin_B0_w2\n"
    "s_sub_u32 s94, s67, s65\n"
    "s_cmp_gt_u32 s94, 256\n"
    "s_cbranch_scc1 .Ldin_B0_dma\n"
    "s_cmp_eq_u32 s94, 64\n"
    "s_waitcnt vmcnt(2)\n"
    "s_cbranch_scc1 .Ldin_B0_nd\n"
    "s_waitcnt vmcnt(0)\n"
    "s_branch .Ldin_B0_nd\n"
    ".Ldin_B0_slp:\n"
    "s_sleep 1\n"
    ".Ldin_B0_w2:\n"
    "s_memrealtime s[34:35]\n"
    "s_waitcnt lgkmcnt(0)\n"
    "s_sub_u32 s94, s34, s76\n"
    "s_cmp_lt_i32 s94, 0\n"
    "s_cbranch_scc1 .Ldin_B0_slp\n"
    "s_branch .Ldin_B0_go\n"
    ".Ldin_stub_13:\n"
    "s_cbranch_execz .Ldin_t3_ret\n"
    "s_nop 0\n"
    ".p2align 5\n"
    ".Ldin_L1:\n"
    "s_setprio 0\n"
    "s_cmp_ge_i32 s59, s57\n"
    "s_cbranch_scc1 .Ldin_E1\n"
    "s_add_u32 s76, s76, s73\n"
    "v_mov_b32_e64 v253, s63\n"
    "ds_read_b64 v[232:233], v253\n"
    "v_mfma_f32_4x4x4_16b_bf16 v[128:131], v[184:185], v[32:33], v[64:67] cbsz:2 abid:0\n"
    "ds_read_b128 v[112:115], v252 offset:17408\n"
    "v_mfma_f32_4x4x4_16b_bf16 v[132:135], v[184:185], v[34:35], v[68:71] cbsz:2 abid:1\n"
    "ds_read_b128 v[116:119], v252 offset:17424\n"
    "v_mfma_f32_4x4x4_16b_bf16 v[136:139], v[184:185], v[36:37], v[72:75] cbsz:2 abid:2\n"
    "ds_read_b128 v[120:123], v252 offset:17536\n"
    "ds_read_b128 v[124:127], v252 offset:17552\n"
    "v_cvt_pk_bf16_f32 v0, v128, v129\n"
    "v_cvt_pk_bf16_f32 v1, v130, v131\n"
    "v_mfma_f32_4x4x4_16b_bf16 v[140:143], v[184:185], v[38:39], v[76:79] cbsz:2 abid:3\n"
    "v_cvt_pk_bf16_f32 v2, v132, v133\n"
    "v_cvt_pk_bf16_f32 v3, v134, v135\n"
    "v_mfma_f32_4x4x4_16b_bf16 v[128:131], v[184:185], v[40:41], v[80:83] cbsz:2 abid:0\n"
    "v_cvt_pk_bf16_f32 v4, v136, v137\n"
    "v_cvt_pk_bf16_f32 v5, v138, v139\n"
    "v_mfma_f32_4x4x4_16b_bf16 v[132:135], v[184:185], v[42:43], v[84:87] cbsz:2 abid:1\n"
    "v_cvt_pk_bf16_f32 v6, v140, v141\n"
    "v_cvt_pk_bf16_f32 v7, v142, v143\n"
    "v_mfma_f32_4x4x4_16b_bf16 v[136:139], v[184:185], v[44:45], v[88:91] cbsz:2 abid:2\n"
    "v_cvt_pk_bf16_f32 v8, v128, v129\n"
    "v_cvt_pk_bf16_f32 v9, v130, v131\n"
    "v_mfma_f32_4x4x4_16b_bf16 v[140:143], v[184:185], v[46:47], v[92:95] cbsz:2 abid:3\n"
    "v_cvt_pk_bf16_f32 v10, v132, v133\n"
    "v_cvt_pk_bf16_f32 v11, v134, v135\n"
    "v_mfma_f32_4x4x4_16b_bf16 v[128:131], v[184:185], v[48:49], v[192:195] cbsz:2 abid:0\n"
    "v_cvt_pk_bf16_f32 v12, v136, v137\n"
    "v_cvt_pk_bf16_f32 v13, v138, v139\n"
    "v_mfma_f32_4x4x4_16b_bf16 v[132:135], v[184:185], v[50:51], v[196:199] cbsz:2 abid:1\n"
    "v_cvt_pk_bf16_f32 v14, v140, v141\n"
    "v_cvt_pk_bf16_f32 v15, v142, v143\n"
    "v_mfma_f32_4x4x4_16b_bf16 v[136:139], v[184:185], v[52:53], v[200:203] cbsz:2 abid:2\n"
    "s_waitcnt lgkmcnt(0)\n"
    "v_readfirstlane_b32 s60, v232\n"
    "v_cvt_pk_bf16_f32 v16, v128, v129\n"
    "v_cvt_pk_bf16_f32 v17, v130, v131\n"
    "v_mfma_f32_4x4x4_16b_bf16 v[140:143], v[184:185], v[54:55], v[204:207] cbsz:2 abid:3\n"
    "s_cmp_eq_u32 s60, -1\n"
    "s_cbranch_scc1 .Ldin_L1_poll\n"
    ".Ldin_L1_pr:\n"
    "v_readfirstlane_b32 s95, v233\n"
    "s_and_b32 s94, s60, 63\n"
    "s_lshl_b32 s94, s94, 6\n"
    "s_add_u32 s94, s94, 68480\n"
    "s_add_u32 s63, s63, 8\n"
    "v_lshl_add_u32 v252, v249, 1, s94\n"
    "ds_read_b32 v252, v252\n"
    "s_sub_u32 s95, s95, s76\n"
    "s_ashr_i32 s95, s95, 4\n"
    "s_min_i32 s95, s95, s74\n"
    "s_max_i32 s72, s95, s75\n"
    "v_cvt_pk_bf16_f32 v18, v132, v133\n"
    "v_cvt_pk_bf16_f32 v19, v134, v135\n"
    "v_mfma_f32_4x4x4_16b_bf16 v[128:131], v[184:185], v[56:57], v[208:211] cbsz:2 abid:0\n"
    "v_cvt_pk_bf16_f32 v20, v136, v137\n"
    "v_cvt_pk_bf16_f32 v21, v138, v139\n"
    ".Ldin_stub_14:\n"
    "s_cbranch_execz .Ldin_t3_ret\n"
    "s_nop 0\n"
    "v_mfma_f32_4x4x4_16b_bf16 v[132:135], v[184:185], v[58:59], v[212:215] cbsz:2 abid:1\n"
    "v_cvt_pk_bf16_f32 v22, v140, v141\n"
    "v_cvt_pk_bf16_f32 v23, v142, v143\n"
    "v_mfma_f32_4x4x4_16b_bf16 v[136:139], v[184:185], v[60:61], v[216:219] cbsz:2 abid:2\n"
    "v_cvt_pk_bf16_f32 v24, v128, v129\n"
    "v_cvt_pk_bf16_f32 v25, v130, v131\n"
    "v_mfma_f32_4x4x4_16b_bf16 v[140:143], v[184:185], v[62:63], v[220:223] cbsz:2 abid:3\n"
    "v_cvt_pk_bf16_f32 v26, v132, v133\n"
    "v_cvt_pk_bf16_f32 v27, v134, v135\n"
    "v_cvt_pk_bf16_f32 v28, v136, v137\n"
    "v_cvt_pk_bf16_f32 v29, v138, v139\n"
    "v_exp_f32 v152, v152 clamp\n"
    "v_cvt_pk_bf16_f32 v30, v140, v141\n"
    "v_cvt_pk_bf16_f32 v31, v142, v143\n"
    "v_mfma_f32_16x16x32_bf16 v[128:131], v[0:3], v[160:163], v[112:115]\n"
    "v_exp_f32 v144, v144 clamp\n"
    "v_mfma_f32_16x16x32_bf16 v[128:131], v[4:7], v[164:167], v[128:131]\n"
    "v_exp_f32 v145, v145 clamp\n"
    "v_mfma_f32_16x16x32_bf16 v[132:135], v[8:11], v[160:163], v[116:119]\n"
    "v_exp_f32 v146, v146 clamp\n"
    "v_mfma_f32_16x16x32_bf16 v[132:135], v[12:15], v[164:167], v[132:135]\n"
    "v_exp_f32 v147, v147 clamp\n"
    "v_mfma_f32_16x16x32_bf16 v[136:139], v[16:19], v[160:163], v[120:123]\n"
    "v_exp_f32 v148, v148 clamp\n"
    "v_mfma_f32_16x16x32_bf16 v[136:139], v[20:23], v[164:167], v[136:139]\n"
    "v_exp_f32 v149, v149 clamp\n"
    "v_mfma_f32_16x16x32_bf16 v[140:143], v[24:27], v[160:163], v[124:127]\n"
    "v_exp_f32 v150, v150 clamp\n"
    "v_mfma_f32_16x16x32_bf16 v[140:143], v[28:31], v[164:167], v[140:143]\n"
    "v_exp_f32 v151, v151 clamp\n"
    "s_add_u32 m0, s68, 4096\n"
    "v_exp_f32 v153, v153 clamp\n"
    "v_add_u32 v253, s67, v244\n"
    "v_mfma_f32_4x4x4_16b_bf16 v[224:227], v[228:229], v[168:169], v[224:227] cbsz:4 abid:0\n"
    "v_exp_f32 v154, v154 clamp\n"
    "v_exp_f32 v155, v155 clamp\n"
    "v_mfma_f32_4x4x4_16b_bf16 v[224:227], v[228:229], v[170:171], v[224:227] cbsz:4 abid:1\n"
    "v_exp_f32 v156, v156 clamp\n"
    "v_exp_f32 v157, v157 clamp\n"
    "v_mfma_f32_4x4x4_16b_bf16 v[224:227], v[228:229], v[172:173], v[224:227] cbsz:4 abid:2\n"
    "v_exp_f32 v158, v158 clamp\n"
    "v_exp_f32 v159, v159 clamp\n"
    "v_mfma_f32_4x4x4_16b_bf16 v[224:227], v[228:229], v[174:175], v[224:227] cbsz:4 abid:3\n"
    "s_waitcnt vmcnt(4)\n"
    "ds_read_b64_tr_b16 v[168:169], v240 offset:4096\n"
    "ds_read_b64_tr_b16 v[170:171], v241 offset:4352\n"
    "ds_read_b64_tr_b16 v[172:173], v242 offset:4608\n"
    "ds_read_b64_tr_b16 v[174:175], v243 offset:4864\n"
    "ds_read_b32 v236, v253 offset:0\n"
    "ds_read_b32 v238, v253 offset:4\n"
    "ds_read_b128 v[160:163], v234 offset:6144\n"
    "ds_read_b128 v[164:167], v235 offset:6144\n"
    "s_waitcnt lgkmcnt(8)\n"
    "s_nop 0\n"
    "v_readlane_b32 s20, v252, 0\n"
    "v_readlane_b32 s21, v252, 1\n"
    "v_readlane_b32 s22, v252, 2\n"
    "v_readlane_b32 s23, v252, 3\n"
    "v_readlane_b32 s24, v252, 4\n"
    "v_readlane_b32 s25, v252, 5\n"
    "v_readlane_b32 s26, v252, 6\n"
    "v_readlane_b32 s27, v252, 7\n"
    "v_pk_fma_f32 v[144:145], v[144:145], s[70:71], v[96:97]\n"
    "v_pk_fma_f32 v[146:147], v[146:147], s[70:71], v[98:99]\n"
    "v_pk_add_f32 v[230:231], v[144:145], v[146:147]\n"
    "v_pk_mul_f32 v[232:233], v[144:145], v[146:147]\n"
    "v_pk_fma_f32 v[148:149], v[148:149], s[70:71], v[100:101]\n"
    "v_pk_fma_f32 v[230:231], v[230:231], v[148:149], v[232:233]\n"
    "v_pk_mul_f32 v[232:233], v[232:233], v[148:149]\n"
    "v_pk_fma_f32 v[150:151], v[150:151], s[70:71], v[102:103]\n"
    "v_pk_fma_f32 v[230:231], v[230:231], v[150:151], v[232:233]\n"
    "v_pk_mul_f32 v[232:233], v[232:233], v[150:151]\n"
    "v_pk_fma_f32 v[152:153], v[152:153], s[70:71], v[104:105]\n"
    "v_pk_fma_f32 v[230:231], v[230:231], v[152:153], v[232:233]\n"
    "v_pk_mul_f32 v[232:233], v[232:233], v[152:153]\n"
    "v_pk_fma_f32 v[154:155], v[154:155], s[70:71], v[106:107]\n"
    "v_pk_fma_f32 v[230:231], v[230:231], v[154:155], v[232:233]\n"
    "v_pk_mul_f32 v[232:233], v[232:233], v[154:155]\n"
    "v_pk_fma_f32 v[156:157], v[156:157], s[70:71], v[108:109]\n"
    "v_pk_fma_f32 v[230:231], v[230:231], v[156:157], v[232:233]\n"
    "v_pk_mul_f32 v[232:233], v[232:233], v[156:157]\n"
    "v_pk_fma_f32 v[158:159], v[158:159], s[70:71], v[110:111]\n"
    "v_pk_fma_f32 v[230:231], v[230:231], v[158:159], v[232:233]\n"
    "v_pk_mul_f32 v[232:233], v[232:233], v[158:159]\n"
    "v_rcp_f32 v232, v232\n"
    "v_rcp_f32 v233, v233\n"
    "v_fma_f32 v230, v230, v232, v247\n"
    "v_fmac_f32 v230, v231, v233\n"
    "v_cvt_pk_bf16_f32 v230, v230, v230\n"
    "s_add_u32 s67, s67, 64\n"
    "s_waitcnt lgkmcnt(0)\n"
    "ds_write_b16 v245, v230\n"
    "ds_read_b64 v[228:229], v246\n"
    "s_lshl1_add_u32 s76, s72, s76\n"
    "buffer_load_dwordx4 v[236:237], s[40:43], 0 idxen offen offset:0 nt lds\n"
    "buffer_load_dwordx4 v[238:239], s[40:43], 0 idxen offen offset:1024 nt lds\n"
    "s_and_b32 s62, s58, 63\n"
    "s_mul_i32 s62, s62, 272\n"
    "s_mov_b32 s58, s59\n"
    "s_mov_b32 s59, s60\n"
    "s_mov_b32 s81, s65\n"
    "s_xor_b32 s65, s65, s64\n"
    "s_mov_b32 s80, s83\n"
    "s_add_u32 s82, s65, 256\n"
    "s_cmp_lt_i32 s59, s57\n"
    "s_cselect_b32 s76, s76, -1\n"
    "s_bitcmp1_b32 s58, 30\n"
    "s_cbranch_scc1 .Ldin_A2s\n"
    ".Ldin_A2:\n"
    "v_mfma_f32_16x16x32_bf16 v[144:147], v[0:3], v[160:163], v[112:115]\n"
    "v_exp_f32 v128, v128 clamp\n"
    "v_mfma_f32_16x16x32_bf16 v[144:147], v[4:7], v[164:167], v[144:147]\n"
    "v_exp_f32 v129, v129 clamp\n"
    "v_mfma_f32_16x16x32_bf16 v[148:151], v[8:11], v[160:163], v[116:119]\n"
    "v_exp_f32 v130, v130 clamp\n"
    "v_mfma_f32_16x16x32_bf16 v[148:151], v[12:15], v[164:167], v[148:151]\n"
    "v_exp_f32 v131, v131 clamp\n"
    "v_mfma_f32_16x16x32_bf16 v[152:155], v[16:19], v[160:163], v[120:123]\n"
    "v_exp_f32 v132, v132 clamp\n"
    "v_mfma_f32_16x16x32_bf16 v[152:155], v[20:23], v[164:167], v[152:155]\n"
    "v_exp_f32 v133, v133 clamp\n"
    "v_mfma_f32_16x16x32_bf16 v[156:159], v[24:27], v[160:163], v[124:127]\n"
    "v_exp_f32 v134, v134 clamp\n"
    "v_mfma_f32_16x16x32_bf16 v[156:159], v[28:31], v[164:167], v[156:159]\n"
    "v_exp_f32 v135, v135 clamp\n"
    "v_exp_f32 v136, v136 clamp\n"
    "s_mov_b32 m0, s62\n"
    "v_exp_f32 v137, v137 clamp\n"
    "v_add_u32 v253, s67, v244\n"
    "s_waitcnt lgkmcnt(0)\n"
    "v_mfma_f32_4x4x4_16b_bf16 v[224:227], v[228:229], v[176:177], v[224:227] cbsz:4 abid:0\n"
    "v_exp_f32 v138, v138 clamp\n"
    "v_exp_f32 v139, v139 clamp\n"
    "v_mfma_f32_4x4x4_16b_bf16 v[224:227], v[228:229], v[178:179], v[224:227] cbsz:4 abid:1\n"
    "v_exp_f32 v140, v140 clamp\n"
    "v_exp_f32 v141, v141 clamp\n"
    "v_mfma_f32_4x4x4_16b_bf16 v[224:227], v[228:229], v[180:181], v[224:227] cbsz:4 abid:2\n"
    "v_exp_f32 v142, v142 clamp\n"
    "v_exp_f32 v143, v143 clamp\n"
    "v_mfma_f32_4x4x4_16b_bf16 v[224:227], v[228:229], v[182:183], v[224:227] cbsz:4 abid:3\n"
    "s_waitcnt vmcnt(4)\n"
    "ds_read_b64_tr_b16 v[176:177], v240 offset:6144\n"
    "ds_read_b64_tr_b16 v[178:179], v241 offset:6400\n"
    "ds_read_b64_tr_b16 v[180:181], v242 offset:6656\n"
    "ds_read_b64_tr_b16 v[182:183], v243 offset:6912\n"
    "ds_read_b32 v236, v253 offset:0\n"
    "ds_read_b32 v238, v253 offset:4\n"
    "ds_read_b128 v[160:163], v234 offset:0\n"
    "ds_read_b128 v[164:167], v235 offset:0\n"
    "s_cbranch_vccz .Ldin_A2_np\n"
    "s_setprio 1\n"
    ".Ldin_A2_np:\n"
    "v_pk_fma_f32 v[128:129], v[128:129], s[70:71], v[96:97]\n"
    "v_pk_fma_f32 v[130:131], v[130:131], s[70:71], v[98:99]\n"
    "v_pk_add_f32 v[190:191], v[224:225], v[226:227]\n"
    "v_add_f32 v190, v190, v191\n"
    "ds_write_addtid_b32 v190 offset:32768\n"
    "s_add_u32 m0, s68, 6144\n"
    "s_bfe_u32 s94, s59, 0x180006\n"
    "s_mul_i32 s86, s94, s56\n"
    "v_mbcnt_lo_u32_b32 v184, s20, 0\n"
    "v_mbcnt_lo_u32_b32 v185, s22, 0\n"
    "v_mbcnt_lo_u32_b32 v186, s24, 0\n"
    "v_mbcnt_lo_u32_b32 v187, s26, 0\n"
    "s_bcnt1_i32_b64 s90, s[20:21]\n"
    "s_bcnt1_i32_b64 s91, s[22:23]\n"
    "s_bcnt1_i32_b64 s92, s[24:25]\n"
    "s_bcnt1_i32_b64 s93, s[26:27]\n"
    "v_mbcnt_hi_u32_b32 v184, s21, v184\n"
    "v_mbcnt_hi_u32_b32 v185, s23, v185\n"
    "v_mbcnt_hi_u32_b32 v186, s25, v186\n"
    "v_mbcnt_hi_u32_b32 v187, s27, v187\n"
    "s_lshl2_add_u32 s90, s90, s65\n"
    "s_lshl2_add_u32 s91, s91, s90\n"
    "s_lshl2_add_u32 s92, s92, s91\n"
    "s_lshl2_add_u32 s93, s93, s92\n"
    "v_add_u32_e64 v188, s86, v251\n"
    "v_pk_add_f32 v[230:231], v[128:129], v[130:131]\n"
    "v_pk_mul_f32 v[232:233], v[128:129], v[130:131]\n"
    "v_pk_fma_f32 v[132:133], v[132:133], s[70:71], v[100:101]\n"
    "v_pk_fma_f32 v[230:231], v[230:231], v[132:133], v[232:233]\n"
    "v_pk_mul_f32 v[232:233], v[232:233], v[132:133]\n"
    "v_lshl_add_u32 v184, v184, 2, s65\n"
    "v_lshl_add_u32 v185, v185, 2, s90\n"
    "v_lshl_add_u32 v186, v186, 2, s91\n"
    "v_lshl_add_u32 v187, v187, 2, s92\n"
    "v_mov_b32 v191, -1\n"
    "v_lshl_add_u32 v190, v249, 1, s93\n"
    "ds_write2_b32 v190, v191, v191 offset1:64\n"
    "s_mov_b64 exec, s[20:21]\n"
    "ds_write_b32 v184, v188\n"
    "s_mov_b64 exec, s[22:23]\n"
    "v_add_u32 v189, 64, v188\n"
    "ds_write_b32 v185, v189\n"
    "s_mov_b64 exec, s[24:25]\n"
    "v_add_u32 v190, 128, v188\n"
    "ds_write_b32 v186, v190\n"
    "s_mov_b64 exec, s[26:27]\n"
    "v_add_u32 v189, 192, v188\n"
    "ds_write_b32 v187, v189\n"
    "s_mov_b64 exec, -1\n"
    "s_add_u32 s95, s93, 60\n"
    "s_add_u32 s94, s82, 128\n"
    "s_andn2_b32 s95, s95, 63\n"
    "s_max_u32 s83, s95, s94\n"
    "v_pk_fma_f32 v[134:135], v[134:135], s[70:71], v[102:103]\n"
    "v_pk_fma_f32 v[230:231], v[230:231], v[134:135], v[232:233]\n"
    "v_pk_mul_f32 v[232:233], v[232:233], v[134:135]\n"
    "v_pk_fma_f32 v[136:137], v[136:137], s[70:71], v[104:105]\n"
    "v_pk_fma_f32 v[230:231], v[230:231], v[136:137], v[232:233]\n"
    "v_pk_mul_f32 v[232:233], v[232:233], v[136:137]\n"
    "v_pk_fma_f32 v[138:139], v[138:139], s[70:71], v[106:107]\n"
    "v_pk_fma_f32 v[230:231], v[230:231], v[138:139], v[232:233]\n"
    "v_pk_mul_f32 v[232:233], v[232:233], v[138:139]\n"
    "v_pk_fma_f32 v[140:141], v[140:141], s[70:71], v[108:109]\n"
    "v_pk_fma_f32 v[230:231], v[230:231], v[140:141], v[232:233]\n"
    "v_pk_mul_f32 v[232:233], v[232:233], v[140:141]\n"
    "v_pk_fma_f32 v[142:143], v[142:143], s[70:71], v[110:111]\n"
    "v_pk_fma_f32 v[230:231], v[230:231], v[142:143], v[232:233]\n"
    "v_pk_mul_f32 v[232:233], v[232:233], v[142:143]\n"
    "v_rcp_f32 v232, v232\n"
    "v_rcp_f32 v233, v233\n"
    "v_fma_f32 v230, v230, v232, v247\n"
    "v_fmac_f32 v230, v231, v233\n"
    "v_cvt_pk_bf16_f32 v230, v230, v230\n"
    "s_add_u32 s67, s67, 64\n"
    "s_waitcnt lgkmcnt(0)\n"
    "ds_write_b16 v245, v230\n"
    "ds_read_b64 v[228:229], v246\n"
    "buffer_load_dwordx4 v[236:237], s[40:43], 0 idxen offen offset:0 nt lds\n"
    "buffer_load_dwordx4 v[238:239], s[40:43], 0 idxen offen offset:1024 nt lds\n"
    ".Ldin_B3:\n"
    "v_mfma_f32_16x16x32_bf16 v[128:131], v[0:3], v[160:163], v[112:115]\n"
    "v_exp_f32 v144, v144 clamp\n"
    "v_mfma_f32_16x16x32_bf16 v[128:131], v[4:7], v[164:167], v[128:131]\n"
    "v_exp_f32 v145, v145 clamp\n"
    "v_mfma_f32_16x16x32_bf16 v[132:135], v[8:11], v[160:163], v[116:119]\n"
    "v_exp_f32 v146, v146 clamp\n"
    "v_mfma_f32_16x16x32_bf16 v[132:135], v[12:15], v[164:167], v[132:135]\n"
    "v_exp_f32 v147, v147 clamp\n"
    "v_mfma_f32_16x16x32_bf16 v[136:139], v[16:19], v[160:163], v[120:123]\n"
    "v_exp_f32 v148, v148 clamp\n"
    "v_mfma_f32_16x16x32_bf16 v[136:139], v[20:23], v[164:167], v[136:139]\n"
    "v_exp_f32 v149, v149 clamp\n"
    "v_mfma_f32_16x16x32_bf16 v[140:143], v[24:27], v[160:163], v[124:127]\n"
    "v_exp_f32 v150, v150 clamp\n"
    "v_mfma_f32_16x16x32_bf16 v[140:143], v[28:31], v[164:167], v[140:143]\n"
    "v_exp_f32 v151, v151 clamp\n"
    "v_exp_f32 v152, v152 clamp\n"
    "s_cmp_eq_u32 s67, s80\n"
    "s_cselect_b32 s67, s65, s67\n"
    "s_and_b32 s94, s59, 63\n"
    "s_mul_i32 s94, s94, 272\n"
    "v_add_u32 v252, s94, v250\n"
    "s_add_u32 m0, s68, 0\n"
    "v_exp_f32 v153, v153 clamp\n"
    "v_add_u32_e64 v253, s67, v244\n"
    "s_waitcnt lgkmcnt(0)\n"
    "v_mfma_f32_4x4x4_16b_bf16 v[224:227], v[228:229], v[168:169], 0 cbsz:4 abid:0\n"
    "v_exp_f32 v154, v154 clamp\n"
    "v_exp_f32 v155, v155 clamp\n"
    "v_mfma_f32_4x4x4_16b_bf16 v[224:227], v[228:229], v[170:171], v[224:227] cbsz:4 abid:1\n"
    "v_exp_f32 v156, v156 clamp\n"
    "v_exp_f32 v157, v157 clamp\n"
    "v_mfma_f32_4x4x4_16b_bf16 v[224:227], v[228:229], v[172:173], v[224:227] cbsz:4 abid:2\n"
    "v_exp_f32 v158, v158 clamp\n"
    "v_exp_f32 v159, v159 clamp\n"
    "v_mfma_f32_4x4x4_16b_bf16 v[224:227], v[228:229], v[174:175], v[224:227] cbsz:4 abid:3\n"
    "s_waitcnt vmcnt(4)\n"
    "s_memrealtime s[34:35]\n"
    "ds_read_b64_tr_b16 v[168:169], v240 offset:0\n"
    "ds_read_b64_tr_b16 v[170:171], v241 offset:256\n"
    "ds_read_b64_tr_b16 v[172:173], v242 offset:512\n"
    "ds_read_b64_tr_b16 v[174:175], v243 offset:768\n"
    "ds_read_b32 v236, v253 offset:0\n"
    "ds_read_b32 v238, v253 offset:4\n"
    "ds_read_b128 v[160:163], v234 offset:2048\n"
    "ds_read_b128 v[164:167], v235 offset:2048\n"
    "v_add_u32_e64 v184, v248, v252\n"
    "ds_read_b64 v[184:185], v184\n"
    "v_pk_fma_f32 v[144:145], v[144:145], s[70:71], v[96:97]\n"
    "v_pk_fma_f32 v[146:147], v[146:147], s[70:71], v[98:99]\n"
    "v_pk_add_f32 v[230:231], v[144:145], v[146:147]\n"
    "v_pk_mul_f32 v[232:233], v[144:145], v[146:147]\n"
    "v_pk_fma_f32 v[148:149], v[148:149], s[70:71], v[100:101]\n"
    "v_pk_fma_f32 v[230:231], v[230:231], v[148:149], v[232:233]\n"
    "v_pk_mul_f32 v[232:233], v[232:233], v[148:149]\n"
    "v_pk_fma_f32 v[150:151], v[150:151], s[70:71], v[102:103]\n"
    "v_pk_fma_f32 v[230:231], v[230:231], v[150:151], v[232:233]\n"
    "v_pk_mul_f32 v[232:233], v[232:233], v[150:151]\n"
    "v_pk_fma_f32 v[152:153], v[152:153], s[70:71], v[104:105]\n"
    "v_pk_fma_f32 v[230:231], v[230:231], v[152:153], v[232:233]\n"
    "v_pk_mul_f32 v[232:233], v[232:233], v[152:153]\n"
    "v_pk_fma_f32 v[154:155], v[154:155], s[70:71], v[106:107]\n"
    "v_pk_fma_f32 v[230:231], v[230:231], v[154:155], v[232:233]\n"
    "v_pk_mul_f32 v[232:233], v[232:233], v[154:155]\n"
    "v_pk_fma_f32 v[156:157], v[156:157], s[70:71], v[108:109]\n"
    "v_pk_fma_f32 v[230:231], v[230:231], v[156:157], v[232:233]\n"
    "v_pk_mul_f32 v[232:233], v[232:233], v[156:157]\n"
    "v_pk_fma_f32 v[158:159], v[158:159], s[70:71], v[110:111]\n"
    "v_pk_fma_f32 v[230:231], v[230:231], v[158:159], v[232:233]\n"
    "v_pk_mul_f32 v[232:233], v[232:233], v[158:159]\n"
    "v_rcp_f32 v232, v232\n"
    "v_rcp_f32 v233, v233\n"
    "v_fma_f32 v230, v230, v232, v247\n"
    "v_fmac_f32 v230, v231, v233\n"
    "v_cvt_pk_bf16_f32 v230, v230, v230\n"
    "s_add_u32 s67, s67, 64\n"
    "s_waitcnt lgkmcnt(0)\n"
    "ds_write_b16 v245, v230\n"
    "ds_read_b64 v[228:229], v246\n"
    "v_and_b32 v184, v254, v184\n"
    "v_and_b32 v185, v255, v185\n"
    "s_cmp_le_u32 s34, s76\n"
    "s_cbranch_scc1 .Ldin_B3_wait\n"
    ".Ldin_B3_go:\n"
    "s_sub_u32 s94, s34, 60\n"
    "s_max_u32 s76, s76, s94\n"
    "s_add_u32 s76, s76, s72\n"
    ".Ldin_B3_dma:\n"
    "buffer_load_dwordx4 v[236:237], s[40:43], 0 idxen offen offset:0 nt lds\n"
    "buffer_load_dwordx4 v[238:239], s[40:43], 0 idxen offen offset:1024 nt lds\n"
    ".Ldin_B3_nd:\n"
    "s_branch .Ldin_G0\n"
    ".Ldin_L1_poll:\n"
    "s_sleep 1\n"
    "ds_read_b64 v[232:233], v253\n"
    "s_waitcnt lgkmcnt(0)\n"
    "v_readfirstlane_b32 s60, v232\n"
    "s_cmp_eq_u32 s60, -1\n"
    "s_cbranch_scc1 .Ldin_L1_poll\n"
    "s_branch .Ldin_L1_pr\n"
    ".Ldin_B3_wait:\n"
    "s_cmp_lt_i32 s59, s57\n"
    "s_cbranch_scc1 .Ldin_B3_w2\n"
    "s_sub_u32 s94, s67, s65\n"
    "s_cmp_gt_u32 s94, 256\n"
    "s_cbranch_scc1 .Ldin_B3_dma\n"
    "s_cmp_eq_u32 s94, 64\n"
    "s_waitcnt vmcnt(2)\n"
    "s_cbranch_scc1 .Ldin_B3_nd\n"
    "s_waitcnt vmcnt(0)\n"
    "s_branch .Ldin_B3_nd\n"
    ".Ldin_B3_slp:\n"
    "s_sleep 1\n"
    ".Ldin_B3_w2:\n"
    "s_memrealtime s[34:35]\n"
    "s_waitcnt lgkmcnt(0)\n"
    "s_sub_u32 s94, s34, s76\n"
    "s_cmp_lt_i32 s94, 0\n"
    "s_cbranch_scc1 .Ldin_B3_slp\n"
    "s_branch .Ldin_B3_go\n"
    ".p2align 5\n"
    ".Ldin_stub_3:\n"
    "s_cbranch_execz .Ldin_t0_ret\n"
    "s_nop 0\n"
    ".Ldin_L0:\n"
    "s_setprio 0\n"
    "s_cmp_ge_i32 s59, s57\n"
    "s_cbranch_scc1 .Ldin_E0\n"
    "s_add_u32 s76, s76, s73\n"
    "v_mov_b32_e64 v253, s63\n"
    "ds_read_b64 v[232:233], v253\n"
    "v_mfma_f32_4x4x4_16b_bf16 v[144:147], v[184:185], v[32:33], v[64:67] cbsz:2 abid:0\n"
    "ds_read_b128 v[112:115], v252 offset:17408\n"
    "v_mfma_f32_4x4x4_16b_bf16 v[148:151], v[184:185], v[34:35], v[68:71] cbsz:2 abid:1\n"
    "ds_read_b128 v[116:119], v252 offset:17424\n"
    "v_mfma_f32_4x4x4_16b_bf16 v[152:155], v[184:185], v[36:37], v[72:75] cbsz:2 abid:2\n"
    "ds_read_b128 v[120:123], v252 offset:17536\n"
    "ds_read_b128 v[124:127], v252 offset:17552\n"
    "v_cvt_pk_bf16_f32 v0, v144, v145\n"
    "v_cvt_pk_bf16_f32 v1, v146, v147\n"
    "v_mfma_f32_4x4x4_16b_bf16 v[156:159], v[184:185], v[38:39], v[76:79] cbsz:2 abid:3\n"
    "v_cvt_pk_bf16_f32 v2, v148, v149\n"
    "v_cvt_pk_bf16_f32 v3, v150, v151\n"
    "v_mfma_f32_4x4x4_16b_bf16 v[144:147], v[184:185], v[40:41], v[80:83] cbsz:2 abid:0\n"
    "v_cvt_pk_bf16_f32 v4, v152, v153\n"
    "v_cvt_pk_bf16_f32 v5, v154, v155\n"
    "v_mfma_f32_4x4x4_16b_bf16 v[148:151], v[184:185], v[42:43], v[84:87] cbsz:2 abid:1\n"
    "v_cvt_pk_bf16_f32 v6, v156, v157\n"
    "v_cvt_pk_bf16_f32 v7, v158, v159\n"
    "v_mfma_f32_4x4x4_16b_bf16 v[152:155], v[184:185], v[44:45], v[88:91] cbsz:2 abid:2\n"
    "v_cvt_pk_bf16_f32 v8, v144, v145\n"
    "v_cvt_pk_bf16_f32 v9, v146, v147\n"
    "v_mfma_f32_4x4x4_16b_bf16 v[156:159], v[184:185], v[46:47], v[92:95] cbsz:2 abid:3\n"
    "v_cvt_pk_bf16_f32 v10, v148, v149\n"
    "v_cvt_pk_bf16_f32 v11, v150, v151\n"
    "v_mfma_f32_4x4x4_16b_bf16 v[144:147], v[184:185], v[48:49], v[192:195] cbsz:2 abid:0\n"
    "v_cvt_pk_bf16_f32 v12, v152, v153\n"
    "v_cvt_pk_bf16_f32 v13, v154, v155\n"
    "v_mfma_f32_4x4x4_16b_bf16 v[148:151], v[184:185], v[50:51], v[196:199] cbsz:2 abid:1\n"
    "v_cvt_pk_bf16_f32 v14, v156, v157\n"
    "v_cvt_pk_bf16_f32 v15, v158, v159\n"
    "v_mfma_f32_4x4x4_16b_bf16 v[152:155], v[184:185], v[52:53], v[200:203] cbsz:2 abid:2\n"
    "s_waitcnt lgkmcnt(0)\n"
    "v_readfirstlane_b32 s60, v232\n"
    "v_cvt_pk_bf16_f32 v16, v144, v145\n"
    "v_cvt_pk_bf16_f32 v17, v146, v147\n"
    "v_mfma_f32_4x4x4_16b_bf16 v[156:159], v[184:185], v[54:55], v[204:207] cbsz:2 abid:3\n"
    "s_cmp_eq_u32 s60, -1\n"
    "s_cbranch_scc1 .Ldin_L0_poll\n"
    ".Ldin_L0_pr:\n"
    "v_readfirstlane_b32 s95, v233\n"
    "s_and_b32 s94, s60, 63\n"
    "s_lshl_b32 s94, s94, 6\n"
    "s_add_u32 s94, s94, 68480\n"
    "s_add_u32 s63, s63, 8\n"
    "v_lshl_add_u32 v252, v249, 1, s94\n"
    "ds_read_b32 v252, v252\n"
    "s_sub_u32 s95, s95, s76\n"
    "s_ashr_i32 s95, s95, 4\n"
    "s_min_i32 s95, s95, s74\n"
    "s_max_i32 s72, s95, s75\n"
    "v_cvt_pk_bf16_f32 v18, v148, v149\n"
    "v_cvt_pk_bf16_f32 v19, v150, v151\n"
    "v_mfma_f32_4x4x4_16b_bf16 v[144:147], v[184:185], v[56:57], v[208:211] cbsz:2 abid:0\n"
    "v_cvt_pk_bf16_f32 v20, v152, v153\n"
    "v_cvt_pk_bf16_f32 v21, v154, v155\n"
    "v_mfma_f32_4x4x4_16b_bf16 v[148:151], v[184:185], v[58:59], v[212:215] cbsz:2 abid:1\n"
    "v_cvt_pk_bf16_f32 v22, v156, v157\n"
    "v_cvt_pk_bf16_f32 v23, v158, v159\n"
    "v_mfma_f32_4x4x4_16b_bf16 v[152:155], v[184:185], v[60:61], v[216:219] cbsz:2 abid:2\n"
    "v_cvt_pk_bf16_f32 v24, v144, v145\n"
    "v_cvt_pk_bf16_f32 v25, v146, v147\n"
    "v_mfma_f32_4x4x4_16b_bf16 v[156:159], v[184:185], v[62:63], v[220:223] cbsz:2 abid:3\n"
    "v_cvt_pk_bf16_f32 v26, v148, v149\n"
    "v_cvt_pk_bf16_f32 v27, v150, v151\n"
    "v_cvt_pk_bf16_f32 v28, v152, v153\n"
    "v_cvt_pk_bf16_f32 v29, v154, v155\n"
    "v_exp_f32 v136, v136 clamp\n"
    "v_cvt_pk_bf16_f32 v30, v156, v157\n"
    "v_cvt_pk_bf16_f32 v31, v158, v159\n"
    "v_mfma_f32_16x16x32_bf16 v[144:147], v[0:3], v[160:163], v[112:115]\n"
    "v_exp_f32 v128, v128 clamp\n"
    "v_mfma_f32_16x16x32_bf16 v[144:147], v[4:7], v[164:167], v[144:147]\n"
    "v_exp_f32 v129, v129 clamp\n"
    "v_mfma_f32_16x16x32_bf16 v[148:151], v[8:11], v[160:163], v[116:119]\n"
    "v_exp_f32 v130, v130 clamp\n"
    "v_mfma_f32_16x16x32_bf16 v[148:151], v[12:15], v[164:167], v[148:151]\n"
    "v_exp_f32 v131, v131 clamp\n"
    "v_mfma_f32_16x16x32_bf16 v[152:155], v[16:19], v[160:163], v[120:123]\n"
    "v_exp_f32 v132, v132 clamp\n"
    "v_mfma_f32_16x16x32_bf16 v[152:155], v[20:23], v[164:167], v[152:155]\n"
    "v_exp_f32 v133, v133 clamp\n"
    "v_mfma_f32_16x16x32_bf16 v[156:159], v[24:27], v[160:163], v[124:127]\n"
    "v_exp_f32 v134, v134 clamp\n"
    "v_mfma_f32_16x16x32_bf16 v[156:159], v[28:31], v[164:167], v[156:159]\n"
    "v_exp_f32 v135, v135 clamp\n"
    "s_add_u32 m0, s68, 2048\n"
    "v_exp_f32 v137, v137 clamp\n"
    "v_add_u32 v253, s67, v244\n"
    "v_mfma_f32_4x4x4_16b_bf16 v[224:227], v[228:229], v[176:177], v[224:227] cbsz:4 abid:0\n"
    "v_exp_f32 v138, v138 clamp\n"
    "v_exp_f32 v139, v139 clamp\n"
    "v_mfma_f32_4x4x4_16b_bf16 v[224:227], v[228:229], v[178:179], v[224:227] cbsz:4 abid:1\n"
    "v_exp_f32 v140, v140 clamp\n"
    "v_exp_f32 v141, v141 clamp\n"
    "v_mfma_f32_4x4x4_16b_bf16 v[224:227], v[228:229], v[180:181], v[224:227] cbsz:4 abid:2\n"
    "v_exp_f32 v142, v142 clamp\n"
    "v_exp_f32 v143, v143 clamp\n"
    "v_mfma_f32_4x4x4_16b_bf16 v[224:227], v[228:229], v[182:183], v[224:227] cbsz:4 abid:3\n"
    "s_waitcnt vmcnt(4)\n"
    "ds_read_b64_tr_b16 v[176:177], v240 offset:2048\n"
    "ds_read_b64_tr_b16 v[178:179], v241 offset:2304\n"
    "ds_read_b64_tr_b16 v[180:181], v242 offset:2560\n"
    "ds_read_b64_tr_b16 v[182:183], v243 offset:2816\n"
    "ds_read_b32 v236, v253 offset:0\n"
    "ds_read_b32 v238, v253 offset:4\n"
    "ds_read_b128 v[160:163], v234 offset:4096\n"
    "ds_read_b128 v[164:167], v235 offset:4096\n"
    "s_waitcnt lgkmcnt(8)\n"
    "s_nop 0\n"
    "v_readlane_b32 s20, v252, 0\n"
    "v_readlane_b32 s21, v252, 1\n"
    "v_readlane_b32 s22, v252, 2\n"
    "v_readlane_b32 s23, v252, 3\n"
    "v_readlane_b32 s24, v252, 4\n"
    "v_readlane_b32 s25, v252, 5\n"
    "v_readlane_b32 s26, v252, 6\n"
    "v_readlane_b32 s27, v252, 7\n"
    "v_pk_fma_f32 v[128:129], v[128:129], s[70:71], v[96:97]\n"
    "v_pk_fma_f32 v[130:131], v[130:131], s[70:71], v[98:99]\n"
    "v_pk_add_f32 v[230:231], v[128:129], v[130:131]\n"
    "v_pk_mul_f32 v[232:233], v[128:129], v[130:131]\n"
    "v_pk_fma_f32 v[132:133], v[132:133], s[70:71], v[100:101]\n"
    "v_pk_fma_f32 v[230:231], v[230:231], v[132:133], v[232:233]\n"
    "v_pk_mul_f32 v[232:233], v[232:233], v[132:133]\n"
    "v_pk_fma_f32 v[134:135], v[134:135], s[70:71], v[102:103]\n"
    "v_pk_fma_f32 v[230:231], v[230:231], v[134:135], v[232:233]\n"
    "v_pk_mul_f32 v[232:233], v[232:233], v[134:135]\n"
    "v_pk_fma_f32 v[136:137], v[136:137], s[70:71], v[104:105]\n"
    "v_pk_fma_f32 v[230:231], v[230:231], v[136:137], v[232:233]\n"
    "v_pk_mul_f32 v[232:233], v[232:233], v[136:137]\n"
    "v_pk_fma_f32 v[138:139], v[138:139], s[70:71], v[106:107]\n"
    "v_pk_fma_f32 v[230:231], v[230:231], v[138:139], v[232:233]\n"
    "v_pk_mul_f32 v[232:233], v[232:233], v[138:139]\n"
    "v_pk_fma_f32 v[140:141], v[140:141], s[70:71], v[108:109]\n"
    "v_pk_fma_f32 v[230:231], v[230:231], v[140:141], v[232:233]\n"
    "v_pk_mul_f32 v[232:233], v[232:233], v[140:141]\n"
    "v_pk_fma_f32 v[142:143], v[142:143], s[70:71], v[110:111]\n"
    "v_pk_fma_f32 v[230:231], v[230:231], v[142:143], v[232:233]\n"
    "v_pk_mul_f32 v[232:233], v[232:233], v[142:143]\n"
    "v_rcp_f32 v232, v232\n"
    "v_rcp_f32 v233, v233\n"
    "v_fma_f32 v230, v230, v232, v247\n"
    "v_fmac_f32 v230, v231, v233\n"
    "v_cvt_pk_bf16_f32 v230, v230, v230\n"
    "s_add_u32 s67, s67, 64\n"
    "s_waitcnt lgkmcnt(0)\n"
    "ds_write_b16 v245, v230\n"
    "ds_read_b64 v[228:229], v246\n"
    "s_lshl1_add_u32 s76, s72, s76\n"
    "buffer_load_dwordx4 v[236:237], s[40:43], 0 idxen offen offset:0 nt lds\n"
    "buffer_load_dwordx4 v[238:239], s[40:43], 0 idxen offen offset:1024 nt lds\n"
    "s_and_b32 s62, s58, 63\n"
    "s_mul_i32 s62, s62, 272\n"
    "s_mov_b32 s58, s59\n"
    "s_mov_b32 s59, s60\n"
    "s_mov_b32 s81, s65\n"
    "s_xor_b32 s65, s65, s64\n"
    "s_mov_b32 s80, s83\n"
    "s_add_u32 s82, s65, 256\n"
    "s_cmp_lt_i32 s59, s57\n"
    "s_cselect_b32 s76, s76, -1\n"
    "s_bitcmp1_b32 s58, 30\n"
    "s_cbranch_scc1 .Ldin_A1s\n"
    ".Ldin_A1:\n"
    "v_mfma_f32_16x16x32_bf16 v[128:131], v[0:3], v[160:163], v[112:115]\n"
    "v_exp_f32 v144, v144 clamp\n"
    "v_mfma_f32_16x16x32_bf16 v[128:131], v[4:7], v[164:167], v[128:131]\n"
    "v_exp_f32 v145, v145 clamp\n"
    "v_mfma_f32_16x16x32_bf16 v[132:135], v[8:11], v[160:163], v[116:119]\n"
    "v_exp_f32 v146, v146 clamp\n"
    "v_mfma_f32_16x16x32_bf16 v[132:135], v[12:15], v[164:167], v[132:135]\n"
    "v_exp_f32 v147, v147 clamp\n"
    "v_mfma_f32_16x16x32_bf16 v[136:139], v[16:19], v[160:163], v[120:123]\n"
    "v_exp_f32 v148, v148 clamp\n"
    "v_mfma_f32_16x16x32_bf16 v[136:139], v[20:23], v[164:167], v[136:139]\n"
    "v_exp_f32 v149, v149 clamp\n"
    "v_mfma_f32_16x16x32_bf16 v[140:143], v[24:27], v[160:163], v[124:127]\n"
    "v_exp_f32 v150, v150 clamp\n"
    "v_mfma_f32_16x16x32_bf16 v[140:143], v[28:31], v[164:167], v[140:143]\n"
    "v_exp_f32 v151, v151 clamp\n"
    "v_exp_f32 v152, v152 clamp\n"
    "s_mov_b32 m0, s62\n"
    "v_exp_f32 v153, v153 clamp\n"
    "v_add_u32 v253, s67, v244\n"
    "s_waitcnt lgkmcnt(0)\n"
    "v_mfma_f32_4x4x4_16b_bf16 v[224:227], v[228:229], v[168:169], v[224:227] cbsz:4 abid:0\n"
    "v_exp_f32 v154, v154 clamp\n"
    "v_exp_f32 v155, v155 clamp\n"
    "v_mfma_f32_4x4x4_16b_bf16 v[224:227], v[228:229], v[170:171], v[224:227] cbsz:4 abid:1\n"
    "v_exp_f32 v156, v156 clamp\n"
    "v_exp_f32 v157, v157 clamp\n"
    "v_mfma_f32_4x4x4_16b_bf16 v[224:227], v[228:229], v[172:173], v[224:227] cbsz:4 abid:2\n"
    "v_exp_f32 v158, v158 clamp\n"
    "v_exp_f32 v159, v159 clamp\n"
    "v_mfma_f32_4x4x4_16b_bf16 v[224:227], v[228:229], v[174:175], v[224:227] cbsz:4 abid:3\n"
    "s_waitcnt vmcnt(4)\n"
    "ds_read_b64_tr_b16 v[168:169], v240 offset:4096\n"
    "ds_read_b64_tr_b16 v[170:171], v241 offset:4352\n"
    "ds_read_b64_tr_b16 v[172:173], v242 offset:4608\n"
    "ds_read_b64_tr_b16 v[174:175], v243 offset:4864\n"
    "ds_read_b32 v236, v253 offset:0\n"
    "ds_read_b32 v238, v253 offset:4\n"
    "ds_read_b128 v[160:163], v234 offset:6144\n"
    "ds_read_b128 v[164:167], v235 offset:6144\n"
    "s_cbranch_vccz .Ldin_A1_np\n"
    "s_setprio 1\n"
    ".Ldin_A1_np:\n"
    "v_pk_fma_f32 v[144:145], v[144:145], s[70:71], v[96:97]\n"
    "v_pk_fma_f32 v[146:147], v[146:147], s[70:71], v[98:99]\n"
    "v_pk_add_f32 v[190:191], v[224:225], v[226:227]\n"
    "v_add_f32 v190, v190, v191\n"
    "ds_write_addtid_b32 v190 offset:32768\n"
    "s_add_u32 m0, s68, 4096\n"
    "s_bfe_u32 s94, s59, 0x180006\n"
    "s_mul_i32 s86, s94, s56\n"
    "v_mbcnt_lo_u32_b32 v184, s20, 0\n"
    "v_mbcnt_lo_u32_b32 v185, s22, 0\n"
    "v_mbcnt_lo_u32_b32 v186, s24, 0\n"
    "v_mbcnt_lo_u32_b32 v187, s26, 0\n"
    "s_bcnt1_i32_b64 s90, s[20:21]\n"
    "s_bcnt1_i32_b64 s91, s[22:23]\n"
    "s_bcnt1_i32_b64 s92, s[24:25]\n"
    "s_bcnt1_i32_b64 s93, s[26:27]\n"
    "v_mbcnt_hi_u32_b32 v184, s21, v184\n"
    "v_mbcnt_hi_u32_b32 v185, s23, v185\n"
    "v_mbcnt_hi_u32_b32 v186, s25, v186\n"
    "v_mbcnt_hi_u32_b32 v187, s27, v187\n"
    "s_lshl2_add_u32 s90, s90, s65\n"
    "s_lshl2_add_u32 s91, s91, s90\n"
    "s_lshl2_add_u32 s92, s92, s91\n"
    "s_lshl2_add_u32 s93, s93, s92\n"
    "v_add_u32_e64 v188, s86, v251\n"
    "v_pk_add_f32 v[230:231], v[144:145], v[146:147]\n"
    "v_pk_mul_f32 v[232:233], v[144:145], v[146:147]\n"
    ".Ldin_stub_15:\n"
    "s_cbranch_execz .Ldin_t3_ret\n"
    "s_nop 0\n"
    "v_pk_fma_f32 v[148:149], v[148:149], s[70:71], v[100:101]\n"
    "v_pk_fma_f32 v[230:231], v[230:231], v[148:149], v[232:233]\n"
    "v_pk_mul_f32 v[232:233], v[232:233], v[148:149]\n"
    "v_lshl_add_u32 v184, v184, 2, s65\n"
    "v_lshl_add_u32 v185, v185, 2, s90\n"
    "v_lshl_add_u32 v186, v186, 2, s91\n"
    "v_lshl_add_u32 v187, v187, 2, s92\n"
    "v_mov_b32 v191, -1\n"
    "v_lshl_add_u32 v190, v249, 1, s93\n"
    "ds_write2_b32 v190, v191, v191 offset1:64\n"
    "s_mov_b64 exec, s[20:21]\n"
    "ds_write_b32 v184, v188\n"
    "s_mov_b64 exec, s[22:23]\n"
    "v_add_u32 v189, 64, v188\n"
    "ds_write_b32 v185, v189\n"
    "s_mov_b64 exec, s[24:25]\n"
    "v_add_u32 v190, 128, v188\n"
    "ds_write_b32 v186, v190\n"
    "s_mov_b64 exec, s[26:27]\n"
    "v_add_u32 v189, 192, v188\n"
    "ds_write_b32 v187, v189\n"
    "s_mov_b64 exec, -1\n"
    "s_add_u32 s95, s93, 60\n"
    "s_add_u32 s94, s82, 128\n"
    "s_andn2_b32 s95, s95, 63\n"
    "s_max_u32 s83, s95, s94\n"
    "v_pk_fma_f32 v[150:151], v[150:151], s[70:71], v[102:103]\n"
    "v_pk_fma_f32 v[230:231], v[230:231], v[150:151], v[232:233]\n"
    "v_pk_mul_f32 v[232:233], v[232:233], v[150:151]\n"
    "v_pk_fma_f32 v[152:153], v[152:153], s[70:71], v[104:105]\n"
    "v_pk_fma_f32 v[230:231], v[230:231], v[152:153], v[232:233]\n"
    "v_pk_mul_f32 v[232:233], v[232:233], v[152:153]\n"
    "v_pk_fma_f32 v[154:155], v[154:155], s[70:71], v[106:107]\n"
    "v_pk_fma_f32 v[230:231], v[230:231], v[154:155], v[232:233]\n"
    "v_pk_mul_f32 v[232:233], v[232:233], v[154:155]\n"
    "v_pk_fma_f32 v[156:157], v[156:157], s[70:71], v[108:109]\n"
    "v_pk_fma_f32 v[230:231], v[230:231], v[156:157], v[232:233]\n"
    "v_pk_mul_f32 v[232:233], v[232:233], v[156:157]\n"
    "v_pk_fma_f32 v[158:159], v[158:159], s[70:71], v[110:111]\n"
    "v_pk_fma_f32 v[230:231], v[230:231], v[158:159], v[232:233]\n"
    "v_pk_mul_f32 v[232:233], v[232:233], v[158:159]\n"
    "v_rcp_f32 v232, v232\n"
    "v_rcp_f32 v233, v233\n"
    "v_fma_f32 v230, v230, v232, v247\n"
    "v_fmac_f32 v230, v231, v233\n"
    "v_cvt_pk_bf16_f32 v230, v230, v230\n"
    "s_add_u32 s67, s67, 64\n"
    "s_waitcnt lgkmcnt(0)\n"
    "ds_write_b16 v245, v230\n"
    "ds_read_b64 v[228:229], v246\n"
    "buffer_load_dwordx4 v[236:237], s[40:43], 0 idxen offen offset:0 nt lds\n"
    "buffer_load_dwordx4 v[238:239], s[40:43], 0 idxen offen offset:1024 nt lds\n"
    ".Ldin_B2:\n"
    "v_mfma_f32_16x16x32_bf16 v[144:147], v[0:3], v[160:163], v[112:115]\n"
    "v_exp_f32 v128, v128 clamp\n"
    "v_mfma_f32_16x16x32_bf16 v[144:147], v[4:7], v[164:167], v[144:147]\n"
    "v_exp_f32 v129, v129 clamp\n"
    "v_mfma_f32_16x16x32_bf16 v[148:151], v[8:11], v[160:163], v[116:119]\n"
    "v_exp_f32 v130, v130 clamp\n"
    "v_mfma_f32_16x16x32_bf16 v[148:151], v[12:15], v[164:167], v[148:151]\n"
    "v_exp_f32 v131, v131 clamp\n"
    "v_mfma_f32_16x16x32_bf16 v[152:155], v[16:19], v[160:163], v[120:123]\n"
    "v_exp_f32 v132, v132 clamp\n"
    "v_mfma_f32_16x16x32_bf16 v[152:155], v[20:23], v[164:167], v[152:155]\n"
    "v_exp_f32 v133, v133 clamp\n"
    "v_mfma_f32_16x16x32_bf16 v[156:159], v[24:27], v[160:163], v[124:127]\n"
    "v_exp_f32 v134, v134 clamp\n"
    "v_mfma_f32_16x16x32_bf16 v[156:159], v[28:31], v[164:167], v[156:159]\n"
    "v_exp_f32 v135, v135 clamp\n"
    "v_exp_f32 v136, v136 clamp\n"
    "s_cmp_eq_u32 s67, s80\n"
    "s_cselect_b32 s67, s65, s67\n"
    "s_and_b32 s94, s59, 63\n"
    "s_mul_i32 s94, s94, 272\n"
    "v_add_u32 v252, s94, v250\n"
    "s_add_u32 m0, s68, 6144\n"
    "v_exp_f32 v137, v137 clamp\n"
    "v_add_u32 v253, s67, v244\n"
    "s_waitcnt lgkmcnt(0)\n"
    "v_mfma_f32_4x4x4_16b_bf16 v[224:227], v[228:229], v[176:177], 0 cbsz:4 abid:0\n"
    "v_exp_f32 v138, v138 clamp\n"
    "v_exp_f32 v139, v139 clamp\n"
    "v_mfma_f32_4x4x4_16b_bf16 v[224:227], v[228:229], v[178:179], v[224:227] cbsz:4 abid:1\n"
    "v_exp_f32 v140, v140 clamp\n"
    "v_exp_f32 v141, v141 clamp\n"
    "v_mfma_f32_4x4x4_16b_bf16 v[224:227], v[228:229], v[180:181], v[224:227] cbsz:4 abid:2\n"
    "v_exp_f32 v142, v142 clamp\n"
    "v_exp_f32 v143, v143 clamp\n"
    "v_mfma_f32_4x4x4_16b_bf16 v[224:227], v[228:229], v[182:183], v[224:227] cbsz:4 abid:3\n"
    "s_waitcnt vmcnt(4)\n"
    "s_memrealtime s[34:35]\n"
    "ds_read_b64_tr_b16 v[176:177], v240 offset:6144\n"
    "ds_read_b64_tr_b16 v[178:179], v241 offset:6400\n"
    "ds_read_b64_tr_b16 v[180:181], v242 offset:6656\n"
    "ds_read_b64_tr_b16 v[182:183], v243 offset:6912\n"
    "ds_read_b32 v236, v253 offset:0\n"
    "ds_read_b32 v238, v253 offset:4\n"
    "ds_read_b128 v[160:163], v234 offset:0\n"
    "ds_read_b128 v[164:167], v235 offset:0\n"
    "v_add_u32_e64 v184, v248, v252\n"
    "ds_read_b64 v[184:185], v184\n"
    "v_pk_fma_f32 v[128:129], v[128:129], s[70:71], v[96:97]\n"
    "v_pk_fma_f32 v[130:131], v[130:131], s[70:71], v[98:99]\n"
    "v_pk_add_f32 v[230:231], v[128:129], v[130:131]\n"
    "v_pk_mul_f32 v[232:233], v[128:129], v[130:131]\n"
    "v_pk_fma_f32 v[132:133], v[132:133], s[70:71], v[100:101]\n"
    "v_pk_fma_f32 v[230:231], v[230:231], v[132:133], v[232:233]\n"
    "v_pk_mul_f32 v[232:233], v[232:233], v[132:133]\n"
    "v_pk_fma_f32 v[134:135], v[134:135], s[70:71], v[102:103]\n"
    "v_pk_fma_f32 v[230:231], v[230:231], v[134:135], v[232:233]\n"
    "v_pk_mul_f32 v[232:233], v[232:233], v[134:135]\n"
    "v_pk_fma_f32 v[136:137], v[136:137], s[70:71], v[104:105]\n"
    "v_pk_fma_f32 v[230:231], v[230:231], v[136:137], v[232:233]\n"
    "v_pk_mul_f32 v[232:233], v[232:233], v[136:137]\n"
    "v_pk_fma_f32 v[138:139], v[138:139], s[70:71], v[106:107]\n"
    "v_pk_fma_f32 v[230:231], v[230:231], v[138:139], v[232:233]\n"
    "v_pk_mul_f32 v[232:233], v[232:233], v[138:139]\n"
    "v_pk_fma_f32 v[140:141], v[140:141], s[70:71], v[108:109]\n"
    "v_pk_fma_f32 v[230:231], v[230:231], v[140:141], v[232:233]\n"
    "v_pk_mul_f32 v[232:233], v[232:233], v[140:141]\n"
    "v_pk_fma_f32 v[142:143], v[142:143], s[70:71], v[110:111]\n"
    "v_pk_fma_f32 v[230:231], v[230:231], v[142:143], v[232:233]\n"
    "v_pk_mul_f32 v[232:233], v[232:233], v[142:143]\n"
    "v_rcp_f32 v232, v232\n"
    "v_rcp_f32 v233, v233\n"
    "v_fma_f32 v230, v230, v232, v247\n"
    "v_fmac_f32 v230, v231, v233\n"
    "v_cvt_pk_bf16_f32 v230, v230, v230\n"
    "s_add_u32 s67, s67, 64\n"
    "s_waitcnt lgkmcnt(0)\n"
    "ds_write_b16 v245, v230\n"
    "ds_read_b64 v[228:229], v246\n"
    "v_and_b32 v184, v254, v184\n"
    "v_and_b32 v185, v255, v185\n"
    "s_cmp_le_u32 s34, s76\n"
    "s_cbranch_scc1 .Ldin_B2_wait\n"
    ".Ldin_B2_go:\n"
    "s_sub_u32 s94, s34, 60\n"
    "s_max_u32 s76, s76, s94\n"
    "s_add_u32 s76, s76, s72\n"
    ".Ldin_B2_dma:\n"
    "buffer_load_dwordx4 v[236:237], s[40:43], 0 idxen offen offset:0 nt lds\n"
    "buffer_load_dwordx4 v[238:239], s[40:43], 0 idxen offen offset:1024 nt lds\n"
    ".Ldin_B2_nd:\n"
    "s_branch .Ldin_G3\n"
    ".Ldin_L0_poll:\n"
    "s_sleep 1\n"
    "ds_read_b64 v[232:233], v253\n"
    "s_waitcnt lgkmcnt(0)\n"
    "v_readfirstlane_b32 s60, v232\n"
    "s_cmp_eq_u32 s60, -1\n"
    "s_cbranch_scc1 .Ldin_L0_poll\n"
    "s_branch .Ldin_L0_pr\n"
    ".Ldin_B2_wait:\n"
    "s_cmp_lt_i32 s59, s57\n"
    "s_cbranch_scc1 .Ldin_B2_w2\n"
    "s_sub_u32 s94, s67, s65\n"
    "s_cmp_gt_u32 s94, 256\n"
    "s_cbranch_scc1 .Ldin_B2_dma\n"
    "s_cmp_eq_u32 s94, 64\n"
    "s_waitcnt vmcnt(2)\n"
    "s_cbranch_scc1 .Ldin_B2_nd\n"
    "s_waitcnt vmcnt(0)\n"
    "s_branch .Ldin_B2_nd\n"
    ".Ldin_B2_slp:\n"
    "s_sleep 1\n"
    ".Ldin_B2_w2:\n"
    "s_memrealtime s[34:35]\n"
    "s_waitcnt lgkmcnt(0)\n"
    "s_sub_u32 s94, s34, s76\n"
    "s_cmp_lt_i32 s94, 0\n"
    "s_cbranch_scc1 .Ldin_B2_slp\n"
    "s_branch .Ldin_B2_go\n"
    ".p2align 5\n"
    ".Ldin_stub_2:\n"
    "s_cbranch_execz .Ldin_t0_ret\n"
    "s_nop 0\n"
    ".Ldin_L3:\n"
    "s_setprio 0\n"
    "s_cmp_ge_i32 s59, s57\n"
    "s_cbranch_scc1 .Ldin_E1\n"
    "s_add_u32 s76, s76, s73\n"
    "v_mov_b32_e64 v253, s63\n"
    "ds_read_b64 v[232:233], v253\n"
    "v_mfma_f32_4x4x4_16b_bf16 v[128:131], v[184:185], v[32:33], v[64:67] cbsz:2 abid:0\n"
    "ds_read_b128 v[112:115], v252 offset:17408\n"
    "v_mfma_f32_4x4x4_16b_bf16 v[132:135], v[184:185], v[34:35], v[68:71] cbsz:2 abid:1\n"
    "ds_read_b128 v[116:119], v252 offset:17424\n"
    "v_mfma_f32_4x4x4_16b_bf16 v[136:139], v[184:185], v[36:37], v[72:75] cbsz:2 abid:2\n"
    "ds_read_b128 v[120:123], v252 offset:17536\n"
    "ds_read_b128 v[124:127], v252 offset:17552\n"
    "v_cvt_pk_bf16_f32 v0, v128, v129\n"
    "v_cvt_pk_bf16_f32 v1, v130, v131\n"
    "v_mfma_f32_4x4x4_16b_bf16 v[140:143], v[184:185], v[38:39], v[76:79] cbsz:2 abid:3\n"
    "v_cvt_pk_bf16_f32 v2, v132, v133\n"
    "v_cvt_pk_bf16_f32 v3, v134, v135\n"
    "v_mfma_f32_4x4x4_16b_bf16 v[128:131], v[184:185], v[40:41], v[80:83] cbsz:2 abid:0\n"
    "v_cvt_pk_bf16_f32 v4, v136, v137\n"
    "v_cvt_pk_bf16_f32 v5, v138, v139\n"
    "v_mfma_f32_4x4x4_16b_bf16 v[132:135], v[184:185], v[42:43], v[84:87] cbsz:2 abid:1\n"
    "v_cvt_pk_bf16_f32 v6, v140, v141\n"
    "v_cvt_pk_bf16_f32 v7, v142, v143\n"
    "v_mfma_f32_4x4x4_16b_bf16 v[136:139], v[184:185], v[44:45], v[88:91] cbsz:2 abid:2\n"
    "v_cvt_pk_bf16_f32 v8, v128, v129\n"
    "v_cvt_pk_bf16_f32 v9, v130, v131\n"
    "v_mfma_f32_4x4x4_16b_bf16 v[140:143], v[184:185], v[46:47], v[92:95] cbsz:2 abid:3\n"
    "v_cvt_pk_bf16_f32 v10, v132, v133\n"
    "v_cvt_pk_bf16_f32 v11, v134, v135\n"
    "v_mfma_f32_4x4x4_16b_bf16 v[128:131], v[184:185], v[48:49], v[192:195] cbsz:2 abid:0\n"
    "v_cvt_pk_bf16_f32 v12, v136, v137\n"
    "v_cvt_pk_bf16_f32 v13, v138, v139\n"
    "v_mfma_f32_4x4x4_16b_bf16 v[132:135], v[184:185], v[50:51], v[196:199] cbsz:2 abid:1\n"
    "v_cvt_pk_bf16_f32 v14, v140, v141\n"
    "v_cvt_pk_bf16_f32 v15, v142, v143\n"
    "v_mfma_f32_4x4x4_16b_bf16 v[136:139], v[184:185], v[52:53], v[200:203] cbsz:2 abid:2\n"
    "s_waitcnt lgkmcnt(0)\n"
    "v_readfirstlane_b32 s60, v232\n"
    "v_cvt_pk_bf16_f32 v16, v128, v129\n"
    "v_cvt_pk_bf16_f32 v17, v130, v131\n"
    "v_mfma_f32_4x4x4_16b_bf16 v[140:143], v[184:185], v[54:55], v[204:207] cbsz:2 abid:3\n"
    "s_cmp_eq_u32 s60, -1\n"
    "s_cbranch_scc1 .Ldin_L3_poll\n"
    ".Ldin_L3_pr:\n"
    "v_readfirstlane_b32 s95, v233\n"
    "s_and_b32 s94, s60, 63\n"
    "s_lshl_b32 s94, s94, 6\n"
    "s_add_u32 s94, s94, 68480\n"
    "s_add_u32 s63, s63, 8\n"
    "v_lshl_add_u32 v252, v249, 1, s94\n"
    "ds_read_b32 v252, v252\n"
    "s_sub_u32 s95, s95, s76\n"
    "s_ashr_i32 s95, s95, 4\n"
    "s_min_i32 s95, s95, s74\n"
    "s_max_i32 s72, s95, s75\n"
    "v_cvt_pk_bf16_f32 v18, v132, v133\n"
    "v_cvt_pk_bf16_f32 v19, v134, v135\n"
    "v_mfma_f32_4x4x4_16b_bf16 v[128:131], v[184:185], v[56:57], v[208:211] cbsz:2 abid:0\n"
    "v_cvt_pk_bf16_f32 v20, v136, v137\n"
    "v_cvt_pk_bf16_f32 v21, v138, v139\n"
    "v_mfma_f32_4x4x4_16b_bf16 v[132:135], v[184:185], v[58:59], v[212:215] cbsz:2 abid:1\n"
    "v_cvt_pk_bf16_f32 v22, v140, v141\n"
    "v_cvt_pk_bf16_f32 v23, v142, v143\n"
    "v_mfma_f32_4x4x4_16b_bf16 v[136:139], v[184:185], v[60:61], v[216:219] cbsz:2 abid:2\n"
    "v_cvt_pk_bf16_f32 v24, v128, v129\n"
    "v_cvt_pk_bf16_f32 v25, v130, v131\n"
    "v_mfma_f32_4x4x4_16b_bf16 v[140:143], v[184:185], v[62:63], v[220:223] cbsz:2 abid:3\n"
    "v_cvt_pk_bf16_f32 v26, v132, v133\n"
    "v_cvt_pk_bf16_f32 v27, v134, v135\n"
    "v_cvt_pk_bf16_f32 v28, v136, v137\n"
    "v_cvt_pk_bf16_f32 v29, v138, v139\n"
    "v_exp_f32 v152, v152 clamp\n"
    "v_cvt_pk_bf16_f32 v30, v140, v141\n"
    "v_cvt_pk_bf16_f32 v31, v142, v143\n"
    "v_mfma_f32_16x16x32_bf16 v[128:131], v[0:3], v[160:163], v[112:115]\n"
    "v_exp_f32 v144, v144 clamp\n"
    "v_mfma_f32_16x16x32_bf16 v[128:131], v[4:7], v[164:167], v[128:131]\n"
    "v_exp_f32 v145, v145 clamp\n"
    "v_mfma_f32_16x16x32_bf16 v[132:135], v[8:11], v[160:163], v[116:119]\n"
    "v_exp_f32 v146, v146 clamp\n"
    "v_mfma_f32_16x16x32_bf16 v[132:135], v[12:15], v[164:167], v[132:135]\n"
    "v_exp_f32 v147, v147 clamp\n"
    "v_mfma_f32_16x16x32_bf16 v[136:139], v[16:19], v[160:163], v[120:123]\n"
    "v_exp_f32 v148, v148 clamp\n"
    "v_mfma_f32_16x16x32_bf16 v[136:139], v[20:23], v[164:167], v[136:139]\n"
    "v_exp_f32 v149, v149 clamp\n"
    "v_mfma_f32_16x16x32_bf16 v[140:143], v[24:27], v[160:163], v[124:127]\n"
    "v_exp_f32 v150, v150 clamp\n"
    "v_mfma_f32_16x16x32_bf16 v[140:143], v[28:31], v[164:167], v[140:143]\n"
    "v_exp_f32 v151, v151 clamp\n"
    "s_add_u32 m0, s68, 0\n"
    "v_exp_f32 v153, v153 clamp\n"
    "v_add_u32_e64 v253, s67, v244\n"
    "v_mfma_f32_4x4x4_16b_bf16 v[224:227], v[228:229], v[168:169], v[224:227] cbsz:4 abid:0\n"
    "v_exp_f32 v154, v154 clamp\n"
    "v_exp_f32 v155, v155 clamp\n"
    "v_mfma_f32_4x4x4_16b_bf16 v[224:227], v[228:229], v[170:171], v[224:227] cbsz:4 abid:1\n"
    "v_exp_f32 v156, v156 clamp\n"
    "v_exp_f32 v157, v157 clamp\n"
    "v_mfma_f32_4x4x4_16b_bf16 v[224:227], v[228:229], v[172:173], v[224:227] cbsz:4 abid:2\n"
    "v_exp_f32 v158, v158 clamp\n"
    "v_exp_f32 v159, v159 clamp\n"
    "v_mfma_f32_4x4x4_16b_bf16 v[224:227], v[228:229], v[174:175], v[224:227] cbsz:4 abid:3\n"
    "s_waitcnt vmcnt(4)\n"
    "ds_read_b64_tr_b16 v[168:169], v240 offset:0\n"
    "ds_read_b64_tr_b16 v[170:171], v241 offset:256\n"
    "ds_read_b64_tr_b16 v[172:173], v242 offset:512\n"
    "ds_read_b64_tr_b16 v[174:175], v243 offset:768\n"
    "ds_read_b32 v236, v253 offset:0\n"
    "ds_read_b32 v238, v253 offset:4\n"
    "ds_read_b128 v[160:163], v234 offset:2048\n"
    "ds_read_b128 v[164:167], v235 offset:2048\n"
    "s_waitcnt lgkmcnt(8)\n"
    "s_nop 0\n"
    "v_readlane_b32 s20, v252, 0\n"
    "v_readlane_b32 s21, v252, 1\n"
    "v_readlane_b32 s22, v252, 2\n"
    "v_readlane_b32 s23, v252, 3\n"
    "v_readlane_b32 s24, v252, 4\n"
    "v_readlane_b32 s25, v252, 5\n"
    "v_readlane_b32 s26, v252, 6\n"
    "v_readlane_b32 s27, v252, 7\n"
    "v_pk_fma_f32 v[144:145], v[144:145], s[70:71], v[96:97]\n"
    "v_pk_fma_f32 v[146:147], v[146:147], s[70:71], v[98:99]\n"
    "v_pk_add_f32 v[230:231], v[144:145], v[146:147]\n"
    "v_pk_mul_f32 v[232:233], v[144:145], v[146:147]\n"
    "v_pk_fma_f32 v[148:149], v[148:149], s[70:71], v[100:101]\n"
    "v_pk_fma_f32 v[230:231], v[230:231], v[148:149], v[232:233]\n"
    "v_pk_mul_f32 v[232:233], v[232:233], v[148:149]\n"
    "v_pk_fma_f32 v[150:151], v[150:151], s[70:71], v[102:103]\n"
    "v_pk_fma_f32 v[230:231], v[230:231], v[150:151], v[232:233]\n"
    "v_pk_mul_f32 v[232:233], v[232:233], v[150:151]\n"
    "v_pk_fma_f32 v[152:153], v[152:153], s[70:71], v[104:105]\n"
    "v_pk_fma_f32 v[230:231], v[230:231], v[152:153], v[232:233]\n"
    "v_pk_mul_f32 v[232:233], v[232:233], v[152:153]\n"
    "v_pk_fma_f32 v[154:155], v[154:155], s[70:71], v[106:107]\n"
    "v_pk_fma_f32 v[230:231], v[230:231], v[154:155], v[232:233]\n"
    "v_pk_mul_f32 v[232:233], v[232:233], v[154:155]\n"
    "v_pk_fma_f32 v[156:157], v[156:157], s[70:71], v[108:109]\n"
    "v_pk_fma_f32 v[230:231], v[230:231], v[156:157], v[232:233]\n"
    "v_pk_mul_f32 v[232:233], v[232:233], v[156:157]\n"
    "v_pk_fma_f32 v[158:159], v[158:159], s[70:71], v[110:111]\n"
    "v_pk_fma_f32 v[230:231], v[230:231], v[158:159], v[232:233]\n"
    "v_pk_mul_f32 v[232:233], v[232:233], v[158:159]\n"
    "v_rcp_f32 v232, v232\n"
    "v_rcp_f32 v233, v233\n"
    "v_fma_f32 v230, v230, v232, v247\n"
    "v_fmac_f32 v230, v231, v233\n"
    "v_cvt_pk_bf16_f32 v230, v230, v230\n"
    "s_add_u32 s67, s67, 64\n"
    "s_waitcnt lgkmcnt(0)\n"
    "ds_write_b16 v245, v230\n"
    "ds_read_b64 v[228:229], v246\n"
    "s_lshl1_add_u32 s76, s72, s76\n"
    "buffer_load_dwordx4 v[236:237], s[40:43], 0 idxen offen offset:0 nt lds\n"
    "buffer_load_dwordx4 v[238:239], s[40:43], 0 idxen offen offset:1024 nt lds\n"
    "s_and_b32 s62, s58, 63\n"
    "s_mul_i32 s62, s62, 272\n"
    "s_mov_b32 s58, s59\n"
    "s_mov_b32 s59, s60\n"
    "s_mov_b32 s81, s65\n"
    "s_xor_b32 s65, s65, s64\n"
    "s_mov_b32 s80, s83\n"
    "s_add_u32 s82, s65, 256\n"
    "s_cmp_lt_i32 s59, s57\n"
    "s_cselect_b32 s76, s76, -1\n"
    "s_bitcmp1_b32 s58, 30\n"
    "s_cbranch_scc1 .Ldin_A0s\n"
    "s_branch .Ldin_A0\n"
    ".Ldin_L3_poll:\n"
    "s_sleep 1\n"
    "ds_read_b64 v[232:233], v253\n"
    "s_waitcnt lgkmcnt(0)\n"
    "v_readfirstlane_b32 s60, v232\n"
    "s_cmp_eq_u32 s60, -1\n"
    "s_cbranch_scc1 .Ldin_L3_poll\n"
    "s_branch .Ldin_L3_pr\n"
    ".p2align 5\n"
    ".Ldin_stub_0:\n"
    "s_cbranch_execz .Ldin_t0_ret\n"
    "s_nop 0\n"
    ".Ldin_E0:\n"
    "v_exp_f32 v128, v128 clamp\n"
    "v_exp_f32 v129, v129 clamp\n"
    "v_exp_f32 v130, v130 clamp\n"
    "v_exp_f32 v131, v131 clamp\n"
    "v_exp_f32 v132, v132 clamp\n"
    "v_exp_f32 v133, v133 clamp\n"
    "v_exp_f32 v134, v134 clamp\n"
    "v_exp_f32 v135, v135 clamp\n"
    "s_and_b32 s62, s58, 63\n"
    "s_mul_i32 s62, s62, 272\n"
    "s_waitcnt lgkmcnt(0)\n"
    "v_mfma_f32_4x4x4_16b_bf16 v[224:227], v[228:229], v[176:177], v[224:227] cbsz:4 abid:0\n"
    "v_exp_f32 v136, v136 clamp\n"
    "v_exp_f32 v137, v137 clamp\n"
    "v_mfma_f32_4x4x4_16b_bf16 v[224:227], v[228:229], v[178:179], v[224:227] cbsz:4 abid:1\n"
    "v_exp_f32 v138, v138 clamp\n"
    "v_exp_f32 v139, v139 clamp\n"
    "v_mfma_f32_4x4x4_16b_bf16 v[224:227], v[228:229], v[180:181], v[224:227] cbsz:4 abid:2\n"
    "v_exp_f32 v140, v140 clamp\n"
    "v_exp_f32 v141, v141 clamp\n"
    "v_mfma_f32_4x4x4_16b_bf16 v[224:227], v[228:229], v[182:183], v[224:227] cbsz:4 abid:3\n"
    "v_exp_f32 v142, v142 clamp\n"
    "v_exp_f32 v143, v143 clamp\n"
    "v_pk_fma_f32 v[128:129], v[128:129], s[70:71], v[96:97]\n"
    "v_pk_fma_f32 v[130:131], v[130:131], s[70:71], v[98:99]\n"
    "v_pk_add_f32 v[230:231], v[128:129], v[130:131]\n"
    "v_pk_mul_f32 v[232:233], v[128:129], v[130:131]\n"
    "v_pk_fma_f32 v[132:133], v[132:133], s[70:71], v[100:101]\n"
    "v_pk_fma_f32 v[230:231], v[230:231], v[132:133], v[232:233]\n"
    "v_pk_mul_f32 v[232:233], v[232:233], v[132:133]\n"
    "v_pk_fma_f32 v[134:135], v[134:135], s[70:71], v[102:103]\n"
    "v_pk_fma_f32 v[230:231], v[230:231], v[134:135], v[232:233]\n"
    "v_pk_mul_f32 v[232:233], v[232:233], v[134:135]\n"
    "v_pk_fma_f32 v[136:137], v[136:137], s[70:71], v[104:105]\n"
    "v_pk_fma_f32 v[230:231], v[230:231], v[136:137], v[232:233]\n"
    "v_pk_mul_f32 v[232:233], v[232:233], v[136:137]\n"
    "v_pk_fma_f32 v[138:139], v[138:139], s[70:71], v[106:107]\n"
    "v_pk_fma_f32 v[230:231], v[230:231], v[138:139], v[232:233]\n"
    "v_pk_mul_f32 v[232:233], v[232:233], v[138:139]\n"
    "v_pk_fma_f32 v[140:141], v[140:141], s[70:71], v[108:109]\n"
    "v_pk_fma_f32 v[230:231], v[230:231], v[140:141], v[232:233]\n"
    "v_pk_mul_f32 v[232:233], v[232:233], v[140:141]\n"
    "v_pk_fma_f32 v[142:143], v[142:143], s[70:71], v[110:111]\n"
    "v_pk_fma_f32 v[230:231], v[230:231], v[142:143], v[232:233]\n"
    "v_pk_mul_f32 v[232:233], v[232:233], v[142:143]\n"
    "v_rcp_f32 v232, v232\n"
    "v_rcp_f32 v233, v233\n"
    "v_fma_f32 v230, v230, v232, v247\n"
    "v_fmac_f32 v230, v231, v233\n"
    "v_cvt_pk_bf16_f32 v230, v230, v230\n"
    "ds_write_b16 v245, v230\n"
    "ds_read_b64 v[228:229], v246\n"
    ".Ldin_drain0:\n"
    "v_add_u32 v253, s66, v244\n"
    "ds_read_b32 v253, v253\n"
    "v_lshlrev_b32 v202, 3, v249\n"
    "s_mov_b32 m0, s62\n"
    "s_mov_b32 s70, s69\n"
    "s_mov_b32 s71, s69\n"
    "s_waitcnt lgkmcnt(0)\n"
    "v_mfma_f32_4x4x4_16b_bf16 v[224:227], v[228:229], v[168:169], v[224:227] cbsz:4 abid:0\n"
    "v_and_b32 v201, 63, v253\n"
    "v_and_b32 v202, 0x70, v202\n"
    "v_mfma_f32_4x4x4_16b_bf16 v[224:227], v[228:229], v[170:171], v[224:227] cbsz:4 abid:1\n"
    "v_and_b32 v200, 0x3fffffc0, v253\n"
    "v_mul_u32_u24 v201, 272, v201\n"
    "v_mfma_f32_4x4x4_16b_bf16 v[224:227], v[228:229], v[172:173], v[224:227] cbsz:4 abid:2\n"
    "v_lshl_add_u32 v200, v200, 1, v202\n"
    "v_lshl_add_u32 v201, v202, 1, v201\n"
    "v_mfma_f32_4x4x4_16b_bf16 v[224:227], v[228:229], v[174:175], v[224:227] cbsz:4 abid:3\n"
    "v_cmp_gt_u32 vcc, s57, v253\n"
    "v_bfrev_b32 v202, 1\n"
    "s_nop 0\n"
    "v_cndmask_b32 v200, v202, v200, vcc\n"
    "s_nop 0\n"
    "v_pk_add_f32 v[232:233], v[224:225], v[226:227]\n"
    "v_add_f32 v232, v232, v233\n"
    "ds_write_addtid_b32 v232 offset:32768\n"
    "ds_read_b128 v[208:211], v201 offset:32768\n"
    "ds_read_b128 v[212:215], v201 offset:32784\n"
    "s_waitcnt lgkmcnt(1)\n"
    "v_pk_mul_f32 v[208:209], v[208:209], s[70:71]\n"
    "v_pk_mul_f32 v[210:211], v[210:211], s[70:71]\n"
    "v_cvt_pk_bf16_f32 v216, v208, v209\n"
    "v_cvt_pk_bf16_f32 v217, v210, v211\n"
    "s_waitcnt lgkmcnt(0)\n"
    "v_pk_mul_f32 v[212:213], v[212:213], s[70:71]\n"
    "v_pk_mul_f32 v[214:215], v[214:215], s[70:71]\n"
    "v_cvt_pk_bf16_f32 v218, v212, v213\n"
    "v_cvt_pk_bf16_f32 v219, v214, v215\n"
    "s_waitcnt vmcnt(0)\n"
    "buffer_store_dwordx4 v[216:219], v200, s[52:55], 0 offen sc0 sc1 nt\n"
    "s_endpgm\n"
    ".p2align 5\n"
    ".Ldin_stub_1:\n"
    "s_cbranch_execz .Ldin_t0_ret\n"
    "s_nop 0\n"
    ".Ldin_E1:\n"
    "v_exp_f32 v144, v144 clamp\n"
    "v_exp_f32 v145, v145 clamp\n"
    "v_exp_f32 v146, v146 clamp\n"
    "v_exp_f32 v147, v147 clamp\n"
    "v_exp_f32 v148, v148 clamp\n"
    "v_exp_f32 v149, v149 clamp\n"
    "v_exp_f32 v150, v150 clamp\n"
    "v_exp_f32 v151, v151 clamp\n"
    "s_and_b32 s62, s58, 63\n"
    "s_mul_i32 s62, s62, 272\n"
    "s_waitcnt lgkmcnt(0)\n"
    "v_mfma_f32_4x4x4_16b_bf16 v[224:227], v[228:229], v[168:169], v[224:227] cbsz:4 abid:0\n"
    "v_exp_f32 v152, v152 clamp\n"
    "v_exp_f32 v153, v153 clamp\n"
    "v_mfma_f32_4x4x4_16b_bf16 v[224:227], v[228:229], v[170:171], v[224:227] cbsz:4 abid:1\n"
    "v_exp_f32 v154, v154 clamp\n"
    "v_exp_f32 v155, v155 clamp\n"
    "v_mfma_f32_4x4x4_16b_bf16 v[224:227], v[228:229], v[172:173], v[224:227] cbsz:4 abid:2\n"
    "v_exp_f32 v156, v156 clamp\n"
    "v_exp_f32 v157, v157 clamp\n"
    "v_mfma_f32_4x4x4_16b_bf16 v[224:227], v[228:229], v[174:175], v[224:227] cbsz:4 abid:3\n"
    "v_exp_f32 v158, v158 clamp\n"
    "v_exp_f32 v159, v159 clamp\n"
    "v_pk_fma_f32 v[144:145], v[144:145], s[70:71], v[96:97]\n"
    "v_pk_fma_f32 v[146:147], v[146:147], s[70:71], v[98:99]\n"
    "v_pk_add_f32 v[230:231], v[144:145], v[146:147]\n"
    "v_pk_mul_f32 v[232:233], v[144:145], v[146:147]\n"
    "v_pk_fma_f32 v[148:149], v[148:149], s[70:71], v[100:101]\n"
    "v_pk_fma_f32 v[230:231], v[230:231], v[148:149], v[232:233]\n"
    "v_pk_mul_f32 v[232:233], v[232:233], v[148:149]\n"
    "v_pk_fma_f32 v[150:151], v[150:151], s[70:71], v[102:103]\n"
    "v_pk_fma_f32 v[230:231], v[230:231], v[150:151], v[232:233]\n"
    "v_pk_mul_f32 v[232:233], v[232:233], v[150:151]\n"
    "v_pk_fma_f32 v[152:153], v[152:153], s[70:71], v[104:105]\n"
    "v_pk_fma_f32 v[230:231], v[230:231], v[152:153], v[232:233]\n"
    "v_pk_mul_f32 v[232:233], v[232:233], v[152:153]\n"
    "v_pk_fma_f32 v[154:155], v[154:155], s[70:71], v[106:107]\n"
    "v_pk_fma_f32 v[230:231], v[230:231], v[154:155], v[232:233]\n"
    "v_pk_mul_f32 v[232:233], v[232:233], v[154:155]\n"
    "v_pk_fma_f32 v[156:157], v[156:157], s[70:71], v[108:109]\n"
    "v_pk_fma_f32 v[230:231], v[230:231], v[156:157], v[232:233]\n"
    "v_pk_mul_f32 v[232:233], v[232:233], v[156:157]\n"
    "v_pk_fma_f32 v[158:159], v[158:159], s[70:71], v[110:111]\n"
    "v_pk_fma_f32 v[230:231], v[230:231], v[158:159], v[232:233]\n"
    "v_pk_mul_f32 v[232:233], v[232:233], v[158:159]\n"
    "v_rcp_f32 v232, v232\n"
    "v_rcp_f32 v233, v233\n"
    "v_fma_f32 v230, v230, v232, v247\n"
    "v_fmac_f32 v230, v231, v233\n"
    "v_cvt_pk_bf16_f32 v230, v230, v230\n"
    "ds_write_b16 v245, v230\n"
    "ds_read_b64 v[228:229], v246\n"
    ".Ldin_drain1:\n"
    "v_add_u32 v253, s66, v244\n"
    "ds_read_b32 v253, v253\n"
    "v_lshlrev_b32 v202, 3, v249\n"
    "s_mov_b32 m0, s62\n"
    "s_mov_b32 s70, s69\n"
    "s_mov_b32 s71, s69\n"
    "s_waitcnt lgkmcnt(0)\n"
    "v_mfma_f32_4x4x4_16b_bf16 v[224:227], v[228:229], v[176:177], v[224:227] cbsz:4 abid:0\n"
    "v_and_b32 v201, 63, v253\n"
    "v_and_b32 v202, 0x70, v202\n"
    "v_mfma_f32_4x4x4_16b_bf16 v[224:227], v[228:229], v[178:179], v[224:227] cbsz:4 abid:1\n"
    "v_and_b32 v200, 0x3fffffc0, v253\n"
    "v_mul_u32_u24 v201, 272, v201\n"
    "v_mfma_f32_4x4x4_16b_bf16 v[224:227], v[228:229], v[180:181], v[224:227] cbsz:4 abid:2\n"
    "v_lshl_add_u32 v200, v200, 1, v202\n"
    "v_lshl_add_u32 v201, v202, 1, v201\n"
    "v_mfma_f32_4x4x4_16b_bf16 v[224:227], v[228:229], v[182:183], v[224:227] cbsz:4 abid:3\n"
    "v_cmp_gt_u32 vcc, s57, v253\n"
    "v_bfrev_b32 v202, 1\n"
    "s_nop 0\n"
    "v_cndmask_b32 v200, v202, v200, vcc\n"
    "s_nop 0\n"
    "v_pk_add_f32 v[232:233], v[224:225], v[226:227]\n"
    "v_add_f32 v232, v232, v233\n"
    "ds_write_addtid_b32 v232 offset:32768\n"
    "ds_read_b128 v[208:211], v201 offset:32768\n"
    "ds_read_b128 v[212:215], v201 offset:32784\n"
    "s_waitcnt lgkmcnt(1)\n"
    "v_pk_mul_f32 v[208:209], v[208:209], s[70:71]\n"
    "v_pk_mul_f32 v[210:211], v[210:211], s[70:71]\n"
    "v_cvt_pk_bf16_f32 v216, v208, v209\n"
    "v_cvt_pk_bf16_f32 v217, v210, v211\n"
    "s_waitcnt lgkmcnt(0)\n"
    "v_pk_mul_f32 v[212:213], v[212:213], s[70:71]\n"
    "v_pk_mul_f32 v[214:215], v[214:215], s[70:71]\n"
    "v_cvt_pk_bf16_f32 v218, v212, v213\n"
    "v_cvt_pk_bf16_f32 v219, v214, v215\n"
    "s_waitcnt vmcnt(0)\n"
    "buffer_store_dwordx4 v[216:219], v200, s[52:55], 0 offen sc0 sc1 nt\n"
    "s_endpgm\n"
    ".Ldin_A0s:\n"
    "v_mfma_f32_16x16x32_bf16 v[144:147], v[0:3], v[160:163], v[112:115]\n"
    "v_exp_f32 v128, v128 clamp\n"
    "v_mfma_f32_16x16x32_bf16 v[144:147], v[4:7], v[164:167], v[144:147]\n"
    "v_exp_f32 v129, v129 clamp\n"
    "v_mfma_f32_16x16x32_bf16 v[148:151], v[8:11], v[160:163], v[116:119]\n"
    "v_exp_f32 v130, v130 clamp\n"
    "v_mfma_f32_16x16x32_bf16 v[148:151], v[12:15], v[164:167], v[148:151]\n"
    "v_exp_f32 v131, v131 clamp\n"
    "v_mfma_f32_16x16x32_bf16 v[152:155], v[16:19], v[160:163], v[120:123]\n"
    "v_exp_f32 v132, v132 clamp\n"
    "v_mfma_f32_16x16x32_bf16 v[152:155], v[20:23], v[164:167], v[152:155]\n"
    "v_exp_f32 v133, v133 clamp\n"
    "v_mfma_f32_16x16x32_bf16 v[156:159], v[24:27], v[160:163], v[124:127]\n"
    "v_exp_f32 v134, v134 clamp\n"
    "v_mfma_f32_16x16x32_bf16 v[156:159], v[28:31], v[164:167], v[156:159]\n"
    "v_exp_f32 v135, v135 clamp\n"
    "v_exp_f32 v136, v136 clamp\n"
    "s_mov_b32 m0, s62\n"
    "v_exp_f32 v137, v137 clamp\n"
    "v_add_u32 v253, s67, v244\n"
    "s_waitcnt lgkmcnt(0)\n"
    "v_mfma_f32_4x4x4_16b_bf16 v[224:227], v[228:229], v[176:177], v[224:227] cbsz:4 abid:0\n"
    "v_exp_f32 v138, v138 clamp\n"
    "v_exp_f32 v139, v139 clamp\n"
    "v_mfma_f32_4x4x4_16b_bf16 v[224:227], v[228:229], v[178:179], v[224:227] cbsz:4 abid:1\n"
    "v_exp_f32 v140, v140 clamp\n"
    "v_exp_f32 v141, v141 clamp\n"
    "v_mfma_f32_4x4x4_16b_bf16 v[224:227], v[228:229], v[180:181], v[224:227] cbsz:4 abid:2\n"
    "v_exp_f32 v142, v142 clamp\n"
    "v_exp_f32 v143, v143 clamp\n"
    "v_mfma_f32_4x4x4_16b_bf16 v[224:227], v[228:229], v[182:183], v[224:227] cbsz:4 abid:3\n"
    "s_waitcnt vmcnt(4)\n"
    "ds_read_b64_tr_b16 v[176:177], v240 offset:2048\n"
    "ds_read_b64_tr_b16 v[178:179], v241 offset:2304\n"
    "ds_read_b64_tr_b16 v[180:181], v242 offset:2560\n"
    "ds_read_b64_tr_b16 v[182:183], v243 offset:2816\n"
    "ds_read_b32 v236, v253 offset:0\n"
    "ds_read_b32 v238, v253 offset:4\n"
    "ds_read_b128 v[160:163], v234 offset:4096\n"
    "ds_read_b128 v[164:167], v235 offset:4096\n"
    "s_cbranch_vccz .Ldin_A0s_np\n"
    "s_setprio 1\n"
    ".Ldin_A0s_np:\n"
    "v_pk_fma_f32 v[128:129], v[128:129], s[70:71], v[96:97]\n"
    "v_pk_fma_f32 v[130:131], v[130:131], s[70:71], v[98:99]\n"
    "v_pk_add_f32 v[190:191], v[224:225], v[226:227]\n"
    "v_add_f32 v190, v190, v191\n"
    "ds_write_addtid_b32 v190 offset:32768\n"
    "s_add_u32 m0, s68, 2048\n"
    "s_bfe_u32 s94, s59, 0x180006\n"
    "s_mul_i32 s86, s94, s56\n"
    "v_mbcnt_lo_u32_b32 v184, s20, 0\n"
    "v_mbcnt_lo_u32_b32 v185, s22, 0\n"
    "v_mbcnt_lo_u32_b32 v186, s24, 0\n"
    "v_mbcnt_lo_u32_b32 v187, s26, 0\n"
    "s_bcnt1_i32_b64 s90, s[20:21]\n"
    "s_bcnt1_i32_b64 s91, s[22:23]\n"
    "s_bcnt1_i32_b64 s92, s[24:25]\n"
    "s_bcnt1_i32_b64 s93, s[26:27]\n"
    "v_mbcnt_hi_u32_b32 v184, s21, v184\n"
    "v_mbcnt_hi_u32_b32 v185, s23, v185\n"
    "v_mbcnt_hi_u32_b32 v186, s25, v186\n"
    "v_mbcnt_hi_u32_b32 v187, s27, v187\n"
    "s_lshl2_add_u32 s90, s90, s65\n"
    "s_lshl2_add_u32 s91, s91, s90\n"
    "s_lshl2_add_u32 s92, s92, s91\n"
    "s_lshl2_add_u32 s93, s93, s92\n"
    "v_add_u32_e64 v188, s86, v251\n"
    "v_pk_add_f32 v[230:231], v[128:129], v[130:131]\n"
    "v_pk_mul_f32 v[232:233], v[128:129], v[130:131]\n"
    "v_pk_fma_f32 v[132:133], v[132:133], s[70:71], v[100:101]\n"
    "v_pk_fma_f32 v[230:231], v[230:231], v[132:133], v[232:233]\n"
    "v_pk_mul_f32 v[232:233], v[232:233], v[132:133]\n"
    "v_lshl_add_u32 v184, v184, 2, s65\n"
    "v_lshl_add_u32 v185, v185, 2, s90\n"
    "v_lshl_add_u32 v186, v186, 2, s91\n"
    "v_lshl_add_u32 v187, v187, 2, s92\n"
    "v_mov_b32 v191, -1\n"
    "v_lshl_add_u32 v190, v249, 1, s93\n"
    "ds_write2_b32 v190, v191, v191 offset1:64\n"
    "s_mov_b64 exec, s[20:21]\n"
    "ds_write_b32 v184, v188\n"
    "s_mov_b64 exec, s[22:23]\n"
    "v_add_u32 v189, 64, v188\n"
    "ds_write_b32 v185, v189\n"
    "s_mov_b64 exec, s[24:25]\n"
    "v_add_u32 v190, 128, v188\n"
    "ds_write_b32 v186, v190\n"
    "s_mov_b64 exec, s[26:27]\n"
    "v_add_u32 v189, 192, v188\n"
    "ds_write_b32 v187, v189\n"
    "s_mov_b64 exec, -1\n"
    "s_add_u32 s95, s93, 60\n"
    "s_add_u32 s94, s82, 128\n"
    "s_andn2_b32 s95, s95, 63\n"
    "s_max_u32 s83, s95, s94\n"
    "v_pk_fma_f32 v[134:135], v[134:135], s[70:71], v[102:103]\n"
    "v_pk_fma_f32 v[230:231], v[230:231], v[134:135], v[232:233]\n"
    "v_pk_mul_f32 v[232:233], v[232:233], v[134:135]\n"
    "v_pk_fma_f32 v[136:137], v[136:137], s[70:71], v[104:105]\n"
    "v_pk_fma_f32 v[230:231], v[230:231], v[136:137], v[232:233]\n"
    "v_pk_mul_f32 v[232:233], v[232:233], v[136:137]\n"
    "v_pk_fma_f32 v[138:139], v[138:139], s[70:71], v[106:107]\n"
    "v_pk_fma_f32 v[230:231], v[230:231], v[138:139], v[232:233]\n"
    "v_pk_mul_f32 v[232:233], v[232:233], v[138:139]\n"
    "v_pk_fma_f32 v[140:141], v[140:141], s[70:71], v[108:109]\n"
    "v_pk_fma_f32 v[230:231], v[230:231], v[140:141], v[232:233]\n"
    "v_pk_mul_f32 v[232:233], v[232:233], v[140:141]\n"
    "v_pk_fma_f32 v[142:143], v[142:143], s[70:71], v[110:111]\n"
    "v_pk_fma_f32 v[230:231], v[230:231], v[142:143], v[232:233]\n"
    "v_pk_mul_f32 v[232:233], v[232:233], v[142:143]\n"
    "v_rcp_f32 v232, v232\n"
    "v_rcp_f32 v233, v233\n"
    "v_fma_f32 v230, v230, v232, v247\n"
    "v_fmac_f32 v230, v231, v233\n"
    "v_lshlrev_b32 v232, 2, v249\n"
    "v_lshrrev_b32 v233, 2, v249\n"
    "v_and_b32 v232, 56, v232\n"
    "v_and_b32 v233, 4, v233\n"
    "v_add3_u32 v232, v232, v233, s81\n"
    "ds_read_b32 v232, v232\n"
    "s_add_u32 s81, s81, 64\n"
    "s_waitcnt lgkmcnt(0)\n"
    "v_lshlrev_b32 v232, 1, v232\n"
    "buffer_load_ushort v232, v232, s[44:47], 0 offen\n"
    "s_waitcnt vmcnt(0)\n"
    "v_lshlrev_b32 v232, 16, v232\n"
    "v_mul_f32 v230, v230, v232\n"
    "v_cvt_pk_bf16_f32 v230, v230, v230\n"
    "s_add_u32 s67, s67, 64\n"
    "s_waitcnt lgkmcnt(0)\n"
    "ds_write_b16 v245, v230\n"
    "ds_read_b64 v[228:229], v246\n"
    "buffer_load_dwordx4 v[236:237], s[40:43], 0 idxen offen offset:0 nt lds\n"
    "buffer_load_dwordx4 v[238:239], s[40:43], 0 idxen offen offset:1024 nt lds\n"
    ".Ldin_B1s:\n"
    "v_mfma_f32_16x16x32_bf16 v[128:131], v[0:3], v[160:163], v[112:115]\n"
    "v_exp_f32 v144, v144 clamp\n"
    "v_mfma_f32_16x16x32_bf16 v[128:131], v[4:7], v[164:167], v[128:131]\n"
    "v_exp_f32 v145, v145 clamp\n"
    "v_mfma_f32_16x16x32_bf16 v[132:135], v[8:11], v[160:163], v[116:119]\n"
    "v_exp_f32 v146, v146 clamp\n"
    "v_mfma_f32_16x16x32_bf16 v[132:135], v[12:15], v[164:167], v[132:135]\n"
    "v_exp_f32 v147, v147 clamp\n"
    "v_mfma_f32_16x16x32_bf16 v[136:139], v[16:19], v[160:163], v[120:123]\n"
    "v_exp_f32 v148, v148 clamp\n"
    "v_mfma_f32_16x16x32_bf16 v[136:139], v[20:23], v[164:167], v[136:139]\n"
    "v_exp_f32 v149, v149 clamp\n"
    "v_mfma_f32_16x16x32_bf16 v[140:143], v[24:27], v[160:163], v[124:127]\n"
    "v_exp_f32 v150, v150 clamp\n"
    "v_mfma_f32_16x16x32_bf16 v[140:143], v[28:31], v[164:167], v[140:143]\n"
    "v_exp_f32 v151, v151 clamp\n"
    "v_exp_f32 v152, v152 clamp\n"
    "s_cmp_eq_u32 s67, s80\n"
    "s_cselect_b32 s67, s65, s67\n"
    "s_and_b32 s94, s59, 63\n"
    "s_mul_i32 s94, s94, 272\n"
    "v_add_u32 v252, s94, v250\n"
    "s_add_u32 m0, s68, 4096\n"
    "v_exp_f32 v153, v153 clamp\n"
    "v_add_u32 v253, s67, v244\n"
    "s_waitcnt lgkmcnt(0)\n"
    "v_mfma_f32_4x4x4_16b_bf16 v[224:227], v[228:229], v[168:169], 0 cbsz:4 abid:0\n"
    "v_exp_f32 v154, v154 clamp\n"
    "v_exp_f32 v155, v155 clamp\n"
    "v_mfma_f32_4x4x4_16b_bf16 v[224:227], v[228:229], v[170:171], v[224:227] cbsz:4 abid:1\n"
    "v_exp_f32 v156, v156 clamp\n"
    "v_exp_f32 v157, v157 clamp\n"
    "v_mfma_f32_4x4x4_16b_bf16 v[224:227], v[228:229], v[172:173], v[224:227] cbsz:4 abid:2\n"
    "v_exp_f32 v158, v158 clamp\n"
    "v_exp_f32 v159, v159 clamp\n"
    "v_mfma_f32_4x4x4_16b_bf16 v[224:227], v[228:229], v[174:175], v[224:227] cbsz:4 abid:3\n"
    "s_waitcnt vmcnt(4)\n"
    "s_memrealtime s[34:35]\n"
    "ds_read_b64_tr_b16 v[168:169], v240 offset:4096\n"
    "ds_read_b64_tr_b16 v[170:171], v241 offset:4352\n"
    "ds_read_b64_tr_b16 v[172:173], v242 offset:4608\n"
    "ds_read_b64_tr_b16 v[174:175], v243 offset:4864\n"
    "ds_read_b32 v236, v253 offset:0\n"
    "ds_read_b32 v238, v253 offset:4\n"
    "ds_read_b128 v[160:163], v234 offset:6144\n"
    "ds_read_b128 v[164:167], v235 offset:6144\n"
    "v_add_u32_e64 v184, v248, v252\n"
    "ds_read_b64 v[184:185], v184\n"
    "v_pk_fma_f32 v[144:145], v[144:145], s[70:71], v[96:97]\n"
    "v_pk_fma_f32 v[146:147], v[146:147], s[70:71], v[98:99]\n"
    "v_pk_add_f32 v[230:231], v[144:145], v[146:147]\n"
    "v_pk_mul_f32 v[232:233], v[144:145], v[146:147]\n"
    "v_pk_fma_f32 v[148:149], v[148:149], s[70:71], v[100:101]\n"
    "v_pk_fma_f32 v[230:231], v[230:231], v[148:149], v[232:233]\n"
    "v_pk_mul_f32 v[232:233], v[232:233], v[148:149]\n"
    "v_pk_fma_f32 v[150:151], v[150:151], s[70:71], v[102:103]\n"
    "v_pk_fma_f32 v[230:231], v[230:231], v[150:151], v[232:233]\n"
    "v_pk_mul_f32 v[232:233], v[232:233], v[150:151]\n"
    "v_pk_fma_f32 v[152:153], v[152:153], s[70:71], v[104:105]\n"
    "v_pk_fma_f32 v[230:231], v[230:231], v[152:153], v[232:233]\n"
    "v_pk_mul_f32 v[232:233], v[232:233], v[152:153]\n"
    "v_pk_fma_f32 v[154:155], v[154:155], s[70:71], v[106:107]\n"
    "v_pk_fma_f32 v[230:231], v[230:231], v[154:155], v[232:233]\n"
    "v_pk_mul_f32 v[232:233], v[232:233], v[154:155]\n"
    "v_pk_fma_f32 v[156:157], v[156:157], s[70:71], v[108:109]\n"
    "v_pk_fma_f32 v[230:231], v[230:231], v[156:157], v[232:233]\n"
    "v_pk_mul_f32 v[232:233], v[232:233], v[156:157]\n"
    "v_pk_fma_f32 v[158:159], v[158:159], s[70:71], v[110:111]\n"
    "v_pk_fma_f32 v[230:231], v[230:231], v[158:159], v[232:233]\n"
    "v_pk_mul_f32 v[232:233], v[232:233], v[158:159]\n"
    "v_rcp_f32 v232, v232\n"
    "v_rcp_f32 v233, v233\n"
    "v_fma_f32 v230, v230, v232, v247\n"
    "v_fmac_f32 v230, v231, v233\n"
    "v_lshlrev_b32 v232, 2, v249\n"
    "v_lshrrev_b32 v233, 2, v249\n"
    "v_and_b32 v232, 56, v232\n"
    "v_and_b32 v233, 4, v233\n"
    "v_add3_u32 v232, v232, v233, s81\n"
    "ds_read_b32 v232, v232\n"
    "s_add_u32 s81, s81, 64\n"
    "s_waitcnt lgkmcnt(0)\n"
    "v_lshlrev_b32 v232, 1, v232\n"
    "buffer_load_ushort v232, v232, s[44:47], 0 offen\n"
    "s_waitcnt vmcnt(0)\n"
    "v_lshlrev_b32 v232, 16, v232\n"
    "v_mul_f32 v230, v230, v232\n"
    "v_cvt_pk_bf16_f32 v230, v230, v230\n"
    "s_add_u32 s67, s67, 64\n"
    "s_waitcnt lgkmcnt(0)\n"
    "ds_write_b16 v245, v230\n"
    "ds_read_b64 v[228:229], v246\n"
    "v_and_b32 v184, v254, v184\n"
    "v_and_b32 v185, v255, v185\n"
    "s_cmp_le_u32 s34, s76\n"
    "s_cbranch_scc1 .Ldin_B1s_wait\n"
    ".Ldin_B1s_go:\n"
    "s_sub_u32 s94, s34, 60\n"
    "s_max_u32 s76, s76, s94\n"
    "s_add_u32 s76, s76, s72\n"
    ".Ldin_B1s_dma:\n"
    "buffer_load_dwordx4 v[236:237], s[40:43], 0 idxen offen offset:0 nt lds\n"
    "buffer_load_dwordx4 v[238:239], s[40:43], 0 idxen offen offset:1024 nt lds\n"
    ".Ldin_B1s_nd:\n"
    ".Ldin_G2s:\n"
    "v_mfma_f32_16x16x32_bf16 v[144:147], v[0:3], v[160:163], v[112:115]\n"
    "v_exp_f32 v128, v128 clamp\n"
    "v_mfma_f32_16x16x32_bf16 v[144:147], v[4:7], v[164:167], v[144:147]\n"
    "v_exp_f32 v129, v129 clamp\n"
    "v_mfma_f32_16x16x32_bf16 v[148:151], v[8:11], v[160:163], v[116:119]\n"
    "v_exp_f32 v130, v130 clamp\n"
    "v_mfma_f32_16x16x32_bf16 v[148:151], v[12:15], v[164:167], v[148:151]\n"
    "v_exp_f32 v131, v131 clamp\n"
    "v_mfma_f32_16x16x32_bf16 v[152:155], v[16:19], v[160:163], v[120:123]\n"
    "v_exp_f32 v132, v132 clamp\n"
    "v_mfma_f32_16x16x32_bf16 v[152:155], v[20:23], v[164:167], v[152:155]\n"
    "v_exp_f32 v133, v133 clamp\n"
    "v_mfma_f32_16x16x32_bf16 v[156:159], v[24:27], v[160:163], v[124:127]\n"
    "v_exp_f32 v134, v134 clamp\n"
    "v_mfma_f32_16x16x32_bf16 v[156:159], v[28:31], v[164:167], v[156:159]\n"
    "v_exp_f32 v135, v135 clamp\n"
    "v_exp_f32 v136, v136 clamp\n"
    "s_cmp_eq_u32 s67, s80\n"
    "s_cselect_b32 s67, s65, s67\n"
    "s_add_u32 m0, s68, 6144\n"
    "v_exp_f32 v137, v137 clamp\n"
    "v_add_u32_e64 v253, s67, v244\n"
    "s_waitcnt lgkmcnt(0)\n"
    "v_mfma_f32_4x4x4_16b_bf16 v[224:227], v[228:229], v[176:177], v[224:227] cbsz:4 abid:0\n"
    "v_exp_f32 v138, v138 clamp\n"
    "v_exp_f32 v139, v139 clamp\n"
    "v_mfma_f32_4x4x4_16b_bf16 v[224:227], v[228:229], v[178:179], v[224:227] cbsz:4 abid:1\n"
    "v_exp_f32 v140, v140 clamp\n"
    "v_exp_f32 v141, v141 clamp\n"
    "v_mfma_f32_4x4x4_16b_bf16 v[224:227], v[228:229], v[180:181], v[224:227] cbsz:4 abid:2\n"
    "v_exp_f32 v142, v142 clamp\n"
    "v_exp_f32 v143, v143 clamp\n"
    "v_mfma_f32_4x4x4_16b_bf16 v[224:227], v[228:229], v[182:183], v[224:227] cbsz:4 abid:3\n"
    "s_waitcnt vmcnt(4)\n"
    "s_memrealtime s[34:35]\n"
    "ds_read_b64_tr_b16 v[176:177], v240 offset:6144\n"
    "ds_read_b64_tr_b16 v[178:179], v241 offset:6400\n"
    "ds_read_b64_tr_b16 v[180:181], v242 offset:6656\n"
    "ds_read_b64_tr_b16 v[182:183], v243 offset:6912\n"
    "ds_read_b32 v236, v253 offset:0\n"
    "ds_read_b32 v238, v253 offset:4\n"
    "ds_read_b128 v[160:163], v234 offset:0\n"
    "ds_read_b128 v[164:167], v235 offset:0\n"
    "v_pk_fma_f32 v[128:129], v[128:129], s[70:71], v[96:97]\n"
    "v_pk_fma_f32 v[130:131], v[130:131], s[70:71], v[98:99]\n"
    "v_pk_add_f32 v[230:231], v[128:129], v[130:131]\n"
    "v_pk_mul_f32 v[232:233], v[128:129], v[130:131]\n"
    "v_pk_fma_f32 v[132:133], v[132:133], s[70:71], v[100:101]\n"
    "v_pk_fma_f32 v[230:231], v[230:231], v[132:133], v[232:233]\n"
    "v_pk_mul_f32 v[232:233], v[232:233], v[132:133]\n"
    "v_pk_fma_f32 v[134:135], v[134:135], s[70:71], v[102:103]\n"
    "v_pk_fma_f32 v[230:231], v[230:231], v[134:135], v[232:233]\n"
    "v_pk_mul_f32 v[232:233], v[232:233], v[134:135]\n"
    "v_pk_fma_f32 v[136:137], v[136:137], s[70:71], v[104:105]\n"
    "v_pk_fma_f32 v[230:231], v[230:231], v[136:137], v[232:233]\n"
    "v_pk_mul_f32 v[232:233], v[232:233], v[136:137]\n"
    "v_pk_fma_f32 v[138:139], v[138:139], s[70:71], v[106:107]\n"
    "v_pk_fma_f32 v[230:231], v[230:231], v[138:139], v[232:233]\n"
    "v_pk_mul_f32 v[232:233], v[232:233], v[138:139]\n"
    "v_pk_fma_f32 v[140:141], v[140:141], s[70:71], v[108:109]\n"
    "v_pk_fma_f32 v[230:231], v[230:231], v[140:141], v[232:233]\n"
    "v_pk_mul_f32 v[232:233], v[232:233], v[140:141]\n"
    "v_pk_fma_f32 v[142:143], v[142:143], s[70:71], v[110:111]\n"
    "v_pk_fma_f32 v[230:231], v[230:231], v[142:143], v[232:233]\n"
    "v_pk_mul_f32 v[232:233], v[232:233], v[142:143]\n"
    "v_rcp_f32 v232, v232\n"
    "v_rcp_f32 v233, v233\n"
    "v_fma_f32 v230, v230, v232, v247\n"
    "v_fmac_f32 v230, v231, v233\n"
    "v_lshlrev_b32 v232, 2, v249\n"
    "v_lshrrev_b32 v233, 2, v249\n"
    "v_and_b32 v232, 56, v232\n"
    "v_and_b32 v233, 4, v233\n"
    "v_add3_u32 v232, v232, v233, s81\n"
    "ds_read_b32 v232, v232\n"
    "s_add_u32 s81, s81, 64\n"
    "s_waitcnt lgkmcnt(0)\n"
    "v_lshlrev_b32 v232, 1, v232\n"
    "buffer_load_ushort v232, v232, s[44:47], 0 offen\n"
    "s_waitcnt vmcnt(0)\n"
    "v_lshlrev_b32 v232, 16, v232\n"
    "v_mul_f32 v230, v230, v232\n"
    "v_cvt_pk_bf16_f32 v230, v230, v230\n"
    "s_add_u32 s67, s67, 64\n"
    "s_waitcnt lgkmcnt(0)\n"
    "ds_write_b16 v245, v230\n"
    "ds_read_b64 v[228:229], v246\n"
    "s_cmp_le_u32 s34, s76\n"
    "s_cbranch_scc1 .Ldin_G2s_wait\n"
    ".Ldin_G2s_go:\n"
    "s_sub_u32 s94, s34, 60\n"
    "s_max_u32 s76, s76, s94\n"
    "s_add_u32 s76, s76, s72\n"
    ".Ldin_G2s_dma:\n"
    "buffer_load_dwordx4 v[236:237], s[40:43], 0 idxen offen offset:0 nt lds\n"
    "buffer_load_dwordx4 v[238:239], s[40:43], 0 idxen offen offset:1024 nt lds\n"
    ".Ldin_G2s_nd:\n"
    "s_cmp_eq_u32 s67, s82\n"
    "s_cbranch_scc1 .Ldin_L3s\n"
    ".Ldin_G3s:\n"
    "v_mfma_f32_16x16x32_bf16 v[128:131], v[0:3], v[160:163], v[112:115]\n"
    "v_exp_f32 v144, v144 clamp\n"
    "v_mfma_f32_16x16x32_bf16 v[128:131], v[4:7], v[164:167], v[128:131]\n"
    "v_exp_f32 v145, v145 clamp\n"
    "v_mfma_f32_16x16x32_bf16 v[132:135], v[8:11], v[160:163], v[116:119]\n"
    "v_exp_f32 v146, v146 clamp\n"
    "v_mfma_f32_16x16x32_bf16 v[132:135], v[12:15], v[164:167], v[132:135]\n"
    "v_exp_f32 v147, v147 clamp\n"
    "v_mfma_f32_16x16x32_bf16 v[136:139], v[16:19], v[160:163], v[120:123]\n"
    "v_exp_f32 v148, v148 clamp\n"
    "v_mfma_f32_16x16x32_bf16 v[136:139], v[20:23], v[164:167], v[136:139]\n"
    "v_exp_f32 v149, v149 clamp\n"
    "v_mfma_f32_16x16x32_bf16 v[140:143], v[24:27], v[160:163], v[124:127]\n"
    "v_exp_f32 v150, v150 clamp\n"
    "v_mfma_f32_16x16x32_bf16 v[140:143], v[28:31], v[164:167], v[140:143]\n"
    "v_exp_f32 v151, v151 clamp\n"
    "v_exp_f32 v152, v152 clamp\n"
    "s_cmp_eq_u32 s67, s80\n"
    "s_cselect_b32 s67, s65, s67\n"
    "s_add_u32 m0, s68, 0\n"
    "v_exp_f32 v153, v153 clamp\n"
    "v_add_u32 v253, s67, v244\n"
    "s_waitcnt lgkmcnt(0)\n"
    "v_mfma_f32_4x4x4_16b_bf16 v[224:227], v[228:229], v[168:169], v[224:227] cbsz:4 abid:0\n"
    "v_exp_f32 v154, v154 clamp\n"
    "v_exp_f32 v155, v155 clamp\n"
    "v_mfma_f32_4x4x4_16b_bf16 v[224:227], v[228:229], v[170:171], v[224:227] cbsz:4 abid:1\n"
    "v_exp_f32 v156, v156 clamp\n"
    "v_exp_f32 v157, v157 clamp\n"
    "v_mfma_f32_4x4x4_16b_bf16 v[224:227], v[228:229], v[172:173], v[224:227] cbsz:4 abid:2\n"
    "v_exp_f32 v158, v158 clamp\n"
    "v_exp_f32 v159, v159 clamp\n"
    "v_mfma_f32_4x4x4_16b_bf16 v[224:227], v[228:229], v[174:175], v[224:227] cbsz:4 abid:3\n"
    "s_waitcnt vmcnt(4)\n"
    "s_memrealtime s[34:35]\n"
    "ds_read_b64_tr_b16 v[168:169], v240 offset:0\n"
    "ds_read_b64_tr_b16 v[170:171], v241 offset:256\n"
    "ds_read_b64_tr_b16 v[172:173], v242 offset:512\n"
    "ds_read_b64_tr_b16 v[174:175], v243 offset:768\n"
    "ds_read_b32 v236, v253 offset:0\n"
    "ds_read_b32 v238, v253 offset:4\n"
    "ds_read_b128 v[160:163], v234 offset:2048\n"
    "ds_read_b128 v[164:167], v235 offset:2048\n"
    "v_pk_fma_f32 v[144:145], v[144:145], s[70:71], v[96:97]\n"
    "v_pk_fma_f32 v[146:147], v[146:147], s[70:71], v[98:99]\n"
    "v_pk_add_f32 v[230:231], v[144:145], v[146:147]\n"
    "v_pk_mul_f32 v[232:233], v[144:145], v[146:147]\n"
    "v_pk_fma_f32 v[148:149], v[148:149], s[70:71], v[100:101]\n"
    "v_pk_fma_f32 v[230:231], v[230:231], v[148:149], v[232:233]\n"
    "v_pk_mul_f32 v[232:233], v[232:233], v[148:149]\n"
    "v_pk_fma_f32 v[150:151], v[150:151], s[70:71], v[102:103]\n"
    "v_pk_fma_f32 v[230:231], v[230:231], v[150:151], v[232:233]\n"
    "v_pk_mul_f32 v[232:233], v[232:233], v[150:151]\n"
    "v_pk_fma_f32 v[152:153], v[152:153], s[70:71], v[104:105]\n"
    "v_pk_fma_f32 v[230:231], v[230:231], v[152:153], v[232:233]\n"
    "v_pk_mul_f32 v[232:233], v[232:233], v[152:153]\n"
    "v_pk_fma_f32 v[154:155], v[154:155], s[70:71], v[106:107]\n"
    "v_pk_fma_f32 v[230:231], v[230:231], v[154:155], v[232:233]\n"
    "v_pk_mul_f32 v[232:233], v[232:233], v[154:155]\n"
    "v_pk_fma_f32 v[156:157], v[156:157], s[70:71], v[108:109]\n"
    "v_pk_fma_f32 v[230:231], v[230:231], v[156:157], v[232:233]\n"
    "v_pk_mul_f32 v[232:233], v[232:233], v[156:157]\n"
    "v_pk_fma_f32 v[158:159], v[158:159], s[70:71], v[110:111]\n"
    "v_pk_fma_f32 v[230:231], v[230:231], v[158:159], v[232:233]\n"
    "v_pk_mul_f32 v[232:233], v[232:233], v[158:159]\n"
    "v_rcp_f32 v232, v232\n"
    "v_rcp_f32 v233, v233\n"
    "v_fma_f32 v230, v230, v232, v247\n"
    "v_fmac_f32 v230, v231, v233\n"
    "v_lshlrev_b32 v232, 2, v249\n"
    "v_lshrrev_b32 v233, 2, v249\n"
    "v_and_b32 v232, 56, v232\n"
    "v_and_b32 v233, 4, v233\n"
    "v_add3_u32 v232, v232, v233, s81\n"
    "ds_read_b32 v232, v232\n"
    "s_add_u32 s81, s81, 64\n"
    "s_waitcnt lgkmcnt(0)\n"
    "v_lshlrev_b32 v232, 1, v232\n"
    "buffer_load_ushort v232, v232, s[44:47], 0 offen\n"
    "s_waitcnt vmcnt(0)\n"
    "v_lshlrev_b32 v232, 16, v232\n"
    "v_mul_f32 v230, v230, v232\n"
    "v_cvt_pk_bf16_f32 v230, v230, v230\n"
    "s_add_u32 s67, s67, 64\n"
    "s_waitcnt lgkmcnt(0)\n"
    "ds_write_b16 v245, v230\n"
    "ds_read_b64 v[228:229], v246\n"
    "s_cmp_le_u32 s34, s76\n"
    "s_cbranch_scc1 .Ldin_G3s_wait\n"
    ".Ldin_G3s_go:\n"
    "s_add_u32 s76, s76, s72\n"
    ".Ldin_G3s_dma:\n"
    "buffer_load_dwordx4 v[236:237], s[40:43], 0 idxen offen offset:0 nt lds\n"
    "buffer_load_dwordx4 v[238:239], s[40:43], 0 idxen offen offset:1024 nt lds\n"
    ".Ldin_G3s_nd:\n"
    "s_cmp_eq_u32 s67, s82\n"
    "s_cbranch_scc1 .Ldin_L0s\n"
    ".Ldin_G0s:\n"
    "v_mfma_f32_16x16x32_bf16 v[144:147], v[0:3], v[160:163], v[112:115]\n"
    "v_exp_f32 v128, v128 clamp\n"
    "v_mfma_f32_16x16x32_bf16 v[144:147], v[4:7], v[164:167], v[144:147]\n"
    "v_exp_f32 v129, v129 clamp\n"
    "v_mfma_f32_16x16x32_bf16 v[148:151], v[8:11], v[160:163], v[116:119]\n"
    "v_exp_f32 v130, v130 clamp\n"
    "v_mfma_f32_16x16x32_bf16 v[148:151], v[12:15], v[164:167], v[148:151]\n"
    "v_exp_f32 v131, v131 clamp\n"
    "v_mfma_f32_16x16x32_bf16 v[152:155], v[16:19], v[160:163], v[120:123]\n"
    "v_exp_f32 v132, v132 clamp\n"
    "v_mfma_f32_16x16x32_bf16 v[152:155], v[20:23], v[164:167], v[152:155]\n"
    "v_exp_f32 v133, v133 clamp\n"
    "v_mfma_f32_16x16x32_bf16 v[156:159], v[24:27], v[160:163], v[124:127]\n"
    "v_exp_f32 v134, v134 clamp\n"
    "v_mfma_f32_16x16x32_bf16 v[156:159], v[28:31], v[164:167], v[156:159]\n"
    "v_exp_f32 v135, v135 clamp\n"
    "v_exp_f32 v136, v136 clamp\n"
    "s_cmp_eq_u32 s67, s80\n"
    "s_cselect_b32 s67, s65, s67\n"
    "s_add_u32 m0, s68, 2048\n"
    "v_exp_f32 v137, v137 clamp\n"
    "v_add_u32_e64 v253, s67, v244\n"
    "s_waitcnt lgkmcnt(0)\n"
    "v_mfma_f32_4x4x4_16b_bf16 v[224:227], v[228:229], v[176:177], v[224:227] cbsz:4 abid:0\n"
    "v_exp_f32 v138, v138 clamp\n"
    "v_exp_f32 v139, v139 clamp\n"
    "v_mfma_f32_4x4x4_16b_bf16 v[224:227], v[228:229], v[178:179], v[224:227] cbsz:4 abid:1\n"
    "v_exp_f32 v140, v140 clamp\n"
    "v_exp_f32 v141, v141 clamp\n"
    "v_mfma_f32_4x4x4_16b_bf16 v[224:227], v[228:229], v[180:181], v[224:227] cbsz:4 abid:2\n"
    "v_exp_f32 v142, v142 clamp\n"
    "v_exp_f32 v143, v143 clamp\n"
    "v_mfma_f32_4x4x4_16b_bf16 v[224:227], v[228:229], v[182:183], v[224:227] cbsz:4 abid:3\n"
    "s_waitcnt vmcnt(4)\n"
    "s_memrealtime s[34:35]\n"
    "ds_read_b64_tr_b16 v[176:177], v240 offset:2048\n"
    "ds_read_b64_tr_b16 v[178:179], v241 offset:2304\n"
    "ds_read_b64_tr_b16 v[180:181], v242 offset:2560\n"
    "ds_read_b64_tr_b16 v[182:183], v243 offset:2816\n"
    "ds_read_b32 v236, v253 offset:0\n"
    "ds_read_b32 v238, v253 offset:4\n"
    "ds_read_b128 v[160:163], v234 offset:4096\n"
    "ds_read_b128 v[164:167], v235 offset:4096\n"
    "v_pk_fma_f32 v[128:129], v[128:129], s[70:71], v[96:97]\n"
    "v_pk_fma_f32 v[130:131], v[130:131], s[70:71], v[98:99]\n"
    "v_pk_add_f32 v[230:231], v[128:129], v[130:131]\n"
    "v_pk_mul_f32 v[232:233], v[128:129], v[130:131]\n"
    "v_pk_fma_f32 v[132:133], v[132:133], s[70:71], v[100:101]\n"
    "v_pk_fma_f32 v[230:231], v[230:231], v[132:133], v[232:233]\n"
    "v_pk_mul_f32 v[232:233], v[232:233], v[132:133]\n"
    "v_pk_fma_f32 v[134:135], v[134:135], s[70:71], v[102:103]\n"
    "v_pk_fma_f32 v[230:231], v[230:231], v[134:135], v[232:233]\n"
    "v_pk_mul_f32 v[232:233], v[232:233], v[134:135]\n"
    "v_pk_fma_f32 v[136:137], v[136:137], s[70:71], v[104:105]\n"
    "v_pk_fma_f32 v[230:231], v[230:231], v[136:137], v[232:233]\n"
    "v_pk_mul_f32 v[232:233], v[232:233], v[136:137]\n"
    "v_pk_fma_f32 v[138:139], v[138:139], s[70:71], v[106:107]\n"
    "v_pk_fma_f32 v[230:231], v[230:231], v[138:139], v[232:233]\n"
    "v_pk_mul_f32 v[232:233], v[232:233], v[138:139]\n"
    "v_pk_fma_f32 v[140:141], v[140:141], s[70:71], v[108:109]\n"
    "v_pk_fma_f32 v[230:231], v[230:231], v[140:141], v[232:233]\n"
    "v_pk_mul_f32 v[232:233], v[232:233], v[140:141]\n"
    "v_pk_fma_f32 v[142:143], v[142:143], s[70:71], v[110:111]\n"
    "v_pk_fma_f32 v[230:231], v[230:231], v[142:143], v[232:233]\n"
    "v_pk_mul_f32 v[232:233], v[232:233], v[142:143]\n"
    "v_rcp_f32 v232, v232\n"
    "v_rcp_f32 v233, v233\n"
    "v_fma_f32 v230, v230, v232, v247\n"
    "v_fmac_f32 v230, v231, v233\n"
    "v_lshlrev_b32 v232, 2, v249\n"
    "v_lshrrev_b32 v233, 2, v249\n"
    "v_and_b32 v232, 56, v232\n"
    "v_and_b32 v233, 4, v233\n"
    "v_add3_u32 v232, v232, v233, s81\n"
    "ds_read_b32 v232, v232\n"
    "s_add_u32 s81, s81, 64\n"
    "s_waitcnt lgkmcnt(0)\n"
    "v_lshlrev_b32 v232, 1, v232\n"
    "buffer_load_ushort v232, v232, s[44:47], 0 offen\n"
    "s_waitcnt vmcnt(0)\n"
    "v_lshlrev_b32 v232, 16, v232\n"
    "v_mul_f32 v230, v230, v232\n"
    "v_cvt_pk_bf16_f32 v230, v230, v230\n"
    "s_add_u32 s67, s67, 64\n"
    "s_waitcnt lgkmcnt(0)\n"
    "ds_write_b16 v245, v230\n"
    "ds_read_b64 v[228:229], v246\n"
    "s_cmp_le_u32 s34, s76\n"
    "s_cbranch_scc1 .Ldin_G0s_wait\n"
    ".Ldin_G0s_go:\n"
    "s_sub_u32 s94, s34, 60\n"
    "s_max_u32 s76, s76, s94\n"
    "s_add_u32 s76, s76, s72\n"
    ".Ldin_G0s_dma:\n"
    "buffer_load_dwordx4 v[236:237], s[40:43], 0 idxen offen offset:0 nt lds\n"
    "buffer_load_dwordx4 v[238:239], s[40:43], 0 idxen offen offset:1024 nt lds\n"
    ".Ldin_G0s_nd:\n"
    "s_cmp_eq_u32 s67, s82\n"
    "s_cbranch_scc1 .Ldin_L1s\n"
    ".Ldin_G1s:\n"
    "v_mfma_f32_16x16x32_bf16 v[128:131], v[0:3], v[160:163], v[112:115]\n"
    "v_exp_f32 v144, v144 clamp\n"
    "v_mfma_f32_16x16x32_bf16 v[128:131], v[4:7], v[164:167], v[128:131]\n"
    "v_exp_f32 v145, v145 clamp\n"
    "v_mfma_f32_16x16x32_bf16 v[132:135], v[8:11], v[160:163], v[116:119]\n"
    "v_exp_f32 v146, v146 clamp\n"
    "v_mfma_f32_16x16x32_bf16 v[132:135], v[12:15], v[164:167], v[132:135]\n"
    "v_exp_f32 v147, v147 clamp\n"
    "v_mfma_f32_16x16x32_bf16 v[136:139], v[16:19], v[160:163], v[120:123]\n"
    "v_exp_f32 v148, v148 clamp\n"
    "v_mfma_f32_16x16x32_bf16 v[136:139], v[20:23], v[164:167], v[136:139]\n"
    "v_exp_f32 v149, v149 clamp\n"
    "v_mfma_f32_16x16x32_bf16 v[140:143], v[24:27], v[160:163], v[124:127]\n"
    "v_exp_f32 v150, v150 clamp\n"
    "v_mfma_f32_16x16x32_bf16 v[140:143], v[28:31], v[164:167], v[140:143]\n"
    "v_exp_f32 v151, v151 clamp\n"
    "v_exp_f32 v152, v152 clamp\n"
    "s_cmp_eq_u32 s67, s80\n"
    "s_cselect_b32 s67, s65, s67\n"
    "s_add_u32 m0, s68, 4096\n"
    "v_exp_f32 v153, v153 clamp\n"
    "v_add_u32_e64 v253, s67, v244\n"
    "s_waitcnt lgkmcnt(0)\n"
    "v_mfma_f32_4x4x4_16b_bf16 v[224:227], v[228:229], v[168:169], v[224:227] cbsz:4 abid:0\n"
    "v_exp_f32 v154, v154 clamp\n"
    "v_exp_f32 v155, v155 clamp\n"
    "v_mfma_f32_4x4x4_16b_bf16 v[224:227], v[228:229], v[170:171], v[224:227] cbsz:4 abid:1\n"
    "v_exp_f32 v156, v156 clamp\n"
    "v_exp_f32 v157, v157 clamp\n"
    "v_mfma_f32_4x4x4_16b_bf16 v[224:227], v[228:229], v[172:173], v[224:227] cbsz:4 abid:2\n"
    "v_exp_f32 v158, v158 clamp\n"
    "v_exp_f32 v159, v159 clamp\n"
    "v_mfma_f32_4x4x4_16b_bf16 v[224:227], v[228:229], v[174:175], v[224:227] cbsz:4 abid:3\n"
    "s_waitcnt vmcnt(4)\n"
    "s_memrealtime s[34:35]\n"
    "ds_read_b64_tr_b16 v[168:169], v240 offset:4096\n"
    "ds_read_b64_tr_b16 v[170:171], v241 offset:4352\n"
    "ds_read_b64_tr_b16 v[172:173], v242 offset:4608\n"
    "ds_read_b64_tr_b16 v[174:175], v243 offset:4864\n"
    "ds_read_b32 v236, v253 offset:0\n"
    "ds_read_b32 v238, v253 offset:4\n"
    "ds_read_b128 v[160:163], v234 offset:6144\n"
    "ds_read_b128 v[164:167], v235 offset:6144\n"
    "v_pk_fma_f32 v[144:145], v[144:145], s[70:71], v[96:97]\n"
    "v_pk_fma_f32 v[146:147], v[146:147], s[70:71], v[98:99]\n"
    "v_pk_add_f32 v[230:231], v[144:145], v[146:147]\n"
    "v_pk_mul_f32 v[232:233], v[144:145], v[146:147]\n"
    "v_pk_fma_f32 v[148:149], v[148:149], s[70:71], v[100:101]\n"
    "v_pk_fma_f32 v[230:231], v[230:231], v[148:149], v[232:233]\n"
    "v_pk_mul_f32 v[232:233], v[232:233], v[148:149]\n"
    "v_pk_fma_f32 v[150:151], v[150:151], s[70:71], v[102:103]\n"
    "v_pk_fma_f32 v[230:231], v[230:231], v[150:151], v[232:233]\n"
    "v_pk_mul_f32 v[232:233], v[232:233], v[150:151]\n"
    "v_pk_fma_f32 v[152:153], v[152:153], s[70:71], v[104:105]\n"
    "v_pk_fma_f32 v[230:231], v[230:231], v[152:153], v[232:233]\n"
    "v_pk_mul_f32 v[232:233], v[232:233], v[152:153]\n"
    "v_pk_fma_f32 v[154:155], v[154:155], s[70:71], v[106:107]\n"
    "v_pk_fma_f32 v[230:231], v[230:231], v[154:155], v[232:233]\n"
    "v_pk_mul_f32 v[232:233], v[232:233], v[154:155]\n"
    "v_pk_fma_f32 v[156:157], v[156:157], s[70:71], v[108:109]\n"
    "v_pk_fma_f32 v[230:231], v[230:231], v[156:157], v[232:233]\n"
    "v_pk_mul_f32 v[232:233], v[232:233], v[156:157]\n"
    "v_pk_fma_f32 v[158:159], v[158:159], s[70:71], v[110:111]\n"
    "v_pk_fma_f32 v[230:231], v[230:231], v[158:159], v[232:233]\n"
    "v_pk_mul_f32 v[232:233], v[232:233], v[158:159]\n"
    "v_rcp_f32 v232, v232\n"
    "v_rcp_f32 v233, v233\n"
    "v_fma_f32 v230, v230, v232, v247\n"
    "v_fmac_f32 v230, v231, v233\n"
    "v_lshlrev_b32 v232, 2, v249\n"
    "v_lshrrev_b32 v233, 2, v249\n"
    "v_and_b32 v232, 56, v232\n"
    "v_and_b32 v233, 4, v233\n"
    "v_add3_u32 v232, v232, v233, s81\n"
    "ds_read_b32 v232, v232\n"
    "s_add_u32 s81, s81, 64\n"
    "s_waitcnt lgkmcnt(0)\n"
    "v_lshlrev_b32 v232, 1, v232\n"
    "buffer_load_ushort v232, v232, s[44:47], 0 offen\n"
    "s_waitcnt vmcnt(0)\n"
    "v_lshlrev_b32 v232, 16, v232\n"
    "v_mul_f32 v230, v230, v232\n"
    "v_cvt_pk_bf16_f32 v230, v230, v230\n"
    "s_add_u32 s67, s67, 64\n"
    "s_waitcnt lgkmcnt(0)\n"
    "ds_write_b16 v245, v230\n"
    "ds_read_b64 v[228:229], v246\n"
    "s_cmp_le_u32 s34, s76\n"
    "s_cbranch_scc1 .Ldin_G1s_wait\n"
    ".Ldin_G1s_go:\n"
    "s_add_u32 s76, s76, s72\n"
    ".Ldin_G1s_dma:\n"
    "buffer_load_dwordx4 v[236:237], s[40:43], 0 idxen offen offset:0 nt lds\n"
    "buffer_load_dwordx4 v[238:239], s[40:43], 0 idxen offen offset:1024 nt lds\n"
    ".Ldin_G1s_nd:\n"
    "s_cmp_eq_u32 s67, s82\n"
    "s_cbranch_scc1 .Ldin_L2s\n"
    "s_branch .Ldin_G2s\n"
    ".Ldin_B1s_wait:\n"
    "s_cmp_lt_i32 s59, s57\n"
    "s_cbranch_scc1 .Ldin_B1s_w2\n"
    "s_sub_u32 s94, s67, s65\n"
    "s_cmp_gt_u32 s94, 256\n"
    "s_cbranch_scc1 .Ldin_B1s_dma\n"
    "s_cmp_eq_u32 s94, 64\n"
    "s_waitcnt vmcnt(2)\n"
    "s_cbranch_scc1 .Ldin_B1s_nd\n"
    "s_waitcnt vmcnt(0)\n"
    "s_branch .Ldin_B1s_nd\n"
    ".Ldin_B1s_slp:\n"
    "s_sleep 1\n"
    ".Ldin_B1s_w2:\n"
    "s_memrealtime s[34:35]\n"
    "s_waitcnt lgkmcnt(0)\n"
    "s_sub_u32 s94, s34, s76\n"
    "s_cmp_lt_i32 s94, 0\n"
    "s_cbranch_scc1 .Ldin_B1s_slp\n"
    "s_branch .Ldin_B1s_go\n"
    ".Ldin_G2s_wait:\n"
    "s_cmp_lt_i32 s59, s57\n"
    "s_cbranch_scc1 .Ldin_G2s_w2\n"
    "s_sub_u32 s94, s67, s65\n"
    "s_cmp_gt_u32 s94, 256\n"
    "s_cbranch_scc1 .Ldin_G2s_dma\n"
    "s_cmp_eq_u32 s94, 64\n"
    "s_waitcnt vmcnt(2)\n"
    "s_cbranch_scc1 .Ldin_G2s_nd\n"
    "s_waitcnt vmcnt(0)\n"
    "s_branch .Ldin_G2s_nd\n"
    ".Ldin_G2s_slp:\n"
    "s_sleep 1\n"
    ".Ldin_G2s_w2:\n"
    "s_memrealtime s[34:35]\n"
    "s_waitcnt lgkmcnt(0)\n"
    "s_sub_u32 s94, s34, s76\n"
    "s_cmp_lt_i32 s94, 0\n"
    "s_cbranch_scc1 .Ldin_G2s_slp\n"
    "s_branch .Ldin_G2s_go\n"
    ".Ldin_G3s_wait:\n"
    "s_cmp_lt_i32 s59, s57\n"
    "s_cbranch_scc1 .Ldin_G3s_w2\n"
    "s_sub_u32 s94, s67, s65\n"
    "s_cmp_gt_u32 s94, 256\n"
    "s_cbranch_scc1 .Ldin_G3s_dma\n"
    "s_cmp_eq_u32 s94, 64\n"
    "s_waitcnt vmcnt(2)\n"
    "s_cbranch_scc1 .Ldin_G3s_nd\n"
    "s_waitcnt vmcnt(0)\n"
    "s_branch .Ldin_G3s_nd\n"
    ".Ldin_G3s_slp:\n"
    "s_sleep 1\n"
    ".Ldin_G3s_w2:\n"
    "s_memrealtime s[34:35]\n"
    "s_waitcnt lgkmcnt(0)\n"
    "s_sub_u32 s94, s34, s76\n"
    "s_cmp_lt_i32 s94, 0\n"
    "s_cbranch_scc1 .Ldin_G3s_slp\n"
    "s_branch .Ldin_G3s_go\n"
    ".Ldin_G0s_wait:\n"
    "s_cmp_lt_i32 s59, s57\n"
    "s_cbranch_scc1 .Ldin_G0s_w2\n"
    "s_sub_u32 s94, s67, s65\n"
    "s_cmp_gt_u32 s94, 256\n"
    "s_cbranch_scc1 .Ldin_G0s_dma\n"
    "s_cmp_eq_u32 s94, 64\n"
    "s_waitcnt vmcnt(2)\n"
    "s_cbranch_scc1 .Ldin_G0s_nd\n"
    "s_waitcnt vmcnt(0)\n"
    "s_branch .Ldin_G0s_nd\n"
    ".Ldin_G0s_slp:\n"
    "s_sleep 1\n"
    ".Ldin_G0s_w2:\n"
    "s_memrealtime s[34:35]\n"
    "s_waitcnt lgkmcnt(0)\n"
    "s_sub_u32 s94, s34, s76\n"
    "s_cmp_lt_i32 s94, 0\n"
    "s_cbranch_scc1 .Ldin_G0s_slp\n"
    "s_branch .Ldin_G0s_go\n"
    ".Ldin_G1s_wait:\n"
    "s_cmp_lt_i32 s59, s57\n"
    "s_cbranch_scc1 .Ldin_G1s_w2\n"
    "s_sub_u32 s94, s67, s65\n"
    "s_cmp_gt_u32 s94, 256\n"
    "s_cbranch_scc1 .Ldin_G1s_dma\n"
    "s_cmp_eq_u32 s94, 64\n"
    "s_waitcnt vmcnt(2)\n"
    "s_cbranch_scc1 .Ldin_G1s_nd\n"
    "s_waitcnt vmcnt(0)\n"
    "s_branch .Ldin_G1s_nd\n"
    ".Ldin_G1s_slp:\n"
    "s_sleep 1\n"
    ".Ldin_G1s_w2:\n"
    "s_memrealtime s[34:35]\n"
    "s_waitcnt lgkmcnt(0)\n"
    "s_sub_u32 s94, s34, s76\n"
    "s_cmp_lt_i32 s94, 0\n"
    "s_cbranch_scc1 .Ldin_G1s_slp\n"
    "s_branch .Ldin_G1s_go\n"
    ".p2align 5\n"
    ".Ldin_L2s:\n"
    "s_setprio 0\n"
    "s_cmp_ge_i32 s59, s57\n"
    "s_cbranch_scc1 .Ldin_E0s\n"
    "s_add_u32 s76, s76, s73\n"
    "v_mov_b32_e64 v253, s63\n"
    "ds_read_b64 v[232:233], v253\n"
    "v_mfma_f32_4x4x4_16b_bf16 v[144:147], v[184:185], v[32:33], v[64:67] cbsz:2 abid:0\n"
    "ds_read_b128 v[112:115], v252 offset:17408\n"
    "v_mfma_f32_4x4x4_16b_bf16 v[148:151], v[184:185], v[34:35], v[68:71] cbsz:2 abid:1\n"
    "ds_read_b128 v[116:119], v252 offset:17424\n"
    "v_mfma_f32_4x4x4_16b_bf16 v[152:155], v[184:185], v[36:37], v[72:75] cbsz:2 abid:2\n"
    "ds_read_b128 v[120:123], v252 offset:17536\n"
    "ds_read_b128 v[124:127], v252 offset:17552\n"
    "v_cvt_pk_bf16_f32 v0, v144, v145\n"
    "v_cvt_pk_bf16_f32 v1, v146, v147\n"
    "v_mfma_f32_4x4x4_16b_bf16 v[156:159], v[184:185], v[38:39], v[76:79] cbsz:2 abid:3\n"
    "v_cvt_pk_bf16_f32 v2, v148, v149\n"
    "v_cvt_pk_bf16_f32 v3, v150, v151\n"
    "v_mfma_f32_4x4x4_16b_bf16 v[144:147], v[184:185], v[40:41], v[80:83] cbsz:2 abid:0\n"
    "v_cvt_pk_bf16_f32 v4, v152, v153\n"
    "v_cvt_pk_bf16_f32 v5, v154, v155\n"
    "v_mfma_f32_4x4x4_16b_bf16 v[148:151], v[184:185], v[42:43], v[84:87] cbsz:2 abid:1\n"
    "v_cvt_pk_bf16_f32 v6, v156, v157\n"
    "v_cvt_pk_bf16_f32 v7, v158, v159\n"
    "v_mfma_f32_4x4x4_16b_bf16 v[152:155], v[184:185], v[44:45], v[88:91] cbsz:2 abid:2\n"
    "v_cvt_pk_bf16_f32 v8, v144, v145\n"
    "v_cvt_pk_bf16_f32 v9, v146, v147\n"
    "v_mfma_f32_4x4x4_16b_bf16 v[156:159], v[184:185], v[46:47], v[92:95] cbsz:2 abid:3\n"
    "v_cvt_pk_bf16_f32 v10, v148, v149\n"
    "v_cvt_pk_bf16_f32 v11, v150, v151\n"
    "v_mfma_f32_4x4x4_16b_bf16 v[144:147], v[184:185], v[48:49], v[192:195] cbsz:2 abid:0\n"
    "v_cvt_pk_bf16_f32 v12, v152, v153\n"
    "v_cvt_pk_bf16_f32 v13, v154, v155\n"
    "v_mfma_f32_4x4x4_16b_bf16 v[148:151], v[184:185], v[50:51], v[196:199] cbsz:2 abid:1\n"
    "v_cvt_pk_bf16_f32 v14, v156, v157\n"
    "v_cvt_pk_bf16_f32 v15, v158, v159\n"
    "v_mfma_f32_4x4x4_16b_bf16 v[152:155], v[184:185], v[52:53], v[200:203] cbsz:2 abid:2\n"
    "s_waitcnt lgkmcnt(0)\n"
    "v_readfirstlane_b32 s60, v232\n"
    "v_cvt_pk_bf16_f32 v16, v144, v145\n"
    "v_cvt_pk_bf16_f32 v17, v146, v147\n"
    "v_mfma_f32_4x4x4_16b_bf16 v[156:159], v[184:185], v[54:55], v[204:207] cbsz:2 abid:3\n"
    "s_cmp_eq_u32 s60, -1\n"
    "s_cbranch_scc1 .Ldin_L2s_poll\n"
    ".Ldin_L2s_pr:\n"
    "v_readfirstlane_b32 s95, v233\n"
    "s_and_b32 s94, s60, 63\n"
    "s_lshl_b32 s94, s94, 6\n"
    "s_add_u32 s94, s94, 68480\n"
    "s_add_u32 s63, s63, 8\n"
    "v_lshl_add_u32 v252, v249, 1, s94\n"
    "ds_read_b32 v252, v252\n"
    "s_sub_u32 s95, s95, s76\n"
    "s_ashr_i32 s95, s95, 4\n"
    "s_min_i32 s95, s95, s74\n"
    "s_max_i32 s72, s95, s75\n"
    "v_cvt_pk_bf16_f32 v18, v148, v149\n"
    "v_cvt_pk_bf16_f32 v19, v150, v151\n"
    "v_mfma_f32_4x4x4_16b_bf16 v[144:147], v[184:185], v[56:57], v[208:211] cbsz:2 abid:0\n"
    "v_cvt_pk_bf16_f32 v20, v152, v153\n"
    "v_cvt_pk_bf16_f32 v21, v154, v155\n"
    "v_mfma_f32_4x4x4_16b_bf16 v[148:151], v[184:185], v[58:59], v[212:215] cbsz:2 abid:1\n"
    "v_cvt_pk_bf16_f32 v22, v156, v157\n"
    "v_cvt_pk_bf16_f32 v23, v158, v159\n"
    "v_mfma_f32_4x4x4_16b_bf16 v[152:155], v[184:185], v[60:61], v[216:219] cbsz:2 abid:2\n"
    "v_cvt_pk_bf16_f32 v24, v144, v145\n"
    "v_cvt_pk_bf16_f32 v25, v146, v147\n"
    "v_mfma_f32_4x4x4_16b_bf16 v[156:159], v[184:185], v[62:63], v[220:223] cbsz:2 abid:3\n"
    "v_cvt_pk_bf16_f32 v26, v148, v149\n"
    "v_cvt_pk_bf16_f32 v27, v150, v151\n"
    "v_cvt_pk_bf16_f32 v28, v152, v153\n"
    "v_cvt_pk_bf16_f32 v29, v154, v155\n"
    "v_exp_f32 v136, v136 clamp\n"
    "v_cvt_pk_bf16_f32 v30, v156, v157\n"
    "v_cvt_pk_bf16_f32 v31, v158, v159\n"
    "v_mfma_f32_16x16x32_bf16 v[144:147], v[0:3], v[160:163], v[112:115]\n"
    "v_exp_f32 v128, v128 clamp\n"
    "v_mfma_f32_16x16x32_bf16 v[144:147], v[4:7], v[164:167], v[144:147]\n"
    "v_exp_f32 v129, v129 clamp\n"
    "v_mfma_f32_16x16x32_bf16 v[148:151], v[8:11], v[160:163], v[116:119]\n"
    "v_exp_f32 v130, v130 clamp\n"
    "v_mfma_f32_16x16x32_bf16 v[148:151], v[12:15], v[164:167], v[148:151]\n"
    "v_exp_f32 v131, v131 clamp\n"
    "v_mfma_f32_16x16x32_bf16 v[152:155], v[16:19], v[160:163], v[120:123]\n"
    "v_exp_f32 v132, v132 clamp\n"
    "v_mfma_f32_16x16x32_bf16 v[152:155], v[20:23], v[164:167], v[152:155]\n"
    "v_exp_f32 v133, v133 clamp\n"
    "v_mfma_f32_16x16x32_bf16 v[156:159], v[24:27], v[160:163], v[124:127]\n"
    "v_exp_f32 v134, v134 clamp\n"
    "v_mfma_f32_16x16x32_bf16 v[156:159], v[28:31], v[164:167], v[156:159]\n"
    "v_exp_f32 v135, v135 clamp\n"
    "s_add_u32 m0, s68, 6144\n"
    "v_exp_f32 v137, v137 clamp\n"
    "v_add_u32 v253, s67, v244\n"
    "v_mfma_f32_4x4x4_16b_bf16 v[224:227], v[228:229], v[176:177], v[224:227] cbsz:4 abid:0\n"
    "v_exp_f32 v138, v138 clamp\n"
    "v_exp_f32 v139, v139 clamp\n"
    "v_mfma_f32_4x4x4_16b_bf16 v[224:227], v[228:229], v[178:179], v[224:227] cbsz:4 abid:1\n"
    "v_exp_f32 v140, v140 clamp\n"
    "v_exp_f32 v141, v141 clamp\n"
    "v_mfma_f32_4x4x4_16b_bf16 v[224:227], v[228:229], v[180:181], v[224:227] cbsz:4 abid:2\n"
    "v_exp_f32 v142, v142 clamp\n"
    "v_exp_f32 v143, v143 clamp\n"
    "v_mfma_f32_4x4x4_16b_bf16 v[224:227], v[228:229], v[182:183], v[224:227] cbsz:4 abid:3\n"
    "s_waitcnt vmcnt(4)\n"
    "ds_read_b64_tr_b16 v[176:177], v240 offset:6144\n"
    "ds_read_b64_tr_b16 v[178:179], v241 offset:6400\n"
    "ds_read_b64_tr_b16 v[180:181], v242 offset:6656\n"
    "ds_read_b64_tr_b16 v[182:183], v243 offset:6912\n"
    "ds_read_b32 v236, v253 offset:0\n"
    "ds_read_b32 v238, v253 offset:4\n"
    "ds_read_b128 v[160:163], v234 offset:0\n"
    "ds_read_b128 v[164:167], v235 offset:0\n"
    "s_waitcnt lgkmcnt(8)\n"
    "s_nop 0\n"
    "v_readlane_b32 s20, v252, 0\n"
    "v_readlane_b32 s21, v252, 1\n"
    "v_readlane_b32 s22, v252, 2\n"
    "v_readlane_b32 s23, v252, 3\n"
    "v_readlane_b32 s24, v252, 4\n"
    "v_readlane_b32 s25, v252, 5\n"
    "v_readlane_b32 s26, v252, 6\n"
    "v_readlane_b32 s27, v252, 7\n"
    "v_pk_fma_f32 v[128:129], v[128:129], s[70:71], v[96:97]\n"
    "v_pk_fma_f32 v[130:131], v[130:131], s[70:71], v[98:99]\n"
    "v_pk_add_f32 v[230:231], v[128:129], v[130:131]\n"
    "v_pk_mul_f32 v[232:233], v[128:129], v[130:131]\n"
    "v_pk_fma_f32 v[132:133], v[132:133], s[70:71], v[100:101]\n"
    "v_pk_fma_f32 v[230:231], v[230:231], v[132:133], v[232:233]\n"
    "v_pk_mul_f32 v[232:233], v[232:233], v[132:133]\n"
    "v_pk_fma_f32 v[134:135], v[134:135], s[70:71], v[102:103]\n"
    "v_pk_fma_f32 v[230:231], v[230:231], v[134:135], v[232:233]\n"
    "v_pk_mul_f32 v[232:233], v[232:233], v[134:135]\n"
    "v_pk_fma_f32 v[136:137], v[136:137], s[70:71], v[104:105]\n"
    "v_pk_fma_f32 v[230:231], v[230:231], v[136:137], v[232:233]\n"
    "v_pk_mul_f32 v[232:233], v[232:233], v[136:137]\n"
    "v_pk_fma_f32 v[138:139], v[138:139], s[70:71], v[106:107]\n"
    "v_pk_fma_f32 v[230:231], v[230:231], v[138:139], v[232:233]\n"
    "v_pk_mul_f32 v[232:233], v[232:233], v[138:139]\n"
    "v_pk_fma_f32 v[140:141], v[140:141], s[70:71], v[108:109]\n"
    "v_pk_fma_f32 v[230:231], v[230:231], v[140:141], v[232:233]\n"
    "v_pk_mul_f32 v[232:233], v[232:233], v[140:141]\n"
    "v_pk_fma_f32 v[142:143], v[142:143], s[70:71], v[110:111]\n"
    "v_pk_fma_f32 v[230:231], v[230:231], v[142:143], v[232:233]\n"
    "v_pk_mul_f32 v[232:233], v[232:233], v[142:143]\n"
    "v_rcp_f32 v232, v232\n"
    "v_rcp_f32 v233, v233\n"
    "v_fma_f32 v230, v230, v232, v247\n"
    "v_fmac_f32 v230, v231, v233\n"
    "v_lshlrev_b32 v232, 2, v249\n"
    "v_lshrrev_b32 v233, 2, v249\n"
    "v_and_b32 v232, 56, v232\n"
    "v_and_b32 v233, 4, v233\n"
    "v_add3_u32 v232, v232, v233, s81\n"
    "ds_read_b32 v232, v232\n"
    "s_add_u32 s81, s81, 64\n"
    "s_waitcnt lgkmcnt(0)\n"
    "v_lshlrev_b32 v232, 1, v232\n"
    "buffer_load_ushort v232, v232, s[44:47], 0 offen\n"
    "s_waitcnt vmcnt(0)\n"
    "v_lshlrev_b32 v232, 16, v232\n"
    "v_mul_f32 v230, v230, v232\n"
    "v_cvt_pk_bf16_f32 v230, v230, v230\n"
    "s_add_u32 s67, s67, 64\n"
    "s_waitcnt lgkmcnt(0)\n"
    "ds_write_b16 v245, v230\n"
    "ds_read_b64 v[228:229], v246\n"
    "s_lshl1_add_u32 s76, s72, s76\n"
    "buffer_load_dwordx4 v[236:237], s[40:43], 0 idxen offen offset:0 nt lds\n"
    "buffer_load_dwordx4 v[238:239], s[40:43], 0 idxen offen offset:1024 nt lds\n"
    "s_and_b32 s62, s58, 63\n"
    "s_mul_i32 s62, s62, 272\n"
    "s_mov_b32 s58, s59\n"
    "s_mov_b32 s59, s60\n"
    "s_mov_b32 s81, s65\n"
    "s_xor_b32 s65, s65, s64\n"
    "s_mov_b32 s80, s83\n"
    "s_add_u32 s82, s65, 256\n"
    "s_cmp_lt_i32 s59, s57\n"
    "s_cselect_b32 s76, s76, -1\n"
    "s_bitcmp1_b32 s58, 30\n"
    "s_cbranch_scc0 .Ldin_A3\n"
    ".Ldin_A3s:\n"
    "v_mfma_f32_16x16x32_bf16 v[128:131], v[0:3], v[160:163], v[112:115]\n"
    "v_exp_f32 v144, v144 clamp\n"
    "v_mfma_f32_16x16x32_bf16 v[128:131], v[4:7], v[164:167], v[128:131]\n"
    "v_exp_f32 v145, v145 clamp\n"
    "v_mfma_f32_16x16x32_bf16 v[132:135], v[8:11], v[160:163], v[116:119]\n"
    "v_exp_f32 v146, v146 clamp\n"
    "v_mfma_f32_16x16x32_bf16 v[132:135], v[12:15], v[164:167], v[132:135]\n"
    "v_exp_f32 v147, v147 clamp\n"
    "v_mfma_f32_16x16x32_bf16 v[136:139], v[16:19], v[160:163], v[120:123]\n"
    "v_exp_f32 v148, v148 clamp\n"
    "v_mfma_f32_16x16x32_bf16 v[136:139], v[20:23], v[164:167], v[136:139]\n"
    "v_exp_f32 v149, v149 clamp\n"
    "v_mfma_f32_16x16x32_bf16 v[140:143], v[24:27], v[160:163], v[124:127]\n"
    "v_exp_f32 v150, v150 clamp\n"
    "v_mfma_f32_16x16x32_bf16 v[140:143], v[28:31], v[164:167], v[140:143]\n"
    "v_exp_f32 v151, v151 clamp\n"
    "v_exp_f32 v152, v152 clamp\n"
    "s_mov_b32 m0, s62\n"
    "v_exp_f32 v153, v153 clamp\n"
    "v_add_u32 v253, s67, v244\n"
    "s_waitcnt lgkmcnt(0)\n"
    "v_mfma_f32_4x4x4_16b_bf16 v[224:227], v[228:229], v[168:169], v[224:227] cbsz:4 abid:0\n"
    "v_exp_f32 v154, v154 clamp\n"
    "v_exp_f32 v155, v155 clamp\n"
    "v_mfma_f32_4x4x4_16b_bf16 v[224:227], v[228:229], v[170:171], v[224:227] cbsz:4 abid:1\n"
    "v_exp_f32 v156, v156 clamp\n"
    "v_exp_f32 v157, v157 clamp\n"
    "v_mfma_f32_4x4x4_16b_bf16 v[224:227], v[228:229], v[172:173], v[224:227] cbsz:4 abid:2\n"
    "v_exp_f32 v158, v158 clamp\n"
    "v_exp_f32 v159, v159 clamp\n"
    "v_mfma_f32_4x4x4_16b_bf16 v[224:227], v[228:229], v[174:175], v[224:227] cbsz:4 abid:3\n"
    "s_waitcnt vmcnt(4)\n"
    "ds_read_b64_tr_b16 v[168:169], v240 offset:0\n"
    "ds_read_b64_tr_b16 v[170:171], v241 offset:256\n"
    "ds_read_b64_tr_b16 v[172:173], v242 offset:512\n"
    "ds_read_b64_tr_b16 v[174:175], v243 offset:768\n"
    "ds_read_b32 v236, v253 offset:0\n"
    "ds_read_b32 v238, v253 offset:4\n"
    "ds_read_b128 v[160:163], v234 offset:2048\n"
    "ds_read_b128 v[164:167], v235 offset:2048\n"
    "s_cbranch_vccz .Ldin_A3s_np\n"
    "s_setprio 1\n"
    ".Ldin_A3s_np:\n"
    "v_pk_fma_f32 v[144:145], v[144:145], s[70:71], v[96:97]\n"
    "v_pk_fma_f32 v[146:147], v[146:147], s[70:71], v[98:99]\n"
    "v_pk_add_f32 v[190:191], v[224:225], v[226:227]\n"
    "v_add_f32 v190, v190, v191\n"
    "ds_write_addtid_b32 v190 offset:32768\n"
    "s_add_u32 m0, s68, 0\n"
    "s_bfe_u32 s94, s59, 0x180006\n"
    "s_mul_i32 s86, s94, s56\n"
    "v_mbcnt_lo_u32_b32 v184, s20, 0\n"
    "v_mbcnt_lo_u32_b32 v185, s22, 0\n"
    "v_mbcnt_lo_u32_b32 v186, s24, 0\n"
    "v_mbcnt_lo_u32_b32 v187, s26, 0\n"
    "s_bcnt1_i32_b64 s90, s[20:21]\n"
    "s_bcnt1_i32_b64 s91, s[22:23]\n"
    "s_bcnt1_i32_b64 s92, s[24:25]\n"
    "s_bcnt1_i32_b64 s93, s[26:27]\n"
    "v_mbcnt_hi_u32_b32 v184, s21, v184\n"
    "v_mbcnt_hi_u32_b32 v185, s23, v185\n"
    "v_mbcnt_hi_u32_b32 v186, s25, v186\n"
    "v_mbcnt_hi_u32_b32 v187, s27, v187\n"
    "s_lshl2_add_u32 s90, s90, s65\n"
    "s_lshl2_add_u32 s91, s91, s90\n"
    "s_lshl2_add_u32 s92, s92, s91\n"
    "s_lshl2_add_u32 s93, s93, s92\n"
    "v_add_u32 v188, s86, v251\n"
    "v_pk_add_f32 v[230:231], v[144:145], v[146:147]\n"
    "v_pk_mul_f32 v[232:233], v[144:145], v[146:147]\n"
    "v_pk_fma_f32 v[148:149], v[148:149], s[70:71], v[100:101]\n"
    "v_pk_fma_f32 v[230:231], v[230:231], v[148:149], v[232:233]\n"
    "v_pk_mul_f32 v[232:233], v[232:233], v[148:149]\n"
    "v_lshl_add_u32 v184, v184, 2, s65\n"
    "v_lshl_add_u32 v185, v185, 2, s90\n"
    "v_lshl_add_u32 v186, v186, 2, s91\n"
    "v_lshl_add_u32 v187, v187, 2, s92\n"
    "v_mov_b32 v191, -1\n"
    "v_lshl_add_u32 v190, v249, 1, s93\n"
    "ds_write2_b32 v190, v191, v191 offset1:64\n"
    "s_mov_b64 exec, s[20:21]\n"
    "ds_write_b32 v184, v188\n"
    "s_mov_b64 exec, s[22:23]\n"
    "v_add_u32 v189, 64, v188\n"
    "ds_write_b32 v185, v189\n"
    "s_mov_b64 exec, s[24:25]\n"
    "v_add_u32 v190, 128, v188\n"
    "ds_write_b32 v186, v190\n"
    "s_mov_b64 exec, s[26:27]\n"
    "v_add_u32 v189, 192, v188\n"
    "ds_write_b32 v187, v189\n"
    "s_mov_b64 exec, -1\n"
    "s_add_u32 s95, s93, 60\n"
    "s_add_u32 s94, s82, 128\n"
    "s_andn2_b32 s95, s95, 63\n"
    "s_max_u32 s83, s95, s94\n"
    "v_pk_fma_f32 v[150:151], v[150:151], s[70:71], v[102:103]\n"
    "v_pk_fma_f32 v[230:231], v[230:231], v[150:151], v[232:233]\n"
    "v_pk_mul_f32 v[232:233], v[232:233], v[150:151]\n"
    "v_pk_fma_f32 v[152:153], v[152:153], s[70:71], v[104:105]\n"
    "v_pk_fma_f32 v[230:231], v[230:231], v[152:153], v[232:233]\n"
    "v_pk_mul_f32 v[232:233], v[232:233], v[152:153]\n"
    "v_pk_fma_f32 v[154:155], v[154:155], s[70:71], v[106:107]\n"
    "v_pk_fma_f32 v[230:231], v[230:231], v[154:155], v[232:233]\n"
    "v_pk_mul_f32 v[232:233], v[232:233], v[154:155]\n"
    "v_pk_fma_f32 v[156:157], v[156:157], s[70:71], v[108:109]\n"
    "v_pk_fma_f32 v[230:231], v[230:231], v[156:157], v[232:233]\n"
    "v_pk_mul_f32 v[232:233], v[232:233], v[156:157]\n"
    "v_pk_fma_f32 v[158:159], v[158:159], s[70:71], v[110:111]\n"
    "v_pk_fma_f32 v[230:231], v[230:231], v[158:159], v[232:233]\n"
    "v_pk_mul_f32 v[232:233], v[232:233], v[158:159]\n"
    "v_rcp_f32 v232, v232\n"
    "v_rcp_f32 v233, v233\n"
    "v_fma_f32 v230, v230, v232, v247\n"
    "v_fmac_f32 v230, v231, v233\n"
    "v_lshlrev_b32 v232, 2, v249\n"
    "v_lshrrev_b32 v233, 2, v249\n"
    "v_and_b32 v232, 56, v232\n"
    "v_and_b32 v233, 4, v233\n"
    "v_add3_u32 v232, v232, v233, s81\n"
    "ds_read_b32 v232, v232\n"
    "s_add_u32 s81, s81, 64\n"
    "s_waitcnt lgkmcnt(0)\n"
    "v_lshlrev_b32 v232, 1, v232\n"
    "buffer_load_ushort v232, v232, s[44:47], 0 offen\n"
    "s_waitcnt vmcnt(0)\n"
    "v_lshlrev_b32 v232, 16, v232\n"
    "v_mul_f32 v230, v230, v232\n"
    "v_cvt_pk_bf16_f32 v230, v230, v230\n"
    "s_add_u32 s67, s67, 64\n"
    "s_waitcnt lgkmcnt(0)\n"
    "ds_write_b16 v245, v230\n"
    "ds_read_b64 v[228:229], v246\n"
    "buffer_load_dwordx4 v[236:237], s[40:43], 0 idxen offen offset:0 nt lds\n"
    "buffer_load_dwordx4 v[238:239], s[40:43], 0 idxen offen offset:1024 nt lds\n"
    ".Ldin_B0s:\n"
    "v_mfma_f32_16x16x32_bf16 v[144:147], v[0:3], v[160:163], v[112:115]\n"
    "v_exp_f32 v128, v128 clamp\n"
    "v_mfma_f32_16x16x32_bf16 v[144:147], v[4:7], v[164:167], v[144:147]\n"
    "v_exp_f32 v129, v129 clamp\n"
    "v_mfma_f32_16x16x32_bf16 v[148:151], v[8:11], v[160:163], v[116:119]\n"
    "v_exp_f32 v130, v130 clamp\n"
    "v_mfma_f32_16x16x32_bf16 v[148:151], v[12:15], v[164:167], v[148:151]\n"
    "v_exp_f32 v131, v131 clamp\n"
    "v_mfma_f32_16x16x32_bf16 v[152:155], v[16:19], v[160:163], v[120:123]\n"
    "v_exp_f32 v132, v132 clamp\n"
    "v_mfma_f32_16x16x32_bf16 v[152:155], v[20:23], v[164:167], v[152:155]\n"
    "v_exp_f32 v133, v133 clamp\n"
    "v_mfma_f32_16x16x32_bf16 v[156:159], v[24:27], v[160:163], v[124:127]\n"
    "v_exp_f32 v134, v134 clamp\n"
    "v_mfma_f32_16x16x32_bf16 v[156:159], v[28:31], v[164:167], v[156:159]\n"
    "v_exp_f32 v135, v135 clamp\n"
    "v_exp_f32 v136, v136 clamp\n"
    "s_cmp_eq_u32 s67, s80\n"
    "s_cselect_b32 s67, s65, s67\n"
    "s_and_b32 s94, s59, 63\n"
    "s_mul_i32 s94, s94, 272\n"
    "v_add_u32 v252, s94, v250\n"
    "s_add_u32 m0, s68, 2048\n"
    "v_exp_f32 v137, v137 clamp\n"
    "v_add_u32 v253, s67, v244\n"
    "s_waitcnt lgkmcnt(0)\n"
    "v_mfma_f32_4x4x4_16b_bf16 v[224:227], v[228:229], v[176:177], 0 cbsz:4 abid:0\n"
    "v_exp_f32 v138, v138 clamp\n"
    "v_exp_f32 v139, v139 clamp\n"
    "v_mfma_f32_4x4x4_16b_bf16 v[224:227], v[228:229], v[178:179], v[224:227] cbsz:4 abid:1\n"
    "v_exp_f32 v140, v140 clamp\n"
    "v_exp_f32 v141, v141 clamp\n"
    "v_mfma_f32_4x4x4_16b_bf16 v[224:227], v[228:229], v[180:181], v[224:227] cbsz:4 abid:2\n"
    "v_exp_f32 v142, v142 clamp\n"
    "v_exp_f32 v143, v143 clamp\n"
    "v_mfma_f32_4x4x4_16b_bf16 v[224:227], v[228:229], v[182:183], v[224:227] cbsz:4 abid:3\n"
    "s_waitcnt vmcnt(4)\n"
    "s_memrealtime s[34:35]\n"
    "ds_read_b64_tr_b16 v[176:177], v240 offset:2048\n"
    "ds_read_b64_tr_b16 v[178:179], v241 offset:2304\n"
    "ds_read_b64_tr_b16 v[180:181], v242 offset:2560\n"
    "ds_read_b64_tr_b16 v[182:183], v243 offset:2816\n"
    "ds_read_b32 v236, v253 offset:0\n"
    "ds_read_b32 v238, v253 offset:4\n"
    "ds_read_b128 v[160:163], v234 offset:4096\n"
    "ds_read_b128 v[164:167], v235 offset:4096\n"
    "v_add_u32_e64 v184, v248, v252\n"
    "ds_read_b64 v[184:185], v184\n"
    "v_pk_fma_f32 v[128:129], v[128:129], s[70:71], v[96:97]\n"
    "v_pk_fma_f32 v[130:131], v[130:131], s[70:71], v[98:99]\n"
    "v_pk_add_f32 v[230:231], v[128:129], v[130:131]\n"
    "v_pk_mul_f32 v[232:233], v[128:129], v[130:131]\n"
    "v_pk_fma_f32 v[132:133], v[132:133], s[70:71], v[100:101]\n"
    "v_pk_fma_f32 v[230:231], v[230:231], v[132:133], v[232:233]\n"
    "v_pk_mul_f32 v[232:233], v[232:233], v[132:133]\n"
    "v_pk_fma_f32 v[134:135], v[134:135], s[70:71], v[102:103]\n"
    "v_pk_fma_f32 v[230:231], v[230:231], v[134:135], v[232:233]\n"
    "v_pk_mul_f32 v[232:233], v[232:233], v[134:135]\n"
    "v_pk_fma_f32 v[136:137], v[136:137], s[70:71], v[104:105]\n"
    "v_pk_fma_f32 v[230:231], v[230:231], v[136:137], v[232:233]\n"
    "v_pk_mul_f32 v[232:233], v[232:233], v[136:137]\n"
    "v_pk_fma_f32 v[138:139], v[138:139], s[70:71], v[106:107]\n"
    "v_pk_fma_f32 v[230:231], v[230:231], v[138:139], v[232:233]\n"
    "v_pk_mul_f32 v[232:233], v[232:233], v[138:139]\n"
    "v_pk_fma_f32 v[140:141], v[140:141], s[70:71], v[108:109]\n"
    "v_pk_fma_f32 v[230:231], v[230:231], v[140:141], v[232:233]\n"
    "v_pk_mul_f32 v[232:233], v[232:233], v[140:141]\n"
    "v_pk_fma_f32 v[142:143], v[142:143], s[70:71], v[110:111]\n"
    "v_pk_fma_f32 v[230:231], v[230:231], v[142:143], v[232:233]\n"
    "v_pk_mul_f32 v[232:233], v[232:233], v[142:143]\n"
    "v_rcp_f32 v232, v232\n"
    "v_rcp_f32 v233, v233\n"
    "v_fma_f32 v230, v230, v232, v247\n"
    "v_fmac_f32 v230, v231, v233\n"
    "v_lshlrev_b32 v232, 2, v249\n"
    "v_lshrrev_b32 v233, 2, v249\n"
    "v_and_b32 v232, 56, v232\n"
    "v_and_b32 v233, 4, v233\n"
    "v_add3_u32 v232, v232, v233, s81\n"
    "ds_read_b32 v232, v232\n"
    "s_add_u32 s81, s81, 64\n"
    "s_waitcnt lgkmcnt(0)\n"
    "v_lshlrev_b32 v232, 1, v232\n"
    "buffer_load_ushort v232, v232, s[44:47], 0 offen\n"
    "s_waitcnt vmcnt(0)\n"
    "v_lshlrev_b32 v232, 16, v232\n"
    "v_mul_f32 v230, v230, v232\n"
    "v_cvt_pk_bf16_f32 v230, v230, v230\n"
    "s_add_u32 s67, s67, 64\n"
    "s_waitcnt lgkmcnt(0)\n"
    "ds_write_b16 v245, v230\n"
    "ds_read_b64 v[228:229], v246\n"
    "v_and_b32 v184, v254, v184\n"
    "v_and_b32 v185, v255, v185\n"
    "s_cmp_le_u32 s34, s76\n"
    "s_cbranch_scc1 .Ldin_B0s_wait\n"
    ".Ldin_B0s_go:\n"
    "s_sub_u32 s94, s34, 60\n"
    "s_max_u32 s76, s76, s94\n"
    "s_add_u32 s76, s76, s72\n"
    ".Ldin_B0s_dma:\n"
    "buffer_load_dwordx4 v[236:237], s[40:43], 0 idxen offen offset:0 nt lds\n"
    "buffer_load_dwordx4 v[238:239], s[40:43], 0 idxen offen offset:1024 nt lds\n"
    ".Ldin_B0s_nd:\n"
    "s_branch .Ldin_G1s\n"
    ".Ldin_L2s_poll:\n"
    "s_sleep 1\n"
    "ds_read_b64 v[232:233], v253\n"
    "s_waitcnt lgkmcnt(0)\n"
    "v_readfirstlane_b32 s60, v232\n"
    "s_cmp_eq_u32 s60, -1\n"
    "s_cbranch_scc1 .Ldin_L2s_poll\n"
    "s_branch .Ldin_L2s_pr\n"
    ".Ldin_B0s_wait:\n"
    "s_cmp_lt_i32 s59, s57\n"
    "s_cbranch_scc1 .Ldin_B0s_w2\n"
    "s_sub_u32 s94, s67, s65\n"
    "s_cmp_gt_u32 s94, 256\n"
    "s_cbranch_scc1 .Ldin_B0s_dma\n"
    "s_cmp_eq_u32 s94, 64\n"
    "s_waitcnt vmcnt(2)\n"
    "s_cbranch_scc1 .Ldin_B0s_nd\n"
    "s_waitcnt vmcnt(0)\n"
    "s_branch .Ldin_B0s_nd\n"
    ".Ldin_B0s_slp:\n"
    "s_sleep 1\n"
    ".Ldin_B0s_w2:\n"
    "s_memrealtime s[34:35]\n"
    "s_waitcnt lgkmcnt(0)\n"
    "s_sub_u32 s94, s34, s76\n"
    "s_cmp_lt_i32 s94, 0\n"
    "s_cbranch_scc1 .Ldin_B0s_slp\n"
    "s_branch .Ldin_B0s_go\n"
    ".p2align 5\n"
    ".Ldin_L1s:\n"
    "s_setprio 0\n"
    "s_cmp_ge_i32 s59, s57\n"
    "s_cbranch_scc1 .Ldin_E1s\n"
    "s_add_u32 s76, s76, s73\n"
    "v_mov_b32_e64 v253, s63\n"
    "ds_read_b64 v[232:233], v253\n"
    "v_mfma_f32_4x4x4_16b_bf16 v[128:131], v[184:185], v[32:33], v[64:67] cbsz:2 abid:0\n"
    "ds_read_b128 v[112:115], v252 offset:17408\n"
    "v_mfma_f32_4x4x4_16b_bf16 v[132:135], v[184:185], v[34:35], v[68:71] cbsz:2 abid:1\n"
    "ds_read_b128 v[116:119], v252 offset:17424\n"
    "v_mfma_f32_4x4x4_16b_bf16 v[136:139], v[184:185], v[36:37], v[72:75] cbsz:2 abid:2\n"
    "ds_read_b128 v[120:123], v252 offset:17536\n"
    "ds_read_b128 v[124:127], v252 offset:17552\n"
    "v_cvt_pk_bf16_f32 v0, v128, v129\n"
    "v_cvt_pk_bf16_f32 v1, v130, v131\n"
    "v_mfma_f32_4x4x4_16b_bf16 v[140:143], v[184:185], v[38:39], v[76:79] cbsz:2 abid:3\n"
    "v_cvt_pk_bf16_f32 v2, v132, v133\n"
    "v_cvt_pk_bf16_f32 v3, v134, v135\n"
    "v_mfma_f32_4x4x4_16b_bf16 v[128:131], v[184:185], v[40:41], v[80:83] cbsz:2 abid:0\n"
    "v_cvt_pk_bf16_f32 v4, v136, v137\n"
    "v_cvt_pk_bf16_f32 v5, v138, v139\n"
    "v_mfma_f32_4x4x4_16b_bf16 v[132:135], v[184:185], v[42:43], v[84:87] cbsz:2 abid:1\n"
    "v_cvt_pk_bf16_f32 v6, v140, v141\n"
    "v_cvt_pk_bf16_f32 v7, v142, v143\n"
    "v_mfma_f32_4x4x4_16b_bf16 v[136:139], v[184:185], v[44:45], v[88:91] cbsz:2 abid:2\n"
    "v_cvt_pk_bf16_f32 v8, v128, v129\n"
    "v_cvt_pk_bf16_f32 v9, v130, v131\n"
    "v_mfma_f32_4x4x4_16b_bf16 v[140:143], v[184:185], v[46:47], v[92:95] cbsz:2 abid:3\n"
    "v_cvt_pk_bf16_f32 v10, v132, v133\n"
    "v_cvt_pk_bf16_f32 v11, v134, v135\n"
    "v_mfma_f32_4x4x4_16b_bf16 v[128:131], v[184:185], v[48:49], v[192:195] cbsz:2 abid:0\n"
    "v_cvt_pk_bf16_f32 v12, v136, v137\n"
    "v_cvt_pk_bf16_f32 v13, v138, v139\n"
    "v_mfma_f32_4x4x4_16b_bf16 v[132:135], v[184:185], v[50:51], v[196:199] cbsz:2 abid:1\n"
    "v_cvt_pk_bf16_f32 v14, v140, v141\n"
    "v_cvt_pk_bf16_f32 v15, v142, v143\n"
    "v_mfma_f32_4x4x4_16b_bf16 v[136:139], v[184:185], v[52:53], v[200:203] cbsz:2 abid:2\n"
    "s_waitcnt lgkmcnt(0)\n"
    "v_readfirstlane_b32 s60, v232\n"
    "v_cvt_pk_bf16_f32 v16, v128, v129\n"
    "v_cvt_pk_bf16_f32 v17, v130, v131\n"
    "v_mfma_f32_4x4x4_16b_bf16 v[140:143], v[184:185], v[54:55], v[204:207] cbsz:2 abid:3\n"
    "s_cmp_eq_u32 s60, -1\n"
    "s_cbranch_scc1 .Ldin_L1s_poll\n"
    ".Ldin_L1s_pr:\n"
    "v_readfirstlane_b32 s95, v233\n"
    "s_and_b32 s94, s60, 63\n"
    "s_lshl_b32 s94, s94, 6\n"
    "s_add_u32 s94, s94, 68480\n"
    "s_add_u32 s63, s63, 8\n"
    "v_lshl_add_u32 v252, v249, 1, s94\n"
    "ds_read_b32 v252, v252\n"
    "s_sub_u32 s95, s95, s76\n"
    "s_ashr_i32 s95, s95, 4\n"
    "s_min_i32 s95, s95, s74\n"
    "s_max_i32 s72, s95, s75\n"
    "v_cvt_pk_bf16_f32 v18, v132, v133\n"
    "v_cvt_pk_bf16_f32 v19, v134, v135\n"
    "v_mfma_f32_4x4x4_16b_bf16 v[128:131], v[184:185], v[56:57], v[208:211] cbsz:2 abid:0\n"
    "v_cvt_pk_bf16_f32 v20, v136, v137\n"
    "v_cvt_pk_bf16_f32 v21, v138, v139\n"
    "v_mfma_f32_4x4x4_16b_bf16 v[132:135], v[184:185], v[58:59], v[212:215] cbsz:2 abid:1\n"
    "v_cvt_pk_bf16_f32 v22, v140, v141\n"
    "v_cvt_pk_bf16_f32 v23, v142, v143\n"
    "v_mfma_f32_4x4x4_16b_bf16 v[136:139], v[184:185], v[60:61], v[216:219] cbsz:2 abid:2\n"
    "v_cvt_pk_bf16_f32 v24, v128, v129\n"
    "v_cvt_pk_bf16_f32 v25, v130, v131\n"
    "v_mfma_f32_4x4x4_16b_bf16 v[140:143], v[184:185], v[62:63], v[220:223] cbsz:2 abid:3\n"
    "v_cvt_pk_bf16_f32 v26, v132, v133\n"
    "v_cvt_pk_bf16_f32 v27, v134, v135\n"
    "v_cvt_pk_bf16_f32 v28, v136, v137\n"
    "v_cvt_pk_bf16_f32 v29, v138, v139\n"
    "v_exp_f32 v152, v152 clamp\n"
    "v_cvt_pk_bf16_f32 v30, v140, v141\n"
    "v_cvt_pk_bf16_f32 v31, v142, v143\n"
    "v_mfma_f32_16x16x32_bf16 v[128:131], v[0:3], v[160:163], v[112:115]\n"
    "v_exp_f32 v144, v144 clamp\n"
    "v_mfma_f32_16x16x32_bf16 v[128:131], v[4:7], v[164:167], v[128:131]\n"
    "v_exp_f32 v145, v145 clamp\n"
    "v_mfma_f32_16x16x32_bf16 v[132:135], v[8:11], v[160:163], v[116:119]\n"
    "v_exp_f32 v146, v146 clamp\n"
    "v_mfma_f32_16x16x32_bf16 v[132:135], v[12:15], v[164:167], v[132:135]\n"
    "v_exp_f32 v147, v147 clamp\n"
    "v_mfma_f32_16x16x32_bf16 v[136:139], v[16:19], v[160:163], v[120:123]\n"
    "v_exp_f32 v148, v148 clamp\n"
    "v_mfma_f32_16x16x32_bf16 v[136:139], v[20:23], v[164:167], v[136:139]\n"
    "v_exp_f32 v149, v149 clamp\n"
    "v_mfma_f32_16x16x32_bf16 v[140:143], v[24:27], v[160:163], v[124:127]\n"
    "v_exp_f32 v150, v150 clamp\n"
    "v_mfma_f32_16x16x32_bf16 v[140:143], v[28:31], v[164:167], v[140:143]\n"
    "v_exp_f32 v151, v151 clamp\n"
    "s_add_u32 m0, s68, 4096\n"
    "v_exp_f32 v153, v153 clamp\n"
    "v_add_u32 v253, s67, v244\n"
    "v_mfma_f32_4x4x4_16b_bf16 v[224:227], v[228:229], v[168:169], v[224:227] cbsz:4 abid:0\n"
    "v_exp_f32 v154, v154 clamp\n"
    "v_exp_f32 v155, v155 clamp\n"
    "v_mfma_f32_4x4x4_16b_bf16 v[224:227], v[228:229], v[170:171], v[224:227] cbsz:4 abid:1\n"
    "v_exp_f32 v156, v156 clamp\n"
    "v_exp_f32 v157, v157 clamp\n"
    "v_mfma_f32_4x4x4_16b_bf16 v[224:227], v[228:229], v[172:173], v[224:227] cbsz:4 abid:2\n"
    "v_exp_f32 v158, v158 clamp\n"
    "v_exp_f32 v159, v159 clamp\n"
    "v_mfma_f32_4x4x4_16b_bf16 v[224:227], v[228:229], v[174:175], v[224:227] cbsz:4 abid:3\n"
    "s_waitcnt vmcnt(4)\n"
    "ds_read_b64_tr_b16 v[168:169], v240 offset:4096\n"
    "ds_read_b64_tr_b16 v[170:171], v241 offset:4352\n"
    "ds_read_b64_tr_b16 v[172:173], v242 offset:4608\n"
    "ds_read_b64_tr_b16 v[174:175], v243 offset:4864\n"
    "ds_read_b32 v236, v253 offset:0\n"
    "ds_read_b32 v238, v253 offset:4\n"
    "ds_read_b128 v[160:163], v234 offset:6144\n"
    "ds_read_b128 v[164:167], v235 offset:6144\n"
    "s_waitcnt lgkmcnt(8)\n"
    "s_nop 0\n"
    "v_readlane_b32 s20, v252, 0\n"
    "v_readlane_b32 s21, v252, 1\n"
    "v_readlane_b32 s22, v252, 2\n"
    "v_readlane_b32 s23, v252, 3\n"
    "v_readlane_b32 s24, v252, 4\n"
    "v_readlane_b32 s25, v252, 5\n"
    "v_readlane_b32 s26, v252, 6\n"
    "v_readlane_b32 s27, v252, 7\n"
    "v_pk_fma_f32 v[144:145], v[144:145], s[70:71], v[96:97]\n"
    "v_pk_fma_f32 v[146:147], v[146:147], s[70:71], v[98:99]\n"
    "v_pk_add_f32 v[230:231], v[144:145], v[146:147]\n"
    "v_pk_mul_f32 v[232:233], v[144:145], v[146:147]\n"
    "v_pk_fma_f32 v[148:149], v[148:149], s[70:71], v[100:101]\n"
    "v_pk_fma_f32 v[230:231], v[230:231], v[148:149], v[232:233]\n"
    "v_pk_mul_f32 v[232:233], v[232:233], v[148:149]\n"
    "v_pk_fma_f32 v[150:151], v[150:151], s[70:71], v[102:103]\n"
    "v_pk_fma_f32 v[230:231], v[230:231], v[150:151], v[232:233]\n"
    "v_pk_mul_f32 v[232:233], v[232:233], v[150:151]\n"
    "v_pk_fma_f32 v[152:153], v[152:153], s[70:71], v[104:105]\n"
    "v_pk_fma_f32 v[230:231], v[230:231], v[152:153], v[232:233]\n"
    "v_pk_mul_f32 v[232:233], v[232:233], v[152:153]\n"
    "v_pk_fma_f32 v[154:155], v[154:155], s[70:71], v[106:107]\n"
    "v_pk_fma_f32 v[230:231], v[230:231], v[154:155], v[232:233]\n"
    "v_pk_mul_f32 v[232:233], v[232:233], v[154:155]\n"
    "v_pk_fma_f32 v[156:157], v[156:157], s[70:71], v[108:109]\n"
    "v_pk_fma_f32 v[230:231], v[230:231], v[156:157], v[232:233]\n"
    "v_pk_mul_f32 v[232:233], v[232:233], v[156:157]\n"
    "v_pk_fma_f32 v[158:159], v[158:159], s[70:71], v[110:111]\n"
    "v_pk_fma_f32 v[230:231], v[230:231], v[158:159], v[232:233]\n"
    "v_pk_mul_f32 v[232:233], v[232:233], v[158:159]\n"
    "v_rcp_f32 v232, v232\n"
    "v_rcp_f32 v233, v233\n"
    "v_fma_f32 v230, v230, v232, v247\n"
    "v_fmac_f32 v230, v231, v233\n"
    "v_lshlrev_b32 v232, 2, v249\n"
    "v_lshrrev_b32 v233, 2, v249\n"
    "v_and_b32 v232, 56, v232\n"
    "v_and_b32 v233, 4, v233\n"
    "v_add3_u32 v232, v232, v233, s81\n"
    "ds_read_b32 v232, v232\n"
    "s_add_u32 s81, s81, 64\n"
    "s_waitcnt lgkmcnt(0)\n"
    "v_lshlrev_b32 v232, 1, v232\n"
    "buffer_load_ushort v232, v232, s[44:47], 0 offen\n"
    "s_waitcnt vmcnt(0)\n"
    "v_lshlrev_b32 v232, 16, v232\n"
    "v_mul_f32 v230, v230, v232\n"
    "v_cvt_pk_bf16_f32 v230, v230, v230\n"
    "s_add_u32 s67, s67, 64\n"
    "s_waitcnt lgkmcnt(0)\n"
    "ds_write_b16 v245, v230\n"
    "ds_read_b64 v[228:229], v246\n"
    "s_lshl1_add_u32 s76, s72, s76\n"
    "buffer_load_dwordx4 v[236:237], s[40:43], 0 idxen offen offset:0 nt lds\n"
    "buffer_load_dwordx4 v[238:239], s[40:43], 0 idxen offen offset:1024 nt lds\n"
    "s_and_b32 s62, s58, 63\n"
    "s_mul_i32 s62, s62, 272\n"
    "s_mov_b32 s58, s59\n"
    "s_mov_b32 s59, s60\n"
    "s_mov_b32 s81, s65\n"
    "s_xor_b32 s65, s65, s64\n"
    "s_mov_b32 s80, s83\n"
    "s_add_u32 s82, s65, 256\n"
    "s_cmp_lt_i32 s59, s57\n"
    "s_cselect_b32 s76, s76, -1\n"
    "s_bitcmp1_b32 s58, 30\n"
    "s_cbranch_scc0 .Ldin_A2\n"
    ".Ldin_A2s:\n"
    "v_mfma_f32_16x16x32_bf16 v[144:147], v[0:3], v[160:163], v[112:115]\n"
    "v_exp_f32 v128, v128 clamp\n"
    "v_mfma_f32_16x16x32_bf16 v[144:147], v[4:7], v[164:167], v[144:147]\n"
    "v_exp_f32 v129, v129 clamp\n"
    "v_mfma_f32_16x16x32_bf16 v[148:151], v[8:11], v[160:163], v[116:119]\n"
    "v_exp_f32 v130, v130 clamp\n"
    "v_mfma_f32_16x16x32_bf16 v[148:151], v[12:15], v[164:167], v[148:151]\n"
    "v_exp_f32 v131, v131 clamp\n"
    "v_mfma_f32_16x16x32_bf16 v[152:155], v[16:19], v[160:163], v[120:123]\n"
    "v_exp_f32 v132, v132 clamp\n"
    "v_mfma_f32_16x16x32_bf16 v[152:155], v[20:23], v[164:167], v[152:155]\n"
    "v_exp_f32 v133, v133 clamp\n"
    "v_mfma_f32_16x16x32_bf16 v[156:159], v[24:27], v[160:163], v[124:127]\n"
    "v_exp_f32 v134, v134 clamp\n"
    "v_mfma_f32_16x16x32_bf16 v[156:159], v[28:31], v[164:167], v[156:159]\n"
    "v_exp_f32 v135, v135 clamp\n"
    "v_exp_f32 v136, v136 clamp\n"
    "s_mov_b32 m0, s62\n"
    "v_exp_f32 v137, v137 clamp\n"
    "v_add_u32 v253, s67, v244\n"
    "s_waitcnt lgkmcnt(0)\n"
    "v_mfma_f32_4x4x4_16b_bf16 v[224:227], v[228:229], v[176:177], v[224:227] cbsz:4 abid:0\n"
    "v_exp_f32 v138, v138 clamp\n"
    "v_exp_f32 v139, v139 clamp\n"
    "v_mfma_f32_4x4x4_16b_bf16 v[224:227], v[228:229], v[178:179], v[224:227] cbsz:4 abid:1\n"
    "v_exp_f32 v140, v140 clamp\n"
    "v_exp_f32 v141, v141 clamp\n"
    "v_mfma_f32_4x4x4_16b_bf16 v[224:227], v[228:229], v[180:181], v[224:227] cbsz:4 abid:2\n"
    "v_exp_f32 v142, v142 clamp\n"
    "v_exp_f32 v143, v143 clamp\n"
    "v_mfma_f32_4x4x4_16b_bf16 v[224:227], v[228:229], v[182:183], v[224:227] cbsz:4 abid:3\n"
    "s_waitcnt vmcnt(4)\n"
    "ds_read_b64_tr_b16 v[176:177], v240 offset:6144\n"
    "ds_read_b64_tr_b16 v[178:179], v241 offset:6400\n"
    "ds_read_b64_tr_b16 v[180:181], v242 offset:6656\n"
    "ds_read_b64_tr_b16 v[182:183], v243 offset:6912\n"
    "ds_read_b32 v236, v253 offset:0\n"
    "ds_read_b32 v238, v253 offset:4\n"
    "ds_read_b128 v[160:163], v234 offset:0\n"
    "ds_read_b128 v[164:167], v235 offset:0\n"
    "s_cbranch_vccz .Ldin_A2s_np\n"
    "s_setprio 1\n"
    ".Ldin_A2s_np:\n"
    "v_pk_fma_f32 v[128:129], v[128:129], s[70:71], v[96:97]\n"
    "v_pk_fma_f32 v[130:131], v[130:131], s[70:71], v[98:99]\n"
    "v_pk_add_f32 v[190:191], v[224:225], v[226:227]\n"
    "v_add_f32 v190, v190, v191\n"
    "ds_write_addtid_b32 v190 offset:32768\n"
    "s_add_u32 m0, s68, 6144\n"
    "s_bfe_u32 s94, s59, 0x180006\n"
    "s_mul_i32 s86, s94, s56\n"
    "v_mbcnt_lo_u32_b32 v184, s20, 0\n"
    "v_mbcnt_lo_u32_b32 v185, s22, 0\n"
    "v_mbcnt_lo_u32_b32 v186, s24, 0\n"
    "v_mbcnt_lo_u32_b32 v187, s26, 0\n"
    "s_bcnt1_i32_b64 s90, s[20:21]\n"
    "s_bcnt1_i32_b64 s91, s[22:23]\n"
    "s_bcnt1_i32_b64 s92, s[24:25]\n"
    "s_bcnt1_i32_b64 s93, s[26:27]\n"
    "v_mbcnt_hi_u32_b32 v184, s21, v184\n"
    "v_mbcnt_hi_u32_b32 v185, s23, v185\n"
    "v_mbcnt_hi_u32_b32 v186, s25, v186\n"
    "v_mbcnt_hi_u32_b32 v187, s27, v187\n"
    "s_lshl2_add_u32 s90, s90, s65\n"
    "s_lshl2_add_u32 s91, s91, s90\n"
    "s_lshl2_add_u32 s92, s92, s91\n"
    "s_lshl2_add_u32 s93, s93, s92\n"
    "v_add_u32_e64 v188, s86, v251\n"
    "v_pk_add_f32 v[230:231], v[128:129], v[130:131]\n"
    "v_pk_mul_f32 v[232:233], v[128:129], v[130:131]\n"
    "v_pk_fma_f32 v[132:133], v[132:133], s[70:71], v[100:101]\n"
    "v_pk_fma_f32 v[230:231], v[230:231], v[132:133], v[232:233]\n"
    "v_pk_mul_f32 v[232:233], v[232:233], v[132:133]\n"
    "v_lshl_add_u32 v184, v184, 2, s65\n"
    "v_lshl_add_u32 v185, v185, 2, s90\n"
    "v_lshl_add_u32 v186, v186, 2, s91\n"
    "v_lshl_add_u32 v187, v187, 2, s92\n"
    "v_mov_b32 v191, -1\n"
    "v_lshl_add_u32 v190, v249, 1, s93\n"
    "ds_write2_b32 v190, v191, v191 offset1:64\n"
    "s_mov_b64 exec, s[20:21]\n"
    "ds_write_b32 v184, v188\n"
    "s_mov_b64 exec, s[22:23]\n"
    "v_add_u32 v189, 64, v188\n"
    "ds_write_b32 v185, v189\n"
    "s_mov_b64 exec, s[24:25]\n"
    "v_add_u32 v190, 128, v188\n"
    "ds_write_b32 v186, v190\n"
    "s_mov_b64 exec, s[26:27]\n"
    "v_add_u32 v189, 192, v188\n"
    "ds_write_b32 v187, v189\n"
    "s_mov_b64 exec, -1\n"
    "s_add_u32 s95, s93, 60\n"
    "s_add_u32 s94, s82, 128\n"
    "s_andn2_b32 s95, s95, 63\n"
    "s_max_u32 s83, s95, s94\n"
    "v_pk_fma_f32 v[134:135], v[134:135], s[70:71], v[102:103]\n"
    "v_pk_fma_f32 v[230:231], v[230:231], v[134:135], v[232:233]\n"
    "v_pk_mul_f32 v[232:233], v[232:233], v[134:135]\n"
    "v_pk_fma_f32 v[136:137], v[136:137], s[70:71], v[104:105]\n"
    "v_pk_fma_f32 v[230:231], v[230:231], v[136:137], v[232:233]\n"
    "v_pk_mul_f32 v[232:233], v[232:233], v[136:137]\n"
    "v_pk_fma_f32 v[138:139], v[138:139], s[70:71], v[106:107]\n"
    "v_pk_fma_f32 v[230:231], v[230:231], v[138:139], v[232:233]\n"
    "v_pk_mul_f32 v[232:233], v[232:233], v[138:139]\n"
    "v_pk_fma_f32 v[140:141], v[140:141], s[70:71], v[108:109]\n"
    "v_pk_fma_f32 v[230:231], v[230:231], v[140:141], v[232:233]\n"
    "v_pk_mul_f32 v[232:233], v[232:233], v[140:141]\n"
    "v_pk_fma_f32 v[142:143], v[142:143], s[70:71], v[110:111]\n"
    "v_pk_fma_f32 v[230:231], v[230:231], v[142:143], v[232:233]\n"
    "v_pk_mul_f32 v[232:233], v[232:233], v[142:143]\n"
    "v_rcp_f32 v232, v232\n"
    "v_rcp_f32 v233, v233\n"
    "v_fma_f32 v230, v230, v232, v247\n"
    "v_fmac_f32 v230, v231, v233\n"
    "v_lshlrev_b32 v232, 2, v249\n"
    "v_lshrrev_b32 v233, 2, v249\n"
    "v_and_b32 v232, 56, v232\n"
    "v_and_b32 v233, 4, v233\n"
    "v_add3_u32 v232, v232, v233, s81\n"
    "ds_read_b32 v232, v232\n"
    "s_add_u32 s81, s81, 64\n"
    "s_waitcnt lgkmcnt(0)\n"
    "v_lshlrev_b32 v232, 1, v232\n"
    "buffer_load_ushort v232, v232, s[44:47], 0 offen\n"
    "s_waitcnt vmcnt(0)\n"
    "v_lshlrev_b32 v232, 16, v232\n"
    "v_mul_f32 v230, v230, v232\n"
    "v_cvt_pk_bf16_f32 v230, v230, v230\n"
    "s_add_u32 s67, s67, 64\n"
    "s_waitcnt lgkmcnt(0)\n"
    "ds_write_b16 v245, v230\n"
    "ds_read_b64 v[228:229], v246\n"
    "buffer_load_dwordx4 v[236:237], s[40:43], 0 idxen offen offset:0 nt lds\n"
    "buffer_load_dwordx4 v[238:239], s[40:43], 0 idxen offen offset:1024 nt lds\n"
    ".Ldin_B3s:\n"
    "v_mfma_f32_16x16x32_bf16 v[128:131], v[0:3], v[160:163], v[112:115]\n"
    "v_exp_f32 v144, v144 clamp\n"
    "v_mfma_f32_16x16x32_bf16 v[128:131], v[4:7], v[164:167], v[128:131]\n"
    "v_exp_f32 v145, v145 clamp\n"
    "v_mfma_f32_16x16x32_bf16 v[132:135], v[8:11], v[160:163], v[116:119]\n"
    "v_exp_f32 v146, v146 clamp\n"
    "v_mfma_f32_16x16x32_bf16 v[132:135], v[12:15], v[164:167], v[132:135]\n"
    "v_exp_f32 v147, v147 clamp\n"
    "v_mfma_f32_16x16x32_bf16 v[136:139], v[16:19], v[160:163], v[120:123]\n"
    "v_exp_f32 v148, v148 clamp\n"
    "v_mfma_f32_16x16x32_bf16 v[136:139], v[20:23], v[164:167], v[136:139]\n"
    "v_exp_f32 v149, v149 clamp\n"
    "v_mfma_f32_16x16x32_bf16 v[140:143], v[24:27], v[160:163], v[124:127]\n"
    "v_exp_f32 v150, v150 clamp\n"
    "v_mfma_f32_16x16x32_bf16 v[140:143], v[28:31], v[164:167], v[140:143]\n"
    "v_exp_f32 v151, v151 clamp\n"
    "v_exp_f32 v152, v152 clamp\n"
    "s_cmp_eq_u32 s67, s80\n"
    "s_cselect_b32 s67, s65, s67\n"
    "s_and_b32 s94, s59, 63\n"
    "s_mul_i32 s94, s94, 272\n"
    "v_add_u32 v252, s94, v250\n"
    "s_add_u32 m0, s68, 0\n"
    "v_exp_f32 v153, v153 clamp\n"
    "v_add_u32_e64 v253, s67, v244\n"
    "s_waitcnt lgkmcnt(0)\n"
    "v_mfma_f32_4x4x4_16b_bf16 v[224:227], v[228:229], v[168:169], 0 cbsz:4 abid:0\n"
    "v_exp_f32 v154, v154 clamp\n"
    "v_exp_f32 v155, v155 clamp\n"
    "v_mfma_f32_4x4x4_16b_bf16 v[224:227], v[228:229], v[170:171], v[224:227] cbsz:4 abid:1\n"
    "v_exp_f32 v156, v156 clamp\n"
    "v_exp_f32 v157, v157 clamp\n"
    "v_mfma_f32_4x4x4_16b_bf16 v[224:227], v[228:229], v[172:173], v[224:227] cbsz:4 abid:2\n"
    "v_exp_f32 v158, v158 clamp\n"
    "v_exp_f32 v159, v159 clamp\n"
    "v_mfma_f32_4x4x4_16b_bf16 v[224:227], v[228:229], v[174:175], v[224:227] cbsz:4 abid:3\n"
    "s_waitcnt vmcnt(4)\n"
    "s_memrealtime s[34:35]\n"
    "ds_read_b64_tr_b16 v[168:169], v240 offset:0\n"
    "ds_read_b64_tr_b16 v[170:171], v241 offset:256\n"
    "ds_read_b64_tr_b16 v[172:173], v242 offset:512\n"
    "ds_read_b64_tr_b16 v[174:175], v243 offset:768\n"
    "ds_read_b32 v236, v253 offset:0\n"
    "ds_read_b32 v238, v253 offset:4\n"
    "ds_read_b128 v[160:163], v234 offset:2048\n"
    "ds_read_b128 v[164:167], v235 offset:2048\n"
    "v_add_u32_e64 v184, v248, v252\n"
    "ds_read_b64 v[184:185], v184\n"
    "v_pk_fma_f32 v[144:145], v[144:145], s[70:71], v[96:97]\n"
    "v_pk_fma_f32 v[146:147], v[146:147], s[70:71], v[98:99]\n"
    "v_pk_add_f32 v[230:231], v[144:145], v[146:147]\n"
    "v_pk_mul_f32 v[232:233], v[144:145], v[146:147]\n"
    "v_pk_fma_f32 v[148:149], v[148:149], s[70:71], v[100:101]\n"
    "v_pk_fma_f32 v[230:231], v[230:231], v[148:149], v[232:233]\n"
    "v_pk_mul_f32 v[232:233], v[232:233], v[148:149]\n"
    "v_pk_fma_f32 v[150:151], v[150:151], s[70:71], v[102:103]\n"
    "v_pk_fma_f32 v[230:231], v[230:231], v[150:151], v[232:233]\n"
    "v_pk_mul_f32 v[232:233], v[232:233], v[150:151]\n"
    "v_pk_fma_f32 v[152:153], v[152:153], s[70:71], v[104:105]\n"
    "v_pk_fma_f32 v[230:231], v[230:231], v[152:153], v[232:233]\n"
    "v_pk_mul_f32 v[232:233], v[232:233], v[152:153]\n"
    "v_pk_fma_f32 v[154:155], v[154:155], s[70:71], v[106:107]\n"
    "v_pk_fma_f32 v[230:231], v[230:231], v[154:155], v[232:233]\n"
    "v_pk_mul_f32 v[232:233], v[232:233], v[154:155]\n"
    "v_pk_fma_f32 v[156:157], v[156:157], s[70:71], v[108:109]\n"
    "v_pk_fma_f32 v[230:231], v[230:231], v[156:157], v[232:233]\n"
    "v_pk_mul_f32 v[232:233], v[232:233], v[156:157]\n"
    "v_pk_fma_f32 v[158:159], v[158:159], s[70:71], v[110:111]\n"
    "v_pk_fma_f32 v[230:231], v[230:231], v[158:159], v[232:233]\n"
    "v_pk_mul_f32 v[232:233], v[232:233], v[158:159]\n"
    "v_rcp_f32 v232, v232\n"
    "v_rcp_f32 v233, v233\n"
    "v_fma_f32 v230, v230, v232, v247\n"
    "v_fmac_f32 v230, v231, v233\n"
    "v_lshlrev_b32 v232, 2, v249\n"
    "v_lshrrev_b32 v233, 2, v249\n"
    "v_and_b32 v232, 56, v232\n"
    "v_and_b32 v233, 4, v233\n"
    "v_add3_u32 v232, v232, v233, s81\n"
    "ds_read_b32 v232, v232\n"
    "s_add_u32 s81, s81, 64\n"
    "s_waitcnt lgkmcnt(0)\n"
    "v_lshlrev_b32 v232, 1, v232\n"
    "buffer_load_ushort v232, v232, s[44:47], 0 offen\n"
    "s_waitcnt vmcnt(0)\n"
    "v_lshlrev_b32 v232, 16, v232\n"
    "v_mul_f32 v230, v230, v232\n"
    "v_cvt_pk_bf16_f32 v230, v230, v230\n"
    "s_add_u32 s67, s67, 64\n"
    "s_waitcnt lgkmcnt(0)\n"
    "ds_write_b16 v245, v230\n"
    "ds_read_b64 v[228:229], v246\n"
    "v_and_b32 v184, v254, v184\n"
    "v_and_b32 v185, v255, v185\n"
    "s_cmp_le_u32 s34, s76\n"
    "s_cbranch_scc1 .Ldin_B3s_wait\n"
    ".Ldin_B3s_go:\n"
    "s_sub_u32 s94, s34, 60\n"
    "s_max_u32 s76, s76, s94\n"
    "s_add_u32 s76, s76, s72\n"
    ".Ldin_B3s_dma:\n"
    "buffer_load_dwordx4 v[236:237], s[40:43], 0 idxen offen offset:0 nt lds\n"
    "buffer_load_dwordx4 v[238:239], s[40:43], 0 idxen offen offset:1024 nt lds\n"
    ".Ldin_B3s_nd:\n"
    "s_branch .Ldin_G0s\n"
    ".Ldin_L1s_poll:\n"
    "s_sleep 1\n"
    "ds_read_b64 v[232:233], v253\n"
    "s_waitcnt lgkmcnt(0)\n"
    "v_readfirstlane_b32 s60, v232\n"
    "s_cmp_eq_u32 s60, -1\n"
    "s_cbranch_scc1 .Ldin_L1s_poll\n"
    "s_branch .Ldin_L1s_pr\n"
    ".Ldin_B3s_wait:\n"
    "s_cmp_lt_i32 s59, s57\n"
    "s_cbranch_scc1 .Ldin_B3s_w2\n"
    "s_sub_u32 s94, s67, s65\n"
    "s_cmp_gt_u32 s94, 256\n"
    "s_cbranch_scc1 .Ldin_B3s_dma\n"
    "s_cmp_eq_u32 s94, 64\n"
    "s_waitcnt vmcnt(2)\n"
    "s_cbranch_scc1 .Ldin_B3s_nd\n"
    "s_waitcnt vmcnt(0)\n"
    "s_branch .Ldin_B3s_nd\n"
    ".Ldin_B3s_slp:\n"
    "s_sleep 1\n"
    ".Ldin_B3s_w2:\n"
    "s_memrealtime s[34:35]\n"
    "s_waitcnt lgkmcnt(0)\n"
    "s_sub_u32 s94, s34, s76\n"
    "s_cmp_lt_i32 s94, 0\n"
    "s_cbranch_scc1 .Ldin_B3s_slp\n"
    "s_branch .Ldin_B3s_go\n"
    ".p2align 5\n"
    ".Ldin_L0s:\n"
    "s_setprio 0\n"
    "s_cmp_ge_i32 s59, s57\n"
    "s_cbranch_scc1 .Ldin_E0s\n"
    "s_add_u32 s76, s76, s73\n"
    "v_mov_b32_e64 v253, s63\n"
    "ds_read_b64 v[232:233], v253\n"
    "v_mfma_f32_4x4x4_16b_bf16 v[144:147], v[184:185], v[32:33], v[64:67] cbsz:2 abid:0\n"
    "ds_read_b128 v[112:115], v252 offset:17408\n"
    "v_mfma_f32_4x4x4_16b_bf16 v[148:151], v[184:185], v[34:35], v[68:71] cbsz:2 abid:1\n"
    "ds_read_b128 v[116:119], v252 offset:17424\n"
    "v_mfma_f32_4x4x4_16b_bf16 v[152:155], v[184:185], v[36:37], v[72:75] cbsz:2 abid:2\n"
    "ds_read_b128 v[120:123], v252 offset:17536\n"
    "ds_read_b128 v[124:127], v252 offset:17552\n"
    "v_cvt_pk_bf16_f32 v0, v144, v145\n"
    "v_cvt_pk_bf16_f32 v1, v146, v147\n"
    "v_mfma_f32_4x4x4_16b_bf16 v[156:159], v[184:185], v[38:39], v[76:79] cbsz:2 abid:3\n"
    "v_cvt_pk_bf16_f32 v2, v148, v149\n"
    "v_cvt_pk_bf16_f32 v3, v150, v151\n"
    "v_mfma_f32_4x4x4_16b_bf16 v[144:147], v[184:185], v[40:41], v[80:83] cbsz:2 abid:0\n"
    "v_cvt_pk_bf16_f32 v4, v152, v153\n"
    "v_cvt_pk_bf16_f32 v5, v154, v155\n"
    "v_mfma_f32_4x4x4_16b_bf16 v[148:151], v[184:185], v[42:43], v[84:87] cbsz:2 abid:1\n"
    "v_cvt_pk_bf16_f32 v6, v156, v157\n"
    "v_cvt_pk_bf16_f32 v7, v158, v159\n"
    "v_mfma_f32_4x4x4_16b_bf16 v[152:155], v[184:185], v[44:45], v[88:91] cbsz:2 abid:2\n"
    "v_cvt_pk_bf16_f32 v8, v144, v145\n"
    "v_cvt_pk_bf16_f32 v9, v146, v147\n"
    "v_mfma_f32_4x4x4_16b_bf16 v[156:159], v[184:185], v[46:47], v[92:95] cbsz:2 abid:3\n"
    "v_cvt_pk_bf16_f32 v10, v148, v149\n"
    "v_cvt_pk_bf16_f32 v11, v150, v151\n"
    "v_mfma_f32_4x4x4_16b_bf16 v[144:147], v[184:185], v[48:49], v[192:195] cbsz:2 abid:0\n"
    "v_cvt_pk_bf16_f32 v12, v152, v153\n"
    "v_cvt_pk_bf16_f32 v13, v154, v155\n"
    "v_mfma_f32_4x4x4_16b_bf16 v[148:151], v[184:185], v[50:51], v[196:199] cbsz:2 abid:1\n"
    "v_cvt_pk_bf16_f32 v14, v156, v157\n"
    "v_cvt_pk_bf16_f32 v15, v158, v159\n"
    "v_mfma_f32_4x4x4_16b_bf16 v[152:155], v[184:185], v[52:53], v[200:203] cbsz:2 abid:2\n"
    "s_waitcnt lgkmcnt(0)\n"
    "v_readfirstlane_b32 s60, v232\n"
    "v_cvt_pk_bf16_f32 v16, v144, v145\n"
    "v_cvt_pk_bf16_f32 v17, v146, v147\n"
    "v_mfma_f32_4x4x4_16b_bf16 v[156:159], v[184:185], v[54:55], v[204:207] cbsz:2 abid:3\n"
    "s_cmp_eq_u32 s60, -1\n"
    "s_cbranch_scc1 .Ldin_L0s_poll\n"
    ".Ldin_L0s_pr:\n"
    "v_readfirstlane_b32 s95, v233\n"
    "s_and_b32 s94, s60, 63\n"
    "s_lshl_b32 s94, s94, 6\n"
    "s_add_u32 s94, s94, 68480\n"
    "s_add_u32 s63, s63, 8\n"
    "v_lshl_add_u32 v252, v249, 1, s94\n"
    "ds_read_b32 v252, v252\n"
    "s_sub_u32 s95, s95, s76\n"
    "s_ashr_i32 s95, s95, 4\n"
    "s_min_i32 s95, s95, s74\n"
    "s_max_i32 s72, s95, s75\n"
    "v_cvt_pk_bf16_f32 v18, v148, v149\n"
    "v_cvt_pk_bf16_f32 v19, v150, v151\n"
    "v_mfma_f32_4x4x4_16b_bf16 v[144:147], v[184:185], v[56:57], v[208:211] cbsz:2 abid:0\n"
    "v_cvt_pk_bf16_f32 v20, v152, v153\n"
    "v_cvt_pk_bf16_f32 v21, v154, v155\n"
    "v_mfma_f32_4x4x4_16b_bf16 v[148:151], v[184:185], v[58:59], v[212:215] cbsz:2 abid:1\n"
    "v_cvt_pk_bf16_f32 v22, v156, v157\n"
    "v_cvt_pk_bf16_f32 v23, v158, v159\n"
    "v_mfma_f32_4x4x4_16b_bf16 v[152:155], v[184:185], v[60:61], v[216:219] cbsz:2 abid:2\n"
    "v_cvt_pk_bf16_f32 v24, v144, v145\n"
    "v_cvt_pk_bf16_f32 v25, v146, v147\n"
    "v_mfma_f32_4x4x4_16b_bf16 v[156:159], v[184:185], v[62:63], v[220:223] cbsz:2 abid:3\n"
    "v_cvt_pk_bf16_f32 v26, v148, v149\n"
    "v_cvt_pk_bf16_f32 v27, v150, v151\n"
    "v_cvt_pk_bf16_f32 v28, v152, v153\n"
    "v_cvt_pk_bf16_f32 v29, v154, v155\n"
    "v_exp_f32 v136, v136 clamp\n"
    "v_cvt_pk_bf16_f32 v30, v156, v157\n"
    "v_cvt_pk_bf16_f32 v31, v158, v159\n"
    "v_mfma_f32_16x16x32_bf16 v[144:147], v[0:3], v[160:163], v[112:115]\n"
    "v_exp_f32 v128, v128 clamp\n"
    "v_mfma_f32_16x16x32_bf16 v[144:147], v[4:7], v[164:167], v[144:147]\n"
    "v_exp_f32 v129, v129 clamp\n"
    "v_mfma_f32_16x16x32_bf16 v[148:151], v[8:11], v[160:163], v[116:119]\n"
    "v_exp_f32 v130, v130 clamp\n"
    "v_mfma_f32_16x16x32_bf16 v[148:151], v[12:15], v[164:167], v[148:151]\n"
    "v_exp_f32 v131, v131 clamp\n"
    "v_mfma_f32_16x16x32_bf16 v[152:155], v[16:19], v[160:163], v[120:123]\n"
    "v_exp_f32 v132, v132 clamp\n"
    "v_mfma_f32_16x16x32_bf16 v[152:155], v[20:23], v[164:167], v[152:155]\n"
    "v_exp_f32 v133, v133 clamp\n"
    "v_mfma_f32_16x16x32_bf16 v[156:159], v[24:27], v[160:163], v[124:127]\n"
    "v_exp_f32 v134, v134 clamp\n"
    "v_mfma_f32_16x16x32_bf16 v[156:159], v[28:31], v[164:167], v[156:159]\n"
    "v_exp_f32 v135, v135 clamp\n"
    "s_add_u32 m0, s68, 2048\n"
    "v_exp_f32 v137, v137 clamp\n"
    "v_add_u32 v253, s67, v244\n"
    "v_mfma_f32_4x4x4_16b_bf16 v[224:227], v[228:229], v[176:177], v[224:227] cbsz:4 abid:0\n"
    "v_exp_f32 v138, v138 clamp\n"
    "v_exp_f32 v139, v139 clamp\n"
    "v_mfma_f32_4x4x4_16b_bf16 v[224:227], v[228:229], v[178:179], v[224:227] cbsz:4 abid:1\n"
    "v_exp_f32 v140, v140 clamp\n"
    "v_exp_f32 v141, v141 clamp\n"
    "v_mfma_f32_4x4x4_16b_bf16 v[224:227], v[228:229], v[180:181], v[224:227] cbsz:4 abid:2\n"
    "v_exp_f32 v142, v142 clamp\n"
    "v_exp_f32 v143, v143 clamp\n"
    "v_mfma_f32_4x4x4_16b_bf16 v[224:227], v[228:229], v[182:183], v[224:227] cbsz:4 abid:3\n"
    "s_waitcnt vmcnt(4)\n"
    "ds_read_b64_tr_b16 v[176:177], v240 offset:2048\n"
    "ds_read_b64_tr_b16 v[178:179], v241 offset:2304\n"
    "ds_read_b64_tr_b16 v[180:181], v242 offset:2560\n"
    "ds_read_b64_tr_b16 v[182:183], v243 offset:2816\n"
    "ds_read_b32 v236, v253 offset:0\n"
    "ds_read_b32 v238, v253 offset:4\n"
    "ds_read_b128 v[160:163], v234 offset:4096\n"
    "ds_read_b128 v[164:167], v235 offset:4096\n"
    "s_waitcnt lgkmcnt(8)\n"
    "s_nop 0\n"
    "v_readlane_b32 s20, v252, 0\n"
    "v_readlane_b32 s21, v252, 1\n"
    "v_readlane_b32 s22, v252, 2\n"
    "v_readlane_b32 s23, v252, 3\n"
    "v_readlane_b32 s24, v252, 4\n"
    "v_readlane_b32 s25, v252, 5\n"
    "v_readlane_b32 s26, v252, 6\n"
    "v_readlane_b32 s27, v252, 7\n"
    "v_pk_fma_f32 v[128:129], v[128:129], s[70:71], v[96:97]\n"
    "v_pk_fma_f32 v[130:131], v[130:131], s[70:71], v[98:99]\n"
    "v_pk_add_f32 v[230:231], v[128:129], v[130:131]\n"
    "v_pk_mul_f32 v[232:233], v[128:129], v[130:131]\n"
    "v_pk_fma_f32 v[132:133], v[132:133], s[70:71], v[100:101]\n"
    "v_pk_fma_f32 v[230:231], v[230:231], v[132:133], v[232:233]\n"
    "v_pk_mul_f32 v[232:233], v[232:233], v[132:133]\n"
    "v_pk_fma_f32 v[134:135], v[134:135], s[70:71], v[102:103]\n"
    "v_pk_fma_f32 v[230:231], v[230:231], v[134:135], v[232:233]\n"
    "v_pk_mul_f32 v[232:233], v[232:233], v[134:135]\n"
    "v_pk_fma_f32 v[136:137], v[136:137], s[70:71], v[104:105]\n"
    "v_pk_fma_f32 v[230:231], v[230:231], v[136:137], v[232:233]\n"
    "v_pk_mul_f32 v[232:233], v[232:233], v[136:137]\n"
    "v_pk_fma_f32 v[138:139], v[138:139], s[70:71], v[106:107]\n"
    "v_pk_fma_f32 v[230:231], v[230:231], v[138:139], v[232:233]\n"
    "v_pk_mul_f32 v[232:233], v[232:233], v[138:139]\n"
    "v_pk_fma_f32 v[140:141], v[140:141], s[70:71], v[108:109]\n"
    "v_pk_fma_f32 v[230:231], v[230:231], v[140:141], v[232:233]\n"
    "v_pk_mul_f32 v[232:233], v[232:233], v[140:141]\n"
    "v_pk_fma_f32 v[142:143], v[142:143], s[70:71], v[110:111]\n"
    "v_pk_fma_f32 v[230:231], v[230:231], v[142:143], v[232:233]\n"
    "v_pk_mul_f32 v[232:233], v[232:233], v[142:143]\n"
    "v_rcp_f32 v232, v232\n"
    "v_rcp_f32 v233, v233\n"
    "v_fma_f32 v230, v230, v232, v247\n"
    "v_fmac_f32 v230, v231, v233\n"
    "v_lshlrev_b32 v232, 2, v249\n"
    "v_lshrrev_b32 v233, 2, v249\n"
    "v_and_b32 v232, 56, v232\n"
    "v_and_b32 v233, 4, v233\n"
    "v_add3_u32 v232, v232, v233, s81\n"
    "ds_read_b32 v232, v232\n"
    "s_add_u32 s81, s81, 64\n"
    "s_waitcnt lgkmcnt(0)\n"
    "v_lshlrev_b32 v232, 1, v232\n"
    "buffer_load_ushort v232, v232, s[44:47], 0 offen\n"
    "s_waitcnt vmcnt(0)\n"
    "v_lshlrev_b32 v232, 16, v232\n"
    "v_mul_f32 v230, v230, v232\n"
    "v_cvt_pk_bf16_f32 v230, v230, v230\n"
    "s_add_u32 s67, s67, 64\n"
    "s_waitcnt lgkmcnt(0)\n"
    "ds_write_b16 v245, v230\n"
    "ds_read_b64 v[228:229], v246\n"
    "s_lshl1_add_u32 s76, s72, s76\n"
    "buffer_load_dwordx4 v[236:237], s[40:43], 0 idxen offen offset:0 nt lds\n"
    "buffer_load_dwordx4 v[238:239], s[40:43], 0 idxen offen offset:1024 nt lds\n"
    "s_and_b32 s62, s58, 63\n"
    "s_mul_i32 s62, s62, 272\n"
    "s_mov_b32 s58, s59\n"
    "s_mov_b32 s59, s60\n"
    "s_mov_b32 s81, s65\n"
    "s_xor_b32 s65, s65, s64\n"
    "s_mov_b32 s80, s83\n"
    "s_add_u32 s82, s65, 256\n"
    "s_cmp_lt_i32 s59, s57\n"
    "s_cselect_b32 s76, s76, -1\n"
    "s_bitcmp1_b32 s58, 30\n"
    "s_cbranch_scc0 .Ldin_A1\n"
    ".Ldin_A1s:\n"
    "v_mfma_f32_16x16x32_bf16 v[128:131], v[0:3], v[160:163], v[112:115]\n"
    "v_exp_f32 v144, v144 clamp\n"
    "v_mfma_f32_16x16x32_bf16 v[128:131], v[4:7], v[164:167], v[128:131]\n"
    "v_exp_f32 v145, v145 clamp\n"
    "v_mfma_f32_16x16x32_bf16 v[132:135], v[8:11], v[160:163], v[116:119]\n"
    "v_exp_f32 v146, v146 clamp\n"
    "v_mfma_f32_16x16x32_bf16 v[132:135], v[12:15], v[164:167], v[132:135]\n"
    "v_exp_f32 v147, v147 clamp\n"
    "v_mfma_f32_16x16x32_bf16 v[136:139], v[16:19], v[160:163], v[120:123]\n"
    "v_exp_f32 v148, v148 clamp\n"
    "v_mfma_f32_16x16x32_bf16 v[136:139], v[20:23], v[164:167], v[136:139]\n"
    "v_exp_f32 v149, v149 clamp\n"
    "v_mfma_f32_16x16x32_bf16 v[140:143], v[24:27], v[160:163], v[124:127]\n"
    "v_exp_f32 v150, v150 clamp\n"
    "v_mfma_f32_16x16x32_bf16 v[140:143], v[28:31], v[164:167], v[140:143]\n"
    "v_exp_f32 v151, v151 clamp\n"
    "v_exp_f32 v152, v152 clamp\n"
    "s_mov_b32 m0, s62\n"
    "v_exp_f32 v153, v153 clamp\n"
    "v_add_u32 v253, s67, v244\n"
    "s_waitcnt lgkmcnt(0)\n"
    "v_mfma_f32_4x4x4_16b_bf16 v[224:227], v[228:229], v[168:169], v[224:227] cbsz:4 abid:0\n"
    "v_exp_f32 v154, v154 clamp\n"
    "v_exp_f32 v155, v155 clamp\n"
    "v_mfma_f32_4x4x4_16b_bf16 v[224:227], v[228:229], v[170:171], v[224:227] cbsz:4 abid:1\n"
    "v_exp_f32 v156, v156 clamp\n"
    "v_exp_f32 v157, v157 clamp\n"
    "v_mfma_f32_4x4x4_16b_bf16 v[224:227], v[228:229], v[172:173], v[224:227] cbsz:4 abid:2\n"
    "v_exp_f32 v158, v158 clamp\n"
    "v_exp_f32 v159, v159 clamp\n"
    "v_mfma_f32_4x4x4_16b_bf16 v[224:227], v[228:229], v[174:175], v[224:227] cbsz:4 abid:3\n"
    "s_waitcnt vmcnt(4)\n"
    "ds_read_b64_tr_b16 v[168:169], v240 offset:4096\n"
    "ds_read_b64_tr_b16 v[170:171], v241 offset:4352\n"
    "ds_read_b64_tr_b16 v[172:173], v242 offset:4608\n"
    "ds_read_b64_tr_b16 v[174:175], v243 offset:4864\n"
    "ds_read_b32 v236, v253 offset:0\n"
    "ds_read_b32 v238, v253 offset:4\n"
    "ds_read_b128 v[160:163], v234 offset:6144\n"
    "ds_read_b128 v[164:167], v235 offset:6144\n"
    "s_cbranch_vccz .Ldin_A1s_np\n"
    "s_setprio 1\n"
    ".Ldin_A1s_np:\n"
    "v_pk_fma_f32 v[144:145], v[144:145], s[70:71], v[96:97]\n"
    "v_pk_fma_f32 v[146:147], v[146:147], s[70:71], v[98:99]\n"
    "v_pk_add_f32 v[190:191], v[224:225], v[226:227]\n"
    "v_add_f32 v190, v190, v191\n"
    "ds_write_addtid_b32 v190 offset:32768\n"
    "s_add_u32 m0, s68, 4096\n"
    "s_bfe_u32 s94, s59, 0x180006\n"
    "s_mul_i32 s86, s94, s56\n"
    "v_mbcnt_lo_u32_b32 v184, s20, 0\n"
    "v_mbcnt_lo_u32_b32 v185, s22, 0\n"
    "v_mbcnt_lo_u32_b32 v186, s24, 0\n"
    "v_mbcnt_lo_u32_b32 v187, s26, 0\n"
    "s_bcnt1_i32_b64 s90, s[20:21]\n"
    "s_bcnt1_i32_b64 s91, s[22:23]\n"
    "s_bcnt1_i32_b64 s92, s[24:25]\n"
    "s_bcnt1_i32_b64 s93, s[26:27]\n"
    "v_mbcnt_hi_u32_b32 v184, s21, v184\n"
    "v_mbcnt_hi_u32_b32 v185, s23, v185\n"
    "v_mbcnt_hi_u32_b32 v186, s25, v186\n"
    "v_mbcnt_hi_u32_b32 v187, s27, v187\n"
    "s_lshl2_add_u32 s90, s90, s65\n"
    "s_lshl2_add_u32 s91, s91, s90\n"
    "s_lshl2_add_u32 s92, s92, s91\n"
    "s_lshl2_add_u32 s93, s93, s92\n"
    "v_add_u32_e64 v188, s86, v251\n"
    "v_pk_add_f32 v[230:231], v[144:145], v[146:147]\n"
    "v_pk_mul_f32 v[232:233], v[144:145], v[146:147]\n"
    "v_pk_fma_f32 v[148:149], v[148:149], s[70:71], v[100:101]\n"
    "v_pk_fma_f32 v[230:231], v[230:231], v[148:149], v[232:233]\n"
    "v_pk_mul_f32 v[232:233], v[232:233], v[148:149]\n"
    "v_lshl_add_u32 v184, v184, 2, s65\n"
    "v_lshl_add_u32 v185, v185, 2, s90\n"
    "v_lshl_add_u32 v186, v186, 2, s91\n"
    "v_lshl_add_u32 v187, v187, 2, s92\n"
    "v_mov_b32 v191, -1\n"
    "v_lshl_add_u32 v190, v249, 1, s93\n"
    "ds_write2_b32 v190, v191, v191 offset1:64\n"
    "s_mov_b64 exec, s[20:21]\n"
    "ds_write_b32 v184, v188\n"
    "s_mov_b64 exec, s[22:23]\n"
    "v_add_u32 v189, 64, v188\n"
    "ds_write_b32 v185, v189\n"
    "s_mov_b64 exec, s[24:25]\n"
    "v_add_u32 v190, 128, v188\n"
    "ds_write_b32 v186, v190\n"
    "s_mov_b64 exec, s[26:27]\n"
    "v_add_u32 v189, 192, v188\n"
    "ds_write_b32 v187, v189\n"
    "s_mov_b64 exec, -1\n"
    "s_add_u32 s95, s93, 60\n"
    "s_add_u32 s94, s82, 128\n"
    "s_andn2_b32 s95, s95, 63\n"
    "s_max_u32 s83, s95, s94\n"
    "v_pk_fma_f32 v[150:151], v[150:151], s[70:71], v[102:103]\n"
    "v_pk_fma_f32 v[230:231], v[230:231], v[150:151], v[232:233]\n"
    "v_pk_mul_f32 v[232:233], v[232:233], v[150:151]\n"
    "v_pk_fma_f32 v[152:153], v[152:153], s[70:71], v[104:105]\n"
    "v_pk_fma_f32 v[230:231], v[230:231], v[152:153], v[232:233]\n"
    "v_pk_mul_f32 v[232:233], v[232:233], v[152:153]\n"
    "v_pk_fma_f32 v[154:155], v[154:155], s[70:71], v[106:107]\n"
    "v_pk_fma_f32 v[230:231], v[230:231], v[154:155], v[232:233]\n"
    "v_pk_mul_f32 v[232:233], v[232:233], v[154:155]\n"
    "v_pk_fma_f32 v[156:157], v[156:157], s[70:71], v[108:109]\n"
    "v_pk_fma_f32 v[230:231], v[230:231], v[156:157], v[232:233]\n"
    "v_pk_mul_f32 v[232:233], v[232:233], v[156:157]\n"
    "v_pk_fma_f32 v[158:159], v[158:159], s[70:71], v[110:111]\n"
    "v_pk_fma_f32 v[230:231], v[230:231], v[158:159], v[232:233]\n"
    "v_pk_mul_f32 v[232:233], v[232:233], v[158:159]\n"
    "v_rcp_f32 v232, v232\n"
    "v_rcp_f32 v233, v233\n"
    "v_fma_f32 v230, v230, v232, v247\n"
    "v_fmac_f32 v230, v231, v233\n"
    "v_lshlrev_b32 v232, 2, v249\n"
    "v_lshrrev_b32 v233, 2, v249\n"
    "v_and_b32 v232, 56, v232\n"
    "v_and_b32 v233, 4, v233\n"
    "v_add3_u32 v232, v232, v233, s81\n"
    "ds_read_b32 v232, v232\n"
    "s_add_u32 s81, s81, 64\n"
    "s_waitcnt lgkmcnt(0)\n"
    "v_lshlrev_b32 v232, 1, v232\n"
    "buffer_load_ushort v232, v232, s[44:47], 0 offen\n"
    "s_waitcnt vmcnt(0)\n"
    "v_lshlrev_b32 v232, 16, v232\n"
    "v_mul_f32 v230, v230, v232\n"
    "v_cvt_pk_bf16_f32 v230, v230, v230\n"
    "s_add_u32 s67, s67, 64\n"
    "s_waitcnt lgkmcnt(0)\n"
    "ds_write_b16 v245, v230\n"
    "ds_read_b64 v[228:229], v246\n"
    "buffer_load_dwordx4 v[236:237], s[40:43], 0 idxen offen offset:0 nt lds\n"
    "buffer_load_dwordx4 v[238:239], s[40:43], 0 idxen offen offset:1024 nt lds\n"
    ".Ldin_B2s:\n"
    "v_mfma_f32_16x16x32_bf16 v[144:147], v[0:3], v[160:163], v[112:115]\n"
    "v_exp_f32 v128, v128 clamp\n"
    "v_mfma_f32_16x16x32_bf16 v[144:147], v[4:7], v[164:167], v[144:147]\n"
    "v_exp_f32 v129, v129 clamp\n"
    "v_mfma_f32_16x16x32_bf16 v[148:151], v[8:11], v[160:163], v[116:119]\n"
    "v_exp_f32 v130, v130 clamp\n"
    "v_mfma_f32_16x16x32_bf16 v[148:151], v[12:15], v[164:167], v[148:151]\n"
    "v_exp_f32 v131, v131 clamp\n"
    "v_mfma_f32_16x16x32_bf16 v[152:155], v[16:19], v[160:163], v[120:123]\n"
    "v_exp_f32 v132, v132 clamp\n"
    "v_mfma_f32_16x16x32_bf16 v[152:155], v[20:23], v[164:167], v[152:155]\n"
    "v_exp_f32 v133, v133 clamp\n"
    "v_mfma_f32_16x16x32_bf16 v[156:159], v[24:27], v[160:163], v[124:127]\n"
    "v_exp_f32 v134, v134 clamp\n"
    "v_mfma_f32_16x16x32_bf16 v[156:159], v[28:31], v[164:167], v[156:159]\n"
    "v_exp_f32 v135, v135 clamp\n"
    "v_exp_f32 v136, v136 clamp\n"
    "s_cmp_eq_u32 s67, s80\n"
    "s_cselect_b32 s67, s65, s67\n"
    "s_and_b32 s94, s59, 63\n"
    "s_mul_i32 s94, s94, 272\n"
    "v_add_u32 v252, s94, v250\n"
    "s_add_u32 m0, s68, 6144\n"
    "v_exp_f32 v137, v137 clamp\n"
    "v_add_u32 v253, s67, v244\n"
    "s_waitcnt lgkmcnt(0)\n"
    "v_mfma_f32_4x4x4_16b_bf16 v[224:227], v[228:229], v[176:177], 0 cbsz:4 abid:0\n"
    "v_exp_f32 v138, v138 clamp\n"
    "v_exp_f32 v139, v139 clamp\n"
    "v_mfma_f32_4x4x4_16b_bf16 v[224:227], v[228:229], v[178:179], v[224:227] cbsz:4 abid:1\n"
    "v_exp_f32 v140, v140 clamp\n"
    "v_exp_f32 v141, v141 clamp\n"
    "v_mfma_f32_4x4x4_16b_bf16 v[224:227], v[228:229], v[180:181], v[224:227] cbsz:4 abid:2\n"
    "v_exp_f32 v142, v142 clamp\n"
    "v_exp_f32 v143, v143 clamp\n"
    "v_mfma_f32_4x4x4_16b_bf16 v[224:227], v[228:229], v[182:183], v[224:227] cbsz:4 abid:3\n"
    "s_waitcnt vmcnt(4)\n"
    "s_memrealtime s[34:35]\n"
    "ds_read_b64_tr_b16 v[176:177], v240 offset:6144\n"
    "ds_read_b64_tr_b16 v[178:179], v241 offset:6400\n"
    "ds_read_b64_tr_b16 v[180:181], v242 offset:6656\n"
    "ds_read_b64_tr_b16 v[182:183], v243 offset:6912\n"
    "ds_read_b32 v236, v253 offset:0\n"
    "ds_read_b32 v238, v253 offset:4\n"
    "ds_read_b128 v[160:163], v234 offset:0\n"
    "ds_read_b128 v[164:167], v235 offset:0\n"
    "v_add_u32_e64 v184, v248, v252\n"
    "ds_read_b64 v[184:185], v184\n"
    "v_pk_fma_f32 v[128:129], v[128:129], s[70:71], v[96:97]\n"
    "v_pk_fma_f32 v[130:131], v[130:131], s[70:71], v[98:99]\n"
    "v_pk_add_f32 v[230:231], v[128:129], v[130:131]\n"
    "v_pk_mul_f32 v[232:233], v[128:129], v[130:131]\n"
    "v_pk_fma_f32 v[132:133], v[132:133], s[70:71], v[100:101]\n"
    "v_pk_fma_f32 v[230:231], v[230:231], v[132:133], v[232:233]\n"
    "v_pk_mul_f32 v[232:233], v[232:233], v[132:133]\n"
    "v_pk_fma_f32 v[134:135], v[134:135], s[70:71], v[102:103]\n"
    "v_pk_fma_f32 v[230:231], v[230:231], v[134:135], v[232:233]\n"
    "v_pk_mul_f32 v[232:233], v[232:233], v[134:135]\n"
    "v_pk_fma_f32 v[136:137], v[136:137], s[70:71], v[104:105]\n"
    "v_pk_fma_f32 v[230:231], v[230:231], v[136:137], v[232:233]\n"
    "v_pk_mul_f32 v[232:233], v[232:233], v[136:137]\n"
    "v_pk_fma_f32 v[138:139], v[138:139], s[70:71], v[106:107]\n"
    "v_pk_fma_f32 v[230:231], v[230:231], v[138:139], v[232:233]\n"
    "v_pk_mul_f32 v[232:233], v[232:233], v[138:139]\n"
    "v_pk_fma_f32 v[140:141], v[140:141], s[70:71], v[108:109]\n"
    "v_pk_fma_f32 v[230:231], v[230:231], v[140:141], v[232:233]\n"
    "v_pk_mul_f32 v[232:233], v[232:233], v[140:141]\n"
    "v_pk_fma_f32 v[142:143], v[142:143], s[70:71], v[110:111]\n"
    "v_pk_fma_f32 v[230:231], v[230:231], v[142:143], v[232:233]\n"
    "v_pk_mul_f32 v[232:233], v[232:233], v[142:143]\n"
    "v_rcp_f32 v232, v232\n"
    "v_rcp_f32 v233, v233\n"
    "v_fma_f32 v230, v230, v232, v247\n"
    "v_fmac_f32 v230, v231, v233\n"
    "v_lshlrev_b32 v232, 2, v249\n"
    "v_lshrrev_b32 v233, 2, v249\n"
    "v_and_b32 v232, 56, v232\n"
    "v_and_b32 v233, 4, v233\n"
    "v_add3_u32 v232, v232, v233, s81\n"
    "ds_read_b32 v232, v232\n"
    "s_add_u32 s81, s81, 64\n"
    "s_waitcnt lgkmcnt(0)\n"
    "v_lshlrev_b32 v232, 1, v232\n"
    "buffer_load_ushort v232, v232, s[44:47], 0 offen\n"
    "s_waitcnt vmcnt(0)\n"
    "v_lshlrev_b32 v232, 16, v232\n"
    "v_mul_f32 v230, v230, v232\n"
    "v_cvt_pk_bf16_f32 v230, v230, v230\n"
    "s_add_u32 s67, s67, 64\n"
    "s_waitcnt lgkmcnt(0)\n"
    "ds_write_b16 v245, v230\n"
    "ds_read_b64 v[228:229], v246\n"
    "v_and_b32 v184, v254, v184\n"
    "v_and_b32 v185, v255, v185\n"
    "s_cmp_le_u32 s34, s76\n"
    "s_cbranch_scc1 .Ldin_B2s_wait\n"
    ".Ldin_B2s_go:\n"
    "s_sub_u32 s94, s34, 60\n"
    "s_max_u32 s76, s76, s94\n"
    "s_add_u32 s76, s76, s72\n"
    ".Ldin_B2s_dma:\n"
    "buffer_load_dwordx4 v[236:237], s[40:43], 0 idxen offen offset:0 nt lds\n"
    "buffer_load_dwordx4 v[238:239], s[40:43], 0 idxen offen offset:1024 nt lds\n"
    ".Ldin_B2s_nd:\n"
    "s_branch .Ldin_G3s\n"
    ".Ldin_L0s_poll:\n"
    "s_sleep 1\n"
    "ds_read_b64 v[232:233], v253\n"
    "s_waitcnt lgkmcnt(0)\n"
    "v_readfirstlane_b32 s60, v232\n"
    "s_cmp_eq_u32 s60, -1\n"
    "s_cbranch_scc1 .Ldin_L0s_poll\n"
    "s_branch .Ldin_L0s_pr\n"
    ".Ldin_B2s_wait:\n"
    "s_cmp_lt_i32 s59, s57\n"
    "s_cbranch_scc1 .Ldin_B2s_w2\n"
    "s_sub_u32 s94, s67, s65\n"
    "s_cmp_gt_u32 s94, 256\n"
    "s_cbranch_scc1 .Ldin_B2s_dma\n"
    "s_cmp_eq_u32 s94, 64\n"
    "s_waitcnt vmcnt(2)\n"
    "s_cbranch_scc1 .Ldin_B2s_nd\n"
    "s_waitcnt vmcnt(0)\n"
    "s_branch .Ldin_B2s_nd\n"
    ".Ldin_B2s_slp:\n"
    "s_sleep 1\n"
    ".Ldin_B2s_w2:\n"
    "s_memrealtime s[34:35]\n"
    "s_waitcnt lgkmcnt(0)\n"
    "s_sub_u32 s94, s34, s76\n"
    "s_cmp_lt_i32 s94, 0\n"
    "s_cbranch_scc1 .Ldin_B2s_slp\n"
    "s_branch .Ldin_B2s_go\n"
    ".p2align 5\n"
    ".Ldin_L3s:\n"
    "s_setprio 0\n"
    "s_cmp_ge_i32 s59, s57\n"
    "s_cbranch_scc1 .Ldin_E1s\n"
    "s_add_u32 s76, s76, s73\n"
    "v_mov_b32_e64 v253, s63\n"
    "ds_read_b64 v[232:233], v253\n"
    "v_mfma_f32_4x4x4_16b_bf16 v[128:131], v[184:185], v[32:33], v[64:67] cbsz:2 abid:0\n"
    "ds_read_b128 v[112:115], v252 offset:17408\n"
    "v_mfma_f32_4x4x4_16b_bf16 v[132:135], v[184:185], v[34:35], v[68:71] cbsz:2 abid:1\n"
    "ds_read_b128 v[116:119], v252 offset:17424\n"
    "v_mfma_f32_4x4x4_16b_bf16 v[136:139], v[184:185], v[36:37], v[72:75] cbsz:2 abid:2\n"
    "ds_read_b128 v[120:123], v252 offset:17536\n"
    "ds_read_b128 v[124:127], v252 offset:17552\n"
    "v_cvt_pk_bf16_f32 v0, v128, v129\n"
    "v_cvt_pk_bf16_f32 v1, v130, v131\n"
    "v_mfma_f32_4x4x4_16b_bf16 v[140:143], v[184:185], v[38:39], v[76:79] cbsz:2 abid:3\n"
    "v_cvt_pk_bf16_f32 v2, v132, v133\n"
    "v_cvt_pk_bf16_f32 v3, v134, v135\n"
    "v_mfma_f32_4x4x4_16b_bf16 v[128:131], v[184:185], v[40:41], v[80:83] cbsz:2 abid:0\n"
    "v_cvt_pk_bf16_f32 v4, v136, v137\n"
    "v_cvt_pk_bf16_f32 v5, v138, v139\n"
    "v_mfma_f32_4x4x4_16b_bf16 v[132:135], v[184:185], v[42:43], v[84:87] cbsz:2 abid:1\n"
    "v_cvt_pk_bf16_f32 v6, v140, v141\n"
    "v_cvt_pk_bf16_f32 v7, v142, v143\n"
    "v_mfma_f32_4x4x4_16b_bf16 v[136:139], v[184:185], v[44:45], v[88:91] cbsz:2 abid:2\n"
    "v_cvt_pk_bf16_f32 v8, v128, v129\n"
    "v_cvt_pk_bf16_f32 v9, v130, v131\n"
    "v_mfma_f32_4x4x4_16b_bf16 v[140:143], v[184:185], v[46:47], v[92:95] cbsz:2 abid:3\n"
    "v_cvt_pk_bf16_f32 v10, v132, v133\n"
    "v_cvt_pk_bf16_f32 v11, v134, v135\n"
    "v_mfma_f32_4x4x4_16b_bf16 v[128:131], v[184:185], v[48:49], v[192:195] cbsz:2 abid:0\n"
    "v_cvt_pk_bf16_f32 v12, v136, v137\n"
    "v_cvt_pk_bf16_f32 v13, v138, v139\n"
    "v_mfma_f32_4x4x4_16b_bf16 v[132:135], v[184:185], v[50:51], v[196:199] cbsz:2 abid:1\n"
    "v_cvt_pk_bf16_f32 v14, v140, v141\n"
    "v_cvt_pk_bf16_f32 v15, v142, v143\n"
    "v_mfma_f32_4x4x4_16b_bf16 v[136:139], v[184:185], v[52:53], v[200:203] cbsz:2 abid:2\n"
    "s_waitcnt lgkmcnt(0)\n"
    "v_readfirstlane_b32 s60, v232\n"
    "v_cvt_pk_bf16_f32 v16, v128, v129\n"
    "v_cvt_pk_bf16_f32 v17, v130, v131\n"
    "v_mfma_f32_4x4x4_16b_bf16 v[140:143], v[184:185], v[54:55], v[204:207] cbsz:2 abid:3\n"
    "s_cmp_eq_u32 s60, -1\n"
    "s_cbranch_scc1 .Ldin_L3s_poll\n"
    ".Ldin_L3s_pr:\n"
    "v_readfirstlane_b32 s95, v233\n"
    "s_and_b32 s94, s60, 63\n"
    "s_lshl_b32 s94, s94, 6\n"
    "s_add_u32 s94, s94, 68480\n"
    "s_add_u32 s63, s63, 8\n"
    "v_lshl_add_u32 v252, v249, 1, s94\n"
    "ds_read_b32 v252, v252\n"
    "s_sub_u32 s95, s95, s76\n"
    "s_ashr_i32 s95, s95, 4\n"
    "s_min_i32 s95, s95, s74\n"
    "s_max_i32 s72, s95, s75\n"
    "v_cvt_pk_bf16_f32 v18, v132, v133\n"
    "v_cvt_pk_bf16_f32 v19, v134, v135\n"
    "v_mfma_f32_4x4x4_16b_bf16 v[128:131], v[184:185], v[56:57], v[208:211] cbsz:2 abid:0\n"
    "v_cvt_pk_bf16_f32 v20, v136, v137\n"
    "v_cvt_pk_bf16_f32 v21, v138, v139\n"
    "v_mfma_f32_4x4x4_16b_bf16 v[132:135], v[184:185], v[58:59], v[212:215] cbsz:2 abid:1\n"
    "v_cvt_pk_bf16_f32 v22, v140, v141\n"
    "v_cvt_pk_bf16_f32 v23, v142, v143\n"
    "v_mfma_f32_4x4x4_16b_bf16 v[136:139], v[184:185], v[60:61], v[216:219] cbsz:2 abid:2\n"
    "v_cvt_pk_bf16_f32 v24, v128, v129\n"
    "v_cvt_pk_bf16_f32 v25, v130, v131\n"
    "v_mfma_f32_4x4x4_16b_bf16 v[140:143], v[184:185], v[62:63], v[220:223] cbsz:2 abid:3\n"
    "v_cvt_pk_bf16_f32 v26, v132, v133\n"
    "v_cvt_pk_bf16_f32 v27, v134, v135\n"
    "v_cvt_pk_bf16_f32 v28, v136, v137\n"
    "v_cvt_pk_bf16_f32 v29, v138, v139\n"
    "v_exp_f32 v152, v152 clamp\n"
    "v_cvt_pk_bf16_f32 v30, v140, v141\n"
    "v_cvt_pk_bf16_f32 v31, v142, v143\n"
    "v_mfma_f32_16x16x32_bf16 v[128:131], v[0:3], v[160:163], v[112:115]\n"
    "v_exp_f32 v144, v144 clamp\n"
    "v_mfma_f32_16x16x32_bf16 v[128:131], v[4:7], v[164:167], v[128:131]\n"
    "v_exp_f32 v145, v145 clamp\n"
    "v_mfma_f32_16x16x32_bf16 v[132:135], v[8:11], v[160:163], v[116:119]\n"
    "v_exp_f32 v146, v146 clamp\n"
    "v_mfma_f32_16x16x32_bf16 v[132:135], v[12:15], v[164:167], v[132:135]\n"
    "v_exp_f32 v147, v147 clamp\n"
    "v_mfma_f32_16x16x32_bf16 v[136:139], v[16:19], v[160:163], v[120:123]\n"
    "v_exp_f32 v148, v148 clamp\n"
    "v_mfma_f32_16x16x32_bf16 v[136:139], v[20:23], v[164:167], v[136:139]\n"
    "v_exp_f32 v149, v149 clamp\n"
    "v_mfma_f32_16x16x32_bf16 v[140:143], v[24:27], v[160:163], v[124:127]\n"
    "v_exp_f32 v150, v150 clamp\n"
    "v_mfma_f32_16x16x32_bf16 v[140:143], v[28:31], v[164:167], v[140:143]\n"
    "v_exp_f32 v151, v151 clamp\n"
    "s_add_u32 m0, s68, 0\n"
    "v_exp_f32 v153, v153 clamp\n"
    "v_add_u32_e64 v253, s67, v244\n"
    "v_mfma_f32_4x4x4_16b_bf16 v[224:227], v[228:229], v[168:169], v[224:227] cbsz:4 abid:0\n"
    "v_exp_f32 v154, v154 clamp\n"
    "v_exp_f32 v155, v155 clamp\n"
    "v_mfma_f32_4x4x4_16b_bf16 v[224:227], v[228:229], v[170:171], v[224:227] cbsz:4 abid:1\n"
    "v_exp_f32 v156, v156 clamp\n"
    "v_exp_f32 v157, v157 clamp\n"
    "v_mfma_f32_4x4x4_16b_bf16 v[224:227], v[228:229], v[172:173], v[224:227] cbsz:4 abid:2\n"
    "v_exp_f32 v158, v158 clamp\n"
    "v_exp_f32 v159, v159 clamp\n"
    "v_mfma_f32_4x4x4_16b_bf16 v[224:227], v[228:229], v[174:175], v[224:227] cbsz:4 abid:3\n"
    "s_waitcnt vmcnt(4)\n"
    "ds_read_b64_tr_b16 v[168:169], v240 offset:0\n"
    "ds_read_b64_tr_b16 v[170:171], v241 offset:256\n"
    "ds_read_b64_tr_b16 v[172:173], v242 offset:512\n"
    "ds_read_b64_tr_b16 v[174:175], v243 offset:768\n"
    "ds_read_b32 v236, v253 offset:0\n"
    "ds_read_b32 v238, v253 offset:4\n"
    "ds_read_b128 v[160:163], v234 offset:2048\n"
    "ds_read_b128 v[164:167], v235 offset:2048\n"
    "s_waitcnt lgkmcnt(8)\n"
    "s_nop 0\n"
    "v_readlane_b32 s20, v252, 0\n"
    "v_readlane_b32 s21, v252, 1\n"
    "v_readlane_b32 s22, v252, 2\n"
    "v_readlane_b32 s23, v252, 3\n"
    "v_readlane_b32 s24, v252, 4\n"
    "v_readlane_b32 s25, v252, 5\n"
    "v_readlane_b32 s26, v252, 6\n"
    "v_readlane_b32 s27, v252, 7\n"
    "v_pk_fma_f32 v[144:145], v[144:145], s[70:71], v[96:97]\n"
    "v_pk_fma_f32 v[146:147], v[146:147], s[70:71], v[98:99]\n"
    "v_pk_add_f32 v[230:231], v[144:145], v[146:147]\n"
    "v_pk_mul_f32 v[232:233], v[144:145], v[146:147]\n"
    "v_pk_fma_f32 v[148:149], v[148:149], s[70:71], v[100:101]\n"
    "v_pk_fma_f32 v[230:231], v[230:231], v[148:149], v[232:233]\n"
    "v_pk_mul_f32 v[232:233], v[232:233], v[148:149]\n"
    "v_pk_fma_f32 v[150:151], v[150:151], s[70:71], v[102:103]\n"
    "v_pk_fma_f32 v[230:231], v[230:231], v[150:151], v[232:233]\n"
    "v_pk_mul_f32 v[232:233], v[232:233], v[150:151]\n"
    "v_pk_fma_f32 v[152:153], v[152:153], s[70:71], v[104:105]\n"
    "v_pk_fma_f32 v[230:231], v[230:231], v[152:153], v[232:233]\n"
    "v_pk_mul_f32 v[232:233], v[232:233], v[152:153]\n"
    "v_pk_fma_f32 v[154:155], v[154:155], s[70:71], v[106:107]\n"
    "v_pk_fma_f32 v[230:231], v[230:231], v[154:155], v[232:233]\n"
    "v_pk_mul_f32 v[232:233], v[232:233], v[154:155]\n"
    "v_pk_fma_f32 v[156:157], v[156:157], s[70:71], v[108:109]\n"
    "v_pk_fma_f32 v[230:231], v[230:231], v[156:157], v[232:233]\n"
    "v_pk_mul_f32 v[232:233], v[232:233], v[156:157]\n"
    "v_pk_fma_f32 v[158:159], v[158:159], s[70:71], v[110:111]\n"
    "v_pk_fma_f32 v[230:231], v[230:231], v[158:159], v[232:233]\n"
    "v_pk_mul_f32 v[232:233], v[232:233], v[158:159]\n"
    "v_rcp_f32 v232, v232\n"
    "v_rcp_f32 v233, v233\n"
    "v_fma_f32 v230, v230, v232, v247\n"
    "v_fmac_f32 v230, v231, v233\n"
    "v_lshlrev_b32 v232, 2, v249\n"
    "v_lshrrev_b32 v233, 2, v249\n"
    "v_and_b32 v232, 56, v232\n"
    "v_and_b32 v233, 4, v233\n"
    "v_add3_u32 v232, v232, v233, s81\n"
    "ds_read_b32 v232, v232\n"
    "s_add_u32 s81, s81, 64\n"
    "s_waitcnt lgkmcnt(0)\n"
    "v_lshlrev_b32 v232, 1, v232\n"
    "buffer_load_ushort v232, v232, s[44:47], 0 offen\n"
    "s_waitcnt vmcnt(0)\n"
    "v_lshlrev_b32 v232, 16, v232\n"
    "v_mul_f32 v230, v230, v232\n"
    "v_cvt_pk_bf16_f32 v230, v230, v230\n"
    "s_sub_u32 s67, s67, -64\n"
    "s_waitcnt lgkmcnt(0)\n"
    "ds_write_b16 v245, v230\n"
    "ds_read_b64 v[228:229], v246\n"
    "s_lshl1_add_u32 s76, s72, s76\n"
    "buffer_load_dwordx4 v[236:237], s[40:43], 0 idxen offen offset:0 nt lds\n"
    "buffer_load_dwordx4 v[238:239], s[40:43], 0 idxen offen offset:1024 nt lds\n"
    "s_and_b32 s62, s58, 63\n"
    "s_mul_i32 s62, s62, 272\n"
    "s_mov_b32 s58, s59\n"
    "s_mov_b32 s59, s60\n"
    "s_mov_b32 s81, s65\n"
    "s_xor_b32 s65, s65, s64\n"
    "s_mov_b32 s80, s83\n"
    "s_add_u32 s82, s65, 256\n"
    "s_cmp_lt_i32 s59, s57\n"
    "s_cselect_b32 s76, s76, -1\n"
    "s_bitcmp1_b32 s58, 30\n"
    "s_cbranch_scc0 .Ldin_A0\n"
    "s_branch .Ldin_A0s\n"
    ".Ldin_L3s_poll:\n"
    "s_sleep 1\n"
    "ds_read_b64 v[232:233], v253\n"
    "s_waitcnt lgkmcnt(0)\n"
    "v_readfirstlane_b32 s60, v232\n"
    "s_cmp_eq_u32 s60, -1\n"
    "s_cbranch_scc1 .Ldin_L3s_poll\n"
    "s_branch .Ldin_L3s_pr\n"
    ".Ldin_E0s:\n"
    "v_exp_f32 v128, v128 clamp\n"
    "v_exp_f32 v129, v129 clamp\n"
    "v_exp_f32 v130, v130 clamp\n"
    "v_exp_f32 v131, v131 clamp\n"
    "v_exp_f32 v132, v132 clamp\n"
    "v_exp_f32 v133, v133 clamp\n"
    "v_exp_f32 v134, v134 clamp\n"
    "v_exp_f32 v135, v135 clamp\n"
    "s_and_b32 s62, s58, 63\n"
    "s_mul_i32 s62, s62, 272\n"
    "s_waitcnt lgkmcnt(0)\n"
    "v_mfma_f32_4x4x4_16b_bf16 v[224:227], v[228:229], v[176:177], v[224:227] cbsz:4 abid:0\n"
    "v_exp_f32 v136, v136 clamp\n"
    "v_exp_f32 v137, v137 clamp\n"
    "v_mfma_f32_4x4x4_16b_bf16 v[224:227], v[228:229], v[178:179], v[224:227] cbsz:4 abid:1\n"
    "v_exp_f32 v138, v138 clamp\n"
    "v_exp_f32 v139, v139 clamp\n"
    "v_mfma_f32_4x4x4_16b_bf16 v[224:227], v[228:229], v[180:181], v[224:227] cbsz:4 abid:2\n"
    "v_exp_f32 v140, v140 clamp\n"
    "v_exp_f32 v141, v141 clamp\n"
    "v_mfma_f32_4x4x4_16b_bf16 v[224:227], v[228:229], v[182:183], v[224:227] cbsz:4 abid:3\n"
    "v_exp_f32 v142, v142 clamp\n"
    "v_exp_f32 v143, v143 clamp\n"
    "v_pk_fma_f32 v[128:129], v[128:129], s[70:71], v[96:97]\n"
    "v_pk_fma_f32 v[130:131], v[130:131], s[70:71], v[98:99]\n"
    "v_pk_add_f32 v[230:231], v[128:129], v[130:131]\n"
    "v_pk_mul_f32 v[232:233], v[128:129], v[130:131]\n"
    "v_pk_fma_f32 v[132:133], v[132:133], s[70:71], v[100:101]\n"
    "v_pk_fma_f32 v[230:231], v[230:231], v[132:133], v[232:233]\n"
    "v_pk_mul_f32 v[232:233], v[232:233], v[132:133]\n"
    "v_pk_fma_f32 v[134:135], v[134:135], s[70:71], v[102:103]\n"
    "v_pk_fma_f32 v[230:231], v[230:231], v[134:135], v[232:233]\n"
    "v_pk_mul_f32 v[232:233], v[232:233], v[134:135]\n"
    "v_pk_fma_f32 v[136:137], v[136:137], s[70:71], v[104:105]\n"
    "v_pk_fma_f32 v[230:231], v[230:231], v[136:137], v[232:233]\n"
    "v_pk_mul_f32 v[232:233], v[232:233], v[136:137]\n"
    "v_pk_fma_f32 v[138:139], v[138:139], s[70:71], v[106:107]\n"
    "v_pk_fma_f32 v[230:231], v[230:231], v[138:139], v[232:233]\n"
    "v_pk_mul_f32 v[232:233], v[232:233], v[138:139]\n"
    "v_pk_fma_f32 v[140:141], v[140:141], s[70:71], v[108:109]\n"
    "v_pk_fma_f32 v[230:231], v[230:231], v[140:141], v[232:233]\n"
    "v_pk_mul_f32 v[232:233], v[232:233], v[140:141]\n"
    "v_pk_fma_f32 v[142:143], v[142:143], s[70:71], v[110:111]\n"
    "v_pk_fma_f32 v[230:231], v[230:231], v[142:143], v[232:233]\n"
    "v_pk_mul_f32 v[232:233], v[232:233], v[142:143]\n"
    "v_rcp_f32 v232, v232\n"
    "v_rcp_f32 v233, v233\n"
    "v_fma_f32 v230, v230, v232, v247\n"
    "v_fmac_f32 v230, v231, v233\n"
    "v_lshlrev_b32 v232, 2, v249\n"
    "v_lshrrev_b32 v233, 2, v249\n"
    "v_and_b32 v232, 56, v232\n"
    "v_and_b32 v233, 4, v233\n"
    "v_add3_u32 v232, v232, v233, s81\n"
    "ds_read_b32 v232, v232\n"
    "s_add_u32 s81, s81, 64\n"
    "s_waitcnt lgkmcnt(0)\n"
    "v_lshlrev_b32 v232, 1, v232\n"
    "buffer_load_ushort v232, v232, s[44:47], 0 offen\n"
    "s_waitcnt vmcnt(0)\n"
    "v_lshlrev_b32 v232, 16, v232\n"
    "v_mul_f32 v230, v230, v232\n"
    "v_cvt_pk_bf16_f32 v230, v230, v230\n"
    "ds_write_b16 v245, v230\n"
    "ds_read_b64 v[228:229], v246\n"
    "s_branch .Ldin_drain0\n"
    ".Ldin_E1s:\n"
    "v_exp_f32 v144, v144 clamp\n"
    "v_exp_f32 v145, v145 clamp\n"
    "v_exp_f32 v146, v146 clamp\n"
    "v_exp_f32 v147, v147 clamp\n"
    "v_exp_f32 v148, v148 clamp\n"
    "v_exp_f32 v149, v149 clamp\n"
    "v_exp_f32 v150, v150 clamp\n"
    "v_exp_f32 v151, v151 clamp\n"
    "s_and_b32 s62, s58, 63\n"
    "s_mul_i32 s62, s62, 272\n"
    "s_waitcnt lgkmcnt(0)\n"
    "v_mfma_f32_4x4x4_16b_bf16 v[224:227], v[228:229], v[168:169], v[224:227] cbsz:4 abid:0\n"
    "v_exp_f32 v152, v152 clamp\n"
    "v_exp_f32 v153, v153 clamp\n"
    "v_mfma_f32_4x4x4_16b_bf16 v[224:227], v[228:229], v[170:171], v[224:227] cbsz:4 abid:1\n"
    "v_exp_f32 v154, v154 clamp\n"
    "v_exp_f32 v155, v155 clamp\n"
    "v_mfma_f32_4x4x4_16b_bf16 v[224:227], v[228:229], v[172:173], v[224:227] cbsz:4 abid:2\n"
    "v_exp_f32 v156, v156 clamp\n"
    "v_exp_f32 v157, v157 clamp\n"
    "v_mfma_f32_4x4x4_16b_bf16 v[224:227], v[228:229], v[174:175], v[224:227] cbsz:4 abid:3\n"
    "v_exp_f32 v158, v158 clamp\n"
    "v_exp_f32 v159, v159 clamp\n"
    "v_pk_fma_f32 v[144:145], v[144:145], s[70:71], v[96:97]\n"
    "v_pk_fma_f32 v[146:147], v[146:147], s[70:71], v[98:99]\n"
    "v_pk_add_f32 v[230:231], v[144:145], v[146:147]\n"
    "v_pk_mul_f32 v[232:233], v[144:145], v[146:147]\n"
    "v_pk_fma_f32 v[148:149], v[148:149], s[70:71], v[100:101]\n"
    "v_pk_fma_f32 v[230:231], v[230:231], v[148:149], v[232:233]\n"
    "v_pk_mul_f32 v[232:233], v[232:233], v[148:149]\n"
    "v_pk_fma_f32 v[150:151], v[150:151], s[70:71], v[102:103]\n"
    "v_pk_fma_f32 v[230:231], v[230:231], v[150:151], v[232:233]\n"
    "v_pk_mul_f32 v[232:233], v[232:233], v[150:151]\n"
    "v_pk_fma_f32 v[152:153], v[152:153], s[70:71], v[104:105]\n"
    "v_pk_fma_f32 v[230:231], v[230:231], v[152:153], v[232:233]\n"
    "v_pk_mul_f32 v[232:233], v[232:233], v[152:153]\n"
    "v_pk_fma_f32 v[154:155], v[154:155], s[70:71], v[106:107]\n"
    "v_pk_fma_f32 v[230:231], v[230:231], v[154:155], v[232:233]\n"
    "v_pk_mul_f32 v[232:233], v[232:233], v[154:155]\n"
    "v_pk_fma_f32 v[156:157], v[156:157], s[70:71], v[108:109]\n"
    "v_pk_fma_f32 v[230:231], v[230:231], v[156:157], v[232:233]\n"
    "v_pk_mul_f32 v[232:233], v[232:233], v[156:157]\n"
    "v_pk_fma_f32 v[158:159], v[158:159], s[70:71], v[110:111]\n"
    "v_pk_fma_f32 v[230:231], v[230:231], v[158:159], v[232:233]\n"
    "v_pk_mul_f32 v[232:233], v[232:233], v[158:159]\n"
    "v_rcp_f32 v232, v232\n"
    "v_rcp_f32 v233, v233\n"
    "v_fma_f32 v230, v230, v232, v247\n"
    "v_fmac_f32 v230, v231, v233\n"
    "v_lshlrev_b32 v232, 2, v249\n"
    "v_lshrrev_b32 v233, 2, v249\n"
    "v_and_b32 v232, 56, v232\n"
    "v_and_b32 v233, 4, v233\n"
    "v_add3_u32 v232, v232, v233, s81\n"
    "ds_read_b32 v232, v232\n"
    "s_add_u32 s81, s81, 64\n"
    "s_waitcnt lgkmcnt(0)\n"
    "v_lshlrev_b32 v232, 1, v232\n"
    "buffer_load_ushort v232, v232, s[44:47], 0 offen\n"
    "s_waitcnt vmcnt(0)\n"
    "v_lshlrev_b32 v232, 16, v232\n"
    "v_mul_f32 v230, v230, v232\n"
    "v_cvt_pk_bf16_f32 v230, v230, v230\n"
    "ds_write_b16 v245, v230\n"
    "ds_read_b64 v[228:229], v246\n"
    "s_branch .Ldin_drain1\n"
    ".Ldin_exit:\n"

      : "+{s[0:1]}"(kp), "+{v0}"(tid)
      : "{s2}"(blockIdx.x)
      : "memory", "vcc", "scc", "v1", "v2", "v3", "v4", "v5", "v6", "v7", "v8", "v9", "v10", "v11", "v12", "v13", "v14", "v15", "v16", "v17", "v18", "v19", "v20", "v21", "v22", "v23", "v24", "v25", "v26", "v27", "v28", "v29", "v30", "v31", "v32", "v33", "v34", "v35", "v36", "v37", "v38", "v39", "v40", "v41", "v42", "v43", "v44", "v45", "v46", "v47", "v48", "v49", "v50", "v51", "v52", "v53", "v54", "v55", "v56", "v57", "v58", "v59", "v60", "v61", "v62", "v63", "v64", "v65", "v66", "v67", "v68", "v69", "v70", "v71", "v72", "v73", "v74", "v75", "v76", "v77", "v78", "v79", "v80", "v81", "v82", "v83", "v84", "v85", "v86", "v87", "v88", "v89", "v90", "v91", "v92", "v93", "v94", "v95", "v96", "v97", "v98", "v99", "v100", "v101", "v102", "v103", "v104", "v105", "v106", "v107", "v108", "v109", "v110", "v111", "v112", "v113", "v114", "v115", "v116", "v117", "v118", "v119", "v120", "v121", "v122", "v123", "v124", "v125", "v126", "v127", "v128", "v129", "v130", "v131", "v132", "v133", "v134", "v135", "v136", "v137", "v138", "v139", "v140", "v141", "v142", "v143", "v144", "v145", "v146", "v147", "v148", "v149", "v150", "v151", "v152", "v153", "v154", "v155", "v156", "v157", "v158", "v159", "v160", "v161", "v162", "v163", "v164", "v165", "v166", "v167", "v168", "v169", "v170", "v171", "v172", "v173", "v174", "v175", "v176", "v177", "v178", "v179", "v180", "v181", "v182", "v183", "v184", "v185", "v186", "v187", "v188", "v189", "v190", "v191", "v192", "v193", "v194", "v195", "v196", "v197", "v198", "v199", "v200", "v201", "v202", "v203", "v204", "v205", "v206", "v207", "v208", "v209", "v210", "v211", "v212", "v213", "v214", "v215", "v216", "v217", "v218", "v219", "v220", "v221", "v222", "v223", "v224", "v225", "v226", "v227", "v228", "v229", "v230", "v231", "v232", "v233", "v234", "v235", "v236", "v237", "v238", "v239", "v240", "v241", "v242", "v243", "v244", "v245", "v246", "v247", "v248", "v249", "v250", "v251", "v252", "v253", "v254", "v255", "s3", "s4", "s5", "s6", "s7", "s8", "s9", "s10", "s11", "s12", "s13", "s14", "s15", "s16", "s17", "s18", "s19", "s20", "s21", "s22", "s23", "s24", "s25", "s26", "s27", "s28", "s29", "s30", "s31", "s33", "s34", "s35", "s36", "s37", "s38", "s39", "s40", "s41", "s42", "s43", "s44", "s45", "s46", "s47", "s48", "s49", "s50", "s51", "s52", "s53", "s54", "s55", "s56", "s57", "s58", "s59", "s60", "s61", "s62", "s63", "s64", "s65", "s66", "s67", "s68", "s69", "s70", "s71", "s72", "s73", "s74", "s75", "s76", "s77", "s78", "s79", "s80", "s81", "s82", "s83", "s84", "s85", "s86", "s87", "s88", "s89", "s90", "s91", "s92", "s93", "s94", "s95", "s96");
  asm volatile("" ::"s"((uint32_t)(uintptr_t)smem));
}

__global__ __launch_bounds__(64) void din_fallback_kernel(
    const unsigned short* __restrict__ target, const unsigned short* __restrict__ hist,
    const unsigned short* __restrict__ mask, const unsigned short* __restrict__ w1,
    const unsigned short* __restrict__ b1, const unsigned short* __restrict__ w2,
    const unsigned short* __restrict__ b2, unsigned short* __restrict__ out, int B, int L) {
  __shared__ float s_w[64 * 64];
  __shared__ float s_x[64];
  const int b = blockIdx.x, h = threadIdx.x;
  float bias = bf2f(b1[h]);
  for (int d = 0; d < 64; ++d) {
    const float t = bf2f(target[(size_t)b * 64 + d]);
    const float wc = bf2f(w1[(128 + d) * 64 + h]);
    bias += t * (bf2f(w1[(64 + d) * 64 + h]) - wc);
    s_w[d * 64 + h] = bf2f(w1[d * 64 + h]) + wc + t * bf2f(w1[(192 + d) * 64 + h]);
  }
  const float w2h = bf2f(w2[h]), b2v = bf2f(b2[0]);
  float acc = 0.f;
  for (int l = 0; l < L; ++l) {
    const float m = bf2f(mask[(size_t)b * L + l]);
    if (m == 0.f) continue;
    const float x = bf2f(hist[((size_t)b * L + l) * 64 + h]);
    __syncthreads();
    s_x[h] = x;
    __syncthreads();
    float z = bias;
    for (int d = 0; d < 64; ++d) z += s_x[d] * s_w[d * 64 + h];
    float s = w2h / (1.0f + __expf(-z));
    for (int o = 32; o > 0; o >>= 1) s += __shfl_xor(s, o);
    acc += (s + b2v) * m * x;
  }
  const uint32_t u = __float_as_uint(acc);
  out[(size_t)b * 64 + h] = (unsigned short)((u + 0x7fffu + ((u >> 16) & 1u)) >> 16);
}

extern "C" void kernel_launch(void* const* d_in, const int* in_sizes, int n_in, void* d_out,
                              int out_size, void* d_ws, size_t ws_size, hipStream_t stream) {
  const unsigned short* target = (const unsigned short*)d_in[0];
  const unsigned short* hist = (const unsigned short*)d_in[1];
  const unsigned short* mask = (const unsigned short*)d_in[2];
  const unsigned short* w1 = (const unsigned short*)d_in[3];
  const unsigned short* b1 = (const unsigned short*)d_in[4];
  const unsigned short* w2 = (const unsigned short*)d_in[5];
  const unsigned short* b2 = (const unsigned short*)d_in[6];
  const int B = in_sizes[0] / 64;
  const int L = in_sizes[2] / B;
  if ((L & 1) || L > 256 || B >= (1 << 24) - 64) {
    din_fallback_kernel<<<B, 64, 0, stream>>>(target, hist, mask, w1, b1, w2, b2, (unsigned short*)d_out, B, L);
    return;
  }
  const uint32_t grid = (B + 63) / 64, fl = 0x20000u;
  const bool paced = grid >= 224;
  const bool down = false;
  const uint32_t step = down ? 0u - grid * 8u : grid * 8u;
  din_kernel<<<grid, 512, 0, stream>>>(hist, (uint32_t)B * L, fl, mask, (uint32_t)B * L * 2u, fl, target, (uint32_t)B * 128u, fl,
                                       (unsigned short*)d_out, (uint32_t)B * 128u, fl, w1, w2, b1, b2, (uint32_t)L, down ? grid * 56u : 0u, (uint32_t)B,
                                       step, 2u * L, step * 2u * L, paced ? 52u : 0u, paced ? 15u : 0u);
}
```

```cpp
#include <hip/hip_runtime.h>
#include <stdint.h>

constexpr int SMEM_B = 163712;

__device__ __forceinline__ float bf2f(unsigned short u) { return __uint_as_float(((uint32_t)u) << 16); }

__global__ __launch_bounds__(512, 2) __attribute__((aligned(4096))) void din_kernel(
    const unsigned short* hist, uint32_t rows, uint32_t f0, const unsigned short* mask, uint32_t mask_bytes, uint32_t f1,
    const unsigned short* target, uint32_t target_bytes, uint32_t f2, unsigned short* out, uint32_t out_bytes, uint32_t f3,
    const unsigned short* w1, const unsigned short* w2, const unsigned short* b1, const unsigned short* b2,
    uint32_t L, uint32_t period, uint32_t B, uint32_t round_rows, uint32_t row_bytes, uint32_t round_bytes, uint32_t pad0, uint32_t pad1) {
  __shared__ __attribute__((aligned(128))) char smem[SMEM_B];
  unsigned long long kp = (unsigned long long)(uintptr_t)__builtin_amdgcn_kernarg_segment_ptr();
  uint32_t tid = threadIdx.x;
  asm volatile(
    "s_load_dwordx16 s[40:55], s[0:1], 0x0\n"
    "s_load_dwordx16 s[4:19], s[0:1], 0x40\n"
    "s_memrealtime s[76:77]\n"
    "s_memtime s[20:21]\n"
    "v_readfirstlane_b32 s3, v0\n"
    "v_and_b32 v251, 63, v0\n"
    "s_lshr_b32 s3, s3, 6\n"
    "s_lshl3_add_u32 s61, s2, s3\n"
    "s_getpc_b64 s[92:93]\n"
    "s_and_b32 s94, s2, 0xf8\n"
    "s_add_u32 s94, s94, s3\n"
    "s_min_u32 s94, s94, 199\n"
    "s_lshl_b32 s94, s94, 7\n"
    "v_mov_b32 v1, s94\n"
    "global_load_dword v1, v1, s[92:93] offset:1024\n"
    "s_cmp_lt_u32 s3, 4\n"
    "s_cbranch_scc0 .Ldin_young\n"
    "v_lshlrev_b32 v105, 3, v251\n"
    "v_mov_b32 v8, 0\n"
    "v_mov_b32 v9, 0\n"
    "v_mov_b32 v10, 0\n"
    "v_and_b32 v2, 48, v251\n"
    "v_and_b32 v3, 8, v251\n"
    "v_lshl_or_b32 v2, v3, 3, v2\n"
    "v_and_b32 v3, 4, v251\n"
    "v_lshl_or_b32 v2, v3, 1, v2\n"
    "v_lshlrev_b32 v249, 1, v251\n"
    "s_mul_i32 s68, s3, 11392\n"
    "v_lshlrev_b32_e64 v248, 4, v251\n"
    "s_add_u32 s68, s68, 72576\n"
    "v_and_b32 v24, 0x60, v249\n"
    "s_add_u32 s67, s68, 8192\n"
    "v_or_b32 v250, 0x8000, v24\n"
    "s_add_u32 s65, s68, 9728\n"
    "v_lshrrev_b32 v20, 4, v251\n"
    "s_xor_b32 s64, s67, s65\n"
    "v_and_b32 v244, 56, v251\n"
    "s_add_u32 s96, s68, 11264\n"
    "v_bitop3_b32 v21, v20, v251, 7 bitop3:0x78\n"
    "s_lshl_b32 s66, s3, 7\n"
    "v_lshlrev_b32 v21, 4, v21\n"
    "s_add_u32 s66, s66, 68512\n"
    "v_or_b32 v237, 0x400, v21\n"
    "s_lshr_b32 s95, s67, 2\n"
    "v_xor_b32 v239, 64, v21\n"
    "s_mov_b32 s38, 0xffff\n"
    "s_mov_b32 s57, 0x7fffffc0\n"
    "v_add_u32 v253, s67, v244\n"
    "v_lshlrev_b32 v105, 3, v251\n"
    "v_mov_b32 v201, v237\n"
    "v_mov_b32 v203, v239\n"
    "v_mov_b32 v205, v237\n"
    "v_mov_b32 v207, v239\n"
    "v_mov_b32 v209, v237\n"
    "v_mov_b32 v211, v239\n"
    "v_mov_b32 v213, v237\n"
    "v_mov_b32 v215, v239\n"
    "v_mov_b32 v220, s95\n"
    "v_mov_b32 v224, 0\n"
    "v_mov_b32 v225, 0\n"
    "v_mov_b32 v226, 0\n"
    "v_mov_b32 v227, 0\n"
    "v_bfe_u32 v21, v251, 1, 3\n"
    "v_xor_b32 v21, v20, v21\n"
    "v_and_b32 v22, 0xf0, v248\n"
    "s_mov_b32 s70, 0x47000000\n"
    "v_lshl_add_u32 v22, v22, 3, s68\n"
    "s_mov_b32 s71, s70\n"
    "v_lshl_add_u32 v234, v21, 4, v22\n"
    "s_add_u32 s63, s66, 16\n"
    "v_xor_b32 v235, 64, v234\n"
    "s_mov_b32 s81, s67\n"
    "v_and_b32 v20, 3, v251\n"
    "s_mov_b64 s[92:93], 0xffff\n"
    "v_lshlrev_b64 v[254:255], v248, s[92:93]\n"
    "s_add_u32 s82, s65, 256\n"
    "v_lshl_or_b32 v21, v20, 3, v24\n"
    "s_mul_i32 s62, s3, 272\n"
    "v_and_b32 v22, 8, v251\n"
    "v_mul_u32_u24 v23, 136, v22\n"
    "v_xor_b32 v21, v21, v23\n"
    "v_and_b32 v23, 4, v251\n"
    "v_lshl_or_b32 v21, v23, 5, v21\n"
    "v_add_u32 v240, s68, v21\n"
    "v_xor_b32 v241, 16, v240\n"
    "v_xor_b32 v242, 32, v240\n"
    "v_xor_b32 v243, 48, v240\n"
    "v_add_u32 v252, s62, v250\n"
    "v_and_b32 v21, 24, v249\n"
    "v_add_u32 v21, s96, v21\n"
    "v_lshl_add_u32 v246, v20, 5, v21\n"
    "v_and_b32 v21, 6, v251\n"
    "v_lshl_or_b32 v21, v21, 2, v24\n"
    "v_and_b32 v23, 2, v249\n"
    "v_lshrrev_b32 v22, 1, v22\n"
    "v_or3_b32 v21, v21, v22, v23\n"
    "v_add_u32 v245, s96, v21\n"
    "s_setprio 2\n"
    "s_waitcnt lgkmcnt(0)\n"
    "s_mul_i32 s36, s61, s16\n"
    "s_add_u32 s94, s36, s16\n"
    "s_cmp_lt_u32 s61, s14\n"
    "s_cselect_b32 s46, s94, 0\n"
    "buffer_load_dwordx2 v[228:229], v105, s[44:47], s36 offen nt\n"
    "s_load_dword s10, s[10:11], 0x0\n"
    "global_load_short_d16_hi v8, v249, s[6:7]\n"
    "global_load_short_d16_hi v9, v249, s[8:9]\n"
    "s_setprio 0\n"
    "s_branch .Ldin_old2\n"
    ".Ldin_young:\n"
    "s_setprio 1\n"
    "v_and_b32 v16, 7, v251\n"
    "s_lshl_b32 s94, s3, 11\n"
    "v_lshlrev_b32 v2, 4, v16\n"
    "v_and_b32 v3, 56, v251\n"
    "v_lshl_or_b32 v3, v3, 5, v2\n"
    "v_add_u32 v6, s94, v3\n"
    "v_add_u32 v3, 0x2000, v6\n"
    "v_add_u32 v4, 0xffffe000, v6\n"
    "v_add_u32 v5, 0x4000, v6\n"
    "v_and_b32 v17, 8, v251\n"
    "s_and_b32 s88, s3, 1\n"
    "v_and_b32 v18, 32, v251\n"
    "s_and_b32 s89, s3, 2\n"
    "v_lshl_or_b32 v18, v16, 2, v18\n"
    "s_lshl_b32 s84, s88, 9\n"
    "v_lshl_add_u32 v18, v18, 3, s84\n"
    "s_lshl_b32 s84, s89, 10\n"
    "v_and_b32 v19, 16, v251\n"
    "s_lshl_b32 s85, s89, 9\n"
    "v_lshl_or_b32 v19, v19, 6, v17\n"
    "s_add_u32 s85, s85, 0x4000\n"
    "v_add3_u32 v12, v18, v19, s84\n"
    "s_lshl2_add_u32 s86, s89, s88\n"
    "v_add_u32 v13, 16, v12\n"
    "s_lshl_b32 s86, s86, 4\n"
    "v_and_b32 v19, 24, v251\n"
    "s_add_u32 s87, s94, 0x4000\n"
    "v_lshrrev_b32 v19, 1, v19\n"
    "v_add3_u32 v14, v18, v19, s85\n"
    "v_add_u32 v15, 16, v14\n"
    "v_bfrev_b32 v7, 1\n"
    "v_lshlrev_b32 v26, 4, v251\n"
    "v_and_b32 v26, 0x1f0, v26\n"
    "v_lshrrev_b32 v27, 5, v251\n"
    "v_lshlrev_b32 v249, 1, v251\n"
    "s_mul_i32 s68, s3, 11392\n"
    "v_lshlrev_b32_e64 v248, 4, v251\n"
    "s_add_u32 s68, s68, 72576\n"
    "v_and_b32 v24, 0x60, v249\n"
    "s_add_u32 s67, s68, 8192\n"
    "v_or_b32 v250, 0x8000, v24\n"
    "s_add_u32 s65, s68, 9728\n"
    "v_lshrrev_b32 v20, 4, v251\n"
    "s_xor_b32 s64, s67, s65\n"
    "v_and_b32 v244, 56, v251\n"
    "s_add_u32 s96, s68, 11264\n"
    "v_bitop3_b32 v21, v20, v251, 7 bitop3:0x78\n"
    "s_lshl_b32 s66, s3, 7\n"
    "v_lshlrev_b32 v21, 4, v21\n"
    "s_add_u32 s66, s66, 68512\n"
    "v_or_b32 v237, 0x400, v21\n"
    "s_lshr_b32 s95, s67, 2\n"
    "v_xor_b32 v239, 64, v21\n"
    "s_mov_b32 s38, 0xffff\n"
    "s_mov_b32 s57, 0x7fffffc0\n"
    "v_add_u32 v253, s67, v244\n"
    "v_lshlrev_b32 v105, 3, v251\n"
    "v_mov_b32 v201, v237\n"
    "v_mov_b32 v203, v239\n"
    "v_mov_b32 v205, v237\n"
    "v_mov_b32 v207, v239\n"
    "v_mov_b32 v209, v237\n"
    "v_mov_b32 v211, v239\n"
    "v_mov_b32 v213, v237\n"
    "v_mov_b32 v215, v239\n"
    "v_mov_b32 v220, s95\n"
    "v_mov_b32 v224, 0\n"
    "v_mov_b32 v225, 0\n"
    "v_mov_b32 v226, 0\n"
    "s_waitcnt lgkmcnt(0)\n"
    "global_load_dwordx4 v[8:11], v2, s[6:7]\n"
    "s_memrealtime s[10:11]\n"
    "global_load_dwordx4 v[128:131], v3, s[4:5] offset:0\n"
    "global_load_dwordx4 v[132:135], v3, s[4:5] offset:128\n"
    "v_mov_b32 v227, 0\n"
    "v_bfe_u32 v21, v251, 1, 3\n"
    "v_xor_b32 v21, v20, v21\n"
    "v_and_b32 v22, 0xf0, v248\n"
    "s_mov_b32 s70, 0x47000000\n"
    "v_lshl_add_u32 v22, v22, 3, s68\n"
    "s_mov_b32 s71, s70\n"
    "v_lshl_add_u32 v234, v21, 4, v22\n"
    "s_add_u32 s63, s66, 16\n"
    "v_xor_b32 v235, 64, v234\n"
    "s_mov_b32 s81, s67\n"
    "v_and_b32_e64 v20, 3, v251\n"
    "global_load_dwordx4 v[136:139], v4, s[4:5] offset:0\n"
    "s_mov_b64 s[92:93], 0xffff\n"
    "v_lshlrev_b64 v[254:255], v248, s[92:93]\n"
    "s_add_u32 s82, s65, 256\n"
    "v_lshl_or_b32 v21, v20, 3, v24\n"
    "s_mul_i32 s62, s3, 272\n"
    "v_and_b32 v22, 8, v251\n"
    "v_mul_u32_u24 v23, 136, v22\n"
    "v_xor_b32 v21, v21, v23\n"
    "v_and_b32 v23, 4, v251\n"
    "v_lshl_or_b32 v21, v23, 5, v21\n"
    "v_add_u32 v240, s68, v21\n"
    "v_xor_b32 v241, 16, v240\n"
    "global_load_dwordx4 v[140:143], v4, s[4:5] offset:128\n"
    "v_xor_b32 v242, 32, v240\n"
    "v_xor_b32 v243, 48, v240\n"
    "v_add_u32 v252, s62, v250\n"
    "v_and_b32 v21, 24, v249\n"
    "v_add_u32 v21, s96, v21\n"
    "v_lshl_add_u32 v246, v20, 5, v21\n"
    "v_and_b32 v21, 6, v251\n"
    "v_lshl_or_b32 v21, v21, 2, v24\n"
    "v_and_b32 v23, 2, v249\n"
    "v_lshrrev_b32 v22, 1, v22\n"
    "v_or3_b32 v21, v21, v22, v23\n"
    "v_add_u32 v245, s96, v21\n"
    "global_load_dwordx4 v[144:147], v5, s[4:5] offset:0\n"
    "s_add_u32 s39, s61, s3\n"
    "s_sub_u32 s39, s39, 8\n"
    "v_cmp_gt_u32 vcc, s16, v26\n"
    "v_mad_u32_u24 v27, v27, s16, v26\n"
    "s_mul_i32 s39, s39, s16\n"
    "s_lshl1_add_u32 s39, s17, s39\n"
    "v_cndmask_b32 v27, v7, v27, vcc\n"
    "s_add_u32 s40, s40, 0xfffffc00\n"
    "s_addc_u32 s41, s41, -1\n"
    "s_mul_i32 s94, s61, s12\n"
    "s_bitset1_b32 s41, 23\n"
    "v_lshl_add_u32 v216, v251, 2, s94\n"
    "global_load_dwordx4 v[148:151], v5, s[4:5] offset:128\n"
    "s_mov_b32 s56, s12\n"
    "v_add_u32 v217, 1, v216\n"
    "v_add_u32 v218, 2, v216\n"
    "v_add_u32 v219, 3, v216\n"
    "s_and_b32 s72, s13, 0xffff\n"
    "s_lshr_b32 s73, s13, 16\n"
    "s_cmp_lg_u32 s72, 0\n"
    "s_cselect_b32 s94, 6, 0\n"
    "s_cselect_b32 s95, 6, 0\n"
    "s_add_u32 s74, s72, s94\n"
    "s_sub_u32 s75, s72, s95\n"
    "v_mov_b32 v20, s57\n"
    "s_mov_b64 exec, 0xffffff\n"
    "v_mov_b32 v20, -1\n"
    "s_mov_b64 exec, 0xff\n"
    "v_mov_b32 v20, 0\n"
    "s_mov_b32 exec_lo, -1\n"
    "v_lshl_add_u32 v21, v249, 1, s66\n"
    "v_add_u32 v21, -32, v21\n"
    "ds_write_b32 v21, v20\n"
    "s_mov_b32 exec_hi, -1\n"
    "global_load_dwordx4 v[152:155], v6, s[4:5] offset:0\n"
    "s_mov_b32 s37, 0xffff0000\n"
    "v_and_b32 v19, 15, v251\n"
    "v_mul_u32_u24 v19, 272, v19\n"
    "v_add_u32 v97, v19, v250\n"
    "v_add_u32 v98, s87, v248\n"
    "v_add_u32 v99, s86, v250\n"
    "v_add_u32 v100, s86, v97\n"
    "s_mov_b32 s84, 0xbfb8aa3b\n"
    "global_load_dwordx4 v[156:159], v6, s[4:5] offset:128\n"
    "s_branch .Ldin_y2\n"
    ".Ldin_old2:\n"
    "s_add_u32 s40, s40, 0xfffffc00\n"
    "s_addc_u32 s41, s41, -1\n"
    "s_mul_i32 s94, s61, s12\n"
    "s_bitset1_b32 s41, 23\n"
    "v_lshl_add_u32 v216, v251, 2, s94\n"
    "s_mov_b32 s56, s12\n"
    "v_add_u32 v217, 1, v216\n"
    "v_add_u32 v218, 2, v216\n"
    "v_add_u32 v219, 3, v216\n"
    "s_and_b32 s72, s13, 0xffff\n"
    "s_lshr_b32 s73, s13, 16\n"
    "s_cmp_lg_u32 s72, 0\n"
    "s_cselect_b32 s94, 6, 0\n"
    "s_cselect_b32 s95, 6, 0\n"
    "s_add_u32 s74, s72, s94\n"
    "s_sub_u32 s75, s72, s95\n"
    "v_mov_b32 v20, s57\n"
    "s_mov_b64 exec, 0xffffff\n"
    "v_mov_b32 v20, -1\n"
    "s_mov_b64 exec, 0xff\n"
    "v_mov_b32 v20, 0\n"
    "s_mov_b32 exec_lo, -1\n"
    "v_lshl_add_u32 v21, v249, 1, s66\n"
    "v_add_u32 v21, -32, v21\n"
    "ds_write_b32 v21, v20\n"
    "s_mov_b32 exec_hi, -1\n"
    "v_and_b32 v4, 2, v251\n"
    "v_lshl_add_u32 v4, v4, 6, v250\n"
    "v_and_b32 v5, 16, v248\n"
    "v_and_b32 v6, 12, v251\n"
    "v_or3_b32 v104, v4, v5, v6\n"
    "v_mul_u32_u24 v4, 34, v244\n"
    "v_and_b32 v5, 3, v251\n"
    "v_lshl_add_u32 v4, v5, 5, v4\n"
    "v_and_b32 v5, 4, v251\n"
    "v_lshl_add_u32 v4, v5, 2, v4\n"
    "s_lshl_b32 s86, s62, 3\n"
    "v_add_u32_e64 v18, s86, v4\n"
    "s_mul_i32 s84, s3, s15\n"
    "s_lshl_b32 s39, s61, 7\n"
    "s_sub_u32 s84, s84, s3\n"
    "s_add_u32 s84, s84, s61\n"
    "s_lshl_b32 s84, s84, 7\n"
    "s_lshl_b32 s85, s15, 9\n"
    "s_add_u32 s85, s85, s84\n"
    "s_lshl_b32 s94, s61, 6\n"
    "s_add_u32 s94, s3, s94\n"
    "s_cmp_lt_u32 s61, s14\n"
    "s_cselect_b32 s58, s94, s57\n"
    "s_add_u32 s95, s61, s15\n"
    "s_add_u32 s36, s36, s17\n"
    "s_lshl_b32 s94, s95, 6\n"
    "s_add_u32 s94, s3, s94\n"
    "s_add_u32 s96, s36, s16\n"
    "s_add_u32 s94, s94, 8\n"
    "s_cmp_lt_u32 s95, s14\n"
    "s_cselect_b32 s46, s96, 0\n"
    "s_cselect_b32 s59, s94, s57\n"
    "s_setprio 2\n"
    "buffer_load_dwordx2 v[184:185], v2, s[48:51], s39 offen nt\n"
    "buffer_load_dwordx4 v[128:131], v248, s[48:51], s84 offen nt\n"
    "buffer_load_dwordx4 v[132:135], v248, s[48:51], s85 offen nt\n"
    "buffer_load_short_d16_hi v224, v249, s[44:47], s36 offen offset:0 nt\n"
    "buffer_load_short_d16_hi v225, v249, s[44:47], s36 offen offset:128 nt\n"
    "buffer_load_short_d16_hi v226, v249, s[44:47], s36 offen offset:256 nt\n"
    "buffer_load_short_d16_hi v227, v249, s[44:47], s36 offen offset:384 nt\n"
    "s_mul_i32 s46, s14, s16\n"
    "s_nop 0\n"
    "v_mul_u32_u24 v32, 0x9e3779, v251\n"
    "s_mov_b32 s90, 0x807f807f\n"
    "v_mov_b32 v47, 0x3f003f00\n"
    "v_add_u32 v32, 0x7f4a7c15, v32\n"
    "v_mul_lo_u32 v33, v32, v32\n"
    "v_mul_lo_u32 v34, v33, v32\n"
    "v_mul_lo_u32 v35, v34, v32\n"
    "v_mul_lo_u32 v36, v35, v32\n"
    "v_mul_lo_u32 v37, v36, v32\n"
    "v_mul_lo_u32 v38, v37, v32\n"
    "v_mul_lo_u32 v39, v38, v32\n"
    "v_and_or_b32 v32, v32, s90, v47\n"
    "v_and_or_b32 v33, v33, s90, v47\n"
    "v_and_or_b32 v34, v34, s90, v47\n"
    "v_and_or_b32 v35, v35, s90, v47\n"
    "v_and_or_b32 v36, v36, s90, v47\n"
    "v_and_or_b32 v37, v37, s90, v47\n"
    "v_and_or_b32 v38, v38, s90, v47\n"
    "v_and_or_b32 v39, v39, s90, v47\n"
    "s_nop 1\n"
    "s_nop 0\n"
    "v_mfma_f32_32x32x16_bf16 v[64:79], v[36:39], v[32:35], 0\n"
    "v_mfma_f32_32x32x16_bf16 v[48:63], v[32:35], v[36:39], 0\n"
    "v_mfma_f32_32x32x16_bf16 v[64:79], v[36:39], v[32:35], 0\n"
    "v_mfma_f32_32x32x16_bf16 v[48:63], v[32:35], v[36:39], 0\n"
    "v_mfma_f32_32x32x16_bf16 v[64:79], v[36:39], v[32:35], 0\n"
    "v_mfma_f32_32x32x16_bf16 v[48:63], v[32:35], v[36:39], 0\n"
    "v_mfma_f32_32x32x16_bf16 v[64:79], v[36:39], v[32:35], 0\n"
    "v_mfma_f32_32x32x16_bf16 v[48:63], v[32:35], v[36:39], 0\n"
    "v_mfma_f32_32x32x16_bf16 v[64:79], v[36:39], v[32:35], 0\n"
    "v_mfma_f32_32x32x16_bf16 v[48:63], v[32:35], v[36:39], 0\n"
    "s_setprio 2\n"
    "s_waitcnt vmcnt(9)\n"
    "v_cmp_ne_u16_e64 s[84:85], 0, v228\n"
    "v_cmp_lt_u32_e64 s[86:87], s38, v228\n"
    "v_cmp_ne_u16_e64 s[88:89], 0, v229\n"
    "v_cmp_lt_u32_e64 s[90:91], s38, v229\n"
    "v_mbcnt_lo_u32_b32 v220, s84, v220\n"
    "s_bcnt1_i32_b64 s92, s[84:85]\n"
    "v_mbcnt_hi_u32_b32 v220, s85, v220\n"
    "s_lshl2_add_u32 s92, s92, s67\n"
    "v_mbcnt_lo_u32_b32 v220, s86, v220\n"
    "s_bcnt1_i32_b64 s93, s[86:87]\n"
    "v_mbcnt_hi_u32_b32 v220, s87, v220\n"
    "s_lshl2_add_u32 s93, s93, s92\n"
    "v_mbcnt_lo_u32_b32 v220, s88, v220\n"
    "s_bcnt1_i32_b64 s94, s[88:89]\n"
    "v_mbcnt_hi_u32_b32 v220, s89, v220\n"
    "s_lshl2_add_u32 s94, s94, s93\n"
    "v_mbcnt_lo_u32_b32 v220, s90, v220\n"
    "s_bcnt1_i32_b64 s95, s[90:91]\n"
    "v_mbcnt_hi_u32_b32 v220, s91, v220\n"
    "s_lshl2_add_u32 s95, s95, s94\n"
    "v_mov_b32_e64 v21, -1\n"
    "v_lshl_add_u32 v20, v249, 1, s95\n"
    "v_addc_co_u32_e64 v221, vcc, v220, 0, s[84:85]\n"
    "v_addc_co_u32_e64 v222, vcc, v221, 0, s[86:87]\n"
    "v_addc_co_u32_e64 v223, vcc, v222, 0, s[88:89]\n"
    "ds_write2_b32 v20, v21, v21 offset1:64\n"
    "v_lshlrev_b32 v220, 2, v220\n"
    "v_lshlrev_b32 v221, 2, v221\n"
    "v_lshlrev_b32 v222, 2, v222\n"
    "v_lshlrev_b32_e64 v223, 2, v223\n"
    "s_mov_b64 exec, s[84:85]\n"
    "ds_write_b32 v220, v216\n"
    "s_mov_b64 exec, s[86:87]\n"
    "ds_write_b32 v221, v217\n"
    "s_mov_b64 exec, s[88:89]\n"
    "ds_write_b32 v222, v218\n"
    "s_mov_b64 exec, s[90:91]\n"
    "ds_write_b32 v223, v219\n"
    "s_mov_b64 exec, -1\n"
    "ds_read_b32 v200, v253 offset:0\n"
    "ds_read_b32 v202, v253 offset:4\n"
    "ds_read_b32 v204, v253 offset:64\n"
    "ds_read_b32 v206, v253 offset:68\n"
    "ds_read_b32 v208, v253 offset:128\n"
    "ds_read_b32 v210, v253 offset:132\n"
    "ds_read_b32 v212, v253 offset:192\n"
    "ds_read_b32 v214, v253 offset:196\n"
    "s_add_u32 m0, s68, 0\n"
    "s_waitcnt lgkmcnt(6)\n"
    "buffer_load_dwordx4 v[200:201], s[40:43], 0 idxen offen offset:0 nt lds\n"
    "buffer_load_dwordx4 v[202:203], s[40:43], 0 idxen offen offset:1024 nt lds\n"
    "s_add_u32 s95, s95, 60\n"
    "s_add_u32 s94, s67, 384\n"
    "s_andn2_b32 s95, s95, 63\n"
    "s_max_u32 s80, s95, s94\n"
    "s_add_u32 m0, s68, 2048\n"
    "s_waitcnt lgkmcnt(0)\n"
    "buffer_load_dwordx4 v[204:205], s[40:43], 0 idxen offen offset:0 nt lds\n"
    "s_lshl_b32 s10, s10, 16\n"
    "v_writelane_b32 v10, s10, 0\n"
    "s_waitcnt vmcnt(10)\n"
    "v_and_b32 v11, 0x7fffffff, v8\n"
    "v_mfma_f32_32x32x16_bf16 v[64:79], v[36:39], v[32:35], 0\n"
    "v_and_b32 v12, 0x7fffffff, v10\n"
    "v_min_f32 v14, 0, v8\n"
    "v_add_u32 v12, 0xf6000000, v12\n"
    "v_add_f32 v14, v14, v10\n"
    "v_mfma_f32_32x32x16_bf16 v[48:63], v[32:35], v[36:39], 0\n"
    "v_max_i32 v12, v11, v12\n"
    "v_rcp_f32 v11, v11\n"
    "v_add_f32_dpp v14, v14, v14 row_ror:8 row_mask:0xf bank_mask:0xf\n"
    "v_max_u32_dpp v12, v12, v12 row_ror:8 row_mask:0xf bank_mask:0xf\n"
    "s_mov_b32 s84, 0xbfb8aa3b\n"
    "v_mfma_f32_32x32x16_bf16 v[64:79], v[36:39], v[32:35], 0\n"
    "v_add_f32_dpp v14, v14, v14 row_ror:4 row_mask:0xf bank_mask:0xf\n"
    "v_max_u32_dpp v12, v12, v12 row_ror:4 row_mask:0xf bank_mask:0xf\n"
    "v_bfrev_b32 v13, 1\n"
    "v_add_f32_dpp v14, v14, v14 row_ror:2 row_mask:0xf bank_mask:0xf\n"
    "v_mfma_f32_32x32x16_bf16 v[48:63], v[32:35], v[36:39], 0\n"
    "v_max_u32_dpp v12, v12, v12 row_ror:2 row_mask:0xf bank_mask:0xf\n"
    "v_bitop3_b32 v8, v8, s84, v13 bitop3:0x6c\n"
    "v_add_f32_dpp v14, v14, v14 row_ror:1 row_mask:0xf bank_mask:0xf\n"
    "v_max_u32_dpp v12, v12, v12 row_ror:1 row_mask:0xf bank_mask:0xf\n"
    "s_nop 0\n"
    "v_mfma_f32_32x32x16_bf16 v[64:79], v[36:39], v[32:35], 0\n"
    "v_readlane_b32 s84, v12, 0\n"
    "v_readlane_b32 s85, v12, 16\n"
    "v_readlane_b32 s86, v12, 32\n"
    "buffer_load_dwordx4 v[206:207], s[40:43], 0 idxen offen offset:1024 nt lds\n"
    "v_readlane_b32 s87, v12, 48\n"
    "v_mfma_f32_32x32x16_bf16 v[48:63], v[32:35], v[36:39], 0\n"
    "s_max_u32 s84, s84, s85\n"
    "s_max_u32 s86, s86, s87\n"
    "s_max_u32 s84, s84, s86\n"
    "s_lshr_b32 s84, s84, 23\n"
    "s_max_u32 s84, s84, 15\n"
    "v_mfma_f32_32x32x16_bf16 v[64:79], v[36:39], v[32:35], 0\n"
    "s_min_u32 s84, s84, 253\n"
    "s_lshl_b32 s84, s84, 23\n"
    "s_add_u32 s85, s84, 0xf9000000\n"
    "s_sub_u32 s86, 0x86000000, s84\n"
    "v_mfma_f32_32x32x16_bf16 v[48:63], v[32:35], v[36:39], 0\n"
    "v_mul_f32 v11, s85, v11\n"
    "v_min_f32 v11, 0x46800000, v11\n"
    "v_log_f32 v13, v11\n"
    "v_mul_f32 v14, s86, v14\n"
    "v_mov_b32 v15, s85\n"
    "v_mfma_f32_32x32x16_bf16 v[64:79], v[36:39], v[32:35], 0\n"
    "v_add_f32 v13, 0xc1700000, v13\n"
    "v_fmac_f32 v13, v9, v8\n"
    "ds_write_b32 v104, v13 offset:35200\n"
    "ds_write_b32 v104, v11 offset:34816\n"
    "v_mfma_f32_32x32x16_bf16 v[48:63], v[32:35], v[36:39], 0\n"
    "v_mov_b32_e64 v247, v14\n"
    "s_mov_b32 s69, s85\n"
    "ds_write_b64 v250, v[14:15] offset:35072\n"
    "s_mov_b32 s94, 0x10001\n"
    "v_mfma_f32_32x32x16_bf16 v[64:79], v[36:39], v[32:35], 0\n"
    "s_mov_b32 s95, 0xc080c080\n"
    "v_pk_min_u16 v22, v228, s94\n"
    "v_pk_min_u16 v23, v229, s94\n"
    "v_pk_mad_u16 v228, v22, s95, v228\n"
    "v_pk_mad_u16 v229, v23, s95, v229\n"
    "v_mfma_f32_32x32x16_bf16 v[48:63], v[32:35], v[36:39], 0\n"
    "s_sub_u32 s87, s80, s81\n"
    "v_or_b32 v228, v228, v229\n"
    "s_lshr_b32 s87, s87, 6\n"
    "s_lshl2_add_u32 s86, s3, 68224\n"
    "v_mfma_f32_32x32x16_bf16 v[64:79], v[36:39], v[32:35], 0\n"
    "s_waitcnt vmcnt(8)\n"
    "ds_write_b128 v18, v[128:131] offset:32768\n"
    "ds_write_b128 v18, v[132:135] offset:41472\n"
    "v_and_b32 v184, v254, v184\n"
    "v_and_b32 v185, v255, v185\n"
    "v_mfma_f32_32x32x16_bf16 v[48:63], v[32:35], v[36:39], 0\n"
    "v_mov_b32 v106, s86\n"
    "v_mov_b32 v108, s66\n"
    "v_mov_b32 v109, s58\n"
    "v_mov_b32_e64 v110, s59\n"
    "v_mfma_f32_32x32x16_bf16 v[64:79], v[36:39], v[32:35], 0\n"
    "s_waitcnt vmcnt(4)\n"
    "v_cmp_neq_f32_e64 s[20:21], 0, v224\n"
    "v_cmp_neq_f32_e64 s[22:23], 0, v225\n"
    "v_cmp_neq_f32_e64 s[24:25], 0, v226\n"
    "v_cmp_neq_f32_e64 s[26:27], 0, v227\n"
    "v_mfma_f32_32x32x16_bf16 v[48:63], v[32:35], v[36:39], 0\n"
    "v_fma_f32 v224, v224, v224, -v224\n"
    "v_fma_f32 v225, v225, v225, -v225\n"
    "v_fma_f32 v226, v226, v226, -v226\n"
    "v_fma_f32 v227, v227, v227, -v227\n"
    "v_mfma_f32_32x32x16_bf16 v[64:79], v[36:39], v[32:35], 0\n"
    "s_bcnt1_i32_b64 s88, s[20:21]\n"
    "s_bcnt1_i32_b64 s89, s[22:23]\n"
    "s_bcnt1_i32_b64 s90, s[24:25]\n"
    "s_bcnt1_i32_b64 s91, s[26:27]\n"
    "v_or3_b32 v228, v228, v224, v225\n"
    "v_mfma_f32_32x32x16_bf16 v[48:63], v[32:35], v[36:39], 0\n"
    "s_add_u32 s88, s88, s89\n"
    "v_or3_b32 v228, v228, v226, v227\n"
    "s_add_u32 s90, s90, s91\n"
    "v_cmp_ne_u32_e64 s[84:85], 0, v228\n"
    "v_mfma_f32_32x32x16_bf16 v[64:79], v[36:39], v[32:35], 0\n"
    "s_add_u32 s88, s88, s90\n"
    "s_pack_ll_b32_b16 s88, s87, s88\n"
    "s_mov_b64 exec, 1\n"
    "v_mov_b32 v107, s88\n"
    "ds_write2_b32 v108, v109, v110 offset1:2\n"
    "s_cmp_lg_u64 s[84:85], 0\n"
    "ds_write_b32 v106, v107\n"
    "s_cselect_b32 s91, 2.0, 0\n"
    "s_mov_b64 exec, -1\n"
    "s_or_b32 s58, s58, s91\n"
    "v_mfma_f32_32x32x16_bf16 v[48:63], v[32:35], v[36:39], 0\n"
    "s_or_b32 s59, s59, s91\n"
    "s_nop 11\n"
    "s_mov_b64 vcc, 0\n"
    "s_waitcnt lgkmcnt(0)\n"
    "s_barrier\n"
    "s_setprio 0\n"
    ".Ldin_entry:\n"
    "ds_read_b128 v[32:35], v248 offset:16384\n"
    "ds_read_b128 v[64:67], v248 offset:0\n"
    "ds_read_b128 v[68:71], v248 offset:1024\n"
    "ds_read_b128 v[36:39], v248 offset:17408\n"
    "ds_read_b128 v[72:75], v248 offset:2048\n"
    "ds_read_b128 v[76:79], v248 offset:3072\n"
    "ds_read_b128 v[40:43], v248 offset:18432\n"
    "ds_read_b128 v[80:83], v248 offset:4096\n"
    "ds_read_b128 v[84:87], v248 offset:5120\n"
    "ds_read_b128 v[44:47], v248 offset:19456\n"
    "ds_read_b128 v[88:91], v248 offset:6144\n"
    "ds_read_b128 v[92:95], v248 offset:7168\n"
    "s_add_u32 s67, s67, 320\n"
    "s_waitcnt lgkmcnt(9)\n"
    "ds_read_b128 v[48:51], v248 offset:20480\n"
    "ds_read_b128 v[192:195], v248 offset:8192\n"
    "ds_read_b128 v[196:199], v248 offset:9216\n"
    "v_mfma_f32_4x4x4_16b_bf16 v[144:147], v[184:185], v[32:33], v[64:67] cbsz:2 abid:0\n"
    "s_nop 0\n"
    "v_mfma_f32_4x4x4_16b_bf16 v[148:151], v[184:185], v[34:35], v[68:71] cbsz:2 abid:1\n"
    "s_add_u32 m0, s68, 4096\n"
    "s_cbranch_vccnz .Ldin_e1\n"
    "buffer_load_dwordx4 v[208:209], s[40:43], 0 idxen offen offset:0 nt lds\n"
    "buffer_load_dwordx4 v[210:211], s[40:43], 0 idxen offen offset:1024 nt lds\n"
    ".Ldin_e1:\n"
    "s_waitcnt lgkmcnt(9)\n"
    "ds_read_b128 v[52:55], v248 offset:21504\n"
    "ds_read_b128 v[200:203], v248 offset:10240\n"
    "ds_read_b128 v[204:207], v248 offset:11264\n"
    "v_mfma_f32_4x4x4_16b_bf16 v[152:155], v[184:185], v[36:37], v[72:75] cbsz:2 abid:2\n"
    "s_nop 1\n"
    "v_cvt_pk_bf16_f32 v0, v144, v145\n"
    "v_cvt_pk_bf16_f32 v1, v146, v147\n"
    "v_mfma_f32_4x4x4_16b_bf16 v[156:159], v[184:185], v[38:39], v[76:79] cbsz:2 abid:3\n"
    "s_add_u32 m0, s68, 6144\n"
    "s_cbranch_vccnz .Ldin_e2\n"
    "buffer_load_dwordx4 v[212:213], s[40:43], 0 idxen offen offset:0 nt lds\n"
    "buffer_load_dwordx4 v[214:215], s[40:43], 0 idxen offen offset:1024 nt lds\n"
    ".Ldin_e2:\n"
    "s_waitcnt lgkmcnt(9)\n"
    "s_nop 0\n"
    "ds_read_b128 v[56:59], v248 offset:22528\n"
    "ds_read_b128 v[208:211], v248 offset:12288\n"
    "ds_read_b128 v[212:215], v248 offset:13312\n"
    "v_cvt_pk_bf16_f32 v2, v148, v149\n"
    "v_cvt_pk_bf16_f32 v3, v150, v151\n"
    "v_mfma_f32_4x4x4_16b_bf16 v[144:147], v[184:185], v[40:41], v[80:83] cbsz:2 abid:0\n"
    "v_cvt_pk_bf16_f32 v4, v152, v153\n"
    "v_cvt_pk_bf16_f32 v5, v154, v155\n"
    "v_mfma_f32_4x4x4_16b_bf16 v[148:151], v[184:185], v[42:43], v[84:87] cbsz:2 abid:1\n"
    "s_cbranch_vccnz .Ldin_e3\n"
    "s_barrier\n"
    ".Ldin_e3:\n"
    "s_waitcnt lgkmcnt(9)\n"
    "s_nop 0\n"
    "ds_read_b128 v[60:63], v248 offset:23552\n"
    "ds_read_b128 v[216:219], v248 offset:14336\n"
    "ds_read_b128 v[220:223], v248 offset:15360\n"
    "v_cvt_pk_bf16_f32 v6, v156, v157\n"
    "v_cvt_pk_bf16_f32 v7, v158, v159\n"
    "v_mfma_f32_4x4x4_16b_bf16 v[152:155], v[184:185], v[44:45], v[88:91] cbsz:2 abid:2\n"
    "v_cvt_pk_bf16_f32 v8, v144, v145\n"
    "v_cvt_pk_bf16_f32 v9, v146, v147\n"
    "v_mfma_f32_4x4x4_16b_bf16 v[156:159], v[184:185], v[46:47], v[92:95] cbsz:2 abid:3\n"
    "s_waitcnt lgkmcnt(9)\n"
    "v_cvt_pk_bf16_f32 v10, v148, v149\n"
    "v_cvt_pk_bf16_f32 v11, v150, v151\n"
    "v_mfma_f32_4x4x4_16b_bf16 v[144:147], v[184:185], v[48:49], v[192:195] cbsz:2 abid:0\n"
    "v_cvt_pk_bf16_f32 v12, v152, v153\n"
    "v_cvt_pk_bf16_f32 v13, v154, v155\n"
    "v_mfma_f32_4x4x4_16b_bf16 v[148:151], v[184:185], v[50:51], v[196:199] cbsz:2 abid:1\n"
    "s_waitcnt lgkmcnt(6)\n"
    "v_cvt_pk_bf16_f32 v14, v156, v157\n"
    "v_cvt_pk_bf16_f32 v15, v158, v159\n"
    "v_mfma_f32_4x4x4_16b_bf16 v[152:155], v[184:185], v[52:53], v[200:203] cbsz:2 abid:2\n"
    "v_cvt_pk_bf16_f32 v16, v144, v145\n"
    "v_cvt_pk_bf16_f32 v17, v146, v147\n"
    "v_mfma_f32_4x4x4_16b_bf16 v[156:159], v[184:185], v[54:55], v[204:207] cbsz:2 abid:3\n"
    "s_waitcnt lgkmcnt(3)\n"
    "s_waitcnt vmcnt(6)\n"
    "ds_read_b128 v[160:163], v234 offset:0\n"
    "ds_read_b128 v[164:167], v235 offset:0\n"
    "ds_read_b32 v236, v253 offset:256\n"
    "ds_read_b32 v238, v253 offset:260\n"
    "ds_read_b64_tr_b16 v[168:169], v240 offset:0\n"
    "ds_read_b64_tr_b16 v[170:171], v241 offset:256\n"
    "ds_read_b64_tr_b16 v[172:173], v242 offset:512\n"
    "ds_read_b64_tr_b16 v[174:175], v243 offset:768\n"
    "v_cvt_pk_bf16_f32 v18, v148, v149\n"
    "v_cvt_pk_bf16_f32 v19, v150, v151\n"
    "v_mfma_f32_4x4x4_16b_bf16 v[144:147], v[184:185], v[56:57], v[208:211] cbsz:2 abid:0\n"
    "v_cvt_pk_bf16_f32 v20, v152, v153\n"
    "v_cvt_pk_bf16_f32 v21, v154, v155\n"
    "v_mfma_f32_4x4x4_16b_bf16 v[148:151], v[184:185], v[58:59], v[212:215] cbsz:2 abid:1\n"
    "s_waitcnt lgkmcnt(8)\n"
    "v_cvt_pk_bf16_f32 v22, v156, v157\n"
    "v_cvt_pk_bf16_f32 v23, v158, v159\n"
    "v_mfma_f32_4x4x4_16b_bf16 v[152:155], v[184:185], v[60:61], v[216:219] cbsz:2 abid:2\n"
    "v_cvt_pk_bf16_f32 v24, v144, v145\n"
    "v_cvt_pk_bf16_f32 v25, v146, v147\n"
    "v_mfma_f32_4x4x4_16b_bf16 v[156:159], v[184:185], v[62:63], v[220:223] cbsz:2 abid:3\n"
    "s_cbranch_vccnz .Ldin_e8\n"
    "s_barrier\n"
    ".Ldin_e8:\n"
    "s_nop 0\n"
    "ds_read_b128 v[112:115], v252 offset:17408\n"
    "ds_read_b128 v[116:119], v252 offset:17424\n"
    "ds_read_b128 v[120:123], v252 offset:17536\n"
    "ds_read_b128 v[124:127], v252 offset:17552\n"
    "v_cvt_pk_bf16_f32 v26, v148, v149\n"
    "v_cvt_pk_bf16_f32 v27, v150, v151\n"
    "v_cvt_pk_bf16_f32 v28, v152, v153\n"
    "v_cvt_pk_bf16_f32 v29, v154, v155\n"
    "s_nop 0\n"
    "v_cvt_pk_bf16_f32 v30, v156, v157\n"
    "v_cvt_pk_bf16_f32 v31, v158, v159\n"
    "s_add_u32 m0, s68, 0\n"
    "s_waitcnt lgkmcnt(0)\n"
    "v_mfma_f32_16x16x32_bf16 v[128:131], v[0:3], v[160:163], v[112:115]\n"
    "ds_read_b128 v[96:99], v250 offset:34816\n"
    "v_mfma_f32_16x16x32_bf16 v[128:131], v[4:7], v[164:167], v[128:131]\n"
    "ds_read_b128 v[100:103], v250 offset:34832\n"
    "v_mfma_f32_16x16x32_bf16 v[132:135], v[8:11], v[160:163], v[116:119]\n"
    "ds_read_b128 v[104:107], v250 offset:34944\n"
    "v_mfma_f32_16x16x32_bf16 v[132:135], v[12:15], v[164:167], v[132:135]\n"
    "ds_read_b128 v[108:111], v250 offset:34960\n"
    "v_mfma_f32_16x16x32_bf16 v[136:139], v[16:19], v[160:163], v[120:123]\n"
    "v_mfma_f32_16x16x32_bf16 v[136:139], v[20:23], v[164:167], v[136:139]\n"
    "v_mfma_f32_16x16x32_bf16 v[140:143], v[24:27], v[160:163], v[124:127]\n"
    "v_mfma_f32_16x16x32_bf16 v[140:143], v[28:31], v[164:167], v[140:143]\n"
    "s_waitcnt vmcnt(4)\n"
    "ds_read_b128 v[160:163], v234 offset:2048\n"
    "ds_read_b128 v[164:167], v235 offset:2048\n"
    "buffer_load_dwordx4 v[236:237], s[40:43], 0 idxen offen offset:0 nt lds\n"
    "buffer_load_dwordx4 v[238:239], s[40:43], 0 idxen offen offset:1024 nt lds\n"
    "v_and_b32 v248, 24, v249\n"
    "s_memrealtime s[34:35]\n"
    "s_bitcmp1_b32 s7, 0\n"
    "s_cselect_b32 s94, 52, 58\n"
    "s_cselect_b32 s95, 10, 35\n"
    "s_cmp_lg_u64 vcc, 0\n"
    "s_cselect_b32 s94, s94, 52\n"
    "s_cselect_b32 s95, s95, 20\n"
    "s_cmp_lg_u32 s72, 0\n"
    "s_cselect_b32 s72, s94, 0\n"
    "s_cselect_b32 s95, s95, 0\n"
    "s_waitcnt lgkmcnt(0)\n"
    "s_lshl1_add_u32 s76, s95, s34\n"
    "s_cmp_lt_i32 s59, s57\n"
    "s_cselect_b32 s76, s76, -1\n"
    "s_bitcmp1_b32 s58, 30\n"
    "s_cbranch_scc1 .Ldin_odd\n"
    "s_nop 0\n"
    ".Ldin_A0:\n"
    "v_mfma_f32_16x16x32_bf16 v[144:147], v[0:3], v[160:163], v[112:115]\n"
    "v_exp_f32 v128, v128 clamp\n"
    "v_mfma_f32_16x16x32_bf16 v[144:147], v[4:7], v[164:167], v[144:147]\n"
    "v_exp_f32 v129, v129 clamp\n"
    "v_mfma_f32_16x16x32_bf16 v[148:151], v[8:11], v[160:163], v[116:119]\n"
    "v_exp_f32 v130, v130 clamp\n"
    "v_mfma_f32_16x16x32_bf16 v[148:151], v[12:15], v[164:167], v[148:151]\n"
    "v_exp_f32 v131, v131 clamp\n"
    "v_mfma_f32_16x16x32_bf16 v[152:155], v[16:19], v[160:163], v[120:123]\n"
    "v_exp_f32 v132, v132 clamp\n"
    "v_mfma_f32_16x16x32_bf16 v[152:155], v[20:23], v[164:167], v[152:155]\n"
    "v_exp_f32 v133, v133 clamp\n"
    "v_mfma_f32_16x16x32_bf16 v[156:159], v[24:27], v[160:163], v[124:127]\n"
    "v_exp_f32 v134, v134 clamp\n"
    "v_mfma_f32_16x16x32_bf16 v[156:159], v[28:31], v[164:167], v[156:159]\n"
    "v_exp_f32 v135, v135 clamp\n"
    "v_exp_f32 v136, v136 clamp\n"
    "s_mov_b32 m0, s62\n"
    "v_exp_f32 v137, v137 clamp\n"
    "v_add_u32_e64 v253, s67, v244\n"
    "s_waitcnt lgkmcnt(0)\n"
    "v_mfma_f32_4x4x4_16b_bf16 v[224:227], v[228:229], v[176:177], v[224:227] cbsz:4 abid:0\n"
    "v_exp_f32 v138, v138 clamp\n"
    "v_exp_f32 v139, v139 clamp\n"
    "v_mfma_f32_4x4x4_16b_bf16 v[224:227], v[228:229], v[178:179], v[224:227] cbsz:4 abid:1\n"
    "v_exp_f32 v140, v140 clamp\n"
    "v_exp_f32 v141, v141 clamp\n"
    "v_mfma_f32_4x4x4_16b_bf16 v[224:227], v[228:229], v[180:181], v[224:227] cbsz:4 abid:2\n"
    "v_exp_f32 v142, v142 clamp\n"
    "v_exp_f32 v143, v143 clamp\n"
    "v_mfma_f32_4x4x4_16b_bf16 v[224:227], v[228:229], v[182:183], v[224:227] cbsz:4 abid:3\n"
    "s_waitcnt vmcnt(4)\n"
    "ds_read_b64_tr_b16 v[176:177], v240 offset:2048\n"
    "ds_read_b64_tr_b16 v[178:179], v241 offset:2304\n"
    "ds_read_b64_tr_b16 v[180:181], v242 offset:2560\n"
    "ds_read_b64_tr_b16 v[182:183], v243 offset:2816\n"
    "ds_read_b32 v236, v253 offset:0\n"
    "ds_read_b32 v238, v253 offset:4\n"
    "ds_read_b128 v[160:163], v234 offset:4096\n"
    "ds_read_b128 v[164:167], v235 offset:4096\n"
    "s_cbranch_vccz .Ldin_A0_np\n"
    "s_setprio 1\n"
    ".Ldin_A0_np:\n"
    "v_pk_fma_f32 v[128:129], v[128:129], s[70:71], v[96:97]\n"
    "v_pk_fma_f32 v[130:131], v[130:131], s[70:71], v[98:99]\n"
    "v_pk_add_f32 v[190:191], v[224:225], v[226:227]\n"
    "v_add_f32 v190, v190, v191\n"
    "ds_write_addtid_b32 v190 offset:32768\n"
    "s_add_u32 m0, s68, 2048\n"
    "s_bfe_u32 s94, s59, 0x180006\n"
    "s_mul_i32 s86, s94, s56\n"
    "v_mbcnt_lo_u32_b32 v184, s20, 0\n"
    "v_mbcnt_lo_u32_b32 v185, s22, 0\n"
    "v_mbcnt_lo_u32_b32 v186, s24, 0\n"
    "v_mbcnt_lo_u32_b32 v187, s26, 0\n"
    "s_bcnt1_i32_b64 s90, s[20:21]\n"
    "s_bcnt1_i32_b64 s91, s[22:23]\n"
    "s_bcnt1_i32_b64 s92, s[24:25]\n"
    "s_bcnt1_i32_b64 s93, s[26:27]\n"
    "v_mbcnt_hi_u32_b32 v184, s21, v184\n"
    "v_mbcnt_hi_u32_b32 v185, s23, v185\n"
    "v_mbcnt_hi_u32_b32 v186, s25, v186\n"
    "v_mbcnt_hi_u32_b32 v187, s27, v187\n"
    "s_lshl2_add_u32 s90, s90, s65\n"
    "s_lshl2_add_u32 s91, s91, s90\n"
    "s_lshl2_add_u32 s92, s92, s91\n"
    "s_lshl2_add_u32 s93, s93, s92\n"
    "v_add_u32_e64 v188, s86, v251\n"
    "v_pk_add_f32 v[230:231], v[128:129], v[130:131]\n"
    "v_pk_mul_f32 v[232:233], v[128:129], v[130:131]\n"
    "v_pk_fma_f32 v[132:133], v[132:133], s[70:71], v[100:101]\n"
    "v_pk_fma_f32 v[230:231], v[230:231], v[132:133], v[232:233]\n"
    "v_pk_mul_f32 v[232:233], v[232:233], v[132:133]\n"
    "v_lshl_add_u32 v184, v184, 2, s65\n"
    "v_lshl_add_u32 v185, v185, 2, s90\n"
    "v_lshl_add_u32 v186, v186, 2, s91\n"
    "v_lshl_add_u32 v187, v187, 2, s92\n"
    "v_mov_b32 v191, -1\n"
    "v_lshl_add_u32 v190, v249, 1, s93\n"
    "ds_write2_b32 v190, v191, v191 offset1:64\n"
    "s_mov_b64 exec, s[20:21]\n"
    "ds_write_b32 v184, v188\n"
    "s_mov_b64 exec, s[22:23]\n"
    "v_add_u32 v189, 64, v188\n"
    "ds_write_b32 v185, v189\n"
    "s_mov_b64 exec, s[24:25]\n"
    "v_add_u32 v190, 128, v188\n"
    "ds_write_b32 v186, v190\n"
    "s_mov_b64 exec, s[26:27]\n"
    "v_add_u32 v189, 192, v188\n"
    "ds_write_b32 v187, v189\n"
    "s_mov_b64 exec, -1\n"
    "s_add_u32 s95, s93, 60\n"
    "s_add_u32 s94, s82, 128\n"
    "s_andn2_b32 s95, s95, 63\n"
    "s_max_u32 s83, s95, s94\n"
    "v_pk_fma_f32 v[134:135], v[134:135], s[70:71], v[102:103]\n"
    "v_pk_fma_f32 v[230:231], v[230:231], v[134:135], v[232:233]\n"
    "v_pk_mul_f32 v[232:233], v[232:233], v[134:135]\n"
    "v_pk_fma_f32 v[136:137], v[136:137], s[70:71], v[104:105]\n"
    "v_pk_fma_f32 v[230:231], v[230:231], v[136:137], v[232:233]\n"
    "v_pk_mul_f32 v[232:233], v[232:233], v[136:137]\n"
    "v_pk_fma_f32 v[138:139], v[138:139], s[70:71], v[106:107]\n"
    "v_pk_fma_f32 v[230:231], v[230:231], v[138:139], v[232:233]\n"
    "v_pk_mul_f32 v[232:233], v[232:233], v[138:139]\n"
    "v_pk_fma_f32 v[140:141], v[140:141], s[70:71], v[108:109]\n"
    "v_pk_fma_f32 v[230:231], v[230:231], v[140:141], v[232:233]\n"
    "v_pk_mul_f32 v[232:233], v[232:233], v[140:141]\n"
    "v_pk_fma_f32 v[142:143], v[142:143], s[70:71], v[110:111]\n"
    "v_pk_fma_f32 v[230:231], v[230:231], v[142:143], v[232:233]\n"
    "v_pk_mul_f32 v[232:233], v[232:233], v[142:143]\n"
    "v_rcp_f32 v232, v232\n"
    "v_rcp_f32 v233, v233\n"
    "v_fma_f32 v230, v230, v232, v247\n"
    "v_fmac_f32 v230, v231, v233\n"
    "v_cvt_pk_bf16_f32 v230, v230, v230\n"
    "s_add_u32 s67, s67, 64\n"
    "s_waitcnt lgkmcnt(0)\n"
    "ds_write_b16 v245, v230\n"
    "ds_read_b64 v[228:229], v246\n"
    "buffer_load_dwordx4 v[236:237], s[40:43], 0 idxen offen offset:0 nt lds\n"
    "buffer_load_dwordx4 v[238:239], s[40:43], 0 idxen offen offset:1024 nt lds\n"
    ".Ldin_B1:\n"
    "v_mfma_f32_16x16x32_bf16 v[128:131], v[0:3], v[160:163], v[112:115]\n"
    "v_exp_f32 v144, v144 clamp\n"
    "v_mfma_f32_16x16x32_bf16 v[128:131], v[4:7], v[164:167], v[128:131]\n"
    "v_exp_f32 v145, v145 clamp\n"
    "v_mfma_f32_16x16x32_bf16 v[132:135], v[8:11], v[160:163], v[116:119]\n"
    "v_exp_f32 v146, v146 clamp\n"
    "v_mfma_f32_16x16x32_bf16 v[132:135], v[12:15], v[164:167], v[132:135]\n"
    "v_exp_f32 v147, v147 clamp\n"
    "v_mfma_f32_16x16x32_bf16 v[136:139], v[16:19], v[160:163], v[120:123]\n"
    "v_exp_f32 v148, v148 clamp\n"
    "v_mfma_f32_16x16x32_bf16 v[136:139], v[20:23], v[164:167], v[136:139]\n"
    "v_exp_f32 v149, v149 clamp\n"
    "v_mfma_f32_16x16x32_bf16 v[140:143], v[24:27], v[160:163], v[124:127]\n"
    "v_exp_f32 v150, v150 clamp\n"
    "v_mfma_f32_16x16x32_bf16 v[140:143], v[28:31], v[164:167], v[140:143]\n"
    "v_exp_f32 v151, v151 clamp\n"
    "v_exp_f32 v152, v152 clamp\n"
    "s_cmp_eq_u32 s67, s80\n"
    "s_cselect_b32 s67, s65, s67\n"
    "s_and_b32 s94, s59, 63\n"
    "s_mul_i32 s94, s94, 272\n"
    "v_add_u32 v252, s94, v250\n"
    "s_add_u32 m0, s68, 4096\n"
    "v_exp_f32 v153, v153 clamp\n"
    "v_add_u32 v253, s67, v244\n"
    "s_waitcnt lgkmcnt(0)\n"
    "v_mfma_f32_4x4x4_16b_bf16 v[224:227], v[228:229], v[168:169], 0 cbsz:4 abid:0\n"
    "v_exp_f32 v154, v154 clamp\n"
    "v_exp_f32 v155, v155 clamp\n"
    "v_mfma_f32_4x4x4_16b_bf16 v[224:227], v[228:229], v[170:171], v[224:227] cbsz:4 abid:1\n"
    "v_exp_f32 v156, v156 clamp\n"
    "v_exp_f32 v157, v157 clamp\n"
    "v_mfma_f32_4x4x4_16b_bf16 v[224:227], v[228:229], v[172:173], v[224:227] cbsz:4 abid:2\n"
    "v_exp_f32 v158, v158 clamp\n"
    "v_exp_f32 v159, v159 clamp\n"
    "v_mfma_f32_4x4x4_16b_bf16 v[224:227], v[228:229], v[174:175], v[224:227] cbsz:4 abid:3\n"
    "s_waitcnt vmcnt(4)\n"
    "s_memrealtime s[34:35]\n"
    "ds_read_b64_tr_b16 v[168:169], v240 offset:4096\n"
    "ds_read_b64_tr_b16 v[170:171], v241 offset:4352\n"
    "ds_read_b64_tr_b16 v[172:173], v242 offset:4608\n"
    "ds_read_b64_tr_b16 v[174:175], v243 offset:4864\n"
    "ds_read_b32 v236, v253 offset:0\n"
    "ds_read_b32 v238, v253 offset:4\n"
    "ds_read_b128 v[160:163], v234 offset:6144\n"
    "ds_read_b128 v[164:167], v235 offset:6144\n"
    "v_add_u32_e64 v184, v248, v252\n"
    "ds_read_b64 v[184:185], v184\n"
    "v_pk_fma_f32 v[144:145], v[144:145], s[70:71], v[96:97]\n"
    "v_pk_fma_f32 v[146:147], v[146:147], s[70:71], v[98:99]\n"
    "v_pk_add_f32 v[230:231], v[144:145], v[146:147]\n"
    "v_pk_mul_f32 v[232:233], v[144:145], v[146:147]\n"
    "v_pk_fma_f32 v[148:149], v[148:149], s[70:71], v[100:101]\n"
    "v_pk_fma_f32 v[230:231], v[230:231], v[148:149], v[232:233]\n"
    "v_pk_mul_f32 v[232:233], v[232:233], v[148:149]\n"
    "v_pk_fma_f32 v[150:151], v[150:151], s[70:71], v[102:103]\n"
    "v_pk_fma_f32 v[230:231], v[230:231], v[150:151], v[232:233]\n"
    "v_pk_mul_f32 v[232:233], v[232:233], v[150:151]\n"
    "v_pk_fma_f32 v[152:153], v[152:153], s[70:71], v[104:105]\n"
    "v_pk_fma_f32 v[230:231], v[230:231], v[152:153], v[232:233]\n"
    "v_pk_mul_f32 v[232:233], v[232:233], v[152:153]\n"
    "v_pk_fma_f32 v[154:155], v[154:155], s[70:71], v[106:107]\n"
    "v_pk_fma_f32 v[230:231], v[230:231], v[154:155], v[232:233]\n"
    "v_pk_mul_f32 v[232:233], v[232:233], v[154:155]\n"
    "v_pk_fma_f32 v[156:157], v[156:157], s[70:71], v[108:109]\n"
    "v_pk_fma_f32 v[230:231], v[230:231], v[156:157], v[232:233]\n"
    "v_pk_mul_f32 v[232:233], v[232:233], v[156:157]\n"
    "v_pk_fma_f32 v[158:159], v[158:159], s[70:71], v[110:111]\n"
    "v_pk_fma_f32 v[230:231], v[230:231], v[158:159], v[232:233]\n"
    "v_pk_mul_f32 v[232:233], v[232:233], v[158:159]\n"
    "v_rcp_f32 v232, v232\n"
    "v_rcp_f32 v233, v233\n"
    "v_fma_f32 v230, v230, v232, v247\n"
    "v_fmac_f32 v230, v231, v233\n"
    "v_cvt_pk_bf16_f32 v230, v230, v230\n"
    "s_add_u32 s67, s67, 64\n"
    "s_waitcnt lgkmcnt(0)\n"
    "ds_write_b16 v245, v230\n"
    "ds_read_b64 v[228:229], v246\n"
    "v_and_b32 v184, v254, v184\n"
    "v_and_b32 v185, v255, v185\n"
    "s_cmp_le_u32 s34, s76\n"
    "s_cbranch_scc1 .Ldin_B1_wait\n"
    ".Ldin_B1_go:\n"
    "s_sub_u32 s94, s34, 60\n"
    "s_max_u32 s76, s76, s94\n"
    "s_add_u32 s76, s76, s72\n"
    ".Ldin_B1_dma:\n"
    "buffer_load_dwordx4 v[236:237], s[40:43], 0 idxen offen offset:0 nt lds\n"
    "buffer_load_dwordx4 v[238:239], s[40:43], 0 idxen offen offset:1024 nt lds\n"
    ".Ldin_B1_nd:\n"
    ".Ldin_G2:\n"
    "v_mfma_f32_16x16x32_bf16 v[144:147], v[0:3], v[160:163], v[112:115]\n"
    "v_exp_f32 v128, v128 clamp\n"
    "v_mfma_f32_16x16x32_bf16 v[144:147], v[4:7], v[164:167], v[144:147]\n"
    "v_exp_f32 v129, v129 clamp\n"
    "v_mfma_f32_16x16x32_bf16 v[148:151], v[8:11], v[160:163], v[116:119]\n"
    "v_exp_f32 v130, v130 clamp\n"
    "v_mfma_f32_16x16x32_bf16 v[148:151], v[12:15], v[164:167], v[148:151]\n"
    "v_exp_f32 v131, v131 clamp\n"
    "v_mfma_f32_16x16x32_bf16 v[152:155], v[16:19], v[160:163], v[120:123]\n"
    "v_exp_f32 v132, v132 clamp\n"
    "v_mfma_f32_16x16x32_bf16 v[152:155], v[20:23], v[164:167], v[152:155]\n"
    "v_exp_f32 v133, v133 clamp\n"
    "v_mfma_f32_16x16x32_bf16 v[156:159], v[24:27], v[160:163], v[124:127]\n"
    "v_exp_f32 v134, v134 clamp\n"
    "v_mfma_f32_16x16x32_bf16 v[156:159], v[28:31], v[164:167], v[156:159]\n"
    "v_exp_f32 v135, v135 clamp\n"
    "v_exp_f32 v136, v136 clamp\n"
    "s_cmp_eq_u32 s67, s80\n"
    "s_cselect_b32 s67, s65, s67\n"
    "s_add_u32 m0, s68, 6144\n"
    "v_exp_f32 v137, v137 clamp\n"
    "v_add_u32_e64 v253, s67, v244\n"
    "s_waitcnt lgkmcnt(0)\n"
    "v_mfma_f32_4x4x4_16b_bf16 v[224:227], v[228:229], v[176:177], v[224:227] cbsz:4 abid:0\n"
    "v_exp_f32 v138, v138 clamp\n"
    "v_exp_f32 v139, v139 clamp\n"
    "v_mfma_f32_4x4x4_16b_bf16 v[224:227], v[228:229], v[178:179], v[224:227] cbsz:4 abid:1\n"
    "v_exp_f32 v140, v140 clamp\n"
    "v_exp_f32 v141, v141 clamp\n"
    "v_mfma_f32_4x4x4_16b_bf16 v[224:227], v[228:229], v[180:181], v[224:227] cbsz:4 abid:2\n"
    "v_exp_f32 v142, v142 clamp\n"
    "v_exp_f32 v143, v143 clamp\n"
    "v_mfma_f32_4x4x4_16b_bf16 v[224:227], v[228:229], v[182:183], v[224:227] cbsz:4 abid:3\n"
    "s_waitcnt vmcnt(4)\n"
    "s_memrealtime s[34:35]\n"
    "ds_read_b64_tr_b16 v[176:177], v240 offset:6144\n"
    "ds_read_b64_tr_b16 v[178:179], v241 offset:6400\n"
    "ds_read_b64_tr_b16 v[180:181], v242 offset:6656\n"
    "ds_read_b64_tr_b16 v[182:183], v243 offset:6912\n"
    "ds_read_b32 v236, v253 offset:0\n"
    "ds_read_b32 v238, v253 offset:4\n"
    "ds_read_b128 v[160:163], v234 offset:0\n"
    "ds_read_b128 v[164:167], v235 offset:0\n"
    "v_pk_fma_f32 v[128:129], v[128:129], s[70:71], v[96:97]\n"
    "v_pk_fma_f32 v[130:131], v[130:131], s[70:71], v[98:99]\n"
    "v_pk_add_f32 v[230:231], v[128:129], v[130:131]\n"
    "v_pk_mul_f32 v[232:233], v[128:129], v[130:131]\n"
    "v_pk_fma_f32 v[132:133], v[132:133], s[70:71], v[100:101]\n"
    "v_pk_fma_f32 v[230:231], v[230:231], v[132:133], v[232:233]\n"
    "v_pk_mul_f32 v[232:233], v[232:233], v[132:133]\n"
    "v_pk_fma_f32 v[134:135], v[134:135], s[70:71], v[102:103]\n"
    "v_pk_fma_f32 v[230:231], v[230:231], v[134:135], v[232:233]\n"
    "v_pk_mul_f32 v[232:233], v[232:233], v[134:135]\n"
    "v_pk_fma_f32 v[136:137], v[136:137], s[70:71], v[104:105]\n"
    "v_pk_fma_f32 v[230:231], v[230:231], v[136:137], v[232:233]\n"
    "v_pk_mul_f32 v[232:233], v[232:233], v[136:137]\n"
    "v_pk_fma_f32 v[138:139], v[138:139], s[70:71], v[106:107]\n"
    "v_pk_fma_f32 v[230:231], v[230:231], v[138:139], v[232:233]\n"
    "v_pk_mul_f32 v[232:233], v[232:233], v[138:139]\n"
    "v_pk_fma_f32 v[140:141], v[140:141], s[70:71], v[108:109]\n"
    "v_pk_fma_f32 v[230:231], v[230:231], v[140:141], v[232:233]\n"
    "v_pk_mul_f32 v[232:233], v[232:233], v[140:141]\n"
    "v_pk_fma_f32 v[142:143], v[142:143], s[70:71], v[110:111]\n"
    "v_pk_fma_f32 v[230:231], v[230:231], v[142:143], v[232:233]\n"
    "v_pk_mul_f32 v[232:233], v[232:233], v[142:143]\n"
    "v_rcp_f32 v232, v232\n"
    "v_rcp_f32 v233, v233\n"
    "v_fma_f32 v230, v230, v232, v247\n"
    "v_fmac_f32 v230, v231, v233\n"
    "v_cvt_pk_bf16_f32 v230, v230, v230\n"
    "s_add_u32 s67, s67, 64\n"
    "s_waitcnt lgkmcnt(0)\n"
    "ds_write_b16 v245, v230\n"
    "ds_read_b64 v[228:229], v246\n"
    "s_cmp_le_u32 s34, s76\n"
    "s_cbranch_scc1 .Ldin_G2_wait\n"
    ".Ldin_G2_go:\n"
    "s_sub_u32 s94, s34, 60\n"
    "s_max_u32 s76, s76, s94\n"
    "s_add_u32 s76, s76, s72\n"
    ".Ldin_G2_dma:\n"
    "buffer_load_dwordx4 v[236:237], s[40:43], 0 idxen offen offset:0 nt lds\n"
    "buffer_load_dwordx4 v[238:239], s[40:43], 0 idxen offen offset:1024 nt lds\n"
    ".Ldin_G2_nd:\n"
    "s_cmp_eq_u32 s67, s82\n"
    "s_cbranch_scc1 .Ldin_L3\n"
    ".Ldin_G3:\n"
    "v_mfma_f32_16x16x32_bf16 v[128:131], v[0:3], v[160:163], v[112:115]\n"
    "v_exp_f32 v144, v144 clamp\n"
    "v_mfma_f32_16x16x32_bf16 v[128:131], v[4:7], v[164:167], v[128:131]\n"
    "v_exp_f32 v145, v145 clamp\n"
    "v_mfma_f32_16x16x32_bf16 v[132:135], v[8:11], v[160:163], v[116:119]\n"
    "v_exp_f32 v146, v146 clamp\n"
    "v_mfma_f32_16x16x32_bf16 v[132:135], v[12:15], v[164:167], v[132:135]\n"
    "v_exp_f32 v147, v147 clamp\n"
    "v_mfma_f32_16x16x32_bf16 v[136:139], v[16:19], v[160:163], v[120:123]\n"
    "v_exp_f32 v148, v148 clamp\n"
    "v_mfma_f32_16x16x32_bf16 v[136:139], v[20:23], v[164:167], v[136:139]\n"
    "v_exp_f32 v149, v149 clamp\n"
    "v_mfma_f32_16x16x32_bf16 v[140:143], v[24:27], v[160:163], v[124:127]\n"
    "v_exp_f32 v150, v150 clamp\n"
    "v_mfma_f32_16x16x32_bf16 v[140:143], v[28:31], v[164:167], v[140:143]\n"
    "v_exp_f32 v151, v151 clamp\n"
    "v_exp_f32 v152, v152 clamp\n"
    "s_cmp_eq_u32 s67, s80\n"
    "s_cselect_b32 s67, s65, s67\n"
    "s_add_u32 m0, s68, 0\n"
    "v_exp_f32 v153, v153 clamp\n"
    "v_add_u32 v253, s67, v244\n"
    "s_waitcnt lgkmcnt(0)\n"
    "v_mfma_f32_4x4x4_16b_bf16 v[224:227], v[228:229], v[168:169], v[224:227] cbsz:4 abid:0\n"
    "v_exp_f32 v154, v154 clamp\n"
    "v_exp_f32 v155, v155 clamp\n"
    "v_mfma_f32_4x4x4_16b_bf16 v[224:227], v[228:229], v[170:171], v[224:227] cbsz:4 abid:1\n"
    "v_exp_f32 v156, v156 clamp\n"
    "v_exp_f32 v157, v157 clamp\n"
    "v_mfma_f32_4x4x4_16b_bf16 v[224:227], v[228:229], v[172:173], v[224:227] cbsz:4 abid:2\n"
    "v_exp_f32 v158, v158 clamp\n"
    "v_exp_f32 v159, v159 clamp\n"
    "v_mfma_f32_4x4x4_16b_bf16 v[224:227], v[228:229], v[174:175], v[224:227] cbsz:4 abid:3\n"
    "s_waitcnt vmcnt(4)\n"
    "s_memrealtime s[34:35]\n"
    "ds_read_b64_tr_b16 v[168:169], v240 offset:0\n"
    "ds_read_b64_tr_b16 v[170:171], v241 offset:256\n"
    "ds_read_b64_tr_b16 v[172:173], v242 offset:512\n"
    "ds_read_b64_tr_b16 v[174:175], v243 offset:768\n"
    "ds_read_b32 v236, v253 offset:0\n"
    "ds_read_b32 v238, v253 offset:4\n"
    "ds_read_b128 v[160:163], v234 offset:2048\n"
    "ds_read_b128 v[164:167], v235 offset:2048\n"
    "v_pk_fma_f32 v[144:145], v[144:145], s[70:71], v[96:97]\n"
    "v_pk_fma_f32 v[146:147], v[146:147], s[70:71], v[98:99]\n"
    "v_pk_add_f32 v[230:231], v[144:145], v[146:147]\n"
    "v_pk_mul_f32 v[232:233], v[144:145], v[146:147]\n"
    "v_pk_fma_f32 v[148:149], v[148:149], s[70:71], v[100:101]\n"
    "v_pk_fma_f32 v[230:231], v[230:231], v[148:149], v[232:233]\n"
    "v_pk_mul_f32 v[232:233], v[232:233], v[148:149]\n"
    "v_pk_fma_f32 v[150:151], v[150:151], s[70:71], v[102:103]\n"
    "v_pk_fma_f32 v[230:231], v[230:231], v[150:151], v[232:233]\n"
    "v_pk_mul_f32 v[232:233], v[232:233], v[150:151]\n"
    "v_pk_fma_f32 v[152:153], v[152:153], s[70:71], v[104:105]\n"
    "v_pk_fma_f32 v[230:231], v[230:231], v[152:153], v[232:233]\n"
    "v_pk_mul_f32 v[232:233], v[232:233], v[152:153]\n"
    "v_pk_fma_f32 v[154:155], v[154:155], s[70:71], v[106:107]\n"
    "v_pk_fma_f32 v[230:231], v[230:231], v[154:155], v[232:233]\n"
    "v_pk_mul_f32 v[232:233], v[232:233], v[154:155]\n"
    "v_pk_fma_f32 v[156:157], v[156:157], s[70:71], v[108:109]\n"
    "v_pk_fma_f32 v[230:231], v[230:231], v[156:157], v[232:233]\n"
    "v_pk_mul_f32 v[232:233], v[232:233], v[156:157]\n"
    "v_pk_fma_f32 v[158:159], v[158:159], s[70:71], v[110:111]\n"
    "v_pk_fma_f32 v[230:231], v[230:231], v[158:159], v[232:233]\n"
    "v_pk_mul_f32 v[232:233], v[232:233], v[158:159]\n"
    "v_rcp_f32 v232, v232\n"
    "v_rcp_f32 v233, v233\n"
    "v_fma_f32 v230, v230, v232, v247\n"
    "v_fmac_f32 v230, v231, v233\n"
    "v_cvt_pk_bf16_f32 v230, v230, v230\n"
    "s_add_u32 s67, s67, 64\n"
    "s_waitcnt lgkmcnt(0)\n"
    "ds_write_b16 v245, v230\n"
    "ds_read_b64 v[228:229], v246\n"
    "s_cmp_le_u32 s34, s76\n"
    "s_cbranch_scc1 .Ldin_G3_wait\n"
    ".Ldin_G3_go:\n"
    "s_add_u32 s76, s76, s72\n"
    ".Ldin_G3_dma:\n"
    "buffer_load_dwordx4 v[236:237], s[40:43], 0 idxen offen offset:0 nt lds\n"
    "buffer_load_dwordx4 v[238:239], s[40:43], 0 idxen offen offset:1024 nt lds\n"
    ".Ldin_G3_nd:\n"
    "s_cmp_eq_u32 s67, s82\n"
    "s_cbranch_scc1 .Ldin_L0\n"
    ".Ldin_G0:\n"
    "v_mfma_f32_16x16x32_bf16 v[144:147], v[0:3], v[160:163], v[112:115]\n"
    "v_exp_f32 v128, v128 clamp\n"
    "v_mfma_f32_16x16x32_bf16 v[144:147], v[4:7], v[164:167], v[144:147]\n"
    "v_exp_f32 v129, v129 clamp\n"
    "v_mfma_f32_16x16x32_bf16 v[148:151], v[8:11], v[160:163], v[116:119]\n"
    "v_exp_f32 v130, v130 clamp\n"
    "v_mfma_f32_16x16x32_bf16 v[148:151], v[12:15], v[164:167], v[148:151]\n"
    "v_exp_f32 v131, v131 clamp\n"
    "v_mfma_f32_16x16x32_bf16 v[152:155], v[16:19], v[160:163], v[120:123]\n"
    "v_exp_f32 v132, v132 clamp\n"
    "v_mfma_f32_16x16x32_bf16 v[152:155], v[20:23], v[164:167], v[152:155]\n"
    "v_exp_f32 v133, v133 clamp\n"
    "v_mfma_f32_16x16x32_bf16 v[156:159], v[24:27], v[160:163], v[124:127]\n"
    "v_exp_f32 v134, v134 clamp\n"
    "v_mfma_f32_16x16x32_bf16 v[156:159], v[28:31], v[164:167], v[156:159]\n"
    "v_exp_f32 v135, v135 clamp\n"
    "v_exp_f32 v136, v136 clamp\n"
    "s_cmp_eq_u32 s67, s80\n"
    "s_cselect_b32 s67, s65, s67\n"
    "s_add_u32 m0, s68, 2048\n"
    "v_exp_f32 v137, v137 clamp\n"
    "v_add_u32_e64 v253, s67, v244\n"
    "s_waitcnt lgkmcnt(0)\n"
    "v_mfma_f32_4x4x4_16b_bf16 v[224:227], v[228:229], v[176:177], v[224:227] cbsz:4 abid:0\n"
    "v_exp_f32 v138, v138 clamp\n"
    "v_exp_f32 v139, v139 clamp\n"
    "v_mfma_f32_4x4x4_16b_bf16 v[224:227], v[228:229], v[178:179], v[224:227] cbsz:4 abid:1\n"
    "v_exp_f32 v140, v140 clamp\n"
    "v_exp_f32 v141, v141 clamp\n"
    "v_mfma_f32_4x4x4_16b_bf16 v[224:227], v[228:229], v[180:181], v[224:227] cbsz:4 abid:2\n"
    "v_exp_f32 v142, v142 clamp\n"
    "v_exp_f32 v143, v143 clamp\n"
    "v_mfma_f32_4x4x4_16b_bf16 v[224:227], v[228:229], v[182:183], v[224:227] cbsz:4 abid:3\n"
    "s_waitcnt vmcnt(4)\n"
    "s_memrealtime s[34:35]\n"
    "ds_read_b64_tr_b16 v[176:177], v240 offset:2048\n"
    "ds_read_b64_tr_b16 v[178:179], v241 offset:2304\n"
    "ds_read_b64_tr_b16 v[180:181], v242 offset:2560\n"
    "ds_read_b64_tr_b16 v[182:183], v243 offset:2816\n"
    "ds_read_b32 v236, v253 offset:0\n"
    "ds_read_b32 v238, v253 offset:4\n"
    "ds_read_b128 v[160:163], v234 offset:4096\n"
    "ds_read_b128 v[164:167], v235 offset:4096\n"
    "v_pk_fma_f32 v[128:129], v[128:129], s[70:71], v[96:97]\n"
    "v_pk_fma_f32 v[130:131], v[130:131], s[70:71], v[98:99]\n"
    "v_pk_add_f32 v[230:231], v[128:129], v[130:131]\n"
    "v_pk_mul_f32 v[232:233], v[128:129], v[130:131]\n"
    "v_pk_fma_f32 v[132:133], v[132:133], s[70:71], v[100:101]\n"
    "v_pk_fma_f32 v[230:231], v[230:231], v[132:133], v[232:233]\n"
    "v_pk_mul_f32 v[232:233], v[232:233], v[132:133]\n"
    "v_pk_fma_f32 v[134:135], v[134:135], s[70:71], v[102:103]\n"
    "v_pk_fma_f32 v[230:231], v[230:231], v[134:135], v[232:233]\n"
    "v_pk_mul_f32 v[232:233], v[232:233], v[134:135]\n"
    "v_pk_fma_f32 v[136:137], v[136:137], s[70:71], v[104:105]\n"
    "v_pk_fma_f32 v[230:231], v[230:231], v[136:137], v[232:233]\n"
    "v_pk_mul_f32 v[232:233], v[232:233], v[136:137]\n"
    "v_pk_fma_f32 v[138:139], v[138:139], s[70:71], v[106:107]\n"
    "v_pk_fma_f32 v[230:231], v[230:231], v[138:139], v[232:233]\n"
    "v_pk_mul_f32 v[232:233], v[232:233], v[138:139]\n"
    "v_pk_fma_f32 v[140:141], v[140:141], s[70:71], v[108:109]\n"
    "v_pk_fma_f32 v[230:231], v[230:231], v[140:141], v[232:233]\n"
    "v_pk_mul_f32 v[232:233], v[232:233], v[140:141]\n"
    "v_pk_fma_f32 v[142:143], v[142:143], s[70:71], v[110:111]\n"
    "v_pk_fma_f32 v[230:231], v[230:231], v[142:143], v[232:233]\n"
    "v_pk_mul_f32 v[232:233], v[232:233], v[142:143]\n"
    "v_rcp_f32 v232, v232\n"
    "v_rcp_f32 v233, v233\n"
    "v_fma_f32 v230, v230, v232, v247\n"
    "v_fmac_f32 v230, v231, v233\n"
    "v_cvt_pk_bf16_f32 v230, v230, v230\n"
    "s_add_u32 s67, s67, 64\n"
    "s_waitcnt lgkmcnt(0)\n"
    "ds_write_b16 v245, v230\n"
    "ds_read_b64 v[228:229], v246\n"
    "s_cmp_le_u32 s34, s76\n"
    "s_cbranch_scc1 .Ldin_G0_wait\n"
    ".Ldin_G0_go:\n"
    "s_sub_u32 s94, s34, 60\n"
    "s_max_u32 s76, s76, s94\n"
    "s_add_u32 s76, s76, s72\n"
    ".Ldin_G0_dma:\n"
    "buffer_load_dwordx4 v[236:237], s[40:43], 0 idxen offen offset:0 nt lds\n"
    "buffer_load_dwordx4 v[238:239], s[40:43], 0 idxen offen offset:1024 nt lds\n"
    ".Ldin_G0_nd:\n"
    "s_cmp_eq_u32 s67, s82\n"
    "s_cbranch_scc1 .Ldin_L1\n"
    ".Ldin_G1:\n"
    "v_mfma_f32_16x16x32_bf16 v[128:131], v[0:3], v[160:163], v[112:115]\n"
    "v_exp_f32 v144, v144 clamp\n"
    "v_mfma_f32_16x16x32_bf16 v[128:131], v[4:7], v[164:167], v[128:131]\n"
    "v_exp_f32 v145, v145 clamp\n"
    "v_mfma_f32_16x16x32_bf16 v[132:135], v[8:11], v[160:163], v[116:119]\n"
    "v_exp_f32 v146, v146 clamp\n"
    "v_mfma_f32_16x16x32_bf16 v[132:135], v[12:15], v[164:167], v[132:135]\n"
    "v_exp_f32 v147, v147 clamp\n"
    "v_mfma_f32_16x16x32_bf16 v[136:139], v[16:19], v[160:163], v[120:123]\n"
    "v_exp_f32 v148, v148 clamp\n"
    "v_mfma_f32_16x16x32_bf16 v[136:139], v[20:23], v[164:167], v[136:139]\n"
    "v_exp_f32 v149, v149 clamp\n"
    "v_mfma_f32_16x16x32_bf16 v[140:143], v[24:27], v[160:163], v[124:127]\n"
    "v_exp_f32 v150, v150 clamp\n"
    "v_mfma_f32_16x16x32_bf16 v[140:143], v[28:31], v[164:167], v[140:143]\n"
    "v_exp_f32 v151, v151 clamp\n"
    "v_exp_f32 v152, v152 clamp\n"
    "s_cmp_eq_u32 s67, s80\n"
    "s_cselect_b32 s67, s65, s67\n"
    "s_add_u32 m0, s68, 4096\n"
    "v_exp_f32 v153, v153 clamp\n"
    "v_add_u32_e64 v253, s67, v244\n"
    "s_waitcnt lgkmcnt(0)\n"
    "v_mfma_f32_4x4x4_16b_bf16 v[224:227], v[228:229], v[168:169], v[224:227] cbsz:4 abid:0\n"
    "v_exp_f32 v154, v154 clamp\n"
    "v_exp_f32 v155, v155 clamp\n"
    "v_mfma_f32_4x4x4_16b_bf16 v[224:227], v[228:229], v[170:171], v[224:227] cbsz:4 abid:1\n"
    "v_exp_f32 v156, v156 clamp\n"
    "v_exp_f32 v157, v157 clamp\n"
    "v_mfma_f32_4x4x4_16b_bf16 v[224:227], v[228:229], v[172:173], v[224:227] cbsz:4 abid:2\n"
    "v_exp_f32 v158, v158 clamp\n"
    "v_exp_f32 v159, v159 clamp\n"
    "v_mfma_f32_4x4x4_16b_bf16 v[224:227], v[228:229], v[174:175], v[224:227] cbsz:4 abid:3\n"
    "s_waitcnt vmcnt(4)\n"
    "s_memrealtime s[34:35]\n"
    "ds_read_b64_tr_b16 v[168:169], v240 offset:4096\n"
    "ds_read_b64_tr_b16 v[170:171], v241 offset:4352\n"
    "ds_read_b64_tr_b16 v[172:173], v242 offset:4608\n"
    "ds_read_b64_tr_b16 v[174:175], v243 offset:4864\n"
    "ds_read_b32 v236, v253 offset:0\n"
    "ds_read_b32 v238, v253 offset:4\n"
    "ds_read_b128 v[160:163], v234 offset:6144\n"
    "ds_read_b128 v[164:167], v235 offset:6144\n"
    "v_pk_fma_f32 v[144:145], v[144:145], s[70:71], v[96:97]\n"
    "v_pk_fma_f32 v[146:147], v[146:147], s[70:71], v[98:99]\n"
    "v_pk_add_f32 v[230:231], v[144:145], v[146:147]\n"
    "v_pk_mul_f32 v[232:233], v[144:145], v[146:147]\n"
    "v_pk_fma_f32 v[148:149], v[148:149], s[70:71], v[100:101]\n"
    "v_pk_fma_f32 v[230:231], v[230:231], v[148:149], v[232:233]\n"
    "v_pk_mul_f32 v[232:233], v[232:233], v[148:149]\n"
    "v_pk_fma_f32 v[150:151], v[150:151], s[70:71], v[102:103]\n"
    "v_pk_fma_f32 v[230:231], v[230:231], v[150:151], v[232:233]\n"
    "v_pk_mul_f32 v[232:233], v[232:233], v[150:151]\n"
    "v_pk_fma_f32 v[152:153], v[152:153], s[70:71], v[104:105]\n"
    "v_pk_fma_f32 v[230:231], v[230:231], v[152:153], v[232:233]\n"
    "v_pk_mul_f32 v[232:233], v[232:233], v[152:153]\n"
    "v_pk_fma_f32 v[154:155], v[154:155], s[70:71], v[106:107]\n"
    "v_pk_fma_f32 v[230:231], v[230:231], v[154:155], v[232:233]\n"
    "v_pk_mul_f32 v[232:233], v[232:233], v[154:155]\n"
    "v_pk_fma_f32 v[156:157], v[156:157], s[70:71], v[108:109]\n"
    "v_pk_fma_f32 v[230:231], v[230:231], v[156:157], v[232:233]\n"
    "v_pk_mul_f32 v[232:233], v[232:233], v[156:157]\n"
    "v_pk_fma_f32 v[158:159], v[158:159], s[70:71], v[110:111]\n"
    "v_pk_fma_f32 v[230:231], v[230:231], v[158:159], v[232:233]\n"
    "v_pk_mul_f32 v[232:233], v[232:233], v[158:159]\n"
    "v_rcp_f32 v232, v232\n"
    "v_rcp_f32 v233, v233\n"
    "v_fma_f32 v230, v230, v232, v247\n"
    "v_fmac_f32 v230, v231, v233\n"
    "v_cvt_pk_bf16_f32 v230, v230, v230\n"
    "s_add_u32 s67, s67, 64\n"
    "s_waitcnt lgkmcnt(0)\n"
    "ds_write_b16 v245, v230\n"
    "ds_read_b64 v[228:229], v246\n"
    "s_cmp_le_u32 s34, s76\n"
    "s_cbranch_scc1 .Ldin_G1_wait\n"
    ".Ldin_G1_go:\n"
    "s_add_u32 s76, s76, s72\n"
    ".Ldin_G1_dma:\n"
    "buffer_load_dwordx4 v[236:237], s[40:43], 0 idxen offen offset:0 nt lds\n"
    "buffer_load_dwordx4 v[238:239], s[40:43], 0 idxen offen offset:1024 nt lds\n"
    ".Ldin_G1_nd:\n"
    "s_cmp_eq_u32 s67, s82\n"
    "s_cbranch_scc1 .Ldin_L2\n"
    "s_branch .Ldin_G2\n"
    ".Ldin_B1_wait:\n"
    "s_cmp_lt_i32 s59, s57\n"
    "s_cbranch_scc1 .Ldin_B1_w2\n"
    "s_sub_u32 s94, s67, s65\n"
    "s_cmp_gt_u32 s94, 256\n"
    "s_cbranch_scc1 .Ldin_B1_dma\n"
    "s_cmp_eq_u32 s94, 64\n"
    "s_waitcnt vmcnt(2)\n"
    "s_cbranch_scc1 .Ldin_B1_nd\n"
    "s_waitcnt vmcnt(0)\n"
    "s_branch .Ldin_B1_nd\n"
    ".Ldin_B1_slp:\n"
    "s_sleep 1\n"
    ".Ldin_B1_w2:\n"
    "s_memrealtime s[34:35]\n"
    "s_waitcnt lgkmcnt(0)\n"
    "s_sub_u32 s94, s34, s76\n"
    "s_cmp_lt_i32 s94, 0\n"
    "s_cbranch_scc1 .Ldin_B1_slp\n"
    "s_branch .Ldin_B1_go\n"
    ".Ldin_G2_wait:\n"
    "s_cmp_lt_i32 s59, s57\n"
    "s_cbranch_scc1 .Ldin_G2_w2\n"
    "s_sub_u32 s94, s67, s65\n"
    "s_cmp_gt_u32 s94, 256\n"
    "s_cbranch_scc1 .Ldin_G2_dma\n"
    "s_cmp_eq_u32 s94, 64\n"
    "s_waitcnt vmcnt(2)\n"
    "s_cbranch_scc1 .Ldin_G2_nd\n"
    "s_waitcnt vmcnt(0)\n"
    "s_branch .Ldin_G2_nd\n"
    ".Ldin_G2_slp:\n"
    "s_sleep 1\n"
    ".Ldin_G2_w2:\n"
    "s_memrealtime s[34:35]\n"
    "s_waitcnt lgkmcnt(0)\n"
    "s_sub_u32 s94, s34, s76\n"
    "s_cmp_lt_i32 s94, 0\n"
    "s_cbranch_scc1 .Ldin_G2_slp\n"
    "s_branch .Ldin_G2_go\n"
    ".Ldin_G3_wait:\n"
    "s_cmp_lt_i32 s59, s57\n"
    "s_cbranch_scc1 .Ldin_G3_w2\n"
    "s_sub_u32 s94, s67, s65\n"
    "s_cmp_gt_u32 s94, 256\n"
    "s_cbranch_scc1 .Ldin_G3_dma\n"
    "s_cmp_eq_u32 s94, 64\n"
    "s_waitcnt vmcnt(2)\n"
    "s_cbranch_scc1 .Ldin_G3_nd\n"
    "s_waitcnt vmcnt(0)\n"
    "s_branch .Ldin_G3_nd\n"
    ".Ldin_G3_slp:\n"
    "s_sleep 1\n"
    ".Ldin_G3_w2:\n"
    "s_memrealtime s[34:35]\n"
    "s_waitcnt lgkmcnt(0)\n"
    "s_sub_u32 s94, s34, s76\n"
    "s_cmp_lt_i32 s94, 0\n"
    "s_cbranch_scc1 .Ldin_G3_slp\n"
    "s_branch .Ldin_G3_go\n"
    ".Ldin_G0_wait:\n"
    "s_cmp_lt_i32 s59, s57\n"
    "s_cbranch_scc1 .Ldin_G0_w2\n"
    "s_sub_u32 s94, s67, s65\n"
    "s_cmp_gt_u32 s94, 256\n"
    "s_cbranch_scc1 .Ldin_G0_dma\n"
    "s_cmp_eq_u32 s94, 64\n"
    "s_waitcnt vmcnt(2)\n"
    "s_cbranch_scc1 .Ldin_G0_nd\n"
    "s_waitcnt vmcnt(0)\n"
    "s_branch .Ldin_G0_nd\n"
    ".Ldin_G0_slp:\n"
    "s_sleep 1\n"
    ".Ldin_G0_w2:\n"
    "s_memrealtime s[34:35]\n"
    "s_waitcnt lgkmcnt(0)\n"
    "s_sub_u32 s94, s34, s76\n"
    "s_cmp_lt_i32 s94, 0\n"
    "s_cbranch_scc1 .Ldin_G0_slp\n"
    "s_branch .Ldin_G0_go\n"
    ".Ldin_G1_wait:\n"
    "s_cmp_lt_i32 s59, s57\n"
    "s_cbranch_scc1 .Ldin_G1_w2\n"
    "s_sub_u32 s94, s67, s65\n"
    "s_cmp_gt_u32 s94, 256\n"
    "s_cbranch_scc1 .Ldin_G1_dma\n"
    "s_cmp_eq_u32 s94, 64\n"
    "s_waitcnt vmcnt(2)\n"
    "s_cbranch_scc1 .Ldin_G1_nd\n"
    "s_waitcnt vmcnt(0)\n"
    "s_branch .Ldin_G1_nd\n"
    ".Ldin_G1_slp:\n"
    "s_sleep 1\n"
    ".Ldin_G1_w2:\n"
    "s_memrealtime s[34:35]\n"
    "s_waitcnt lgkmcnt(0)\n"
    "s_sub_u32 s94, s34, s76\n"
    "s_cmp_lt_i32 s94, 0\n"
    "s_cbranch_scc1 .Ldin_G1_slp\n"
    "s_branch .Ldin_G1_go\n"
    ".Ldin_odd:\n"
    "s_cmp_lt_i32 s58, s57\n"
    "s_cbranch_scc1 .Ldin_A0s\n"
    "s_waitcnt vmcnt(0)\n"
    "s_branch .Ldin_exit\n"
    ".p2align 5\n"
    ".Ldin_y2:\n"
    "s_waitcnt vmcnt(8)\n"
    "v_lshlrev_b32 v56, 16, v8\n"
    "v_lshlrev_b32 v60, 16, v9\n"
    "v_lshlrev_b32 v64, 16, v10\n"
    "v_lshlrev_b32_e64 v68, 16, v11\n"
    "v_bitop3_b32 v56, v56, s84, v7 bitop3:0x6c\n"
    "v_bitop3_b32 v58, v8, s84, v7 bitop3:0x6c\n"
    "v_bitop3_b32 v60, v60, s84, v7 bitop3:0x6c\n"
    "v_bitop3_b32 v62, v9, s84, v7 bitop3:0x6c\n"
    "v_bitop3_b32 v64, v64, s84, v7 bitop3:0x6c\n"
    "v_bitop3_b32 v66, v10, s84, v7 bitop3:0x6c\n"
    "v_bitop3_b32 v68, v68, s84, v7 bitop3:0x6c\n"
    "v_bitop3_b32 v70, v11, s84, v7 bitop3:0x6c\n"
    "s_waitcnt vmcnt(6)\n"
    "buffer_load_dwordx4 v[32:35], v27, s[44:47], s39 offen nt\n"
    "s_add_u32 s39, s39, s17\n"
    "v_lshlrev_b32 v112, 16, v128\n"
    "v_and_b32 v114, s37, v128\n"
    "v_lshlrev_b32 v116, 16, v129\n"
    "v_and_b32 v118, s37, v129\n"
    "v_lshlrev_b32 v120, 16, v130\n"
    "v_and_b32 v122, s37, v130\n"
    "v_lshlrev_b32 v124, 16, v131\n"
    "v_and_b32 v126, s37, v131\n"
    "buffer_load_dwordx4 v[36:39], v27, s[44:47], s39 offen nt\n"
    "s_add_u32 s39, s39, s17\n"
    "v_lshlrev_b32 v113, 16, v132\n"
    "v_and_b32 v115, s37, v132\n"
    "v_lshlrev_b32 v117, 16, v133\n"
    "v_and_b32 v119, s37, v133\n"
    "v_lshlrev_b32 v121, 16, v134\n"
    "v_and_b32 v123, s37, v134\n"
    "v_lshlrev_b32 v125, 16, v135\n"
    "v_and_b32_e64 v127, s37, v135\n"
    "v_pk_mul_f32 v[112:113], v[112:113], v[56:57] op_sel_hi:[1,0]\n"
    "v_pk_mul_f32 v[114:115], v[114:115], v[58:59] op_sel_hi:[1,0]\n"
    "v_pk_mul_f32 v[116:117], v[116:117], v[60:61] op_sel_hi:[1,0]\n"
    "v_pk_mul_f32 v[118:119], v[118:119], v[62:63] op_sel_hi:[1,0]\n"
    "v_pk_mul_f32 v[120:121], v[120:121], v[64:65] op_sel_hi:[1,0]\n"
    "v_pk_mul_f32 v[122:123], v[122:123], v[66:67] op_sel_hi:[1,0]\n"
    "v_pk_mul_f32 v[124:125], v[124:125], v[68:69] op_sel_hi:[1,0]\n"
    "v_pk_mul_f32 v[126:127], v[126:127], v[70:71] op_sel_hi:[1,0]\n"
    "s_waitcnt vmcnt(6)\n"
    "buffer_load_dwordx4 v[40:43], v27, s[44:47], s39 offen nt\n"
    "s_add_u32 s39, s39, s17\n"
    "v_lshlrev_b32 v168, 16, v136\n"
    "v_and_b32 v170, s37, v136\n"
    "v_lshlrev_b32 v172, 16, v137\n"
    "v_and_b32 v174, s37, v137\n"
    "v_lshlrev_b32 v176, 16, v138\n"
    "v_and_b32 v178, s37, v138\n"
    "v_lshlrev_b32 v180, 16, v139\n"
    "v_and_b32 v182, s37, v139\n"
    "buffer_load_dwordx4 v[44:47], v27, s[44:47], s39 offen nt\n"
    "s_add_u32 s39, s39, s17\n"
    "v_lshlrev_b32 v169, 16, v140\n"
    "v_and_b32 v171, s37, v140\n"
    "v_lshlrev_b32 v173, 16, v141\n"
    "v_and_b32 v175, s37, v141\n"
    "v_lshlrev_b32 v177, 16, v142\n"
    "v_and_b32 v179, s37, v142\n"
    "v_lshlrev_b32 v181, 16, v143\n"
    "v_and_b32_e64 v183, s37, v143\n"
    "v_pk_fma_f32 v[168:169], v[168:169], v[56:57], v[112:113] op_sel_hi:[1,0,1]\n"
    "v_pk_fma_f32 v[170:171], v[170:171], v[58:59], v[114:115] op_sel_hi:[1,0,1]\n"
    "ds_write2st64_b64 v12, v[168:169], v[170:171] offset0:0 offset1:8\n"
    "v_pk_fma_f32 v[172:173], v[172:173], v[60:61], v[116:117] op_sel_hi:[1,0,1]\n"
    "v_pk_fma_f32 v[174:175], v[174:175], v[62:63], v[118:119] op_sel_hi:[1,0,1]\n"
    "ds_write2st64_b64 v12, v[172:173], v[174:175] offset0:16 offset1:24\n"
    "v_pk_fma_f32 v[176:177], v[176:177], v[64:65], v[120:121] op_sel_hi:[1,0,1]\n"
    "v_pk_fma_f32 v[178:179], v[178:179], v[66:67], v[122:123] op_sel_hi:[1,0,1]\n"
    "ds_write2st64_b64 v13, v[176:177], v[178:179] offset0:0 offset1:8\n"
    "v_pk_fma_f32 v[180:181], v[180:181], v[68:69], v[124:125] op_sel_hi:[1,0,1]\n"
    "v_pk_fma_f32 v[182:183], v[182:183], v[70:71], v[126:127] op_sel_hi:[1,0,1]\n"
    "ds_write2st64_b64 v13, v[180:181], v[182:183] offset0:16 offset1:24\n"
    "s_waitcnt vmcnt(6)\n"
    "buffer_load_dwordx4 v[48:51], v27, s[44:47], s39 offen nt\n"
    "s_add_u32 s39, s39, s17\n"
    "v_lshlrev_b32 v80, 16, v144\n"
    "v_and_b32 v82, s37, v144\n"
    "v_lshlrev_b32 v84, 16, v145\n"
    "v_and_b32 v86, s37, v145\n"
    "v_lshlrev_b32 v88, 16, v146\n"
    "v_and_b32 v90, s37, v146\n"
    "v_lshlrev_b32 v92, 16, v147\n"
    "v_and_b32 v94, s37, v147\n"
    "buffer_load_dwordx4 v[52:55], v27, s[44:47], s39 offen nt\n"
    "v_lshlrev_b32 v81, 16, v148\n"
    "v_and_b32 v83, s37, v148\n"
    "v_lshlrev_b32 v85, 16, v149\n"
    "v_and_b32 v87, s37, v149\n"
    "v_lshlrev_b32 v89, 16, v150\n"
    "v_and_b32 v91, s37, v150\n"
    "v_lshlrev_b32 v93, 16, v151\n"
    "v_and_b32 v95, s37, v151\n"
    "v_pk_mul_f32 v[80:81], v[80:81], v[56:57] op_sel_hi:[1,0]\n"
    "v_cvt_pk_bf16_f32 v80, v80, v81\n"
    "v_pk_mul_f32 v[82:83], v[82:83], v[58:59] op_sel_hi:[1,0]\n"
    "v_cvt_pk_bf16_f32 v82, v82, v83\n"
    "ds_write2st64_b32 v14, v80, v82 offset0:0 offset1:8\n"
    "v_pk_mul_f32 v[84:85], v[84:85], v[60:61] op_sel_hi:[1,0]\n"
    "v_cvt_pk_bf16_f32 v84, v84, v85\n"
    "v_pk_mul_f32 v[86:87], v[86:87], v[62:63] op_sel_hi:[1,0]\n"
    "v_cvt_pk_bf16_f32 v86, v86, v87\n"
    "ds_write2st64_b32 v14, v84, v86 offset0:16 offset1:24\n"
    "v_pk_mul_f32 v[88:89], v[88:89], v[64:65] op_sel_hi:[1,0]\n"
    "v_cvt_pk_bf16_f32 v88, v88, v89\n"
    "v_pk_mul_f32 v[90:91], v[90:91], v[66:67] op_sel_hi:[1,0]\n"
    "v_cvt_pk_bf16_f32 v90, v90, v91\n"
    "ds_write2st64_b32 v15, v88, v90 offset0:0 offset1:8\n"
    "v_pk_mul_f32 v[92:93], v[92:93], v[68:69] op_sel_hi:[1,0]\n"
    "v_cvt_pk_bf16_f32 v92, v92, v93\n"
    "v_pk_mul_f32 v[94:95], v[94:95], v[70:71] op_sel_hi:[1,0]\n"
    "v_cvt_pk_bf16_f32 v94, v94, v95\n"
    "ds_write2st64_b32 v15, v92, v94 offset0:16 offset1:24\n"
    "s_waitcnt vmcnt(6)\n"
    "v_lshlrev_b32 v184, 16, v152\n"
    "v_and_b32 v186, s37, v152\n"
    "v_lshlrev_b32 v188, 16, v153\n"
    "v_and_b32 v190, s37, v153\n"
    "v_lshlrev_b32 v192, 16, v154\n"
    "v_and_b32 v194, s37, v154\n"
    "v_lshlrev_b32 v196, 16, v155\n"
    "v_and_b32 v198, s37, v155\n"
    "v_lshlrev_b32 v185, 16, v156\n"
    "v_and_b32 v187, s37, v156\n"
    "v_lshlrev_b32 v189, 16, v157\n"
    "v_and_b32 v191, s37, v157\n"
    "v_lshlrev_b32 v193, 16, v158\n"
    "v_and_b32 v195, s37, v158\n"
    "v_lshlrev_b32 v197, 16, v159\n"
    "v_and_b32_e64 v199, s37, v159\n"
    "s_waitcnt lgkmcnt(0)\n"
    "s_barrier\n"
    "ds_read_b128 v[128:131], v97 offset:0\n"
    "ds_read_b128 v[132:135], v97 offset:16\n"
    "ds_read_b128 v[28:31], v99 offset:35200\n"
    "v_pk_fma_f32 v[184:185], v[184:185], v[56:57], v[112:113] op_sel_hi:[1,0,1] neg_lo:[0,0,1] neg_hi:[0,0,1]\n"
    "v_cvt_pk_bf16_f32 v184, v184, v185\n"
    "v_pk_fma_f32 v[186:187], v[186:187], v[58:59], v[114:115] op_sel_hi:[1,0,1] neg_lo:[0,0,1] neg_hi:[0,0,1]\n"
    "v_cvt_pk_bf16_f32 v186, v186, v187\n"
    "ds_write2st64_b32 v14, v184, v186 offset0:32 offset1:40\n"
    "v_pk_fma_f32 v[188:189], v[188:189], v[60:61], v[116:117] op_sel_hi:[1,0,1] neg_lo:[0,0,1] neg_hi:[0,0,1]\n"
    "v_cvt_pk_bf16_f32 v188, v188, v189\n"
    "v_pk_fma_f32 v[190:191], v[190:191], v[62:63], v[118:119] op_sel_hi:[1,0,1] neg_lo:[0,0,1] neg_hi:[0,0,1]\n"
    "v_cvt_pk_bf16_f32 v190, v190, v191\n"
    "ds_write2st64_b32 v14, v188, v190 offset0:48 offset1:56\n"
    "v_pk_fma_f32 v[192:193], v[192:193], v[64:65], v[120:121] op_sel_hi:[1,0,1] neg_lo:[0,0,1] neg_hi:[0,0,1]\n"
    "v_cvt_pk_bf16_f32 v192, v192, v193\n"
    "v_pk_fma_f32 v[194:195], v[194:195], v[66:67], v[122:123] op_sel_hi:[1,0,1] neg_lo:[0,0,1] neg_hi:[0,0,1]\n"
    "v_cvt_pk_bf16_f32 v194, v194, v195\n"
    "ds_write2st64_b32 v15, v192, v194 offset0:32 offset1:40\n"
    "v_pk_fma_f32 v[196:197], v[196:197], v[68:69], v[124:125] op_sel_hi:[1,0,1] neg_lo:[0,0,1] neg_hi:[0,0,1]\n"
    "v_cvt_pk_bf16_f32 v196, v196, v197\n"
    "v_pk_fma_f32 v[198:199], v[198:199], v[70:71], v[126:127] op_sel_hi:[1,0,1] neg_lo:[0,0,1] neg_hi:[0,0,1]\n"
    "v_cvt_pk_bf16_f32 v198, v198, v199\n"
    "ds_write2st64_b32 v15, v196, v198 offset0:48 offset1:56\n"
    "s_mul_i32 s36, s61, s16\n"
    "s_add_u32 s94, s36, s16\n"
    "s_cmp_lt_u32 s61, s14\n"
    "s_cselect_b32 s46, s94, 0\n"
    "buffer_load_dwordx2 v[228:229], v105, s[44:47], s36 offen nt\n"
    "s_lshl_b32 s94, s61, 6\n"
    "s_add_u32 s94, s3, s94\n"
    "s_cmp_lt_u32 s61, s14\n"
    "s_cselect_b32 s58, s94, s57\n"
    "s_add_u32 s95, s61, s15\n"
    "s_add_u32 s36, s36, s17\n"
    "s_lshl_b32 s94, s95, 6\n"
    "s_add_u32 s94, s3, s94\n"
    "s_add_u32 s96, s36, s16\n"
    "s_add_u32 s94, s94, 8\n"
    "s_cmp_lt_u32 s95, s14\n"
    "s_cselect_b32 s46, s96, 0\n"
    "s_cselect_b32 s59, s94, s57\n"
    "buffer_load_short_d16_hi v224, v249, s[44:47], s36 offen offset:0 nt\n"
    "buffer_load_short_d16_hi v225, v249, s[44:47], s36 offen offset:128 nt\n"
    "buffer_load_short_d16_hi v226, v249, s[44:47], s36 offen offset:256 nt\n"
    "buffer_load_short_d16_hi v227, v249, s[44:47], s36 offen offset:384 nt\n"
    "s_mul_i32 s46, s14, s16\n"
    "s_waitcnt lgkmcnt(0)\n"
    "s_barrier\n"
    "ds_read_b128 v[8:11], v98 offset:0\n"
    "ds_read_b128 v[12:15], v98 offset:1024\n"
    "s_waitcnt lgkmcnt(1)\n"
    "v_mfma_f32_16x16x32_bf16 v[112:115], v[8:11], v[128:131], v[28:31]\n"
    "s_waitcnt lgkmcnt(0)\n"
    "v_mfma_f32_16x16x32_bf16 v[112:115], v[12:15], v[132:135], v[112:115]\n"
    "s_nop 7\n"
    "ds_write_b128 v100, v[112:115] offset:17408\n"
    "s_memtime s[88:89]\n"
    "s_memrealtime s[90:91]\n"
    "s_waitcnt lgkmcnt(0)\n"
    "s_barrier\n"
    "s_sub_u32 s88, s88, s20\n"
    "s_sub_u32 s90, s90, s76\n"
    "s_lshl3_add_u32 s88, s88, 5360\n"
    "s_mul_i32 s90, s90, 181\n"
    "s_cmp_lt_u32 s88, s90\n"
    "s_cselect_b32 s7, 1, 0\n"
    "s_cbranch_scc1 .Ldin_xo\n"
    "s_setprio 0\n"
    ".Ldin_xo:\n"
    "s_mov_b64 exec, 0\n"
    "s_bitcmp1_b32 s3, 1\n"
    "s_cbranch_scc1 .Ldin_t2_hi\n"
    "s_bitcmp1_b32 s3, 0\n"
    "s_cbranch_scc0 .Ldin_stub_8\n"
    "s_branch .Ldin_stub_9\n"
    ".Ldin_t2_hi:\n"
    "s_bitcmp1_b32 s3, 0\n"
    "s_cbranch_scc0 .Ldin_stub_10\n"
    "s_branch .Ldin_stub_11\n"
    ".Ldin_t2_ret:\n"
    "s_mov_b64 exec, -1\n"
    "v_and_b32 v184, 24, v249\n"
    "v_add_u32 v184, v184, v252\n"
    "ds_read_b64 v[184:185], v184\n"
    "ds_read_b32 v247, v250 offset:35072\n"
    "ds_read_b32 v232, v250 offset:35076\n"
    "s_and_b32 s84, s12, 7\n"
    "v_sub_u32 v4, s16, v26\n"
    "s_lshl_b32 s84, -1, s84\n"
    "v_cmp_gt_i32 vcc, 16, v4\n"
    "s_not_b32 s84, s84\n"
    "v_mov_b32 v6, s84\n"
    "s_add_u32 s85, s66, 480\n"
    "v_cndmask_b32 v6, -1, v6, vcc\n"
    "v_and_b32 v4, 32, v251\n"
    "v_add3_u32 v7, v4, v251, s85\n"
    "v_mov_b32 v5, 0\n"
    "s_mov_b32 s4, 0x10001\n"
    "s_mov_b32 s5, 0xc080c080\n"
    "s_waitcnt lgkmcnt(0)\n"
    "v_readfirstlane_b32 s69, v232\n"
    "v_and_b32 v184, v254, v184\n"
    "v_and_b32_e64 v185, v255, v185\n"
    "s_waitcnt vmcnt(4)\n"
    "v_cmp_ne_u16_e64 s[84:85], 0, v228\n"
    "v_cmp_lt_u32_e64 s[86:87], s38, v228\n"
    "v_cmp_ne_u16_e64 s[88:89], 0, v229\n"
    "v_cmp_lt_u32_e64 s[90:91], s38, v229\n"
    "v_mbcnt_lo_u32_b32 v220, s84, v220\n"
    "s_bcnt1_i32_b64 s92, s[84:85]\n"
    "v_mbcnt_hi_u32_b32 v220, s85, v220\n"
    "s_lshl2_add_u32 s92, s92, s67\n"
    "v_mbcnt_lo_u32_b32 v220, s86, v220\n"
    "s_bcnt1_i32_b64 s93, s[86:87]\n"
    "v_mbcnt_hi_u32_b32 v220, s87, v220\n"
    "s_lshl2_add_u32 s93, s93, s92\n"
    "v_mbcnt_lo_u32_b32 v220, s88, v220\n"
    "s_bcnt1_i32_b64 s94, s[88:89]\n"
    "v_mbcnt_hi_u32_b32 v220, s89, v220\n"
    "s_lshl2_add_u32 s94, s94, s93\n"
    "v_mbcnt_lo_u32_b32 v220, s90, v220\n"
    "s_bcnt1_i32_b64 s95, s[90:91]\n"
    "v_mbcnt_hi_u32_b32 v220, s91, v220\n"
    "s_lshl2_add_u32 s95, s95, s94\n"
    "v_mov_b32_e64 v21, -1\n"
    "v_lshl_add_u32 v20, v249, 1, s95\n"
    "v_addc_co_u32_e64 v221, vcc, v220, 0, s[84:85]\n"
    "v_addc_co_u32_e64 v222, vcc, v221, 0, s[86:87]\n"
    "v_addc_co_u32_e64 v223, vcc, v222, 0, s[88:89]\n"
    "ds_write2_b32 v20, v21, v21 offset1:64\n"
    "v_lshlrev_b32 v220, 2, v220\n"
    "v_lshlrev_b32 v221, 2, v221\n"
    "v_lshlrev_b32 v222, 2, v222\n"
    "v_lshlrev_b32_e64 v223, 2, v223\n"
    "s_mov_b64 exec, s[84:85]\n"
    "ds_write_b32 v220, v216\n"
    "s_mov_b64 exec, s[86:87]\n"
    "ds_write_b32 v221, v217\n"
    "s_mov_b64 exec, s[88:89]\n"
    "ds_write_b32 v222, v218\n"
    "s_mov_b64 exec, s[90:91]\n"
    "ds_write_b32 v223, v219\n"
    "s_mov_b64 exec, -1\n"
    "ds_read_b32 v200, v253 offset:0\n"
    "ds_read_b32 v202, v253 offset:4\n"
    "ds_read_b32 v204, v253 offset:64\n"
    "ds_read_b32 v206, v253 offset:68\n"
    "ds_read_b32 v208, v253 offset:128\n"
    "ds_read_b32 v210, v253 offset:132\n"
    "ds_read_b32 v212, v253 offset:192\n"
    "ds_read_b32 v214, v253 offset:196\n"
    "s_add_u32 m0, s68, 0\n"
    "s_waitcnt lgkmcnt(6)\n"
    "buffer_load_dwordx4 v[200:201], s[40:43], 0 idxen offen offset:0 nt lds\n"
    "buffer_load_dwordx4 v[202:203], s[40:43], 0 idxen offen offset:1024 nt lds\n"
    "s_add_u32 s95, s95, 60\n"
    "s_add_u32 s94, s67, 384\n"
    "s_andn2_b32 s95, s95, 63\n"
    "s_max_u32 s80, s95, s94\n"
    "s_add_u32 m0, s68, 2048\n"
    "s_waitcnt lgkmcnt(0)\n"
    "buffer_load_dwordx4 v[204:205], s[40:43], 0 idxen offen offset:0 nt lds\n"
    "s_mov_b32 s94, 0x10001\n"
    "s_mov_b32 s95, 0xc080c080\n"
    "v_pk_min_u16 v22, v228, s94\n"
    "v_pk_min_u16 v23, v229, s94\n"
    "v_pk_mad_u16 v228, v22, s95, v228\n"
    "v_pk_mad_u16 v229, v23, s95, v229\n"
    "s_sub_u32 s87, s80, s81\n"
    "v_or_b32 v228, v228, v229\n"
    "s_lshr_b32 s87, s87, 6\n"
    "s_lshl2_add_u32 s86, s3, 68224\n"
    "v_mov_b32 v106, s86\n"
    "v_mov_b32 v108, s66\n"
    "v_mov_b32 v109, s58\n"
    "v_mov_b32 v110, s59\n"
    "s_waitcnt vmcnt(3)\n"
    "v_cmp_neq_f32_e64 s[20:21], 0, v224\n"
    "v_cmp_neq_f32_e64 s[22:23], 0, v225\n"
    "v_cmp_neq_f32_e64 s[24:25], 0, v226\n"
    "v_cmp_neq_f32_e64 s[26:27], 0, v227\n"
    "v_fma_f32 v224, v224, v224, -v224\n"
    "v_fma_f32 v225, v225, v225, -v225\n"
    "v_fma_f32 v226, v226, v226, -v226\n"
    "v_fma_f32 v227, v227, v227, -v227\n"
    "s_bcnt1_i32_b64 s88, s[20:21]\n"
    "s_bcnt1_i32_b64 s89, s[22:23]\n"
    "buffer_load_dwordx4 v[206:207], s[40:43], 0 idxen offen offset:1024 nt lds\n"
    "s_bcnt1_i32_b64 s90, s[24:25]\n"
    "s_bcnt1_i32_b64 s91, s[26:27]\n"
    "v_or3_b32 v228, v228, v224, v225\n"
    "s_add_u32 s88, s88, s89\n"
    "v_or3_b32 v228, v228, v226, v227\n"
    "s_add_u32 s90, s90, s91\n"
    "v_cmp_ne_u32_e64 s[84:85], 0, v228\n"
    "s_add_u32 s88, s88, s90\n"
    "s_pack_ll_b32_b16 s88, s87, s88\n"
    "s_mov_b64 exec, 1\n"
    "v_mov_b32_e64 v107, s88\n"
    "ds_write2_b32 v108, v109, v110 offset1:2\n"
    "s_cmp_lg_u64 s[84:85], 0\n"
    "ds_write_b32 v106, v107\n"
    "s_cselect_b32 s91, 2.0, 0\n"
    "s_mov_b64 exec, -1\n"
    "s_or_b32 s58, s58, s91\n"
    "s_or_b32 s59, s59, s91\n"
    "v_pk_min_u16 v0, v32, s4\n"
    "v_pk_min_u16 v1, v33, s4\n"
    "v_pk_min_u16 v2, v34, s4\n"
    "v_pk_min_u16 v3, v35, s4\n"
    "v_pk_mad_u16 v32, v0, s5, v32\n"
    "v_pk_mad_u16 v33, v1, s5, v33\n"
    "s_add_u32 m0, s68, 4096\n"
    "s_nop 0\n"
    "buffer_load_dwordx4 v[208:209], s[40:43], 0 idxen offen offset:0 nt lds\n"
    "v_pk_mad_u16 v34, v2, s5, v34\n"
    "v_pk_mad_u16 v35, v3, s5, v35\n"
    "v_lshl_or_b32 v0, v1, 2, v0\n"
    "v_lshl_or_b32 v0, v2, 4, v0\n"
    "v_lshl_or_b32 v0, v3, 6, v0\n"
    "v_or3_b32 v5, v5, v32, v33\n"
    "v_lshrrev_b32 v1, 15, v0\n"
    "v_or3_b32 v5, v5, v34, v35\n"
    "v_bitop3_b32 v16, v1, v6, v0 bitop3:0xc8\n"
    "ds_write_b8 v7, v16 offset:0\n"
    "v_pk_min_u16 v0, v36, s4\n"
    "v_pk_min_u16 v1, v37, s4\n"
    "v_pk_min_u16 v2, v38, s4\n"
    "v_pk_min_u16 v3, v39, s4\n"
    "v_pk_mad_u16 v36, v0, s5, v36\n"
    "v_pk_mad_u16 v37, v1, s5, v37\n"
    "v_pk_mad_u16 v38, v2, s5, v38\n"
    "v_pk_mad_u16 v39, v3, s5, v39\n"
    "v_lshl_or_b32 v0, v1, 2, v0\n"
    "v_lshl_or_b32 v0, v2, 4, v0\n"
    "v_lshl_or_b32 v0, v3, 6, v0\n"
    "v_or3_b32 v5, v5, v36, v37\n"
    "v_lshrrev_b32_e64 v1, 15, v0\n"
    "v_or3_b32 v5, v5, v38, v39\n"
    "v_bitop3_b32 v17, v1, v6, v0 bitop3:0xc8\n"
    "buffer_load_dwordx4 v[210:211], s[40:43], 0 idxen offen offset:1024 nt lds\n"
    "ds_write_b8 v7, v17 offset:512\n"
    "v_pk_min_u16 v0, v40, s4\n"
    "v_pk_min_u16 v1, v41, s4\n"
    "v_pk_min_u16 v2, v42, s4\n"
    "v_pk_min_u16 v3, v43, s4\n"
    "v_pk_mad_u16 v40, v0, s5, v40\n"
    "v_pk_mad_u16 v41, v1, s5, v41\n"
    "v_pk_mad_u16 v42, v2, s5, v42\n"
    "v_pk_mad_u16 v43, v3, s5, v43\n"
    "v_lshl_or_b32 v0, v1, 2, v0\n"
    "v_lshl_or_b32 v0, v2, 4, v0\n"
    "v_lshl_or_b32 v0, v3, 6, v0\n"
    "v_or3_b32 v5, v5, v40, v41\n"
    "v_lshrrev_b32_e64 v1, 15, v0\n"
    "v_or3_b32 v5, v5, v42, v43\n"
    "v_bitop3_b32 v18, v1, v6, v0 bitop3:0xc8\n"
    "ds_write_b8 v7, v18 offset:1024\n"
    "v_pk_min_u16 v0, v44, s4\n"
    "v_pk_min_u16 v1, v45, s4\n"
    "v_pk_min_u16 v2, v46, s4\n"
    "v_pk_min_u16 v3, v47, s4\n"
    "v_pk_mad_u16 v44, v0, s5, v44\n"
    "v_pk_mad_u16 v45, v1, s5, v45\n"
    "v_pk_mad_u16 v46, v2, s5, v46\n"
    "v_pk_mad_u16 v47, v3, s5, v47\n"
    "s_add_u32 m0, s68, 6144\n"
    "s_nop 0\n"
    "buffer_load_dwordx4 v[212:213], s[40:43], 0 idxen offen offset:0 nt lds\n"
    "v_lshl_or_b32 v0, v1, 2, v0\n"
    "v_lshl_or_b32 v0, v2, 4, v0\n"
    "v_lshl_or_b32 v0, v3, 6, v0\n"
    "v_or3_b32 v5, v5, v44, v45\n"
    "v_lshrrev_b32 v1, 15, v0\n"
    "v_or3_b32 v5, v5, v46, v47\n"
    "v_bitop3_b32 v19, v1, v6, v0 bitop3:0xc8\n"
    "ds_write_b8 v7, v19 offset:1536\n"
    "v_pk_min_u16 v0, v48, s4\n"
    "v_pk_min_u16 v1, v49, s4\n"
    "v_pk_min_u16 v2, v50, s4\n"
    "v_pk_min_u16 v3, v51, s4\n"
    "v_pk_mad_u16 v48, v0, s5, v48\n"
    "v_pk_mad_u16 v49, v1, s5, v49\n"
    "v_pk_mad_u16 v50, v2, s5, v50\n"
    "v_pk_mad_u16 v51, v3, s5, v51\n"
    "v_lshl_or_b32 v0, v1, 2, v0\n"
    "v_lshl_or_b32 v0, v2, 4, v0\n"
    "v_lshl_or_b32 v0, v3, 6, v0\n"
    "v_or3_b32 v5, v5, v48, v49\n"
    "v_lshrrev_b32_e64 v1, 15, v0\n"
    "v_or3_b32 v5, v5, v50, v51\n"
    "v_bitop3_b32 v16, v1, v6, v0 bitop3:0xc8\n"
    "ds_write_b8 v7, v16 offset:2048\n"
    "v_pk_min_u16 v0, v52, s4\n"
    "buffer_load_dwordx4 v[214:215], s[40:43], 0 idxen offen offset:1024 nt lds\n"
    "v_pk_min_u16 v1, v53, s4\n"
    "v_pk_min_u16 v2, v54, s4\n"
    "v_pk_min_u16 v3, v55, s4\n"
    "v_pk_mad_u16 v52, v0, s5, v52\n"
    "v_pk_mad_u16 v53, v1, s5, v53\n"
    "v_pk_mad_u16 v54, v2, s5, v54\n"
    "v_pk_mad_u16 v55, v3, s5, v55\n"
    "v_lshl_or_b32 v0, v1, 2, v0\n"
    "v_lshl_or_b32 v0, v2, 4, v0\n"
    "v_lshl_or_b32 v0, v3, 6, v0\n"
    "v_or3_b32 v5, v5, v52, v53\n"
    "v_lshrrev_b32_e64 v1, 15, v0\n"
    "v_or3_b32 v5, v5, v54, v55\n"
    "v_bitop3_b32 v17, v1, v6, v0 bitop3:0xc8\n"
    "ds_write_b8 v7, v17 offset:2560\n"
    "ds_read_b128 v[128:131], v97 offset:4352\n"
    "ds_read_b128 v[132:135], v97 offset:4368\n"
    "ds_read_b128 v[144:147], v97 offset:8704\n"
    "ds_read_b128 v[148:151], v97 offset:8720\n"
    "ds_read_b128 v[160:163], v97 offset:13056\n"
    "ds_read_b128 v[164:167], v97 offset:13072\n"
    "s_waitcnt lgkmcnt(4)\n"
    "v_mfma_f32_16x16x32_bf16 v[116:119], v[8:11], v[128:131], v[28:31]\n"
    "v_mfma_f32_16x16x32_bf16 v[116:119], v[12:15], v[132:135], v[116:119]\n"
    "s_waitcnt lgkmcnt(2)\n"
    "v_mfma_f32_16x16x32_bf16 v[120:123], v[8:11], v[144:147], v[28:31]\n"
    "v_mfma_f32_16x16x32_bf16 v[120:123], v[12:15], v[148:151], v[120:123]\n"
    "s_waitcnt lgkmcnt(0)\n"
    "v_mfma_f32_16x16x32_bf16 v[124:127], v[8:11], v[160:163], v[28:31]\n"
    "v_mfma_f32_16x16x32_bf16 v[124:127], v[12:15], v[164:167], v[124:127]\n"
    "s_nop 7\n"
    "ds_write_b128 v100, v[116:119] offset:21760\n"
    "ds_write_b128 v100, v[120:123] offset:26112\n"
    "ds_write_b128 v100, v[124:127] offset:30464\n"
    "v_cmp_ne_u32 vcc, 0, v5\n"
    "s_cmp_lg_u64 vcc, 0\n"
    "s_cselect_b32 s94, 0x101, 1\n"
    "s_mov_b64 exec, 1\n"
    "v_mov_b32 v10, s94\n"
    "ds_add_rtn_u32 v10, v250, v10 offset:35840\n"
    "s_mov_b64 exec, -1\n"
    "s_waitcnt lgkmcnt(0)\n"
    "v_readfirstlane_b32 s95, v10\n"
    "s_add_u32 s96, s95, s94\n"
    "s_bitcmp1_b32 s96, 2\n"
    "s_cbranch_scc1 .Ldin_sched\n"
    ".Ldin_ygate:\n"
    ".Ldin_yentry:\n"
    "s_mov_b64 vcc, -1\n"
    "s_branch .Ldin_entry\n"
    ".Ldin_swait:\n"
    "s_sleep 1\n"
    "ds_read_b32 v10, v250 offset:35840\n"
    "s_waitcnt lgkmcnt(0)\n"
    "v_readfirstlane_b32 s96, v10\n"
    ".Ldin_sched:\n"
    "s_setprio 1\n"
    "v_lshrrev_b32 v2, 3, v251\n"
    "v_and_b32 v3, 7, v251\n"
    "v_lshlrev_b32 v6, 3, v3\n"
    "v_add_u32 v7, 16, v251\n"
    "s_mov_b32 s84, 0x10b80\n"
    "v_lshl_add_u32 v12, v7, 6, s84\n"
    "ds_read_b128 v[32:35], v12\n"
    "ds_read_b128 v[36:39], v12 offset:16\n"
    "s_mov_b32 s85, 0x10a80\n"
    "v_lshl_add_u32 v4, v2, 2, s85\n"
    "v_lshl_add_u32 v5, v3, 2, s85\n"
    "ds_read_u16 v17, v4 offset:2\n"
    "ds_read_u16 v4, v4\n"
    "ds_read_u16 v16, v5 offset:2\n"
    "ds_read_u16 v5, v5\n"
    "s_sub_u32 s85, s61, s3\n"
    "s_lshl1_add_u32 s85, s15, s85\n"
    "v_mul_lo_u32 v8, v2, s15\n"
    "v_add3_u32 v8, v8, v3, s85\n"
    "s_cmp_ge_u32 s96, 256\n"
    "v_cmp_gt_u32_e64 s[86:87], s14, v8\n"
    "s_cselect_b32 s85, 2.0, 0\n"
    "v_lshlrev_b32 v8, 6, v8\n"
    "s_and_b32 s87, s87, 0xffff\n"
    "v_or3_b32 v8, v8, v7, s85\n"
    "v_mov_b32 v12, s57\n"
    "v_cndmask_b32_e64 v8, v12, v8, s[86:87]\n"
    "v_cmp_eq_u32 vcc, s3, v2\n"
    "v_lshrrev_b32 v14, 2, v2\n"
    "v_mul_u32_u24 v14, 2, v14\n"
    "v_addc_co_u32_e64 v14, s[88:89], v14, 0, vcc\n"
    "v_addc_co_u32_e64 v14, s[88:89], v14, 0, vcc\n"
    "v_cmp_eq_u32 vcc, s3, v3\n"
    "v_lshrrev_b32 v15, 2, v3\n"
    "v_mul_u32_u24 v15, 2, v15\n"
    "v_addc_co_u32_e64 v15, s[88:89], v15, 0, vcc\n"
    "v_addc_co_u32_e64 v15, s[88:89], v15, 0, vcc\n"
    "s_waitcnt lgkmcnt(4)\n"
    "v_bcnt_u32_b32 v9, v32, 15\n"
    "v_bcnt_u32_b32 v9, v33, v9\n"
    "v_bcnt_u32_b32 v9, v34, v9\n"
    "v_bcnt_u32_b32 v9, v35, v9\n"
    "v_bcnt_u32_b32 v9, v36, v9\n"
    "v_bcnt_u32_b32 v9, v37, v9\n"
    "v_bcnt_u32_b32 v9, v38, v9\n"
    "v_bcnt_u32_b32 v9, v39, v9\n"
    "v_lshrrev_b32 v9, 4, v9\n"
    "v_max_u32 v9, 6, v9\n"
    "v_lshlrev_b32 v9, 3, v9\n"
    "v_cndmask_b32_e64 v9, 0, v9, s[86:87]\n"
    "v_sub_u32 v10, v3, v9\n"
    "s_waitcnt lgkmcnt(0)\n"
    "v_add_u32 v16, 15, v16\n"
    "v_lshrrev_b32 v16, 4, v16\n"
    "v_max_u32 v16, 6, v16\n"
    "v_add_u32 v17, 15, v17\n"
    "v_lshrrev_b32 v17, 4, v17\n"
    "v_max_u32 v17, 6, v17\n"
    "v_add3_u32 v4, v4, v14, v17\n"
    "v_add3_u32 v5, v5, v15, v16\n"
    "v_mov_b32_dpp v12, v10 row_half_mirror row_mask:0xf bank_mask:0xf\n"
    "v_lshrrev_b32 v14, 2, v2\n"
    "v_mul_u32_u24 v14, 3, v14\n"
    "v_xor_b32 v14, v14, v2\n"
    "v_lshl_or_b32 v4, v4, 3, v14\n"
    "v_lshrrev_b32_e64 v14, 2, v3\n"
    "v_mul_u32_u24 v14, 3, v14\n"
    "v_xor_b32 v14, v14, v3\n"
    "v_lshl_or_b32 v5, v5, 3, v14\n"
    "v_sub_u32_dpp v32, v10, v10 quad_perm:[1,0,3,2] row_mask:0xf bank_mask:0xf\n"
    "v_sub_u32_dpp v33, v10, v10 quad_perm:[2,3,0,1] row_mask:0xf bank_mask:0xf\n"
    "v_sub_u32_dpp v34, v10, v10 quad_perm:[3,2,1,0] row_mask:0xf bank_mask:0xf\n"
    "v_sub_u32_dpp v35, v12, v10 quad_perm:[0,1,2,3] row_mask:0xf bank_mask:0xf\n"
    "v_sub_u32_dpp v36, v12, v10 quad_perm:[1,0,3,2] row_mask:0xf bank_mask:0xf\n"
    "v_sub_u32_dpp v37, v12, v10 quad_perm:[2,3,0,1] row_mask:0xf bank_mask:0xf\n"
    "v_sub_u32_dpp v38, v12, v10 quad_perm:[3,2,1,0] row_mask:0xf bank_mask:0xf\n"
    "v_lshrrev_b32 v32, 31, v32\n"
    "v_lshrrev_b32 v33, 31, v33\n"
    "v_lshrrev_b32 v34, 31, v34\n"
    "v_lshrrev_b32 v35, 31, v35\n"
    "v_lshrrev_b32 v36, 31, v36\n"
    "v_lshrrev_b32 v37, 31, v37\n"
    "v_lshrrev_b32 v38, 31, v38\n"
    "v_add3_u32 v11, v32, v33, v244\n"
    "v_add3_u32 v11, v34, v35, v11\n"
    "v_add3_u32 v11, v36, v37, v11\n"
    "v_add_u32 v11, v38, v11\n"
    "v_add_u32 v12, s65, v11\n"
    "v_lshl_add_u32 v14, v11, 3, s65\n"
    "ds_write_b8 v12, v9\n"
    "ds_write_b32 v14, v8 offset:64\n"
    "v_mov_b32 v12, s65\n"
    "ds_read_b128 v[40:43], v12 offset:0\n"
    "ds_read_b128 v[44:47], v12 offset:16\n"
    "ds_read_b128 v[48:51], v12 offset:32\n"
    "v_lshlrev_b32 v16, 3, v16\n"
    "v_sub_u32 v72, v5, v16\n"
    "v_cmp_lt_i32 vcc, v5, v4\n"
    "v_mov_b32_e64 v73, v5\n"
    "s_waitcnt lgkmcnt(0)\n"
    "v_lshrrev_b64 v[20:21], v6, vcc\n"
    "v_lshrrev_b64 v[18:19], v244, vcc\n"
    "v_and_b32 v20, 0xff, v20\n"
    "v_and_b32 v18, 0xff, v18\n"
    "v_bcnt_u32_b32 v20, v20, 0\n"
    "v_bcnt_u32_b32 v18, v18, 0\n"
    "v_lshl_add_u32 v20, v20, 3, s65\n"
    "v_lshlrev_b32_e64 v18, 3, v18\n"
    "ds_read_b32 v56, v20 offset:64\n"
    "v_lshrrev_b64 v[22:23], v20, v[40:41]\n"
    "v_lshrrev_b64 v[18:19], v18, v[40:41]\n"
    "v_and_b32 v22, 0xff, v22\n"
    "v_and_b32 v18, 0xff, v18\n"
    "v_add_u32 v5, v5, v22\n"
    "v_add_u32 v4, v4, v18\n"
    "v_cmp_lt_i32 vcc, v5, v4\n"
    "v_mov_b32_e64 v74, v5\n"
    "s_nop 0\n"
    "v_lshrrev_b64 v[20:21], v6, vcc\n"
    "v_lshrrev_b64 v[18:19], v244, vcc\n"
    "v_and_b32 v20, 0xff, v20\n"
    "v_and_b32 v18, 0xff, v18\n"
    "v_bcnt_u32_b32 v20, v20, 0\n"
    "v_bcnt_u32_b32 v18, v18, 0\n"
    "v_lshl_add_u32 v20, v20, 3, s65\n"
    "v_lshlrev_b32_e64 v18, 3, v18\n"
    "ds_read_b32 v58, v20 offset:128\n"
    "v_lshrrev_b64 v[22:23], v20, v[42:43]\n"
    "v_lshrrev_b64 v[18:19], v18, v[42:43]\n"
    "v_and_b32 v22, 0xff, v22\n"
    "v_and_b32 v18, 0xff, v18\n"
    "v_add_u32 v5, v5, v22\n"
    "v_add_u32 v4, v4, v18\n"
    "v_cmp_lt_i32 vcc, v5, v4\n"
    "v_mov_b32_e64 v75, v5\n"
    "s_nop 0\n"
    "v_lshrrev_b64 v[20:21], v6, vcc\n"
    "v_lshrrev_b64 v[18:19], v244, vcc\n"
    "v_and_b32 v20, 0xff, v20\n"
    "v_and_b32 v18, 0xff, v18\n"
    "v_bcnt_u32_b32 v20, v20, 0\n"
    "v_bcnt_u32_b32 v18, v18, 0\n"
    "v_lshl_add_u32 v20, v20, 3, s65\n"
    "v_lshlrev_b32_e64 v18, 3, v18\n"
    "ds_read_b32 v60, v20 offset:192\n"
    "v_lshrrev_b64 v[22:23], v20, v[44:45]\n"
    "v_lshrrev_b64 v[18:19], v18, v[44:45]\n"
    "v_and_b32 v22, 0xff, v22\n"
    "v_and_b32 v18, 0xff, v18\n"
    "v_add_u32 v5, v5, v22\n"
    "v_add_u32 v4, v4, v18\n"
    "v_cmp_lt_i32 vcc, v5, v4\n"
    "v_mov_b32_e64 v76, v5\n"
    "s_nop 0\n"
    "v_lshrrev_b64 v[20:21], v6, vcc\n"
    "v_lshrrev_b64 v[18:19], v244, vcc\n"
    "v_and_b32 v20, 0xff, v20\n"
    "v_and_b32 v18, 0xff, v18\n"
    "v_bcnt_u32_b32 v20, v20, 0\n"
    "v_bcnt_u32_b32 v18, v18, 0\n"
    "v_lshl_add_u32 v20, v20, 3, s65\n"
    "v_lshlrev_b32_e64 v18, 3, v18\n"
    "ds_read_b32 v62, v20 offset:256\n"
    "v_lshrrev_b64 v[22:23], v20, v[46:47]\n"
    "v_lshrrev_b64 v[18:19], v18, v[46:47]\n"
    "v_and_b32 v22, 0xff, v22\n"
    "v_and_b32 v18, 0xff, v18\n"
    "v_add_u32 v5, v5, v22\n"
    "v_add_u32 v4, v4, v18\n"
    "v_cmp_lt_i32 vcc, v5, v4\n"
    "v_mov_b32_e64 v77, v5\n"
    "s_nop 0\n"
    "v_lshrrev_b64 v[20:21], v6, vcc\n"
    "v_lshrrev_b64 v[18:19], v244, vcc\n"
    "v_and_b32 v20, 0xff, v20\n"
    "v_and_b32 v18, 0xff, v18\n"
    "v_bcnt_u32_b32 v20, v20, 0\n"
    "v_bcnt_u32_b32 v18, v18, 0\n"
    "v_lshl_add_u32 v20, v20, 3, s65\n"
    "v_lshlrev_b32_e64 v18, 3, v18\n"
    "ds_read_b32 v64, v20 offset:320\n"
    "v_lshrrev_b64 v[22:23], v20, v[48:49]\n"
    "v_lshrrev_b64 v[18:19], v18, v[48:49]\n"
    "v_and_b32 v22, 0xff, v22\n"
    "v_and_b32 v18, 0xff, v18\n"
    "v_add_u32 v5, v5, v22\n"
    "v_add_u32 v4, v4, v18\n"
    "v_cmp_lt_i32 vcc, v5, v4\n"
    "v_mov_b32_e64 v78, v5\n"
    "s_nop 0\n"
    "v_lshrrev_b64 v[20:21], v6, vcc\n"
    "v_and_b32 v20, 0xff, v20\n"
    "v_bcnt_u32_b32 v20, v20, 0\n"
    "v_lshl_add_u32 v20, v20, 3, s65\n"
    "ds_read_b32 v66, v20 offset:384\n"
    "v_lshrrev_b64 v[22:23], v20, v[50:51]\n"
    "v_and_b32 v22, 0xff, v22\n"
    "v_add_u32 v5, v5, v22\n"
    "s_add_u32 s85, s10, 230\n"
    "s_lshl_b32 s86, s72, 4\n"
    "v_add_u32_dpp v12, v5, v5 quad_perm:[1,0,3,2] row_mask:0xf bank_mask:0xf\n"
    "s_add_u32 s85, s85, s86\n"
    "s_nop 0\n"
    "v_add_u32_dpp v12, v12, v12 quad_perm:[2,3,0,1] row_mask:0xf bank_mask:0xf\n"
    "s_nop 1\n"
    "v_add_u32_dpp v12, v12, v12 row_half_mirror row_mask:0xf bank_mask:0xf\n"
    "v_add_u32 v12, 158, v12\n"
    "v_and_b32 v12, -256, v12\n"
    "v_lshrrev_b32 v12, 3, v12\n"
    "v_sub_u32 v12, v12, v5\n"
    "v_add_u32 v72, v72, v12\n"
    "v_mul_u32_u24 v72, s72, v72\n"
    "v_lshrrev_b32 v72, 3, v72\n"
    "s_add_u32 s85, s85, s73\n"
    "v_add_u32 v57, s85, v72\n"
    "v_add_u32 v73, v73, v12\n"
    "v_mul_u32_u24 v73, s72, v73\n"
    "v_lshrrev_b32 v73, 3, v73\n"
    "s_add_u32 s85, s85, s73\n"
    "v_add_u32 v59, s85, v73\n"
    "v_add_u32 v74, v74, v12\n"
    "v_mul_u32_u24 v74, s72, v74\n"
    "v_lshrrev_b32 v74, 3, v74\n"
    "s_add_u32 s85, s85, s73\n"
    "v_add_u32 v61, s85, v74\n"
    "v_add_u32 v75, v75, v12\n"
    "v_mul_u32_u24 v75, s72, v75\n"
    "v_lshrrev_b32 v75, 3, v75\n"
    "s_add_u32 s85, s85, s73\n"
    "v_add_u32 v63, s85, v75\n"
    "v_add_u32 v76, v76, v12\n"
    "v_mul_u32_u24 v76, s72, v76\n"
    "v_lshrrev_b32 v76, 3, v76\n"
    "s_add_u32 s85, s85, s73\n"
    "v_add_u32 v65, s85, v76\n"
    "v_add_u32 v77, v77, v12\n"
    "v_mul_u32_u24 v77, s72, v77\n"
    "v_lshrrev_b32 v77, 3, v77\n"
    "s_add_u32 s85, s85, s73\n"
    "v_add_u32 v67, s85, v77\n"
    "v_lshl_add_u32 v12, v3, 7, s84\n"
    "s_mov_b64 exec, 0xff\n"
    "s_waitcnt lgkmcnt(0)\n"
    "ds_write_b128 v12, v[56:59] offset:48\n"
    "ds_write_b128 v12, v[60:63] offset:64\n"
    "ds_write_b128 v12, v[64:67] offset:80\n"
    "s_mov_b64 exec, -1\n"
    "s_branch .Ldin_ygate\n"
    ".p2align 5\n"
    ".Ldin_L2:\n"
    "s_setprio 0\n"
    "s_cmp_ge_i32 s59, s57\n"
    "s_cbranch_scc1 .Ldin_E0\n"
    "s_add_u32 s76, s76, s73\n"
    "v_mov_b32_e64 v253, s63\n"
    "ds_read_b64 v[232:233], v253\n"
    "v_mfma_f32_4x4x4_16b_bf16 v[144:147], v[184:185], v[32:33], v[64:67] cbsz:2 abid:0\n"
    "ds_read_b128 v[112:115], v252 offset:17408\n"
    "v_mfma_f32_4x4x4_16b_bf16 v[148:151], v[184:185], v[34:35], v[68:71] cbsz:2 abid:1\n"
    "ds_read_b128 v[116:119], v252 offset:17424\n"
    "v_mfma_f32_4x4x4_16b_bf16 v[152:155], v[184:185], v[36:37], v[72:75] cbsz:2 abid:2\n"
    "ds_read_b128 v[120:123], v252 offset:17536\n"
    "ds_read_b128 v[124:127], v252 offset:17552\n"
    "v_cvt_pk_bf16_f32 v0, v144, v145\n"
    "v_cvt_pk_bf16_f32 v1, v146, v147\n"
    "v_mfma_f32_4x4x4_16b_bf16 v[156:159], v[184:185], v[38:39], v[76:79] cbsz:2 abid:3\n"
    "v_cvt_pk_bf16_f32 v2, v148, v149\n"
    "v_cvt_pk_bf16_f32 v3, v150, v151\n"
    "v_mfma_f32_4x4x4_16b_bf16 v[144:147], v[184:185], v[40:41], v[80:83] cbsz:2 abid:0\n"
    "v_cvt_pk_bf16_f32 v4, v152, v153\n"
    "v_cvt_pk_bf16_f32 v5, v154, v155\n"
    "v_mfma_f32_4x4x4_16b_bf16 v[148:151], v[184:185], v[42:43], v[84:87] cbsz:2 abid:1\n"
    "v_cvt_pk_bf16_f32 v6, v156, v157\n"
    "v_cvt_pk_bf16_f32 v7, v158, v159\n"
    "v_mfma_f32_4x4x4_16b_bf16 v[152:155], v[184:185], v[44:45], v[88:91] cbsz:2 abid:2\n"
    "v_cvt_pk_bf16_f32 v8, v144, v145\n"
    "v_cvt_pk_bf16_f32 v9, v146, v147\n"
    "v_mfma_f32_4x4x4_16b_bf16 v[156:159], v[184:185], v[46:47], v[92:95] cbsz:2 abid:3\n"
    "v_cvt_pk_bf16_f32 v10, v148, v149\n"
    "v_cvt_pk_bf16_f32 v11, v150, v151\n"
    "v_mfma_f32_4x4x4_16b_bf16 v[144:147], v[184:185], v[48:49], v[192:195] cbsz:2 abid:0\n"
    "v_cvt_pk_bf16_f32 v12, v152, v153\n"
    "v_cvt_pk_bf16_f32 v13, v154, v155\n"
    "v_mfma_f32_4x4x4_16b_bf16 v[148:151], v[184:185], v[50:51], v[196:199] cbsz:2 abid:1\n"
    "v_cvt_pk_bf16_f32 v14, v156, v157\n"
    "v_cvt_pk_bf16_f32 v15, v158, v159\n"
    "v_mfma_f32_4x4x4_16b_bf16 v[152:155], v[184:185], v[52:53], v[200:203] cbsz:2 abid:2\n"
    "s_waitcnt lgkmcnt(0)\n"
    "v_readfirstlane_b32 s60, v232\n"
    "v_cvt_pk_bf16_f32 v16, v144, v145\n"
    "v_cvt_pk_bf16_f32 v17, v146, v147\n"
    "v_mfma_f32_4x4x4_16b_bf16 v[156:159], v[184:185], v[54:55], v[204:207] cbsz:2 abid:3\n"
    "s_cmp_eq_u32 s60, -1\n"
    "s_cbranch_scc1 .Ldin_L2_poll\n"
    ".Ldin_L2_pr:\n"
    "v_readfirstlane_b32 s95, v233\n"
    "s_and_b32 s94, s60, 63\n"
    "s_lshl_b32 s94, s94, 6\n"
    "s_add_u32 s94, s94, 68480\n"
    "s_add_u32 s63, s63, 8\n"
    "v_lshl_add_u32 v252, v249, 1, s94\n"
    "ds_read_b32 v252, v252\n"
    "s_sub_u32 s95, s95, s76\n"
    "s_ashr_i32 s95, s95, 4\n"
    "s_min_i32 s95, s95, s74\n"
    "s_max_i32 s72, s95, s75\n"
    "v_cvt_pk_bf16_f32 v18, v148, v149\n"
    "v_cvt_pk_bf16_f32 v19, v150, v151\n"
    "v_mfma_f32_4x4x4_16b_bf16 v[144:147], v[184:185], v[56:57], v[208:211] cbsz:2 abid:0\n"
    "v_cvt_pk_bf16_f32 v20, v152, v153\n"
    "v_cvt_pk_bf16_f32 v21, v154, v155\n"
    "v_mfma_f32_4x4x4_16b_bf16 v[148:151], v[184:185], v[58:59], v[212:215] cbsz:2 abid:1\n"
    "v_cvt_pk_bf16_f32 v22, v156, v157\n"
    "v_cvt_pk_bf16_f32 v23, v158, v159\n"
    "v_mfma_f32_4x4x4_16b_bf16 v[152:155], v[184:185], v[60:61], v[216:219] cbsz:2 abid:2\n"
    "v_cvt_pk_bf16_f32 v24, v144, v145\n"
    "v_cvt_pk_bf16_f32 v25, v146, v147\n"
    "v_mfma_f32_4x4x4_16b_bf16 v[156:159], v[184:185], v[62:63], v[220:223] cbsz:2 abid:3\n"
    "v_cvt_pk_bf16_f32 v26, v148, v149\n"
    "v_cvt_pk_bf16_f32 v27, v150, v151\n"
    "v_cvt_pk_bf16_f32 v28, v152, v153\n"
    "v_cvt_pk_bf16_f32 v29, v154, v155\n"
    "v_exp_f32 v136, v136 clamp\n"
    "v_cvt_pk_bf16_f32 v30, v156, v157\n"
    "v_cvt_pk_bf16_f32 v31, v158, v159\n"
    "v_mfma_f32_16x16x32_bf16 v[144:147], v[0:3], v[160:163], v[112:115]\n"
    "v_exp_f32 v128, v128 clamp\n"
    "v_mfma_f32_16x16x32_bf16 v[144:147], v[4:7], v[164:167], v[144:147]\n"
    "v_exp_f32 v129, v129 clamp\n"
    "v_mfma_f32_16x16x32_bf16 v[148:151], v[8:11], v[160:163], v[116:119]\n"
    "v_exp_f32 v130, v130 clamp\n"
    "v_mfma_f32_16x16x32_bf16 v[148:151], v[12:15], v[164:167], v[148:151]\n"
    "v_exp_f32 v131, v131 clamp\n"
    "v_mfma_f32_16x16x32_bf16 v[152:155], v[16:19], v[160:163], v[120:123]\n"
    "v_exp_f32 v132, v132 clamp\n"
    "v_mfma_f32_16x16x32_bf16 v[152:155], v[20:23], v[164:167], v[152:155]\n"
    "v_exp_f32 v133, v133 clamp\n"
    "v_mfma_f32_16x16x32_bf16 v[156:159], v[24:27], v[160:163], v[124:127]\n"
    "v_exp_f32 v134, v134 clamp\n"
    "v_mfma_f32_16x16x32_bf16 v[156:159], v[28:31], v[164:167], v[156:159]\n"
    "v_exp_f32 v135, v135 clamp\n"
    "s_add_u32 m0, s68, 6144\n"
    "v_exp_f32 v137, v137 clamp\n"
    "v_add_u32 v253, s67, v244\n"
    "v_mfma_f32_4x4x4_16b_bf16 v[224:227], v[228:229], v[176:177], v[224:227] cbsz:4 abid:0\n"
    "v_exp_f32 v138, v138 clamp\n"
    "v_exp_f32 v139, v139 clamp\n"
    "v_mfma_f32_4x4x4_16b_bf16 v[224:227], v[228:229], v[178:179], v[224:227] cbsz:4 abid:1\n"
    "v_exp_f32 v140, v140 clamp\n"
    "v_exp_f32 v141, v141 clamp\n"
    "v_mfma_f32_4x4x4_16b_bf16 v[224:227], v[228:229], v[180:181], v[224:227] cbsz:4 abid:2\n"
    "v_exp_f32 v142, v142 clamp\n"
    "v_exp_f32 v143, v143 clamp\n"
    "v_mfma_f32_4x4x4_16b_bf16 v[224:227], v[228:229], v[182:183], v[224:227] cbsz:4 abid:3\n"
    "s_waitcnt vmcnt(4)\n"
    "ds_read_b64_tr_b16 v[176:177], v240 offset:6144\n"
    "ds_read_b64_tr_b16 v[178:179], v241 offset:6400\n"
    "ds_read_b64_tr_b16 v[180:181], v242 offset:6656\n"
    "ds_read_b64_tr_b16 v[182:183], v243 offset:6912\n"
    "ds_read_b32 v236, v253 offset:0\n"
    "ds_read_b32 v238, v253 offset:4\n"
    "ds_read_b128 v[160:163], v234 offset:0\n"
    "ds_read_b128 v[164:167], v235 offset:0\n"
    "s_waitcnt lgkmcnt(8)\n"
    "s_nop 0\n"
    "v_readlane_b32 s20, v252, 0\n"
    "v_readlane_b32 s21, v252, 1\n"
    "v_readlane_b32 s22, v252, 2\n"
    "v_readlane_b32 s23, v252, 3\n"
    "v_readlane_b32 s24, v252, 4\n"
    "v_readlane_b32 s25, v252, 5\n"
    "v_readlane_b32 s26, v252, 6\n"
    "v_readlane_b32 s27, v252, 7\n"
    "v_pk_fma_f32 v[128:129], v[128:129], s[70:71], v[96:97]\n"
    "v_pk_fma_f32 v[130:131], v[130:131], s[70:71], v[98:99]\n"
    "v_pk_add_f32 v[230:231], v[128:129], v[130:131]\n"
    "v_pk_mul_f32 v[232:233], v[128:129], v[130:131]\n"
    "v_pk_fma_f32 v[132:133], v[132:133], s[70:71], v[100:101]\n"
    "v_pk_fma_f32 v[230:231], v[230:231], v[132:133], v[232:233]\n"
    "v_pk_mul_f32 v[232:233], v[232:233], v[132:133]\n"
    "v_pk_fma_f32 v[134:135], v[134:135], s[70:71], v[102:103]\n"
    "v_pk_fma_f32 v[230:231], v[230:231], v[134:135], v[232:233]\n"
    "v_pk_mul_f32 v[232:233], v[232:233], v[134:135]\n"
    "v_pk_fma_f32 v[136:137], v[136:137], s[70:71], v[104:105]\n"
    "v_pk_fma_f32 v[230:231], v[230:231], v[136:137], v[232:233]\n"
    "v_pk_mul_f32 v[232:233], v[232:233], v[136:137]\n"
    "v_pk_fma_f32 v[138:139], v[138:139], s[70:71], v[106:107]\n"
    "v_pk_fma_f32 v[230:231], v[230:231], v[138:139], v[232:233]\n"
    "v_pk_mul_f32 v[232:233], v[232:233], v[138:139]\n"
    "v_pk_fma_f32 v[140:141], v[140:141], s[70:71], v[108:109]\n"
    "v_pk_fma_f32 v[230:231], v[230:231], v[140:141], v[232:233]\n"
    "v_pk_mul_f32 v[232:233], v[232:233], v[140:141]\n"
    "v_pk_fma_f32 v[142:143], v[142:143], s[70:71], v[110:111]\n"
    "v_pk_fma_f32 v[230:231], v[230:231], v[142:143], v[232:233]\n"
    "v_pk_mul_f32 v[232:233], v[232:233], v[142:143]\n"
    "v_rcp_f32 v232, v232\n"
    "v_rcp_f32 v233, v233\n"
    "v_fma_f32 v230, v230, v232, v247\n"
    "v_fmac_f32 v230, v231, v233\n"
    "v_cvt_pk_bf16_f32 v230, v230, v230\n"
    "s_add_u32 s67, s67, 64\n"
    "s_waitcnt lgkmcnt(0)\n"
    "ds_write_b16 v245, v230\n"
    "ds_read_b64 v[228:229], v246\n"
    "s_lshl1_add_u32 s76, s72, s76\n"
    "buffer_load_dwordx4 v[236:237], s[40:43], 0 idxen offen offset:0 nt lds\n"
    "buffer_load_dwordx4 v[238:239], s[40:43], 0 idxen offen offset:1024 nt lds\n"
    "s_and_b32 s62, s58, 63\n"
    "s_mul_i32 s62, s62, 272\n"
    "s_mov_b32 s58, s59\n"
    "s_mov_b32 s59, s60\n"
    "s_mov_b32 s81, s65\n"
    "s_xor_b32 s65, s65, s64\n"
    "s_mov_b32 s80, s83\n"
    "s_add_u32 s82, s65, 256\n"
    "s_cmp_lt_i32 s59, s57\n"
    "s_cselect_b32 s76, s76, -1\n"
    "s_bitcmp1_b32 s58, 30\n"
    "s_cbranch_scc1 .Ldin_A3s\n"
    ".Ldin_A3:\n"
    "v_mfma_f32_16x16x32_bf16 v[128:131], v[0:3], v[160:163], v[112:115]\n"
    "v_exp_f32 v144, v144 clamp\n"
    "v_mfma_f32_16x16x32_bf16 v[128:131], v[4:7], v[164:167], v[128:131]\n"
    "v_exp_f32 v145, v145 clamp\n"
    "v_mfma_f32_16x16x32_bf16 v[132:135], v[8:11], v[160:163], v[116:119]\n"
    "v_exp_f32 v146, v146 clamp\n"
    "v_mfma_f32_16x16x32_bf16 v[132:135], v[12:15], v[164:167], v[132:135]\n"
    "v_exp_f32 v147, v147 clamp\n"
    "v_mfma_f32_16x16x32_bf16 v[136:139], v[16:19], v[160:163], v[120:123]\n"
    "v_exp_f32 v148, v148 clamp\n"
    "v_mfma_f32_16x16x32_bf16 v[136:139], v[20:23], v[164:167], v[136:139]\n"
    "v_exp_f32 v149, v149 clamp\n"
    "v_mfma_f32_16x16x32_bf16 v[140:143], v[24:27], v[160:163], v[124:127]\n"
    "v_exp_f32 v150, v150 clamp\n"
    "v_mfma_f32_16x16x32_bf16 v[140:143], v[28:31], v[164:167], v[140:143]\n"
    "v_exp_f32 v151, v151 clamp\n"
    "v_exp_f32 v152, v152 clamp\n"
    "s_mov_b32 m0, s62\n"
    "v_exp_f32 v153, v153 clamp\n"
    "v_add_u32 v253, s67, v244\n"
    "s_waitcnt lgkmcnt(0)\n"
    "v_mfma_f32_4x4x4_16b_bf16 v[224:227], v[228:229], v[168:169], v[224:227] cbsz:4 abid:0\n"
    "v_exp_f32 v154, v154 clamp\n"
    "v_exp_f32 v155, v155 clamp\n"
    "v_mfma_f32_4x4x4_16b_bf16 v[224:227], v[228:229], v[170:171], v[224:227] cbsz:4 abid:1\n"
    "v_exp_f32 v156, v156 clamp\n"
    "v_exp_f32 v157, v157 clamp\n"
    "v_mfma_f32_4x4x4_16b_bf16 v[224:227], v[228:229], v[172:173], v[224:227] cbsz:4 abid:2\n"
    "v_exp_f32 v158, v158 clamp\n"
    "v_exp_f32 v159, v159 clamp\n"
    "v_mfma_f32_4x4x4_16b_bf16 v[224:227], v[228:229], v[174:175], v[224:227] cbsz:4 abid:3\n"
    "s_waitcnt vmcnt(4)\n"
    "ds_read_b64_tr_b16 v[168:169], v240 offset:0\n"
    "ds_read_b64_tr_b16 v[170:171], v241 offset:256\n"
    "ds_read_b64_tr_b16 v[172:173], v242 offset:512\n"
    "ds_read_b64_tr_b16 v[174:175], v243 offset:768\n"
    "ds_read_b32 v236, v253 offset:0\n"
    "ds_read_b32 v238, v253 offset:4\n"
    "ds_read_b128 v[160:163], v234 offset:2048\n"
    "ds_read_b128 v[164:167], v235 offset:2048\n"
    "s_cbranch_vccz .Ldin_A3_np\n"
    "s_setprio 1\n"
    ".Ldin_A3_np:\n"
    "v_pk_fma_f32 v[144:145], v[144:145], s[70:71], v[96:97]\n"
    "v_pk_fma_f32 v[146:147], v[146:147], s[70:71], v[98:99]\n"
    "v_pk_add_f32 v[190:191], v[224:225], v[226:227]\n"
    "v_add_f32 v190, v190, v191\n"
    "ds_write_addtid_b32 v190 offset:32768\n"
    "s_add_u32 m0, s68, 0\n"
    "s_bfe_u32 s94, s59, 0x180006\n"
    "s_mul_i32 s86, s94, s56\n"
    "v_mbcnt_lo_u32_b32 v184, s20, 0\n"
    "v_mbcnt_lo_u32_b32 v185, s22, 0\n"
    "v_mbcnt_lo_u32_b32 v186, s24, 0\n"
    "v_mbcnt_lo_u32_b32 v187, s26, 0\n"
    "s_bcnt1_i32_b64 s90, s[20:21]\n"
    "s_bcnt1_i32_b64 s91, s[22:23]\n"
    "s_bcnt1_i32_b64 s92, s[24:25]\n"
    "s_bcnt1_i32_b64 s93, s[26:27]\n"
    "v_mbcnt_hi_u32_b32 v184, s21, v184\n"
    "v_mbcnt_hi_u32_b32 v185, s23, v185\n"
    "v_mbcnt_hi_u32_b32 v186, s25, v186\n"
    "v_mbcnt_hi_u32_b32 v187, s27, v187\n"
    "s_lshl2_add_u32 s90, s90, s65\n"
    "s_lshl2_add_u32 s91, s91, s90\n"
    "s_lshl2_add_u32 s92, s92, s91\n"
    "s_lshl2_add_u32 s93, s93, s92\n"
    "v_add_u32 v188, s86, v251\n"
    "v_pk_add_f32 v[230:231], v[144:145], v[146:147]\n"
    "v_pk_mul_f32 v[232:233], v[144:145], v[146:147]\n"
    "v_pk_fma_f32 v[148:149], v[148:149], s[70:71], v[100:101]\n"
    "v_pk_fma_f32 v[230:231], v[230:231], v[148:149], v[232:233]\n"
    "v_pk_mul_f32 v[232:233], v[232:233], v[148:149]\n"
    "v_lshl_add_u32 v184, v184, 2, s65\n"
    "v_lshl_add_u32 v185, v185, 2, s90\n"
    "v_lshl_add_u32 v186, v186, 2, s91\n"
    "v_lshl_add_u32 v187, v187, 2, s92\n"
    "v_mov_b32 v191, -1\n"
    "v_lshl_add_u32 v190, v249, 1, s93\n"
    "ds_write2_b32 v190, v191, v191 offset1:64\n"
    "s_mov_b64 exec, s[20:21]\n"
    "ds_write_b32 v184, v188\n"
    "s_mov_b64 exec, s[22:23]\n"
    "v_add_u32 v189, 64, v188\n"
    "ds_write_b32 v185, v189\n"
    "s_mov_b64 exec, s[24:25]\n"
    "v_add_u32 v190, 128, v188\n"
    "ds_write_b32 v186, v190\n"
    "s_mov_b64 exec, s[26:27]\n"
    "v_add_u32 v189, 192, v188\n"
    "ds_write_b32 v187, v189\n"
    "s_mov_b64 exec, -1\n"
    "s_add_u32 s95, s93, 60\n"
    "s_add_u32 s94, s82, 128\n"
    "s_andn2_b32 s95, s95, 63\n"
    "s_max_u32 s83, s95, s94\n"
    "v_pk_fma_f32 v[150:151], v[150:151], s[70:71], v[102:103]\n"
    "v_pk_fma_f32 v[230:231], v[230:231], v[150:151], v[232:233]\n"
    "v_pk_mul_f32 v[232:233], v[232:233], v[150:151]\n"
    "v_pk_fma_f32 v[152:153], v[152:153], s[70:71], v[104:105]\n"
    "v_pk_fma_f32 v[230:231], v[230:231], v[152:153], v[232:233]\n"
    "v_pk_mul_f32 v[232:233], v[232:233], v[152:153]\n"
    "v_pk_fma_f32 v[154:155], v[154:155], s[70:71], v[106:107]\n"
    "v_pk_fma_f32 v[230:231], v[230:231], v[154:155], v[232:233]\n"
    "v_pk_mul_f32 v[232:233], v[232:233], v[154:155]\n"
    "v_pk_fma_f32 v[156:157], v[156:157], s[70:71], v[108:109]\n"
    "v_pk_fma_f32 v[230:231], v[230:231], v[156:157], v[232:233]\n"
    "v_pk_mul_f32 v[232:233], v[232:233], v[156:157]\n"
    "v_pk_fma_f32 v[158:159], v[158:159], s[70:71], v[110:111]\n"
    "v_pk_fma_f32 v[230:231], v[230:231], v[158:159], v[232:233]\n"
    "v_pk_mul_f32 v[232:233], v[232:233], v[158:159]\n"
    "v_rcp_f32 v232, v232\n"
    "v_rcp_f32 v233, v233\n"
    "v_fma_f32 v230, v230, v232, v247\n"
    "v_fmac_f32 v230, v231, v233\n"
    "v_cvt_pk_bf16_f32 v230, v230, v230\n"
    "s_add_u32 s67, s67, 64\n"
    "s_waitcnt lgkmcnt(0)\n"
    "ds_write_b16 v245, v230\n"
    "ds_read_b64 v[228:229], v246\n"
    "buffer_load_dwordx4 v[236:237], s[40:43], 0 idxen offen offset:0 nt lds\n"
    "buffer_load_dwordx4 v[238:239], s[40:43], 0 idxen offen offset:1024 nt lds\n"
    ".Ldin_B0:\n"
    "v_mfma_f32_16x16x32_bf16 v[144:147], v[0:3], v[160:163], v[112:115]\n"
    "v_exp_f32 v128, v128 clamp\n"
    "v_mfma_f32_16x16x32_bf16 v[144:147], v[4:7], v[164:167], v[144:147]\n"
    "v_exp_f32 v129, v129 clamp\n"
    "v_mfma_f32_16x16x32_bf16 v[148:151], v[8:11], v[160:163], v[116:119]\n"
    "v_exp_f32 v130, v130 clamp\n"
    "v_mfma_f32_16x16x32_bf16 v[148:151], v[12:15], v[164:167], v[148:151]\n"
    "v_exp_f32 v131, v131 clamp\n"
    "v_mfma_f32_16x16x32_bf16 v[152:155], v[16:19], v[160:163], v[120:123]\n"
    "v_exp_f32 v132, v132 clamp\n"
    "v_mfma_f32_16x16x32_bf16 v[152:155], v[20:23], v[164:167], v[152:155]\n"
    "v_exp_f32 v133, v133 clamp\n"
    "v_mfma_f32_16x16x32_bf16 v[156:159], v[24:27], v[160:163], v[124:127]\n"
    "v_exp_f32 v134, v134 clamp\n"
    "v_mfma_f32_16x16x32_bf16 v[156:159], v[28:31], v[164:167], v[156:159]\n"
    "v_exp_f32 v135, v135 clamp\n"
    "v_exp_f32 v136, v136 clamp\n"
    "s_cmp_eq_u32 s67, s80\n"
    "s_cselect_b32 s67, s65, s67\n"
    "s_and_b32 s94, s59, 63\n"
    "s_mul_i32 s94, s94, 272\n"
    "v_add_u32 v252, s94, v250\n"
    "s_add_u32 m0, s68, 2048\n"
    "v_exp_f32 v137, v137 clamp\n"
    "v_add_u32 v253, s67, v244\n"
    "s_waitcnt lgkmcnt(0)\n"
    "v_mfma_f32_4x4x4_16b_bf16 v[224:227], v[228:229], v[176:177], 0 cbsz:4 abid:0\n"
    "v_exp_f32 v138, v138 clamp\n"
    "v_exp_f32 v139, v139 clamp\n"
    "v_mfma_f32_4x4x4_16b_bf16 v[224:227], v[228:229], v[178:179], v[224:227] cbsz:4 abid:1\n"
    "v_exp_f32 v140, v140 clamp\n"
    "v_exp_f32 v141, v141 clamp\n"
    "v_mfma_f32_4x4x4_16b_bf16 v[224:227], v[228:229], v[180:181], v[224:227] cbsz:4 abid:2\n"
    "v_exp_f32 v142, v142 clamp\n"
    "v_exp_f32 v143, v143 clamp\n"
    "v_mfma_f32_4x4x4_16b_bf16 v[224:227], v[228:229], v[182:183], v[224:227] cbsz:4 abid:3\n"
    "s_waitcnt vmcnt(4)\n"
    "s_memrealtime s[34:35]\n"
    "ds_read_b64_tr_b16 v[176:177], v240 offset:2048\n"
    "ds_read_b64_tr_b16 v[178:179], v241 offset:2304\n"
    "ds_read_b64_tr_b16 v[180:181], v242 offset:2560\n"
    "ds_read_b64_tr_b16 v[182:183], v243 offset:2816\n"
    "ds_read_b32 v236, v253 offset:0\n"
    "ds_read_b32 v238, v253 offset:4\n"
    "ds_read_b128 v[160:163], v234 offset:4096\n"
    "ds_read_b128 v[164:167], v235 offset:4096\n"
    "v_add_u32_e64 v184, v248, v252\n"
    "ds_read_b64 v[184:185], v184\n"
    "v_pk_fma_f32 v[128:129], v[128:129], s[70:71], v[96:97]\n"
    "v_pk_fma_f32 v[130:131], v[130:131], s[70:71], v[98:99]\n"
    "v_pk_add_f32 v[230:231], v[128:129], v[130:131]\n"
    "v_pk_mul_f32 v[232:233], v[128:129], v[130:131]\n"
    "v_pk_fma_f32 v[132:133], v[132:133], s[70:71], v[100:101]\n"
    "v_pk_fma_f32 v[230:231], v[230:231], v[132:133], v[232:233]\n"
    "v_pk_mul_f32 v[232:233], v[232:233], v[132:133]\n"
    "v_pk_fma_f32 v[134:135], v[134:135], s[70:71], v[102:103]\n"
    "v_pk_fma_f32 v[230:231], v[230:231], v[134:135], v[232:233]\n"
    "v_pk_mul_f32 v[232:233], v[232:233], v[134:135]\n"
    "v_pk_fma_f32 v[136:137], v[136:137], s[70:71], v[104:105]\n"
    "v_pk_fma_f32 v[230:231], v[230:231], v[136:137], v[232:233]\n"
    "v_pk_mul_f32 v[232:233], v[232:233], v[136:137]\n"
    "v_pk_fma_f32 v[138:139], v[138:139], s[70:71], v[106:107]\n"
    "v_pk_fma_f32 v[230:231], v[230:231], v[138:139], v[232:233]\n"
    "v_pk_mul_f32 v[232:233], v[232:233], v[138:139]\n"
    "v_pk_fma_f32 v[140:141], v[140:141], s[70:71], v[108:109]\n"
    "v_pk_fma_f32 v[230:231], v[230:231], v[140:141], v[232:233]\n"
    "v_pk_mul_f32 v[232:233], v[232:233], v[140:141]\n"
    "v_pk_fma_f32 v[142:143], v[142:143], s[70:71], v[110:111]\n"
    "v_pk_fma_f32 v[230:231], v[230:231], v[142:143], v[232:233]\n"
    "v_pk_mul_f32 v[232:233], v[232:233], v[142:143]\n"
    "v_rcp_f32 v232, v232\n"
    "v_rcp_f32 v233, v233\n"
    "v_fma_f32 v230, v230, v232, v247\n"
    "v_fmac_f32 v230, v231, v233\n"
    "v_cvt_pk_bf16_f32 v230, v230, v230\n"
    "s_add_u32 s67, s67, 64\n"
    "s_waitcnt lgkmcnt(0)\n"
    "ds_write_b16 v245, v230\n"
    "ds_read_b64 v[228:229], v246\n"
    "v_and_b32 v184, v254, v184\n"
    "v_and_b32 v185, v255, v185\n"
    "s_cmp_le_u32 s34, s76\n"
    "s_cbranch_scc1 .Ldin_B0_wait\n"
    ".Ldin_B0_go:\n"
    "s_sub_u32 s94, s34, 60\n"
    "s_max_u32 s76, s76, s94\n"
    "s_add_u32 s76, s76, s72\n"
    ".Ldin_B0_dma:\n"
    "buffer_load_dwordx4 v[236:237], s[40:43], 0 idxen offen offset:0 nt lds\n"
    "buffer_load_dwordx4 v[238:239], s[40:43], 0 idxen offen offset:1024 nt lds\n"
    ".Ldin_B0_nd:\n"
    "s_branch .Ldin_G1\n"
    ".Ldin_L2_poll:\n"
    "s_sleep 1\n"
    "ds_read_b64 v[232:233], v253\n"
    "s_waitcnt lgkmcnt(0)\n"
    "v_readfirstlane_b32 s60, v232\n"
    "s_cmp_eq_u32 s60, -1\n"
    "s_cbranch_scc1 .Ldin_L2_poll\n"
    "s_branch .Ldin_L2_pr\n"
    ".Ldin_B0_wait:\n"
    "s_cmp_lt_i32 s59, s57\n"
    "s_cbranch_scc1 .Ldin_B0_w2\n"
    "s_sub_u32 s94, s67, s65\n"
    "s_cmp_gt_u32 s94, 256\n"
    "s_cbranch_scc1 .Ldin_B0_dma\n"
    "s_cmp_eq_u32 s94, 64\n"
    "s_waitcnt vmcnt(2)\n"
    "s_cbranch_scc1 .Ldin_B0_nd\n"
    "s_waitcnt vmcnt(0)\n"
    "s_branch .Ldin_B0_nd\n"
    ".Ldin_B0_slp:\n"
    "s_sleep 1\n"
    ".Ldin_B0_w2:\n"
    "s_memrealtime s[34:35]\n"
    "s_waitcnt lgkmcnt(0)\n"
    "s_sub_u32 s94, s34, s76\n"
    "s_cmp_lt_i32 s94, 0\n"
    "s_cbranch_scc1 .Ldin_B0_slp\n"
    "s_branch .Ldin_B0_go\n"
    ".p2align 5\n"
    ".Ldin_L1:\n"
    "s_setprio 0\n"
    "s_cmp_ge_i32 s59, s57\n"
    "s_cbranch_scc1 .Ldin_E1\n"
    "s_add_u32 s76, s76, s73\n"
    "v_mov_b32_e64 v253, s63\n"
    "ds_read_b64 v[232:233], v253\n"
    "v_mfma_f32_4x4x4_16b_bf16 v[128:131], v[184:185], v[32:33], v[64:67] cbsz:2 abid:0\n"
    "ds_read_b128 v[112:115], v252 offset:17408\n"
    "v_mfma_f32_4x4x4_16b_bf16 v[132:135], v[184:185], v[34:35], v[68:71] cbsz:2 abid:1\n"
    "ds_read_b128 v[116:119], v252 offset:17424\n"
    "v_mfma_f32_4x4x4_16b_bf16 v[136:139], v[184:185], v[36:37], v[72:75] cbsz:2 abid:2\n"
    "ds_read_b128 v[120:123], v252 offset:17536\n"
    "ds_read_b128 v[124:127], v252 offset:17552\n"
    "v_cvt_pk_bf16_f32 v0, v128, v129\n"
    "v_cvt_pk_bf16_f32 v1, v130, v131\n"
    "v_mfma_f32_4x4x4_16b_bf16 v[140:143], v[184:185], v[38:39], v[76:79] cbsz:2 abid:3\n"
    "v_cvt_pk_bf16_f32 v2, v132, v133\n"
    "v_cvt_pk_bf16_f32 v3, v134, v135\n"
    "v_mfma_f32_4x4x4_16b_bf16 v[128:131], v[184:185], v[40:41], v[80:83] cbsz:2 abid:0\n"
    "v_cvt_pk_bf16_f32 v4, v136, v137\n"
    "v_cvt_pk_bf16_f32 v5, v138, v139\n"
    "v_mfma_f32_4x4x4_16b_bf16 v[132:135], v[184:185], v[42:43], v[84:87] cbsz:2 abid:1\n"
    "v_cvt_pk_bf16_f32 v6, v140, v141\n"
    "v_cvt_pk_bf16_f32 v7, v142, v143\n"
    "v_mfma_f32_4x4x4_16b_bf16 v[136:139], v[184:185], v[44:45], v[88:91] cbsz:2 abid:2\n"
    "v_cvt_pk_bf16_f32 v8, v128, v129\n"
    "v_cvt_pk_bf16_f32 v9, v130, v131\n"
    "v_mfma_f32_4x4x4_16b_bf16 v[140:143], v[184:185], v[46:47], v[92:95] cbsz:2 abid:3\n"
    "v_cvt_pk_bf16_f32 v10, v132, v133\n"
    "v_cvt_pk_bf16_f32 v11, v134, v135\n"
    "v_mfma_f32_4x4x4_16b_bf16 v[128:131], v[184:185], v[48:49], v[192:195] cbsz:2 abid:0\n"
    "v_cvt_pk_bf16_f32 v12, v136, v137\n"
    "v_cvt_pk_bf16_f32 v13, v138, v139\n"
    "v_mfma_f32_4x4x4_16b_bf16 v[132:135], v[184:185], v[50:51], v[196:199] cbsz:2 abid:1\n"
    "v_cvt_pk_bf16_f32 v14, v140, v141\n"
    "v_cvt_pk_bf16_f32 v15, v142, v143\n"
    "v_mfma_f32_4x4x4_16b_bf16 v[136:139], v[184:185], v[52:53], v[200:203] cbsz:2 abid:2\n"
    "s_waitcnt lgkmcnt(0)\n"
    "v_readfirstlane_b32 s60, v232\n"
    "v_cvt_pk_bf16_f32 v16, v128, v129\n"
    "v_cvt_pk_bf16_f32 v17, v130, v131\n"
    "v_mfma_f32_4x4x4_16b_bf16 v[140:143], v[184:185], v[54:55], v[204:207] cbsz:2 abid:3\n"
    "s_cmp_eq_u32 s60, -1\n"
    "s_cbranch_scc1 .Ldin_L1_poll\n"
    ".Ldin_L1_pr:\n"
    "v_readfirstlane_b32 s95, v233\n"
    "s_and_b32 s94, s60, 63\n"
    "s_lshl_b32 s94, s94, 6\n"
    "s_add_u32 s94, s94, 68480\n"
    "s_add_u32 s63, s63, 8\n"
    "v_lshl_add_u32 v252, v249, 1, s94\n"
    "ds_read_b32 v252, v252\n"
    "s_sub_u32 s95, s95, s76\n"
    "s_ashr_i32 s95, s95, 4\n"
    "s_min_i32 s95, s95, s74\n"
    "s_max_i32 s72, s95, s75\n"
    "v_cvt_pk_bf16_f32 v18, v132, v133\n"
    "v_cvt_pk_bf16_f32 v19, v134, v135\n"
    "v_mfma_f32_4x4x4_16b_bf16 v[128:131], v[184:185], v[56:57], v[208:211] cbsz:2 abid:0\n"
    "v_cvt_pk_bf16_f32 v20, v136, v137\n"
    "v_cvt_pk_bf16_f32 v21, v138, v139\n"
    "v_mfma_f32_4x4x4_16b_bf16 v[132:135], v[184:185], v[58:59], v[212:215] cbsz:2 abid:1\n"
    "v_cvt_pk_bf16_f32 v22, v140, v141\n"
    "v_cvt_pk_bf16_f32 v23, v142, v143\n"
    "v_mfma_f32_4x4x4_16b_bf16 v[136:139], v[184:185], v[60:61], v[216:219] cbsz:2 abid:2\n"
    "v_cvt_pk_bf16_f32 v24, v128, v129\n"
    "v_cvt_pk_bf16_f32 v25, v130, v131\n"
    "v_mfma_f32_4x4x4_16b_bf16 v[140:143], v[184:185], v[62:63], v[220:223] cbsz:2 abid:3\n"
    "v_cvt_pk_bf16_f32 v26, v132, v133\n"
    "v_cvt_pk_bf16_f32 v27, v134, v135\n"
    "v_cvt_pk_bf16_f32 v28, v136, v137\n"
    "v_cvt_pk_bf16_f32 v29, v138, v139\n"
    "v_exp_f32 v152, v152 clamp\n"
    "v_cvt_pk_bf16_f32 v30, v140, v141\n"
    "v_cvt_pk_bf16_f32 v31, v142, v143\n"
    "v_mfma_f32_16x16x32_bf16 v[128:131], v[0:3], v[160:163], v[112:115]\n"
    "v_exp_f32 v144, v144 clamp\n"
    "v_mfma_f32_16x16x32_bf16 v[128:131], v[4:7], v[164:167], v[128:131]\n"
    "v_exp_f32 v145, v145 clamp\n"
    "v_mfma_f32_16x16x32_bf16 v[132:135], v[8:11], v[160:163], v[116:119]\n"
    "v_exp_f32 v146, v146 clamp\n"
    "v_mfma_f32_16x16x32_bf16 v[132:135], v[12:15], v[164:167], v[132:135]\n"
    "v_exp_f32 v147, v147 clamp\n"
    "v_mfma_f32_16x16x32_bf16 v[136:139], v[16:19], v[160:163], v[120:123]\n"
    "v_exp_f32 v148, v148 clamp\n"
    "v_mfma_f32_16x16x32_bf16 v[136:139], v[20:23], v[164:167], v[136:139]\n"
    "v_exp_f32 v149, v149 clamp\n"
    "v_mfma_f32_16x16x32_bf16 v[140:143], v[24:27], v[160:163], v[124:127]\n"
    "v_exp_f32 v150, v150 clamp\n"
    "v_mfma_f32_16x16x32_bf16 v[140:143], v[28:31], v[164:167], v[140:143]\n"
    "v_exp_f32 v151, v151 clamp\n"
    "s_add_u32 m0, s68, 4096\n"
    "v_exp_f32 v153, v153 clamp\n"
    "v_add_u32 v253, s67, v244\n"
    "v_mfma_f32_4x4x4_16b_bf16 v[224:227], v[228:229], v[168:169], v[224:227] cbsz:4 abid:0\n"
    "v_exp_f32 v154, v154 clamp\n"
    "v_exp_f32 v155, v155 clamp\n"
    "v_mfma_f32_4x4x4_16b_bf16 v[224:227], v[228:229], v[170:171], v[224:227] cbsz:4 abid:1\n"
    "v_exp_f32 v156, v156 clamp\n"
    "v_exp_f32 v157, v157 clamp\n"
    "v_mfma_f32_4x4x4_16b_bf16 v[224:227], v[228:229], v[172:173], v[224:227] cbsz:4 abid:2\n"
    "v_exp_f32 v158, v158 clamp\n"
    "v_exp_f32 v159, v159 clamp\n"
    "v_mfma_f32_4x4x4_16b_bf16 v[224:227], v[228:229], v[174:175], v[224:227] cbsz:4 abid:3\n"
    "s_waitcnt vmcnt(4)\n"
    "ds_read_b64_tr_b16 v[168:169], v240 offset:4096\n"
    "ds_read_b64_tr_b16 v[170:171], v241 offset:4352\n"
    "ds_read_b64_tr_b16 v[172:173], v242 offset:4608\n"
    "ds_read_b64_tr_b16 v[174:175], v243 offset:4864\n"
    "ds_read_b32 v236, v253 offset:0\n"
    "ds_read_b32 v238, v253 offset:4\n"
    "ds_read_b128 v[160:163], v234 offset:6144\n"
    "ds_read_b128 v[164:167], v235 offset:6144\n"
    "s_waitcnt lgkmcnt(8)\n"
    "s_nop 0\n"
    "v_readlane_b32 s20, v252, 0\n"
    "v_readlane_b32 s21, v252, 1\n"
    "v_readlane_b32 s22, v252, 2\n"
    "v_readlane_b32 s23, v252, 3\n"
    "v_readlane_b32 s24, v252, 4\n"
    "v_readlane_b32 s25, v252, 5\n"
    "v_readlane_b32 s26, v252, 6\n"
    "v_readlane_b32 s27, v252, 7\n"
    "v_pk_fma_f32 v[144:145], v[144:145], s[70:71], v[96:97]\n"
    "v_pk_fma_f32 v[146:147], v[146:147], s[70:71], v[98:99]\n"
    "v_pk_add_f32 v[230:231], v[144:145], v[146:147]\n"
    "v_pk_mul_f32 v[232:233], v[144:145], v[146:147]\n"
    "v_pk_fma_f32 v[148:149], v[148:149], s[70:71], v[100:101]\n"
    "v_pk_fma_f32 v[230:231], v[230:231], v[148:149], v[232:233]\n"
    "v_pk_mul_f32 v[232:233], v[232:233], v[148:149]\n"
    "v_pk_fma_f32 v[150:151], v[150:151], s[70:71], v[102:103]\n"
    "v_pk_fma_f32 v[230:231], v[230:231], v[150:151], v[232:233]\n"
    "v_pk_mul_f32 v[232:233], v[232:233], v[150:151]\n"
    "v_pk_fma_f32 v[152:153], v[152:153], s[70:71], v[104:105]\n"
    "v_pk_fma_f32 v[230:231], v[230:231], v[152:153], v[232:233]\n"
    "v_pk_mul_f32 v[232:233], v[232:233], v[152:153]\n"
    "v_pk_fma_f32 v[154:155], v[154:155], s[70:71], v[106:107]\n"
    "v_pk_fma_f32 v[230:231], v[230:231], v[154:155], v[232:233]\n"
    "v_pk_mul_f32 v[232:233], v[232:233], v[154:155]\n"
    "v_pk_fma_f32 v[156:157], v[156:157], s[70:71], v[108:109]\n"
    "v_pk_fma_f32 v[230:231], v[230:231], v[156:157], v[232:233]\n"
    "v_pk_mul_f32 v[232:233], v[232:233], v[156:157]\n"
    "v_pk_fma_f32 v[158:159], v[158:159], s[70:71], v[110:111]\n"
    "v_pk_fma_f32 v[230:231], v[230:231], v[158:159], v[232:233]\n"
    "v_pk_mul_f32 v[232:233], v[232:233], v[158:159]\n"
    "v_rcp_f32 v232, v232\n"
    "v_rcp_f32 v233, v233\n"
    "v_fma_f32 v230, v230, v232, v247\n"
    "v_fmac_f32 v230, v231, v233\n"
    "v_cvt_pk_bf16_f32 v230, v230, v230\n"
    "s_add_u32 s67, s67, 64\n"
    "s_waitcnt lgkmcnt(0)\n"
    "ds_write_b16 v245, v230\n"
    "ds_read_b64 v[228:229], v246\n"
    "s_lshl1_add_u32 s76, s72, s76\n"
    "buffer_load_dwordx4 v[236:237], s[40:43], 0 idxen offen offset:0 nt lds\n"
    "buffer_load_dwordx4 v[238:239], s[40:43], 0 idxen offen offset:1024 nt lds\n"
    "s_and_b32 s62, s58, 63\n"
    "s_mul_i32 s62, s62, 272\n"
    "s_mov_b32 s58, s59\n"
    "s_mov_b32 s59, s60\n"
    "s_mov_b32 s81, s65\n"
    "s_xor_b32 s65, s65, s64\n"
    "s_mov_b32 s80, s83\n"
    "s_add_u32 s82, s65, 256\n"
    "s_cmp_lt_i32 s59, s57\n"
    "s_cselect_b32 s76, s76, -1\n"
    "s_bitcmp1_b32 s58, 30\n"
    "s_cbranch_scc1 .Ldin_A2s\n"
    ".Ldin_A2:\n"
    "v_mfma_f32_16x16x32_bf16 v[144:147], v[0:3], v[160:163], v[112:115]\n"
    "v_exp_f32 v128, v128 clamp\n"
    "v_mfma_f32_16x16x32_bf16 v[144:147], v[4:7], v[164:167], v[144:147]\n"
    "v_exp_f32 v129, v129 clamp\n"
    "v_mfma_f32_16x16x32_bf16 v[148:151], v[8:11], v[160:163], v[116:119]\n"
    "v_exp_f32 v130, v130 clamp\n"
    "v_mfma_f32_16x16x32_bf16 v[148:151], v[12:15], v[164:167], v[148:151]\n"
    "v_exp_f32 v131, v131 clamp\n"
    "v_mfma_f32_16x16x32_bf16 v[152:155], v[16:19], v[160:163], v[120:123]\n"
    "v_exp_f32 v132, v132 clamp\n"
    "v_mfma_f32_16x16x32_bf16 v[152:155], v[20:23], v[164:167], v[152:155]\n"
    "v_exp_f32 v133, v133 clamp\n"
    "v_mfma_f32_16x16x32_bf16 v[156:159], v[24:27], v[160:163], v[124:127]\n"
    "v_exp_f32 v134, v134 clamp\n"
    "v_mfma_f32_16x16x32_bf16 v[156:159], v[28:31], v[164:167], v[156:159]\n"
    "v_exp_f32 v135, v135 clamp\n"
    "v_exp_f32 v136, v136 clamp\n"
    "s_mov_b32 m0, s62\n"
    "v_exp_f32 v137, v137 clamp\n"
    "v_add_u32 v253, s67, v244\n"
    "s_waitcnt lgkmcnt(0)\n"
    "v_mfma_f32_4x4x4_16b_bf16 v[224:227], v[228:229], v[176:177], v[224:227] cbsz:4 abid:0\n"
    "v_exp_f32 v138, v138 clamp\n"
    "v_exp_f32 v139, v139 clamp\n"
    "v_mfma_f32_4x4x4_16b_bf16 v[224:227], v[228:229], v[178:179], v[224:227] cbsz:4 abid:1\n"
    "v_exp_f32 v140, v140 clamp\n"
    "v_exp_f32 v141, v141 clamp\n"
    "v_mfma_f32_4x4x4_16b_bf16 v[224:227], v[228:229], v[180:181], v[224:227] cbsz:4 abid:2\n"
    "v_exp_f32 v142, v142 clamp\n"
    "v_exp_f32 v143, v143 clamp\n"
    "v_mfma_f32_4x4x4_16b_bf16 v[224:227], v[228:229], v[182:183], v[224:227] cbsz:4 abid:3\n"
    "s_waitcnt vmcnt(4)\n"
    "ds_read_b64_tr_b16 v[176:177], v240 offset:6144\n"
    "ds_read_b64_tr_b16 v[178:179], v241 offset:6400\n"
    "ds_read_b64_tr_b16 v[180:181], v242 offset:6656\n"
    "ds_read_b64_tr_b16 v[182:183], v243 offset:6912\n"
    "ds_read_b32 v236, v253 offset:0\n"
    "ds_read_b32 v238, v253 offset:4\n"
    "ds_read_b128 v[160:163], v234 offset:0\n"
    "ds_read_b128 v[164:167], v235 offset:0\n"
    "s_cbranch_vccz .Ldin_A2_np\n"
    "s_setprio 1\n"
    ".Ldin_A2_np:\n"
    "v_pk_fma_f32 v[128:129], v[128:129], s[70:71], v[96:97]\n"
    "v_pk_fma_f32 v[130:131], v[130:131], s[70:71], v[98:99]\n"
    "v_pk_add_f32 v[190:191], v[224:225], v[226:227]\n"
    "v_add_f32 v190, v190, v191\n"
    "ds_write_addtid_b32 v190 offset:32768\n"
    "s_add_u32 m0, s68, 6144\n"
    "s_bfe_u32 s94, s59, 0x180006\n"
    "s_mul_i32 s86, s94, s56\n"
    "v_mbcnt_lo_u32_b32 v184, s20, 0\n"
    "v_mbcnt_lo_u32_b32 v185, s22, 0\n"
    "v_mbcnt_lo_u32_b32 v186, s24, 0\n"
    "v_mbcnt_lo_u32_b32 v187, s26, 0\n"
    "s_bcnt1_i32_b64 s90, s[20:21]\n"
    "s_bcnt1_i32_b64 s91, s[22:23]\n"
    "s_bcnt1_i32_b64 s92, s[24:25]\n"
    "s_bcnt1_i32_b64 s93, s[26:27]\n"
    "v_mbcnt_hi_u32_b32 v184, s21, v184\n"
    "v_mbcnt_hi_u32_b32 v185, s23, v185\n"
    "v_mbcnt_hi_u32_b32 v186, s25, v186\n"
    "v_mbcnt_hi_u32_b32 v187, s27, v187\n"
    "s_lshl2_add_u32 s90, s90, s65\n"
    "s_lshl2_add_u32 s91, s91, s90\n"
    "s_lshl2_add_u32 s92, s92, s91\n"
    "s_lshl2_add_u32 s93, s93, s92\n"
    "v_add_u32_e64 v188, s86, v251\n"
    "v_pk_add_f32 v[230:231], v[128:129], v[130:131]\n"
    "v_pk_mul_f32 v[232:233], v[128:129], v[130:131]\n"
    "v_pk_fma_f32 v[132:133], v[132:133], s[70:71], v[100:101]\n"
    "v_pk_fma_f32 v[230:231], v[230:231], v[132:133], v[232:233]\n"
    "v_pk_mul_f32 v[232:233], v[232:233], v[132:133]\n"
    "v_lshl_add_u32 v184, v184, 2, s65\n"
    "v_lshl_add_u32 v185, v185, 2, s90\n"
    "v_lshl_add_u32 v186, v186, 2, s91\n"
    "v_lshl_add_u32 v187, v187, 2, s92\n"
    "v_mov_b32 v191, -1\n"
    "v_lshl_add_u32 v190, v249, 1, s93\n"
    "ds_write2_b32 v190, v191, v191 offset1:64\n"
    "s_mov_b64 exec, s[20:21]\n"
    "ds_write_b32 v184, v188\n"
    "s_mov_b64 exec, s[22:23]\n"
    "v_add_u32 v189, 64, v188\n"
    "ds_write_b32 v185, v189\n"
    "s_mov_b64 exec, s[24:25]\n"
    "v_add_u32 v190, 128, v188\n"
    "ds_write_b32 v186, v190\n"
    "s_mov_b64 exec, s[26:27]\n"
    "v_add_u32 v189, 192, v188\n"
    "ds_write_b32 v187, v189\n"
    "s_mov_b64 exec, -1\n"
    "s_add_u32 s95, s93, 60\n"
    "s_add_u32 s94, s82, 128\n"
    "s_andn2_b32 s95, s95, 63\n"
    "s_max_u32 s83, s95, s94\n"
    "v_pk_fma_f32 v[134:135], v[134:135], s[70:71], v[102:103]\n"
    "v_pk_fma_f32 v[230:231], v[230:231], v[134:135], v[232:233]\n"
    "v_pk_mul_f32 v[232:233], v[232:233], v[134:135]\n"
    "v_pk_fma_f32 v[136:137], v[136:137], s[70:71], v[104:105]\n"
    "v_pk_fma_f32 v[230:231], v[230:231], v[136:137], v[232:233]\n"
    "v_pk_mul_f32 v[232:233], v[232:233], v[136:137]\n"
    "v_pk_fma_f32 v[138:139], v[138:139], s[70:71], v[106:107]\n"
    "v_pk_fma_f32 v[230:231], v[230:231], v[138:139], v[232:233]\n"
    "v_pk_mul_f32 v[232:233], v[232:233], v[138:139]\n"
    "v_pk_fma_f32 v[140:141], v[140:141], s[70:71], v[108:109]\n"
    "v_pk_fma_f32 v[230:231], v[230:231], v[140:141], v[232:233]\n"
    "v_pk_mul_f32 v[232:233], v[232:233], v[140:141]\n"
    "v_pk_fma_f32 v[142:143], v[142:143], s[70:71], v[110:111]\n"
    "v_pk_fma_f32 v[230:231], v[230:231], v[142:143], v[232:233]\n"
    "v_pk_mul_f32 v[232:233], v[232:233], v[142:143]\n"
    "v_rcp_f32 v232, v232\n"
    "v_rcp_f32 v233, v233\n"
    "v_fma_f32 v230, v230, v232, v247\n"
    "v_fmac_f32 v230, v231, v233\n"
    "v_cvt_pk_bf16_f32 v230, v230, v230\n"
    "s_add_u32 s67, s67, 64\n"
    "s_waitcnt lgkmcnt(0)\n"
    "ds_write_b16 v245, v230\n"
    "ds_read_b64 v[228:229], v246\n"
    "buffer_load_dwordx4 v[236:237], s[40:43], 0 idxen offen offset:0 nt lds\n"
    "buffer_load_dwordx4 v[238:239], s[40:43], 0 idxen offen offset:1024 nt lds\n"
    ".Ldin_B3:\n"
    "v_mfma_f32_16x16x32_bf16 v[128:131], v[0:3], v[160:163], v[112:115]\n"
    "v_exp_f32 v144, v144 clamp\n"
    "v_mfma_f32_16x16x32_bf16 v[128:131], v[4:7], v[164:167], v[128:131]\n"
    "v_exp_f32 v145, v145 clamp\n"
    "v_mfma_f32_16x16x32_bf16 v[132:135], v[8:11], v[160:163], v[116:119]\n"
    "v_exp_f32 v146, v146 clamp\n"
    "v_mfma_f32_16x16x32_bf16 v[132:135], v[12:15], v[164:167], v[132:135]\n"
    "v_exp_f32 v147, v147 clamp\n"
    "v_mfma_f32_16x16x32_bf16 v[136:139], v[16:19], v[160:163], v[120:123]\n"
    "v_exp_f32 v148, v148 clamp\n"
    "v_mfma_f32_16x16x32_bf16 v[136:139], v[20:23], v[164:167], v[136:139]\n"
    "v_exp_f32 v149, v149 clamp\n"
    "v_mfma_f32_16x16x32_bf16 v[140:143], v[24:27], v[160:163], v[124:127]\n"
    "v_exp_f32 v150, v150 clamp\n"
    "v_mfma_f32_16x16x32_bf16 v[140:143], v[28:31], v[164:167], v[140:143]\n"
    "v_exp_f32 v151, v151 clamp\n"
    "v_exp_f32 v152, v152 clamp\n"
    "s_cmp_eq_u32 s67, s80\n"
    "s_cselect_b32 s67, s65, s67\n"
    "s_and_b32 s94, s59, 63\n"
    "s_mul_i32 s94, s94, 272\n"
    "v_add_u32 v252, s94, v250\n"
    "s_add_u32 m0, s68, 0\n"
    "v_exp_f32 v153, v153 clamp\n"
    "v_add_u32_e64 v253, s67, v244\n"
    "s_waitcnt lgkmcnt(0)\n"
    "v_mfma_f32_4x4x4_16b_bf16 v[224:227], v[228:229], v[168:169], 0 cbsz:4 abid:0\n"
    "v_exp_f32 v154, v154 clamp\n"
    "v_exp_f32 v155, v155 clamp\n"
    "v_mfma_f32_4x4x4_16b_bf16 v[224:227], v[228:229], v[170:171], v[224:227] cbsz:4 abid:1\n"
    "v_exp_f32 v156, v156 clamp\n"
    "v_exp_f32 v157, v157 clamp\n"
    "v_mfma_f32_4x4x4_16b_bf16 v[224:227], v[228:229], v[172:173], v[224:227] cbsz:4 abid:2\n"
    "v_exp_f32 v158, v158 clamp\n"
    "v_exp_f32 v159, v159 clamp\n"
    "v_mfma_f32_4x4x4_16b_bf16 v[224:227], v[228:229], v[174:175], v[224:227] cbsz:4 abid:3\n"
    "s_waitcnt vmcnt(4)\n"
    "s_memrealtime s[34:35]\n"
    "ds_read_b64_tr_b16 v[168:169], v240 offset:0\n"
    "ds_read_b64_tr_b16 v[170:171], v241 offset:256\n"
    "ds_read_b64_tr_b16 v[172:173], v242 offset:512\n"
    "ds_read_b64_tr_b16 v[174:175], v243 offset:768\n"
    "ds_read_b32 v236, v253 offset:0\n"
    "ds_read_b32 v238, v253 offset:4\n"
    "ds_read_b128 v[160:163], v234 offset:2048\n"
    "ds_read_b128 v[164:167], v235 offset:2048\n"
    "v_add_u32_e64 v184, v248, v252\n"
    "ds_read_b64 v[184:185], v184\n"
    "v_pk_fma_f32 v[144:145], v[144:145], s[70:71], v[96:97]\n"
    "v_pk_fma_f32 v[146:147], v[146:147], s[70:71], v[98:99]\n"
    "v_pk_add_f32 v[230:231], v[144:145], v[146:147]\n"
    "v_pk_mul_f32 v[232:233], v[144:145], v[146:147]\n"
    "v_pk_fma_f32 v[148:149], v[148:149], s[70:71], v[100:101]\n"
    "v_pk_fma_f32 v[230:231], v[230:231], v[148:149], v[232:233]\n"
    "v_pk_mul_f32 v[232:233], v[232:233], v[148:149]\n"
    "v_pk_fma_f32 v[150:151], v[150:151], s[70:71], v[102:103]\n"
    "v_pk_fma_f32 v[230:231], v[230:231], v[150:151], v[232:233]\n"
    "v_pk_mul_f32 v[232:233], v[232:233], v[150:151]\n"
    "v_pk_fma_f32 v[152:153], v[152:153], s[70:71], v[104:105]\n"
    "v_pk_fma_f32 v[230:231], v[230:231], v[152:153], v[232:233]\n"
    "v_pk_mul_f32 v[232:233], v[232:233], v[152:153]\n"
    "v_pk_fma_f32 v[154:155], v[154:155], s[70:71], v[106:107]\n"
    "v_pk_fma_f32 v[230:231], v[230:231], v[154:155], v[232:233]\n"
    "v_pk_mul_f32 v[232:233], v[232:233], v[154:155]\n"
    "v_pk_fma_f32 v[156:157], v[156:157], s[70:71], v[108:109]\n"
    "v_pk_fma_f32 v[230:231], v[230:231], v[156:157], v[232:233]\n"
    "v_pk_mul_f32 v[232:233], v[232:233], v[156:157]\n"
    "v_pk_fma_f32 v[158:159], v[158:159], s[70:71], v[110:111]\n"
    "v_pk_fma_f32 v[230:231], v[230:231], v[158:159], v[232:233]\n"
    "v_pk_mul_f32 v[232:233], v[232:233], v[158:159]\n"
    "v_rcp_f32 v232, v232\n"
    "v_rcp_f32 v233, v233\n"
    "v_fma_f32 v230, v230, v232, v247\n"
    "v_fmac_f32 v230, v231, v233\n"
    "v_cvt_pk_bf16_f32 v230, v230, v230\n"
    "s_add_u32 s67, s67, 64\n"
    "s_waitcnt lgkmcnt(0)\n"
    "ds_write_b16 v245, v230\n"
    "ds_read_b64 v[228:229], v246\n"
    "v_and_b32 v184, v254, v184\n"
    "v_and_b32 v185, v255, v185\n"
    "s_cmp_le_u32 s34, s76\n"
    "s_cbranch_scc1 .Ldin_B3_wait\n"
    ".Ldin_B3_go:\n"
    "s_sub_u32 s94, s34, 60\n"
    "s_max_u32 s76, s76, s94\n"
    "s_add_u32 s76, s76, s72\n"
    ".Ldin_B3_dma:\n"
    "buffer_load_dwordx4 v[236:237], s[40:43], 0 idxen offen offset:0 nt lds\n"
    "buffer_load_dwordx4 v[238:239], s[40:43], 0 idxen offen offset:1024 nt lds\n"
    ".Ldin_B3_nd:\n"
    "s_branch .Ldin_G0\n"
    ".Ldin_L1_poll:\n"
    "s_sleep 1\n"
    "ds_read_b64 v[232:233], v253\n"
    "s_waitcnt lgkmcnt(0)\n"
    "v_readfirstlane_b32 s60, v232\n"
    "s_cmp_eq_u32 s60, -1\n"
    "s_cbranch_scc1 .Ldin_L1_poll\n"
    "s_branch .Ldin_L1_pr\n"
    ".Ldin_B3_wait:\n"
    "s_cmp_lt_i32 s59, s57\n"
    "s_cbranch_scc1 .Ldin_B3_w2\n"
    "s_sub_u32 s94, s67, s65\n"
    "s_cmp_gt_u32 s94, 256\n"
    "s_cbranch_scc1 .Ldin_B3_dma\n"
    "s_cmp_eq_u32 s94, 64\n"
    "s_waitcnt vmcnt(2)\n"
    "s_cbranch_scc1 .Ldin_B3_nd\n"
    "s_waitcnt vmcnt(0)\n"
    "s_branch .Ldin_B3_nd\n"
    ".Ldin_B3_slp:\n"
    "s_sleep 1\n"
    ".Ldin_B3_w2:\n"
    "s_memrealtime s[34:35]\n"
    "s_waitcnt lgkmcnt(0)\n"
    "s_sub_u32 s94, s34, s76\n"
    "s_cmp_lt_i32 s94, 0\n"
    "s_cbranch_scc1 .Ldin_B3_slp\n"
    "s_branch .Ldin_B3_go\n"
    ".Ldin_stub_11:\n"
    "s_cbranch_execz .Ldin_t2_ret\n"
    "s_nop 0\n"
    ".p2align 5\n"
    ".Ldin_L0:\n"
    "s_setprio 0\n"
    "s_cmp_ge_i32 s59, s57\n"
    "s_cbranch_scc1 .Ldin_E0\n"
    "s_add_u32 s76, s76, s73\n"
    "v_mov_b32_e64 v253, s63\n"
    "ds_read_b64 v[232:233], v253\n"
    "v_mfma_f32_4x4x4_16b_bf16 v[144:147], v[184:185], v[32:33], v[64:67] cbsz:2 abid:0\n"
    "ds_read_b128 v[112:115], v252 offset:17408\n"
    "v_mfma_f32_4x4x4_16b_bf16 v[148:151], v[184:185], v[34:35], v[68:71] cbsz:2 abid:1\n"
    "ds_read_b128 v[116:119], v252 offset:17424\n"
    "v_mfma_f32_4x4x4_16b_bf16 v[152:155], v[184:185], v[36:37], v[72:75] cbsz:2 abid:2\n"
    "ds_read_b128 v[120:123], v252 offset:17536\n"
    "ds_read_b128 v[124:127], v252 offset:17552\n"
    "v_cvt_pk_bf16_f32 v0, v144, v145\n"
    "v_cvt_pk_bf16_f32 v1, v146, v147\n"
    "v_mfma_f32_4x4x4_16b_bf16 v[156:159], v[184:185], v[38:39], v[76:79] cbsz:2 abid:3\n"
    "v_cvt_pk_bf16_f32 v2, v148, v149\n"
    "v_cvt_pk_bf16_f32 v3, v150, v151\n"
    "v_mfma_f32_4x4x4_16b_bf16 v[144:147], v[184:185], v[40:41], v[80:83] cbsz:2 abid:0\n"
    "v_cvt_pk_bf16_f32 v4, v152, v153\n"
    "v_cvt_pk_bf16_f32 v5, v154, v155\n"
    "v_mfma_f32_4x4x4_16b_bf16 v[148:151], v[184:185], v[42:43], v[84:87] cbsz:2 abid:1\n"
    "v_cvt_pk_bf16_f32 v6, v156, v157\n"
    "v_cvt_pk_bf16_f32 v7, v158, v159\n"
    "v_mfma_f32_4x4x4_16b_bf16 v[152:155], v[184:185], v[44:45], v[88:91] cbsz:2 abid:2\n"
    "v_cvt_pk_bf16_f32 v8, v144, v145\n"
    "v_cvt_pk_bf16_f32 v9, v146, v147\n"
    "v_mfma_f32_4x4x4_16b_bf16 v[156:159], v[184:185], v[46:47], v[92:95] cbsz:2 abid:3\n"
    "v_cvt_pk_bf16_f32 v10, v148, v149\n"
    "v_cvt_pk_bf16_f32 v11, v150, v151\n"
    "v_mfma_f32_4x4x4_16b_bf16 v[144:147], v[184:185], v[48:49], v[192:195] cbsz:2 abid:0\n"
    "v_cvt_pk_bf16_f32 v12, v152, v153\n"
    "v_cvt_pk_bf16_f32 v13, v154, v155\n"
    "v_mfma_f32_4x4x4_16b_bf16 v[148:151], v[184:185], v[50:51], v[196:199] cbsz:2 abid:1\n"
    "v_cvt_pk_bf16_f32 v14, v156, v157\n"
    "v_cvt_pk_bf16_f32 v15, v158, v159\n"
    "v_mfma_f32_4x4x4_16b_bf16 v[152:155], v[184:185], v[52:53], v[200:203] cbsz:2 abid:2\n"
    "s_waitcnt lgkmcnt(0)\n"
    "v_readfirstlane_b32 s60, v232\n"
    "v_cvt_pk_bf16_f32 v16, v144, v145\n"
    "v_cvt_pk_bf16_f32 v17, v146, v147\n"
    "v_mfma_f32_4x4x4_16b_bf16 v[156:159], v[184:185], v[54:55], v[204:207] cbsz:2 abid:3\n"
    "s_cmp_eq_u32 s60, -1\n"
    "s_cbranch_scc1 .Ldin_L0_poll\n"
    ".Ldin_L0_pr:\n"
    "v_readfirstlane_b32 s95, v233\n"
    "s_and_b32 s94, s60, 63\n"
    "s_lshl_b32 s94, s94, 6\n"
    "s_add_u32 s94, s94, 68480\n"
    "s_add_u32 s63, s63, 8\n"
    "v_lshl_add_u32 v252, v249, 1, s94\n"
    "ds_read_b32 v252, v252\n"
    "s_sub_u32 s95, s95, s76\n"
    "s_ashr_i32 s95, s95, 4\n"
    "s_min_i32 s95, s95, s74\n"
    "s_max_i32 s72, s95, s75\n"
    "v_cvt_pk_bf16_f32 v18, v148, v149\n"
    "v_cvt_pk_bf16_f32 v19, v150, v151\n"
    "v_mfma_f32_4x4x4_16b_bf16 v[144:147], v[184:185], v[56:57], v[208:211] cbsz:2 abid:0\n"
    "v_cvt_pk_bf16_f32 v20, v152, v153\n"
    "v_cvt_pk_bf16_f32 v21, v154, v155\n"
    "v_mfma_f32_4x4x4_16b_bf16 v[148:151], v[184:185], v[58:59], v[212:215] cbsz:2 abid:1\n"
    "v_cvt_pk_bf16_f32 v22, v156, v157\n"
    "v_cvt_pk_bf16_f32 v23, v158, v159\n"
    "v_mfma_f32_4x4x4_16b_bf16 v[152:155], v[184:185], v[60:61], v[216:219] cbsz:2 abid:2\n"
    "v_cvt_pk_bf16_f32 v24, v144, v145\n"
    "v_cvt_pk_bf16_f32 v25, v146, v147\n"
    "v_mfma_f32_4x4x4_16b_bf16 v[156:159], v[184:185], v[62:63], v[220:223] cbsz:2 abid:3\n"
    "v_cvt_pk_bf16_f32 v26, v148, v149\n"
    "v_cvt_pk_bf16_f32 v27, v150, v151\n"
    "v_cvt_pk_bf16_f32 v28, v152, v153\n"
    "v_cvt_pk_bf16_f32 v29, v154, v155\n"
    "v_exp_f32 v136, v136 clamp\n"
    "v_cvt_pk_bf16_f32 v30, v156, v157\n"
    "v_cvt_pk_bf16_f32 v31, v158, v159\n"
    "v_mfma_f32_16x16x32_bf16 v[144:147], v[0:3], v[160:163], v[112:115]\n"
    "v_exp_f32 v128, v128 clamp\n"
    "v_mfma_f32_16x16x32_bf16 v[144:147], v[4:7], v[164:167], v[144:147]\n"
    "v_exp_f32 v129, v129 clamp\n"
    "v_mfma_f32_16x16x32_bf16 v[148:151], v[8:11], v[160:163], v[116:119]\n"
    "v_exp_f32 v130, v130 clamp\n"
    "v_mfma_f32_16x16x32_bf16 v[148:151], v[12:15], v[164:167], v[148:151]\n"
    "v_exp_f32 v131, v131 clamp\n"
    "v_mfma_f32_16x16x32_bf16 v[152:155], v[16:19], v[160:163], v[120:123]\n"
    "v_exp_f32 v132, v132 clamp\n"
    "v_mfma_f32_16x16x32_bf16 v[152:155], v[20:23], v[164:167], v[152:155]\n"
    "v_exp_f32 v133, v133 clamp\n"
    "v_mfma_f32_16x16x32_bf16 v[156:159], v[24:27], v[160:163], v[124:127]\n"
    "v_exp_f32 v134, v134 clamp\n"
    "v_mfma_f32_16x16x32_bf16 v[156:159], v[28:31], v[164:167], v[156:159]\n"
    "v_exp_f32 v135, v135 clamp\n"
    "s_add_u32 m0, s68, 2048\n"
    "v_exp_f32 v137, v137 clamp\n"
    "v_add_u32 v253, s67, v244\n"
    "v_mfma_f32_4x4x4_16b_bf16 v[224:227], v[228:229], v[176:177], v[224:227] cbsz:4 abid:0\n"
    "v_exp_f32 v138, v138 clamp\n"
    "v_exp_f32 v139, v139 clamp\n"
    "v_mfma_f32_4x4x4_16b_bf16 v[224:227], v[228:229], v[178:179], v[224:227] cbsz:4 abid:1\n"
    "v_exp_f32 v140, v140 clamp\n"
    "v_exp_f32 v141, v141 clamp\n"
    "v_mfma_f32_4x4x4_16b_bf16 v[224:227], v[228:229], v[180:181], v[224:227] cbsz:4 abid:2\n"
    "v_exp_f32 v142, v142 clamp\n"
    "v_exp_f32 v143, v143 clamp\n"
    "v_mfma_f32_4x4x4_16b_bf16 v[224:227], v[228:229], v[182:183], v[224:227] cbsz:4 abid:3\n"
    "s_waitcnt vmcnt(4)\n"
    "ds_read_b64_tr_b16 v[176:177], v240 offset:2048\n"
    "ds_read_b64_tr_b16 v[178:179], v241 offset:2304\n"
    "ds_read_b64_tr_b16 v[180:181], v242 offset:2560\n"
    "ds_read_b64_tr_b16 v[182:183], v243 offset:2816\n"
    "ds_read_b32 v236, v253 offset:0\n"
    "ds_read_b32 v238, v253 offset:4\n"
    "ds_read_b128 v[160:163], v234 offset:4096\n"
    "ds_read_b128 v[164:167], v235 offset:4096\n"
    "s_waitcnt lgkmcnt(8)\n"
    "s_nop 0\n"
    "v_readlane_b32 s20, v252, 0\n"
    "v_readlane_b32 s21, v252, 1\n"
    "v_readlane_b32 s22, v252, 2\n"
    "v_readlane_b32 s23, v252, 3\n"
    "v_readlane_b32 s24, v252, 4\n"
    "v_readlane_b32 s25, v252, 5\n"
    "v_readlane_b32 s26, v252, 6\n"
    "v_readlane_b32 s27, v252, 7\n"
    "v_pk_fma_f32 v[128:129], v[128:129], s[70:71], v[96:97]\n"
    "v_pk_fma_f32 v[130:131], v[130:131], s[70:71], v[98:99]\n"
    "v_pk_add_f32 v[230:231], v[128:129], v[130:131]\n"
    "v_pk_mul_f32 v[232:233], v[128:129], v[130:131]\n"
    "v_pk_fma_f32 v[132:133], v[132:133], s[70:71], v[100:101]\n"
    "v_pk_fma_f32 v[230:231], v[230:231], v[132:133], v[232:233]\n"
    "v_pk_mul_f32 v[232:233], v[232:233], v[132:133]\n"
    "v_pk_fma_f32 v[134:135], v[134:135], s[70:71], v[102:103]\n"
    "v_pk_fma_f32 v[230:231], v[230:231], v[134:135], v[232:233]\n"
    "v_pk_mul_f32 v[232:233], v[232:233], v[134:135]\n"
    "v_pk_fma_f32 v[136:137], v[136:137], s[70:71], v[104:105]\n"
    "v_pk_fma_f32 v[230:231], v[230:231], v[136:137], v[232:233]\n"
    "v_pk_mul_f32 v[232:233], v[232:233], v[136:137]\n"
    "v_pk_fma_f32 v[138:139], v[138:139], s[70:71], v[106:107]\n"
    "v_pk_fma_f32 v[230:231], v[230:231], v[138:139], v[232:233]\n"
    "v_pk_mul_f32 v[232:233], v[232:233], v[138:139]\n"
    "v_pk_fma_f32 v[140:141], v[140:141], s[70:71], v[108:109]\n"
    "v_pk_fma_f32 v[230:231], v[230:231], v[140:141], v[232:233]\n"
    "v_pk_mul_f32 v[232:233], v[232:233], v[140:141]\n"
    "v_pk_fma_f32 v[142:143], v[142:143], s[70:71], v[110:111]\n"
    "v_pk_fma_f32 v[230:231], v[230:231], v[142:143], v[232:233]\n"
    "v_pk_mul_f32 v[232:233], v[232:233], v[142:143]\n"
    "v_rcp_f32 v232, v232\n"
    "v_rcp_f32 v233, v233\n"
    "v_fma_f32 v230, v230, v232, v247\n"
    "v_fmac_f32 v230, v231, v233\n"
    "v_cvt_pk_bf16_f32 v230, v230, v230\n"
    "s_add_u32 s67, s67, 64\n"
    "s_waitcnt lgkmcnt(0)\n"
    "ds_write_b16 v245, v230\n"
    "ds_read_b64 v[228:229], v246\n"
    "s_lshl1_add_u32 s76, s72, s76\n"
    "buffer_load_dwordx4 v[236:237], s[40:43], 0 idxen offen offset:0 nt lds\n"
    "buffer_load_dwordx4 v[238:239], s[40:43], 0 idxen offen offset:1024 nt lds\n"
    "s_and_b32 s62, s58, 63\n"
    "s_mul_i32 s62, s62, 272\n"
    "s_mov_b32 s58, s59\n"
    "s_mov_b32 s59, s60\n"
    "s_mov_b32 s81, s65\n"
    "s_xor_b32 s65, s65, s64\n"
    "s_mov_b32 s80, s83\n"
    "s_add_u32 s82, s65, 256\n"
    "s_cmp_lt_i32 s59, s57\n"
    "s_cselect_b32 s76, s76, -1\n"
    "s_bitcmp1_b32 s58, 30\n"
    "s_cbranch_scc1 .Ldin_A1s\n"
    ".Ldin_A1:\n"
    "v_mfma_f32_16x16x32_bf16 v[128:131], v[0:3], v[160:163], v[112:115]\n"
    "v_exp_f32 v144, v144 clamp\n"
    "v_mfma_f32_16x16x32_bf16 v[128:131], v[4:7], v[164:167], v[128:131]\n"
    "v_exp_f32 v145, v145 clamp\n"
    "v_mfma_f32_16x16x32_bf16 v[132:135], v[8:11], v[160:163], v[116:119]\n"
    "v_exp_f32 v146, v146 clamp\n"
    "v_mfma_f32_16x16x32_bf16 v[132:135], v[12:15], v[164:167], v[132:135]\n"
    "v_exp_f32 v147, v147 clamp\n"
    "v_mfma_f32_16x16x32_bf16 v[136:139], v[16:19], v[160:163], v[120:123]\n"
    "v_exp_f32 v148, v148 clamp\n"
    "v_mfma_f32_16x16x32_bf16 v[136:139], v[20:23], v[164:167], v[136:139]\n"
    "v_exp_f32 v149, v149 clamp\n"
    "v_mfma_f32_16x16x32_bf16 v[140:143], v[24:27], v[160:163], v[124:127]\n"
    "v_exp_f32 v150, v150 clamp\n"
    "v_mfma_f32_16x16x32_bf16 v[140:143], v[28:31], v[164:167], v[140:143]\n"
    "v_exp_f32 v151, v151 clamp\n"
    "v_exp_f32 v152, v152 clamp\n"
    "s_mov_b32 m0, s62\n"
    "v_exp_f32 v153, v153 clamp\n"
    "v_add_u32 v253, s67, v244\n"
    "s_waitcnt lgkmcnt(0)\n"
    "v_mfma_f32_4x4x4_16b_bf16 v[224:227], v[228:229], v[168:169], v[224:227] cbsz:4 abid:0\n"
    "v_exp_f32 v154, v154 clamp\n"
    "v_exp_f32 v155, v155 clamp\n"
    "v_mfma_f32_4x4x4_16b_bf16 v[224:227], v[228:229], v[170:171], v[224:227] cbsz:4 abid:1\n"
    "v_exp_f32 v156, v156 clamp\n"
    "v_exp_f32 v157, v157 clamp\n"
    "v_mfma_f32_4x4x4_16b_bf16 v[224:227], v[228:229], v[172:173], v[224:227] cbsz:4 abid:2\n"
    "v_exp_f32 v158, v158 clamp\n"
    "v_exp_f32 v159, v159 clamp\n"
    "v_mfma_f32_4x4x4_16b_bf16 v[224:227], v[228:229], v[174:175], v[224:227] cbsz:4 abid:3\n"
    "s_waitcnt vmcnt(4)\n"
    "ds_read_b64_tr_b16 v[168:169], v240 offset:4096\n"
    "ds_read_b64_tr_b16 v[170:171], v241 offset:4352\n"
    "ds_read_b64_tr_b16 v[172:173], v242 offset:4608\n"
    "ds_read_b64_tr_b16 v[174:175], v243 offset:4864\n"
    "ds_read_b32 v236, v253 offset:0\n"
    "ds_read_b32 v238, v253 offset:4\n"
    "ds_read_b128 v[160:163], v234 offset:6144\n"
    "ds_read_b128 v[164:167], v235 offset:6144\n"
    "s_cbranch_vccz .Ldin_A1_np\n"
    "s_setprio 1\n"
    ".Ldin_A1_np:\n"
    "v_pk_fma_f32 v[144:145], v[144:145], s[70:71], v[96:97]\n"
    "v_pk_fma_f32 v[146:147], v[146:147], s[70:71], v[98:99]\n"
    "v_pk_add_f32 v[190:191], v[224:225], v[226:227]\n"
    "v_add_f32 v190, v190, v191\n"
    "ds_write_addtid_b32 v190 offset:32768\n"
    "s_add_u32 m0, s68, 4096\n"
    "s_bfe_u32 s94, s59, 0x180006\n"
    "s_mul_i32 s86, s94, s56\n"
    "v_mbcnt_lo_u32_b32 v184, s20, 0\n"
    "v_mbcnt_lo_u32_b32 v185, s22, 0\n"
    "v_mbcnt_lo_u32_b32 v186, s24, 0\n"
    "v_mbcnt_lo_u32_b32 v187, s26, 0\n"
    "s_bcnt1_i32_b64 s90, s[20:21]\n"
    "s_bcnt1_i32_b64 s91, s[22:23]\n"
    "s_bcnt1_i32_b64 s92, s[24:25]\n"
    "s_bcnt1_i32_b64 s93, s[26:27]\n"
    "v_mbcnt_hi_u32_b32 v184, s21, v184\n"
    "v_mbcnt_hi_u32_b32 v185, s23, v185\n"
    "v_mbcnt_hi_u32_b32 v186, s25, v186\n"
    "v_mbcnt_hi_u32_b32 v187, s27, v187\n"
    "s_lshl2_add_u32 s90, s90, s65\n"
    "s_lshl2_add_u32 s91, s91, s90\n"
    "s_lshl2_add_u32 s92, s92, s91\n"
    "s_lshl2_add_u32 s93, s93, s92\n"
    "v_add_u32_e64 v188, s86, v251\n"
    "v_pk_add_f32 v[230:231], v[144:145], v[146:147]\n"
    "v_pk_mul_f32 v[232:233], v[144:145], v[146:147]\n"
    "v_pk_fma_f32 v[148:149], v[148:149], s[70:71], v[100:101]\n"
    "v_pk_fma_f32 v[230:231], v[230:231], v[148:149], v[232:233]\n"
    "v_pk_mul_f32 v[232:233], v[232:233], v[148:149]\n"
    "v_lshl_add_u32 v184, v184, 2, s65\n"
    "v_lshl_add_u32 v185, v185, 2, s90\n"
    "v_lshl_add_u32 v186, v186, 2, s91\n"
    "v_lshl_add_u32 v187, v187, 2, s92\n"
    "v_mov_b32 v191, -1\n"
    "v_lshl_add_u32 v190, v249, 1, s93\n"
    "ds_write2_b32 v190, v191, v191 offset1:64\n"
    "s_mov_b64 exec, s[20:21]\n"
    "ds_write_b32 v184, v188\n"
    "s_mov_b64 exec, s[22:23]\n"
    "v_add_u32 v189, 64, v188\n"
    "ds_write_b32 v185, v189\n"
    "s_mov_b64 exec, s[24:25]\n"
    "v_add_u32 v190, 128, v188\n"
    "ds_write_b32 v186, v190\n"
    "s_mov_b64 exec, s[26:27]\n"
    "v_add_u32 v189, 192, v188\n"
    "ds_write_b32 v187, v189\n"
    "s_mov_b64 exec, -1\n"
    "s_add_u32 s95, s93, 60\n"
    "s_add_u32 s94, s82, 128\n"
    "s_andn2_b32 s95, s95, 63\n"
    "s_max_u32 s83, s95, s94\n"
    "v_pk_fma_f32 v[150:151], v[150:151], s[70:71], v[102:103]\n"
    "v_pk_fma_f32 v[230:231], v[230:231], v[150:151], v[232:233]\n"
    "v_pk_mul_f32 v[232:233], v[232:233], v[150:151]\n"
    "v_pk_fma_f32 v[152:153], v[152:153], s[70:71], v[104:105]\n"
    "v_pk_fma_f32 v[230:231], v[230:231], v[152:153], v[232:233]\n"
    "v_pk_mul_f32 v[232:233], v[232:233], v[152:153]\n"
    "v_pk_fma_f32 v[154:155], v[154:155], s[70:71], v[106:107]\n"
    "v_pk_fma_f32 v[230:231], v[230:231], v[154:155], v[232:233]\n"
    "v_pk_mul_f32 v[232:233], v[232:233], v[154:155]\n"
    "v_pk_fma_f32 v[156:157], v[156:157], s[70:71], v[108:109]\n"
    "v_pk_fma_f32 v[230:231], v[230:231], v[156:157], v[232:233]\n"
    "v_pk_mul_f32 v[232:233], v[232:233], v[156:157]\n"
    "v_pk_fma_f32 v[158:159], v[158:159], s[70:71], v[110:111]\n"
    "v_pk_fma_f32 v[230:231], v[230:231], v[158:159], v[232:233]\n"
    "v_pk_mul_f32 v[232:233], v[232:233], v[158:159]\n"
    "v_rcp_f32 v232, v232\n"
    "v_rcp_f32 v233, v233\n"
    "v_fma_f32 v230, v230, v232, v247\n"
    "v_fmac_f32 v230, v231, v233\n"
    "v_cvt_pk_bf16_f32 v230, v230, v230\n"
    "s_add_u32 s67, s67, 64\n"
    "s_waitcnt lgkmcnt(0)\n"
    "ds_write_b16 v245, v230\n"
    "ds_read_b64 v[228:229], v246\n"
    "buffer_load_dwordx4 v[236:237], s[40:43], 0 idxen offen offset:0 nt lds\n"
    "buffer_load_dwordx4 v[238:239], s[40:43], 0 idxen offen offset:1024 nt lds\n"
    ".Ldin_B2:\n"
    "v_mfma_f32_16x16x32_bf16 v[144:147], v[0:3], v[160:163], v[112:115]\n"
    "v_exp_f32 v128, v128 clamp\n"
    "v_mfma_f32_16x16x32_bf16 v[144:147], v[4:7], v[164:167], v[144:147]\n"
    "v_exp_f32 v129, v129 clamp\n"
    "v_mfma_f32_16x16x32_bf16 v[148:151], v[8:11], v[160:163], v[116:119]\n"
    "v_exp_f32 v130, v130 clamp\n"
    "v_mfma_f32_16x16x32_bf16 v[148:151], v[12:15], v[164:167], v[148:151]\n"
    "v_exp_f32 v131, v131 clamp\n"
    "v_mfma_f32_16x16x32_bf16 v[152:155], v[16:19], v[160:163], v[120:123]\n"
    "v_exp_f32 v132, v132 clamp\n"
    "v_mfma_f32_16x16x32_bf16 v[152:155], v[20:23], v[164:167], v[152:155]\n"
    "v_exp_f32 v133, v133 clamp\n"
    "v_mfma_f32_16x16x32_bf16 v[156:159], v[24:27], v[160:163], v[124:127]\n"
    "v_exp_f32 v134, v134 clamp\n"
    "v_mfma_f32_16x16x32_bf16 v[156:159], v[28:31], v[164:167], v[156:159]\n"
    "v_exp_f32 v135, v135 clamp\n"
    "v_exp_f32 v136, v136 clamp\n"
    "s_cmp_eq_u32 s67, s80\n"
    "s_cselect_b32 s67, s65, s67\n"
    "s_and_b32 s94, s59, 63\n"
    "s_mul_i32 s94, s94, 272\n"
    "v_add_u32 v252, s94, v250\n"
    "s_add_u32 m0, s68, 6144\n"
    "v_exp_f32 v137, v137 clamp\n"
    "v_add_u32 v253, s67, v244\n"
    "s_waitcnt lgkmcnt(0)\n"
    "v_mfma_f32_4x4x4_16b_bf16 v[224:227], v[228:229], v[176:177], 0 cbsz:4 abid:0\n"
    "v_exp_f32 v138, v138 clamp\n"
    "v_exp_f32 v139, v139 clamp\n"
    "v_mfma_f32_4x4x4_16b_bf16 v[224:227], v[228:229], v[178:179], v[224:227] cbsz:4 abid:1\n"
    "v_exp_f32 v140, v140 clamp\n"
    "v_exp_f32 v141, v141 clamp\n"
    "v_mfma_f32_4x4x4_16b_bf16 v[224:227], v[228:229], v[180:181], v[224:227] cbsz:4 abid:2\n"
    "v_exp_f32 v142, v142 clamp\n"
    "v_exp_f32 v143, v143 clamp\n"
    "v_mfma_f32_4x4x4_16b_bf16 v[224:227], v[228:229], v[182:183], v[224:227] cbsz:4 abid:3\n"
    "s_waitcnt vmcnt(4)\n"
    "s_memrealtime s[34:35]\n"
    "ds_read_b64_tr_b16 v[176:177], v240 offset:6144\n"
    "ds_read_b64_tr_b16 v[178:179], v241 offset:6400\n"
    "ds_read_b64_tr_b16 v[180:181], v242 offset:6656\n"
    "ds_read_b64_tr_b16 v[182:183], v243 offset:6912\n"
    "ds_read_b32 v236, v253 offset:0\n"
    "ds_read_b32 v238, v253 offset:4\n"
    "ds_read_b128 v[160:163], v234 offset:0\n"
    "ds_read_b128 v[164:167], v235 offset:0\n"
    "v_add_u32_e64 v184, v248, v252\n"
    "ds_read_b64 v[184:185], v184\n"
    "v_pk_fma_f32 v[128:129], v[128:129], s[70:71], v[96:97]\n"
    "v_pk_fma_f32 v[130:131], v[130:131], s[70:71], v[98:99]\n"
    "v_pk_add_f32 v[230:231], v[128:129], v[130:131]\n"
    "v_pk_mul_f32 v[232:233], v[128:129], v[130:131]\n"
    "v_pk_fma_f32 v[132:133], v[132:133], s[70:71], v[100:101]\n"
    "v_pk_fma_f32 v[230:231], v[230:231], v[132:133], v[232:233]\n"
    "v_pk_mul_f32 v[232:233], v[232:233], v[132:133]\n"
    "v_pk_fma_f32 v[134:135], v[134:135], s[70:71], v[102:103]\n"
    "v_pk_fma_f32 v[230:231], v[230:231], v[134:135], v[232:233]\n"
    "v_pk_mul_f32 v[232:233], v[232:233], v[134:135]\n"
    "v_pk_fma_f32 v[136:137], v[136:137], s[70:71], v[104:105]\n"
    "v_pk_fma_f32 v[230:231], v[230:231], v[136:137], v[232:233]\n"
    "v_pk_mul_f32 v[232:233], v[232:233], v[136:137]\n"
    "v_pk_fma_f32 v[138:139], v[138:139], s[70:71], v[106:107]\n"
    "v_pk_fma_f32 v[230:231], v[230:231], v[138:139], v[232:233]\n"
    "v_pk_mul_f32 v[232:233], v[232:233], v[138:139]\n"
    "v_pk_fma_f32 v[140:141], v[140:141], s[70:71], v[108:109]\n"
    "v_pk_fma_f32 v[230:231], v[230:231], v[140:141], v[232:233]\n"
    "v_pk_mul_f32 v[232:233], v[232:233], v[140:141]\n"
    "v_pk_fma_f32 v[142:143], v[142:143], s[70:71], v[110:111]\n"
    "v_pk_fma_f32 v[230:231], v[230:231], v[142:143], v[232:233]\n"
    "v_pk_mul_f32 v[232:233], v[232:233], v[142:143]\n"
    "v_rcp_f32 v232, v232\n"
    "v_rcp_f32 v233, v233\n"
    "v_fma_f32 v230, v230, v232, v247\n"
    "v_fmac_f32 v230, v231, v233\n"
    "v_cvt_pk_bf16_f32 v230, v230, v230\n"
    "s_add_u32 s67, s67, 64\n"
    "s_waitcnt lgkmcnt(0)\n"
    "ds_write_b16 v245, v230\n"
    "ds_read_b64 v[228:229], v246\n"
    "v_and_b32 v184, v254, v184\n"
    "v_and_b32 v185, v255, v185\n"
    "s_cmp_le_u32 s34, s76\n"
    "s_cbranch_scc1 .Ldin_B2_wait\n"
    ".Ldin_B2_go:\n"
    "s_sub_u32 s94, s34, 60\n"
    "s_max_u32 s76, s76, s94\n"
    "s_add_u32 s76, s76, s72\n"
    ".Ldin_B2_dma:\n"
    "buffer_load_dwordx4 v[236:237], s[40:43], 0 idxen offen offset:0 nt lds\n"
    "buffer_load_dwordx4 v[238:239], s[40:43], 0 idxen offen offset:1024 nt lds\n"
    ".Ldin_B2_nd:\n"
    "s_branch .Ldin_G3\n"
    ".Ldin_L0_poll:\n"
    "s_sleep 1\n"
    "ds_read_b64 v[232:233], v253\n"
    "s_waitcnt lgkmcnt(0)\n"
    "v_readfirstlane_b32 s60, v232\n"
    "s_cmp_eq_u32 s60, -1\n"
    "s_cbranch_scc1 .Ldin_L0_poll\n"
    "s_branch .Ldin_L0_pr\n"
    ".Ldin_B2_wait:\n"
    "s_cmp_lt_i32 s59, s57\n"
    "s_cbranch_scc1 .Ldin_B2_w2\n"
    "s_sub_u32 s94, s67, s65\n"
    "s_cmp_gt_u32 s94, 256\n"
    "s_cbranch_scc1 .Ldin_B2_dma\n"
    "s_cmp_eq_u32 s94, 64\n"
    "s_waitcnt vmcnt(2)\n"
    "s_cbranch_scc1 .Ldin_B2_nd\n"
    "s_waitcnt vmcnt(0)\n"
    "s_branch .Ldin_B2_nd\n"
    ".Ldin_B2_slp:\n"
    "s_sleep 1\n"
    ".Ldin_B2_w2:\n"
    "s_memrealtime s[34:35]\n"
    "s_waitcnt lgkmcnt(0)\n"
    "s_sub_u32 s94, s34, s76\n"
    "s_cmp_lt_i32 s94, 0\n"
    "s_cbranch_scc1 .Ldin_B2_slp\n"
    "s_branch .Ldin_B2_go\n"
    ".Ldin_stub_10:\n"
    "s_cbranch_execz .Ldin_t2_ret\n"
    "s_nop 0\n"
    ".p2align 5\n"
    ".Ldin_L3:\n"
    "s_setprio 0\n"
    "s_cmp_ge_i32 s59, s57\n"
    "s_cbranch_scc1 .Ldin_E1\n"
    "s_add_u32 s76, s76, s73\n"
    "v_mov_b32_e64 v253, s63\n"
    "ds_read_b64 v[232:233], v253\n"
    "v_mfma_f32_4x4x4_16b_bf16 v[128:131], v[184:185], v[32:33], v[64:67] cbsz:2 abid:0\n"
    "ds_read_b128 v[112:115], v252 offset:17408\n"
    "v_mfma_f32_4x4x4_16b_bf16 v[132:135], v[184:185], v[34:35], v[68:71] cbsz:2 abid:1\n"
    "ds_read_b128 v[116:119], v252 offset:17424\n"
    "v_mfma_f32_4x4x4_16b_bf16 v[136:139], v[184:185], v[36:37], v[72:75] cbsz:2 abid:2\n"
    "ds_read_b128 v[120:123], v252 offset:17536\n"
    "ds_read_b128 v[124:127], v252 offset:17552\n"
    "v_cvt_pk_bf16_f32 v0, v128, v129\n"
    "v_cvt_pk_bf16_f32 v1, v130, v131\n"
    "v_mfma_f32_4x4x4_16b_bf16 v[140:143], v[184:185], v[38:39], v[76:79] cbsz:2 abid:3\n"
    "v_cvt_pk_bf16_f32 v2, v132, v133\n"
    "v_cvt_pk_bf16_f32 v3, v134, v135\n"
    "v_mfma_f32_4x4x4_16b_bf16 v[128:131], v[184:185], v[40:41], v[80:83] cbsz:2 abid:0\n"
    "v_cvt_pk_bf16_f32 v4, v136, v137\n"
    "v_cvt_pk_bf16_f32 v5, v138, v139\n"
    "v_mfma_f32_4x4x4_16b_bf16 v[132:135], v[184:185], v[42:43], v[84:87] cbsz:2 abid:1\n"
    "v_cvt_pk_bf16_f32 v6, v140, v141\n"
    "v_cvt_pk_bf16_f32 v7, v142, v143\n"
    "v_mfma_f32_4x4x4_16b_bf16 v[136:139], v[184:185], v[44:45], v[88:91] cbsz:2 abid:2\n"
    "v_cvt_pk_bf16_f32 v8, v128, v129\n"
    "v_cvt_pk_bf16_f32 v9, v130, v131\n"
    "v_mfma_f32_4x4x4_16b_bf16 v[140:143], v[184:185], v[46:47], v[92:95] cbsz:2 abid:3\n"
    "v_cvt_pk_bf16_f32 v10, v132, v133\n"
    "v_cvt_pk_bf16_f32 v11, v134, v135\n"
    "v_mfma_f32_4x4x4_16b_bf16 v[128:131], v[184:185], v[48:49], v[192:195] cbsz:2 abid:0\n"
    "v_cvt_pk_bf16_f32 v12, v136, v137\n"
    "v_cvt_pk_bf16_f32 v13, v138, v139\n"
    "v_mfma_f32_4x4x4_16b_bf16 v[132:135], v[184:185], v[50:51], v[196:199] cbsz:2 abid:1\n"
    "v_cvt_pk_bf16_f32 v14, v140, v141\n"
    "v_cvt_pk_bf16_f32 v15, v142, v143\n"
    "v_mfma_f32_4x4x4_16b_bf16 v[136:139], v[184:185], v[52:53], v[200:203] cbsz:2 abid:2\n"
    "s_waitcnt lgkmcnt(0)\n"
    "v_readfirstlane_b32 s60, v232\n"
    "v_cvt_pk_bf16_f32 v16, v128, v129\n"
    "v_cvt_pk_bf16_f32 v17, v130, v131\n"
    "v_mfma_f32_4x4x4_16b_bf16 v[140:143], v[184:185], v[54:55], v[204:207] cbsz:2 abid:3\n"
    "s_cmp_eq_u32 s60, -1\n"
    "s_cbranch_scc1 .Ldin_L3_poll\n"
    ".Ldin_L3_pr:\n"
    "v_readfirstlane_b32 s95, v233\n"
    "s_and_b32 s94, s60, 63\n"
    "s_lshl_b32 s94, s94, 6\n"
    "s_add_u32 s94, s94, 68480\n"
    "s_add_u32 s63, s63, 8\n"
    "v_lshl_add_u32 v252, v249, 1, s94\n"
    "ds_read_b32 v252, v252\n"
    "s_sub_u32 s95, s95, s76\n"
    "s_ashr_i32 s95, s95, 4\n"
    "s_min_i32 s95, s95, s74\n"
    "s_max_i32 s72, s95, s75\n"
    "v_cvt_pk_bf16_f32 v18, v132, v133\n"
    "v_cvt_pk_bf16_f32 v19, v134, v135\n"
    "v_mfma_f32_4x4x4_16b_bf16 v[128:131], v[184:185], v[56:57], v[208:211] cbsz:2 abid:0\n"
    "v_cvt_pk_bf16_f32 v20, v136, v137\n"
    "v_cvt_pk_bf16_f32 v21, v138, v139\n"
    "v_mfma_f32_4x4x4_16b_bf16 v[132:135], v[184:185], v[58:59], v[212:215] cbsz:2 abid:1\n"
    "v_cvt_pk_bf16_f32 v22, v140, v141\n"
    "v_cvt_pk_bf16_f32 v23, v142, v143\n"
    "v_mfma_f32_4x4x4_16b_bf16 v[136:139], v[184:185], v[60:61], v[216:219] cbsz:2 abid:2\n"
    "v_cvt_pk_bf16_f32 v24, v128, v129\n"
    "v_cvt_pk_bf16_f32 v25, v130, v131\n"
    "v_mfma_f32_4x4x4_16b_bf16 v[140:143], v[184:185], v[62:63], v[220:223] cbsz:2 abid:3\n"
    "v_cvt_pk_bf16_f32 v26, v132, v133\n"
    "v_cvt_pk_bf16_f32 v27, v134, v135\n"
    "v_cvt_pk_bf16_f32 v28, v136, v137\n"
    "v_cvt_pk_bf16_f32 v29, v138, v139\n"
    "v_exp_f32 v152, v152 clamp\n"
    "v_cvt_pk_bf16_f32 v30, v140, v141\n"
    "v_cvt_pk_bf16_f32 v31, v142, v143\n"
    "v_mfma_f32_16x16x32_bf16 v[128:131], v[0:3], v[160:163], v[112:115]\n"
    "v_exp_f32 v144, v144 clamp\n"
    "v_mfma_f32_16x16x32_bf16 v[128:131], v[4:7], v[164:167], v[128:131]\n"
    "v_exp_f32 v145, v145 clamp\n"
    "v_mfma_f32_16x16x32_bf16 v[132:135], v[8:11], v[160:163], v[116:119]\n"
    "v_exp_f32 v146, v146 clamp\n"
    "v_mfma_f32_16x16x32_bf16 v[132:135], v[12:15], v[164:167], v[132:135]\n"
    "v_exp_f32 v147, v147 clamp\n"
    "v_mfma_f32_16x16x32_bf16 v[136:139], v[16:19], v[160:163], v[120:123]\n"
    "v_exp_f32 v148, v148 clamp\n"
    "v_mfma_f32_16x16x32_bf16 v[136:139], v[20:23], v[164:167], v[136:139]\n"
    "v_exp_f32 v149, v149 clamp\n"
    "v_mfma_f32_16x16x32_bf16 v[140:143], v[24:27], v[160:163], v[124:127]\n"
    "v_exp_f32 v150, v150 clamp\n"
    "v_mfma_f32_16x16x32_bf16 v[140:143], v[28:31], v[164:167], v[140:143]\n"
    "v_exp_f32 v151, v151 clamp\n"
    "s_add_u32 m0, s68, 0\n"
    "v_exp_f32 v153, v153 clamp\n"
    "v_add_u32_e64 v253, s67, v244\n"
    "v_mfma_f32_4x4x4_16b_bf16 v[224:227], v[228:229], v[168:169], v[224:227] cbsz:4 abid:0\n"
    "v_exp_f32 v154, v154 clamp\n"
    "v_exp_f32 v155, v155 clamp\n"
    "v_mfma_f32_4x4x4_16b_bf16 v[224:227], v[228:229], v[170:171], v[224:227] cbsz:4 abid:1\n"
    "v_exp_f32 v156, v156 clamp\n"
    "v_exp_f32 v157, v157 clamp\n"
    "v_mfma_f32_4x4x4_16b_bf16 v[224:227], v[228:229], v[172:173], v[224:227] cbsz:4 abid:2\n"
    "v_exp_f32 v158, v158 clamp\n"
    "v_exp_f32 v159, v159 clamp\n"
    "v_mfma_f32_4x4x4_16b_bf16 v[224:227], v[228:229], v[174:175], v[224:227] cbsz:4 abid:3\n"
    "s_waitcnt vmcnt(4)\n"
    "ds_read_b64_tr_b16 v[168:169], v240 offset:0\n"
    "ds_read_b64_tr_b16 v[170:171], v241 offset:256\n"
    "ds_read_b64_tr_b16 v[172:173], v242 offset:512\n"
    "ds_read_b64_tr_b16 v[174:175], v243 offset:768\n"
    "ds_read_b32 v236, v253 offset:0\n"
    "ds_read_b32 v238, v253 offset:4\n"
    "ds_read_b128 v[160:163], v234 offset:2048\n"
    "ds_read_b128 v[164:167], v235 offset:2048\n"
    "s_waitcnt lgkmcnt(8)\n"
    "s_nop 0\n"
    "v_readlane_b32 s20, v252, 0\n"
    "v_readlane_b32 s21, v252, 1\n"
    "v_readlane_b32 s22, v252, 2\n"
    "v_readlane_b32 s23, v252, 3\n"
    "v_readlane_b32 s24, v252, 4\n"
    "v_readlane_b32 s25, v252, 5\n"
    "v_readlane_b32 s26, v252, 6\n"
    "v_readlane_b32 s27, v252, 7\n"
    "v_pk_fma_f32 v[144:145], v[144:145], s[70:71], v[96:97]\n"
    "v_pk_fma_f32 v[146:147], v[146:147], s[70:71], v[98:99]\n"
    "v_pk_add_f32 v[230:231], v[144:145], v[146:147]\n"
    "v_pk_mul_f32 v[232:233], v[144:145], v[146:147]\n"
    "v_pk_fma_f32 v[148:149], v[148:149], s[70:71], v[100:101]\n"
    "v_pk_fma_f32 v[230:231], v[230:231], v[148:149], v[232:233]\n"
    "v_pk_mul_f32 v[232:233], v[232:233], v[148:149]\n"
    "v_pk_fma_f32 v[150:151], v[150:151], s[70:71], v[102:103]\n"
    "v_pk_fma_f32 v[230:231], v[230:231], v[150:151], v[232:233]\n"
    "v_pk_mul_f32 v[232:233], v[232:233], v[150:151]\n"
    "v_pk_fma_f32 v[152:153], v[152:153], s[70:71], v[104:105]\n"
    "v_pk_fma_f32 v[230:231], v[230:231], v[152:153], v[232:233]\n"
    "v_pk_mul_f32 v[232:233], v[232:233], v[152:153]\n"
    "v_pk_fma_f32 v[154:155], v[154:155], s[70:71], v[106:107]\n"
    "v_pk_fma_f32 v[230:231], v[230:231], v[154:155], v[232:233]\n"
    "v_pk_mul_f32 v[232:233], v[232:233], v[154:155]\n"
    "v_pk_fma_f32 v[156:157], v[156:157], s[70:71], v[108:109]\n"
    "v_pk_fma_f32 v[230:231], v[230:231], v[156:157], v[232:233]\n"
    "v_pk_mul_f32 v[232:233], v[232:233], v[156:157]\n"
    "v_pk_fma_f32 v[158:159], v[158:159], s[70:71], v[110:111]\n"
    "v_pk_fma_f32 v[230:231], v[230:231], v[158:159], v[232:233]\n"
    "v_pk_mul_f32 v[232:233], v[232:233], v[158:159]\n"
    "v_rcp_f32 v232, v232\n"
    "v_rcp_f32 v233, v233\n"
    "v_fma_f32 v230, v230, v232, v247\n"
    "v_fmac_f32 v230, v231, v233\n"
    "v_cvt_pk_bf16_f32 v230, v230, v230\n"
    "s_add_u32 s67, s67, 64\n"
    "s_waitcnt lgkmcnt(0)\n"
    "ds_write_b16 v245, v230\n"
    "ds_read_b64 v[228:229], v246\n"
    "s_lshl1_add_u32 s76, s72, s76\n"
    "buffer_load_dwordx4 v[236:237], s[40:43], 0 idxen offen offset:0 nt lds\n"
    "buffer_load_dwordx4 v[238:239], s[40:43], 0 idxen offen offset:1024 nt lds\n"
    "s_and_b32 s62, s58, 63\n"
    "s_mul_i32 s62, s62, 272\n"
    "s_mov_b32 s58, s59\n"
    "s_mov_b32 s59, s60\n"
    "s_mov_b32 s81, s65\n"
    "s_xor_b32 s65, s65, s64\n"
    "s_mov_b32 s80, s83\n"
    "s_add_u32 s82, s65, 256\n"
    "s_cmp_lt_i32 s59, s57\n"
    "s_cselect_b32 s76, s76, -1\n"
    "s_bitcmp1_b32 s58, 30\n"
    "s_cbranch_scc1 .Ldin_A0s\n"
    "s_branch .Ldin_A0\n"
    ".Ldin_L3_poll:\n"
    "s_sleep 1\n"
    "ds_read_b64 v[232:233], v253\n"
    "s_waitcnt lgkmcnt(0)\n"
    "v_readfirstlane_b32 s60, v232\n"
    "s_cmp_eq_u32 s60, -1\n"
    "s_cbranch_scc1 .Ldin_L3_poll\n"
    "s_branch .Ldin_L3_pr\n"
    ".p2align 5\n"
    ".Ldin_stub_8:\n"
    "s_cbranch_execz .Ldin_t2_ret\n"
    "s_nop 0\n"
    ".Ldin_E0:\n"
    "v_exp_f32 v128, v128 clamp\n"
    "v_exp_f32 v129, v129 clamp\n"
    "v_exp_f32 v130, v130 clamp\n"
    "v_exp_f32 v131, v131 clamp\n"
    "v_exp_f32 v132, v132 clamp\n"
    "v_exp_f32 v133, v133 clamp\n"
    "v_exp_f32 v134, v134 clamp\n"
    "v_exp_f32 v135, v135 clamp\n"
    "s_and_b32 s62, s58, 63\n"
    "s_mul_i32 s62, s62, 272\n"
    "s_waitcnt lgkmcnt(0)\n"
    "v_mfma_f32_4x4x4_16b_bf16 v[224:227], v[228:229], v[176:177], v[224:227] cbsz:4 abid:0\n"
    "v_exp_f32 v136, v136 clamp\n"
    "v_exp_f32 v137, v137 clamp\n"
    "v_mfma_f32_4x4x4_16b_bf16 v[224:227], v[228:229], v[178:179], v[224:227] cbsz:4 abid:1\n"
    "v_exp_f32 v138, v138 clamp\n"
    "v_exp_f32 v139, v139 clamp\n"
    "v_mfma_f32_4x4x4_16b_bf16 v[224:227], v[228:229], v[180:181], v[224:227] cbsz:4 abid:2\n"
    "v_exp_f32 v140, v140 clamp\n"
    "v_exp_f32 v141, v141 clamp\n"
    "v_mfma_f32_4x4x4_16b_bf16 v[224:227], v[228:229], v[182:183], v[224:227] cbsz:4 abid:3\n"
    "v_exp_f32 v142, v142 clamp\n"
    "v_exp_f32 v143, v143 clamp\n"
    "v_pk_fma_f32 v[128:129], v[128:129], s[70:71], v[96:97]\n"
    "v_pk_fma_f32 v[130:131], v[130:131], s[70:71], v[98:99]\n"
    "v_pk_add_f32 v[230:231], v[128:129], v[130:131]\n"
    "v_pk_mul_f32 v[232:233], v[128:129], v[130:131]\n"
    "v_pk_fma_f32 v[132:133], v[132:133], s[70:71], v[100:101]\n"
    "v_pk_fma_f32 v[230:231], v[230:231], v[132:133], v[232:233]\n"
    "v_pk_mul_f32 v[232:233], v[232:233], v[132:133]\n"
    "v_pk_fma_f32 v[134:135], v[134:135], s[70:71], v[102:103]\n"
    "v_pk_fma_f32 v[230:231], v[230:231], v[134:135], v[232:233]\n"
    "v_pk_mul_f32 v[232:233], v[232:233], v[134:135]\n"
    "v_pk_fma_f32 v[136:137], v[136:137], s[70:71], v[104:105]\n"
    "v_pk_fma_f32 v[230:231], v[230:231], v[136:137], v[232:233]\n"
    "v_pk_mul_f32 v[232:233], v[232:233], v[136:137]\n"
    "v_pk_fma_f32 v[138:139], v[138:139], s[70:71], v[106:107]\n"
    "v_pk_fma_f32 v[230:231], v[230:231], v[138:139], v[232:233]\n"
    "v_pk_mul_f32 v[232:233], v[232:233], v[138:139]\n"
    "v_pk_fma_f32 v[140:141], v[140:141], s[70:71], v[108:109]\n"
    "v_pk_fma_f32 v[230:231], v[230:231], v[140:141], v[232:233]\n"
    "v_pk_mul_f32 v[232:233], v[232:233], v[140:141]\n"
    "v_pk_fma_f32 v[142:143], v[142:143], s[70:71], v[110:111]\n"
    "v_pk_fma_f32 v[230:231], v[230:231], v[142:143], v[232:233]\n"
    "v_pk_mul_f32 v[232:233], v[232:233], v[142:143]\n"
    "v_rcp_f32 v232, v232\n"
    "v_rcp_f32 v233, v233\n"
    "v_fma_f32 v230, v230, v232, v247\n"
    "v_fmac_f32 v230, v231, v233\n"
    "v_cvt_pk_bf16_f32 v230, v230, v230\n"
    "ds_write_b16 v245, v230\n"
    "ds_read_b64 v[228:229], v246\n"
    ".Ldin_drain0:\n"
    "v_add_u32 v253, s66, v244\n"
    "ds_read_b32 v253, v253\n"
    "v_lshlrev_b32 v202, 3, v249\n"
    "s_mov_b32 m0, s62\n"
    "s_mov_b32 s70, s69\n"
    "s_mov_b32 s71, s69\n"
    "s_waitcnt lgkmcnt(0)\n"
    "v_mfma_f32_4x4x4_16b_bf16 v[224:227], v[228:229], v[168:169], v[224:227] cbsz:4 abid:0\n"
    "v_and_b32 v201, 63, v253\n"
    "v_and_b32 v202, 0x70, v202\n"
    "v_mfma_f32_4x4x4_16b_bf16 v[224:227], v[228:229], v[170:171], v[224:227] cbsz:4 abid:1\n"
    "v_and_b32 v200, 0x3fffffc0, v253\n"
    "v_mul_u32_u24 v201, 272, v201\n"
    "v_mfma_f32_4x4x4_16b_bf16 v[224:227], v[228:229], v[172:173], v[224:227] cbsz:4 abid:2\n"
    "v_lshl_add_u32 v200, v200, 1, v202\n"
    "v_lshl_add_u32 v201, v202, 1, v201\n"
    "v_mfma_f32_4x4x4_16b_bf16 v[224:227], v[228:229], v[174:175], v[224:227] cbsz:4 abid:3\n"
    "v_cmp_gt_u32 vcc, s57, v253\n"
    "v_bfrev_b32 v202, 1\n"
    "s_nop 0\n"
    "v_cndmask_b32 v200, v202, v200, vcc\n"
    "s_nop 0\n"
    "v_pk_add_f32 v[232:233], v[224:225], v[226:227]\n"
    "v_add_f32 v232, v232, v233\n"
    "ds_write_addtid_b32 v232 offset:32768\n"
    "ds_read_b128 v[208:211], v201 offset:32768\n"
    "ds_read_b128 v[212:215], v201 offset:32784\n"
    "s_waitcnt lgkmcnt(1)\n"
    "v_pk_mul_f32 v[208:209], v[208:209], s[70:71]\n"
    "v_pk_mul_f32 v[210:211], v[210:211], s[70:71]\n"
    "v_cvt_pk_bf16_f32 v216, v208, v209\n"
    "v_cvt_pk_bf16_f32 v217, v210, v211\n"
    "s_waitcnt lgkmcnt(0)\n"
    "v_pk_mul_f32 v[212:213], v[212:213], s[70:71]\n"
    "v_pk_mul_f32 v[214:215], v[214:215], s[70:71]\n"
    "v_cvt_pk_bf16_f32 v218, v212, v213\n"
    "v_cvt_pk_bf16_f32 v219, v214, v215\n"
    "s_waitcnt vmcnt(0)\n"
    "buffer_store_dwordx4 v[216:219], v200, s[52:55], 0 offen sc0 sc1 nt\n"
    "s_endpgm\n"
    ".p2align 5\n"
    ".Ldin_stub_9:\n"
    "s_cbranch_execz .Ldin_t2_ret\n"
    "s_nop 0\n"
    ".Ldin_E1:\n"
    "v_exp_f32 v144, v144 clamp\n"
    "v_exp_f32 v145, v145 clamp\n"
    "v_exp_f32 v146, v146 clamp\n"
    "v_exp_f32 v147, v147 clamp\n"
    "v_exp_f32 v148, v148 clamp\n"
    "v_exp_f32 v149, v149 clamp\n"
    "v_exp_f32 v150, v150 clamp\n"
    "v_exp_f32 v151, v151 clamp\n"
    "s_and_b32 s62, s58, 63\n"
    "s_mul_i32 s62, s62, 272\n"
    "s_waitcnt lgkmcnt(0)\n"
    "v_mfma_f32_4x4x4_16b_bf16 v[224:227], v[228:229], v[168:169], v[224:227] cbsz:4 abid:0\n"
    "v_exp_f32 v152, v152 clamp\n"
    "v_exp_f32 v153, v153 clamp\n"
    "v_mfma_f32_4x4x4_16b_bf16 v[224:227], v[228:229], v[170:171], v[224:227] cbsz:4 abid:1\n"
    "v_exp_f32 v154, v154 clamp\n"
    "v_exp_f32 v155, v155 clamp\n"
    "v_mfma_f32_4x4x4_16b_bf16 v[224:227], v[228:229], v[172:173], v[224:227] cbsz:4 abid:2\n"
    "v_exp_f32 v156, v156 clamp\n"
    "v_exp_f32 v157, v157 clamp\n"
    "v_mfma_f32_4x4x4_16b_bf16 v[224:227], v[228:229], v[174:175], v[224:227] cbsz:4 abid:3\n"
    "v_exp_f32 v158, v158 clamp\n"
    "v_exp_f32 v159, v159 clamp\n"
    "v_pk_fma_f32 v[144:145], v[144:145], s[70:71], v[96:97]\n"
    "v_pk_fma_f32 v[146:147], v[146:147], s[70:71], v[98:99]\n"
    "v_pk_add_f32 v[230:231], v[144:145], v[146:147]\n"
    "v_pk_mul_f32 v[232:233], v[144:145], v[146:147]\n"
    "v_pk_fma_f32 v[148:149], v[148:149], s[70:71], v[100:101]\n"
    "v_pk_fma_f32 v[230:231], v[230:231], v[148:149], v[232:233]\n"
    "v_pk_mul_f32 v[232:233], v[232:233], v[148:149]\n"
    "v_pk_fma_f32 v[150:151], v[150:151], s[70:71], v[102:103]\n"
    "v_pk_fma_f32 v[230:231], v[230:231], v[150:151], v[232:233]\n"
    "v_pk_mul_f32 v[232:233], v[232:233], v[150:151]\n"
    "v_pk_fma_f32 v[152:153], v[152:153], s[70:71], v[104:105]\n"
    "v_pk_fma_f32 v[230:231], v[230:231], v[152:153], v[232:233]\n"
    "v_pk_mul_f32 v[232:233], v[232:233], v[152:153]\n"
    "v_pk_fma_f32 v[154:155], v[154:155], s[70:71], v[106:107]\n"
    "v_pk_fma_f32 v[230:231], v[230:231], v[154:155], v[232:233]\n"
    "v_pk_mul_f32 v[232:233], v[232:233], v[154:155]\n"
    "v_pk_fma_f32 v[156:157], v[156:157], s[70:71], v[108:109]\n"
    "v_pk_fma_f32 v[230:231], v[230:231], v[156:157], v[232:233]\n"
    "v_pk_mul_f32 v[232:233], v[232:233], v[156:157]\n"
    "v_pk_fma_f32 v[158:159], v[158:159], s[70:71], v[110:111]\n"
    "v_pk_fma_f32 v[230:231], v[230:231], v[158:159], v[232:233]\n"
    "v_pk_mul_f32 v[232:233], v[232:233], v[158:159]\n"
    "v_rcp_f32 v232, v232\n"
    "v_rcp_f32 v233, v233\n"
    "v_fma_f32 v230, v230, v232, v247\n"
    "v_fmac_f32 v230, v231, v233\n"
    "v_cvt_pk_bf16_f32 v230, v230, v230\n"
    "ds_write_b16 v245, v230\n"
    "ds_read_b64 v[228:229], v246\n"
    ".Ldin_drain1:\n"
    "v_add_u32 v253, s66, v244\n"
    "ds_read_b32 v253, v253\n"
    "v_lshlrev_b32 v202, 3, v249\n"
    "s_mov_b32 m0, s62\n"
    "s_mov_b32 s70, s69\n"
    "s_mov_b32 s71, s69\n"
    "s_waitcnt lgkmcnt(0)\n"
    "v_mfma_f32_4x4x4_16b_bf16 v[224:227], v[228:229], v[176:177], v[224:227] cbsz:4 abid:0\n"
    "v_and_b32 v201, 63, v253\n"
    "v_and_b32 v202, 0x70, v202\n"
    "v_mfma_f32_4x4x4_16b_bf16 v[224:227], v[228:229], v[178:179], v[224:227] cbsz:4 abid:1\n"
    "v_and_b32 v200, 0x3fffffc0, v253\n"
    "v_mul_u32_u24 v201, 272, v201\n"
    "v_mfma_f32_4x4x4_16b_bf16 v[224:227], v[228:229], v[180:181], v[224:227] cbsz:4 abid:2\n"
    "v_lshl_add_u32 v200, v200, 1, v202\n"
    "v_lshl_add_u32 v201, v202, 1, v201\n"
    "v_mfma_f32_4x4x4_16b_bf16 v[224:227], v[228:229], v[182:183], v[224:227] cbsz:4 abid:3\n"
    "v_cmp_gt_u32 vcc, s57, v253\n"
    "v_bfrev_b32 v202, 1\n"
    "s_nop 0\n"
    "v_cndmask_b32 v200, v202, v200, vcc\n"
    "s_nop 0\n"
    "v_pk_add_f32 v[232:233], v[224:225], v[226:227]\n"
    "v_add_f32 v232, v232, v233\n"
    "ds_write_addtid_b32 v232 offset:32768\n"
    "ds_read_b128 v[208:211], v201 offset:32768\n"
    "ds_read_b128 v[212:215], v201 offset:32784\n"
    "s_waitcnt lgkmcnt(1)\n"
    "v_pk_mul_f32 v[208:209], v[208:209], s[70:71]\n"
    "v_pk_mul_f32 v[210:211], v[210:211], s[70:71]\n"
    "v_cvt_pk_bf16_f32 v216, v208, v209\n"
    "v_cvt_pk_bf16_f32 v217, v210, v211\n"
    "s_waitcnt lgkmcnt(0)\n"
    "v_pk_mul_f32 v[212:213], v[212:213], s[70:71]\n"
    "v_pk_mul_f32 v[214:215], v[214:215], s[70:71]\n"
    "v_cvt_pk_bf16_f32 v218, v212, v213\n"
    "v_cvt_pk_bf16_f32 v219, v214, v215\n"
    "s_waitcnt vmcnt(0)\n"
    "buffer_store_dwordx4 v[216:219], v200, s[52:55], 0 offen sc0 sc1 nt\n"
    "s_endpgm\n"
    ".Ldin_A0s:\n"
    "v_mfma_f32_16x16x32_bf16 v[144:147], v[0:3], v[160:163], v[112:115]\n"
    "v_exp_f32 v128, v128 clamp\n"
    "v_mfma_f32_16x16x32_bf16 v[144:147], v[4:7], v[164:167], v[144:147]\n"
    "v_exp_f32 v129, v129 clamp\n"
    "v_mfma_f32_16x16x32_bf16 v[148:151], v[8:11], v[160:163], v[116:119]\n"
    "v_exp_f32 v130, v130 clamp\n"
    "v_mfma_f32_16x16x32_bf16 v[148:151], v[12:15], v[164:167], v[148:151]\n"
    "v_exp_f32 v131, v131 clamp\n"
    "v_mfma_f32_16x16x32_bf16 v[152:155], v[16:19], v[160:163], v[120:123]\n"
    "v_exp_f32 v132, v132 clamp\n"
    "v_mfma_f32_16x16x32_bf16 v[152:155], v[20:23], v[164:167], v[152:155]\n"
    "v_exp_f32 v133, v133 clamp\n"
    "v_mfma_f32_16x16x32_bf16 v[156:159], v[24:27], v[160:163], v[124:127]\n"
    "v_exp_f32 v134, v134 clamp\n"
    "v_mfma_f32_16x16x32_bf16 v[156:159], v[28:31], v[164:167], v[156:159]\n"
    "v_exp_f32 v135, v135 clamp\n"
    "v_exp_f32 v136, v136 clamp\n"
    "s_mov_b32 m0, s62\n"
    "v_exp_f32 v137, v137 clamp\n"
    "v_add_u32 v253, s67, v244\n"
    "s_waitcnt lgkmcnt(0)\n"
    "v_mfma_f32_4x4x4_16b_bf16 v[224:227], v[228:229], v[176:177], v[224:227] cbsz:4 abid:0\n"
    "v_exp_f32 v138, v138 clamp\n"
    "v_exp_f32 v139, v139 clamp\n"
    "v_mfma_f32_4x4x4_16b_bf16 v[224:227], v[228:229], v[178:179], v[224:227] cbsz:4 abid:1\n"
    "v_exp_f32 v140, v140 clamp\n"
    "v_exp_f32 v141, v141 clamp\n"
    "v_mfma_f32_4x4x4_16b_bf16 v[224:227], v[228:229], v[180:181], v[224:227] cbsz:4 abid:2\n"
    "v_exp_f32 v142, v142 clamp\n"
    "v_exp_f32 v143, v143 clamp\n"
    "v_mfma_f32_4x4x4_16b_bf16 v[224:227], v[228:229], v[182:183], v[224:227] cbsz:4 abid:3\n"
    "s_waitcnt vmcnt(4)\n"
    "ds_read_b64_tr_b16 v[176:177], v240 offset:2048\n"
    "ds_read_b64_tr_b16 v[178:179], v241 offset:2304\n"
    "ds_read_b64_tr_b16 v[180:181], v242 offset:2560\n"
    "ds_read_b64_tr_b16 v[182:183], v243 offset:2816\n"
    "ds_read_b32 v236, v253 offset:0\n"
    "ds_read_b32 v238, v253 offset:4\n"
    "ds_read_b128 v[160:163], v234 offset:4096\n"
    "ds_read_b128 v[164:167], v235 offset:4096\n"
    "s_cbranch_vccz .Ldin_A0s_np\n"
    "s_setprio 1\n"
    ".Ldin_A0s_np:\n"
    "v_pk_fma_f32 v[128:129], v[128:129], s[70:71], v[96:97]\n"
    "v_pk_fma_f32 v[130:131], v[130:131], s[70:71], v[98:99]\n"
    "v_pk_add_f32 v[190:191], v[224:225], v[226:227]\n"
    "v_add_f32 v190, v190, v191\n"
    "ds_write_addtid_b32 v190 offset:32768\n"
    "s_add_u32 m0, s68, 2048\n"
    "s_bfe_u32 s94, s59, 0x180006\n"
    "s_mul_i32 s86, s94, s56\n"
    "v_mbcnt_lo_u32_b32 v184, s20, 0\n"
    "v_mbcnt_lo_u32_b32 v185, s22, 0\n"
    "v_mbcnt_lo_u32_b32 v186, s24, 0\n"
    "v_mbcnt_lo_u32_b32 v187, s26, 0\n"
    "s_bcnt1_i32_b64 s90, s[20:21]\n"
    "s_bcnt1_i32_b64 s91, s[22:23]\n"
    "s_bcnt1_i32_b64 s92, s[24:25]\n"
    "s_bcnt1_i32_b64 s93, s[26:27]\n"
    "v_mbcnt_hi_u32_b32 v184, s21, v184\n"
    "v_mbcnt_hi_u32_b32 v185, s23, v185\n"
    "v_mbcnt_hi_u32_b32 v186, s25, v186\n"
    "v_mbcnt_hi_u32_b32 v187, s27, v187\n"
    "s_lshl2_add_u32 s90, s90, s65\n"
    "s_lshl2_add_u32 s91, s91, s90\n"
    "s_lshl2_add_u32 s92, s92, s91\n"
    "s_lshl2_add_u32 s93, s93, s92\n"
    "v_add_u32_e64 v188, s86, v251\n"
    "v_pk_add_f32 v[230:231], v[128:129], v[130:131]\n"
    "v_pk_mul_f32 v[232:233], v[128:129], v[130:131]\n"
    "v_pk_fma_f32 v[132:133], v[132:133], s[70:71], v[100:101]\n"
    "v_pk_fma_f32 v[230:231], v[230:231], v[132:133], v[232:233]\n"
    "v_pk_mul_f32 v[232:233], v[232:233], v[132:133]\n"
    "v_lshl_add_u32 v184, v184, 2, s65\n"
    "v_lshl_add_u32 v185, v185, 2, s90\n"
    "v_lshl_add_u32 v186, v186, 2, s91\n"
    "v_lshl_add_u32 v187, v187, 2, s92\n"
    "v_mov_b32 v191, -1\n"
    "v_lshl_add_u32 v190, v249, 1, s93\n"
    "ds_write2_b32 v190, v191, v191 offset1:64\n"
    "s_mov_b64 exec, s[20:21]\n"
    "ds_write_b32 v184, v188\n"
    "s_mov_b64 exec, s[22:23]\n"
    "v_add_u32 v189, 64, v188\n"
    "ds_write_b32 v185, v189\n"
    "s_mov_b64 exec, s[24:25]\n"
    "v_add_u32 v190, 128, v188\n"
    "ds_write_b32 v186, v190\n"
    "s_mov_b64 exec, s[26:27]\n"
    "v_add_u32 v189, 192, v188\n"
    "ds_write_b32 v187, v189\n"
    "s_mov_b64 exec, -1\n"
    "s_add_u32 s95, s93, 60\n"
    "s_add_u32 s94, s82, 128\n"
    "s_andn2_b32 s95, s95, 63\n"
    "s_max_u32 s83, s95, s94\n"
    "v_pk_fma_f32 v[134:135], v[134:135], s[70:71], v[102:103]\n"
    "v_pk_fma_f32 v[230:231], v[230:231], v[134:135], v[232:233]\n"
    "v_pk_mul_f32 v[232:233], v[232:233], v[134:135]\n"
    "v_pk_fma_f32 v[136:137], v[136:137], s[70:71], v[104:105]\n"
    "v_pk_fma_f32 v[230:231], v[230:231], v[136:137], v[232:233]\n"
    "v_pk_mul_f32 v[232:233], v[232:233], v[136:137]\n"
    "v_pk_fma_f32 v[138:139], v[138:139], s[70:71], v[106:107]\n"
    "v_pk_fma_f32 v[230:231], v[230:231], v[138:139], v[232:233]\n"
    "v_pk_mul_f32 v[232:233], v[232:233], v[138:139]\n"
    "v_pk_fma_f32 v[140:141], v[140:141], s[70:71], v[108:109]\n"
    "v_pk_fma_f32 v[230:231], v[230:231], v[140:141], v[232:233]\n"
    "v_pk_mul_f32 v[232:233], v[232:233], v[140:141]\n"
    "v_pk_fma_f32 v[142:143], v[142:143], s[70:71], v[110:111]\n"
    "v_pk_fma_f32 v[230:231], v[230:231], v[142:143], v[232:233]\n"
    "v_pk_mul_f32 v[232:233], v[232:233], v[142:143]\n"
    "v_rcp_f32 v232, v232\n"
    "v_rcp_f32 v233, v233\n"
    "v_fma_f32 v230, v230, v232, v247\n"
    "v_fmac_f32 v230, v231, v233\n"
    "v_lshlrev_b32 v232, 2, v249\n"
    "v_lshrrev_b32 v233, 2, v249\n"
    "v_and_b32 v232, 56, v232\n"
    "v_and_b32 v233, 4, v233\n"
    "v_add3_u32 v232, v232, v233, s81\n"
    "ds_read_b32 v232, v232\n"
    "s_add_u32 s81, s81, 64\n"
    "s_waitcnt lgkmcnt(0)\n"
    "v_lshlrev_b32 v232, 1, v232\n"
    "buffer_load_ushort v232, v232, s[44:47], 0 offen\n"
    "s_waitcnt vmcnt(0)\n"
    "v_lshlrev_b32 v232, 16, v232\n"
    "v_mul_f32 v230, v230, v232\n"
    "v_cvt_pk_bf16_f32 v230, v230, v230\n"
    "s_add_u32 s67, s67, 64\n"
    "s_waitcnt lgkmcnt(0)\n"
    "ds_write_b16 v245, v230\n"
    "ds_read_b64 v[228:229], v246\n"
    "buffer_load_dwordx4 v[236:237], s[40:43], 0 idxen offen offset:0 nt lds\n"
    "buffer_load_dwordx4 v[238:239], s[40:43], 0 idxen offen offset:1024 nt lds\n"
    ".Ldin_B1s:\n"
    "v_mfma_f32_16x16x32_bf16 v[128:131], v[0:3], v[160:163], v[112:115]\n"
    "v_exp_f32 v144, v144 clamp\n"
    "v_mfma_f32_16x16x32_bf16 v[128:131], v[4:7], v[164:167], v[128:131]\n"
    "v_exp_f32 v145, v145 clamp\n"
    "v_mfma_f32_16x16x32_bf16 v[132:135], v[8:11], v[160:163], v[116:119]\n"
    "v_exp_f32 v146, v146 clamp\n"
    "v_mfma_f32_16x16x32_bf16 v[132:135], v[12:15], v[164:167], v[132:135]\n"
    "v_exp_f32 v147, v147 clamp\n"
    "v_mfma_f32_16x16x32_bf16 v[136:139], v[16:19], v[160:163], v[120:123]\n"
    "v_exp_f32 v148, v148 clamp\n"
    "v_mfma_f32_16x16x32_bf16 v[136:139], v[20:23], v[164:167], v[136:139]\n"
    "v_exp_f32 v149, v149 clamp\n"
    "v_mfma_f32_16x16x32_bf16 v[140:143], v[24:27], v[160:163], v[124:127]\n"
    "v_exp_f32 v150, v150 clamp\n"
    "v_mfma_f32_16x16x32_bf16 v[140:143], v[28:31], v[164:167], v[140:143]\n"
    "v_exp_f32 v151, v151 clamp\n"
    "v_exp_f32 v152, v152 clamp\n"
    "s_cmp_eq_u32 s67, s80\n"
    "s_cselect_b32 s67, s65, s67\n"
    "s_and_b32 s94, s59, 63\n"
    "s_mul_i32 s94, s94, 272\n"
    "v_add_u32 v252, s94, v250\n"
    "s_add_u32 m0, s68, 4096\n"
    "v_exp_f32 v153, v153 clamp\n"
    "v_add_u32 v253, s67, v244\n"
    "s_waitcnt lgkmcnt(0)\n"
    "v_mfma_f32_4x4x4_16b_bf16 v[224:227], v[228:229], v[168:169], 0 cbsz:4 abid:0\n"
    "v_exp_f32 v154, v154 clamp\n"
    "v_exp_f32 v155, v155 clamp\n"
    "v_mfma_f32_4x4x4_16b_bf16 v[224:227], v[228:229], v[170:171], v[224:227] cbsz:4 abid:1\n"
    "v_exp_f32 v156, v156 clamp\n"
    "v_exp_f32 v157, v157 clamp\n"
    "v_mfma_f32_4x4x4_16b_bf16 v[224:227], v[228:229], v[172:173], v[224:227] cbsz:4 abid:2\n"
    "v_exp_f32 v158, v158 clamp\n"
    "v_exp_f32 v159, v159 clamp\n"
    "v_mfma_f32_4x4x4_16b_bf16 v[224:227], v[228:229], v[174:175], v[224:227] cbsz:4 abid:3\n"
    "s_waitcnt vmcnt(4)\n"
    "s_memrealtime s[34:35]\n"
    "ds_read_b64_tr_b16 v[168:169], v240 offset:4096\n"
    "ds_read_b64_tr_b16 v[170:171], v241 offset:4352\n"
    "ds_read_b64_tr_b16 v[172:173], v242 offset:4608\n"
    "ds_read_b64_tr_b16 v[174:175], v243 offset:4864\n"
    "ds_read_b32 v236, v253 offset:0\n"
    "ds_read_b32 v238, v253 offset:4\n"
    "ds_read_b128 v[160:163], v234 offset:6144\n"
    "ds_read_b128 v[164:167], v235 offset:6144\n"
    "v_add_u32_e64 v184, v248, v252\n"
    "ds_read_b64 v[184:185], v184\n"
    "v_pk_fma_f32 v[144:145], v[144:145], s[70:71], v[96:97]\n"
    "v_pk_fma_f32 v[146:147], v[146:147], s[70:71], v[98:99]\n"
    "v_pk_add_f32 v[230:231], v[144:145], v[146:147]\n"
    "v_pk_mul_f32 v[232:233], v[144:145], v[146:147]\n"
    "v_pk_fma_f32 v[148:149], v[148:149], s[70:71], v[100:101]\n"
    "v_pk_fma_f32 v[230:231], v[230:231], v[148:149], v[232:233]\n"
    "v_pk_mul_f32 v[232:233], v[232:233], v[148:149]\n"
    "v_pk_fma_f32 v[150:151], v[150:151], s[70:71], v[102:103]\n"
    "v_pk_fma_f32 v[230:231], v[230:231], v[150:151], v[232:233]\n"
    "v_pk_mul_f32 v[232:233], v[232:233], v[150:151]\n"
    "v_pk_fma_f32 v[152:153], v[152:153], s[70:71], v[104:105]\n"
    "v_pk_fma_f32 v[230:231], v[230:231], v[152:153], v[232:233]\n"
    "v_pk_mul_f32 v[232:233], v[232:233], v[152:153]\n"
    "v_pk_fma_f32 v[154:155], v[154:155], s[70:71], v[106:107]\n"
    "v_pk_fma_f32 v[230:231], v[230:231], v[154:155], v[232:233]\n"
    "v_pk_mul_f32 v[232:233], v[232:233], v[154:155]\n"
    "v_pk_fma_f32 v[156:157], v[156:157], s[70:71], v[108:109]\n"
    "v_pk_fma_f32 v[230:231], v[230:231], v[156:157], v[232:233]\n"
    "v_pk_mul_f32 v[232:233], v[232:233], v[156:157]\n"
    "v_pk_fma_f32 v[158:159], v[158:159], s[70:71], v[110:111]\n"
    "v_pk_fma_f32 v[230:231], v[230:231], v[158:159], v[232:233]\n"
    "v_pk_mul_f32 v[232:233], v[232:233], v[158:159]\n"
    "v_rcp_f32 v232, v232\n"
    "v_rcp_f32 v233, v233\n"
    "v_fma_f32 v230, v230, v232, v247\n"
    "v_fmac_f32 v230, v231, v233\n"
    "v_lshlrev_b32 v232, 2, v249\n"
    "v_lshrrev_b32 v233, 2, v249\n"
    "v_and_b32 v232, 56, v232\n"
    "v_and_b32 v233, 4, v233\n"
    "v_add3_u32 v232, v232, v233, s81\n"
    "ds_read_b32 v232, v232\n"
    "s_add_u32 s81, s81, 64\n"
    "s_waitcnt lgkmcnt(0)\n"
    "v_lshlrev_b32 v232, 1, v232\n"
    "buffer_load_ushort v232, v232, s[44:47], 0 offen\n"
    "s_waitcnt vmcnt(0)\n"
    "v_lshlrev_b32 v232, 16, v232\n"
    "v_mul_f32 v230, v230, v232\n"
    "v_cvt_pk_bf16_f32 v230, v230, v230\n"
    "s_add_u32 s67, s67, 64\n"
    "s_waitcnt lgkmcnt(0)\n"
    "ds_write_b16 v245, v230\n"
    "ds_read_b64 v[228:229], v246\n"
    "v_and_b32 v184, v254, v184\n"
    "v_and_b32 v185, v255, v185\n"
    "s_cmp_le_u32 s34, s76\n"
    "s_cbranch_scc1 .Ldin_B1s_wait\n"
    ".Ldin_B1s_go:\n"
    "s_sub_u32 s94, s34, 60\n"
    "s_max_u32 s76, s76, s94\n"
    "s_add_u32 s76, s76, s72\n"
    ".Ldin_B1s_dma:\n"
    "buffer_load_dwordx4 v[236:237], s[40:43], 0 idxen offen offset:0 nt lds\n"
    "buffer_load_dwordx4 v[238:239], s[40:43], 0 idxen offen offset:1024 nt lds\n"
    ".Ldin_B1s_nd:\n"
    ".Ldin_G2s:\n"
    "v_mfma_f32_16x16x32_bf16 v[144:147], v[0:3], v[160:163], v[112:115]\n"
    "v_exp_f32 v128, v128 clamp\n"
    "v_mfma_f32_16x16x32_bf16 v[144:147], v[4:7], v[164:167], v[144:147]\n"
    "v_exp_f32 v129, v129 clamp\n"
    "v_mfma_f32_16x16x32_bf16 v[148:151], v[8:11], v[160:163], v[116:119]\n"
    "v_exp_f32 v130, v130 clamp\n"
    "v_mfma_f32_16x16x32_bf16 v[148:151], v[12:15], v[164:167], v[148:151]\n"
    "v_exp_f32 v131, v131 clamp\n"
    "v_mfma_f32_16x16x32_bf16 v[152:155], v[16:19], v[160:163], v[120:123]\n"
    "v_exp_f32 v132, v132 clamp\n"
    "v_mfma_f32_16x16x32_bf16 v[152:155], v[20:23], v[164:167], v[152:155]\n"
    "v_exp_f32 v133, v133 clamp\n"
    "v_mfma_f32_16x16x32_bf16 v[156:159], v[24:27], v[160:163], v[124:127]\n"
    "v_exp_f32 v134, v134 clamp\n"
    "v_mfma_f32_16x16x32_bf16 v[156:159], v[28:31], v[164:167], v[156:159]\n"
    "v_exp_f32 v135, v135 clamp\n"
    "v_exp_f32 v136, v136 clamp\n"
    "s_cmp_eq_u32 s67, s80\n"
    "s_cselect_b32 s67, s65, s67\n"
    "s_add_u32 m0, s68, 6144\n"
    "v_exp_f32 v137, v137 clamp\n"
    "v_add_u32_e64 v253, s67, v244\n"
    "s_waitcnt lgkmcnt(0)\n"
    "v_mfma_f32_4x4x4_16b_bf16 v[224:227], v[228:229], v[176:177], v[224:227] cbsz:4 abid:0\n"
    "v_exp_f32 v138, v138 clamp\n"
    "v_exp_f32 v139, v139 clamp\n"
    "v_mfma_f32_4x4x4_16b_bf16 v[224:227], v[228:229], v[178:179], v[224:227] cbsz:4 abid:1\n"
    "v_exp_f32 v140, v140 clamp\n"
    "v_exp_f32 v141, v141 clamp\n"
    "v_mfma_f32_4x4x4_16b_bf16 v[224:227], v[228:229], v[180:181], v[224:227] cbsz:4 abid:2\n"
    "v_exp_f32 v142, v142 clamp\n"
    "v_exp_f32 v143, v143 clamp\n"
    "v_mfma_f32_4x4x4_16b_bf16 v[224:227], v[228:229], v[182:183], v[224:227] cbsz:4 abid:3\n"
    "s_waitcnt vmcnt(4)\n"
    "s_memrealtime s[34:35]\n"
    "ds_read_b64_tr_b16 v[176:177], v240 offset:6144\n"
    "ds_read_b64_tr_b16 v[178:179], v241 offset:6400\n"
    "ds_read_b64_tr_b16 v[180:181], v242 offset:6656\n"
    "ds_read_b64_tr_b16 v[182:183], v243 offset:6912\n"
    "ds_read_b32 v236, v253 offset:0\n"
    "ds_read_b32 v238, v253 offset:4\n"
    "ds_read_b128 v[160:163], v234 offset:0\n"
    "ds_read_b128 v[164:167], v235 offset:0\n"
    "v_pk_fma_f32 v[128:129], v[128:129], s[70:71], v[96:97]\n"
    "v_pk_fma_f32 v[130:131], v[130:131], s[70:71], v[98:99]\n"
    "v_pk_add_f32 v[230:231], v[128:129], v[130:131]\n"
    "v_pk_mul_f32 v[232:233], v[128:129], v[130:131]\n"
    "v_pk_fma_f32 v[132:133], v[132:133], s[70:71], v[100:101]\n"
    "v_pk_fma_f32 v[230:231], v[230:231], v[132:133], v[232:233]\n"
    "v_pk_mul_f32 v[232:233], v[232:233], v[132:133]\n"
    "v_pk_fma_f32 v[134:135], v[134:135], s[70:71], v[102:103]\n"
    "v_pk_fma_f32 v[230:231], v[230:231], v[134:135], v[232:233]\n"
    "v_pk_mul_f32 v[232:233], v[232:233], v[134:135]\n"
    "v_pk_fma_f32 v[136:137], v[136:137], s[70:71], v[104:105]\n"
    "v_pk_fma_f32 v[230:231], v[230:231], v[136:137], v[232:233]\n"
    "v_pk_mul_f32 v[232:233], v[232:233], v[136:137]\n"
    "v_pk_fma_f32 v[138:139], v[138:139], s[70:71], v[106:107]\n"
    "v_pk_fma_f32 v[230:231], v[230:231], v[138:139], v[232:233]\n"
    "v_pk_mul_f32 v[232:233], v[232:233], v[138:139]\n"
    "v_pk_fma_f32 v[140:141], v[140:141], s[70:71], v[108:109]\n"
    "v_pk_fma_f32 v[230:231], v[230:231], v[140:141], v[232:233]\n"
    "v_pk_mul_f32 v[232:233], v[232:233], v[140:141]\n"
    "v_pk_fma_f32 v[142:143], v[142:143], s[70:71], v[110:111]\n"
    "v_pk_fma_f32 v[230:231], v[230:231], v[142:143], v[232:233]\n"
    "v_pk_mul_f32 v[232:233], v[232:233], v[142:143]\n"
    "v_rcp_f32 v232, v232\n"
    "v_rcp_f32 v233, v233\n"
    "v_fma_f32 v230, v230, v232, v247\n"
    "v_fmac_f32 v230, v231, v233\n"
    "v_lshlrev_b32 v232, 2, v249\n"
    "v_lshrrev_b32 v233, 2, v249\n"
    "v_and_b32 v232, 56, v232\n"
    "v_and_b32 v233, 4, v233\n"
    "v_add3_u32 v232, v232, v233, s81\n"
    "ds_read_b32 v232, v232\n"
    "s_add_u32 s81, s81, 64\n"
    "s_waitcnt lgkmcnt(0)\n"
    "v_lshlrev_b32 v232, 1, v232\n"
    "buffer_load_ushort v232, v232, s[44:47], 0 offen\n"
    "s_waitcnt vmcnt(0)\n"
    "v_lshlrev_b32 v232, 16, v232\n"
    "v_mul_f32 v230, v230, v232\n"
    "v_cvt_pk_bf16_f32 v230, v230, v230\n"
    "s_add_u32 s67, s67, 64\n"
    "s_waitcnt lgkmcnt(0)\n"
    "ds_write_b16 v245, v230\n"
    "ds_read_b64 v[228:229], v246\n"
    "s_cmp_le_u32 s34, s76\n"
    "s_cbranch_scc1 .Ldin_G2s_wait\n"
    ".Ldin_G2s_go:\n"
    "s_sub_u32 s94, s34, 60\n"
    "s_max_u32 s76, s76, s94\n"
    "s_add_u32 s76, s76, s72\n"
    ".Ldin_G2s_dma:\n"
    "buffer_load_dwordx4 v[236:237], s[40:43], 0 idxen offen offset:0 nt lds\n"
    "buffer_load_dwordx4 v[238:239], s[40:43], 0 idxen offen offset:1024 nt lds\n"
    ".Ldin_G2s_nd:\n"
    "s_cmp_eq_u32 s67, s82\n"
    "s_cbranch_scc1 .Ldin_L3s\n"
    ".Ldin_G3s:\n"
    "v_mfma_f32_16x16x32_bf16 v[128:131], v[0:3], v[160:163], v[112:115]\n"
    "v_exp_f32 v144, v144 clamp\n"
    "v_mfma_f32_16x16x32_bf16 v[128:131], v[4:7], v[164:167], v[128:131]\n"
    "v_exp_f32 v145, v145 clamp\n"
    "v_mfma_f32_16x16x32_bf16 v[132:135], v[8:11], v[160:163], v[116:119]\n"
    "v_exp_f32 v146, v146 clamp\n"
    "v_mfma_f32_16x16x32_bf16 v[132:135], v[12:15], v[164:167], v[132:135]\n"
    "v_exp_f32 v147, v147 clamp\n"
    "v_mfma_f32_16x16x32_bf16 v[136:139], v[16:19], v[160:163], v[120:123]\n"
    "v_exp_f32 v148, v148 clamp\n"
    "v_mfma_f32_16x16x32_bf16 v[136:139], v[20:23], v[164:167], v[136:139]\n"
    "v_exp_f32 v149, v149 clamp\n"
    "v_mfma_f32_16x16x32_bf16 v[140:143], v[24:27], v[160:163], v[124:127]\n"
    "v_exp_f32 v150, v150 clamp\n"
    "v_mfma_f32_16x16x32_bf16 v[140:143], v[28:31], v[164:167], v[140:143]\n"
    "v_exp_f32 v151, v151 clamp\n"
    "v_exp_f32 v152, v152 clamp\n"
    "s_cmp_eq_u32 s67, s80\n"
    "s_cselect_b32 s67, s65, s67\n"
    "s_add_u32 m0, s68, 0\n"
    "v_exp_f32 v153, v153 clamp\n"
    "v_add_u32 v253, s67, v244\n"
    "s_waitcnt lgkmcnt(0)\n"
    "v_mfma_f32_4x4x4_16b_bf16 v[224:227], v[228:229], v[168:169], v[224:227] cbsz:4 abid:0\n"
    "v_exp_f32 v154, v154 clamp\n"
    "v_exp_f32 v155, v155 clamp\n"
    "v_mfma_f32_4x4x4_16b_bf16 v[224:227], v[228:229], v[170:171], v[224:227] cbsz:4 abid:1\n"
    "v_exp_f32 v156, v156 clamp\n"
    "v_exp_f32 v157, v157 clamp\n"
    "v_mfma_f32_4x4x4_16b_bf16 v[224:227], v[228:229], v[172:173], v[224:227] cbsz:4 abid:2\n"
    "v_exp_f32 v158, v158 clamp\n"
    "v_exp_f32 v159, v159 clamp\n"
    "v_mfma_f32_4x4x4_16b_bf16 v[224:227], v[228:229], v[174:175], v[224:227] cbsz:4 abid:3\n"
    "s_waitcnt vmcnt(4)\n"
    "s_memrealtime s[34:35]\n"
    "ds_read_b64_tr_b16 v[168:169], v240 offset:0\n"
    "ds_read_b64_tr_b16 v[170:171], v241 offset:256\n"
    "ds_read_b64_tr_b16 v[172:173], v242 offset:512\n"
    "ds_read_b64_tr_b16 v[174:175], v243 offset:768\n"
    "ds_read_b32 v236, v253 offset:0\n"
    "ds_read_b32 v238, v253 offset:4\n"
    "ds_read_b128 v[160:163], v234 offset:2048\n"
    "ds_read_b128 v[164:167], v235 offset:2048\n"
    "v_pk_fma_f32 v[144:145], v[144:145], s[70:71], v[96:97]\n"
    "v_pk_fma_f32 v[146:147], v[146:147], s[70:71], v[98:99]\n"
    "v_pk_add_f32 v[230:231], v[144:145], v[146:147]\n"
    "v_pk_mul_f32 v[232:233], v[144:145], v[146:147]\n"
    "v_pk_fma_f32 v[148:149], v[148:149], s[70:71], v[100:101]\n"
    "v_pk_fma_f32 v[230:231], v[230:231], v[148:149], v[232:233]\n"
    "v_pk_mul_f32 v[232:233], v[232:233], v[148:149]\n"
    "v_pk_fma_f32 v[150:151], v[150:151], s[70:71], v[102:103]\n"
    "v_pk_fma_f32 v[230:231], v[230:231], v[150:151], v[232:233]\n"
    "v_pk_mul_f32 v[232:233], v[232:233], v[150:151]\n"
    "v_pk_fma_f32 v[152:153], v[152:153], s[70:71], v[104:105]\n"
    "v_pk_fma_f32 v[230:231], v[230:231], v[152:153], v[232:233]\n"
    "v_pk_mul_f32 v[232:233], v[232:233], v[152:153]\n"
    "v_pk_fma_f32 v[154:155], v[154:155], s[70:71], v[106:107]\n"
    "v_pk_fma_f32 v[230:231], v[230:231], v[154:155], v[232:233]\n"
    "v_pk_mul_f32 v[232:233], v[232:233], v[154:155]\n"
    "v_pk_fma_f32 v[156:157], v[156:157], s[70:71], v[108:109]\n"
    "v_pk_fma_f32 v[230:231], v[230:231], v[156:157], v[232:233]\n"
    "v_pk_mul_f32 v[232:233], v[232:233], v[156:157]\n"
    "v_pk_fma_f32 v[158:159], v[158:159], s[70:71], v[110:111]\n"
    "v_pk_fma_f32 v[230:231], v[230:231], v[158:159], v[232:233]\n"
    "v_pk_mul_f32 v[232:233], v[232:233], v[158:159]\n"
    "v_rcp_f32 v232, v232\n"
    "v_rcp_f32 v233, v233\n"
    "v_fma_f32 v230, v230, v232, v247\n"
    "v_fmac_f32 v230, v231, v233\n"
    "v_lshlrev_b32 v232, 2, v249\n"
    "v_lshrrev_b32 v233, 2, v249\n"
    "v_and_b32 v232, 56, v232\n"
    "v_and_b32 v233, 4, v233\n"
    "v_add3_u32 v232, v232, v233, s81\n"
    "ds_read_b32 v232, v232\n"
    "s_add_u32 s81, s81, 64\n"
    "s_waitcnt lgkmcnt(0)\n"
    "v_lshlrev_b32 v232, 1, v232\n"
    "buffer_load_ushort v232, v232, s[44:47], 0 offen\n"
    "s_waitcnt vmcnt(0)\n"
    "v_lshlrev_b32 v232, 16, v232\n"
    "v_mul_f32 v230, v230, v232\n"
    "v_cvt_pk_bf16_f32 v230, v230, v230\n"
    "s_add_u32 s67, s67, 64\n"
    "s_waitcnt lgkmcnt(0)\n"
    "ds_write_b16 v245, v230\n"
    "ds_read_b64 v[228:229], v246\n"
    "s_cmp_le_u32 s34, s76\n"
    "s_cbranch_scc1 .Ldin_G3s_wait\n"
    ".Ldin_G3s_go:\n"
    "s_add_u32 s76, s76, s72\n"
    ".Ldin_G3s_dma:\n"
    "buffer_load_dwordx4 v[236:237], s[40:43], 0 idxen offen offset:0 nt lds\n"
    "buffer_load_dwordx4 v[238:239], s[40:43], 0 idxen offen offset:1024 nt lds\n"
    ".Ldin_G3s_nd:\n"
    "s_cmp_eq_u32 s67, s82\n"
    "s_cbranch_scc1 .Ldin_L0s\n"
    ".Ldin_G0s:\n"
    "v_mfma_f32_16x16x32_bf16 v[144:147], v[0:3], v[160:163], v[112:115]\n"
    "v_exp_f32 v128, v128 clamp\n"
    "v_mfma_f32_16x16x32_bf16 v[144:147], v[4:7], v[164:167], v[144:147]\n"
    "v_exp_f32 v129, v129 clamp\n"
    "v_mfma_f32_16x16x32_bf16 v[148:151], v[8:11], v[160:163], v[116:119]\n"
    "v_exp_f32 v130, v130 clamp\n"
    "v_mfma_f32_16x16x32_bf16 v[148:151], v[12:15], v[164:167], v[148:151]\n"
    "v_exp_f32 v131, v131 clamp\n"
    "v_mfma_f32_16x16x32_bf16 v[152:155], v[16:19], v[160:163], v[120:123]\n"
    "v_exp_f32 v132, v132 clamp\n"
    "v_mfma_f32_16x16x32_bf16 v[152:155], v[20:23], v[164:167], v[152:155]\n"
    "v_exp_f32 v133, v133 clamp\n"
    "v_mfma_f32_16x16x32_bf16 v[156:159], v[24:27], v[160:163], v[124:127]\n"
    "v_exp_f32 v134, v134 clamp\n"
    "v_mfma_f32_16x16x32_bf16 v[156:159], v[28:31], v[164:167], v[156:159]\n"
    "v_exp_f32 v135, v135 clamp\n"
    "v_exp_f32 v136, v136 clamp\n"
    "s_cmp_eq_u32 s67, s80\n"
    "s_cselect_b32 s67, s65, s67\n"
    "s_add_u32 m0, s68, 2048\n"
    "v_exp_f32 v137, v137 clamp\n"
    "v_add_u32_e64 v253, s67, v244\n"
    "s_waitcnt lgkmcnt(0)\n"
    "v_mfma_f32_4x4x4_16b_bf16 v[224:227], v[228:229], v[176:177], v[224:227] cbsz:4 abid:0\n"
    "v_exp_f32 v138, v138 clamp\n"
    "v_exp_f32 v139, v139 clamp\n"
    "v_mfma_f32_4x4x4_16b_bf16 v[224:227], v[228:229], v[178:179], v[224:227] cbsz:4 abid:1\n"
    "v_exp_f32 v140, v140 clamp\n"
    "v_exp_f32 v141, v141 clamp\n"
    "v_mfma_f32_4x4x4_16b_bf16 v[224:227], v[228:229], v[180:181], v[224:227] cbsz:4 abid:2\n"
    "v_exp_f32 v142, v142 clamp\n"
    "v_exp_f32 v143, v143 clamp\n"
    "v_mfma_f32_4x4x4_16b_bf16 v[224:227], v[228:229], v[182:183], v[224:227] cbsz:4 abid:3\n"
    "s_waitcnt vmcnt(4)\n"
    "s_memrealtime s[34:35]\n"
    "ds_read_b64_tr_b16 v[176:177], v240 offset:2048\n"
    "ds_read_b64_tr_b16 v[178:179], v241 offset:2304\n"
    "ds_read_b64_tr_b16 v[180:181], v242 offset:2560\n"
    "ds_read_b64_tr_b16 v[182:183], v243 offset:2816\n"
    "ds_read_b32 v236, v253 offset:0\n"
    "ds_read_b32 v238, v253 offset:4\n"
    "ds_read_b128 v[160:163], v234 offset:4096\n"
    "ds_read_b128 v[164:167], v235 offset:4096\n"
    "v_pk_fma_f32 v[128:129], v[128:129], s[70:71], v[96:97]\n"
    "v_pk_fma_f32 v[130:131], v[130:131], s[70:71], v[98:99]\n"
    "v_pk_add_f32 v[230:231], v[128:129], v[130:131]\n"
    "v_pk_mul_f32 v[232:233], v[128:129], v[130:131]\n"
    "v_pk_fma_f32 v[132:133], v[132:133], s[70:71], v[100:101]\n"
    "v_pk_fma_f32 v[230:231], v[230:231], v[132:133], v[232:233]\n"
    "v_pk_mul_f32 v[232:233], v[232:233], v[132:133]\n"
    "v_pk_fma_f32 v[134:135], v[134:135], s[70:71], v[102:103]\n"
    "v_pk_fma_f32 v[230:231], v[230:231], v[134:135], v[232:233]\n"
    "v_pk_mul_f32 v[232:233], v[232:233], v[134:135]\n"
    "v_pk_fma_f32 v[136:137], v[136:137], s[70:71], v[104:105]\n"
    "v_pk_fma_f32 v[230:231], v[230:231], v[136:137], v[232:233]\n"
    "v_pk_mul_f32 v[232:233], v[232:233], v[136:137]\n"
    "v_pk_fma_f32 v[138:139], v[138:139], s[70:71], v[106:107]\n"
    "v_pk_fma_f32 v[230:231], v[230:231], v[138:139], v[232:233]\n"
    "v_pk_mul_f32 v[232:233], v[232:233], v[138:139]\n"
    "v_pk_fma_f32 v[140:141], v[140:141], s[70:71], v[108:109]\n"
    "v_pk_fma_f32 v[230:231], v[230:231], v[140:141], v[232:233]\n"
    "v_pk_mul_f32 v[232:233], v[232:233], v[140:141]\n"
    "v_pk_fma_f32 v[142:143], v[142:143], s[70:71], v[110:111]\n"
    "v_pk_fma_f32 v[230:231], v[230:231], v[142:143], v[232:233]\n"
    "v_pk_mul_f32 v[232:233], v[232:233], v[142:143]\n"
    "v_rcp_f32 v232, v232\n"
    "v_rcp_f32 v233, v233\n"
    "v_fma_f32 v230, v230, v232, v247\n"
    "v_fmac_f32 v230, v231, v233\n"
    "v_lshlrev_b32 v232, 2, v249\n"
    "v_lshrrev_b32 v233, 2, v249\n"
    "v_and_b32 v232, 56, v232\n"
    "v_and_b32 v233, 4, v233\n"
    "v_add3_u32 v232, v232, v233, s81\n"
    "ds_read_b32 v232, v232\n"
    "s_add_u32 s81, s81, 64\n"
    "s_waitcnt lgkmcnt(0)\n"
    "v_lshlrev_b32 v232, 1, v232\n"
    "buffer_load_ushort v232, v232, s[44:47], 0 offen\n"
    "s_waitcnt vmcnt(0)\n"
    "v_lshlrev_b32 v232, 16, v232\n"
    "v_mul_f32 v230, v230, v232\n"
    "v_cvt_pk_bf16_f32 v230, v230, v230\n"
    "s_add_u32 s67, s67, 64\n"
    "s_waitcnt lgkmcnt(0)\n"
    "ds_write_b16 v245, v230\n"
    "ds_read_b64 v[228:229], v246\n"
    "s_cmp_le_u32 s34, s76\n"
    "s_cbranch_scc1 .Ldin_G0s_wait\n"
    ".Ldin_G0s_go:\n"
    "s_sub_u32 s94, s34, 60\n"
    "s_max_u32 s76, s76, s94\n"
    "s_add_u32 s76, s76, s72\n"
    ".Ldin_G0s_dma:\n"
    "buffer_load_dwordx4 v[236:237], s[40:43], 0 idxen offen offset:0 nt lds\n"
    "buffer_load_dwordx4 v[238:239], s[40:43], 0 idxen offen offset:1024 nt lds\n"
    ".Ldin_G0s_nd:\n"
    "s_cmp_eq_u32 s67, s82\n"
    "s_cbranch_scc1 .Ldin_L1s\n"
    ".Ldin_G1s:\n"
    "v_mfma_f32_16x16x32_bf16 v[128:131], v[0:3], v[160:163], v[112:115]\n"
    "v_exp_f32 v144, v144 clamp\n"
    "v_mfma_f32_16x16x32_bf16 v[128:131], v[4:7], v[164:167], v[128:131]\n"
    "v_exp_f32 v145, v145 clamp\n"
    "v_mfma_f32_16x16x32_bf16 v[132:135], v[8:11], v[160:163], v[116:119]\n"
    "v_exp_f32 v146, v146 clamp\n"
    "v_mfma_f32_16x16x32_bf16 v[132:135], v[12:15], v[164:167], v[132:135]\n"
    "v_exp_f32 v147, v147 clamp\n"
    "v_mfma_f32_16x16x32_bf16 v[136:139], v[16:19], v[160:163], v[120:123]\n"
    "v_exp_f32 v148, v148 clamp\n"
    "v_mfma_f32_16x16x32_bf16 v[136:139], v[20:23], v[164:167], v[136:139]\n"
    "v_exp_f32 v149, v149 clamp\n"
    "v_mfma_f32_16x16x32_bf16 v[140:143], v[24:27], v[160:163], v[124:127]\n"
    "v_exp_f32 v150, v150 clamp\n"
    "v_mfma_f32_16x16x32_bf16 v[140:143], v[28:31], v[164:167], v[140:143]\n"
    "v_exp_f32 v151, v151 clamp\n"
    "v_exp_f32 v152, v152 clamp\n"
    "s_cmp_eq_u32 s67, s80\n"
    "s_cselect_b32 s67, s65, s67\n"
    "s_add_u32 m0, s68, 4096\n"
    "v_exp_f32 v153, v153 clamp\n"
    "v_add_u32_e64 v253, s67, v244\n"
    "s_waitcnt lgkmcnt(0)\n"
    "v_mfma_f32_4x4x4_16b_bf16 v[224:227], v[228:229], v[168:169], v[224:227] cbsz:4 abid:0\n"
    "v_exp_f32 v154, v154 clamp\n"
    "v_exp_f32 v155, v155 clamp\n"
    "v_mfma_f32_4x4x4_16b_bf16 v[224:227], v[228:229], v[170:171], v[224:227] cbsz:4 abid:1\n"
    "v_exp_f32 v156, v156 clamp\n"
    "v_exp_f32 v157, v157 clamp\n"
    "v_mfma_f32_4x4x4_16b_bf16 v[224:227], v[228:229], v[172:173], v[224:227] cbsz:4 abid:2\n"
    "v_exp_f32 v158, v158 clamp\n"
    "v_exp_f32 v159, v159 clamp\n"
    "v_mfma_f32_4x4x4_16b_bf16 v[224:227], v[228:229], v[174:175], v[224:227] cbsz:4 abid:3\n"
    "s_waitcnt vmcnt(4)\n"
    "s_memrealtime s[34:35]\n"
    "ds_read_b64_tr_b16 v[168:169], v240 offset:4096\n"
    "ds_read_b64_tr_b16 v[170:171], v241 offset:4352\n"
    "ds_read_b64_tr_b16 v[172:173], v242 offset:4608\n"
    "ds_read_b64_tr_b16 v[174:175], v243 offset:4864\n"
    "ds_read_b32 v236, v253 offset:0\n"
    "ds_read_b32 v238, v253 offset:4\n"
    "ds_read_b128 v[160:163], v234 offset:6144\n"
    "ds_read_b128 v[164:167], v235 offset:6144\n"
    "v_pk_fma_f32 v[144:145], v[144:145], s[70:71], v[96:97]\n"
    "v_pk_fma_f32 v[146:147], v[146:147], s[70:71], v[98:99]\n"
    "v_pk_add_f32 v[230:231], v[144:145], v[146:147]\n"
    "v_pk_mul_f32 v[232:233], v[144:145], v[146:147]\n"
    "v_pk_fma_f32 v[148:149], v[148:149], s[70:71], v[100:101]\n"
    "v_pk_fma_f32 v[230:231], v[230:231], v[148:149], v[232:233]\n"
    "v_pk_mul_f32 v[232:233], v[232:233], v[148:149]\n"
    "v_pk_fma_f32 v[150:151], v[150:151], s[70:71], v[102:103]\n"
    "v_pk_fma_f32 v[230:231], v[230:231], v[150:151], v[232:233]\n"
    "v_pk_mul_f32 v[232:233], v[232:233], v[150:151]\n"
    "v_pk_fma_f32 v[152:153], v[152:153], s[70:71], v[104:105]\n"
    "v_pk_fma_f32 v[230:231], v[230:231], v[152:153], v[232:233]\n"
    "v_pk_mul_f32 v[232:233], v[232:233], v[152:153]\n"
    "v_pk_fma_f32 v[154:155], v[154:155], s[70:71], v[106:107]\n"
    "v_pk_fma_f32 v[230:231], v[230:231], v[154:155], v[232:233]\n"
    "v_pk_mul_f32 v[232:233], v[232:233], v[154:155]\n"
    "v_pk_fma_f32 v[156:157], v[156:157], s[70:71], v[108:109]\n"
    "v_pk_fma_f32 v[230:231], v[230:231], v[156:157], v[232:233]\n"
    "v_pk_mul_f32 v[232:233], v[232:233], v[156:157]\n"
    "v_pk_fma_f32 v[158:159], v[158:159], s[70:71], v[110:111]\n"
    "v_pk_fma_f32 v[230:231], v[230:231], v[158:159], v[232:233]\n"
    "v_pk_mul_f32 v[232:233], v[232:233], v[158:159]\n"
    "v_rcp_f32 v232, v232\n"
    "v_rcp_f32 v233, v233\n"
    "v_fma_f32 v230, v230, v232, v247\n"
    "v_fmac_f32 v230, v231, v233\n"
    "v_lshlrev_b32 v232, 2, v249\n"
    "v_lshrrev_b32 v233, 2, v249\n"
    "v_and_b32 v232, 56, v232\n"
    "v_and_b32 v233, 4, v233\n"
    "v_add3_u32 v232, v232, v233, s81\n"
    "ds_read_b32 v232, v232\n"
    "s_add_u32 s81, s81, 64\n"
    "s_waitcnt lgkmcnt(0)\n"
    "v_lshlrev_b32 v232, 1, v232\n"
    "buffer_load_ushort v232, v232, s[44:47], 0 offen\n"
    "s_waitcnt vmcnt(0)\n"
    "v_lshlrev_b32 v232, 16, v232\n"
    "v_mul_f32 v230, v230, v232\n"
    "v_cvt_pk_bf16_f32 v230, v230, v230\n"
    "s_add_u32 s67, s67, 64\n"
    "s_waitcnt lgkmcnt(0)\n"
    "ds_write_b16 v245, v230\n"
    "ds_read_b64 v[228:229], v246\n"
    "s_cmp_le_u32 s34, s76\n"
    "s_cbranch_scc1 .Ldin_G1s_wait\n"
    ".Ldin_G1s_go:\n"
    "s_add_u32 s76, s76, s72\n"
    ".Ldin_G1s_dma:\n"
    "buffer_load_dwordx4 v[236:237], s[40:43], 0 idxen offen offset:0 nt lds\n"
    "buffer_load_dwordx4 v[238:239], s[40:43], 0 idxen offen offset:1024 nt lds\n"
    ".Ldin_G1s_nd:\n"
    "s_cmp_eq_u32 s67, s82\n"
    "s_cbranch_scc1 .Ldin_L2s\n"
    "s_branch .Ldin_G2s\n"
    ".Ldin_B1s_wait:\n"
    "s_cmp_lt_i32 s59, s57\n"
    "s_cbranch_scc1 .Ldin_B1s_w2\n"
    "s_sub_u32 s94, s67, s65\n"
    "s_cmp_gt_u32 s94, 256\n"
    "s_cbranch_scc1 .Ldin_B1s_dma\n"
    "s_cmp_eq_u32 s94, 64\n"
    "s_waitcnt vmcnt(2)\n"
    "s_cbranch_scc1 .Ldin_B1s_nd\n"
    "s_waitcnt vmcnt(0)\n"
    "s_branch .Ldin_B1s_nd\n"
    ".Ldin_B1s_slp:\n"
    "s_sleep 1\n"
    ".Ldin_B1s_w2:\n"
    "s_memrealtime s[34:35]\n"
    "s_waitcnt lgkmcnt(0)\n"
    "s_sub_u32 s94, s34, s76\n"
    "s_cmp_lt_i32 s94, 0\n"
    "s_cbranch_scc1 .Ldin_B1s_slp\n"
    "s_branch .Ldin_B1s_go\n"
    ".Ldin_G2s_wait:\n"
    "s_cmp_lt_i32 s59, s57\n"
    "s_cbranch_scc1 .Ldin_G2s_w2\n"
    "s_sub_u32 s94, s67, s65\n"
    "s_cmp_gt_u32 s94, 256\n"
    "s_cbranch_scc1 .Ldin_G2s_dma\n"
    "s_cmp_eq_u32 s94, 64\n"
    "s_waitcnt vmcnt(2)\n"
    "s_cbranch_scc1 .Ldin_G2s_nd\n"
    "s_waitcnt vmcnt(0)\n"
    "s_branch .Ldin_G2s_nd\n"
    ".Ldin_G2s_slp:\n"
    "s_sleep 1\n"
    ".Ldin_G2s_w2:\n"
    "s_memrealtime s[34:35]\n"
    "s_waitcnt lgkmcnt(0)\n"
    "s_sub_u32 s94, s34, s76\n"
    "s_cmp_lt_i32 s94, 0\n"
    "s_cbranch_scc1 .Ldin_G2s_slp\n"
    "s_branch .Ldin_G2s_go\n"
    ".Ldin_G3s_wait:\n"
    "s_cmp_lt_i32 s59, s57\n"
    "s_cbranch_scc1 .Ldin_G3s_w2\n"
    "s_sub_u32 s94, s67, s65\n"
    "s_cmp_gt_u32 s94, 256\n"
    "s_cbranch_scc1 .Ldin_G3s_dma\n"
    "s_cmp_eq_u32 s94, 64\n"
    "s_waitcnt vmcnt(2)\n"
    "s_cbranch_scc1 .Ldin_G3s_nd\n"
    "s_waitcnt vmcnt(0)\n"
    "s_branch .Ldin_G3s_nd\n"
    ".Ldin_G3s_slp:\n"
    "s_sleep 1\n"
    ".Ldin_G3s_w2:\n"
    "s_memrealtime s[34:35]\n"
    "s_waitcnt lgkmcnt(0)\n"
    "s_sub_u32 s94, s34, s76\n"
    "s_cmp_lt_i32 s94, 0\n"
    "s_cbranch_scc1 .Ldin_G3s_slp\n"
    "s_branch .Ldin_G3s_go\n"
    ".Ldin_G0s_wait:\n"
    "s_cmp_lt_i32 s59, s57\n"
    "s_cbranch_scc1 .Ldin_G0s_w2\n"
    "s_sub_u32 s94, s67, s65\n"
    "s_cmp_gt_u32 s94, 256\n"
    "s_cbranch_scc1 .Ldin_G0s_dma\n"
    "s_cmp_eq_u32 s94, 64\n"
    "s_waitcnt vmcnt(2)\n"
    "s_cbranch_scc1 .Ldin_G0s_nd\n"
    "s_waitcnt vmcnt(0)\n"
    "s_branch .Ldin_G0s_nd\n"
    ".Ldin_G0s_slp:\n"
    "s_sleep 1\n"
    ".Ldin_G0s_w2:\n"
    "s_memrealtime s[34:35]\n"
    "s_waitcnt lgkmcnt(0)\n"
    "s_sub_u32 s94, s34, s76\n"
    "s_cmp_lt_i32 s94, 0\n"
    "s_cbranch_scc1 .Ldin_G0s_slp\n"
    "s_branch .Ldin_G0s_go\n"
    ".Ldin_G1s_wait:\n"
    "s_cmp_lt_i32 s59, s57\n"
    "s_cbranch_scc1 .Ldin_G1s_w2\n"
    "s_sub_u32 s94, s67, s65\n"
    "s_cmp_gt_u32 s94, 256\n"
    "s_cbranch_scc1 .Ldin_G1s_dma\n"
    "s_cmp_eq_u32 s94, 64\n"
    "s_waitcnt vmcnt(2)\n"
    "s_cbranch_scc1 .Ldin_G1s_nd\n"
    "s_waitcnt vmcnt(0)\n"
    "s_branch .Ldin_G1s_nd\n"
    ".Ldin_G1s_slp:\n"
    "s_sleep 1\n"
    ".Ldin_G1s_w2:\n"
    "s_memrealtime s[34:35]\n"
    "s_waitcnt lgkmcnt(0)\n"
    "s_sub_u32 s94, s34, s76\n"
    "s_cmp_lt_i32 s94, 0\n"
    "s_cbranch_scc1 .Ldin_G1s_slp\n"
    "s_branch .Ldin_G1s_go\n"
    ".p2align 5\n"
    ".Ldin_L2s:\n"
    "s_setprio 0\n"
    "s_cmp_ge_i32 s59, s57\n"
    "s_cbranch_scc1 .Ldin_E0s\n"
    "s_add_u32 s76, s76, s73\n"
    "v_mov_b32_e64 v253, s63\n"
    "ds_read_b64 v[232:233], v253\n"
    "v_mfma_f32_4x4x4_16b_bf16 v[144:147], v[184:185], v[32:33], v[64:67] cbsz:2 abid:0\n"
    "ds_read_b128 v[112:115], v252 offset:17408\n"
    "v_mfma_f32_4x4x4_16b_bf16 v[148:151], v[184:185], v[34:35], v[68:71] cbsz:2 abid:1\n"
    "ds_read_b128 v[116:119], v252 offset:17424\n"
    "v_mfma_f32_4x4x4_16b_bf16 v[152:155], v[184:185], v[36:37], v[72:75] cbsz:2 abid:2\n"
    "ds_read_b128 v[120:123], v252 offset:17536\n"
    "ds_read_b128 v[124:127], v252 offset:17552\n"
    "v_cvt_pk_bf16_f32 v0, v144, v145\n"
    "v_cvt_pk_bf16_f32 v1, v146, v147\n"
    "v_mfma_f32_4x4x4_16b_bf16 v[156:159], v[184:185], v[38:39], v[76:79] cbsz:2 abid:3\n"
    "v_cvt_pk_bf16_f32 v2, v148, v149\n"
    "v_cvt_pk_bf16_f32 v3, v150, v151\n"
    "v_mfma_f32_4x4x4_16b_bf16 v[144:147], v[184:185], v[40:41], v[80:83] cbsz:2 abid:0\n"
    "v_cvt_pk_bf16_f32 v4, v152, v153\n"
    "v_cvt_pk_bf16_f32 v5, v154, v155\n"
    "v_mfma_f32_4x4x4_16b_bf16 v[148:151], v[184:185], v[42:43], v[84:87] cbsz:2 abid:1\n"
    "v_cvt_pk_bf16_f32 v6, v156, v157\n"
    "v_cvt_pk_bf16_f32 v7, v158, v159\n"
    "v_mfma_f32_4x4x4_16b_bf16 v[152:155], v[184:185], v[44:45], v[88:91] cbsz:2 abid:2\n"
    "v_cvt_pk_bf16_f32 v8, v144, v145\n"
    "v_cvt_pk_bf16_f32 v9, v146, v147\n"
    "v_mfma_f32_4x4x4_16b_bf16 v[156:159], v[184:185], v[46:47], v[92:95] cbsz:2 abid:3\n"
    "v_cvt_pk_bf16_f32 v10, v148, v149\n"
    "v_cvt_pk_bf16_f32 v11, v150, v151\n"
    "v_mfma_f32_4x4x4_16b_bf16 v[144:147], v[184:185], v[48:49], v[192:195] cbsz:2 abid:0\n"
    "v_cvt_pk_bf16_f32 v12, v152, v153\n"
    "v_cvt_pk_bf16_f32 v13, v154, v155\n"
    "v_mfma_f32_4x4x4_16b_bf16 v[148:151], v[184:185], v[50:51], v[196:199] cbsz:2 abid:1\n"
    "v_cvt_pk_bf16_f32 v14, v156, v157\n"
    "v_cvt_pk_bf16_f32 v15, v158, v159\n"
    "v_mfma_f32_4x4x4_16b_bf16 v[152:155], v[184:185], v[52:53], v[200:203] cbsz:2 abid:2\n"
    "s_waitcnt lgkmcnt(0)\n"
    "v_readfirstlane_b32 s60, v232\n"
    "v_cvt_pk_bf16_f32 v16, v144, v145\n"
    "v_cvt_pk_bf16_f32 v17, v146, v147\n"
    "v_mfma_f32_4x4x4_16b_bf16 v[156:159], v[184:185], v[54:55], v[204:207] cbsz:2 abid:3\n"
    "s_cmp_eq_u32 s60, -1\n"
    "s_cbranch_scc1 .Ldin_L2s_poll\n"
    ".Ldin_L2s_pr:\n"
    "v_readfirstlane_b32 s95, v233\n"
    "s_and_b32 s94, s60, 63\n"
    "s_lshl_b32 s94, s94, 6\n"
    "s_add_u32 s94, s94, 68480\n"
    "s_add_u32 s63, s63, 8\n"
    "v_lshl_add_u32 v252, v249, 1, s94\n"
    "ds_read_b32 v252, v252\n"
    "s_sub_u32 s95, s95, s76\n"
    "s_ashr_i32 s95, s95, 4\n"
    "s_min_i32 s95, s95, s74\n"
    "s_max_i32 s72, s95, s75\n"
    "v_cvt_pk_bf16_f32 v18, v148, v149\n"
    "v_cvt_pk_bf16_f32 v19, v150, v151\n"
    "v_mfma_f32_4x4x4_16b_bf16 v[144:147], v[184:185], v[56:57], v[208:211] cbsz:2 abid:0\n"
    "v_cvt_pk_bf16_f32 v20, v152, v153\n"
    "v_cvt_pk_bf16_f32 v21, v154, v155\n"
    "v_mfma_f32_4x4x4_16b_bf16 v[148:151], v[184:185], v[58:59], v[212:215] cbsz:2 abid:1\n"
    "v_cvt_pk_bf16_f32 v22, v156, v157\n"
    "v_cvt_pk_bf16_f32 v23, v158, v159\n"
    "v_mfma_f32_4x4x4_16b_bf16 v[152:155], v[184:185], v[60:61], v[216:219] cbsz:2 abid:2\n"
    "v_cvt_pk_bf16_f32 v24, v144, v145\n"
    "v_cvt_pk_bf16_f32 v25, v146, v147\n"
    "v_mfma_f32_4x4x4_16b_bf16 v[156:159], v[184:185], v[62:63], v[220:223] cbsz:2 abid:3\n"
    "v_cvt_pk_bf16_f32 v26, v148, v149\n"
    "v_cvt_pk_bf16_f32 v27, v150, v151\n"
    "v_cvt_pk_bf16_f32 v28, v152, v153\n"
    "v_cvt_pk_bf16_f32 v29, v154, v155\n"
    "v_exp_f32 v136, v136 clamp\n"
    "v_cvt_pk_bf16_f32 v30, v156, v157\n"
    "v_cvt_pk_bf16_f32 v31, v158, v159\n"
    "v_mfma_f32_16x16x32_bf16 v[144:147], v[0:3], v[160:163], v[112:115]\n"
    "v_exp_f32 v128, v128 clamp\n"
    "v_mfma_f32_16x16x32_bf16 v[144:147], v[4:7], v[164:167], v[144:147]\n"
    "v_exp_f32 v129, v129 clamp\n"
    "v_mfma_f32_16x16x32_bf16 v[148:151], v[8:11], v[160:163], v[116:119]\n"
    "v_exp_f32 v130, v130 clamp\n"
    "v_mfma_f32_16x16x32_bf16 v[148:151], v[12:15], v[164:167], v[148:151]\n"
    "v_exp_f32 v131, v131 clamp\n"
    "v_mfma_f32_16x16x32_bf16 v[152:155], v[16:19], v[160:163], v[120:123]\n"
    "v_exp_f32 v132, v132 clamp\n"
    "v_mfma_f32_16x16x32_bf16 v[152:155], v[20:23], v[164:167], v[152:155]\n"
    "v_exp_f32 v133, v133 clamp\n"
    "v_mfma_f32_16x16x32_bf16 v[156:159], v[24:27], v[160:163], v[124:127]\n"
    "v_exp_f32 v134, v134 clamp\n"
    "v_mfma_f32_16x16x32_bf16 v[156:159], v[28:31], v[164:167], v[156:159]\n"
    "v_exp_f32 v135, v135 clamp\n"
    "s_add_u32 m0, s68, 6144\n"
    "v_exp_f32 v137, v137 clamp\n"
    "v_add_u32 v253, s67, v244\n"
    "v_mfma_f32_4x4x4_16b_bf16 v[224:227], v[228:229], v[176:177], v[224:227] cbsz:4 abid:0\n"
    "v_exp_f32 v138, v138 clamp\n"
    "v_exp_f32 v139, v139 clamp\n"
    "v_mfma_f32_4x4x4_16b_bf16 v[224:227], v[228:229], v[178:179], v[224:227] cbsz:4 abid:1\n"
    "v_exp_f32 v140, v140 clamp\n"
    "v_exp_f32 v141, v141 clamp\n"
    "v_mfma_f32_4x4x4_16b_bf16 v[224:227], v[228:229], v[180:181], v[224:227] cbsz:4 abid:2\n"
    "v_exp_f32 v142, v142 clamp\n"
    "v_exp_f32 v143, v143 clamp\n"
    "v_mfma_f32_4x4x4_16b_bf16 v[224:227], v[228:229], v[182:183], v[224:227] cbsz:4 abid:3\n"
    "s_waitcnt vmcnt(4)\n"
    "ds_read_b64_tr_b16 v[176:177], v240 offset:6144\n"
    "ds_read_b64_tr_b16 v[178:179], v241 offset:6400\n"
    "ds_read_b64_tr_b16 v[180:181], v242 offset:6656\n"
    "ds_read_b64_tr_b16 v[182:183], v243 offset:6912\n"
    "ds_read_b32 v236, v253 offset:0\n"
    "ds_read_b32 v238, v253 offset:4\n"
    "ds_read_b128 v[160:163], v234 offset:0\n"
    "ds_read_b128 v[164:167], v235 offset:0\n"
    "s_waitcnt lgkmcnt(8)\n"
    "s_nop 0\n"
    "v_readlane_b32 s20, v252, 0\n"
    "v_readlane_b32 s21, v252, 1\n"
    "v_readlane_b32 s22, v252, 2\n"
    "v_readlane_b32 s23, v252, 3\n"
    "v_readlane_b32 s24, v252, 4\n"
    "v_readlane_b32 s25, v252, 5\n"
    "v_readlane_b32 s26, v252, 6\n"
    "v_readlane_b32 s27, v252, 7\n"
    "v_pk_fma_f32 v[128:129], v[128:129], s[70:71], v[96:97]\n"
    "v_pk_fma_f32 v[130:131], v[130:131], s[70:71], v[98:99]\n"
    "v_pk_add_f32 v[230:231], v[128:129], v[130:131]\n"
    "v_pk_mul_f32 v[232:233], v[128:129], v[130:131]\n"
    "v_pk_fma_f32 v[132:133], v[132:133], s[70:71], v[100:101]\n"
    "v_pk_fma_f32 v[230:231], v[230:231], v[132:133], v[232:233]\n"
    "v_pk_mul_f32 v[232:233], v[232:233], v[132:133]\n"
    "v_pk_fma_f32 v[134:135], v[134:135], s[70:71], v[102:103]\n"
    "v_pk_fma_f32 v[230:231], v[230:231], v[134:135], v[232:233]\n"
    "v_pk_mul_f32 v[232:233], v[232:233], v[134:135]\n"
    "v_pk_fma_f32 v[136:137], v[136:137], s[70:71], v[104:105]\n"
    "v_pk_fma_f32 v[230:231], v[230:231], v[136:137], v[232:233]\n"
    "v_pk_mul_f32 v[232:233], v[232:233], v[136:137]\n"
    "v_pk_fma_f32 v[138:139], v[138:139], s[70:71], v[106:107]\n"
    "v_pk_fma_f32 v[230:231], v[230:231], v[138:139], v[232:233]\n"
    "v_pk_mul_f32 v[232:233], v[232:233], v[138:139]\n"
    "v_pk_fma_f32 v[140:141], v[140:141], s[70:71], v[108:109]\n"
    "v_pk_fma_f32 v[230:231], v[230:231], v[140:141], v[232:233]\n"
    "v_pk_mul_f32 v[232:233], v[232:233], v[140:141]\n"
    "v_pk_fma_f32 v[142:143], v[142:143], s[70:71], v[110:111]\n"
    "v_pk_fma_f32 v[230:231], v[230:231], v[142:143], v[232:233]\n"
    "v_pk_mul_f32 v[232:233], v[232:233], v[142:143]\n"
    "v_rcp_f32 v232, v232\n"
    "v_rcp_f32 v233, v233\n"
    "v_fma_f32 v230, v230, v232, v247\n"
    "v_fmac_f32 v230, v231, v233\n"
    "v_lshlrev_b32 v232, 2, v249\n"
    "v_lshrrev_b32 v233, 2, v249\n"
    "v_and_b32 v232, 56, v232\n"
    "v_and_b32 v233, 4, v233\n"
    "v_add3_u32 v232, v232, v233, s81\n"
    "ds_read_b32 v232, v232\n"
    "s_add_u32 s81, s81, 64\n"
    "s_waitcnt lgkmcnt(0)\n"
    "v_lshlrev_b32 v232, 1, v232\n"
    "buffer_load_ushort v232, v232, s[44:47], 0 offen\n"
    "s_waitcnt vmcnt(0)\n"
    "v_lshlrev_b32 v232, 16, v232\n"
    "v_mul_f32 v230, v230, v232\n"
    "v_cvt_pk_bf16_f32 v230, v230, v230\n"
    "s_add_u32 s67, s67, 64\n"
    "s_waitcnt lgkmcnt(0)\n"
    "ds_write_b16 v245, v230\n"
    "ds_read_b64 v[228:229], v246\n"
    "s_lshl1_add_u32 s76, s72, s76\n"
    "buffer_load_dwordx4 v[236:237], s[40:43], 0 idxen offen offset:0 nt lds\n"
    "buffer_load_dwordx4 v[238:239], s[40:43], 0 idxen offen offset:1024 nt lds\n"
    "s_and_b32 s62, s58, 63\n"
    "s_mul_i32 s62, s62, 272\n"
    "s_mov_b32 s58, s59\n"
    "s_mov_b32 s59, s60\n"
    "s_mov_b32 s81, s65\n"
    "s_xor_b32 s65, s65, s64\n"
    "s_mov_b32 s80, s83\n"
    "s_add_u32 s82, s65, 256\n"
    "s_cmp_lt_i32 s59, s57\n"
    "s_cselect_b32 s76, s76, -1\n"
    "s_bitcmp1_b32 s58, 30\n"
    "s_cbranch_scc0 .Ldin_A3\n"
    ".Ldin_A3s:\n"
    "v_mfma_f32_16x16x32_bf16 v[128:131], v[0:3], v[160:163], v[112:115]\n"
    "v_exp_f32 v144, v144 clamp\n"
    "v_mfma_f32_16x16x32_bf16 v[128:131], v[4:7], v[164:167], v[128:131]\n"
    "v_exp_f32 v145, v145 clamp\n"
    "v_mfma_f32_16x16x32_bf16 v[132:135], v[8:11], v[160:163], v[116:119]\n"
    "v_exp_f32 v146, v146 clamp\n"
    "v_mfma_f32_16x16x32_bf16 v[132:135], v[12:15], v[164:167], v[132:135]\n"
    "v_exp_f32 v147, v147 clamp\n"
    "v_mfma_f32_16x16x32_bf16 v[136:139], v[16:19], v[160:163], v[120:123]\n"
    "v_exp_f32 v148, v148 clamp\n"
    "v_mfma_f32_16x16x32_bf16 v[136:139], v[20:23], v[164:167], v[136:139]\n"
    "v_exp_f32 v149, v149 clamp\n"
    "v_mfma_f32_16x16x32_bf16 v[140:143], v[24:27], v[160:163], v[124:127]\n"
    "v_exp_f32 v150, v150 clamp\n"
    "v_mfma_f32_16x16x32_bf16 v[140:143], v[28:31], v[164:167], v[140:143]\n"
    "v_exp_f32 v151, v151 clamp\n"
    "v_exp_f32 v152, v152 clamp\n"
    "s_mov_b32 m0, s62\n"
    "v_exp_f32 v153, v153 clamp\n"
    "v_add_u32 v253, s67, v244\n"
    "s_waitcnt lgkmcnt(0)\n"
    "v_mfma_f32_4x4x4_16b_bf16 v[224:227], v[228:229], v[168:169], v[224:227] cbsz:4 abid:0\n"
    "v_exp_f32 v154, v154 clamp\n"
    "v_exp_f32 v155, v155 clamp\n"
    "v_mfma_f32_4x4x4_16b_bf16 v[224:227], v[228:229], v[170:171], v[224:227] cbsz:4 abid:1\n"
    "v_exp_f32 v156, v156 clamp\n"
    "v_exp_f32 v157, v157 clamp\n"
    "v_mfma_f32_4x4x4_16b_bf16 v[224:227], v[228:229], v[172:173], v[224:227] cbsz:4 abid:2\n"
    "v_exp_f32 v158, v158 clamp\n"
    "v_exp_f32 v159, v159 clamp\n"
    "v_mfma_f32_4x4x4_16b_bf16 v[224:227], v[228:229], v[174:175], v[224:227] cbsz:4 abid:3\n"
    "s_waitcnt vmcnt(4)\n"
    "ds_read_b64_tr_b16 v[168:169], v240 offset:0\n"
    "ds_read_b64_tr_b16 v[170:171], v241 offset:256\n"
    "ds_read_b64_tr_b16 v[172:173], v242 offset:512\n"
    "ds_read_b64_tr_b16 v[174:175], v243 offset:768\n"
    "ds_read_b32 v236, v253 offset:0\n"
    "ds_read_b32 v238, v253 offset:4\n"
    "ds_read_b128 v[160:163], v234 offset:2048\n"
    "ds_read_b128 v[164:167], v235 offset:2048\n"
    "s_cbranch_vccz .Ldin_A3s_np\n"
    "s_setprio 1\n"
    ".Ldin_A3s_np:\n"
    "v_pk_fma_f32 v[144:145], v[144:145], s[70:71], v[96:97]\n"
    "v_pk_fma_f32 v[146:147], v[146:147], s[70:71], v[98:99]\n"
    "v_pk_add_f32 v[190:191], v[224:225], v[226:227]\n"
    "v_add_f32 v190, v190, v191\n"
    "ds_write_addtid_b32 v190 offset:32768\n"
    "s_add_u32 m0, s68, 0\n"
    "s_bfe_u32 s94, s59, 0x180006\n"
    "s_mul_i32 s86, s94, s56\n"
    "v_mbcnt_lo_u32_b32 v184, s20, 0\n"
    "v_mbcnt_lo_u32_b32 v185, s22, 0\n"
    "v_mbcnt_lo_u32_b32 v186, s24, 0\n"
    "v_mbcnt_lo_u32_b32 v187, s26, 0\n"
    "s_bcnt1_i32_b64 s90, s[20:21]\n"
    "s_bcnt1_i32_b64 s91, s[22:23]\n"
    "s_bcnt1_i32_b64 s92, s[24:25]\n"
    "s_bcnt1_i32_b64 s93, s[26:27]\n"
    "v_mbcnt_hi_u32_b32 v184, s21, v184\n"
    "v_mbcnt_hi_u32_b32 v185, s23, v185\n"
    "v_mbcnt_hi_u32_b32 v186, s25, v186\n"
    "v_mbcnt_hi_u32_b32 v187, s27, v187\n"
    "s_lshl2_add_u32 s90, s90, s65\n"
    "s_lshl2_add_u32 s91, s91, s90\n"
    "s_lshl2_add_u32 s92, s92, s91\n"
    "s_lshl2_add_u32 s93, s93, s92\n"
    "v_add_u32 v188, s86, v251\n"
    "v_pk_add_f32 v[230:231], v[144:145], v[146:147]\n"
    "v_pk_mul_f32 v[232:233], v[144:145], v[146:147]\n"
    "v_pk_fma_f32 v[148:149], v[148:149], s[70:71], v[100:101]\n"
    "v_pk_fma_f32 v[230:231], v[230:231], v[148:149], v[232:233]\n"
    "v_pk_mul_f32 v[232:233], v[232:233], v[148:149]\n"
    "v_lshl_add_u32 v184, v184, 2, s65\n"
    "v_lshl_add_u32 v185, v185, 2, s90\n"
    "v_lshl_add_u32 v186, v186, 2, s91\n"
    "v_lshl_add_u32 v187, v187, 2, s92\n"
    "v_mov_b32 v191, -1\n"
    "v_lshl_add_u32 v190, v249, 1, s93\n"
    "ds_write2_b32 v190, v191, v191 offset1:64\n"
    "s_mov_b64 exec, s[20:21]\n"
    "ds_write_b32 v184, v188\n"
    "s_mov_b64 exec, s[22:23]\n"
    "v_add_u32 v189, 64, v188\n"
    "ds_write_b32 v185, v189\n"
    "s_mov_b64 exec, s[24:25]\n"
    "v_add_u32 v190, 128, v188\n"
    "ds_write_b32 v186, v190\n"
    "s_mov_b64 exec, s[26:27]\n"
    "v_add_u32 v189, 192, v188\n"
    "ds_write_b32 v187, v189\n"
    "s_mov_b64 exec, -1\n"
    "s_add_u32 s95, s93, 60\n"
    "s_add_u32 s94, s82, 128\n"
    "s_andn2_b32 s95, s95, 63\n"
    "s_max_u32 s83, s95, s94\n"
    "v_pk_fma_f32 v[150:151], v[150:151], s[70:71], v[102:103]\n"
    "v_pk_fma_f32 v[230:231], v[230:231], v[150:151], v[232:233]\n"
    "v_pk_mul_f32 v[232:233], v[232:233], v[150:151]\n"
    "v_pk_fma_f32 v[152:153], v[152:153], s[70:71], v[104:105]\n"
    "v_pk_fma_f32 v[230:231], v[230:231], v[152:153], v[232:233]\n"
    "v_pk_mul_f32 v[232:233], v[232:233], v[152:153]\n"
    "v_pk_fma_f32 v[154:155], v[154:155], s[70:71], v[106:107]\n"
    "v_pk_fma_f32 v[230:231], v[230:231], v[154:155], v[232:233]\n"
    "v_pk_mul_f32 v[232:233], v[232:233], v[154:155]\n"
    "v_pk_fma_f32 v[156:157], v[156:157], s[70:71], v[108:109]\n"
    "v_pk_fma_f32 v[230:231], v[230:231], v[156:157], v[232:233]\n"
    "v_pk_mul_f32 v[232:233], v[232:233], v[156:157]\n"
    "v_pk_fma_f32 v[158:159], v[158:159], s[70:71], v[110:111]\n"
    "v_pk_fma_f32 v[230:231], v[230:231], v[158:159], v[232:233]\n"
    "v_pk_mul_f32 v[232:233], v[232:233], v[158:159]\n"
    "v_rcp_f32 v232, v232\n"
    "v_rcp_f32 v233, v233\n"
    "v_fma_f32 v230, v230, v232, v247\n"
    "v_fmac_f32 v230, v231, v233\n"
    "v_lshlrev_b32 v232, 2, v249\n"
    "v_lshrrev_b32 v233, 2, v249\n"
    "v_and_b32 v232, 56, v232\n"
    "v_and_b32 v233, 4, v233\n"
    "v_add3_u32 v232, v232, v233, s81\n"
    "ds_read_b32 v232, v232\n"
    "s_add_u32 s81, s81, 64\n"
    "s_waitcnt lgkmcnt(0)\n"
    "v_lshlrev_b32 v232, 1, v232\n"
    "buffer_load_ushort v232, v232, s[44:47], 0 offen\n"
    "s_waitcnt vmcnt(0)\n"
    "v_lshlrev_b32 v232, 16, v232\n"
    "v_mul_f32 v230, v230, v232\n"
    "v_cvt_pk_bf16_f32 v230, v230, v230\n"
    "s_add_u32 s67, s67, 64\n"
    "s_waitcnt lgkmcnt(0)\n"
    "ds_write_b16 v245, v230\n"
    "ds_read_b64 v[228:229], v246\n"
    "buffer_load_dwordx4 v[236:237], s[40:43], 0 idxen offen offset:0 nt lds\n"
    "buffer_load_dwordx4 v[238:239], s[40:43], 0 idxen offen offset:1024 nt lds\n"
    ".Ldin_B0s:\n"
    "v_mfma_f32_16x16x32_bf16 v[144:147], v[0:3], v[160:163], v[112:115]\n"
    "v_exp_f32 v128, v128 clamp\n"
    "v_mfma_f32_16x16x32_bf16 v[144:147], v[4:7], v[164:167], v[144:147]\n"
    "v_exp_f32 v129, v129 clamp\n"
    "v_mfma_f32_16x16x32_bf16 v[148:151], v[8:11], v[160:163], v[116:119]\n"
    "v_exp_f32 v130, v130 clamp\n"
    "v_mfma_f32_16x16x32_bf16 v[148:151], v[12:15], v[164:167], v[148:151]\n"
    "v_exp_f32 v131, v131 clamp\n"
    "v_mfma_f32_16x16x32_bf16 v[152:155], v[16:19], v[160:163], v[120:123]\n"
    "v_exp_f32 v132, v132 clamp\n"
    "v_mfma_f32_16x16x32_bf16 v[152:155], v[20:23], v[164:167], v[152:155]\n"
    "v_exp_f32 v133, v133 clamp\n"
    "v_mfma_f32_16x16x32_bf16 v[156:159], v[24:27], v[160:163], v[124:127]\n"
    "v_exp_f32 v134, v134 clamp\n"
    "v_mfma_f32_16x16x32_bf16 v[156:159], v[28:31], v[164:167], v[156:159]\n"
    "v_exp_f32 v135, v135 clamp\n"
    "v_exp_f32 v136, v136 clamp\n"
    "s_cmp_eq_u32 s67, s80\n"
    "s_cselect_b32 s67, s65, s67\n"
    "s_and_b32 s94, s59, 63\n"
    "s_mul_i32 s94, s94, 272\n"
    "v_add_u32 v252, s94, v250\n"
    "s_add_u32 m0, s68, 2048\n"
    "v_exp_f32 v137, v137 clamp\n"
    "v_add_u32 v253, s67, v244\n"
    "s_waitcnt lgkmcnt(0)\n"
    "v_mfma_f32_4x4x4_16b_bf16 v[224:227], v[228:229], v[176:177], 0 cbsz:4 abid:0\n"
    "v_exp_f32 v138, v138 clamp\n"
    "v_exp_f32 v139, v139 clamp\n"
    "v_mfma_f32_4x4x4_16b_bf16 v[224:227], v[228:229], v[178:179], v[224:227] cbsz:4 abid:1\n"
    "v_exp_f32 v140, v140 clamp\n"
    "v_exp_f32 v141, v141 clamp\n"
    "v_mfma_f32_4x4x4_16b_bf16 v[224:227], v[228:229], v[180:181], v[224:227] cbsz:4 abid:2\n"
    "v_exp_f32 v142, v142 clamp\n"
    "v_exp_f32 v143, v143 clamp\n"
    "v_mfma_f32_4x4x4_16b_bf16 v[224:227], v[228:229], v[182:183], v[224:227] cbsz:4 abid:3\n"
    "s_waitcnt vmcnt(4)\n"
    "s_memrealtime s[34:35]\n"
    "ds_read_b64_tr_b16 v[176:177], v240 offset:2048\n"
    "ds_read_b64_tr_b16 v[178:179], v241 offset:2304\n"
    "ds_read_b64_tr_b16 v[180:181], v242 offset:2560\n"
    "ds_read_b64_tr_b16 v[182:183], v243 offset:2816\n"
    "ds_read_b32 v236, v253 offset:0\n"
    "ds_read_b32 v238, v253 offset:4\n"
    "ds_read_b128 v[160:163], v234 offset:4096\n"
    "ds_read_b128 v[164:167], v235 offset:4096\n"
    "v_add_u32_e64 v184, v248, v252\n"
    "ds_read_b64 v[184:185], v184\n"
    "v_pk_fma_f32 v[128:129], v[128:129], s[70:71], v[96:97]\n"
    "v_pk_fma_f32 v[130:131], v[130:131], s[70:71], v[98:99]\n"
    "v_pk_add_f32 v[230:231], v[128:129], v[130:131]\n"
    "v_pk_mul_f32 v[232:233], v[128:129], v[130:131]\n"
    "v_pk_fma_f32 v[132:133], v[132:133], s[70:71], v[100:101]\n"
    "v_pk_fma_f32 v[230:231], v[230:231], v[132:133], v[232:233]\n"
    "v_pk_mul_f32 v[232:233], v[232:233], v[132:133]\n"
    "v_pk_fma_f32 v[134:135], v[134:135], s[70:71], v[102:103]\n"
    "v_pk_fma_f32 v[230:231], v[230:231], v[134:135], v[232:233]\n"
    "v_pk_mul_f32 v[232:233], v[232:233], v[134:135]\n"
    "v_pk_fma_f32 v[136:137], v[136:137], s[70:71], v[104:105]\n"
    "v_pk_fma_f32 v[230:231], v[230:231], v[136:137], v[232:233]\n"
    "v_pk_mul_f32 v[232:233], v[232:233], v[136:137]\n"
    "v_pk_fma_f32 v[138:139], v[138:139], s[70:71], v[106:107]\n"
    "v_pk_fma_f32 v[230:231], v[230:231], v[138:139], v[232:233]\n"
    "v_pk_mul_f32 v[232:233], v[232:233], v[138:139]\n"
    "v_pk_fma_f32 v[140:141], v[140:141], s[70:71], v[108:109]\n"
    "v_pk_fma_f32 v[230:231], v[230:231], v[140:141], v[232:233]\n"
    "v_pk_mul_f32 v[232:233], v[232:233], v[140:141]\n"
    "v_pk_fma_f32 v[142:143], v[142:143], s[70:71], v[110:111]\n"
    "v_pk_fma_f32 v[230:231], v[230:231], v[142:143], v[232:233]\n"
    "v_pk_mul_f32 v[232:233], v[232:233], v[142:143]\n"
    "v_rcp_f32 v232, v232\n"
    "v_rcp_f32 v233, v233\n"
    "v_fma_f32 v230, v230, v232, v247\n"
    "v_fmac_f32 v230, v231, v233\n"
    "v_lshlrev_b32 v232, 2, v249\n"
    "v_lshrrev_b32 v233, 2, v249\n"
    "v_and_b32 v232, 56, v232\n"
    "v_and_b32 v233, 4, v233\n"
    "v_add3_u32 v232, v232, v233, s81\n"
    "ds_read_b32 v232, v232\n"
    "s_add_u32 s81, s81, 64\n"
    "s_waitcnt lgkmcnt(0)\n"
    "v_lshlrev_b32 v232, 1, v232\n"
    "buffer_load_ushort v232, v232, s[44:47], 0 offen\n"
    "s_waitcnt vmcnt(0)\n"
    "v_lshlrev_b32 v232, 16, v232\n"
    "v_mul_f32 v230, v230, v232\n"
    "v_cvt_pk_bf16_f32 v230, v230, v230\n"
    "s_add_u32 s67, s67, 64\n"
    "s_waitcnt lgkmcnt(0)\n"
    "ds_write_b16 v245, v230\n"
    "ds_read_b64 v[228:229], v246\n"
    "v_and_b32 v184, v254, v184\n"
    "v_and_b32 v185, v255, v185\n"
    "s_cmp_le_u32 s34, s76\n"
    "s_cbranch_scc1 .Ldin_B0s_wait\n"
    ".Ldin_B0s_go:\n"
    "s_sub_u32 s94, s34, 60\n"
    "s_max_u32 s76, s76, s94\n"
    "s_add_u32 s76, s76, s72\n"
    ".Ldin_B0s_dma:\n"
    "buffer_load_dwordx4 v[236:237], s[40:43], 0 idxen offen offset:0 nt lds\n"
    "buffer_load_dwordx4 v[238:239], s[40:43], 0 idxen offen offset:1024 nt lds\n"
    ".Ldin_B0s_nd:\n"
    "s_branch .Ldin_G1s\n"
    ".Ldin_L2s_poll:\n"
    "s_sleep 1\n"
    "ds_read_b64 v[232:233], v253\n"
    "s_waitcnt lgkmcnt(0)\n"
    "v_readfirstlane_b32 s60, v232\n"
    "s_cmp_eq_u32 s60, -1\n"
    "s_cbranch_scc1 .Ldin_L2s_poll\n"
    "s_branch .Ldin_L2s_pr\n"
    ".Ldin_B0s_wait:\n"
    "s_cmp_lt_i32 s59, s57\n"
    "s_cbranch_scc1 .Ldin_B0s_w2\n"
    "s_sub_u32 s94, s67, s65\n"
    "s_cmp_gt_u32 s94, 256\n"
    "s_cbranch_scc1 .Ldin_B0s_dma\n"
    "s_cmp_eq_u32 s94, 64\n"
    "s_waitcnt vmcnt(2)\n"
    "s_cbranch_scc1 .Ldin_B0s_nd\n"
    "s_waitcnt vmcnt(0)\n"
    "s_branch .Ldin_B0s_nd\n"
    ".Ldin_B0s_slp:\n"
    "s_sleep 1\n"
    ".Ldin_B0s_w2:\n"
    "s_memrealtime s[34:35]\n"
    "s_waitcnt lgkmcnt(0)\n"
    "s_sub_u32 s94, s34, s76\n"
    "s_cmp_lt_i32 s94, 0\n"
    "s_cbranch_scc1 .Ldin_B0s_slp\n"
    "s_branch .Ldin_B0s_go\n"
    ".p2align 5\n"
    ".Ldin_L1s:\n"
    "s_setprio 0\n"
    "s_cmp_ge_i32 s59, s57\n"
    "s_cbranch_scc1 .Ldin_E1s\n"
    "s_add_u32 s76, s76, s73\n"
    "v_mov_b32_e64 v253, s63\n"
    "ds_read_b64 v[232:233], v253\n"
    "v_mfma_f32_4x4x4_16b_bf16 v[128:131], v[184:185], v[32:33], v[64:67] cbsz:2 abid:0\n"
    "ds_read_b128 v[112:115], v252 offset:17408\n"
    "v_mfma_f32_4x4x4_16b_bf16 v[132:135], v[184:185], v[34:35], v[68:71] cbsz:2 abid:1\n"
    "ds_read_b128 v[116:119], v252 offset:17424\n"
    "v_mfma_f32_4x4x4_16b_bf16 v[136:139], v[184:185], v[36:37], v[72:75] cbsz:2 abid:2\n"
    "ds_read_b128 v[120:123], v252 offset:17536\n"
    "ds_read_b128 v[124:127], v252 offset:17552\n"
    "v_cvt_pk_bf16_f32 v0, v128, v129\n"
    "v_cvt_pk_bf16_f32 v1, v130, v131\n"
    "v_mfma_f32_4x4x4_16b_bf16 v[140:143], v[184:185], v[38:39], v[76:79] cbsz:2 abid:3\n"
    "v_cvt_pk_bf16_f32 v2, v132, v133\n"
    "v_cvt_pk_bf16_f32 v3, v134, v135\n"
    "v_mfma_f32_4x4x4_16b_bf16 v[128:131], v[184:185], v[40:41], v[80:83] cbsz:2 abid:0\n"
    "v_cvt_pk_bf16_f32 v4, v136, v137\n"
    "v_cvt_pk_bf16_f32 v5, v138, v139\n"
    "v_mfma_f32_4x4x4_16b_bf16 v[132:135], v[184:185], v[42:43], v[84:87] cbsz:2 abid:1\n"
    "v_cvt_pk_bf16_f32 v6, v140, v141\n"
    "v_cvt_pk_bf16_f32 v7, v142, v143\n"
    "v_mfma_f32_4x4x4_16b_bf16 v[136:139], v[184:185], v[44:45], v[88:91] cbsz:2 abid:2\n"
    "v_cvt_pk_bf16_f32 v8, v128, v129\n"
    "v_cvt_pk_bf16_f32 v9, v130, v131\n"
    "v_mfma_f32_4x4x4_16b_bf16 v[140:143], v[184:185], v[46:47], v[92:95] cbsz:2 abid:3\n"
    "v_cvt_pk_bf16_f32 v10, v132, v133\n"
    "v_cvt_pk_bf16_f32 v11, v134, v135\n"
    "v_mfma_f32_4x4x4_16b_bf16 v[128:131], v[184:185], v[48:49], v[192:195] cbsz:2 abid:0\n"
    "v_cvt_pk_bf16_f32 v12, v136, v137\n"
    "v_cvt_pk_bf16_f32 v13, v138, v139\n"
    "v_mfma_f32_4x4x4_16b_bf16 v[132:135], v[184:185], v[50:51], v[196:199] cbsz:2 abid:1\n"
    "v_cvt_pk_bf16_f32 v14, v140, v141\n"
    "v_cvt_pk_bf16_f32 v15, v142, v143\n"
    "v_mfma_f32_4x4x4_16b_bf16 v[136:139], v[184:185], v[52:53], v[200:203] cbsz:2 abid:2\n"
    "s_waitcnt lgkmcnt(0)\n"
    "v_readfirstlane_b32 s60, v232\n"
    "v_cvt_pk_bf16_f32 v16, v128, v129\n"
    "v_cvt_pk_bf16_f32 v17, v130, v131\n"
    "v_mfma_f32_4x4x4_16b_bf16 v[140:143], v[184:185], v[54:55], v[204:207] cbsz:2 abid:3\n"
    "s_cmp_eq_u32 s60, -1\n"
    "s_cbranch_scc1 .Ldin_L1s_poll\n"
    ".Ldin_L1s_pr:\n"
    "v_readfirstlane_b32 s95, v233\n"
    "s_and_b32 s94, s60, 63\n"
    "s_lshl_b32 s94, s94, 6\n"
    "s_add_u32 s94, s94, 68480\n"
    "s_add_u32 s63, s63, 8\n"
    "v_lshl_add_u32 v252, v249, 1, s94\n"
    "ds_read_b32 v252, v252\n"
    "s_sub_u32 s95, s95, s76\n"
    "s_ashr_i32 s95, s95, 4\n"
    "s_min_i32 s95, s95, s74\n"
    "s_max_i32 s72, s95, s75\n"
    "v_cvt_pk_bf16_f32 v18, v132, v133\n"
    "v_cvt_pk_bf16_f32 v19, v134, v135\n"
    "v_mfma_f32_4x4x4_16b_bf16 v[128:131], v[184:185], v[56:57], v[208:211] cbsz:2 abid:0\n"
    "v_cvt_pk_bf16_f32 v20, v136, v137\n"
    "v_cvt_pk_bf16_f32 v21, v138, v139\n"
    "v_mfma_f32_4x4x4_16b_bf16 v[132:135], v[184:185], v[58:59], v[212:215] cbsz:2 abid:1\n"
    "v_cvt_pk_bf16_f32 v22, v140, v141\n"
    "v_cvt_pk_bf16_f32 v23, v142, v143\n"
    "v_mfma_f32_4x4x4_16b_bf16 v[136:139], v[184:185], v[60:61], v[216:219] cbsz:2 abid:2\n"
    "v_cvt_pk_bf16_f32 v24, v128, v129\n"
    "v_cvt_pk_bf16_f32 v25, v130, v131\n"
    "v_mfma_f32_4x4x4_16b_bf16 v[140:143], v[184:185], v[62:63], v[220:223] cbsz:2 abid:3\n"
    "v_cvt_pk_bf16_f32 v26, v132, v133\n"
    "v_cvt_pk_bf16_f32 v27, v134, v135\n"
    "v_cvt_pk_bf16_f32 v28, v136, v137\n"
    "v_cvt_pk_bf16_f32 v29, v138, v139\n"
    "v_exp_f32 v152, v152 clamp\n"
    "v_cvt_pk_bf16_f32 v30, v140, v141\n"
    "v_cvt_pk_bf16_f32 v31, v142, v143\n"
    "v_mfma_f32_16x16x32_bf16 v[128:131], v[0:3], v[160:163], v[112:115]\n"
    "v_exp_f32 v144, v144 clamp\n"
    "v_mfma_f32_16x16x32_bf16 v[128:131], v[4:7], v[164:167], v[128:131]\n"
    "v_exp_f32 v145, v145 clamp\n"
    "v_mfma_f32_16x16x32_bf16 v[132:135], v[8:11], v[160:163], v[116:119]\n"
    "v_exp_f32 v146, v146 clamp\n"
    "v_mfma_f32_16x16x32_bf16 v[132:135], v[12:15], v[164:167], v[132:135]\n"
    "v_exp_f32 v147, v147 clamp\n"
    "v_mfma_f32_16x16x32_bf16 v[136:139], v[16:19], v[160:163], v[120:123]\n"
    "v_exp_f32 v148, v148 clamp\n"
    "v_mfma_f32_16x16x32_bf16 v[136:139], v[20:23], v[164:167], v[136:139]\n"
    "v_exp_f32 v149, v149 clamp\n"
    "v_mfma_f32_16x16x32_bf16 v[140:143], v[24:27], v[160:163], v[124:127]\n"
    "v_exp_f32 v150, v150 clamp\n"
    "v_mfma_f32_16x16x32_bf16 v[140:143], v[28:31], v[164:167], v[140:143]\n"
    "v_exp_f32 v151, v151 clamp\n"
    "s_add_u32 m0, s68, 4096\n"
    "v_exp_f32 v153, v153 clamp\n"
    "v_add_u32 v253, s67, v244\n"
    "v_mfma_f32_4x4x4_16b_bf16 v[224:227], v[228:229], v[168:169], v[224:227] cbsz:4 abid:0\n"
    "v_exp_f32 v154, v154 clamp\n"
    "v_exp_f32 v155, v155 clamp\n"
    "v_mfma_f32_4x4x4_16b_bf16 v[224:227], v[228:229], v[170:171], v[224:227] cbsz:4 abid:1\n"
    "v_exp_f32 v156, v156 clamp\n"
    "v_exp_f32 v157, v157 clamp\n"
    "v_mfma_f32_4x4x4_16b_bf16 v[224:227], v[228:229], v[172:173], v[224:227] cbsz:4 abid:2\n"
    "v_exp_f32 v158, v158 clamp\n"
    "v_exp_f32 v159, v159 clamp\n"
    "v_mfma_f32_4x4x4_16b_bf16 v[224:227], v[228:229], v[174:175], v[224:227] cbsz:4 abid:3\n"
    "s_waitcnt vmcnt(4)\n"
    "ds_read_b64_tr_b16 v[168:169], v240 offset:4096\n"
    "ds_read_b64_tr_b16 v[170:171], v241 offset:4352\n"
    "ds_read_b64_tr_b16 v[172:173], v242 offset:4608\n"
    "ds_read_b64_tr_b16 v[174:175], v243 offset:4864\n"
    "ds_read_b32 v236, v253 offset:0\n"
    "ds_read_b32 v238, v253 offset:4\n"
    "ds_read_b128 v[160:163], v234 offset:6144\n"
    "ds_read_b128 v[164:167], v235 offset:6144\n"
    "s_waitcnt lgkmcnt(8)\n"
    "s_nop 0\n"
    "v_readlane_b32 s20, v252, 0\n"
    "v_readlane_b32 s21, v252, 1\n"
    "v_readlane_b32 s22, v252, 2\n"
    "v_readlane_b32 s23, v252, 3\n"
    "v_readlane_b32 s24, v252, 4\n"
    "v_readlane_b32 s25, v252, 5\n"
    "v_readlane_b32 s26, v252, 6\n"
    "v_readlane_b32 s27, v252, 7\n"
    "v_pk_fma_f32 v[144:145], v[144:145], s[70:71], v[96:97]\n"
    "v_pk_fma_f32 v[146:147], v[146:147], s[70:71], v[98:99]\n"
    "v_pk_add_f32 v[230:231], v[144:145], v[146:147]\n"
    "v_pk_mul_f32 v[232:233], v[144:145], v[146:147]\n"
    "v_pk_fma_f32 v[148:149], v[148:149], s[70:71], v[100:101]\n"
    "v_pk_fma_f32 v[230:231], v[230:231], v[148:149], v[232:233]\n"
    "v_pk_mul_f32 v[232:233], v[232:233], v[148:149]\n"
    "v_pk_fma_f32 v[150:151], v[150:151], s[70:71], v[102:103]\n"
    "v_pk_fma_f32 v[230:231], v[230:231], v[150:151], v[232:233]\n"
    "v_pk_mul_f32 v[232:233], v[232:233], v[150:151]\n"
    "v_pk_fma_f32 v[152:153], v[152:153], s[70:71], v[104:105]\n"
    "v_pk_fma_f32 v[230:231], v[230:231], v[152:153], v[232:233]\n"
    "v_pk_mul_f32 v[232:233], v[232:233], v[152:153]\n"
    "v_pk_fma_f32 v[154:155], v[154:155], s[70:71], v[106:107]\n"
    "v_pk_fma_f32 v[230:231], v[230:231], v[154:155], v[232:233]\n"
    "v_pk_mul_f32 v[232:233], v[232:233], v[154:155]\n"
    "v_pk_fma_f32 v[156:157], v[156:157], s[70:71], v[108:109]\n"
    "v_pk_fma_f32 v[230:231], v[230:231], v[156:157], v[232:233]\n"
    "v_pk_mul_f32 v[232:233], v[232:233], v[156:157]\n"
    "v_pk_fma_f32 v[158:159], v[158:159], s[70:71], v[110:111]\n"
    "v_pk_fma_f32 v[230:231], v[230:231], v[158:159], v[232:233]\n"
    "v_pk_mul_f32 v[232:233], v[232:233], v[158:159]\n"
    "v_rcp_f32 v232, v232\n"
    "v_rcp_f32 v233, v233\n"
    "v_fma_f32 v230, v230, v232, v247\n"
    "v_fmac_f32 v230, v231, v233\n"
    "v_lshlrev_b32 v232, 2, v249\n"
    "v_lshrrev_b32 v233, 2, v249\n"
    "v_and_b32 v232, 56, v232\n"
    "v_and_b32 v233, 4, v233\n"
    "v_add3_u32 v232, v232, v233, s81\n"
    "ds_read_b32 v232, v232\n"
    "s_add_u32 s81, s81, 64\n"
    "s_waitcnt lgkmcnt(0)\n"
    "v_lshlrev_b32 v232, 1, v232\n"
    "buffer_load_ushort v232, v232, s[44:47], 0 offen\n"
    "s_waitcnt vmcnt(0)\n"
    "v_lshlrev_b32 v232, 16, v232\n"
    "v_mul_f32 v230, v230, v232\n"
    "v_cvt_pk_bf16_f32 v230, v230, v230\n"
    "s_add_u32 s67, s67, 64\n"
    "s_waitcnt lgkmcnt(0)\n"
    "ds_write_b16 v245, v230\n"
    "ds_read_b64 v[228:229], v246\n"
    "s_lshl1_add_u32 s76, s72, s76\n"
    "buffer_load_dwordx4 v[236:237], s[40:43], 0 idxen offen offset:0 nt lds\n"
    "buffer_load_dwordx4 v[238:239], s[40:43], 0 idxen offen offset:1024 nt lds\n"
    "s_and_b32 s62, s58, 63\n"
    "s_mul_i32 s62, s62, 272\n"
    "s_mov_b32 s58, s59\n"
    "s_mov_b32 s59, s60\n"
    "s_mov_b32 s81, s65\n"
    "s_xor_b32 s65, s65, s64\n"
    "s_mov_b32 s80, s83\n"
    "s_add_u32 s82, s65, 256\n"
    "s_cmp_lt_i32 s59, s57\n"
    "s_cselect_b32 s76, s76, -1\n"
    "s_bitcmp1_b32 s58, 30\n"
    "s_cbranch_scc0 .Ldin_A2\n"
    ".Ldin_A2s:\n"
    "v_mfma_f32_16x16x32_bf16 v[144:147], v[0:3], v[160:163], v[112:115]\n"
    "v_exp_f32 v128, v128 clamp\n"
    "v_mfma_f32_16x16x32_bf16 v[144:147], v[4:7], v[164:167], v[144:147]\n"
    "v_exp_f32 v129, v129 clamp\n"
    "v_mfma_f32_16x16x32_bf16 v[148:151], v[8:11], v[160:163], v[116:119]\n"
    "v_exp_f32 v130, v130 clamp\n"
    "v_mfma_f32_16x16x32_bf16 v[148:151], v[12:15], v[164:167], v[148:151]\n"
    "v_exp_f32 v131, v131 clamp\n"
    "v_mfma_f32_16x16x32_bf16 v[152:155], v[16:19], v[160:163], v[120:123]\n"
    "v_exp_f32 v132, v132 clamp\n"
    "v_mfma_f32_16x16x32_bf16 v[152:155], v[20:23], v[164:167], v[152:155]\n"
    "v_exp_f32 v133, v133 clamp\n"
    "v_mfma_f32_16x16x32_bf16 v[156:159], v[24:27], v[160:163], v[124:127]\n"
    "v_exp_f32 v134, v134 clamp\n"
    "v_mfma_f32_16x16x32_bf16 v[156:159], v[28:31], v[164:167], v[156:159]\n"
    "v_exp_f32 v135, v135 clamp\n"
    "v_exp_f32 v136, v136 clamp\n"
    "s_mov_b32 m0, s62\n"
    "v_exp_f32 v137, v137 clamp\n"
    "v_add_u32 v253, s67, v244\n"
    "s_waitcnt lgkmcnt(0)\n"
    "v_mfma_f32_4x4x4_16b_bf16 v[224:227], v[228:229], v[176:177], v[224:227] cbsz:4 abid:0\n"
    "v_exp_f32 v138, v138 clamp\n"
    "v_exp_f32 v139, v139 clamp\n"
    "v_mfma_f32_4x4x4_16b_bf16 v[224:227], v[228:229], v[178:179], v[224:227] cbsz:4 abid:1\n"
    "v_exp_f32 v140, v140 clamp\n"
    "v_exp_f32 v141, v141 clamp\n"
    "v_mfma_f32_4x4x4_16b_bf16 v[224:227], v[228:229], v[180:181], v[224:227] cbsz:4 abid:2\n"
    "v_exp_f32 v142, v142 clamp\n"
    "v_exp_f32 v143, v143 clamp\n"
    "v_mfma_f32_4x4x4_16b_bf16 v[224:227], v[228:229], v[182:183], v[224:227] cbsz:4 abid:3\n"
    "s_waitcnt vmcnt(4)\n"
    "ds_read_b64_tr_b16 v[176:177], v240 offset:6144\n"
    "ds_read_b64_tr_b16 v[178:179], v241 offset:6400\n"
    "ds_read_b64_tr_b16 v[180:181], v242 offset:6656\n"
    "ds_read_b64_tr_b16 v[182:183], v243 offset:6912\n"
    "ds_read_b32 v236, v253 offset:0\n"
    "ds_read_b32 v238, v253 offset:4\n"
    "ds_read_b128 v[160:163], v234 offset:0\n"
    "ds_read_b128 v[164:167], v235 offset:0\n"
    "s_cbranch_vccz .Ldin_A2s_np\n"
    "s_setprio 1\n"
    ".Ldin_A2s_np:\n"
    "v_pk_fma_f32 v[128:129], v[128:129], s[70:71], v[96:97]\n"
    "v_pk_fma_f32 v[130:131], v[130:131], s[70:71], v[98:99]\n"
    "v_pk_add_f32 v[190:191], v[224:225], v[226:227]\n"
    "v_add_f32 v190, v190, v191\n"
    "ds_write_addtid_b32 v190 offset:32768\n"
    "s_add_u32 m0, s68, 6144\n"
    "s_bfe_u32 s94, s59, 0x180006\n"
    "s_mul_i32 s86, s94, s56\n"
    "v_mbcnt_lo_u32_b32 v184, s20, 0\n"
    "v_mbcnt_lo_u32_b32 v185, s22, 0\n"
    "v_mbcnt_lo_u32_b32 v186, s24, 0\n"
    "v_mbcnt_lo_u32_b32 v187, s26, 0\n"
    "s_bcnt1_i32_b64 s90, s[20:21]\n"
    "s_bcnt1_i32_b64 s91, s[22:23]\n"
    "s_bcnt1_i32_b64 s92, s[24:25]\n"
    "s_bcnt1_i32_b64 s93, s[26:27]\n"
    "v_mbcnt_hi_u32_b32 v184, s21, v184\n"
    "v_mbcnt_hi_u32_b32 v185, s23, v185\n"
    "v_mbcnt_hi_u32_b32 v186, s25, v186\n"
    "v_mbcnt_hi_u32_b32 v187, s27, v187\n"
    "s_lshl2_add_u32 s90, s90, s65\n"
    "s_lshl2_add_u32 s91, s91, s90\n"
    "s_lshl2_add_u32 s92, s92, s91\n"
    "s_lshl2_add_u32 s93, s93, s92\n"
    "v_add_u32_e64 v188, s86, v251\n"
    "v_pk_add_f32 v[230:231], v[128:129], v[130:131]\n"
    "v_pk_mul_f32 v[232:233], v[128:129], v[130:131]\n"
    "v_pk_fma_f32 v[132:133], v[132:133], s[70:71], v[100:101]\n"
    "v_pk_fma_f32 v[230:231], v[230:231], v[132:133], v[232:233]\n"
    "v_pk_mul_f32 v[232:233], v[232:233], v[132:133]\n"
    "v_lshl_add_u32 v184, v184, 2, s65\n"
    "v_lshl_add_u32 v185, v185, 2, s90\n"
    "v_lshl_add_u32 v186, v186, 2, s91\n"
    "v_lshl_add_u32 v187, v187, 2, s92\n"
    "v_mov_b32 v191, -1\n"
    "v_lshl_add_u32 v190, v249, 1, s93\n"
    "ds_write2_b32 v190, v191, v191 offset1:64\n"
    "s_mov_b64 exec, s[20:21]\n"
    "ds_write_b32 v184, v188\n"
    "s_mov_b64 exec, s[22:23]\n"
    "v_add_u32 v189, 64, v188\n"
    "ds_write_b32 v185, v189\n"
    "s_mov_b64 exec, s[24:25]\n"
    "v_add_u32 v190, 128, v188\n"
    "ds_write_b32 v186, v190\n"
    "s_mov_b64 exec, s[26:27]\n"
    "v_add_u32 v189, 192, v188\n"
    "ds_write_b32 v187, v189\n"
    "s_mov_b64 exec, -1\n"
    "s_add_u32 s95, s93, 60\n"
    "s_add_u32 s94, s82, 128\n"
    "s_andn2_b32 s95, s95, 63\n"
    "s_max_u32 s83, s95, s94\n"
    "v_pk_fma_f32 v[134:135], v[134:135], s[70:71], v[102:103]\n"
    "v_pk_fma_f32 v[230:231], v[230:231], v[134:135], v[232:233]\n"
    "v_pk_mul_f32 v[232:233], v[232:233], v[134:135]\n"
    "v_pk_fma_f32 v[136:137], v[136:137], s[70:71], v[104:105]\n"
    "v_pk_fma_f32 v[230:231], v[230:231], v[136:137], v[232:233]\n"
    "v_pk_mul_f32 v[232:233], v[232:233], v[136:137]\n"
    "v_pk_fma_f32 v[138:139], v[138:139], s[70:71], v[106:107]\n"
    "v_pk_fma_f32 v[230:231], v[230:231], v[138:139], v[232:233]\n"
    "v_pk_mul_f32 v[232:233], v[232:233], v[138:139]\n"
    "v_pk_fma_f32 v[140:141], v[140:141], s[70:71], v[108:109]\n"
    "v_pk_fma_f32 v[230:231], v[230:231], v[140:141], v[232:233]\n"
    "v_pk_mul_f32 v[232:233], v[232:233], v[140:141]\n"
    "v_pk_fma_f32 v[142:143], v[142:143], s[70:71], v[110:111]\n"
    "v_pk_fma_f32 v[230:231], v[230:231], v[142:143], v[232:233]\n"
    "v_pk_mul_f32 v[232:233], v[232:233], v[142:143]\n"
    "v_rcp_f32 v232, v232\n"
    "v_rcp_f32 v233, v233\n"
    "v_fma_f32 v230, v230, v232, v247\n"
    "v_fmac_f32 v230, v231, v233\n"
    "v_lshlrev_b32 v232, 2, v249\n"
    "v_lshrrev_b32 v233, 2, v249\n"
    "v_and_b32 v232, 56, v232\n"
    "v_and_b32 v233, 4, v233\n"
    "v_add3_u32 v232, v232, v233, s81\n"
    "ds_read_b32 v232, v232\n"
    "s_add_u32 s81, s81, 64\n"
    "s_waitcnt lgkmcnt(0)\n"
    "v_lshlrev_b32 v232, 1, v232\n"
    "buffer_load_ushort v232, v232, s[44:47], 0 offen\n"
    "s_waitcnt vmcnt(0)\n"
    "v_lshlrev_b32 v232, 16, v232\n"
    "v_mul_f32 v230, v230, v232\n"
    "v_cvt_pk_bf16_f32 v230, v230, v230\n"
    "s_add_u32 s67, s67, 64\n"
    "s_waitcnt lgkmcnt(0)\n"
    "ds_write_b16 v245, v230\n"
    "ds_read_b64 v[228:229], v246\n"
    "buffer_load_dwordx4 v[236:237], s[40:43], 0 idxen offen offset:0 nt lds\n"
    "buffer_load_dwordx4 v[238:239], s[40:43], 0 idxen offen offset:1024 nt lds\n"
    ".Ldin_B3s:\n"
    "v_mfma_f32_16x16x32_bf16 v[128:131], v[0:3], v[160:163], v[112:115]\n"
    "v_exp_f32 v144, v144 clamp\n"
    "v_mfma_f32_16x16x32_bf16 v[128:131], v[4:7], v[164:167], v[128:131]\n"
    "v_exp_f32 v145, v145 clamp\n"
    "v_mfma_f32_16x16x32_bf16 v[132:135], v[8:11], v[160:163], v[116:119]\n"
    "v_exp_f32 v146, v146 clamp\n"
    "v_mfma_f32_16x16x32_bf16 v[132:135], v[12:15], v[164:167], v[132:135]\n"
    "v_exp_f32 v147, v147 clamp\n"
    "v_mfma_f32_16x16x32_bf16 v[136:139], v[16:19], v[160:163], v[120:123]\n"
    "v_exp_f32 v148, v148 clamp\n"
    "v_mfma_f32_16x16x32_bf16 v[136:139], v[20:23], v[164:167], v[136:139]\n"
    "v_exp_f32 v149, v149 clamp\n"
    "v_mfma_f32_16x16x32_bf16 v[140:143], v[24:27], v[160:163], v[124:127]\n"
    "v_exp_f32 v150, v150 clamp\n"
    "v_mfma_f32_16x16x32_bf16 v[140:143], v[28:31], v[164:167], v[140:143]\n"
    "v_exp_f32 v151, v151 clamp\n"
    "v_exp_f32 v152, v152 clamp\n"
    "s_cmp_eq_u32 s67, s80\n"
    "s_cselect_b32 s67, s65, s67\n"
    "s_and_b32 s94, s59, 63\n"
    "s_mul_i32 s94, s94, 272\n"
    "v_add_u32 v252, s94, v250\n"
    "s_add_u32 m0, s68, 0\n"
    "v_exp_f32 v153, v153 clamp\n"
    "v_add_u32_e64 v253, s67, v244\n"
    "s_waitcnt lgkmcnt(0)\n"
    "v_mfma_f32_4x4x4_16b_bf16 v[224:227], v[228:229], v[168:169], 0 cbsz:4 abid:0\n"
    "v_exp_f32 v154, v154 clamp\n"
    "v_exp_f32 v155, v155 clamp\n"
    "v_mfma_f32_4x4x4_16b_bf16 v[224:227], v[228:229], v[170:171], v[224:227] cbsz:4 abid:1\n"
    "v_exp_f32 v156, v156 clamp\n"
    "v_exp_f32 v157, v157 clamp\n"
    "v_mfma_f32_4x4x4_16b_bf16 v[224:227], v[228:229], v[172:173], v[224:227] cbsz:4 abid:2\n"
    "v_exp_f32 v158, v158 clamp\n"
    "v_exp_f32 v159, v159 clamp\n"
    "v_mfma_f32_4x4x4_16b_bf16 v[224:227], v[228:229], v[174:175], v[224:227] cbsz:4 abid:3\n"
    "s_waitcnt vmcnt(4)\n"
    "s_memrealtime s[34:35]\n"
    "ds_read_b64_tr_b16 v[168:169], v240 offset:0\n"
    "ds_read_b64_tr_b16 v[170:171], v241 offset:256\n"
    "ds_read_b64_tr_b16 v[172:173], v242 offset:512\n"
    "ds_read_b64_tr_b16 v[174:175], v243 offset:768\n"
    "ds_read_b32 v236, v253 offset:0\n"
    "ds_read_b32 v238, v253 offset:4\n"
    "ds_read_b128 v[160:163], v234 offset:2048\n"
    "ds_read_b128 v[164:167], v235 offset:2048\n"
    "v_add_u32_e64 v184, v248, v252\n"
    "ds_read_b64 v[184:185], v184\n"
    "v_pk_fma_f32 v[144:145], v[144:145], s[70:71], v[96:97]\n"
    "v_pk_fma_f32 v[146:147], v[146:147], s[70:71], v[98:99]\n"
    "v_pk_add_f32 v[230:231], v[144:145], v[146:147]\n"
    "v_pk_mul_f32 v[232:233], v[144:145], v[146:147]\n"
    "v_pk_fma_f32 v[148:149], v[148:149], s[70:71], v[100:101]\n"
    "v_pk_fma_f32 v[230:231], v[230:231], v[148:149], v[232:233]\n"
    "v_pk_mul_f32 v[232:233], v[232:233], v[148:149]\n"
    "v_pk_fma_f32 v[150:151], v[150:151], s[70:71], v[102:103]\n"
    "v_pk_fma_f32 v[230:231], v[230:231], v[150:151], v[232:233]\n"
    "v_pk_mul_f32 v[232:233], v[232:233], v[150:151]\n"
    "v_pk_fma_f32 v[152:153], v[152:153], s[70:71], v[104:105]\n"
    "v_pk_fma_f32 v[230:231], v[230:231], v[152:153], v[232:233]\n"
    "v_pk_mul_f32 v[232:233], v[232:233], v[152:153]\n"
    "v_pk_fma_f32 v[154:155], v[154:155], s[70:71], v[106:107]\n"
    "v_pk_fma_f32 v[230:231], v[230:231], v[154:155], v[232:233]\n"
    "v_pk_mul_f32 v[232:233], v[232:233], v[154:155]\n"
    "v_pk_fma_f32 v[156:157], v[156:157], s[70:71], v[108:109]\n"
    "v_pk_fma_f32 v[230:231], v[230:231], v[156:157], v[232:233]\n"
    "v_pk_mul_f32 v[232:233], v[232:233], v[156:157]\n"
    "v_pk_fma_f32 v[158:159], v[158:159], s[70:71], v[110:111]\n"
    "v_pk_fma_f32 v[230:231], v[230:231], v[158:159], v[232:233]\n"
    "v_pk_mul_f32 v[232:233], v[232:233], v[158:159]\n"
    "v_rcp_f32 v232, v232\n"
    "v_rcp_f32 v233, v233\n"
    "v_fma_f32 v230, v230, v232, v247\n"
    "v_fmac_f32 v230, v231, v233\n"
    "v_lshlrev_b32 v232, 2, v249\n"
    "v_lshrrev_b32 v233, 2, v249\n"
    "v_and_b32 v232, 56, v232\n"
    "v_and_b32 v233, 4, v233\n"
    "v_add3_u32 v232, v232, v233, s81\n"
    "ds_read_b32 v232, v232\n"
    "s_add_u32 s81, s81, 64\n"
    "s_waitcnt lgkmcnt(0)\n"
    "v_lshlrev_b32 v232, 1, v232\n"
    "buffer_load_ushort v232, v232, s[44:47], 0 offen\n"
    "s_waitcnt vmcnt(0)\n"
    "v_lshlrev_b32 v232, 16, v232\n"
    "v_mul_f32 v230, v230, v232\n"
    "v_cvt_pk_bf16_f32 v230, v230, v230\n"
    "s_add_u32 s67, s67, 64\n"
    "s_waitcnt lgkmcnt(0)\n"
    "ds_write_b16 v245, v230\n"
    "ds_read_b64 v[228:229], v246\n"
    "v_and_b32 v184, v254, v184\n"
    "v_and_b32 v185, v255, v185\n"
    "s_cmp_le_u32 s34, s76\n"
    "s_cbranch_scc1 .Ldin_B3s_wait\n"
    ".Ldin_B3s_go:\n"
    "s_sub_u32 s94, s34, 60\n"
    "s_max_u32 s76, s76, s94\n"
    "s_add_u32 s76, s76, s72\n"
    ".Ldin_B3s_dma:\n"
    "buffer_load_dwordx4 v[236:237], s[40:43], 0 idxen offen offset:0 nt lds\n"
    "buffer_load_dwordx4 v[238:239], s[40:43], 0 idxen offen offset:1024 nt lds\n"
    ".Ldin_B3s_nd:\n"
    "s_branch .Ldin_G0s\n"
    ".Ldin_L1s_poll:\n"
    "s_sleep 1\n"
    "ds_read_b64 v[232:233], v253\n"
    "s_waitcnt lgkmcnt(0)\n"
    "v_readfirstlane_b32 s60, v232\n"
    "s_cmp_eq_u32 s60, -1\n"
    "s_cbranch_scc1 .Ldin_L1s_poll\n"
    "s_branch .Ldin_L1s_pr\n"
    ".Ldin_B3s_wait:\n"
    "s_cmp_lt_i32 s59, s57\n"
    "s_cbranch_scc1 .Ldin_B3s_w2\n"
    "s_sub_u32 s94, s67, s65\n"
    "s_cmp_gt_u32 s94, 256\n"
    "s_cbranch_scc1 .Ldin_B3s_dma\n"
    "s_cmp_eq_u32 s94, 64\n"
    "s_waitcnt vmcnt(2)\n"
    "s_cbranch_scc1 .Ldin_B3s_nd\n"
    "s_waitcnt vmcnt(0)\n"
    "s_branch .Ldin_B3s_nd\n"
    ".Ldin_B3s_slp:\n"
    "s_sleep 1\n"
    ".Ldin_B3s_w2:\n"
    "s_memrealtime s[34:35]\n"
    "s_waitcnt lgkmcnt(0)\n"
    "s_sub_u32 s94, s34, s76\n"
    "s_cmp_lt_i32 s94, 0\n"
    "s_cbranch_scc1 .Ldin_B3s_slp\n"
    "s_branch .Ldin_B3s_go\n"
    ".p2align 5\n"
    ".Ldin_L0s:\n"
    "s_setprio 0\n"
    "s_cmp_ge_i32 s59, s57\n"
    "s_cbranch_scc1 .Ldin_E0s\n"
    "s_add_u32 s76, s76, s73\n"
    "v_mov_b32_e64 v253, s63\n"
    "ds_read_b64 v[232:233], v253\n"
    "v_mfma_f32_4x4x4_16b_bf16 v[144:147], v[184:185], v[32:33], v[64:67] cbsz:2 abid:0\n"
    "ds_read_b128 v[112:115], v252 offset:17408\n"
    "v_mfma_f32_4x4x4_16b_bf16 v[148:151], v[184:185], v[34:35], v[68:71] cbsz:2 abid:1\n"
    "ds_read_b128 v[116:119], v252 offset:17424\n"
    "v_mfma_f32_4x4x4_16b_bf16 v[152:155], v[184:185], v[36:37], v[72:75] cbsz:2 abid:2\n"
    "ds_read_b128 v[120:123], v252 offset:17536\n"
    "ds_read_b128 v[124:127], v252 offset:17552\n"
    "v_cvt_pk_bf16_f32 v0, v144, v145\n"
    "v_cvt_pk_bf16_f32 v1, v146, v147\n"
    "v_mfma_f32_4x4x4_16b_bf16 v[156:159], v[184:185], v[38:39], v[76:79] cbsz:2 abid:3\n"
    "v_cvt_pk_bf16_f32 v2, v148, v149\n"
    "v_cvt_pk_bf16_f32 v3, v150, v151\n"
    "v_mfma_f32_4x4x4_16b_bf16 v[144:147], v[184:185], v[40:41], v[80:83] cbsz:2 abid:0\n"
    "v_cvt_pk_bf16_f32 v4, v152, v153\n"
    "v_cvt_pk_bf16_f32 v5, v154, v155\n"
    "v_mfma_f32_4x4x4_16b_bf16 v[148:151], v[184:185], v[42:43], v[84:87] cbsz:2 abid:1\n"
    "v_cvt_pk_bf16_f32 v6, v156, v157\n"
    "v_cvt_pk_bf16_f32 v7, v158, v159\n"
    "v_mfma_f32_4x4x4_16b_bf16 v[152:155], v[184:185], v[44:45], v[88:91] cbsz:2 abid:2\n"
    "v_cvt_pk_bf16_f32 v8, v144, v145\n"
    "v_cvt_pk_bf16_f32 v9, v146, v147\n"
    "v_mfma_f32_4x4x4_16b_bf16 v[156:159], v[184:185], v[46:47], v[92:95] cbsz:2 abid:3\n"
    "v_cvt_pk_bf16_f32 v10, v148, v149\n"
    "v_cvt_pk_bf16_f32 v11, v150, v151\n"
    "v_mfma_f32_4x4x4_16b_bf16 v[144:147], v[184:185], v[48:49], v[192:195] cbsz:2 abid:0\n"
    "v_cvt_pk_bf16_f32 v12, v152, v153\n"
    "v_cvt_pk_bf16_f32 v13, v154, v155\n"
    "v_mfma_f32_4x4x4_16b_bf16 v[148:151], v[184:185], v[50:51], v[196:199] cbsz:2 abid:1\n"
    "v_cvt_pk_bf16_f32 v14, v156, v157\n"
    "v_cvt_pk_bf16_f32 v15, v158, v159\n"
    "v_mfma_f32_4x4x4_16b_bf16 v[152:155], v[184:185], v[52:53], v[200:203] cbsz:2 abid:2\n"
    "s_waitcnt lgkmcnt(0)\n"
    "v_readfirstlane_b32 s60, v232\n"
    "v_cvt_pk_bf16_f32 v16, v144, v145\n"
    "v_cvt_pk_bf16_f32 v17, v146, v147\n"
    "v_mfma_f32_4x4x4_16b_bf16 v[156:159], v[184:185], v[54:55], v[204:207] cbsz:2 abid:3\n"
    "s_cmp_eq_u32 s60, -1\n"
    "s_cbranch_scc1 .Ldin_L0s_poll\n"
    ".Ldin_L0s_pr:\n"
    "v_readfirstlane_b32 s95, v233\n"
    "s_and_b32 s94, s60, 63\n"
    "s_lshl_b32 s94, s94, 6\n"
    "s_add_u32 s94, s94, 68480\n"
    "s_add_u32 s63, s63, 8\n"
    "v_lshl_add_u32 v252, v249, 1, s94\n"
    "ds_read_b32 v252, v252\n"
    "s_sub_u32 s95, s95, s76\n"
    "s_ashr_i32 s95, s95, 4\n"
    "s_min_i32 s95, s95, s74\n"
    "s_max_i32 s72, s95, s75\n"
    "v_cvt_pk_bf16_f32 v18, v148, v149\n"
    "v_cvt_pk_bf16_f32 v19, v150, v151\n"
    "v_mfma_f32_4x4x4_16b_bf16 v[144:147], v[184:185], v[56:57], v[208:211] cbsz:2 abid:0\n"
    "v_cvt_pk_bf16_f32 v20, v152, v153\n"
    "v_cvt_pk_bf16_f32 v21, v154, v155\n"
    "v_mfma_f32_4x4x4_16b_bf16 v[148:151], v[184:185], v[58:59], v[212:215] cbsz:2 abid:1\n"
    "v_cvt_pk_bf16_f32 v22, v156, v157\n"
    "v_cvt_pk_bf16_f32 v23, v158, v159\n"
    "v_mfma_f32_4x4x4_16b_bf16 v[152:155], v[184:185], v[60:61], v[216:219] cbsz:2 abid:2\n"
    "v_cvt_pk_bf16_f32 v24, v144, v145\n"
    "v_cvt_pk_bf16_f32 v25, v146, v147\n"
    "v_mfma_f32_4x4x4_16b_bf16 v[156:159], v[184:185], v[62:63], v[220:223] cbsz:2 abid:3\n"
    "v_cvt_pk_bf16_f32 v26, v148, v149\n"
    "v_cvt_pk_bf16_f32 v27, v150, v151\n"
    "v_cvt_pk_bf16_f32 v28, v152, v153\n"
    "v_cvt_pk_bf16_f32 v29, v154, v155\n"
    "v_exp_f32 v136, v136 clamp\n"
    "v_cvt_pk_bf16_f32 v30, v156, v157\n"
    "v_cvt_pk_bf16_f32 v31, v158, v159\n"
    "v_mfma_f32_16x16x32_bf16 v[144:147], v[0:3], v[160:163], v[112:115]\n"
    "v_exp_f32 v128, v128 clamp\n"
    "v_mfma_f32_16x16x32_bf16 v[144:147], v[4:7], v[164:167], v[144:147]\n"
    "v_exp_f32 v129, v129 clamp\n"
    "v_mfma_f32_16x16x32_bf16 v[148:151], v[8:11], v[160:163], v[116:119]\n"
    "v_exp_f32 v130, v130 clamp\n"
    "v_mfma_f32_16x16x32_bf16 v[148:151], v[12:15], v[164:167], v[148:151]\n"
    "v_exp_f32 v131, v131 clamp\n"
    "v_mfma_f32_16x16x32_bf16 v[152:155], v[16:19], v[160:163], v[120:123]\n"
    "v_exp_f32 v132, v132 clamp\n"
    "v_mfma_f32_16x16x32_bf16 v[152:155], v[20:23], v[164:167], v[152:155]\n"
    "v_exp_f32 v133, v133 clamp\n"
    "v_mfma_f32_16x16x32_bf16 v[156:159], v[24:27], v[160:163], v[124:127]\n"
    "v_exp_f32 v134, v134 clamp\n"
    "v_mfma_f32_16x16x32_bf16 v[156:159], v[28:31], v[164:167], v[156:159]\n"
    "v_exp_f32 v135, v135 clamp\n"
    "s_add_u32 m0, s68, 2048\n"
    "v_exp_f32 v137, v137 clamp\n"
    "v_add_u32 v253, s67, v244\n"
    "v_mfma_f32_4x4x4_16b_bf16 v[224:227], v[228:229], v[176:177], v[224:227] cbsz:4 abid:0\n"
    "v_exp_f32 v138, v138 clamp\n"
    "v_exp_f32 v139, v139 clamp\n"
    "v_mfma_f32_4x4x4_16b_bf16 v[224:227], v[228:229], v[178:179], v[224:227] cbsz:4 abid:1\n"
    "v_exp_f32 v140, v140 clamp\n"
    "v_exp_f32 v141, v141 clamp\n"
    "v_mfma_f32_4x4x4_16b_bf16 v[224:227], v[228:229], v[180:181], v[224:227] cbsz:4 abid:2\n"
    "v_exp_f32 v142, v142 clamp\n"
    "v_exp_f32 v143, v143 clamp\n"
    "v_mfma_f32_4x4x4_16b_bf16 v[224:227], v[228:229], v[182:183], v[224:227] cbsz:4 abid:3\n"
    "s_waitcnt vmcnt(4)\n"
    "ds_read_b64_tr_b16 v[176:177], v240 offset:2048\n"
    "ds_read_b64_tr_b16 v[178:179], v241 offset:2304\n"
    "ds_read_b64_tr_b16 v[180:181], v242 offset:2560\n"
    "ds_read_b64_tr_b16 v[182:183], v243 offset:2816\n"
    "ds_read_b32 v236, v253 offset:0\n"
    "ds_read_b32 v238, v253 offset:4\n"
    "ds_read_b128 v[160:163], v234 offset:4096\n"
    "ds_read_b128 v[164:167], v235 offset:4096\n"
    "s_waitcnt lgkmcnt(8)\n"
    "s_nop 0\n"
    "v_readlane_b32 s20, v252, 0\n"
    "v_readlane_b32 s21, v252, 1\n"
    "v_readlane_b32 s22, v252, 2\n"
    "v_readlane_b32 s23, v252, 3\n"
    "v_readlane_b32 s24, v252, 4\n"
    "v_readlane_b32 s25, v252, 5\n"
    "v_readlane_b32 s26, v252, 6\n"
    "v_readlane_b32 s27, v252, 7\n"
    "v_pk_fma_f32 v[128:129], v[128:129], s[70:71], v[96:97]\n"
    "v_pk_fma_f32 v[130:131], v[130:131], s[70:71], v[98:99]\n"
    "v_pk_add_f32 v[230:231], v[128:129], v[130:131]\n"
    "v_pk_mul_f32 v[232:233], v[128:129], v[130:131]\n"
    "v_pk_fma_f32 v[132:133], v[132:133], s[70:71], v[100:101]\n"
    "v_pk_fma_f32 v[230:231], v[230:231], v[132:133], v[232:233]\n"
    "v_pk_mul_f32 v[232:233], v[232:233], v[132:133]\n"
    "v_pk_fma_f32 v[134:135], v[134:135], s[70:71], v[102:103]\n"
    "v_pk_fma_f32 v[230:231], v[230:231], v[134:135], v[232:233]\n"
    "v_pk_mul_f32 v[232:233], v[232:233], v[134:135]\n"
    "v_pk_fma_f32 v[136:137], v[136:137], s[70:71], v[104:105]\n"
    "v_pk_fma_f32 v[230:231], v[230:231], v[136:137], v[232:233]\n"
    "v_pk_mul_f32 v[232:233], v[232:233], v[136:137]\n"
    "v_pk_fma_f32 v[138:139], v[138:139], s[70:71], v[106:107]\n"
    "v_pk_fma_f32 v[230:231], v[230:231], v[138:139], v[232:233]\n"
    "v_pk_mul_f32 v[232:233], v[232:233], v[138:139]\n"
    "v_pk_fma_f32 v[140:141], v[140:141], s[70:71], v[108:109]\n"
    "v_pk_fma_f32 v[230:231], v[230:231], v[140:141], v[232:233]\n"
    "v_pk_mul_f32 v[232:233], v[232:233], v[140:141]\n"
    "v_pk_fma_f32 v[142:143], v[142:143], s[70:71], v[110:111]\n"
    "v_pk_fma_f32 v[230:231], v[230:231], v[142:143], v[232:233]\n"
    "v_pk_mul_f32 v[232:233], v[232:233], v[142:143]\n"
    "v_rcp_f32 v232, v232\n"
    "v_rcp_f32 v233, v233\n"
    "v_fma_f32 v230, v230, v232, v247\n"
    "v_fmac_f32 v230, v231, v233\n"
    "v_lshlrev_b32 v232, 2, v249\n"
    "v_lshrrev_b32 v233, 2, v249\n"
    "v_and_b32 v232, 56, v232\n"
    "v_and_b32 v233, 4, v233\n"
    "v_add3_u32 v232, v232, v233, s81\n"
    "ds_read_b32 v232, v232\n"
    "s_add_u32 s81, s81, 64\n"
    "s_waitcnt lgkmcnt(0)\n"
    "v_lshlrev_b32 v232, 1, v232\n"
    "buffer_load_ushort v232, v232, s[44:47], 0 offen\n"
    "s_waitcnt vmcnt(0)\n"
    "v_lshlrev_b32 v232, 16, v232\n"
    "v_mul_f32 v230, v230, v232\n"
    "v_cvt_pk_bf16_f32 v230, v230, v230\n"
    "s_add_u32 s67, s67, 64\n"
    "s_waitcnt lgkmcnt(0)\n"
    "ds_write_b16 v245, v230\n"
    "ds_read_b64 v[228:229], v246\n"
    "s_lshl1_add_u32 s76, s72, s76\n"
    "buffer_load_dwordx4 v[236:237], s[40:43], 0 idxen offen offset:0 nt lds\n"
    "buffer_load_dwordx4 v[238:239], s[40:43], 0 idxen offen offset:1024 nt lds\n"
    "s_and_b32 s62, s58, 63\n"
    "s_mul_i32 s62, s62, 272\n"
    "s_mov_b32 s58, s59\n"
    "s_mov_b32 s59, s60\n"
    "s_mov_b32 s81, s65\n"
    "s_xor_b32 s65, s65, s64\n"
    "s_mov_b32 s80, s83\n"
    "s_add_u32 s82, s65, 256\n"
    "s_cmp_lt_i32 s59, s57\n"
    "s_cselect_b32 s76, s76, -1\n"
    "s_bitcmp1_b32 s58, 30\n"
    "s_cbranch_scc0 .Ldin_A1\n"
    ".Ldin_A1s:\n"
    "v_mfma_f32_16x16x32_bf16 v[128:131], v[0:3], v[160:163], v[112:115]\n"
    "v_exp_f32 v144, v144 clamp\n"
    "v_mfma_f32_16x16x32_bf16 v[128:131], v[4:7], v[164:167], v[128:131]\n"
    "v_exp_f32 v145, v145 clamp\n"
    "v_mfma_f32_16x16x32_bf16 v[132:135], v[8:11], v[160:163], v[116:119]\n"
    "v_exp_f32 v146, v146 clamp\n"
    "v_mfma_f32_16x16x32_bf16 v[132:135], v[12:15], v[164:167], v[132:135]\n"
    "v_exp_f32 v147, v147 clamp\n"
    "v_mfma_f32_16x16x32_bf16 v[136:139], v[16:19], v[160:163], v[120:123]\n"
    "v_exp_f32 v148, v148 clamp\n"
    "v_mfma_f32_16x16x32_bf16 v[136:139], v[20:23], v[164:167], v[136:139]\n"
    "v_exp_f32 v149, v149 clamp\n"
    "v_mfma_f32_16x16x32_bf16 v[140:143], v[24:27], v[160:163], v[124:127]\n"
    "v_exp_f32 v150, v150 clamp\n"
    "v_mfma_f32_16x16x32_bf16 v[140:143], v[28:31], v[164:167], v[140:143]\n"
    "v_exp_f32 v151, v151 clamp\n"
    "v_exp_f32 v152, v152 clamp\n"
    "s_mov_b32 m0, s62\n"
    "v_exp_f32 v153, v153 clamp\n"
    "v_add_u32 v253, s67, v244\n"
    "s_waitcnt lgkmcnt(0)\n"
    "v_mfma_f32_4x4x4_16b_bf16 v[224:227], v[228:229], v[168:169], v[224:227] cbsz:4 abid:0\n"
    "v_exp_f32 v154, v154 clamp\n"
    "v_exp_f32 v155, v155 clamp\n"
    "v_mfma_f32_4x4x4_16b_bf16 v[224:227], v[228:229], v[170:171], v[224:227] cbsz:4 abid:1\n"
    "v_exp_f32 v156, v156 clamp\n"
    "v_exp_f32 v157, v157 clamp\n"
    "v_mfma_f32_4x4x4_16b_bf16 v[224:227], v[228:229], v[172:173], v[224:227] cbsz:4 abid:2\n"
    "v_exp_f32 v158, v158 clamp\n"
    "v_exp_f32 v159, v159 clamp\n"
    "v_mfma_f32_4x4x4_16b_bf16 v[224:227], v[228:229], v[174:175], v[224:227] cbsz:4 abid:3\n"
    "s_waitcnt vmcnt(4)\n"
    "ds_read_b64_tr_b16 v[168:169], v240 offset:4096\n"
    "ds_read_b64_tr_b16 v[170:171], v241 offset:4352\n"
    "ds_read_b64_tr_b16 v[172:173], v242 offset:4608\n"
    "ds_read_b64_tr_b16 v[174:175], v243 offset:4864\n"
    "ds_read_b32 v236, v253 offset:0\n"
    "ds_read_b32 v238, v253 offset:4\n"
    "ds_read_b128 v[160:163], v234 offset:6144\n"
    "ds_read_b128 v[164:167], v235 offset:6144\n"
    "s_cbranch_vccz .Ldin_A1s_np\n"
    "s_setprio 1\n"
    ".Ldin_A1s_np:\n"
    "v_pk_fma_f32 v[144:145], v[144:145], s[70:71], v[96:97]\n"
    "v_pk_fma_f32 v[146:147], v[146:147], s[70:71], v[98:99]\n"
    "v_pk_add_f32 v[190:191], v[224:225], v[226:227]\n"
    "v_add_f32 v190, v190, v191\n"
    "ds_write_addtid_b32 v190 offset:32768\n"
    "s_add_u32 m0, s68, 4096\n"
    "s_bfe_u32 s94, s59, 0x180006\n"
    "s_mul_i32 s86, s94, s56\n"
    "v_mbcnt_lo_u32_b32 v184, s20, 0\n"
    "v_mbcnt_lo_u32_b32 v185, s22, 0\n"
    "v_mbcnt_lo_u32_b32 v186, s24, 0\n"
    "v_mbcnt_lo_u32_b32 v187, s26, 0\n"
    "s_bcnt1_i32_b64 s90, s[20:21]\n"
    "s_bcnt1_i32_b64 s91, s[22:23]\n"
    "s_bcnt1_i32_b64 s92, s[24:25]\n"
    "s_bcnt1_i32_b64 s93, s[26:27]\n"
    "v_mbcnt_hi_u32_b32 v184, s21, v184\n"
    "v_mbcnt_hi_u32_b32 v185, s23, v185\n"
    "v_mbcnt_hi_u32_b32 v186, s25, v186\n"
    "v_mbcnt_hi_u32_b32 v187, s27, v187\n"
    "s_lshl2_add_u32 s90, s90, s65\n"
    "s_lshl2_add_u32 s91, s91, s90\n"
    "s_lshl2_add_u32 s92, s92, s91\n"
    "s_lshl2_add_u32 s93, s93, s92\n"
    "v_add_u32_e64 v188, s86, v251\n"
    "v_pk_add_f32 v[230:231], v[144:145], v[146:147]\n"
    "v_pk_mul_f32 v[232:233], v[144:145], v[146:147]\n"
    "v_pk_fma_f32 v[148:149], v[148:149], s[70:71], v[100:101]\n"
    "v_pk_fma_f32 v[230:231], v[230:231], v[148:149], v[232:233]\n"
    "v_pk_mul_f32 v[232:233], v[232:233], v[148:149]\n"
    "v_lshl_add_u32 v184, v184, 2, s65\n"
    "v_lshl_add_u32 v185, v185, 2, s90\n"
    "v_lshl_add_u32 v186, v186, 2, s91\n"
    "v_lshl_add_u32 v187, v187, 2, s92\n"
    "v_mov_b32 v191, -1\n"
    "v_lshl_add_u32 v190, v249, 1, s93\n"
    "ds_write2_b32 v190, v191, v191 offset1:64\n"
    "s_mov_b64 exec, s[20:21]\n"
    "ds_write_b32 v184, v188\n"
    "s_mov_b64 exec, s[22:23]\n"
    "v_add_u32 v189, 64, v188\n"
    "ds_write_b32 v185, v189\n"
    "s_mov_b64 exec, s[24:25]\n"
    "v_add_u32 v190, 128, v188\n"
    "ds_write_b32 v186, v190\n"
    "s_mov_b64 exec, s[26:27]\n"
    "v_add_u32 v189, 192, v188\n"
    "ds_write_b32 v187, v189\n"
    "s_mov_b64 exec, -1\n"
    "s_add_u32 s95, s93, 60\n"
    "s_add_u32 s94, s82, 128\n"
    "s_andn2_b32 s95, s95, 63\n"
    "s_max_u32 s83, s95, s94\n"
    "v_pk_fma_f32 v[150:151], v[150:151], s[70:71], v[102:103]\n"
    "v_pk_fma_f32 v[230:231], v[230:231], v[150:151], v[232:233]\n"
    "v_pk_mul_f32 v[232:233], v[232:233], v[150:151]\n"
    "v_pk_fma_f32 v[152:153], v[152:153], s[70:71], v[104:105]\n"
    "v_pk_fma_f32 v[230:231], v[230:231], v[152:153], v[232:233]\n"
    "v_pk_mul_f32 v[232:233], v[232:233], v[152:153]\n"
    "v_pk_fma_f32 v[154:155], v[154:155], s[70:71], v[106:107]\n"
    "v_pk_fma_f32 v[230:231], v[230:231], v[154:155], v[232:233]\n"
    "v_pk_mul_f32 v[232:233], v[232:233], v[154:155]\n"
    "v_pk_fma_f32 v[156:157], v[156:157], s[70:71], v[108:109]\n"
    "v_pk_fma_f32 v[230:231], v[230:231], v[156:157], v[232:233]\n"
    "v_pk_mul_f32 v[232:233], v[232:233], v[156:157]\n"
    "v_pk_fma_f32 v[158:159], v[158:159], s[70:71], v[110:111]\n"
    "v_pk_fma_f32 v[230:231], v[230:231], v[158:159], v[232:233]\n"
    "v_pk_mul_f32 v[232:233], v[232:233], v[158:159]\n"
    "v_rcp_f32 v232, v232\n"
    "v_rcp_f32 v233, v233\n"
    "v_fma_f32 v230, v230, v232, v247\n"
    "v_fmac_f32 v230, v231, v233\n"
    "v_lshlrev_b32 v232, 2, v249\n"
    "v_lshrrev_b32 v233, 2, v249\n"
    "v_and_b32 v232, 56, v232\n"
    "v_and_b32 v233, 4, v233\n"
    "v_add3_u32 v232, v232, v233, s81\n"
    "ds_read_b32 v232, v232\n"
    "s_add_u32 s81, s81, 64\n"
    "s_waitcnt lgkmcnt(0)\n"
    "v_lshlrev_b32 v232, 1, v232\n"
    "buffer_load_ushort v232, v232, s[44:47], 0 offen\n"
    "s_waitcnt vmcnt(0)\n"
    "v_lshlrev_b32 v232, 16, v232\n"
    "v_mul_f32 v230, v230, v232\n"
    "v_cvt_pk_bf16_f32 v230, v230, v230\n"
    "s_add_u32 s67, s67, 64\n"
    "s_waitcnt lgkmcnt(0)\n"
    "ds_write_b16 v245, v230\n"
    "ds_read_b64 v[228:229], v246\n"
    "buffer_load_dwordx4 v[236:237], s[40:43], 0 idxen offen offset:0 nt lds\n"
    "buffer_load_dwordx4 v[238:239], s[40:43], 0 idxen offen offset:1024 nt lds\n"
    ".Ldin_B2s:\n"
    "v_mfma_f32_16x16x32_bf16 v[144:147], v[0:3], v[160:163], v[112:115]\n"
    "v_exp_f32 v128, v128 clamp\n"
    "v_mfma_f32_16x16x32_bf16 v[144:147], v[4:7], v[164:167], v[144:147]\n"
    "v_exp_f32 v129, v129 clamp\n"
    "v_mfma_f32_16x16x32_bf16 v[148:151], v[8:11], v[160:163], v[116:119]\n"
    "v_exp_f32 v130, v130 clamp\n"
    "v_mfma_f32_16x16x32_bf16 v[148:151], v[12:15], v[164:167], v[148:151]\n"
    "v_exp_f32 v131, v131 clamp\n"
    "v_mfma_f32_16x16x32_bf16 v[152:155], v[16:19], v[160:163], v[120:123]\n"
    "v_exp_f32 v132, v132 clamp\n"
    "v_mfma_f32_16x16x32_bf16 v[152:155], v[20:23], v[164:167], v[152:155]\n"
    "v_exp_f32 v133, v133 clamp\n"
    "v_mfma_f32_16x16x32_bf16 v[156:159], v[24:27], v[160:163], v[124:127]\n"
    "v_exp_f32 v134, v134 clamp\n"
    "v_mfma_f32_16x16x32_bf16 v[156:159], v[28:31], v[164:167], v[156:159]\n"
    "v_exp_f32 v135, v135 clamp\n"
    "v_exp_f32 v136, v136 clamp\n"
    "s_cmp_eq_u32 s67, s80\n"
    "s_cselect_b32 s67, s65, s67\n"
    "s_and_b32 s94, s59, 63\n"
    "s_mul_i32 s94, s94, 272\n"
    "v_add_u32 v252, s94, v250\n"
    "s_add_u32 m0, s68, 6144\n"
    "v_exp_f32 v137, v137 clamp\n"
    "v_add_u32 v253, s67, v244\n"
    "s_waitcnt lgkmcnt(0)\n"
    "v_mfma_f32_4x4x4_16b_bf16 v[224:227], v[228:229], v[176:177], 0 cbsz:4 abid:0\n"
    "v_exp_f32 v138, v138 clamp\n"
    "v_exp_f32 v139, v139 clamp\n"
    "v_mfma_f32_4x4x4_16b_bf16 v[224:227], v[228:229], v[178:179], v[224:227] cbsz:4 abid:1\n"
    "v_exp_f32 v140, v140 clamp\n"
    "v_exp_f32 v141, v141 clamp\n"
    "v_mfma_f32_4x4x4_16b_bf16 v[224:227], v[228:229], v[180:181], v[224:227] cbsz:4 abid:2\n"
    "v_exp_f32 v142, v142 clamp\n"
    "v_exp_f32 v143, v143 clamp\n"
    "v_mfma_f32_4x4x4_16b_bf16 v[224:227], v[228:229], v[182:183], v[224:227] cbsz:4 abid:3\n"
    "s_waitcnt vmcnt(4)\n"
    "s_memrealtime s[34:35]\n"
    "ds_read_b64_tr_b16 v[176:177], v240 offset:6144\n"
    "ds_read_b64_tr_b16 v[178:179], v241 offset:6400\n"
    "ds_read_b64_tr_b16 v[180:181], v242 offset:6656\n"
    "ds_read_b64_tr_b16 v[182:183], v243 offset:6912\n"
    "ds_read_b32 v236, v253 offset:0\n"
    "ds_read_b32 v238, v253 offset:4\n"
    "ds_read_b128 v[160:163], v234 offset:0\n"
    "ds_read_b128 v[164:167], v235 offset:0\n"
    "v_add_u32_e64 v184, v248, v252\n"
    "ds_read_b64 v[184:185], v184\n"
    "v_pk_fma_f32 v[128:129], v[128:129], s[70:71], v[96:97]\n"
    "v_pk_fma_f32 v[130:131], v[130:131], s[70:71], v[98:99]\n"
    "v_pk_add_f32 v[230:231], v[128:129], v[130:131]\n"
    "v_pk_mul_f32 v[232:233], v[128:129], v[130:131]\n"
    "v_pk_fma_f32 v[132:133], v[132:133], s[70:71], v[100:101]\n"
    "v_pk_fma_f32 v[230:231], v[230:231], v[132:133], v[232:233]\n"
    "v_pk_mul_f32 v[232:233], v[232:233], v[132:133]\n"
    "v_pk_fma_f32 v[134:135], v[134:135], s[70:71], v[102:103]\n"
    "v_pk_fma_f32 v[230:231], v[230:231], v[134:135], v[232:233]\n"
    "v_pk_mul_f32 v[232:233], v[232:233], v[134:135]\n"
    "v_pk_fma_f32 v[136:137], v[136:137], s[70:71], v[104:105]\n"
    "v_pk_fma_f32 v[230:231], v[230:231], v[136:137], v[232:233]\n"
    "v_pk_mul_f32 v[232:233], v[232:233], v[136:137]\n"
    "v_pk_fma_f32 v[138:139], v[138:139], s[70:71], v[106:107]\n"
    "v_pk_fma_f32 v[230:231], v[230:231], v[138:139], v[232:233]\n"
    "v_pk_mul_f32 v[232:233], v[232:233], v[138:139]\n"
    "v_pk_fma_f32 v[140:141], v[140:141], s[70:71], v[108:109]\n"
    "v_pk_fma_f32 v[230:231], v[230:231], v[140:141], v[232:233]\n"
    "v_pk_mul_f32 v[232:233], v[232:233], v[140:141]\n"
    "v_pk_fma_f32 v[142:143], v[142:143], s[70:71], v[110:111]\n"
    "v_pk_fma_f32 v[230:231], v[230:231], v[142:143], v[232:233]\n"
    "v_pk_mul_f32 v[232:233], v[232:233], v[142:143]\n"
    "v_rcp_f32 v232, v232\n"
    "v_rcp_f32 v233, v233\n"
    "v_fma_f32 v230, v230, v232, v247\n"
    "v_fmac_f32 v230, v231, v233\n"
    "v_lshlrev_b32 v232, 2, v249\n"
    "v_lshrrev_b32 v233, 2, v249\n"
    "v_and_b32 v232, 56, v232\n"
    "v_and_b32 v233, 4, v233\n"
    "v_add3_u32 v232, v232, v233, s81\n"
    "ds_read_b32 v232, v232\n"
    "s_add_u32 s81, s81, 64\n"
    "s_waitcnt lgkmcnt(0)\n"
    "v_lshlrev_b32 v232, 1, v232\n"
    "buffer_load_ushort v232, v232, s[44:47], 0 offen\n"
    "s_waitcnt vmcnt(0)\n"
    "v_lshlrev_b32 v232, 16, v232\n"
    "v_mul_f32 v230, v230, v232\n"
    "v_cvt_pk_bf16_f32 v230, v230, v230\n"
    "s_add_u32 s67, s67, 64\n"
    "s_waitcnt lgkmcnt(0)\n"
    "ds_write_b16 v245, v230\n"
    "ds_read_b64 v[228:229], v246\n"
    "v_and_b32 v184, v254, v184\n"
    "v_and_b32 v185, v255, v185\n"
    "s_cmp_le_u32 s34, s76\n"
    "s_cbranch_scc1 .Ldin_B2s_wait\n"
    ".Ldin_B2s_go:\n"
    "s_sub_u32 s94, s34, 60\n"
    "s_max_u32 s76, s76, s94\n"
    "s_add_u32 s76, s76, s72\n"
    ".Ldin_B2s_dma:\n"
    "buffer_load_dwordx4 v[236:237], s[40:43], 0 idxen offen offset:0 nt lds\n"
    "buffer_load_dwordx4 v[238:239], s[40:43], 0 idxen offen offset:1024 nt lds\n"
    ".Ldin_B2s_nd:\n"
    "s_branch .Ldin_G3s\n"
    ".Ldin_L0s_poll:\n"
    "s_sleep 1\n"
    "ds_read_b64 v[232:233], v253\n"
    "s_waitcnt lgkmcnt(0)\n"
    "v_readfirstlane_b32 s60, v232\n"
    "s_cmp_eq_u32 s60, -1\n"
    "s_cbranch_scc1 .Ldin_L0s_poll\n"
    "s_branch .Ldin_L0s_pr\n"
    ".Ldin_B2s_wait:\n"
    "s_cmp_lt_i32 s59, s57\n"
    "s_cbranch_scc1 .Ldin_B2s_w2\n"
    "s_sub_u32 s94, s67, s65\n"
    "s_cmp_gt_u32 s94, 256\n"
    "s_cbranch_scc1 .Ldin_B2s_dma\n"
    "s_cmp_eq_u32 s94, 64\n"
    "s_waitcnt vmcnt(2)\n"
    "s_cbranch_scc1 .Ldin_B2s_nd\n"
    "s_waitcnt vmcnt(0)\n"
    "s_branch .Ldin_B2s_nd\n"
    ".Ldin_B2s_slp:\n"
    "s_sleep 1\n"
    ".Ldin_B2s_w2:\n"
    "s_memrealtime s[34:35]\n"
    "s_waitcnt lgkmcnt(0)\n"
    "s_sub_u32 s94, s34, s76\n"
    "s_cmp_lt_i32 s94, 0\n"
    "s_cbranch_scc1 .Ldin_B2s_slp\n"
    "s_branch .Ldin_B2s_go\n"
    ".p2align 5\n"
    ".Ldin_L3s:\n"
    "s_setprio 0\n"
    "s_cmp_ge_i32 s59, s57\n"
    "s_cbranch_scc1 .Ldin_E1s\n"
    "s_add_u32 s76, s76, s73\n"
    "v_mov_b32_e64 v253, s63\n"
    "ds_read_b64 v[232:233], v253\n"
    "v_mfma_f32_4x4x4_16b_bf16 v[128:131], v[184:185], v[32:33], v[64:67] cbsz:2 abid:0\n"
    "ds_read_b128 v[112:115], v252 offset:17408\n"
    "v_mfma_f32_4x4x4_16b_bf16 v[132:135], v[184:185], v[34:35], v[68:71] cbsz:2 abid:1\n"
    "ds_read_b128 v[116:119], v252 offset:17424\n"
    "v_mfma_f32_4x4x4_16b_bf16 v[136:139], v[184:185], v[36:37], v[72:75] cbsz:2 abid:2\n"
    "ds_read_b128 v[120:123], v252 offset:17536\n"
    "ds_read_b128 v[124:127], v252 offset:17552\n"
    "v_cvt_pk_bf16_f32 v0, v128, v129\n"
    "v_cvt_pk_bf16_f32 v1, v130, v131\n"
    "v_mfma_f32_4x4x4_16b_bf16 v[140:143], v[184:185], v[38:39], v[76:79] cbsz:2 abid:3\n"
    "v_cvt_pk_bf16_f32 v2, v132, v133\n"
    "v_cvt_pk_bf16_f32 v3, v134, v135\n"
    "v_mfma_f32_4x4x4_16b_bf16 v[128:131], v[184:185], v[40:41], v[80:83] cbsz:2 abid:0\n"
    "v_cvt_pk_bf16_f32 v4, v136, v137\n"
    "v_cvt_pk_bf16_f32 v5, v138, v139\n"
    "v_mfma_f32_4x4x4_16b_bf16 v[132:135], v[184:185], v[42:43], v[84:87] cbsz:2 abid:1\n"
    "v_cvt_pk_bf16_f32 v6, v140, v141\n"
    "v_cvt_pk_bf16_f32 v7, v142, v143\n"
    "v_mfma_f32_4x4x4_16b_bf16 v[136:139], v[184:185], v[44:45], v[88:91] cbsz:2 abid:2\n"
    "v_cvt_pk_bf16_f32 v8, v128, v129\n"
    "v_cvt_pk_bf16_f32 v9, v130, v131\n"
    "v_mfma_f32_4x4x4_16b_bf16 v[140:143], v[184:185], v[46:47], v[92:95] cbsz:2 abid:3\n"
    "v_cvt_pk_bf16_f32 v10, v132, v133\n"
    "v_cvt_pk_bf16_f32 v11, v134, v135\n"
    "v_mfma_f32_4x4x4_16b_bf16 v[128:131], v[184:185], v[48:49], v[192:195] cbsz:2 abid:0\n"
    "v_cvt_pk_bf16_f32 v12, v136, v137\n"
    "v_cvt_pk_bf16_f32 v13, v138, v139\n"
    "v_mfma_f32_4x4x4_16b_bf16 v[132:135], v[184:185], v[50:51], v[196:199] cbsz:2 abid:1\n"
    "v_cvt_pk_bf16_f32 v14, v140, v141\n"
    "v_cvt_pk_bf16_f32 v15, v142, v143\n"
    "v_mfma_f32_4x4x4_16b_bf16 v[136:139], v[184:185], v[52:53], v[200:203] cbsz:2 abid:2\n"
    "s_waitcnt lgkmcnt(0)\n"
    "v_readfirstlane_b32 s60, v232\n"
    "v_cvt_pk_bf16_f32 v16, v128, v129\n"
    "v_cvt_pk_bf16_f32 v17, v130, v131\n"
    "v_mfma_f32_4x4x4_16b_bf16 v[140:143], v[184:185], v[54:55], v[204:207] cbsz:2 abid:3\n"
    "s_cmp_eq_u32 s60, -1\n"
    "s_cbranch_scc1 .Ldin_L3s_poll\n"
    ".Ldin_L3s_pr:\n"
    "v_readfirstlane_b32 s95, v233\n"
    "s_and_b32 s94, s60, 63\n"
    "s_lshl_b32 s94, s94, 6\n"
    "s_add_u32 s94, s94, 68480\n"
    "s_add_u32 s63, s63, 8\n"
    "v_lshl_add_u32 v252, v249, 1, s94\n"
    "ds_read_b32 v252, v252\n"
    "s_sub_u32 s95, s95, s76\n"
    "s_ashr_i32 s95, s95, 4\n"
    "s_min_i32 s95, s95, s74\n"
    "s_max_i32 s72, s95, s75\n"
    "v_cvt_pk_bf16_f32 v18, v132, v133\n"
    "v_cvt_pk_bf16_f32 v19, v134, v135\n"
    "v_mfma_f32_4x4x4_16b_bf16 v[128:131], v[184:185], v[56:57], v[208:211] cbsz:2 abid:0\n"
    "v_cvt_pk_bf16_f32 v20, v136, v137\n"
    "v_cvt_pk_bf16_f32 v21, v138, v139\n"
    "v_mfma_f32_4x4x4_16b_bf16 v[132:135], v[184:185], v[58:59], v[212:215] cbsz:2 abid:1\n"
    "v_cvt_pk_bf16_f32 v22, v140, v141\n"
    "v_cvt_pk_bf16_f32 v23, v142, v143\n"
    "v_mfma_f32_4x4x4_16b_bf16 v[136:139], v[184:185], v[60:61], v[216:219] cbsz:2 abid:2\n"
    "v_cvt_pk_bf16_f32 v24, v128, v129\n"
    "v_cvt_pk_bf16_f32 v25, v130, v131\n"
    "v_mfma_f32_4x4x4_16b_bf16 v[140:143], v[184:185], v[62:63], v[220:223] cbsz:2 abid:3\n"
    "v_cvt_pk_bf16_f32 v26, v132, v133\n"
    "v_cvt_pk_bf16_f32 v27, v134, v135\n"
    "v_cvt_pk_bf16_f32 v28, v136, v137\n"
    "v_cvt_pk_bf16_f32 v29, v138, v139\n"
    "v_exp_f32 v152, v152 clamp\n"
    "v_cvt_pk_bf16_f32 v30, v140, v141\n"
    "v_cvt_pk_bf16_f32 v31, v142, v143\n"
    "v_mfma_f32_16x16x32_bf16 v[128:131], v[0:3], v[160:163], v[112:115]\n"
    "v_exp_f32 v144, v144 clamp\n"
    "v_mfma_f32_16x16x32_bf16 v[128:131], v[4:7], v[164:167], v[128:131]\n"
    "v_exp_f32 v145, v145 clamp\n"
    "v_mfma_f32_16x16x32_bf16 v[132:135], v[8:11], v[160:163], v[116:119]\n"
    "v_exp_f32 v146, v146 clamp\n"
    "v_mfma_f32_16x16x32_bf16 v[132:135], v[12:15], v[164:167], v[132:135]\n"
    "v_exp_f32 v147, v147 clamp\n"
    "v_mfma_f32_16x16x32_bf16 v[136:139], v[16:19], v[160:163], v[120:123]\n"
    "v_exp_f32 v148, v148 clamp\n"
    "v_mfma_f32_16x16x32_bf16 v[136:139], v[20:23], v[164:167], v[136:139]\n"
    "v_exp_f32 v149, v149 clamp\n"
    "v_mfma_f32_16x16x32_bf16 v[140:143], v[24:27], v[160:163], v[124:127]\n"
    "v_exp_f32 v150, v150 clamp\n"
    "v_mfma_f32_16x16x32_bf16 v[140:143], v[28:31], v[164:167], v[140:143]\n"
    "v_exp_f32 v151, v151 clamp\n"
    "s_add_u32 m0, s68, 0\n"
    "v_exp_f32 v153, v153 clamp\n"
    "v_add_u32_e64 v253, s67, v244\n"
    "v_mfma_f32_4x4x4_16b_bf16 v[224:227], v[228:229], v[168:169], v[224:227] cbsz:4 abid:0\n"
    "v_exp_f32 v154, v154 clamp\n"
    "v_exp_f32 v155, v155 clamp\n"
    "v_mfma_f32_4x4x4_16b_bf16 v[224:227], v[228:229], v[170:171], v[224:227] cbsz:4 abid:1\n"
    "v_exp_f32 v156, v156 clamp\n"
    "v_exp_f32 v157, v157 clamp\n"
    "v_mfma_f32_4x4x4_16b_bf16 v[224:227], v[228:229], v[172:173], v[224:227] cbsz:4 abid:2\n"
    "v_exp_f32 v158, v158 clamp\n"
    "v_exp_f32 v159, v159 clamp\n"
    "v_mfma_f32_4x4x4_16b_bf16 v[224:227], v[228:229], v[174:175], v[224:227] cbsz:4 abid:3\n"
    "s_waitcnt vmcnt(4)\n"
    "ds_read_b64_tr_b16 v[168:169], v240 offset:0\n"
    "ds_read_b64_tr_b16 v[170:171], v241 offset:256\n"
    "ds_read_b64_tr_b16 v[172:173], v242 offset:512\n"
    "ds_read_b64_tr_b16 v[174:175], v243 offset:768\n"
    "ds_read_b32 v236, v253 offset:0\n"
    "ds_read_b32 v238, v253 offset:4\n"
    "ds_read_b128 v[160:163], v234 offset:2048\n"
    "ds_read_b128 v[164:167], v235 offset:2048\n"
    "s_waitcnt lgkmcnt(8)\n"
    "s_nop 0\n"
    "v_readlane_b32 s20, v252, 0\n"
    "v_readlane_b32 s21, v252, 1\n"
    "v_readlane_b32 s22, v252, 2\n"
    "v_readlane_b32 s23, v252, 3\n"
    "v_readlane_b32 s24, v252, 4\n"
    "v_readlane_b32 s25, v252, 5\n"
    "v_readlane_b32 s26, v252, 6\n"
    "v_readlane_b32 s27, v252, 7\n"
    "v_pk_fma_f32 v[144:145], v[144:145], s[70:71], v[96:97]\n"
    "v_pk_fma_f32 v[146:147], v[146:147], s[70:71], v[98:99]\n"
    "v_pk_add_f32 v[230:231], v[144:145], v[146:147]\n"
    "v_pk_mul_f32 v[232:233], v[144:145], v[146:147]\n"
    "v_pk_fma_f32 v[148:149], v[148:149], s[70:71], v[100:101]\n"
    "v_pk_fma_f32 v[230:231], v[230:231], v[148:149], v[232:233]\n"
    "v_pk_mul_f32 v[232:233], v[232:233], v[148:149]\n"
    "v_pk_fma_f32 v[150:151], v[150:151], s[70:71], v[102:103]\n"
    "v_pk_fma_f32 v[230:231], v[230:231], v[150:151], v[232:233]\n"
    "v_pk_mul_f32 v[232:233], v[232:233], v[150:151]\n"
    "v_pk_fma_f32 v[152:153], v[152:153], s[70:71], v[104:105]\n"
    "v_pk_fma_f32 v[230:231], v[230:231], v[152:153], v[232:233]\n"
    "v_pk_mul_f32 v[232:233], v[232:233], v[152:153]\n"
    "v_pk_fma_f32 v[154:155], v[154:155], s[70:71], v[106:107]\n"
    "v_pk_fma_f32 v[230:231], v[230:231], v[154:155], v[232:233]\n"
    "v_pk_mul_f32 v[232:233], v[232:233], v[154:155]\n"
    "v_pk_fma_f32 v[156:157], v[156:157], s[70:71], v[108:109]\n"
    "v_pk_fma_f32 v[230:231], v[230:231], v[156:157], v[232:233]\n"
    "v_pk_mul_f32 v[232:233], v[232:233], v[156:157]\n"
    "v_pk_fma_f32 v[158:159], v[158:159], s[70:71], v[110:111]\n"
    "v_pk_fma_f32 v[230:231], v[230:231], v[158:159], v[232:233]\n"
    "v_pk_mul_f32 v[232:233], v[232:233], v[158:159]\n"
    "v_rcp_f32 v232, v232\n"
    "v_rcp_f32 v233, v233\n"
    "v_fma_f32 v230, v230, v232, v247\n"
    "v_fmac_f32 v230, v231, v233\n"
    "v_lshlrev_b32 v232, 2, v249\n"
    "v_lshrrev_b32 v233, 2, v249\n"
    "v_and_b32 v232, 56, v232\n"
    "v_and_b32 v233, 4, v233\n"
    "v_add3_u32 v232, v232, v233, s81\n"
    "ds_read_b32 v232, v232\n"
    "s_add_u32 s81, s81, 64\n"
    "s_waitcnt lgkmcnt(0)\n"
    "v_lshlrev_b32 v232, 1, v232\n"
    "buffer_load_ushort v232, v232, s[44:47], 0 offen\n"
    "s_waitcnt vmcnt(0)\n"
    "v_lshlrev_b32 v232, 16, v232\n"
    "v_mul_f32 v230, v230, v232\n"
    "v_cvt_pk_bf16_f32 v230, v230, v230\n"
    "s_sub_u32 s67, s67, -64\n"
    "s_waitcnt lgkmcnt(0)\n"
    "ds_write_b16 v245, v230\n"
    "ds_read_b64 v[228:229], v246\n"
    "s_lshl1_add_u32 s76, s72, s76\n"
    "buffer_load_dwordx4 v[236:237], s[40:43], 0 idxen offen offset:0 nt lds\n"
    "buffer_load_dwordx4 v[238:239], s[40:43], 0 idxen offen offset:1024 nt lds\n"
    "s_and_b32 s62, s58, 63\n"
    "s_mul_i32 s62, s62, 272\n"
    "s_mov_b32 s58, s59\n"
    "s_mov_b32 s59, s60\n"
    "s_mov_b32 s81, s65\n"
    "s_xor_b32 s65, s65, s64\n"
    "s_mov_b32 s80, s83\n"
    "s_add_u32 s82, s65, 256\n"
    "s_cmp_lt_i32 s59, s57\n"
    "s_cselect_b32 s76, s76, -1\n"
    "s_bitcmp1_b32 s58, 30\n"
    "s_cbranch_scc0 .Ldin_A0\n"
    "s_branch .Ldin_A0s\n"
    ".Ldin_L3s_poll:\n"
    "s_sleep 1\n"
    "ds_read_b64 v[232:233], v253\n"
    "s_waitcnt lgkmcnt(0)\n"
    "v_readfirstlane_b32 s60, v232\n"
    "s_cmp_eq_u32 s60, -1\n"
    "s_cbranch_scc1 .Ldin_L3s_poll\n"
    "s_branch .Ldin_L3s_pr\n"
    ".Ldin_E0s:\n"
    "v_exp_f32 v128, v128 clamp\n"
    "v_exp_f32 v129, v129 clamp\n"
    "v_exp_f32 v130, v130 clamp\n"
    "v_exp_f32 v131, v131 clamp\n"
    "v_exp_f32 v132, v132 clamp\n"
    "v_exp_f32 v133, v133 clamp\n"
    "v_exp_f32 v134, v134 clamp\n"
    "v_exp_f32 v135, v135 clamp\n"
    "s_and_b32 s62, s58, 63\n"
    "s_mul_i32 s62, s62, 272\n"
    "s_waitcnt lgkmcnt(0)\n"
    "v_mfma_f32_4x4x4_16b_bf16 v[224:227], v[228:229], v[176:177], v[224:227] cbsz:4 abid:0\n"
    "v_exp_f32 v136, v136 clamp\n"
    "v_exp_f32 v137, v137 clamp\n"
    "v_mfma_f32_4x4x4_16b_bf16 v[224:227], v[228:229], v[178:179], v[224:227] cbsz:4 abid:1\n"
    "v_exp_f32 v138, v138 clamp\n"
    "v_exp_f32 v139, v139 clamp\n"
    "v_mfma_f32_4x4x4_16b_bf16 v[224:227], v[228:229], v[180:181], v[224:227] cbsz:4 abid:2\n"
    "v_exp_f32 v140, v140 clamp\n"
    "v_exp_f32 v141, v141 clamp\n"
    "v_mfma_f32_4x4x4_16b_bf16 v[224:227], v[228:229], v[182:183], v[224:227] cbsz:4 abid:3\n"
    "v_exp_f32 v142, v142 clamp\n"
    "v_exp_f32 v143, v143 clamp\n"
    "v_pk_fma_f32 v[128:129], v[128:129], s[70:71], v[96:97]\n"
    "v_pk_fma_f32 v[130:131], v[130:131], s[70:71], v[98:99]\n"
    "v_pk_add_f32 v[230:231], v[128:129], v[130:131]\n"
    "v_pk_mul_f32 v[232:233], v[128:129], v[130:131]\n"
    "v_pk_fma_f32 v[132:133], v[132:133], s[70:71], v[100:101]\n"
    "v_pk_fma_f32 v[230:231], v[230:231], v[132:133], v[232:233]\n"
    "v_pk_mul_f32 v[232:233], v[232:233], v[132:133]\n"
    "v_pk_fma_f32 v[134:135], v[134:135], s[70:71], v[102:103]\n"
    "v_pk_fma_f32 v[230:231], v[230:231], v[134:135], v[232:233]\n"
    "v_pk_mul_f32 v[232:233], v[232:233], v[134:135]\n"
    "v_pk_fma_f32 v[136:137], v[136:137], s[70:71], v[104:105]\n"
    "v_pk_fma_f32 v[230:231], v[230:231], v[136:137], v[232:233]\n"
    "v_pk_mul_f32 v[232:233], v[232:233], v[136:137]\n"
    "v_pk_fma_f32 v[138:139], v[138:139], s[70:71], v[106:107]\n"
    "v_pk_fma_f32 v[230:231], v[230:231], v[138:139], v[232:233]\n"
    "v_pk_mul_f32 v[232:233], v[232:233], v[138:139]\n"
    "v_pk_fma_f32 v[140:141], v[140:141], s[70:71], v[108:109]\n"
    "v_pk_fma_f32 v[230:231], v[230:231], v[140:141], v[232:233]\n"
    "v_pk_mul_f32 v[232:233], v[232:233], v[140:141]\n"
    "v_pk_fma_f32 v[142:143], v[142:143], s[70:71], v[110:111]\n"
    "v_pk_fma_f32 v[230:231], v[230:231], v[142:143], v[232:233]\n"
    "v_pk_mul_f32 v[232:233], v[232:233], v[142:143]\n"
    "v_rcp_f32 v232, v232\n"
    "v_rcp_f32 v233, v233\n"
    "v_fma_f32 v230, v230, v232, v247\n"
    "v_fmac_f32 v230, v231, v233\n"
    "v_lshlrev_b32 v232, 2, v249\n"
    "v_lshrrev_b32 v233, 2, v249\n"
    "v_and_b32 v232, 56, v232\n"
    "v_and_b32 v233, 4, v233\n"
    "v_add3_u32 v232, v232, v233, s81\n"
    "ds_read_b32 v232, v232\n"
    "s_add_u32 s81, s81, 64\n"
    "s_waitcnt lgkmcnt(0)\n"
    "v_lshlrev_b32 v232, 1, v232\n"
    "buffer_load_ushort v232, v232, s[44:47], 0 offen\n"
    "s_waitcnt vmcnt(0)\n"
    "v_lshlrev_b32 v232, 16, v232\n"
    "v_mul_f32 v230, v230, v232\n"
    "v_cvt_pk_bf16_f32 v230, v230, v230\n"
    "ds_write_b16 v245, v230\n"
    "ds_read_b64 v[228:229], v246\n"
    "s_branch .Ldin_drain0\n"
    ".Ldin_E1s:\n"
    "v_exp_f32 v144, v144 clamp\n"
    "v_exp_f32 v145, v145 clamp\n"
    "v_exp_f32 v146, v146 clamp\n"
    "v_exp_f32 v147, v147 clamp\n"
    "v_exp_f32 v148, v148 clamp\n"
    "v_exp_f32 v149, v149 clamp\n"
    "v_exp_f32 v150, v150 clamp\n"
    "v_exp_f32 v151, v151 clamp\n"
    "s_and_b32 s62, s58, 63\n"
    "s_mul_i32 s62, s62, 272\n"
    "s_waitcnt lgkmcnt(0)\n"
    "v_mfma_f32_4x4x4_16b_bf16 v[224:227], v[228:229], v[168:169], v[224:227] cbsz:4 abid:0\n"
    "v_exp_f32 v152, v152 clamp\n"
    "v_exp_f32 v153, v153 clamp\n"
    "v_mfma_f32_4x4x4_16b_bf16 v[224:227], v[228:229], v[170:171], v[224:227] cbsz:4 abid:1\n"
    "v_exp_f32 v154, v154 clamp\n"
    "v_exp_f32 v155, v155 clamp\n"
    "v_mfma_f32_4x4x4_16b_bf16 v[224:227], v[228:229], v[172:173], v[224:227] cbsz:4 abid:2\n"
    "v_exp_f32 v156, v156 clamp\n"
    "v_exp_f32 v157, v157 clamp\n"
    "v_mfma_f32_4x4x4_16b_bf16 v[224:227], v[228:229], v[174:175], v[224:227] cbsz:4 abid:3\n"
    "v_exp_f32 v158, v158 clamp\n"
    "v_exp_f32 v159, v159 clamp\n"
    "v_pk_fma_f32 v[144:145], v[144:145], s[70:71], v[96:97]\n"
    "v_pk_fma_f32 v[146:147], v[146:147], s[70:71], v[98:99]\n"
    "v_pk_add_f32 v[230:231], v[144:145], v[146:147]\n"
    "v_pk_mul_f32 v[232:233], v[144:145], v[146:147]\n"
    "v_pk_fma_f32 v[148:149], v[148:149], s[70:71], v[100:101]\n"
    "v_pk_fma_f32 v[230:231], v[230:231], v[148:149], v[232:233]\n"
    "v_pk_mul_f32 v[232:233], v[232:233], v[148:149]\n"
    "v_pk_fma_f32 v[150:151], v[150:151], s[70:71], v[102:103]\n"
    "v_pk_fma_f32 v[230:231], v[230:231], v[150:151], v[232:233]\n"
    "v_pk_mul_f32 v[232:233], v[232:233], v[150:151]\n"
    "v_pk_fma_f32 v[152:153], v[152:153], s[70:71], v[104:105]\n"
    "v_pk_fma_f32 v[230:231], v[230:231], v[152:153], v[232:233]\n"
    "v_pk_mul_f32 v[232:233], v[232:233], v[152:153]\n"
    "v_pk_fma_f32 v[154:155], v[154:155], s[70:71], v[106:107]\n"
    "v_pk_fma_f32 v[230:231], v[230:231], v[154:155], v[232:233]\n"
    "v_pk_mul_f32 v[232:233], v[232:233], v[154:155]\n"
    "v_pk_fma_f32 v[156:157], v[156:157], s[70:71], v[108:109]\n"
    "v_pk_fma_f32 v[230:231], v[230:231], v[156:157], v[232:233]\n"
    "v_pk_mul_f32 v[232:233], v[232:233], v[156:157]\n"
    "v_pk_fma_f32 v[158:159], v[158:159], s[70:71], v[110:111]\n"
    "v_pk_fma_f32 v[230:231], v[230:231], v[158:159], v[232:233]\n"
    "v_pk_mul_f32 v[232:233], v[232:233], v[158:159]\n"
    "v_rcp_f32 v232, v232\n"
    "v_rcp_f32 v233, v233\n"
    "v_fma_f32 v230, v230, v232, v247\n"
    "v_fmac_f32 v230, v231, v233\n"
    "v_lshlrev_b32 v232, 2, v249\n"
    "v_lshrrev_b32 v233, 2, v249\n"
    "v_and_b32 v232, 56, v232\n"
    "v_and_b32 v233, 4, v233\n"
    "v_add3_u32 v232, v232, v233, s81\n"
    "ds_read_b32 v232, v232\n"
    "s_add_u32 s81, s81, 64\n"
    "s_waitcnt lgkmcnt(0)\n"
    "v_lshlrev_b32 v232, 1, v232\n"
    "buffer_load_ushort v232, v232, s[44:47], 0 offen\n"
    "s_waitcnt vmcnt(0)\n"
    "v_lshlrev_b32 v232, 16, v232\n"
    "v_mul_f32 v230, v230, v232\n"
    "v_cvt_pk_bf16_f32 v230, v230, v230\n"
    "ds_write_b16 v245, v230\n"
    "ds_read_b64 v[228:229], v246\n"
    "s_branch .Ldin_drain1\n"
    ".Ldin_exit:\n"

      : "+{s[0:1]}"(kp), "+{v0}"(tid)
      : "{s2}"(blockIdx.x)
      : "memory", "vcc", "scc", "v1", "v2", "v3", "v4", "v5", "v6", "v7", "v8", "v9", "v10", "v11", "v12", "v13", "v14", "v15", "v16", "v17", "v18", "v19", "v20", "v21", "v22", "v23", "v24", "v25", "v26", "v27", "v28", "v29", "v30", "v31", "v32", "v33", "v34", "v35", "v36", "v37", "v38", "v39", "v40", "v41", "v42", "v43", "v44", "v45", "v46", "v47", "v48", "v49", "v50", "v51", "v52", "v53", "v54", "v55", "v56", "v57", "v58", "v59", "v60", "v61", "v62", "v63", "v64", "v65", "v66", "v67", "v68", "v69", "v70", "v71", "v72", "v73", "v74", "v75", "v76", "v77", "v78", "v79", "v80", "v81", "v82", "v83", "v84", "v85", "v86", "v87", "v88", "v89", "v90", "v91", "v92", "v93", "v94", "v95", "v96", "v97", "v98", "v99", "v100", "v101", "v102", "v103", "v104", "v105", "v106", "v107", "v108", "v109", "v110", "v111", "v112", "v113", "v114", "v115", "v116", "v117", "v118", "v119", "v120", "v121", "v122", "v123", "v124", "v125", "v126", "v127", "v128", "v129", "v130", "v131", "v132", "v133", "v134", "v135", "v136", "v137", "v138", "v139", "v140", "v141", "v142", "v143", "v144", "v145", "v146", "v147", "v148", "v149", "v150", "v151", "v152", "v153", "v154", "v155", "v156", "v157", "v158", "v159", "v160", "v161", "v162", "v163", "v164", "v165", "v166", "v167", "v168", "v169", "v170", "v171", "v172", "v173", "v174", "v175", "v176", "v177", "v178", "v179", "v180", "v181", "v182", "v183", "v184", "v185", "v186", "v187", "v188", "v189", "v190", "v191", "v192", "v193", "v194", "v195", "v196", "v197", "v198", "v199", "v200", "v201", "v202", "v203", "v204", "v205", "v206", "v207", "v208", "v209", "v210", "v211", "v212", "v213", "v214", "v215", "v216", "v217", "v218", "v219", "v220", "v221", "v222", "v223", "v224", "v225", "v226", "v227", "v228", "v229", "v230", "v231", "v232", "v233", "v234", "v235", "v236", "v237", "v238", "v239", "v240", "v241", "v242", "v243", "v244", "v245", "v246", "v247", "v248", "v249", "v250", "v251", "v252", "v253", "v254", "v255", "s3", "s4", "s5", "s6", "s7", "s8", "s9", "s10", "s11", "s12", "s13", "s14", "s15", "s16", "s17", "s18", "s19", "s20", "s21", "s22", "s23", "s24", "s25", "s26", "s27", "s28", "s29", "s30", "s31", "s33", "s34", "s35", "s36", "s37", "s38", "s39", "s40", "s41", "s42", "s43", "s44", "s45", "s46", "s47", "s48", "s49", "s50", "s51", "s52", "s53", "s54", "s55", "s56", "s57", "s58", "s59", "s60", "s61", "s62", "s63", "s64", "s65", "s66", "s67", "s68", "s69", "s70", "s71", "s72", "s73", "s74", "s75", "s76", "s77", "s78", "s79", "s80", "s81", "s82", "s83", "s84", "s85", "s86", "s87", "s88", "s89", "s90", "s91", "s92", "s93", "s94", "s95", "s96");
  asm volatile("" ::"s"((uint32_t)(uintptr_t)smem));
}

__global__ __launch_bounds__(64) void din_fallback_kernel(
    const unsigned short* __restrict__ target, const unsigned short* __restrict__ hist,
    const unsigned short* __restrict__ mask, const unsigned short* __restrict__ w1,
    const unsigned short* __restrict__ b1, const unsigned short* __restrict__ w2,
    const unsigned short* __restrict__ b2, unsigned short* __restrict__ out, int B, int L) {
  __shared__ float s_w[64 * 64];
  __shared__ float s_x[64];
  const int b = blockIdx.x, h = threadIdx.x;
  float bias = bf2f(b1[h]);
  for (int d = 0; d < 64; ++d) {
    const float t = bf2f(target[(size_t)b * 64 + d]);
    const float wc = bf2f(w1[(128 + d) * 64 + h]);
    bias += t * (bf2f(w1[(64 + d) * 64 + h]) - wc);
    s_w[d * 64 + h] = bf2f(w1[d * 64 + h]) + wc + t * bf2f(w1[(192 + d) * 64 + h]);
  }
  const float w2h = bf2f(w2[h]), b2v = bf2f(b2[0]);
  float acc = 0.f;
  for (int l = 0; l < L; ++l) {
    const float m = bf2f(mask[(size_t)b * L + l]);
    if (m == 0.f) continue;
    const float x = bf2f(hist[((size_t)b * L + l) * 64 + h]);
    __syncthreads();
    s_x[h] = x;
    __syncthreads();
    float z = bias;
    for (int d = 0; d < 64; ++d) z += s_x[d] * s_w[d * 64 + h];
    float s = w2h / (1.0f + __expf(-z));
    for (int o = 32; o > 0; o >>= 1) s += __shfl_xor(s, o);
    acc += (s + b2v) * m * x;
  }
  const uint32_t u = __float_as_uint(acc);
  out[(size_t)b * 64 + h] = (unsigned short)((u + 0x7fffu + ((u >> 16) & 1u)) >> 16);
}

extern "C" void kernel_launch(void* const* d_in, const int* in_sizes, int n_in, void* d_out,
                              int out_size, void* d_ws, size_t ws_size, hipStream_t stream) {
  const unsigned short* target = (const unsigned short*)d_in[0];
  const unsigned short* hist = (const unsigned short*)d_in[1];
  const unsigned short* mask = (const unsigned short*)d_in[2];
  const unsigned short* w1 = (const unsigned short*)d_in[3];
  const unsigned short* b1 = (const unsigned short*)d_in[4];
  const unsigned short* w2 = (const unsigned short*)d_in[5];
  const unsigned short* b2 = (const unsigned short*)d_in[6];
  const int B = in_sizes[0] / 64;
  const int L = in_sizes[2] / B;
  if ((L & 1) || L > 256 || B >= (1 << 24) - 64) {
    din_fallback_kernel<<<B, 64, 0, stream>>>(target, hist, mask, w1, b1, w2, b2, (unsigned short*)d_out, B, L);
    return;
  }
  const uint32_t grid = (B + 63) / 64, fl = 0x20000u;
  const bool paced = grid >= 224;
  din_kernel<<<grid, 512, 0, stream>>>(hist, (uint32_t)B * L, fl, mask, (uint32_t)B * L * 2u, fl, target, (uint32_t)B * 128u, fl,
                                       (unsigned short*)d_out, (uint32_t)B * 128u, fl, w1, w2, b1, b2, (uint32_t)L, paced ? 52u | 15u << 16 : 0u, (uint32_t)B,
                                       grid * 8u, 2u * L, grid * 16u * L, 0u, 0u);
}
```

```cpp
#include <hip/hip_runtime.h>
#include <stdint.h>

constexpr int SMEM_B = 163712;

__device__ __forceinline__ float bf2f(unsigned short u) { return __uint_as_float(((uint32_t)u) << 16); }

__global__ __launch_bounds__(512, 2) __attribute__((aligned(4096))) void din_kernel(
    const unsigned short* hist, uint32_t rows, uint32_t f0, const unsigned short* mask, uint32_t mask_bytes, uint32_t f1,
    const unsigned short* target, uint32_t target_bytes, uint32_t f2, unsigned short* out, uint32_t out_bytes, uint32_t f3,
    const unsigned short* w1, const unsigned short* w2, const unsigned short* b1, const unsigned short* b2,
    uint32_t L, uint32_t period, uint32_t B, uint32_t round_rows, uint32_t row_bytes, uint32_t round_bytes, uint32_t pad0, uint32_t pad1) {
  __shared__ __attribute__((aligned(128))) char smem[SMEM_B];
  unsigned long long kp = (unsigned long long)(uintptr_t)__builtin_amdgcn_kernarg_segment_ptr();
  uint32_t tid = threadIdx.x;
  asm volatile(
    "s_load_dwordx16 s[40:55], s[0:1], 0x0\n"
    "s_load_dwordx16 s[4:19], s[0:1], 0x40\n"
    "s_memrealtime s[76:77]\n"
    "v_readfirstlane_b32 s3, v0\n"
    "v_and_b32 v251, 63, v0\n"
    "s_lshr_b32 s3, s3, 6\n"
    "s_lshl3_add_u32 s61, s2, s3\n"
    "s_getpc_b64 s[92:93]\n"
    "s_and_b32 s94, s2, 0xf8\n"
    "s_add_u32 s94, s94, s3\n"
    "s_min_u32 s94, s94, 239\n"
    "s_lshl_b32 s94, s94, 7\n"
    "v_mov_b32 v1, s94\n"
    "global_load_dword v1, v1, s[92:93] offset:1024\n"
    "s_cmp_lt_u32 s3, 4\n"
    "s_cbranch_scc0 .Ldin_young\n"
    "v_lshlrev_b32 v105, 3, v251\n"
    "v_mov_b32 v8, 0\n"
    "v_mov_b32 v9, 0\n"
    "v_mov_b32 v10, 0\n"
    "v_and_b32 v2, 48, v251\n"
    "v_and_b32 v3, 8, v251\n"
    "v_lshl_or_b32 v2, v3, 3, v2\n"
    "v_and_b32 v3, 4, v251\n"
    "v_lshl_or_b32 v2, v3, 1, v2\n"
    "v_lshlrev_b32 v249, 1, v251\n"
    "s_mul_i32 s68, s3, 11392\n"
    "v_lshlrev_b32_e64 v248, 4, v251\n"
    "s_add_u32 s68, s68, 72576\n"
    "v_and_b32 v24, 0x60, v249\n"
    "s_add_u32 s67, s68, 8192\n"
    "v_or_b32 v250, 0x8000, v24\n"
    "s_add_u32 s65, s68, 9728\n"
    "v_lshrrev_b32 v20, 4, v251\n"
    "s_xor_b32 s64, s67, s65\n"
    "v_and_b32 v244, 56, v251\n"
    "s_add_u32 s96, s68, 11264\n"
    "v_bitop3_b32 v21, v20, v251, 7 bitop3:0x78\n"
    "s_lshl_b32 s66, s3, 7\n"
    "v_lshlrev_b32 v21, 4, v21\n"
    "s_add_u32 s66, s66, 68512\n"
    "v_or_b32 v237, 0x400, v21\n"
    "s_lshr_b32 s95, s67, 2\n"
    "v_xor_b32 v239, 64, v21\n"
    "s_mov_b32 s38, 0xffff\n"
    "s_mov_b32 s57, 0x7fffffc0\n"
    "v_add_u32 v253, s67, v244\n"
    "v_lshlrev_b32 v105, 3, v251\n"
    "v_mov_b32 v201, v237\n"
    "v_mov_b32 v203, v239\n"
    "v_mov_b32 v205, v237\n"
    "v_mov_b32 v207, v239\n"
    "v_mov_b32 v209, v237\n"
    "v_mov_b32 v211, v239\n"
    "v_mov_b32 v213, v237\n"
    "v_mov_b32 v215, v239\n"
    "v_mov_b32 v220, s95\n"
    "v_mov_b32 v224, 0\n"
    "v_mov_b32 v225, 0\n"
    "v_mov_b32 v226, 0\n"
    "v_mov_b32 v227, 0\n"
    "v_bfe_u32 v21, v251, 1, 3\n"
    "v_xor_b32 v21, v20, v21\n"
    "v_and_b32 v22, 0xf0, v248\n"
    "s_mov_b32 s70, 0x47000000\n"
    "v_lshl_add_u32 v22, v22, 3, s68\n"
    "s_mov_b32 s71, s70\n"
    "v_lshl_add_u32 v234, v21, 4, v22\n"
    "s_add_u32 s63, s66, 16\n"
    "v_xor_b32 v235, 64, v234\n"
    "s_mov_b32 s81, s67\n"
    "v_and_b32 v20, 3, v251\n"
    "s_mov_b64 s[92:93], 0xffff\n"
    "v_lshlrev_b64 v[254:255], v248, s[92:93]\n"
    "s_add_u32 s82, s65, 256\n"
    "v_lshl_or_b32 v21, v20, 3, v24\n"
    "s_mul_i32 s62, s3, 272\n"
    "v_and_b32 v22, 8, v251\n"
    "v_mul_u32_u24 v23, 136, v22\n"
    "v_xor_b32 v21, v21, v23\n"
    "v_and_b32 v23, 4, v251\n"
    "v_lshl_or_b32 v21, v23, 5, v21\n"
    "v_add_u32 v240, s68, v21\n"
    "v_xor_b32 v241, 16, v240\n"
    "v_xor_b32 v242, 32, v240\n"
    "v_xor_b32 v243, 48, v240\n"
    "v_add_u32 v252, s62, v250\n"
    "v_and_b32 v21, 24, v249\n"
    "v_add_u32 v21, s96, v21\n"
    "v_lshl_add_u32 v246, v20, 5, v21\n"
    "v_and_b32 v21, 6, v251\n"
    "v_lshl_or_b32 v21, v21, 2, v24\n"
    "v_and_b32 v23, 2, v249\n"
    "v_lshrrev_b32 v22, 1, v22\n"
    "v_or3_b32 v21, v21, v22, v23\n"
    "v_add_u32 v245, s96, v21\n"
    "s_setprio 2\n"
    "s_waitcnt lgkmcnt(0)\n"
    "s_mul_i32 s36, s61, s16\n"
    "s_add_u32 s94, s36, s16\n"
    "s_cmp_lt_u32 s61, s14\n"
    "s_cselect_b32 s46, s94, 0\n"
    "buffer_load_dwordx2 v[228:229], v105, s[44:47], s36 offen nt\n"
    "s_load_dword s10, s[10:11], 0x0\n"
    "global_load_short_d16_hi v8, v249, s[6:7]\n"
    "global_load_short_d16_hi v9, v249, s[8:9]\n"
    "s_setprio 0\n"
    "s_branch .Ldin_old2\n"
    ".Ldin_young:\n"
    "s_setprio 1\n"
    "v_and_b32 v16, 7, v251\n"
    "s_lshl_b32 s94, s3, 11\n"
    "v_lshlrev_b32 v2, 4, v16\n"
    "v_and_b32 v3, 56, v251\n"
    "v_lshl_or_b32 v3, v3, 5, v2\n"
    "v_add_u32 v6, s94, v3\n"
    "v_add_u32 v3, 0x2000, v6\n"
    "v_add_u32 v4, 0xffffe000, v6\n"
    "v_add_u32 v5, 0x4000, v6\n"
    "v_and_b32 v17, 8, v251\n"
    "s_and_b32 s88, s3, 1\n"
    "v_and_b32 v18, 32, v251\n"
    "s_and_b32 s89, s3, 2\n"
    "v_lshl_or_b32 v18, v16, 2, v18\n"
    "s_lshl_b32 s84, s88, 9\n"
    "v_lshl_add_u32 v18, v18, 3, s84\n"
    "s_lshl_b32 s84, s89, 10\n"
    "v_and_b32 v19, 16, v251\n"
    "s_lshl_b32 s85, s89, 9\n"
    "v_lshl_or_b32 v19, v19, 6, v17\n"
    "s_add_u32 s85, s85, 0x4000\n"
    "v_add3_u32 v12, v18, v19, s84\n"
    "s_lshl2_add_u32 s86, s89, s88\n"
    "v_add_u32 v13, 16, v12\n"
    "s_lshl_b32 s86, s86, 4\n"
    "v_and_b32 v19, 24, v251\n"
    "s_add_u32 s87, s94, 0x4000\n"
    "v_lshrrev_b32 v19, 1, v19\n"
    "v_add3_u32 v14, v18, v19, s85\n"
    "v_add_u32 v15, 16, v14\n"
    "v_bfrev_b32 v7, 1\n"
    "v_lshlrev_b32 v26, 4, v251\n"
    "v_and_b32 v26, 0x1f0, v26\n"
    "v_lshrrev_b32 v27, 5, v251\n"
    "v_lshlrev_b32 v249, 1, v251\n"
    "s_mul_i32 s68, s3, 11392\n"
    "v_lshlrev_b32_e64 v248, 4, v251\n"
    "s_add_u32 s68, s68, 72576\n"
    "v_and_b32 v24, 0x60, v249\n"
    "s_add_u32 s67, s68, 8192\n"
    "v_or_b32 v250, 0x8000, v24\n"
    "s_add_u32 s65, s68, 9728\n"
    "v_lshrrev_b32 v20, 4, v251\n"
    "s_xor_b32 s64, s67, s65\n"
    "v_and_b32 v244, 56, v251\n"
    "s_add_u32 s96, s68, 11264\n"
    "v_bitop3_b32 v21, v20, v251, 7 bitop3:0x78\n"
    "s_lshl_b32 s66, s3, 7\n"
    "v_lshlrev_b32 v21, 4, v21\n"
    "s_add_u32 s66, s66, 68512\n"
    "v_or_b32 v237, 0x400, v21\n"
    "s_lshr_b32 s95, s67, 2\n"
    "v_xor_b32 v239, 64, v21\n"
    "s_mov_b32 s38, 0xffff\n"
    "s_mov_b32 s57, 0x7fffffc0\n"
    "v_add_u32 v253, s67, v244\n"
    "v_lshlrev_b32 v105, 3, v251\n"
    "v_mov_b32 v201, v237\n"
    "v_mov_b32 v203, v239\n"
    "v_mov_b32 v205, v237\n"
    "v_mov_b32 v207, v239\n"
    "v_mov_b32 v209, v237\n"
    "v_mov_b32 v211, v239\n"
    "v_mov_b32 v213, v237\n"
    "v_mov_b32 v215, v239\n"
    "v_mov_b32 v220, s95\n"
    "v_mov_b32 v224, 0\n"
    "v_mov_b32 v225, 0\n"
    "v_mov_b32 v226, 0\n"
    "s_waitcnt lgkmcnt(0)\n"
    "global_load_dwordx4 v[8:11], v2, s[6:7]\n"
    "s_memrealtime s[10:11]\n"
    "s_memtime s[20:21]\n"
    "global_load_dwordx4 v[128:131], v3, s[4:5] offset:0\n"
    "global_load_dwordx4 v[132:135], v3, s[4:5] offset:128\n"
    "v_mov_b32 v227, 0\n"
    "v_bfe_u32 v21, v251, 1, 3\n"
    "v_xor_b32 v21, v20, v21\n"
    "v_and_b32 v22, 0xf0, v248\n"
    "s_mov_b32 s70, 0x47000000\n"
    "v_lshl_add_u32 v22, v22, 3, s68\n"
    "s_mov_b32 s71, s70\n"
    "v_lshl_add_u32 v234, v21, 4, v22\n"
    "s_add_u32 s63, s66, 16\n"
    "v_xor_b32 v235, 64, v234\n"
    "s_mov_b32 s81, s67\n"
    "v_and_b32_e64 v20, 3, v251\n"
    "global_load_dwordx4 v[136:139], v4, s[4:5] offset:0\n"
    "s_mov_b64 s[92:93], 0xffff\n"
    "v_lshlrev_b64 v[254:255], v248, s[92:93]\n"
    "s_add_u32 s82, s65, 256\n"
    "v_lshl_or_b32 v21, v20, 3, v24\n"
    "s_mul_i32 s62, s3, 272\n"
    "v_and_b32 v22, 8, v251\n"
    "v_mul_u32_u24 v23, 136, v22\n"
    "v_xor_b32 v21, v21, v23\n"
    "v_and_b32 v23, 4, v251\n"
    "v_lshl_or_b32 v21, v23, 5, v21\n"
    "v_add_u32 v240, s68, v21\n"
    "v_xor_b32 v241, 16, v240\n"
    "global_load_dwordx4 v[140:143], v4, s[4:5] offset:128\n"
    "v_xor_b32 v242, 32, v240\n"
    "v_xor_b32 v243, 48, v240\n"
    "v_add_u32 v252, s62, v250\n"
    "v_and_b32 v21, 24, v249\n"
    "v_add_u32 v21, s96, v21\n"
    "v_lshl_add_u32 v246, v20, 5, v21\n"
    "v_and_b32 v21, 6, v251\n"
    "v_lshl_or_b32 v21, v21, 2, v24\n"
    "v_and_b32 v23, 2, v249\n"
    "v_lshrrev_b32 v22, 1, v22\n"
    "v_or3_b32 v21, v21, v22, v23\n"
    "v_add_u32 v245, s96, v21\n"
    "global_load_dwordx4 v[144:147], v5, s[4:5] offset:0\n"
    "s_add_u32 s39, s61, s3\n"
    "s_sub_u32 s39, s39, 8\n"
    "v_cmp_gt_u32 vcc, s16, v26\n"
    "v_mad_u32_u24 v27, v27, s16, v26\n"
    "s_mul_i32 s39, s39, s16\n"
    "s_lshl1_add_u32 s39, s17, s39\n"
    "v_cndmask_b32 v27, v7, v27, vcc\n"
    "s_add_u32 s40, s40, 0xfffffc00\n"
    "s_addc_u32 s41, s41, -1\n"
    "s_mul_i32 s94, s61, s12\n"
    "s_bitset1_b32 s41, 23\n"
    "v_lshl_add_u32 v216, v251, 2, s94\n"
    "global_load_dwordx4 v[148:151], v5, s[4:5] offset:128\n"
    "s_mov_b32 s56, s12\n"
    "v_add_u32 v217, 1, v216\n"
    "v_add_u32 v218, 2, v216\n"
    "v_add_u32 v219, 3, v216\n"
    "s_and_b32 s72, s13, 0xffff\n"
    "s_lshr_b32 s73, s13, 16\n"
    "s_cmp_lg_u32 s72, 0\n"
    "s_cselect_b32 s94, 6, 0\n"
    "s_cselect_b32 s95, 8, 0\n"
    "s_add_u32 s74, s72, s94\n"
    "s_sub_u32 s75, s72, s95\n"
    "v_mov_b32 v20, s57\n"
    "s_mov_b64 exec, 0xffffff\n"
    "v_mov_b32 v20, -1\n"
    "s_mov_b64 exec, 0xff\n"
    "v_mov_b32 v20, 0\n"
    "s_mov_b32 exec_lo, -1\n"
    "v_lshl_add_u32 v21, v249, 1, s66\n"
    "v_add_u32 v21, -32, v21\n"
    "ds_write_b32 v21, v20\n"
    "s_mov_b32 exec_hi, -1\n"
    "global_load_dwordx4 v[152:155], v6, s[4:5] offset:0\n"
    "s_mov_b32 s37, 0xffff0000\n"
    "v_and_b32 v19, 15, v251\n"
    "v_mul_u32_u24 v19, 272, v19\n"
    "v_add_u32 v97, v19, v250\n"
    "v_add_u32 v98, s87, v248\n"
    "v_add_u32 v99, s86, v250\n"
    "v_add_u32 v100, s86, v97\n"
    "s_mov_b32 s84, 0xbfb8aa3b\n"
    "global_load_dwordx4 v[156:159], v6, s[4:5] offset:128\n"
    "s_branch .Ldin_y2\n"
    ".Ldin_old2:\n"
    "s_add_u32 s40, s40, 0xfffffc00\n"
    "s_addc_u32 s41, s41, -1\n"
    "s_mul_i32 s94, s61, s12\n"
    "s_bitset1_b32 s41, 23\n"
    "v_lshl_add_u32 v216, v251, 2, s94\n"
    "s_mov_b32 s56, s12\n"
    "v_add_u32 v217, 1, v216\n"
    "v_add_u32 v218, 2, v216\n"
    "v_add_u32 v219, 3, v216\n"
    "s_and_b32 s72, s13, 0xffff\n"
    "s_lshr_b32 s73, s13, 16\n"
    "s_cmp_lg_u32 s72, 0\n"
    "s_cselect_b32 s94, 6, 0\n"
    "s_cselect_b32 s95, 8, 0\n"
    "s_add_u32 s74, s72, s94\n"
    "s_sub_u32 s75, s72, s95\n"
    "v_mov_b32 v20, s57\n"
    "s_mov_b64 exec, 0xffffff\n"
    "v_mov_b32 v20, -1\n"
    "s_mov_b64 exec, 0xff\n"
    "v_mov_b32 v20, 0\n"
    "s_mov_b32 exec_lo, -1\n"
    "v_lshl_add_u32 v21, v249, 1, s66\n"
    "v_add_u32 v21, -32, v21\n"
    "ds_write_b32 v21, v20\n"
    "s_mov_b32 exec_hi, -1\n"
    "v_and_b32 v4, 2, v251\n"
    "v_lshl_add_u32 v4, v4, 6, v250\n"
    "v_and_b32 v5, 16, v248\n"
    "v_and_b32 v6, 12, v251\n"
    "v_or3_b32 v104, v4, v5, v6\n"
    "v_mul_u32_u24 v4, 34, v244\n"
    "v_and_b32 v5, 3, v251\n"
    "v_lshl_add_u32 v4, v5, 5, v4\n"
    "v_and_b32 v5, 4, v251\n"
    "v_lshl_add_u32 v4, v5, 2, v4\n"
    "s_lshl_b32 s86, s62, 3\n"
    "v_add_u32_e64 v18, s86, v4\n"
    "s_mul_i32 s84, s3, s15\n"
    "s_lshl_b32 s39, s61, 7\n"
    "s_sub_u32 s84, s84, s3\n"
    "s_add_u32 s84, s84, s61\n"
    "s_lshl_b32 s84, s84, 7\n"
    "s_lshl_b32 s85, s15, 9\n"
    "s_add_u32 s85, s85, s84\n"
    "s_lshl_b32 s94, s61, 6\n"
    "s_add_u32 s94, s3, s94\n"
    "s_cmp_lt_u32 s61, s14\n"
    "s_cselect_b32 s58, s94, s57\n"
    "s_add_u32 s95, s61, s15\n"
    "s_add_u32 s36, s36, s17\n"
    "s_lshl_b32 s94, s95, 6\n"
    "s_add_u32 s94, s3, s94\n"
    "s_add_u32 s96, s36, s16\n"
    "s_add_u32 s94, s94, 8\n"
    "s_cmp_lt_u32 s95, s14\n"
    "s_cselect_b32 s46, s96, 0\n"
    "s_cselect_b32 s59, s94, s57\n"
    "s_setprio 2\n"
    "buffer_load_dwordx2 v[184:185], v2, s[48:51], s39 offen nt\n"
    "buffer_load_dwordx4 v[128:131], v248, s[48:51], s84 offen nt\n"
    "buffer_load_dwordx4 v[132:135], v248, s[48:51], s85 offen nt\n"
    "buffer_load_short_d16_hi v224, v249, s[44:47], s36 offen offset:0 nt\n"
    "buffer_load_short_d16_hi v225, v249, s[44:47], s36 offen offset:128 nt\n"
    "buffer_load_short_d16_hi v226, v249, s[44:47], s36 offen offset:256 nt\n"
    "buffer_load_short_d16_hi v227, v249, s[44:47], s36 offen offset:384 nt\n"
    "s_mul_i32 s46, s14, s16\n"
    "s_nop 0\n"
    "v_mul_u32_u24 v32, 0x9e3779, v251\n"
    "s_mov_b32 s90, 0x807f807f\n"
    "v_mov_b32 v47, 0x3f003f00\n"
    "v_add_u32 v32, 0x7f4a7c15, v32\n"
    "v_mul_lo_u32 v33, v32, v32\n"
    "v_mul_lo_u32 v34, v33, v32\n"
    "v_mul_lo_u32 v35, v34, v32\n"
    "v_mul_lo_u32 v36, v35, v32\n"
    "v_mul_lo_u32 v37, v36, v32\n"
    "v_mul_lo_u32 v38, v37, v32\n"
    "v_mul_lo_u32 v39, v38, v32\n"
    "v_and_or_b32 v32, v32, s90, v47\n"
    "v_and_or_b32 v33, v33, s90, v47\n"
    "v_and_or_b32 v34, v34, s90, v47\n"
    "v_and_or_b32 v35, v35, s90, v47\n"
    "v_and_or_b32 v36, v36, s90, v47\n"
    "v_and_or_b32 v37, v37, s90, v47\n"
    "v_and_or_b32 v38, v38, s90, v47\n"
    "v_and_or_b32 v39, v39, s90, v47\n"
    "s_nop 1\n"
    "s_nop 0\n"
    "v_mfma_f32_32x32x16_bf16 v[64:79], v[36:39], v[32:35], 0\n"
    "v_mfma_f32_32x32x16_bf16 v[48:63], v[32:35], v[36:39], 0\n"
    "v_mfma_f32_32x32x16_bf16 v[64:79], v[36:39], v[32:35], 0\n"
    "v_mfma_f32_32x32x16_bf16 v[48:63], v[32:35], v[36:39], 0\n"
    "v_mfma_f32_32x32x16_bf16 v[64:79], v[36:39], v[32:35], 0\n"
    "v_mfma_f32_32x32x16_bf16 v[48:63], v[32:35], v[36:39], 0\n"
    "v_mfma_f32_32x32x16_bf16 v[64:79], v[36:39], v[32:35], 0\n"
    "v_mfma_f32_32x32x16_bf16 v[48:63], v[32:35], v[36:39], 0\n"
    "v_mfma_f32_32x32x16_bf16 v[64:79], v[36:39], v[32:35], 0\n"
    "v_mfma_f32_32x32x16_bf16 v[48:63], v[32:35], v[36:39], 0\n"
    "s_setprio 2\n"
    "s_waitcnt vmcnt(9)\n"
    "v_cmp_ne_u16_e64 s[84:85], 0, v228\n"
    "v_cmp_lt_u32_e64 s[86:87], s38, v228\n"
    "v_cmp_ne_u16_e64 s[88:89], 0, v229\n"
    "v_cmp_lt_u32_e64 s[90:91], s38, v229\n"
    "v_mbcnt_lo_u32_b32 v220, s84, v220\n"
    "s_bcnt1_i32_b64 s92, s[84:85]\n"
    "v_mbcnt_hi_u32_b32 v220, s85, v220\n"
    "s_lshl2_add_u32 s92, s92, s67\n"
    "v_mbcnt_lo_u32_b32 v220, s86, v220\n"
    "s_bcnt1_i32_b64 s93, s[86:87]\n"
    "v_mbcnt_hi_u32_b32 v220, s87, v220\n"
    "s_lshl2_add_u32 s93, s93, s92\n"
    "v_mbcnt_lo_u32_b32 v220, s88, v220\n"
    "s_bcnt1_i32_b64 s94, s[88:89]\n"
    "v_mbcnt_hi_u32_b32 v220, s89, v220\n"
    "s_lshl2_add_u32 s94, s94, s93\n"
    "v_mbcnt_lo_u32_b32 v220, s90, v220\n"
    "s_bcnt1_i32_b64 s95, s[90:91]\n"
    "v_mbcnt_hi_u32_b32 v220, s91, v220\n"
    "s_lshl2_add_u32 s95, s95, s94\n"
    "v_mov_b32_e64 v21, -1\n"
    "v_lshl_add_u32 v20, v249, 1, s95\n"
    "v_addc_co_u32_e64 v221, vcc, v220, 0, s[84:85]\n"
    "v_addc_co_u32_e64 v222, vcc, v221, 0, s[86:87]\n"
    "v_addc_co_u32_e64 v223, vcc, v222, 0, s[88:89]\n"
    "ds_write2_b32 v20, v21, v21 offset1:64\n"
    "v_lshlrev_b32 v220, 2, v220\n"
    "v_lshlrev_b32 v221, 2, v221\n"
    "v_lshlrev_b32 v222, 2, v222\n"
    "v_lshlrev_b32_e64 v223, 2, v223\n"
    "s_mov_b64 exec, s[84:85]\n"
    "ds_write_b32 v220, v216\n"
    "s_mov_b64 exec, s[86:87]\n"
    "ds_write_b32 v221, v217\n"
    "s_mov_b64 exec, s[88:89]\n"
    "ds_write_b32 v222, v218\n"
    "s_mov_b64 exec, s[90:91]\n"
    "ds_write_b32 v223, v219\n"
    "s_mov_b64 exec, -1\n"
    "ds_read_b32 v200, v253 offset:0\n"
    "ds_read_b32 v202, v253 offset:4\n"
    "ds_read_b32 v204, v253 offset:64\n"
    "ds_read_b32 v206, v253 offset:68\n"
    "ds_read_b32 v208, v253 offset:128\n"
    "ds_read_b32 v210, v253 offset:132\n"
    "ds_read_b32 v212, v253 offset:192\n"
    "ds_read_b32 v214, v253 offset:196\n"
    "s_add_u32 m0, s68, 0\n"
    "s_waitcnt lgkmcnt(6)\n"
    "buffer_load_dwordx4 v[200:201], s[40:43], 0 idxen offen offset:0 nt lds\n"
    "buffer_load_dwordx4 v[202:203], s[40:43], 0 idxen offen offset:1024 nt lds\n"
    "s_add_u32 s95, s95, 60\n"
    "s_add_u32 s94, s67, 384\n"
    "s_andn2_b32 s95, s95, 63\n"
    "s_max_u32 s80, s95, s94\n"
    "s_add_u32 m0, s68, 2048\n"
    "s_waitcnt lgkmcnt(0)\n"
    "buffer_load_dwordx4 v[204:205], s[40:43], 0 idxen offen offset:0 nt lds\n"
    "s_lshl_b32 s10, s10, 16\n"
    "v_writelane_b32 v10, s10, 0\n"
    "s_waitcnt vmcnt(10)\n"
    "v_and_b32 v11, 0x7fffffff, v8\n"
    "v_mfma_f32_32x32x16_bf16 v[64:79], v[36:39], v[32:35], 0\n"
    "v_and_b32 v12, 0x7fffffff, v10\n"
    "v_min_f32 v14, 0, v8\n"
    "v_add_u32 v12, 0xf6000000, v12\n"
    "v_add_f32 v14, v14, v10\n"
    "v_mfma_f32_32x32x16_bf16 v[48:63], v[32:35], v[36:39], 0\n"
    "v_max_i32 v12, v11, v12\n"
    "v_rcp_f32 v11, v11\n"
    "v_add_f32_dpp v14, v14, v14 row_ror:8 row_mask:0xf bank_mask:0xf\n"
    "v_max_u32_dpp v12, v12, v12 row_ror:8 row_mask:0xf bank_mask:0xf\n"
    "s_mov_b32 s84, 0xbfb8aa3b\n"
    "v_mfma_f32_32x32x16_bf16 v[64:79], v[36:39], v[32:35], 0\n"
    "v_add_f32_dpp v14, v14, v14 row_ror:4 row_mask:0xf bank_mask:0xf\n"
    "v_max_u32_dpp v12, v12, v12 row_ror:4 row_mask:0xf bank_mask:0xf\n"
    "v_bfrev_b32 v13, 1\n"
    "v_add_f32_dpp v14, v14, v14 row_ror:2 row_mask:0xf bank_mask:0xf\n"
    "v_mfma_f32_32x32x16_bf16 v[48:63], v[32:35], v[36:39], 0\n"
    "v_max_u32_dpp v12, v12, v12 row_ror:2 row_mask:0xf bank_mask:0xf\n"
    "v_bitop3_b32 v8, v8, s84, v13 bitop3:0x6c\n"
    "v_add_f32_dpp v14, v14, v14 row_ror:1 row_mask:0xf bank_mask:0xf\n"
    "v_max_u32_dpp v12, v12, v12 row_ror:1 row_mask:0xf bank_mask:0xf\n"
    "s_nop 0\n"
    "v_mfma_f32_32x32x16_bf16 v[64:79], v[36:39], v[32:35], 0\n"
    "v_readlane_b32 s84, v12, 0\n"
    "v_readlane_b32 s85, v12, 16\n"
    "v_readlane_b32 s86, v12, 32\n"
    "buffer_load_dwordx4 v[206:207], s[40:43], 0 idxen offen offset:1024 nt lds\n"
    "v_readlane_b32 s87, v12, 48\n"
    "v_mfma_f32_32x32x16_bf16 v[48:63], v[32:35], v[36:39], 0\n"
    "s_max_u32 s84, s84, s85\n"
    "s_max_u32 s86, s86, s87\n"
    "s_max_u32 s84, s84, s86\n"
    "s_lshr_b32 s84, s84, 23\n"
    "s_max_u32 s84, s84, 15\n"
    "v_mfma_f32_32x32x16_bf16 v[64:79], v[36:39], v[32:35], 0\n"
    "s_min_u32 s84, s84, 253\n"
    "s_lshl_b32 s84, s84, 23\n"
    "s_add_u32 s85, s84, 0xf9000000\n"
    "s_sub_u32 s86, 0x86000000, s84\n"
    "v_mfma_f32_32x32x16_bf16 v[48:63], v[32:35], v[36:39], 0\n"
    "v_mul_f32 v11, s85, v11\n"
    "v_min_f32 v11, 0x46800000, v11\n"
    "v_log_f32 v13, v11\n"
    "v_mul_f32 v14, s86, v14\n"
    "v_mov_b32 v15, s85\n"
    "v_mfma_f32_32x32x16_bf16 v[64:79], v[36:39], v[32:35], 0\n"
    "v_add_f32 v13, 0xc1700000, v13\n"
    "v_fmac_f32 v13, v9, v8\n"
    "ds_write_b32 v104, v13 offset:35200\n"
    "ds_write_b32 v104, v11 offset:34816\n"
    "v_mfma_f32_32x32x16_bf16 v[48:63], v[32:35], v[36:39], 0\n"
    "v_mov_b32_e64 v247, v14\n"
    "s_mov_b32 s69, s85\n"
    "ds_write_b64 v250, v[14:15] offset:35072\n"
    "s_mov_b32 s94, 0x10001\n"
    "v_mfma_f32_32x32x16_bf16 v[64:79], v[36:39], v[32:35], 0\n"
    "s_mov_b32 s95, 0xc080c080\n"
    "v_pk_min_u16 v22, v228, s94\n"
    "v_pk_min_u16 v23, v229, s94\n"
    "v_pk_mad_u16 v228, v22, s95, v228\n"
    "v_pk_mad_u16 v229, v23, s95, v229\n"
    "v_mfma_f32_32x32x16_bf16 v[48:63], v[32:35], v[36:39], 0\n"
    "s_sub_u32 s87, s80, s81\n"
    "v_or_b32 v228, v228, v229\n"
    "s_lshr_b32 s87, s87, 6\n"
    "s_lshl2_add_u32 s86, s3, 68224\n"
    "v_mfma_f32_32x32x16_bf16 v[64:79], v[36:39], v[32:35], 0\n"
    "s_waitcnt vmcnt(8)\n"
    "ds_write_b128 v18, v[128:131] offset:32768\n"
    "ds_write_b128 v18, v[132:135] offset:41472\n"
    "v_and_b32 v184, v254, v184\n"
    "v_and_b32 v185, v255, v185\n"
    "v_mfma_f32_32x32x16_bf16 v[48:63], v[32:35], v[36:39], 0\n"
    "v_mov_b32 v106, s86\n"
    "v_mov_b32 v108, s66\n"
    "v_mov_b32 v109, s58\n"
    "v_mov_b32_e64 v110, s59\n"
    "v_mfma_f32_32x32x16_bf16 v[64:79], v[36:39], v[32:35], 0\n"
    "s_waitcnt vmcnt(4)\n"
    "v_cmp_neq_f32_e64 s[20:21], 0, v224\n"
    "v_cmp_neq_f32_e64 s[22:23], 0, v225\n"
    "v_cmp_neq_f32_e64 s[24:25], 0, v226\n"
    "v_cmp_neq_f32_e64 s[26:27], 0, v227\n"
    "v_mfma_f32_32x32x16_bf16 v[48:63], v[32:35], v[36:39], 0\n"
    "v_fma_f32 v224, v224, v224, -v224\n"
    "v_fma_f32 v225, v225, v225, -v225\n"
    "v_fma_f32 v226, v226, v226, -v226\n"
    "v_fma_f32 v227, v227, v227, -v227\n"
    "v_mfma_f32_32x32x16_bf16 v[64:79], v[36:39], v[32:35], 0\n"
    "s_bcnt1_i32_b64 s88, s[20:21]\n"
    "s_bcnt1_i32_b64 s89, s[22:23]\n"
    "s_bcnt1_i32_b64 s90, s[24:25]\n"
    "s_bcnt1_i32_b64 s91, s[26:27]\n"
    "v_or3_b32 v228, v228, v224, v225\n"
    "v_mfma_f32_32x32x16_bf16 v[48:63], v[32:35], v[36:39], 0\n"
    "s_add_u32 s88, s88, s89\n"
    "v_or3_b32 v228, v228, v226, v227\n"
    "s_add_u32 s90, s90, s91\n"
    "v_cmp_ne_u32_e64 s[84:85], 0, v228\n"
    "v_mfma_f32_32x32x16_bf16 v[64:79], v[36:39], v[32:35], 0\n"
    "s_add_u32 s88, s88, s90\n"
    "s_pack_ll_b32_b16 s88, s87, s88\n"
    "s_mov_b64 exec, 1\n"
    "v_mov_b32 v107, s88\n"
    "ds_write2_b32 v108, v109, v110 offset1:2\n"
    "s_cmp_lg_u64 s[84:85], 0\n"
    "ds_write_b32 v106, v107\n"
    "s_cselect_b32 s91, 2.0, 0\n"
    "s_mov_b64 exec, -1\n"
    "s_or_b32 s58, s58, s91\n"
    "v_mfma_f32_32x32x16_bf16 v[48:63], v[32:35], v[36:39], 0\n"
    "s_or_b32 s59, s59, s91\n"
    "s_nop 11\n"
    "s_mov_b64 vcc, 0\n"
    "s_waitcnt lgkmcnt(0)\n"
    "s_barrier\n"
    "s_setprio 0\n"
    ".Ldin_entry:\n"
    "ds_read_b128 v[32:35], v248 offset:16384\n"
    "ds_read_b128 v[64:67], v248 offset:0\n"
    "ds_read_b128 v[68:71], v248 offset:1024\n"
    "ds_read_b128 v[36:39], v248 offset:17408\n"
    "ds_read_b128 v[72:75], v248 offset:2048\n"
    "ds_read_b128 v[76:79], v248 offset:3072\n"
    "ds_read_b128 v[40:43], v248 offset:18432\n"
    "ds_read_b128 v[80:83], v248 offset:4096\n"
    "ds_read_b128 v[84:87], v248 offset:5120\n"
    "ds_read_b128 v[44:47], v248 offset:19456\n"
    "ds_read_b128 v[88:91], v248 offset:6144\n"
    "ds_read_b128 v[92:95], v248 offset:7168\n"
    "s_add_u32 s67, s67, 320\n"
    "s_waitcnt lgkmcnt(9)\n"
    "ds_read_b128 v[48:51], v248 offset:20480\n"
    "ds_read_b128 v[192:195], v248 offset:8192\n"
    "ds_read_b128 v[196:199], v248 offset:9216\n"
    "v_mfma_f32_4x4x4_16b_bf16 v[144:147], v[184:185], v[32:33], v[64:67] cbsz:2 abid:0\n"
    "s_nop 0\n"
    "v_mfma_f32_4x4x4_16b_bf16 v[148:151], v[184:185], v[34:35], v[68:71] cbsz:2 abid:1\n"
    "s_add_u32 m0, s68, 4096\n"
    "s_cbranch_vccnz .Ldin_e1\n"
    "buffer_load_dwordx4 v[208:209], s[40:43], 0 idxen offen offset:0 nt lds\n"
    "buffer_load_dwordx4 v[210:211], s[40:43], 0 idxen offen offset:1024 nt lds\n"
    ".Ldin_e1:\n"
    "s_waitcnt lgkmcnt(9)\n"
    "ds_read_b128 v[52:55], v248 offset:21504\n"
    "ds_read_b128 v[200:203], v248 offset:10240\n"
    "ds_read_b128 v[204:207], v248 offset:11264\n"
    "v_mfma_f32_4x4x4_16b_bf16 v[152:155], v[184:185], v[36:37], v[72:75] cbsz:2 abid:2\n"
    "s_nop 1\n"
    "v_cvt_pk_bf16_f32 v0, v144, v145\n"
    "v_cvt_pk_bf16_f32 v1, v146, v147\n"
    "v_mfma_f32_4x4x4_16b_bf16 v[156:159], v[184:185], v[38:39], v[76:79] cbsz:2 abid:3\n"
    "s_add_u32 m0, s68, 6144\n"
    "s_cbranch_vccnz .Ldin_e2\n"
    "buffer_load_dwordx4 v[212:213], s[40:43], 0 idxen offen offset:0 nt lds\n"
    "buffer_load_dwordx4 v[214:215], s[40:43], 0 idxen offen offset:1024 nt lds\n"
    ".Ldin_e2:\n"
    "s_waitcnt lgkmcnt(9)\n"
    "s_nop 0\n"
    "ds_read_b128 v[56:59], v248 offset:22528\n"
    "ds_read_b128 v[208:211], v248 offset:12288\n"
    "ds_read_b128 v[212:215], v248 offset:13312\n"
    "v_cvt_pk_bf16_f32 v2, v148, v149\n"
    "v_cvt_pk_bf16_f32 v3, v150, v151\n"
    "v_mfma_f32_4x4x4_16b_bf16 v[144:147], v[184:185], v[40:41], v[80:83] cbsz:2 abid:0\n"
    "v_cvt_pk_bf16_f32 v4, v152, v153\n"
    "v_cvt_pk_bf16_f32 v5, v154, v155\n"
    "v_mfma_f32_4x4x4_16b_bf16 v[148:151], v[184:185], v[42:43], v[84:87] cbsz:2 abid:1\n"
    "s_cbranch_vccnz .Ldin_e3\n"
    "s_barrier\n"
    ".Ldin_e3:\n"
    "s_waitcnt lgkmcnt(9)\n"
    "s_nop 0\n"
    "ds_read_b128 v[60:63], v248 offset:23552\n"
    "ds_read_b128 v[216:219], v248 offset:14336\n"
    "ds_read_b128 v[220:223], v248 offset:15360\n"
    "v_cvt_pk_bf16_f32 v6, v156, v157\n"
    "v_cvt_pk_bf16_f32 v7, v158, v159\n"
    "v_mfma_f32_4x4x4_16b_bf16 v[152:155], v[184:185], v[44:45], v[88:91] cbsz:2 abid:2\n"
    "v_cvt_pk_bf16_f32 v8, v144, v145\n"
    "v_cvt_pk_bf16_f32 v9, v146, v147\n"
    "v_mfma_f32_4x4x4_16b_bf16 v[156:159], v[184:185], v[46:47], v[92:95] cbsz:2 abid:3\n"
    "s_waitcnt lgkmcnt(9)\n"
    "v_cvt_pk_bf16_f32 v10, v148, v149\n"
    "v_cvt_pk_bf16_f32 v11, v150, v151\n"
    "v_mfma_f32_4x4x4_16b_bf16 v[144:147], v[184:185], v[48:49], v[192:195] cbsz:2 abid:0\n"
    "v_cvt_pk_bf16_f32 v12, v152, v153\n"
    "v_cvt_pk_bf16_f32 v13, v154, v155\n"
    "v_mfma_f32_4x4x4_16b_bf16 v[148:151], v[184:185], v[50:51], v[196:199] cbsz:2 abid:1\n"
    "s_waitcnt lgkmcnt(6)\n"
    "v_cvt_pk_bf16_f32 v14, v156, v157\n"
    "v_cvt_pk_bf16_f32 v15, v158, v159\n"
    "v_mfma_f32_4x4x4_16b_bf16 v[152:155], v[184:185], v[52:53], v[200:203] cbsz:2 abid:2\n"
    "v_cvt_pk_bf16_f32 v16, v144, v145\n"
    "v_cvt_pk_bf16_f32 v17, v146, v147\n"
    "v_mfma_f32_4x4x4_16b_bf16 v[156:159], v[184:185], v[54:55], v[204:207] cbsz:2 abid:3\n"
    "s_waitcnt lgkmcnt(3)\n"
    "s_waitcnt vmcnt(6)\n"
    "ds_read_b128 v[160:163], v234 offset:0\n"
    "ds_read_b128 v[164:167], v235 offset:0\n"
    "ds_read_b32 v236, v253 offset:256\n"
    "ds_read_b32 v238, v253 offset:260\n"
    "ds_read_b64_tr_b16 v[168:169], v240 offset:0\n"
    "ds_read_b64_tr_b16 v[170:171], v241 offset:256\n"
    "ds_read_b64_tr_b16 v[172:173], v242 offset:512\n"
    "ds_read_b64_tr_b16 v[174:175], v243 offset:768\n"
    "v_cvt_pk_bf16_f32 v18, v148, v149\n"
    "v_cvt_pk_bf16_f32 v19, v150, v151\n"
    "v_mfma_f32_4x4x4_16b_bf16 v[144:147], v[184:185], v[56:57], v[208:211] cbsz:2 abid:0\n"
    "v_cvt_pk_bf16_f32 v20, v152, v153\n"
    "v_cvt_pk_bf16_f32 v21, v154, v155\n"
    "v_mfma_f32_4x4x4_16b_bf16 v[148:151], v[184:185], v[58:59], v[212:215] cbsz:2 abid:1\n"
    "s_waitcnt lgkmcnt(8)\n"
    "v_cvt_pk_bf16_f32 v22, v156, v157\n"
    "v_cvt_pk_bf16_f32 v23, v158, v159\n"
    "v_mfma_f32_4x4x4_16b_bf16 v[152:155], v[184:185], v[60:61], v[216:219] cbsz:2 abid:2\n"
    "v_cvt_pk_bf16_f32 v24, v144, v145\n"
    "v_cvt_pk_bf16_f32 v25, v146, v147\n"
    "v_mfma_f32_4x4x4_16b_bf16 v[156:159], v[184:185], v[62:63], v[220:223] cbsz:2 abid:3\n"
    "s_cbranch_vccnz .Ldin_e8\n"
    "s_barrier\n"
    ".Ldin_e8:\n"
    "s_nop 0\n"
    "ds_read_b128 v[112:115], v252 offset:17408\n"
    "ds_read_b128 v[116:119], v252 offset:17424\n"
    "ds_read_b128 v[120:123], v252 offset:17536\n"
    "ds_read_b128 v[124:127], v252 offset:17552\n"
    "v_cvt_pk_bf16_f32 v26, v148, v149\n"
    "v_cvt_pk_bf16_f32 v27, v150, v151\n"
    "v_cvt_pk_bf16_f32 v28, v152, v153\n"
    "v_cvt_pk_bf16_f32 v29, v154, v155\n"
    "s_nop 0\n"
    "v_cvt_pk_bf16_f32 v30, v156, v157\n"
    "v_cvt_pk_bf16_f32 v31, v158, v159\n"
    "s_add_u32 m0, s68, 0\n"
    "s_waitcnt lgkmcnt(0)\n"
    "v_mfma_f32_16x16x32_bf16 v[128:131], v[0:3], v[160:163], v[112:115]\n"
    "ds_read_b128 v[96:99], v250 offset:34816\n"
    "v_mfma_f32_16x16x32_bf16 v[128:131], v[4:7], v[164:167], v[128:131]\n"
    "ds_read_b128 v[100:103], v250 offset:34832\n"
    "v_mfma_f32_16x16x32_bf16 v[132:135], v[8:11], v[160:163], v[116:119]\n"
    "ds_read_b128 v[104:107], v250 offset:34944\n"
    "v_mfma_f32_16x16x32_bf16 v[132:135], v[12:15], v[164:167], v[132:135]\n"
    "ds_read_b128 v[108:111], v250 offset:34960\n"
    "v_mfma_f32_16x16x32_bf16 v[136:139], v[16:19], v[160:163], v[120:123]\n"
    "v_mfma_f32_16x16x32_bf16 v[136:139], v[20:23], v[164:167], v[136:139]\n"
    "v_mfma_f32_16x16x32_bf16 v[140:143], v[24:27], v[160:163], v[124:127]\n"
    "v_mfma_f32_16x16x32_bf16 v[140:143], v[28:31], v[164:167], v[140:143]\n"
    "s_waitcnt vmcnt(4)\n"
    "ds_read_b128 v[160:163], v234 offset:2048\n"
    "ds_read_b128 v[164:167], v235 offset:2048\n"
    "buffer_load_dwordx4 v[236:237], s[40:43], 0 idxen offen offset:0 nt lds\n"
    "buffer_load_dwordx4 v[238:239], s[40:43], 0 idxen offen offset:1024 nt lds\n"
    "v_and_b32 v248, 24, v249\n"
    "s_memrealtime s[34:35]\n"
    "s_bitcmp1_b32 s7, 0\n"
    "s_cselect_b32 s94, 52, 58\n"
    "s_cselect_b32 s95, 10, 35\n"
    "s_cmp_lg_u64 vcc, 0\n"
    "s_cselect_b32 s94, s94, 52\n"
    "s_cselect_b32 s95, s95, 20\n"
    "s_cmp_lg_u32 s72, 0\n"
    "s_cselect_b32 s72, s94, 0\n"
    "s_cselect_b32 s95, s95, 0\n"
    "s_waitcnt lgkmcnt(0)\n"
    "s_lshl1_add_u32 s76, s95, s34\n"
    "s_cmp_lt_i32 s59, s57\n"
    "s_cselect_b32 s76, s76, -1\n"
    "s_bitcmp1_b32 s58, 30\n"
    "s_cbranch_scc1 .Ldin_odd\n"
    "s_nop 0\n"
    ".Ldin_A0:\n"
    "v_mfma_f32_16x16x32_bf16 v[144:147], v[0:3], v[160:163], v[112:115]\n"
    "v_exp_f32 v128, v128 clamp\n"
    "v_mfma_f32_16x16x32_bf16 v[144:147], v[4:7], v[164:167], v[144:147]\n"
    "v_exp_f32 v129, v129 clamp\n"
    "v_mfma_f32_16x16x32_bf16 v[148:151], v[8:11], v[160:163], v[116:119]\n"
    "v_exp_f32 v130, v130 clamp\n"
    "v_mfma_f32_16x16x32_bf16 v[148:151], v[12:15], v[164:167], v[148:151]\n"
    "v_exp_f32 v131, v131 clamp\n"
    "v_mfma_f32_16x16x32_bf16 v[152:155], v[16:19], v[160:163], v[120:123]\n"
    "v_exp_f32 v132, v132 clamp\n"
    "v_mfma_f32_16x16x32_bf16 v[152:155], v[20:23], v[164:167], v[152:155]\n"
    "v_exp_f32 v133, v133 clamp\n"
    "v_mfma_f32_16x16x32_bf16 v[156:159], v[24:27], v[160:163], v[124:127]\n"
    "v_exp_f32 v134, v134 clamp\n"
    "v_mfma_f32_16x16x32_bf16 v[156:159], v[28:31], v[164:167], v[156:159]\n"
    "v_exp_f32 v135, v135 clamp\n"
    "v_exp_f32 v136, v136 clamp\n"
    "s_mov_b32 m0, s62\n"
    "v_exp_f32 v137, v137 clamp\n"
    "v_add_u32_e64 v253, s67, v244\n"
    "s_waitcnt lgkmcnt(0)\n"
    "v_mfma_f32_4x4x4_16b_bf16 v[224:227], v[228:229], v[176:177], v[224:227] cbsz:4 abid:0\n"
    "v_exp_f32 v138, v138 clamp\n"
    "v_exp_f32 v139, v139 clamp\n"
    "v_mfma_f32_4x4x4_16b_bf16 v[224:227], v[228:229], v[178:179], v[224:227] cbsz:4 abid:1\n"
    "v_exp_f32 v140, v140 clamp\n"
    "v_exp_f32 v141, v141 clamp\n"
    "v_mfma_f32_4x4x4_16b_bf16 v[224:227], v[228:229], v[180:181], v[224:227] cbsz:4 abid:2\n"
    "v_exp_f32 v142, v142 clamp\n"
    "v_exp_f32 v143, v143 clamp\n"
    "v_mfma_f32_4x4x4_16b_bf16 v[224:227], v[228:229], v[182:183], v[224:227] cbsz:4 abid:3\n"
    "s_waitcnt vmcnt(4)\n"
    "ds_read_b64_tr_b16 v[176:177], v240 offset:2048\n"
    "ds_read_b64_tr_b16 v[178:179], v241 offset:2304\n"
    "ds_read_b64_tr_b16 v[180:181], v242 offset:2560\n"
    "ds_read_b64_tr_b16 v[182:183], v243 offset:2816\n"
    "ds_read_b32 v236, v253 offset:0\n"
    "ds_read_b32 v238, v253 offset:4\n"
    "ds_read_b128 v[160:163], v234 offset:4096\n"
    "ds_read_b128 v[164:167], v235 offset:4096\n"
    "s_cbranch_vccz .Ldin_A0_np\n"
    "s_setprio 1\n"
    ".Ldin_A0_np:\n"
    "v_pk_fma_f32 v[128:129], v[128:129], s[70:71], v[96:97]\n"
    "v_pk_fma_f32 v[130:131], v[130:131], s[70:71], v[98:99]\n"
    "v_pk_add_f32 v[190:191], v[224:225], v[226:227]\n"
    "v_add_f32 v190, v190, v191\n"
    "ds_write_addtid_b32 v190 offset:32768\n"
    "s_add_u32 m0, s68, 2048\n"
    "s_cmp_ge_i32 s59, s57\n"
    "s_cbranch_scc1 .Ldin_A0_nl\n"
    "s_bfe_u32 s94, s59, 0x180006\n"
    "s_mul_i32 s86, s94, s56\n"
    "v_mbcnt_lo_u32_b32 v184, s20, 0\n"
    "v_mbcnt_lo_u32_b32 v185, s22, 0\n"
    "v_mbcnt_lo_u32_b32 v186, s24, 0\n"
    "v_mbcnt_lo_u32_b32 v187, s26, 0\n"
    "s_bcnt1_i32_b64 s90, s[20:21]\n"
    "s_bcnt1_i32_b64 s91, s[22:23]\n"
    "s_bcnt1_i32_b64 s92, s[24:25]\n"
    "s_bcnt1_i32_b64 s93, s[26:27]\n"
    "v_mbcnt_hi_u32_b32 v184, s21, v184\n"
    "v_mbcnt_hi_u32_b32 v185, s23, v185\n"
    "v_mbcnt_hi_u32_b32 v186, s25, v186\n"
    "v_mbcnt_hi_u32_b32 v187, s27, v187\n"
    "s_lshl2_add_u32 s90, s90, s65\n"
    "s_lshl2_add_u32 s91, s91, s90\n"
    "s_lshl2_add_u32 s92, s92, s91\n"
    "s_lshl2_add_u32 s93, s93, s92\n"
    "v_add_u32 v188, s86, v251\n"
    "v_lshl_add_u32 v184, v184, 2, s65\n"
    "v_lshl_add_u32 v185, v185, 2, s90\n"
    "v_lshl_add_u32 v186, v186, 2, s91\n"
    "v_lshl_add_u32 v187, v187, 2, s92\n"
    "v_mov_b32 v191, -1\n"
    "v_lshl_add_u32 v190, v249, 1, s93\n"
    "ds_write2_b32 v190, v191, v191 offset1:64\n"
    "s_mov_b64 exec, s[20:21]\n"
    "ds_write_b32 v184, v188\n"
    "s_mov_b64 exec, s[22:23]\n"
    "v_add_u32 v189, 64, v188\n"
    "ds_write_b32 v185, v189\n"
    "s_mov_b64 exec, s[24:25]\n"
    "v_add_u32 v190, 128, v188\n"
    "ds_write_b32 v186, v190\n"
    "s_mov_b64 exec, s[26:27]\n"
    "v_add_u32 v189, 192, v188\n"
    "ds_write_b32 v187, v189\n"
    "s_mov_b64 exec, -1\n"
    "s_sub_u32 s95, s93, -60\n"
    "s_add_u32 s94, s82, 128\n"
    "s_andn2_b32 s95, s95, 63\n"
    "s_max_u32 s83, s95, s94\n"
    ".Ldin_A0_nl:\n"
    "v_pk_add_f32 v[230:231], v[128:129], v[130:131]\n"
    "v_pk_mul_f32 v[232:233], v[128:129], v[130:131]\n"
    "v_pk_fma_f32 v[132:133], v[132:133], s[70:71], v[100:101]\n"
    "v_pk_fma_f32 v[230:231], v[230:231], v[132:133], v[232:233]\n"
    "v_pk_mul_f32 v[232:233], v[232:233], v[132:133]\n"
    "v_pk_fma_f32 v[134:135], v[134:135], s[70:71], v[102:103]\n"
    "v_pk_fma_f32 v[230:231], v[230:231], v[134:135], v[232:233]\n"
    "v_pk_mul_f32 v[232:233], v[232:233], v[134:135]\n"
    "v_pk_fma_f32 v[136:137], v[136:137], s[70:71], v[104:105]\n"
    "v_pk_fma_f32 v[230:231], v[230:231], v[136:137], v[232:233]\n"
    "v_pk_mul_f32 v[232:233], v[232:233], v[136:137]\n"
    "v_pk_fma_f32 v[138:139], v[138:139], s[70:71], v[106:107]\n"
    "v_pk_fma_f32 v[230:231], v[230:231], v[138:139], v[232:233]\n"
    "v_pk_mul_f32 v[232:233], v[232:233], v[138:139]\n"
    "v_pk_fma_f32 v[140:141], v[140:141], s[70:71], v[108:109]\n"
    "v_pk_fma_f32 v[230:231], v[230:231], v[140:141], v[232:233]\n"
    "v_pk_mul_f32 v[232:233], v[232:233], v[140:141]\n"
    "v_pk_fma_f32 v[142:143], v[142:143], s[70:71], v[110:111]\n"
    "v_pk_fma_f32 v[230:231], v[230:231], v[142:143], v[232:233]\n"
    "v_pk_mul_f32 v[232:233], v[232:233], v[142:143]\n"
    "v_rcp_f32 v232, v232\n"
    "v_rcp_f32 v233, v233\n"
    "v_fma_f32 v230, v230, v232, v247\n"
    "v_fmac_f32 v230, v231, v233\n"
    "v_cvt_pk_bf16_f32 v230, v230, v230\n"
    "s_add_u32 s67, s67, 64\n"
    "s_waitcnt lgkmcnt(0)\n"
    "ds_write_b16 v245, v230\n"
    "ds_read_b64 v[228:229], v246\n"
    "buffer_load_dwordx4 v[236:237], s[40:43], 0 idxen offen offset:0 nt lds\n"
    "buffer_load_dwordx4 v[238:239], s[40:43], 0 idxen offen offset:1024 nt lds\n"
    ".Ldin_B1:\n"
    "v_mfma_f32_16x16x32_bf16 v[128:131], v[0:3], v[160:163], v[112:115]\n"
    "v_exp_f32 v144, v144 clamp\n"
    "v_mfma_f32_16x16x32_bf16 v[128:131], v[4:7], v[164:167], v[128:131]\n"
    "v_exp_f32 v145, v145 clamp\n"
    "v_mfma_f32_16x16x32_bf16 v[132:135], v[8:11], v[160:163], v[116:119]\n"
    "v_exp_f32 v146, v146 clamp\n"
    "v_mfma_f32_16x16x32_bf16 v[132:135], v[12:15], v[164:167], v[132:135]\n"
    "v_exp_f32 v147, v147 clamp\n"
    "v_mfma_f32_16x16x32_bf16 v[136:139], v[16:19], v[160:163], v[120:123]\n"
    "v_exp_f32 v148, v148 clamp\n"
    "v_mfma_f32_16x16x32_bf16 v[136:139], v[20:23], v[164:167], v[136:139]\n"
    "v_exp_f32 v149, v149 clamp\n"
    "v_mfma_f32_16x16x32_bf16 v[140:143], v[24:27], v[160:163], v[124:127]\n"
    "v_exp_f32 v150, v150 clamp\n"
    "v_mfma_f32_16x16x32_bf16 v[140:143], v[28:31], v[164:167], v[140:143]\n"
    "v_exp_f32 v151, v151 clamp\n"
    "v_exp_f32 v152, v152 clamp\n"
    "s_cmp_eq_u32 s67, s80\n"
    "s_cselect_b32 s67, s65, s67\n"
    "s_and_b32 s94, s59, 63\n"
    "s_mul_i32 s94, s94, 272\n"
    "v_add_u32 v252, s94, v250\n"
    "s_add_u32 m0, s68, 4096\n"
    "v_exp_f32 v153, v153 clamp\n"
    "v_add_u32 v253, s67, v244\n"
    "s_waitcnt lgkmcnt(0)\n"
    "v_mfma_f32_4x4x4_16b_bf16 v[224:227], v[228:229], v[168:169], 0 cbsz:4 abid:0\n"
    "v_exp_f32 v154, v154 clamp\n"
    "v_exp_f32 v155, v155 clamp\n"
    "v_mfma_f32_4x4x4_16b_bf16 v[224:227], v[228:229], v[170:171], v[224:227] cbsz:4 abid:1\n"
    "v_exp_f32 v156, v156 clamp\n"
    "v_exp_f32 v157, v157 clamp\n"
    "v_mfma_f32_4x4x4_16b_bf16 v[224:227], v[228:229], v[172:173], v[224:227] cbsz:4 abid:2\n"
    "v_exp_f32 v158, v158 clamp\n"
    "v_exp_f32 v159, v159 clamp\n"
    "v_mfma_f32_4x4x4_16b_bf16 v[224:227], v[228:229], v[174:175], v[224:227] cbsz:4 abid:3\n"
    "s_waitcnt vmcnt(4)\n"
    "s_memrealtime s[34:35]\n"
    "ds_read_b64_tr_b16 v[168:169], v240 offset:4096\n"
    "ds_read_b64_tr_b16 v[170:171], v241 offset:4352\n"
    "ds_read_b64_tr_b16 v[172:173], v242 offset:4608\n"
    "ds_read_b64_tr_b16 v[174:175], v243 offset:4864\n"
    "ds_read_b32 v236, v253 offset:0\n"
    "ds_read_b32 v238, v253 offset:4\n"
    "ds_read_b128 v[160:163], v234 offset:6144\n"
    "ds_read_b128 v[164:167], v235 offset:6144\n"
    "v_add_u32_e64 v184, v248, v252\n"
    "ds_read_b64 v[184:185], v184\n"
    "v_pk_fma_f32 v[144:145], v[144:145], s[70:71], v[96:97]\n"
    "v_pk_fma_f32 v[146:147], v[146:147], s[70:71], v[98:99]\n"
    "v_pk_add_f32 v[230:231], v[144:145], v[146:147]\n"
    "v_pk_mul_f32 v[232:233], v[144:145], v[146:147]\n"
    "v_pk_fma_f32 v[148:149], v[148:149], s[70:71], v[100:101]\n"
    "v_pk_fma_f32 v[230:231], v[230:231], v[148:149], v[232:233]\n"
    "v_pk_mul_f32 v[232:233], v[232:233], v[148:149]\n"
    "v_pk_fma_f32 v[150:151], v[150:151], s[70:71], v[102:103]\n"
    "v_pk_fma_f32 v[230:231], v[230:231], v[150:151], v[232:233]\n"
    "v_pk_mul_f32 v[232:233], v[232:233], v[150:151]\n"
    "v_pk_fma_f32 v[152:153], v[152:153], s[70:71], v[104:105]\n"
    "v_pk_fma_f32 v[230:231], v[230:231], v[152:153], v[232:233]\n"
    "v_pk_mul_f32 v[232:233], v[232:233], v[152:153]\n"
    "v_pk_fma_f32 v[154:155], v[154:155], s[70:71], v[106:107]\n"
    "v_pk_fma_f32 v[230:231], v[230:231], v[154:155], v[232:233]\n"
    "v_pk_mul_f32 v[232:233], v[232:233], v[154:155]\n"
    "v_pk_fma_f32 v[156:157], v[156:157], s[70:71], v[108:109]\n"
    "v_pk_fma_f32 v[230:231], v[230:231], v[156:157], v[232:233]\n"
    "v_pk_mul_f32 v[232:233], v[232:233], v[156:157]\n"
    "v_pk_fma_f32 v[158:159], v[158:159], s[70:71], v[110:111]\n"
    "v_pk_fma_f32 v[230:231], v[230:231], v[158:159], v[232:233]\n"
    "v_pk_mul_f32 v[232:233], v[232:233], v[158:159]\n"
    "v_rcp_f32 v232, v232\n"
    "v_rcp_f32 v233, v233\n"
    "v_fma_f32 v230, v230, v232, v247\n"
    "v_fmac_f32 v230, v231, v233\n"
    "v_cvt_pk_bf16_f32 v230, v230, v230\n"
    "s_add_u32 s67, s67, 64\n"
    "s_waitcnt lgkmcnt(0)\n"
    "ds_write_b16 v245, v230\n"
    "ds_read_b64 v[228:229], v246\n"
    "v_and_b32 v184, v254, v184\n"
    "v_and_b32 v185, v255, v185\n"
    "s_cmp_le_u32 s34, s76\n"
    "s_cbranch_scc1 .Ldin_B1_wait\n"
    ".Ldin_B1_go:\n"
    "s_sub_u32 s94, s34, 60\n"
    "s_max_u32 s76, s76, s94\n"
    "s_add_u32 s76, s76, s72\n"
    ".Ldin_B1_dma:\n"
    "buffer_load_dwordx4 v[236:237], s[40:43], 0 idxen offen offset:0 nt lds\n"
    "buffer_load_dwordx4 v[238:239], s[40:43], 0 idxen offen offset:1024 nt lds\n"
    ".Ldin_B1_nd:\n"
    ".Ldin_G2:\n"
    "v_mfma_f32_16x16x32_bf16 v[144:147], v[0:3], v[160:163], v[112:115]\n"
    "v_exp_f32 v128, v128 clamp\n"
    "v_mfma_f32_16x16x32_bf16 v[144:147], v[4:7], v[164:167], v[144:147]\n"
    "v_exp_f32 v129, v129 clamp\n"
    "v_mfma_f32_16x16x32_bf16 v[148:151], v[8:11], v[160:163], v[116:119]\n"
    "v_exp_f32 v130, v130 clamp\n"
    "v_mfma_f32_16x16x32_bf16 v[148:151], v[12:15], v[164:167], v[148:151]\n"
    "v_exp_f32 v131, v131 clamp\n"
    "v_mfma_f32_16x16x32_bf16 v[152:155], v[16:19], v[160:163], v[120:123]\n"
    "v_exp_f32 v132, v132 clamp\n"
    "v_mfma_f32_16x16x32_bf16 v[152:155], v[20:23], v[164:167], v[152:155]\n"
    "v_exp_f32 v133, v133 clamp\n"
    "v_mfma_f32_16x16x32_bf16 v[156:159], v[24:27], v[160:163], v[124:127]\n"
    "v_exp_f32 v134, v134 clamp\n"
    "v_mfma_f32_16x16x32_bf16 v[156:159], v[28:31], v[164:167], v[156:159]\n"
    "v_exp_f32 v135, v135 clamp\n"
    "v_exp_f32 v136, v136 clamp\n"
    "s_cmp_eq_u32 s67, s80\n"
    "s_cselect_b32 s67, s65, s67\n"
    "s_add_u32 m0, s68, 6144\n"
    "v_exp_f32 v137, v137 clamp\n"
    "v_add_u32_e64 v253, s67, v244\n"
    "s_waitcnt lgkmcnt(0)\n"
    "v_mfma_f32_4x4x4_16b_bf16 v[224:227], v[228:229], v[176:177], v[224:227] cbsz:4 abid:0\n"
    "v_exp_f32 v138, v138 clamp\n"
    "v_exp_f32 v139, v139 clamp\n"
    "v_mfma_f32_4x4x4_16b_bf16 v[224:227], v[228:229], v[178:179], v[224:227] cbsz:4 abid:1\n"
    "v_exp_f32 v140, v140 clamp\n"
    "v_exp_f32 v141, v141 clamp\n"
    "v_mfma_f32_4x4x4_16b_bf16 v[224:227], v[228:229], v[180:181], v[224:227] cbsz:4 abid:2\n"
    "v_exp_f32 v142, v142 clamp\n"
    "v_exp_f32 v143, v143 clamp\n"
    "v_mfma_f32_4x4x4_16b_bf16 v[224:227], v[228:229], v[182:183], v[224:227] cbsz:4 abid:3\n"
    "s_waitcnt vmcnt(4)\n"
    "s_memrealtime s[34:35]\n"
    "ds_read_b64_tr_b16 v[176:177], v240 offset:6144\n"
    "ds_read_b64_tr_b16 v[178:179], v241 offset:6400\n"
    "ds_read_b64_tr_b16 v[180:181], v242 offset:6656\n"
    "ds_read_b64_tr_b16 v[182:183], v243 offset:6912\n"
    "ds_read_b32 v236, v253 offset:0\n"
    "ds_read_b32 v238, v253 offset:4\n"
    "ds_read_b128 v[160:163], v234 offset:0\n"
    "ds_read_b128 v[164:167], v235 offset:0\n"
    "v_pk_fma_f32 v[128:129], v[128:129], s[70:71], v[96:97]\n"
    "v_pk_fma_f32 v[130:131], v[130:131], s[70:71], v[98:99]\n"
    "v_pk_add_f32 v[230:231], v[128:129], v[130:131]\n"
    "v_pk_mul_f32 v[232:233], v[128:129], v[130:131]\n"
    "v_pk_fma_f32 v[132:133], v[132:133], s[70:71], v[100:101]\n"
    "v_pk_fma_f32 v[230:231], v[230:231], v[132:133], v[232:233]\n"
    "v_pk_mul_f32 v[232:233], v[232:233], v[132:133]\n"
    "v_pk_fma_f32 v[134:135], v[134:135], s[70:71], v[102:103]\n"
    "v_pk_fma_f32 v[230:231], v[230:231], v[134:135], v[232:233]\n"
    "v_pk_mul_f32 v[232:233], v[232:233], v[134:135]\n"
    "v_pk_fma_f32 v[136:137], v[136:137], s[70:71], v[104:105]\n"
    "v_pk_fma_f32 v[230:231], v[230:231], v[136:137], v[232:233]\n"
    "v_pk_mul_f32 v[232:233], v[232:233], v[136:137]\n"
    "v_pk_fma_f32 v[138:139], v[138:139], s[70:71], v[106:107]\n"
    "v_pk_fma_f32 v[230:231], v[230:231], v[138:139], v[232:233]\n"
    "v_pk_mul_f32 v[232:233], v[232:233], v[138:139]\n"
    "v_pk_fma_f32 v[140:141], v[140:141], s[70:71], v[108:109]\n"
    "v_pk_fma_f32 v[230:231], v[230:231], v[140:141], v[232:233]\n"
    "v_pk_mul_f32 v[232:233], v[232:233], v[140:141]\n"
    "v_pk_fma_f32 v[142:143], v[142:143], s[70:71], v[110:111]\n"
    "v_pk_fma_f32 v[230:231], v[230:231], v[142:143], v[232:233]\n"
    "v_pk_mul_f32 v[232:233], v[232:233], v[142:143]\n"
    "v_rcp_f32 v232, v232\n"
    "v_rcp_f32 v233, v233\n"
    "v_fma_f32 v230, v230, v232, v247\n"
    "v_fmac_f32 v230, v231, v233\n"
    "v_cvt_pk_bf16_f32 v230, v230, v230\n"
    "s_add_u32 s67, s67, 64\n"
    "s_waitcnt lgkmcnt(0)\n"
    "ds_write_b16 v245, v230\n"
    "ds_read_b64 v[228:229], v246\n"
    "s_cmp_le_u32 s34, s76\n"
    "s_cbranch_scc1 .Ldin_G2_wait\n"
    ".Ldin_G2_go:\n"
    "s_sub_u32 s94, s34, 60\n"
    "s_max_u32 s76, s76, s94\n"
    "s_add_u32 s76, s76, s72\n"
    ".Ldin_G2_dma:\n"
    "buffer_load_dwordx4 v[236:237], s[40:43], 0 idxen offen offset:0 nt lds\n"
    "buffer_load_dwordx4 v[238:239], s[40:43], 0 idxen offen offset:1024 nt lds\n"
    ".Ldin_G2_nd:\n"
    "s_cmp_eq_u32 s67, s82\n"
    "s_cbranch_scc1 .Ldin_L3\n"
    ".Ldin_G3:\n"
    "v_mfma_f32_16x16x32_bf16 v[128:131], v[0:3], v[160:163], v[112:115]\n"
    "v_exp_f32 v144, v144 clamp\n"
    "v_mfma_f32_16x16x32_bf16 v[128:131], v[4:7], v[164:167], v[128:131]\n"
    "v_exp_f32 v145, v145 clamp\n"
    "v_mfma_f32_16x16x32_bf16 v[132:135], v[8:11], v[160:163], v[116:119]\n"
    "v_exp_f32 v146, v146 clamp\n"
    "v_mfma_f32_16x16x32_bf16 v[132:135], v[12:15], v[164:167], v[132:135]\n"
    "v_exp_f32 v147, v147 clamp\n"
    "v_mfma_f32_16x16x32_bf16 v[136:139], v[16:19], v[160:163], v[120:123]\n"
    "v_exp_f32 v148, v148 clamp\n"
    "v_mfma_f32_16x16x32_bf16 v[136:139], v[20:23], v[164:167], v[136:139]\n"
    "v_exp_f32 v149, v149 clamp\n"
    "v_mfma_f32_16x16x32_bf16 v[140:143], v[24:27], v[160:163], v[124:127]\n"
    "v_exp_f32 v150, v150 clamp\n"
    "v_mfma_f32_16x16x32_bf16 v[140:143], v[28:31], v[164:167], v[140:143]\n"
    "v_exp_f32 v151, v151 clamp\n"
    "v_exp_f32 v152, v152 clamp\n"
    "s_cmp_eq_u32 s67, s80\n"
    "s_cselect_b32 s67, s65, s67\n"
    "s_add_u32 m0, s68, 0\n"
    "v_exp_f32 v153, v153 clamp\n"
    "v_add_u32 v253, s67, v244\n"
    "s_waitcnt lgkmcnt(0)\n"
    "v_mfma_f32_4x4x4_16b_bf16 v[224:227], v[228:229], v[168:169], v[224:227] cbsz:4 abid:0\n"
    "v_exp_f32 v154, v154 clamp\n"
    "v_exp_f32 v155, v155 clamp\n"
    "v_mfma_f32_4x4x4_16b_bf16 v[224:227], v[228:229], v[170:171], v[224:227] cbsz:4 abid:1\n"
    "v_exp_f32 v156, v156 clamp\n"
    "v_exp_f32 v157, v157 clamp\n"
    "v_mfma_f32_4x4x4_16b_bf16 v[224:227], v[228:229], v[172:173], v[224:227] cbsz:4 abid:2\n"
    "v_exp_f32 v158, v158 clamp\n"
    "v_exp_f32 v159, v159 clamp\n"
    "v_mfma_f32_4x4x4_16b_bf16 v[224:227], v[228:229], v[174:175], v[224:227] cbsz:4 abid:3\n"
    "s_waitcnt vmcnt(4)\n"
    "s_memrealtime s[34:35]\n"
    "ds_read_b64_tr_b16 v[168:169], v240 offset:0\n"
    "ds_read_b64_tr_b16 v[170:171], v241 offset:256\n"
    "ds_read_b64_tr_b16 v[172:173], v242 offset:512\n"
    "ds_read_b64_tr_b16 v[174:175], v243 offset:768\n"
    "ds_read_b32 v236, v253 offset:0\n"
    "ds_read_b32 v238, v253 offset:4\n"
    "ds_read_b128 v[160:163], v234 offset:2048\n"
    "ds_read_b128 v[164:167], v235 offset:2048\n"
    "v_pk_fma_f32 v[144:145], v[144:145], s[70:71], v[96:97]\n"
    "v_pk_fma_f32 v[146:147], v[146:147], s[70:71], v[98:99]\n"
    "v_pk_add_f32 v[230:231], v[144:145], v[146:147]\n"
    "v_pk_mul_f32 v[232:233], v[144:145], v[146:147]\n"
    "v_pk_fma_f32 v[148:149], v[148:149], s[70:71], v[100:101]\n"
    "v_pk_fma_f32 v[230:231], v[230:231], v[148:149], v[232:233]\n"
    "v_pk_mul_f32 v[232:233], v[232:233], v[148:149]\n"
    "v_pk_fma_f32 v[150:151], v[150:151], s[70:71], v[102:103]\n"
    "v_pk_fma_f32 v[230:231], v[230:231], v[150:151], v[232:233]\n"
    "v_pk_mul_f32 v[232:233], v[232:233], v[150:151]\n"
    "v_pk_fma_f32 v[152:153], v[152:153], s[70:71], v[104:105]\n"
    "v_pk_fma_f32 v[230:231], v[230:231], v[152:153], v[232:233]\n"
    "v_pk_mul_f32 v[232:233], v[232:233], v[152:153]\n"
    "v_pk_fma_f32 v[154:155], v[154:155], s[70:71], v[106:107]\n"
    "v_pk_fma_f32 v[230:231], v[230:231], v[154:155], v[232:233]\n"
    "v_pk_mul_f32 v[232:233], v[232:233], v[154:155]\n"
    "v_pk_fma_f32 v[156:157], v[156:157], s[70:71], v[108:109]\n"
    "v_pk_fma_f32 v[230:231], v[230:231], v[156:157], v[232:233]\n"
    "v_pk_mul_f32 v[232:233], v[232:233], v[156:157]\n"
    "v_pk_fma_f32 v[158:159], v[158:159], s[70:71], v[110:111]\n"
    "v_pk_fma_f32 v[230:231], v[230:231], v[158:159], v[232:233]\n"
    "v_pk_mul_f32 v[232:233], v[232:233], v[158:159]\n"
    "v_rcp_f32 v232, v232\n"
    "v_rcp_f32 v233, v233\n"
    "v_fma_f32 v230, v230, v232, v247\n"
    "v_fmac_f32 v230, v231, v233\n"
    "v_cvt_pk_bf16_f32 v230, v230, v230\n"
    "s_add_u32 s67, s67, 64\n"
    "s_waitcnt lgkmcnt(0)\n"
    "ds_write_b16 v245, v230\n"
    "ds_read_b64 v[228:229], v246\n"
    "s_cmp_le_u32 s34, s76\n"
    "s_cbranch_scc1 .Ldin_G3_wait\n"
    ".Ldin_G3_go:\n"
    "s_add_u32 s76, s76, s72\n"
    ".Ldin_G3_dma:\n"
    "buffer_load_dwordx4 v[236:237], s[40:43], 0 idxen offen offset:0 nt lds\n"
    "buffer_load_dwordx4 v[238:239], s[40:43], 0 idxen offen offset:1024 nt lds\n"
    ".Ldin_G3_nd:\n"
    "s_cmp_eq_u32 s67, s82\n"
    "s_cbranch_scc1 .Ldin_L0\n"
    ".Ldin_G0:\n"
    "v_mfma_f32_16x16x32_bf16 v[144:147], v[0:3], v[160:163], v[112:115]\n"
    "v_exp_f32 v128, v128 clamp\n"
    "v_mfma_f32_16x16x32_bf16 v[144:147], v[4:7], v[164:167], v[144:147]\n"
    "v_exp_f32 v129, v129 clamp\n"
    "v_mfma_f32_16x16x32_bf16 v[148:151], v[8:11], v[160:163], v[116:119]\n"
    "v_exp_f32 v130, v130 clamp\n"
    "v_mfma_f32_16x16x32_bf16 v[148:151], v[12:15], v[164:167], v[148:151]\n"
    "v_exp_f32 v131, v131 clamp\n"
    "v_mfma_f32_16x16x32_bf16 v[152:155], v[16:19], v[160:163], v[120:123]\n"
    "v_exp_f32 v132, v132 clamp\n"
    "v_mfma_f32_16x16x32_bf16 v[152:155], v[20:23], v[164:167], v[152:155]\n"
    "v_exp_f32 v133, v133 clamp\n"
    "v_mfma_f32_16x16x32_bf16 v[156:159], v[24:27], v[160:163], v[124:127]\n"
    "v_exp_f32 v134, v134 clamp\n"
    "v_mfma_f32_16x16x32_bf16 v[156:159], v[28:31], v[164:167], v[156:159]\n"
    "v_exp_f32 v135, v135 clamp\n"
    "v_exp_f32 v136, v136 clamp\n"
    "s_cmp_eq_u32 s67, s80\n"
    "s_cselect_b32 s67, s65, s67\n"
    "s_add_u32 m0, s68, 2048\n"
    "v_exp_f32 v137, v137 clamp\n"
    "v_add_u32_e64 v253, s67, v244\n"
    "s_waitcnt lgkmcnt(0)\n"
    "v_mfma_f32_4x4x4_16b_bf16 v[224:227], v[228:229], v[176:177], v[224:227] cbsz:4 abid:0\n"
    "v_exp_f32 v138, v138 clamp\n"
    "v_exp_f32 v139, v139 clamp\n"
    "v_mfma_f32_4x4x4_16b_bf16 v[224:227], v[228:229], v[178:179], v[224:227] cbsz:4 abid:1\n"
    "v_exp_f32 v140, v140 clamp\n"
    "v_exp_f32 v141, v141 clamp\n"
    "v_mfma_f32_4x4x4_16b_bf16 v[224:227], v[228:229], v[180:181], v[224:227] cbsz:4 abid:2\n"
    "v_exp_f32 v142, v142 clamp\n"
    "v_exp_f32 v143, v143 clamp\n"
    "v_mfma_f32_4x4x4_16b_bf16 v[224:227], v[228:229], v[182:183], v[224:227] cbsz:4 abid:3\n"
    "s_waitcnt vmcnt(4)\n"
    "s_memrealtime s[34:35]\n"
    "ds_read_b64_tr_b16 v[176:177], v240 offset:2048\n"
    "ds_read_b64_tr_b16 v[178:179], v241 offset:2304\n"
    "ds_read_b64_tr_b16 v[180:181], v242 offset:2560\n"
    "ds_read_b64_tr_b16 v[182:183], v243 offset:2816\n"
    "ds_read_b32 v236, v253 offset:0\n"
    "ds_read_b32 v238, v253 offset:4\n"
    "ds_read_b128 v[160:163], v234 offset:4096\n"
    "ds_read_b128 v[164:167], v235 offset:4096\n"
    "v_pk_fma_f32 v[128:129], v[128:129], s[70:71], v[96:97]\n"
    "v_pk_fma_f32 v[130:131], v[130:131], s[70:71], v[98:99]\n"
    "v_pk_add_f32 v[230:231], v[128:129], v[130:131]\n"
    "v_pk_mul_f32 v[232:233], v[128:129], v[130:131]\n"
    "v_pk_fma_f32 v[132:133], v[132:133], s[70:71], v[100:101]\n"
    "v_pk_fma_f32 v[230:231], v[230:231], v[132:133], v[232:233]\n"
    "v_pk_mul_f32 v[232:233], v[232:233], v[132:133]\n"
    "v_pk_fma_f32 v[134:135], v[134:135], s[70:71], v[102:103]\n"
    "v_pk_fma_f32 v[230:231], v[230:231], v[134:135], v[232:233]\n"
    "v_pk_mul_f32 v[232:233], v[232:233], v[134:135]\n"
    "v_pk_fma_f32 v[136:137], v[136:137], s[70:71], v[104:105]\n"
    "v_pk_fma_f32 v[230:231], v[230:231], v[136:137], v[232:233]\n"
    "v_pk_mul_f32 v[232:233], v[232:233], v[136:137]\n"
    "v_pk_fma_f32 v[138:139], v[138:139], s[70:71], v[106:107]\n"
    "v_pk_fma_f32 v[230:231], v[230:231], v[138:139], v[232:233]\n"
    "v_pk_mul_f32 v[232:233], v[232:233], v[138:139]\n"
    "v_pk_fma_f32 v[140:141], v[140:141], s[70:71], v[108:109]\n"
    "v_pk_fma_f32 v[230:231], v[230:231], v[140:141], v[232:233]\n"
    "v_pk_mul_f32 v[232:233], v[232:233], v[140:141]\n"
    "v_pk_fma_f32 v[142:143], v[142:143], s[70:71], v[110:111]\n"
    "v_pk_fma_f32 v[230:231], v[230:231], v[142:143], v[232:233]\n"
    "v_pk_mul_f32 v[232:233], v[232:233], v[142:143]\n"
    "v_rcp_f32 v232, v232\n"
    "v_rcp_f32 v233, v233\n"
    "v_fma_f32 v230, v230, v232, v247\n"
    "v_fmac_f32 v230, v231, v233\n"
    "v_cvt_pk_bf16_f32 v230, v230, v230\n"
    "s_add_u32 s67, s67, 64\n"
    "s_waitcnt lgkmcnt(0)\n"
    "ds_write_b16 v245, v230\n"
    "ds_read_b64 v[228:229], v246\n"
    "s_cmp_le_u32 s34, s76\n"
    "s_cbranch_scc1 .Ldin_G0_wait\n"
    ".Ldin_G0_go:\n"
    "s_sub_u32 s94, s34, 60\n"
    "s_max_u32 s76, s76, s94\n"
    "s_add_u32 s76, s76, s72\n"
    ".Ldin_G0_dma:\n"
    "buffer_load_dwordx4 v[236:237], s[40:43], 0 idxen offen offset:0 nt lds\n"
    "buffer_load_dwordx4 v[238:239], s[40:43], 0 idxen offen offset:1024 nt lds\n"
    ".Ldin_G0_nd:\n"
    "s_cmp_eq_u32 s67, s82\n"
    "s_cbranch_scc1 .Ldin_L1\n"
    ".Ldin_G1:\n"
    "v_mfma_f32_16x16x32_bf16 v[128:131], v[0:3], v[160:163], v[112:115]\n"
    "v_exp_f32 v144, v144 clamp\n"
    "v_mfma_f32_16x16x32_bf16 v[128:131], v[4:7], v[164:167], v[128:131]\n"
    "v_exp_f32 v145, v145 clamp\n"
    "v_mfma_f32_16x16x32_bf16 v[132:135], v[8:11], v[160:163], v[116:119]\n"
    "v_exp_f32 v146, v146 clamp\n"
    "v_mfma_f32_16x16x32_bf16 v[132:135], v[12:15], v[164:167], v[132:135]\n"
    "v_exp_f32 v147, v147 clamp\n"
    "v_mfma_f32_16x16x32_bf16 v[136:139], v[16:19], v[160:163], v[120:123]\n"
    "v_exp_f32 v148, v148 clamp\n"
    "v_mfma_f32_16x16x32_bf16 v[136:139], v[20:23], v[164:167], v[136:139]\n"
    "v_exp_f32 v149, v149 clamp\n"
    "v_mfma_f32_16x16x32_bf16 v[140:143], v[24:27], v[160:163], v[124:127]\n"
    "v_exp_f32 v150, v150 clamp\n"
    "v_mfma_f32_16x16x32_bf16 v[140:143], v[28:31], v[164:167], v[140:143]\n"
    "v_exp_f32 v151, v151 clamp\n"
    "v_exp_f32 v152, v152 clamp\n"
    "s_cmp_eq_u32 s67, s80\n"
    "s_cselect_b32 s67, s65, s67\n"
    "s_add_u32 m0, s68, 4096\n"
    "v_exp_f32 v153, v153 clamp\n"
    "v_add_u32_e64 v253, s67, v244\n"
    "s_waitcnt lgkmcnt(0)\n"
    "v_mfma_f32_4x4x4_16b_bf16 v[224:227], v[228:229], v[168:169], v[224:227] cbsz:4 abid:0\n"
    "v_exp_f32 v154, v154 clamp\n"
    "v_exp_f32 v155, v155 clamp\n"
    "v_mfma_f32_4x4x4_16b_bf16 v[224:227], v[228:229], v[170:171], v[224:227] cbsz:4 abid:1\n"
    "v_exp_f32 v156, v156 clamp\n"
    "v_exp_f32 v157, v157 clamp\n"
    "v_mfma_f32_4x4x4_16b_bf16 v[224:227], v[228:229], v[172:173], v[224:227] cbsz:4 abid:2\n"
    "v_exp_f32 v158, v158 clamp\n"
    "v_exp_f32 v159, v159 clamp\n"
    "v_mfma_f32_4x4x4_16b_bf16 v[224:227], v[228:229], v[174:175], v[224:227] cbsz:4 abid:3\n"
    "s_waitcnt vmcnt(4)\n"
    "s_memrealtime s[34:35]\n"
    "ds_read_b64_tr_b16 v[168:169], v240 offset:4096\n"
    "ds_read_b64_tr_b16 v[170:171], v241 offset:4352\n"
    "ds_read_b64_tr_b16 v[172:173], v242 offset:4608\n"
    "ds_read_b64_tr_b16 v[174:175], v243 offset:4864\n"
    "ds_read_b32 v236, v253 offset:0\n"
    "ds_read_b32 v238, v253 offset:4\n"
    "ds_read_b128 v[160:163], v234 offset:6144\n"
    "ds_read_b128 v[164:167], v235 offset:6144\n"
    "v_pk_fma_f32 v[144:145], v[144:145], s[70:71], v[96:97]\n"
    "v_pk_fma_f32 v[146:147], v[146:147], s[70:71], v[98:99]\n"
    "v_pk_add_f32 v[230:231], v[144:145], v[146:147]\n"
    "v_pk_mul_f32 v[232:233], v[144:145], v[146:147]\n"
    "v_pk_fma_f32 v[148:149], v[148:149], s[70:71], v[100:101]\n"
    "v_pk_fma_f32 v[230:231], v[230:231], v[148:149], v[232:233]\n"
    "v_pk_mul_f32 v[232:233], v[232:233], v[148:149]\n"
    "v_pk_fma_f32 v[150:151], v[150:151], s[70:71], v[102:103]\n"
    "v_pk_fma_f32 v[230:231], v[230:231], v[150:151], v[232:233]\n"
    "v_pk_mul_f32 v[232:233], v[232:233], v[150:151]\n"
    "v_pk_fma_f32 v[152:153], v[152:153], s[70:71], v[104:105]\n"
    "v_pk_fma_f32 v[230:231], v[230:231], v[152:153], v[232:233]\n"
    "v_pk_mul_f32 v[232:233], v[232:233], v[152:153]\n"
    "v_pk_fma_f32 v[154:155], v[154:155], s[70:71], v[106:107]\n"
    "v_pk_fma_f32 v[230:231], v[230:231], v[154:155], v[232:233]\n"
    "v_pk_mul_f32 v[232:233], v[232:233], v[154:155]\n"
    "v_pk_fma_f32 v[156:157], v[156:157], s[70:71], v[108:109]\n"
    "v_pk_fma_f32 v[230:231], v[230:231], v[156:157], v[232:233]\n"
    "v_pk_mul_f32 v[232:233], v[232:233], v[156:157]\n"
    "v_pk_fma_f32 v[158:159], v[158:159], s[70:71], v[110:111]\n"
    "v_pk_fma_f32 v[230:231], v[230:231], v[158:159], v[232:233]\n"
    "v_pk_mul_f32 v[232:233], v[232:233], v[158:159]\n"
    "v_rcp_f32 v232, v232\n"
    "v_rcp_f32 v233, v233\n"
    "v_fma_f32 v230, v230, v232, v247\n"
    "v_fmac_f32 v230, v231, v233\n"
    "v_cvt_pk_bf16_f32 v230, v230, v230\n"
    "s_add_u32 s67, s67, 64\n"
    "s_waitcnt lgkmcnt(0)\n"
    "ds_write_b16 v245, v230\n"
    "ds_read_b64 v[228:229], v246\n"
    "s_cmp_le_u32 s34, s76\n"
    "s_cbranch_scc1 .Ldin_G1_wait\n"
    ".Ldin_G1_go:\n"
    "s_add_u32 s76, s76, s72\n"
    ".Ldin_G1_dma:\n"
    "buffer_load_dwordx4 v[236:237], s[40:43], 0 idxen offen offset:0 nt lds\n"
    "buffer_load_dwordx4 v[238:239], s[40:43], 0 idxen offen offset:1024 nt lds\n"
    ".Ldin_G1_nd:\n"
    "s_cmp_eq_u32 s67, s82\n"
    "s_cbranch_scc1 .Ldin_L2\n"
    "s_branch .Ldin_G2\n"
    ".Ldin_B1_wait:\n"
    "s_cmp_lt_i32 s59, s57\n"
    "s_cbranch_scc1 .Ldin_B1_w2\n"
    "s_sub_u32 s94, s67, s65\n"
    "s_cmp_gt_u32 s94, 256\n"
    "s_cbranch_scc1 .Ldin_B1_dma\n"
    "s_branch .Ldin_T2\n"
    ".Ldin_B1_slp:\n"
    "s_sleep 1\n"
    ".Ldin_B1_w2:\n"
    "s_memrealtime s[34:35]\n"
    "s_waitcnt lgkmcnt(0)\n"
    "s_sub_u32 s94, s34, s76\n"
    "s_cmp_lt_i32 s94, 0\n"
    "s_cbranch_scc1 .Ldin_B1_slp\n"
    "s_branch .Ldin_B1_go\n"
    ".Ldin_G2_wait:\n"
    "s_cmp_lt_i32 s59, s57\n"
    "s_cbranch_scc1 .Ldin_G2_w2\n"
    "s_sub_u32 s94, s67, s65\n"
    "s_cmp_gt_u32 s94, 256\n"
    "s_cbranch_scc1 .Ldin_G2_dma\n"
    "s_branch .Ldin_T3\n"
    ".Ldin_G2_slp:\n"
    "s_sleep 1\n"
    ".Ldin_G2_w2:\n"
    "s_memrealtime s[34:35]\n"
    "s_waitcnt lgkmcnt(0)\n"
    "s_sub_u32 s94, s34, s76\n"
    "s_cmp_lt_i32 s94, 0\n"
    "s_cbranch_scc1 .Ldin_G2_slp\n"
    "s_branch .Ldin_G2_go\n"
    ".Ldin_G3_wait:\n"
    "s_cmp_lt_i32 s59, s57\n"
    "s_cbranch_scc1 .Ldin_G3_w2\n"
    "s_sub_u32 s94, s67, s65\n"
    "s_cmp_gt_u32 s94, 256\n"
    "s_cbranch_scc1 .Ldin_G3_dma\n"
    "s_branch .Ldin_T0\n"
    ".Ldin_G3_slp:\n"
    "s_sleep 1\n"
    ".Ldin_G3_w2:\n"
    "s_memrealtime s[34:35]\n"
    "s_waitcnt lgkmcnt(0)\n"
    "s_sub_u32 s94, s34, s76\n"
    "s_cmp_lt_i32 s94, 0\n"
    "s_cbranch_scc1 .Ldin_G3_slp\n"
    "s_branch .Ldin_G3_go\n"
    ".Ldin_G0_wait:\n"
    "s_cmp_lt_i32 s59, s57\n"
    "s_cbranch_scc1 .Ldin_G0_w2\n"
    "s_sub_u32 s94, s67, s65\n"
    "s_cmp_gt_u32 s94, 256\n"
    "s_cbranch_scc1 .Ldin_G0_dma\n"
    "s_branch .Ldin_T1\n"
    ".Ldin_G0_slp:\n"
    "s_sleep 1\n"
    ".Ldin_G0_w2:\n"
    "s_memrealtime s[34:35]\n"
    "s_waitcnt lgkmcnt(0)\n"
    "s_sub_u32 s94, s34, s76\n"
    "s_cmp_lt_i32 s94, 0\n"
    "s_cbranch_scc1 .Ldin_G0_slp\n"
    "s_branch .Ldin_G0_go\n"
    ".Ldin_G1_wait:\n"
    "s_cmp_lt_i32 s59, s57\n"
    "s_cbranch_scc1 .Ldin_G1_w2\n"
    "s_sub_u32 s94, s67, s65\n"
    "s_cmp_gt_u32 s94, 256\n"
    "s_cbranch_scc1 .Ldin_G1_dma\n"
    "s_branch .Ldin_T2\n"
    ".Ldin_G1_slp:\n"
    "s_sleep 1\n"
    ".Ldin_G1_w2:\n"
    "s_memrealtime s[34:35]\n"
    "s_waitcnt lgkmcnt(0)\n"
    "s_sub_u32 s94, s34, s76\n"
    "s_cmp_lt_i32 s94, 0\n"
    "s_cbranch_scc1 .Ldin_G1_slp\n"
    "s_branch .Ldin_G1_go\n"
    ".Ldin_odd:\n"
    "s_cmp_lt_i32 s58, s57\n"
    "s_cbranch_scc1 .Ldin_A0s\n"
    "s_waitcnt vmcnt(0)\n"
    "s_branch .Ldin_exit\n"
    ".p2align 5\n"
    ".Ldin_y2:\n"
    "s_waitcnt vmcnt(8)\n"
    "v_lshlrev_b32 v56, 16, v8\n"
    "v_lshlrev_b32 v60, 16, v9\n"
    "v_lshlrev_b32 v64, 16, v10\n"
    "v_lshlrev_b32_e64 v68, 16, v11\n"
    "v_bitop3_b32 v56, v56, s84, v7 bitop3:0x6c\n"
    "v_bitop3_b32 v58, v8, s84, v7 bitop3:0x6c\n"
    "v_bitop3_b32 v60, v60, s84, v7 bitop3:0x6c\n"
    "v_bitop3_b32 v62, v9, s84, v7 bitop3:0x6c\n"
    "v_bitop3_b32 v64, v64, s84, v7 bitop3:0x6c\n"
    "v_bitop3_b32 v66, v10, s84, v7 bitop3:0x6c\n"
    "v_bitop3_b32 v68, v68, s84, v7 bitop3:0x6c\n"
    "v_bitop3_b32 v70, v11, s84, v7 bitop3:0x6c\n"
    "s_waitcnt vmcnt(6)\n"
    "buffer_load_dwordx4 v[32:35], v27, s[44:47], s39 offen nt\n"
    "s_add_u32 s39, s39, s17\n"
    "v_lshlrev_b32 v112, 16, v128\n"
    "v_and_b32 v114, s37, v128\n"
    "v_lshlrev_b32 v116, 16, v129\n"
    "v_and_b32 v118, s37, v129\n"
    "v_lshlrev_b32 v120, 16, v130\n"
    "v_and_b32 v122, s37, v130\n"
    "v_lshlrev_b32 v124, 16, v131\n"
    "v_and_b32 v126, s37, v131\n"
    "buffer_load_dwordx4 v[36:39], v27, s[44:47], s39 offen nt\n"
    "s_add_u32 s39, s39, s17\n"
    "v_lshlrev_b32 v113, 16, v132\n"
    "v_and_b32 v115, s37, v132\n"
    "v_lshlrev_b32 v117, 16, v133\n"
    "v_and_b32 v119, s37, v133\n"
    "v_lshlrev_b32 v121, 16, v134\n"
    "v_and_b32 v123, s37, v134\n"
    "v_lshlrev_b32 v125, 16, v135\n"
    "v_and_b32_e64 v127, s37, v135\n"
    "v_pk_mul_f32 v[112:113], v[112:113], v[56:57] op_sel_hi:[1,0]\n"
    "v_pk_mul_f32 v[114:115], v[114:115], v[58:59] op_sel_hi:[1,0]\n"
    "v_pk_mul_f32 v[116:117], v[116:117], v[60:61] op_sel_hi:[1,0]\n"
    "v_pk_mul_f32 v[118:119], v[118:119], v[62:63] op_sel_hi:[1,0]\n"
    "v_pk_mul_f32 v[120:121], v[120:121], v[64:65] op_sel_hi:[1,0]\n"
    "v_pk_mul_f32 v[122:123], v[122:123], v[66:67] op_sel_hi:[1,0]\n"
    "v_pk_mul_f32 v[124:125], v[124:125], v[68:69] op_sel_hi:[1,0]\n"
    "v_pk_mul_f32 v[126:127], v[126:127], v[70:71] op_sel_hi:[1,0]\n"
    "s_waitcnt vmcnt(6)\n"
    "buffer_load_dwordx4 v[40:43], v27, s[44:47], s39 offen nt\n"
    "s_add_u32 s39, s39, s17\n"
    "v_lshlrev_b32 v168, 16, v136\n"
    "v_and_b32 v170, s37, v136\n"
    "v_lshlrev_b32 v172, 16, v137\n"
    "v_and_b32 v174, s37, v137\n"
    "v_lshlrev_b32 v176, 16, v138\n"
    "v_and_b32 v178, s37, v138\n"
    "v_lshlrev_b32 v180, 16, v139\n"
    "v_and_b32 v182, s37, v139\n"
    "buffer_load_dwordx4 v[44:47], v27, s[44:47], s39 offen nt\n"
    "s_add_u32 s39, s39, s17\n"
    "v_lshlrev_b32 v169, 16, v140\n"
    "v_and_b32 v171, s37, v140\n"
    "v_lshlrev_b32 v173, 16, v141\n"
    "v_and_b32 v175, s37, v141\n"
    "v_lshlrev_b32 v177, 16, v142\n"
    "v_and_b32 v179, s37, v142\n"
    "v_lshlrev_b32 v181, 16, v143\n"
    "v_and_b32_e64 v183, s37, v143\n"
    "v_pk_fma_f32 v[168:169], v[168:169], v[56:57], v[112:113] op_sel_hi:[1,0,1]\n"
    "v_pk_fma_f32 v[170:171], v[170:171], v[58:59], v[114:115] op_sel_hi:[1,0,1]\n"
    "ds_write2st64_b64 v12, v[168:169], v[170:171] offset0:0 offset1:8\n"
    "v_pk_fma_f32 v[172:173], v[172:173], v[60:61], v[116:117] op_sel_hi:[1,0,1]\n"
    "v_pk_fma_f32 v[174:175], v[174:175], v[62:63], v[118:119] op_sel_hi:[1,0,1]\n"
    "ds_write2st64_b64 v12, v[172:173], v[174:175] offset0:16 offset1:24\n"
    "v_pk_fma_f32 v[176:177], v[176:177], v[64:65], v[120:121] op_sel_hi:[1,0,1]\n"
    "v_pk_fma_f32 v[178:179], v[178:179], v[66:67], v[122:123] op_sel_hi:[1,0,1]\n"
    "ds_write2st64_b64 v13, v[176:177], v[178:179] offset0:0 offset1:8\n"
    "v_pk_fma_f32 v[180:181], v[180:181], v[68:69], v[124:125] op_sel_hi:[1,0,1]\n"
    "v_pk_fma_f32 v[182:183], v[182:183], v[70:71], v[126:127] op_sel_hi:[1,0,1]\n"
    "ds_write2st64_b64 v13, v[180:181], v[182:183] offset0:16 offset1:24\n"
    "s_waitcnt vmcnt(6)\n"
    "buffer_load_dwordx4 v[48:51], v27, s[44:47], s39 offen nt\n"
    "s_add_u32 s39, s39, s17\n"
    "v_lshlrev_b32 v80, 16, v144\n"
    "v_and_b32 v82, s37, v144\n"
    "v_lshlrev_b32 v84, 16, v145\n"
    "v_and_b32 v86, s37, v145\n"
    "v_lshlrev_b32 v88, 16, v146\n"
    "v_and_b32 v90, s37, v146\n"
    "v_lshlrev_b32 v92, 16, v147\n"
    "v_and_b32 v94, s37, v147\n"
    "buffer_load_dwordx4 v[52:55], v27, s[44:47], s39 offen nt\n"
    "v_lshlrev_b32 v81, 16, v148\n"
    "v_and_b32 v83, s37, v148\n"
    "v_lshlrev_b32 v85, 16, v149\n"
    "v_and_b32 v87, s37, v149\n"
    "v_lshlrev_b32 v89, 16, v150\n"
    "v_and_b32 v91, s37, v150\n"
    "v_lshlrev_b32 v93, 16, v151\n"
    "v_and_b32 v95, s37, v151\n"
    "v_pk_mul_f32 v[80:81], v[80:81], v[56:57] op_sel_hi:[1,0]\n"
    "v_cvt_pk_bf16_f32 v80, v80, v81\n"
    "v_pk_mul_f32 v[82:83], v[82:83], v[58:59] op_sel_hi:[1,0]\n"
    "v_cvt_pk_bf16_f32 v82, v82, v83\n"
    "ds_write2st64_b32 v14, v80, v82 offset0:0 offset1:8\n"
    "v_pk_mul_f32 v[84:85], v[84:85], v[60:61] op_sel_hi:[1,0]\n"
    "v_cvt_pk_bf16_f32 v84, v84, v85\n"
    "v_pk_mul_f32 v[86:87], v[86:87], v[62:63] op_sel_hi:[1,0]\n"
    "v_cvt_pk_bf16_f32 v86, v86, v87\n"
    "ds_write2st64_b32 v14, v84, v86 offset0:16 offset1:24\n"
    "v_pk_mul_f32 v[88:89], v[88:89], v[64:65] op_sel_hi:[1,0]\n"
    "v_cvt_pk_bf16_f32 v88, v88, v89\n"
    "v_pk_mul_f32 v[90:91], v[90:91], v[66:67] op_sel_hi:[1,0]\n"
    "v_cvt_pk_bf16_f32 v90, v90, v91\n"
    "ds_write2st64_b32 v15, v88, v90 offset0:0 offset1:8\n"
    "v_pk_mul_f32 v[92:93], v[92:93], v[68:69] op_sel_hi:[1,0]\n"
    "v_cvt_pk_bf16_f32 v92, v92, v93\n"
    "v_pk_mul_f32 v[94:95], v[94:95], v[70:71] op_sel_hi:[1,0]\n"
    "v_cvt_pk_bf16_f32 v94, v94, v95\n"
    "ds_write2st64_b32 v15, v92, v94 offset0:16 offset1:24\n"
    "s_waitcnt vmcnt(6)\n"
    "v_lshlrev_b32 v184, 16, v152\n"
    "v_and_b32 v186, s37, v152\n"
    "v_lshlrev_b32 v188, 16, v153\n"
    "v_and_b32 v190, s37, v153\n"
    "v_lshlrev_b32 v192, 16, v154\n"
    "v_and_b32 v194, s37, v154\n"
    "v_lshlrev_b32 v196, 16, v155\n"
    "v_and_b32 v198, s37, v155\n"
    "v_lshlrev_b32 v185, 16, v156\n"
    "v_and_b32 v187, s37, v156\n"
    "v_lshlrev_b32 v189, 16, v157\n"
    "v_and_b32 v191, s37, v157\n"
    "v_lshlrev_b32 v193, 16, v158\n"
    "v_and_b32 v195, s37, v158\n"
    "v_lshlrev_b32 v197, 16, v159\n"
    "v_and_b32_e64 v199, s37, v159\n"
    "s_waitcnt lgkmcnt(0)\n"
    "s_barrier\n"
    "ds_read_b128 v[128:131], v97 offset:0\n"
    "ds_read_b128 v[132:135], v97 offset:16\n"
    "ds_read_b128 v[28:31], v99 offset:35200\n"
    "v_pk_fma_f32 v[184:185], v[184:185], v[56:57], v[112:113] op_sel_hi:[1,0,1] neg_lo:[0,0,1] neg_hi:[0,0,1]\n"
    "v_cvt_pk_bf16_f32 v184, v184, v185\n"
    "v_pk_fma_f32 v[186:187], v[186:187], v[58:59], v[114:115] op_sel_hi:[1,0,1] neg_lo:[0,0,1] neg_hi:[0,0,1]\n"
    "v_cvt_pk_bf16_f32 v186, v186, v187\n"
    "ds_write2st64_b32 v14, v184, v186 offset0:32 offset1:40\n"
    "v_pk_fma_f32 v[188:189], v[188:189], v[60:61], v[116:117] op_sel_hi:[1,0,1] neg_lo:[0,0,1] neg_hi:[0,0,1]\n"
    "v_cvt_pk_bf16_f32 v188, v188, v189\n"
    "v_pk_fma_f32 v[190:191], v[190:191], v[62:63], v[118:119] op_sel_hi:[1,0,1] neg_lo:[0,0,1] neg_hi:[0,0,1]\n"
    "v_cvt_pk_bf16_f32 v190, v190, v191\n"
    "ds_write2st64_b32 v14, v188, v190 offset0:48 offset1:56\n"
    "v_pk_fma_f32 v[192:193], v[192:193], v[64:65], v[120:121] op_sel_hi:[1,0,1] neg_lo:[0,0,1] neg_hi:[0,0,1]\n"
    "v_cvt_pk_bf16_f32 v192, v192, v193\n"
    "v_pk_fma_f32 v[194:195], v[194:195], v[66:67], v[122:123] op_sel_hi:[1,0,1] neg_lo:[0,0,1] neg_hi:[0,0,1]\n"
    "v_cvt_pk_bf16_f32 v194, v194, v195\n"
    "ds_write2st64_b32 v15, v192, v194 offset0:32 offset1:40\n"
    "v_pk_fma_f32 v[196:197], v[196:197], v[68:69], v[124:125] op_sel_hi:[1,0,1] neg_lo:[0,0,1] neg_hi:[0,0,1]\n"
    "v_cvt_pk_bf16_f32 v196, v196, v197\n"
    "v_pk_fma_f32 v[198:199], v[198:199], v[70:71], v[126:127] op_sel_hi:[1,0,1] neg_lo:[0,0,1] neg_hi:[0,0,1]\n"
    "v_cvt_pk_bf16_f32 v198, v198, v199\n"
    "ds_write2st64_b32 v15, v196, v198 offset0:48 offset1:56\n"
    "s_mul_i32 s36, s61, s16\n"
    "s_add_u32 s94, s36, s16\n"
    "s_cmp_lt_u32 s61, s14\n"
    "s_cselect_b32 s46, s94, 0\n"
    "buffer_load_dwordx2 v[228:229], v105, s[44:47], s36 offen nt\n"
    "s_lshl_b32 s94, s61, 6\n"
    "s_add_u32 s94, s3, s94\n"
    "s_cmp_lt_u32 s61, s14\n"
    "s_cselect_b32 s58, s94, s57\n"
    "s_add_u32 s95, s61, s15\n"
    "s_add_u32 s36, s36, s17\n"
    "s_lshl_b32 s94, s95, 6\n"
    "s_add_u32 s94, s3, s94\n"
    "s_add_u32 s96, s36, s16\n"
    "s_add_u32 s94, s94, 8\n"
    "s_cmp_lt_u32 s95, s14\n"
    "s_cselect_b32 s46, s96, 0\n"
    "s_cselect_b32 s59, s94, s57\n"
    "buffer_load_short_d16_hi v224, v249, s[44:47], s36 offen offset:0 nt\n"
    "buffer_load_short_d16_hi v225, v249, s[44:47], s36 offen offset:128 nt\n"
    "buffer_load_short_d16_hi v226, v249, s[44:47], s36 offen offset:256 nt\n"
    "buffer_load_short_d16_hi v227, v249, s[44:47], s36 offen offset:384 nt\n"
    "s_mul_i32 s46, s14, s16\n"
    "s_waitcnt lgkmcnt(0)\n"
    "s_barrier\n"
    "ds_read_b128 v[8:11], v98 offset:0\n"
    "ds_read_b128 v[12:15], v98 offset:1024\n"
    "s_waitcnt lgkmcnt(1)\n"
    "v_mfma_f32_16x16x32_bf16 v[112:115], v[8:11], v[128:131], v[28:31]\n"
    "s_waitcnt lgkmcnt(0)\n"
    "v_mfma_f32_16x16x32_bf16 v[112:115], v[12:15], v[132:135], v[112:115]\n"
    "s_nop 7\n"
    "ds_write_b128 v100, v[112:115] offset:17408\n"
    "s_memtime s[88:89]\n"
    "s_memrealtime s[90:91]\n"
    "s_waitcnt lgkmcnt(0)\n"
    "s_barrier\n"
    "s_sub_u32 s88, s88, s20\n"
    "s_sub_u32 s90, s90, s10\n"
    "s_lshl3_add_u32 s88, s88, 4960\n"
    "s_mul_i32 s90, s90, 181\n"
    "s_cmp_lt_u32 s88, s90\n"
    "s_cselect_b32 s7, 1, 0\n"
    "s_cbranch_scc1 .Ldin_xo\n"
    "s_setprio 0\n"
    ".Ldin_xo:\n"
    "s_mov_b64 exec, 0\n"
    "s_bitcmp1_b32 s3, 1\n"
    "s_cbranch_scc1 .Ldin_t2_hi\n"
    "s_bitcmp1_b32 s3, 0\n"
    "s_cbranch_scc0 .Ldin_stub_8\n"
    "s_branch .Ldin_stub_9\n"
    ".Ldin_t2_hi:\n"
    "s_bitcmp1_b32 s3, 0\n"
    "s_cbranch_scc0 .Ldin_stub_10\n"
    "s_branch .Ldin_stub_11\n"
    ".Ldin_t2_ret:\n"
    "s_mov_b64 exec, -1\n"
    "s_mov_b64 exec, 0\n"
    "s_bitcmp1_b32 s3, 1\n"
    "s_cbranch_scc1 .Ldin_t4_hi\n"
    "s_bitcmp1_b32 s3, 0\n"
    "s_cbranch_scc0 .Ldin_stub_16\n"
    "s_branch .Ldin_stub_17\n"
    ".Ldin_t4_hi:\n"
    "s_bitcmp1_b32 s3, 0\n"
    "s_cbranch_scc0 .Ldin_stub_18\n"
    "s_branch .Ldin_stub_19\n"
    ".Ldin_t4_ret:\n"
    "s_mov_b64 exec, -1\n"
    "v_and_b32 v184, 24, v249\n"
    "v_add_u32 v184, v184, v252\n"
    "ds_read_b64 v[184:185], v184\n"
    "ds_read_b32 v247, v250 offset:35072\n"
    "ds_read_b32 v232, v250 offset:35076\n"
    "s_and_b32 s84, s12, 7\n"
    "v_sub_u32 v4, s16, v26\n"
    "s_lshl_b32 s84, -1, s84\n"
    "v_cmp_gt_i32 vcc, 16, v4\n"
    "s_not_b32 s84, s84\n"
    "v_mov_b32 v6, s84\n"
    "s_add_u32 s85, s66, 480\n"
    "v_cndmask_b32 v6, -1, v6, vcc\n"
    "v_and_b32 v4, 32, v251\n"
    "v_add3_u32 v7, v4, v251, s85\n"
    "v_mov_b32 v5, 0\n"
    "s_mov_b32 s4, 0x10001\n"
    "s_mov_b32 s5, 0xc080c080\n"
    "s_waitcnt lgkmcnt(0)\n"
    "v_readfirstlane_b32 s69, v232\n"
    "v_and_b32 v184, v254, v184\n"
    "v_and_b32_e64 v185, v255, v185\n"
    "s_waitcnt vmcnt(4)\n"
    "v_cmp_ne_u16_e64 s[84:85], 0, v228\n"
    "v_cmp_lt_u32_e64 s[86:87], s38, v228\n"
    "v_cmp_ne_u16_e64 s[88:89], 0, v229\n"
    "v_cmp_lt_u32_e64 s[90:91], s38, v229\n"
    "v_mbcnt_lo_u32_b32 v220, s84, v220\n"
    "s_bcnt1_i32_b64 s92, s[84:85]\n"
    "v_mbcnt_hi_u32_b32 v220, s85, v220\n"
    "s_lshl2_add_u32 s92, s92, s67\n"
    "v_mbcnt_lo_u32_b32 v220, s86, v220\n"
    "s_bcnt1_i32_b64 s93, s[86:87]\n"
    "v_mbcnt_hi_u32_b32 v220, s87, v220\n"
    "s_lshl2_add_u32 s93, s93, s92\n"
    "v_mbcnt_lo_u32_b32 v220, s88, v220\n"
    "s_bcnt1_i32_b64 s94, s[88:89]\n"
    "v_mbcnt_hi_u32_b32 v220, s89, v220\n"
    "s_lshl2_add_u32 s94, s94, s93\n"
    "v_mbcnt_lo_u32_b32 v220, s90, v220\n"
    "s_bcnt1_i32_b64 s95, s[90:91]\n"
    "v_mbcnt_hi_u32_b32 v220, s91, v220\n"
    "s_lshl2_add_u32 s95, s95, s94\n"
    "v_mov_b32_e64 v21, -1\n"
    "v_lshl_add_u32 v20, v249, 1, s95\n"
    "v_addc_co_u32_e64 v221, vcc, v220, 0, s[84:85]\n"
    "v_addc_co_u32_e64 v222, vcc, v221, 0, s[86:87]\n"
    "v_addc_co_u32_e64 v223, vcc, v222, 0, s[88:89]\n"
    "ds_write2_b32 v20, v21, v21 offset1:64\n"
    "v_lshlrev_b32 v220, 2, v220\n"
    "v_lshlrev_b32 v221, 2, v221\n"
    "v_lshlrev_b32 v222, 2, v222\n"
    "v_lshlrev_b32_e64 v223, 2, v223\n"
    "s_mov_b64 exec, s[84:85]\n"
    "ds_write_b32 v220, v216\n"
    "s_mov_b64 exec, s[86:87]\n"
    "ds_write_b32 v221, v217\n"
    "s_mov_b64 exec, s[88:89]\n"
    "ds_write_b32 v222, v218\n"
    "s_mov_b64 exec, s[90:91]\n"
    "ds_write_b32 v223, v219\n"
    "s_mov_b64 exec, -1\n"
    "ds_read_b32 v200, v253 offset:0\n"
    "ds_read_b32 v202, v253 offset:4\n"
    "ds_read_b32 v204, v253 offset:64\n"
    "ds_read_b32 v206, v253 offset:68\n"
    "ds_read_b32 v208, v253 offset:128\n"
    "ds_read_b32 v210, v253 offset:132\n"
    "ds_read_b32 v212, v253 offset:192\n"
    "ds_read_b32 v214, v253 offset:196\n"
    "s_add_u32 m0, s68, 0\n"
    "s_waitcnt lgkmcnt(6)\n"
    "buffer_load_dwordx4 v[200:201], s[40:43], 0 idxen offen offset:0 nt lds\n"
    "buffer_load_dwordx4 v[202:203], s[40:43], 0 idxen offen offset:1024 nt lds\n"
    "s_add_u32 s95, s95, 60\n"
    "s_add_u32 s94, s67, 384\n"
    "s_andn2_b32 s95, s95, 63\n"
    "s_max_u32 s80, s95, s94\n"
    "s_add_u32 m0, s68, 2048\n"
    "s_waitcnt lgkmcnt(0)\n"
    "buffer_load_dwordx4 v[204:205], s[40:43], 0 idxen offen offset:0 nt lds\n"
    "s_mov_b32 s94, 0x10001\n"
    "s_mov_b32 s95, 0xc080c080\n"
    "v_pk_min_u16 v22, v228, s94\n"
    "v_pk_min_u16 v23, v229, s94\n"
    "v_pk_mad_u16 v228, v22, s95, v228\n"
    "v_pk_mad_u16 v229, v23, s95, v229\n"
    "s_sub_u32 s87, s80, s81\n"
    "v_or_b32 v228, v228, v229\n"
    "s_lshr_b32 s87, s87, 6\n"
    "s_lshl2_add_u32 s86, s3, 68224\n"
    "v_mov_b32 v106, s86\n"
    "v_mov_b32 v108, s66\n"
    "v_mov_b32 v109, s58\n"
    "v_mov_b32 v110, s59\n"
    "s_waitcnt vmcnt(3)\n"
    "v_cmp_neq_f32_e64 s[20:21], 0, v224\n"
    "v_cmp_neq_f32_e64 s[22:23], 0, v225\n"
    "v_cmp_neq_f32_e64 s[24:25], 0, v226\n"
    "v_cmp_neq_f32_e64 s[26:27], 0, v227\n"
    "v_fma_f32 v224, v224, v224, -v224\n"
    "v_fma_f32 v225, v225, v225, -v225\n"
    "v_fma_f32 v226, v226, v226, -v226\n"
    "v_fma_f32 v227, v227, v227, -v227\n"
    "s_bcnt1_i32_b64 s88, s[20:21]\n"
    "s_bcnt1_i32_b64 s89, s[22:23]\n"
    "buffer_load_dwordx4 v[206:207], s[40:43], 0 idxen offen offset:1024 nt lds\n"
    "s_bcnt1_i32_b64 s90, s[24:25]\n"
    "s_bcnt1_i32_b64 s91, s[26:27]\n"
    "v_or3_b32 v228, v228, v224, v225\n"
    "s_add_u32 s88, s88, s89\n"
    "v_or3_b32 v228, v228, v226, v227\n"
    "s_add_u32 s90, s90, s91\n"
    "v_cmp_ne_u32_e64 s[84:85], 0, v228\n"
    "s_add_u32 s88, s88, s90\n"
    "s_pack_ll_b32_b16 s88, s87, s88\n"
    "s_mov_b64 exec, 1\n"
    "v_mov_b32_e64 v107, s88\n"
    "ds_write2_b32 v108, v109, v110 offset1:2\n"
    "s_cmp_lg_u64 s[84:85], 0\n"
    "ds_write_b32 v106, v107\n"
    "s_cselect_b32 s91, 2.0, 0\n"
    "s_mov_b64 exec, -1\n"
    "s_or_b32 s58, s58, s91\n"
    "s_or_b32 s59, s59, s91\n"
    "v_pk_min_u16 v0, v32, s4\n"
    "v_pk_min_u16 v1, v33, s4\n"
    "v_pk_min_u16 v2, v34, s4\n"
    "v_pk_min_u16 v3, v35, s4\n"
    "v_pk_mad_u16 v32, v0, s5, v32\n"
    "v_pk_mad_u16 v33, v1, s5, v33\n"
    "s_add_u32 m0, s68, 4096\n"
    "s_nop 0\n"
    "buffer_load_dwordx4 v[208:209], s[40:43], 0 idxen offen offset:0 nt lds\n"
    "v_pk_mad_u16 v34, v2, s5, v34\n"
    "v_pk_mad_u16 v35, v3, s5, v35\n"
    "v_lshl_or_b32 v0, v1, 2, v0\n"
    "v_lshl_or_b32 v0, v2, 4, v0\n"
    "v_lshl_or_b32 v0, v3, 6, v0\n"
    "v_or3_b32 v5, v5, v32, v33\n"
    "v_lshrrev_b32 v1, 15, v0\n"
    "v_or3_b32 v5, v5, v34, v35\n"
    "v_bitop3_b32 v16, v1, v6, v0 bitop3:0xc8\n"
    "ds_write_b8 v7, v16 offset:0\n"
    "v_pk_min_u16 v0, v36, s4\n"
    "v_pk_min_u16 v1, v37, s4\n"
    "v_pk_min_u16 v2, v38, s4\n"
    "v_pk_min_u16 v3, v39, s4\n"
    "v_pk_mad_u16 v36, v0, s5, v36\n"
    "v_pk_mad_u16 v37, v1, s5, v37\n"
    "v_pk_mad_u16 v38, v2, s5, v38\n"
    "v_pk_mad_u16 v39, v3, s5, v39\n"
    "v_lshl_or_b32 v0, v1, 2, v0\n"
    "v_lshl_or_b32 v0, v2, 4, v0\n"
    "v_lshl_or_b32 v0, v3, 6, v0\n"
    "v_or3_b32 v5, v5, v36, v37\n"
    "v_lshrrev_b32_e64 v1, 15, v0\n"
    "v_or3_b32 v5, v5, v38, v39\n"
    "v_bitop3_b32 v17, v1, v6, v0 bitop3:0xc8\n"
    "buffer_load_dwordx4 v[210:211], s[40:43], 0 idxen offen offset:1024 nt lds\n"
    "ds_write_b8 v7, v17 offset:512\n"
    "v_pk_min_u16 v0, v40, s4\n"
    "v_pk_min_u16 v1, v41, s4\n"
    "v_pk_min_u16 v2, v42, s4\n"
    "v_pk_min_u16 v3, v43, s4\n"
    "v_pk_mad_u16 v40, v0, s5, v40\n"
    "v_pk_mad_u16 v41, v1, s5, v41\n"
    "v_pk_mad_u16 v42, v2, s5, v42\n"
    "v_pk_mad_u16 v43, v3, s5, v43\n"
    "v_lshl_or_b32 v0, v1, 2, v0\n"
    "v_lshl_or_b32 v0, v2, 4, v0\n"
    "v_lshl_or_b32 v0, v3, 6, v0\n"
    "v_or3_b32 v5, v5, v40, v41\n"
    "v_lshrrev_b32_e64 v1, 15, v0\n"
    "v_or3_b32 v5, v5, v42, v43\n"
    "v_bitop3_b32 v18, v1, v6, v0 bitop3:0xc8\n"
    "ds_write_b8 v7, v18 offset:1024\n"
    "v_pk_min_u16 v0, v44, s4\n"
    "v_pk_min_u16 v1, v45, s4\n"
    "v_pk_min_u16 v2, v46, s4\n"
    "v_pk_min_u16 v3, v47, s4\n"
    "v_pk_mad_u16 v44, v0, s5, v44\n"
    "v_pk_mad_u16 v45, v1, s5, v45\n"
    "v_pk_mad_u16 v46, v2, s5, v46\n"
    "v_pk_mad_u16 v47, v3, s5, v47\n"
    "s_add_u32 m0, s68, 6144\n"
    "s_nop 0\n"
    "buffer_load_dwordx4 v[212:213], s[40:43], 0 idxen offen offset:0 nt lds\n"
    "v_lshl_or_b32 v0, v1, 2, v0\n"
    "v_lshl_or_b32 v0, v2, 4, v0\n"
    "v_lshl_or_b32 v0, v3, 6, v0\n"
    "v_or3_b32 v5, v5, v44, v45\n"
    "v_lshrrev_b32 v1, 15, v0\n"
    "v_or3_b32 v5, v5, v46, v47\n"
    "v_bitop3_b32 v19, v1, v6, v0 bitop3:0xc8\n"
    "ds_write_b8 v7, v19 offset:1536\n"
    "v_pk_min_u16 v0, v48, s4\n"
    "v_pk_min_u16 v1, v49, s4\n"
    "v_pk_min_u16 v2, v50, s4\n"
    "v_pk_min_u16 v3, v51, s4\n"
    "v_pk_mad_u16 v48, v0, s5, v48\n"
    "v_pk_mad_u16 v49, v1, s5, v49\n"
    "v_pk_mad_u16 v50, v2, s5, v50\n"
    "v_pk_mad_u16 v51, v3, s5, v51\n"
    "v_lshl_or_b32 v0, v1, 2, v0\n"
    "v_lshl_or_b32 v0, v2, 4, v0\n"
    "v_lshl_or_b32 v0, v3, 6, v0\n"
    "v_or3_b32 v5, v5, v48, v49\n"
    "v_lshrrev_b32_e64 v1, 15, v0\n"
    "v_or3_b32 v5, v5, v50, v51\n"
    "v_bitop3_b32 v16, v1, v6, v0 bitop3:0xc8\n"
    "ds_write_b8 v7, v16 offset:2048\n"
    "v_pk_min_u16 v0, v52, s4\n"
    "buffer_load_dwordx4 v[214:215], s[40:43], 0 idxen offen offset:1024 nt lds\n"
    "v_pk_min_u16 v1, v53, s4\n"
    "v_pk_min_u16 v2, v54, s4\n"
    "v_pk_min_u16 v3, v55, s4\n"
    "v_pk_mad_u16 v52, v0, s5, v52\n"
    "v_pk_mad_u16 v53, v1, s5, v53\n"
    "v_pk_mad_u16 v54, v2, s5, v54\n"
    "v_pk_mad_u16 v55, v3, s5, v55\n"
    "v_lshl_or_b32 v0, v1, 2, v0\n"
    "v_lshl_or_b32 v0, v2, 4, v0\n"
    "v_lshl_or_b32 v0, v3, 6, v0\n"
    "v_or3_b32 v5, v5, v52, v53\n"
    "v_lshrrev_b32_e64 v1, 15, v0\n"
    "v_or3_b32 v5, v5, v54, v55\n"
    "v_bitop3_b32 v17, v1, v6, v0 bitop3:0xc8\n"
    "ds_write_b8 v7, v17 offset:2560\n"
    "ds_read_b128 v[128:131], v97 offset:4352\n"
    "ds_read_b128 v[132:135], v97 offset:4368\n"
    "ds_read_b128 v[144:147], v97 offset:8704\n"
    "ds_read_b128 v[148:151], v97 offset:8720\n"
    "ds_read_b128 v[160:163], v97 offset:13056\n"
    "ds_read_b128 v[164:167], v97 offset:13072\n"
    "s_waitcnt lgkmcnt(4)\n"
    "v_mfma_f32_16x16x32_bf16 v[116:119], v[8:11], v[128:131], v[28:31]\n"
    "v_mfma_f32_16x16x32_bf16 v[116:119], v[12:15], v[132:135], v[116:119]\n"
    "s_waitcnt lgkmcnt(2)\n"
    "v_mfma_f32_16x16x32_bf16 v[120:123], v[8:11], v[144:147], v[28:31]\n"
    "v_mfma_f32_16x16x32_bf16 v[120:123], v[12:15], v[148:151], v[120:123]\n"
    "s_waitcnt lgkmcnt(0)\n"
    "v_mfma_f32_16x16x32_bf16 v[124:127], v[8:11], v[160:163], v[28:31]\n"
    "v_mfma_f32_16x16x32_bf16 v[124:127], v[12:15], v[164:167], v[124:127]\n"
    "s_nop 7\n"
    "ds_write_b128 v100, v[116:119] offset:21760\n"
    "ds_write_b128 v100, v[120:123] offset:26112\n"
    "ds_write_b128 v100, v[124:127] offset:30464\n"
    "v_cmp_ne_u32 vcc, 0, v5\n"
    "s_cmp_lg_u64 vcc, 0\n"
    "s_cselect_b32 s94, 0x101, 1\n"
    "s_mov_b64 exec, 1\n"
    "v_mov_b32 v10, s94\n"
    "ds_add_rtn_u32 v10, v250, v10 offset:35840\n"
    "s_mov_b64 exec, -1\n"
    "s_waitcnt lgkmcnt(0)\n"
    "v_readfirstlane_b32 s95, v10\n"
    "s_add_u32 s96, s95, s94\n"
    "s_bitcmp1_b32 s96, 2\n"
    "s_cbranch_scc1 .Ldin_sched\n"
    ".Ldin_ygate:\n"
    ".Ldin_yentry:\n"
    "s_mov_b64 vcc, -1\n"
    "s_branch .Ldin_entry\n"
    ".Ldin_swait:\n"
    "s_sleep 1\n"
    "ds_read_b32 v10, v250 offset:35840\n"
    "s_waitcnt lgkmcnt(0)\n"
    "v_readfirstlane_b32 s96, v10\n"
    ".Ldin_sched:\n"
    "s_setprio 1\n"
    "v_lshrrev_b32 v2, 3, v251\n"
    "v_and_b32 v3, 7, v251\n"
    "v_lshlrev_b32 v6, 3, v3\n"
    "v_add_u32 v7, 16, v251\n"
    "s_mov_b32 s84, 0x10b80\n"
    "v_lshl_add_u32 v12, v7, 6, s84\n"
    "ds_read_b128 v[32:35], v12\n"
    "ds_read_b128 v[36:39], v12 offset:16\n"
    "s_mov_b32 s85, 0x10a80\n"
    "v_lshl_add_u32 v4, v2, 2, s85\n"
    "v_lshl_add_u32 v5, v3, 2, s85\n"
    "ds_read_u16 v17, v4 offset:2\n"
    "ds_read_u16 v4, v4\n"
    "ds_read_u16 v16, v5 offset:2\n"
    "ds_read_u16 v5, v5\n"
    "s_sub_u32 s85, s61, s3\n"
    "s_lshl1_add_u32 s85, s15, s85\n"
    "v_mul_lo_u32 v8, v2, s15\n"
    "v_add3_u32 v8, v8, v3, s85\n"
    "s_cmp_ge_u32 s96, 256\n"
    "v_cmp_gt_u32_e64 s[86:87], s14, v8\n"
    "s_cselect_b32 s85, 2.0, 0\n"
    "v_lshlrev_b32 v8, 6, v8\n"
    "s_and_b32 s87, s87, 0xffff\n"
    "v_or3_b32 v8, v8, v7, s85\n"
    "v_mov_b32 v12, s57\n"
    "v_cndmask_b32_e64 v8, v12, v8, s[86:87]\n"
    "v_cmp_eq_u32 vcc, s3, v2\n"
    "v_lshrrev_b32 v14, 2, v2\n"
    "v_mul_u32_u24 v14, 2, v14\n"
    "v_addc_co_u32_e64 v14, s[88:89], v14, 0, vcc\n"
    "v_addc_co_u32_e64 v14, s[88:89], v14, 0, vcc\n"
    "v_cmp_eq_u32 vcc, s3, v3\n"
    "v_lshrrev_b32 v15, 2, v3\n"
    "v_mul_u32_u24 v15, 2, v15\n"
    "v_addc_co_u32_e64 v15, s[88:89], v15, 0, vcc\n"
    "v_addc_co_u32_e64 v15, s[88:89], v15, 0, vcc\n"
    "s_waitcnt lgkmcnt(4)\n"
    "v_bcnt_u32_b32 v9, v32, 15\n"
    "v_bcnt_u32_b32 v9, v33, v9\n"
    "v_bcnt_u32_b32 v9, v34, v9\n"
    "v_bcnt_u32_b32 v9, v35, v9\n"
    "v_bcnt_u32_b32 v9, v36, v9\n"
    "v_bcnt_u32_b32 v9, v37, v9\n"
    "v_bcnt_u32_b32 v9, v38, v9\n"
    "v_bcnt_u32_b32 v9, v39, v9\n"
    "v_lshrrev_b32 v9, 4, v9\n"
    "v_max_u32 v9, 6, v9\n"
    "v_lshlrev_b32 v9, 3, v9\n"
    "v_cndmask_b32_e64 v9, 0, v9, s[86:87]\n"
    "v_sub_u32 v10, v3, v9\n"
    "s_waitcnt lgkmcnt(0)\n"
    "v_add_u32 v16, 15, v16\n"
    "v_lshrrev_b32 v16, 4, v16\n"
    "v_max_u32 v16, 6, v16\n"
    "v_add_u32 v17, 15, v17\n"
    "v_lshrrev_b32 v17, 4, v17\n"
    "v_max_u32 v17, 6, v17\n"
    "v_add3_u32 v4, v4, v14, v17\n"
    "v_add3_u32 v5, v5, v15, v16\n"
    "v_mov_b32_dpp v12, v10 row_half_mirror row_mask:0xf bank_mask:0xf\n"
    "v_lshrrev_b32 v14, 2, v2\n"
    "v_mul_u32_u24 v14, 3, v14\n"
    "v_xor_b32 v14, v14, v2\n"
    "v_lshl_or_b32 v4, v4, 3, v14\n"
    "v_lshrrev_b32_e64 v14, 2, v3\n"
    "v_mul_u32_u24 v14, 3, v14\n"
    "v_xor_b32 v14, v14, v3\n"
    "v_lshl_or_b32 v5, v5, 3, v14\n"
    "v_sub_u32_dpp v32, v10, v10 quad_perm:[1,0,3,2] row_mask:0xf bank_mask:0xf\n"
    "v_sub_u32_dpp v33, v10, v10 quad_perm:[2,3,0,1] row_mask:0xf bank_mask:0xf\n"
    "v_sub_u32_dpp v34, v10, v10 quad_perm:[3,2,1,0] row_mask:0xf bank_mask:0xf\n"
    "v_sub_u32_dpp v35, v12, v10 quad_perm:[0,1,2,3] row_mask:0xf bank_mask:0xf\n"
    "v_sub_u32_dpp v36, v12, v10 quad_perm:[1,0,3,2] row_mask:0xf bank_mask:0xf\n"
    "v_sub_u32_dpp v37, v12, v10 quad_perm:[2,3,0,1] row_mask:0xf bank_mask:0xf\n"
    "v_sub_u32_dpp v38, v12, v10 quad_perm:[3,2,1,0] row_mask:0xf bank_mask:0xf\n"
    "v_lshrrev_b32 v32, 31, v32\n"
    "v_lshrrev_b32 v33, 31, v33\n"
    "v_lshrrev_b32 v34, 31, v34\n"
    "v_lshrrev_b32 v35, 31, v35\n"
    "v_lshrrev_b32 v36, 31, v36\n"
    "v_lshrrev_b32 v37, 31, v37\n"
    "v_lshrrev_b32 v38, 31, v38\n"
    "v_add3_u32 v11, v32, v33, v244\n"
    "v_add3_u32 v11, v34, v35, v11\n"
    "v_add3_u32 v11, v36, v37, v11\n"
    "v_add_u32 v11, v38, v11\n"
    "v_add_u32 v12, s65, v11\n"
    "v_lshl_add_u32 v14, v11, 3, s65\n"
    "ds_write_b8 v12, v9\n"
    "ds_write_b32 v14, v8 offset:64\n"
    "v_mov_b32 v12, s65\n"
    "ds_read_b128 v[40:43], v12 offset:0\n"
    "ds_read_b128 v[44:47], v12 offset:16\n"
    "ds_read_b128 v[48:51], v12 offset:32\n"
    "v_lshlrev_b32 v16, 3, v16\n"
    "v_sub_u32 v72, v5, v16\n"
    "v_cmp_lt_i32 vcc, v5, v4\n"
    "v_mov_b32_e64 v73, v5\n"
    "s_waitcnt lgkmcnt(0)\n"
    "v_lshrrev_b64 v[20:21], v6, vcc\n"
    "v_lshrrev_b64 v[18:19], v244, vcc\n"
    "v_and_b32 v20, 0xff, v20\n"
    "v_and_b32 v18, 0xff, v18\n"
    "v_bcnt_u32_b32 v20, v20, 0\n"
    "v_bcnt_u32_b32 v18, v18, 0\n"
    "v_lshl_add_u32 v20, v20, 3, s65\n"
    "v_lshlrev_b32_e64 v18, 3, v18\n"
    "ds_read_b32 v56, v20 offset:64\n"
    "v_lshrrev_b64 v[22:23], v20, v[40:41]\n"
    "v_lshrrev_b64 v[18:19], v18, v[40:41]\n"
    "v_and_b32 v22, 0xff, v22\n"
    "v_and_b32 v18, 0xff, v18\n"
    "v_add_u32 v5, v5, v22\n"
    "v_add_u32 v4, v4, v18\n"
    "v_cmp_lt_i32 vcc, v5, v4\n"
    "v_mov_b32_e64 v74, v5\n"
    "s_nop 0\n"
    "v_lshrrev_b64 v[20:21], v6, vcc\n"
    "v_lshrrev_b64 v[18:19], v244, vcc\n"
    "v_and_b32 v20, 0xff, v20\n"
    "v_and_b32 v18, 0xff, v18\n"
    "v_bcnt_u32_b32 v20, v20, 0\n"
    "v_bcnt_u32_b32 v18, v18, 0\n"
    "v_lshl_add_u32 v20, v20, 3, s65\n"
    "v_lshlrev_b32_e64 v18, 3, v18\n"
    "ds_read_b32 v58, v20 offset:128\n"
    "v_lshrrev_b64 v[22:23], v20, v[42:43]\n"
    "v_lshrrev_b64 v[18:19], v18, v[42:43]\n"
    "v_and_b32 v22, 0xff, v22\n"
    "v_and_b32 v18, 0xff, v18\n"
    "v_add_u32 v5, v5, v22\n"
    "v_add_u32 v4, v4, v18\n"
    "v_cmp_lt_i32 vcc, v5, v4\n"
    "v_mov_b32_e64 v75, v5\n"
    "s_nop 0\n"
    "v_lshrrev_b64 v[20:21], v6, vcc\n"
    "v_lshrrev_b64 v[18:19], v244, vcc\n"
    "v_and_b32 v20, 0xff, v20\n"
    "v_and_b32 v18, 0xff, v18\n"
    "v_bcnt_u32_b32 v20, v20, 0\n"
    "v_bcnt_u32_b32 v18, v18, 0\n"
    "v_lshl_add_u32 v20, v20, 3, s65\n"
    "v_lshlrev_b32_e64 v18, 3, v18\n"
    "ds_read_b32 v60, v20 offset:192\n"
    "v_lshrrev_b64 v[22:23], v20, v[44:45]\n"
    "v_lshrrev_b64 v[18:19], v18, v[44:45]\n"
    "v_and_b32 v22, 0xff, v22\n"
    "v_and_b32 v18, 0xff, v18\n"
    "v_add_u32 v5, v5, v22\n"
    "v_add_u32 v4, v4, v18\n"
    "v_cmp_lt_i32 vcc, v5, v4\n"
    "v_mov_b32_e64 v76, v5\n"
    "s_nop 0\n"
    "v_lshrrev_b64 v[20:21], v6, vcc\n"
    "v_lshrrev_b64 v[18:19], v244, vcc\n"
    "v_and_b32 v20, 0xff, v20\n"
    "v_and_b32 v18, 0xff, v18\n"
    "v_bcnt_u32_b32 v20, v20, 0\n"
    "v_bcnt_u32_b32 v18, v18, 0\n"
    "v_lshl_add_u32 v20, v20, 3, s65\n"
    "v_lshlrev_b32_e64 v18, 3, v18\n"
    "ds_read_b32 v62, v20 offset:256\n"
    "v_lshrrev_b64 v[22:23], v20, v[46:47]\n"
    "v_lshrrev_b64 v[18:19], v18, v[46:47]\n"
    "v_and_b32 v22, 0xff, v22\n"
    "v_and_b32 v18, 0xff, v18\n"
    "v_add_u32 v5, v5, v22\n"
    "v_add_u32 v4, v4, v18\n"
    "v_cmp_lt_i32 vcc, v5, v4\n"
    "v_mov_b32_e64 v77, v5\n"
    "s_nop 0\n"
    "v_lshrrev_b64 v[20:21], v6, vcc\n"
    "v_lshrrev_b64 v[18:19], v244, vcc\n"
    "v_and_b32 v20, 0xff, v20\n"
    "v_and_b32 v18, 0xff, v18\n"
    "v_bcnt_u32_b32 v20, v20, 0\n"
    "v_bcnt_u32_b32 v18, v18, 0\n"
    "v_lshl_add_u32 v20, v20, 3, s65\n"
    "v_lshlrev_b32_e64 v18, 3, v18\n"
    "ds_read_b32 v64, v20 offset:320\n"
    "v_lshrrev_b64 v[22:23], v20, v[48:49]\n"
    "v_lshrrev_b64 v[18:19], v18, v[48:49]\n"
    "v_and_b32 v22, 0xff, v22\n"
    "v_and_b32 v18, 0xff, v18\n"
    "v_add_u32 v5, v5, v22\n"
    "v_add_u32 v4, v4, v18\n"
    "v_cmp_lt_i32 vcc, v5, v4\n"
    "v_mov_b32_e64 v78, v5\n"
    "s_nop 0\n"
    "v_lshrrev_b64 v[20:21], v6, vcc\n"
    "v_and_b32 v20, 0xff, v20\n"
    "v_bcnt_u32_b32 v20, v20, 0\n"
    "v_lshl_add_u32 v20, v20, 3, s65\n"
    "ds_read_b32 v66, v20 offset:384\n"
    "v_lshrrev_b64 v[22:23], v20, v[50:51]\n"
    "v_and_b32 v22, 0xff, v22\n"
    "v_add_u32 v5, v5, v22\n"
    "s_add_u32 s85, s10, 240\n"
    "s_lshl_b32 s86, s72, 4\n"
    "v_add_u32_dpp v12, v5, v5 quad_perm:[1,0,3,2] row_mask:0xf bank_mask:0xf\n"
    "s_add_u32 s85, s85, s86\n"
    "s_nop 0\n"
    "v_add_u32_dpp v12, v12, v12 quad_perm:[2,3,0,1] row_mask:0xf bank_mask:0xf\n"
    "s_nop 1\n"
    "v_add_u32_dpp v12, v12, v12 row_half_mirror row_mask:0xf bank_mask:0xf\n"
    "v_add_u32 v12, 158, v12\n"
    "v_and_b32 v12, -256, v12\n"
    "v_lshrrev_b32 v12, 3, v12\n"
    "v_sub_u32 v12, v12, v5\n"
    "v_add_u32 v72, v72, v12\n"
    "v_mul_u32_u24 v72, s72, v72\n"
    "v_lshrrev_b32 v72, 3, v72\n"
    "s_add_u32 s85, s85, s73\n"
    "v_add_u32 v57, s85, v72\n"
    "v_add_u32 v73, v73, v12\n"
    "v_mul_u32_u24 v73, s72, v73\n"
    "v_lshrrev_b32 v73, 3, v73\n"
    "s_add_u32 s85, s85, s73\n"
    "v_add_u32 v59, s85, v73\n"
    "v_add_u32 v74, v74, v12\n"
    "v_mul_u32_u24 v74, s72, v74\n"
    "v_lshrrev_b32 v74, 3, v74\n"
    "s_add_u32 s85, s85, s73\n"
    "v_add_u32 v61, s85, v74\n"
    "v_add_u32 v75, v75, v12\n"
    "v_mul_u32_u24 v75, s72, v75\n"
    "v_lshrrev_b32 v75, 3, v75\n"
    "s_add_u32 s85, s85, s73\n"
    "v_add_u32 v63, s85, v75\n"
    "v_add_u32 v76, v76, v12\n"
    "v_mul_u32_u24 v76, s72, v76\n"
    "v_lshrrev_b32 v76, 3, v76\n"
    "s_add_u32 s85, s85, s73\n"
    "v_add_u32 v65, s85, v76\n"
    "v_add_u32 v77, v77, v12\n"
    "v_mul_u32_u24 v77, s72, v77\n"
    "v_lshrrev_b32 v77, 3, v77\n"
    "s_add_u32 s85, s85, s73\n"
    "v_add_u32 v67, s85, v77\n"
    "v_lshl_add_u32 v12, v3, 7, s84\n"
    "s_mov_b64 exec, 0xff\n"
    "s_waitcnt lgkmcnt(0)\n"
    "ds_write_b128 v12, v[56:59] offset:48\n"
    "ds_write_b128 v12, v[60:63] offset:64\n"
    "ds_write_b128 v12, v[64:67] offset:80\n"
    "s_mov_b64 exec, -1\n"
    "s_branch .Ldin_ygate\n"
    ".p2align 5\n"
    ".Ldin_L2:\n"
    "s_setprio 0\n"
    "s_add_u32 s76, s76, s73\n"
    "v_mov_b32_e64 v253, s63\n"
    "ds_read_b64 v[232:233], v253\n"
    "v_mfma_f32_4x4x4_16b_bf16 v[144:147], v[184:185], v[32:33], v[64:67] cbsz:2 abid:0\n"
    "ds_read_b128 v[112:115], v252 offset:17408\n"
    "v_mfma_f32_4x4x4_16b_bf16 v[148:151], v[184:185], v[34:35], v[68:71] cbsz:2 abid:1\n"
    "ds_read_b128 v[116:119], v252 offset:17424\n"
    "v_mfma_f32_4x4x4_16b_bf16 v[152:155], v[184:185], v[36:37], v[72:75] cbsz:2 abid:2\n"
    "ds_read_b128 v[120:123], v252 offset:17536\n"
    "ds_read_b128 v[124:127], v252 offset:17552\n"
    "v_cvt_pk_bf16_f32 v0, v144, v145\n"
    "v_cvt_pk_bf16_f32 v1, v146, v147\n"
    "v_mfma_f32_4x4x4_16b_bf16 v[156:159], v[184:185], v[38:39], v[76:79] cbsz:2 abid:3\n"
    "v_cvt_pk_bf16_f32 v2, v148, v149\n"
    "v_cvt_pk_bf16_f32 v3, v150, v151\n"
    "v_mfma_f32_4x4x4_16b_bf16 v[144:147], v[184:185], v[40:41], v[80:83] cbsz:2 abid:0\n"
    "v_cvt_pk_bf16_f32 v4, v152, v153\n"
    "v_cvt_pk_bf16_f32 v5, v154, v155\n"
    "v_mfma_f32_4x4x4_16b_bf16 v[148:151], v[184:185], v[42:43], v[84:87] cbsz:2 abid:1\n"
    "v_cvt_pk_bf16_f32 v6, v156, v157\n"
    "v_cvt_pk_bf16_f32 v7, v158, v159\n"
    "v_mfma_f32_4x4x4_16b_bf16 v[152:155], v[184:185], v[44:45], v[88:91] cbsz:2 abid:2\n"
    "v_cvt_pk_bf16_f32 v8, v144, v145\n"
    "v_cvt_pk_bf16_f32 v9, v146, v147\n"
    "v_mfma_f32_4x4x4_16b_bf16 v[156:159], v[184:185], v[46:47], v[92:95] cbsz:2 abid:3\n"
    "v_cvt_pk_bf16_f32 v10, v148, v149\n"
    "v_cvt_pk_bf16_f32 v11, v150, v151\n"
    "v_mfma_f32_4x4x4_16b_bf16 v[144:147], v[184:185], v[48:49], v[192:195] cbsz:2 abid:0\n"
    "v_cvt_pk_bf16_f32 v12, v152, v153\n"
    "v_cvt_pk_bf16_f32 v13, v154, v155\n"
    "v_mfma_f32_4x4x4_16b_bf16 v[148:151], v[184:185], v[50:51], v[196:199] cbsz:2 abid:1\n"
    "v_cvt_pk_bf16_f32 v14, v156, v157\n"
    "v_cvt_pk_bf16_f32 v15, v158, v159\n"
    "v_mfma_f32_4x4x4_16b_bf16 v[152:155], v[184:185], v[52:53], v[200:203] cbsz:2 abid:2\n"
    "s_waitcnt lgkmcnt(0)\n"
    "v_readfirstlane_b32 s60, v232\n"
    "v_cvt_pk_bf16_f32 v16, v144, v145\n"
    "v_cvt_pk_bf16_f32 v17, v146, v147\n"
    "v_mfma_f32_4x4x4_16b_bf16 v[156:159], v[184:185], v[54:55], v[204:207] cbsz:2 abid:3\n"
    "s_cmp_eq_u32 s60, -1\n"
    "s_cbranch_scc1 .Ldin_L2_poll\n"
    ".Ldin_L2_pr:\n"
    "v_readfirstlane_b32 s95, v233\n"
    "s_and_b32 s94, s60, 63\n"
    "s_lshl_b32 s94, s94, 6\n"
    "s_add_u32 s94, s94, 68480\n"
    "s_add_u32 s63, s63, 8\n"
    "v_lshl_add_u32 v252, v249, 1, s94\n"
    "ds_read_b32 v252, v252\n"
    "s_sub_u32 s95, s95, s76\n"
    "s_ashr_i32 s95, s95, 4\n"
    "s_min_i32 s95, s95, s74\n"
    "s_max_i32 s72, s95, s75\n"
    "v_cvt_pk_bf16_f32 v18, v148, v149\n"
    "v_cvt_pk_bf16_f32 v19, v150, v151\n"
    "v_mfma_f32_4x4x4_16b_bf16 v[144:147], v[184:185], v[56:57], v[208:211] cbsz:2 abid:0\n"
    "v_cvt_pk_bf16_f32 v20, v152, v153\n"
    "v_cvt_pk_bf16_f32 v21, v154, v155\n"
    "v_mfma_f32_4x4x4_16b_bf16 v[148:151], v[184:185], v[58:59], v[212:215] cbsz:2 abid:1\n"
    "v_cvt_pk_bf16_f32 v22, v156, v157\n"
    "v_cvt_pk_bf16_f32 v23, v158, v159\n"
    "v_mfma_f32_4x4x4_16b_bf16 v[152:155], v[184:185], v[60:61], v[216:219] cbsz:2 abid:2\n"
    "v_cvt_pk_bf16_f32 v24, v144, v145\n"
    "v_cvt_pk_bf16_f32 v25, v146, v147\n"
    "v_mfma_f32_4x4x4_16b_bf16 v[156:159], v[184:185], v[62:63], v[220:223] cbsz:2 abid:3\n"
    "v_cvt_pk_bf16_f32 v26, v148, v149\n"
    "v_cvt_pk_bf16_f32 v27, v150, v151\n"
    "v_cvt_pk_bf16_f32 v28, v152, v153\n"
    "v_cvt_pk_bf16_f32 v29, v154, v155\n"
    "v_exp_f32 v136, v136 clamp\n"
    "v_cvt_pk_bf16_f32 v30, v156, v157\n"
    "v_cvt_pk_bf16_f32 v31, v158, v159\n"
    "v_mfma_f32_16x16x32_bf16 v[144:147], v[0:3], v[160:163], v[112:115]\n"
    "v_exp_f32 v128, v128 clamp\n"
    "v_mfma_f32_16x16x32_bf16 v[144:147], v[4:7], v[164:167], v[144:147]\n"
    "v_exp_f32 v129, v129 clamp\n"
    "v_mfma_f32_16x16x32_bf16 v[148:151], v[8:11], v[160:163], v[116:119]\n"
    "v_exp_f32 v130, v130 clamp\n"
    "v_mfma_f32_16x16x32_bf16 v[148:151], v[12:15], v[164:167], v[148:151]\n"
    "v_exp_f32 v131, v131 clamp\n"
    "v_mfma_f32_16x16x32_bf16 v[152:155], v[16:19], v[160:163], v[120:123]\n"
    "v_exp_f32 v132, v132 clamp\n"
    "v_mfma_f32_16x16x32_bf16 v[152:155], v[20:23], v[164:167], v[152:155]\n"
    "v_exp_f32 v133, v133 clamp\n"
    "v_mfma_f32_16x16x32_bf16 v[156:159], v[24:27], v[160:163], v[124:127]\n"
    "v_exp_f32 v134, v134 clamp\n"
    "v_mfma_f32_16x16x32_bf16 v[156:159], v[28:31], v[164:167], v[156:159]\n"
    "v_exp_f32 v135, v135 clamp\n"
    "s_add_u32 m0, s68, 6144\n"
    "v_exp_f32 v137, v137 clamp\n"
    "v_add_u32 v253, s67, v244\n"
    "v_mfma_f32_4x4x4_16b_bf16 v[224:227], v[228:229], v[176:177], v[224:227] cbsz:4 abid:0\n"
    "v_exp_f32 v138, v138 clamp\n"
    "v_exp_f32 v139, v139 clamp\n"
    "v_mfma_f32_4x4x4_16b_bf16 v[224:227], v[228:229], v[178:179], v[224:227] cbsz:4 abid:1\n"
    "v_exp_f32 v140, v140 clamp\n"
    "v_exp_f32 v141, v141 clamp\n"
    "v_mfma_f32_4x4x4_16b_bf16 v[224:227], v[228:229], v[180:181], v[224:227] cbsz:4 abid:2\n"
    "v_exp_f32 v142, v142 clamp\n"
    "v_exp_f32 v143, v143 clamp\n"
    "v_mfma_f32_4x4x4_16b_bf16 v[224:227], v[228:229], v[182:183], v[224:227] cbsz:4 abid:3\n"
    "s_waitcnt vmcnt(4)\n"
    "ds_read_b64_tr_b16 v[176:177], v240 offset:6144\n"
    "ds_read_b64_tr_b16 v[178:179], v241 offset:6400\n"
    "ds_read_b64_tr_b16 v[180:181], v242 offset:6656\n"
    "ds_read_b64_tr_b16 v[182:183], v243 offset:6912\n"
    "ds_read_b32 v236, v253 offset:0\n"
    "ds_read_b32 v238, v253 offset:4\n"
    "ds_read_b128 v[160:163], v234 offset:0\n"
    "ds_read_b128 v[164:167], v235 offset:0\n"
    "s_waitcnt lgkmcnt(8)\n"
    "s_nop 0\n"
    "v_readlane_b32 s20, v252, 0\n"
    "v_readlane_b32 s21, v252, 1\n"
    "v_readlane_b32 s22, v252, 2\n"
    "v_readlane_b32 s23, v252, 3\n"
    "v_readlane_b32 s24, v252, 4\n"
    "v_readlane_b32 s25, v252, 5\n"
    "v_readlane_b32 s26, v252, 6\n"
    "v_readlane_b32 s27, v252, 7\n"
    "v_pk_fma_f32 v[128:129], v[128:129], s[70:71], v[96:97]\n"
    "v_pk_fma_f32 v[130:131], v[130:131], s[70:71], v[98:99]\n"
    "v_pk_add_f32 v[230:231], v[128:129], v[130:131]\n"
    "v_pk_mul_f32 v[232:233], v[128:129], v[130:131]\n"
    "v_pk_fma_f32 v[132:133], v[132:133], s[70:71], v[100:101]\n"
    "v_pk_fma_f32 v[230:231], v[230:231], v[132:133], v[232:233]\n"
    "v_pk_mul_f32 v[232:233], v[232:233], v[132:133]\n"
    "v_pk_fma_f32 v[134:135], v[134:135], s[70:71], v[102:103]\n"
    "v_pk_fma_f32 v[230:231], v[230:231], v[134:135], v[232:233]\n"
    "v_pk_mul_f32 v[232:233], v[232:233], v[134:135]\n"
    "v_pk_fma_f32 v[136:137], v[136:137], s[70:71], v[104:105]\n"
    "v_pk_fma_f32 v[230:231], v[230:231], v[136:137], v[232:233]\n"
    "v_pk_mul_f32 v[232:233], v[232:233], v[136:137]\n"
    "v_pk_fma_f32 v[138:139], v[138:139], s[70:71], v[106:107]\n"
    "v_pk_fma_f32 v[230:231], v[230:231], v[138:139], v[232:233]\n"
    "v_pk_mul_f32 v[232:233], v[232:233], v[138:139]\n"
    "v_pk_fma_f32 v[140:141], v[140:141], s[70:71], v[108:109]\n"
    "v_pk_fma_f32 v[230:231], v[230:231], v[140:141], v[232:233]\n"
    "v_pk_mul_f32 v[232:233], v[232:233], v[140:141]\n"
    "v_pk_fma_f32 v[142:143], v[142:143], s[70:71], v[110:111]\n"
    "v_pk_fma_f32 v[230:231], v[230:231], v[142:143], v[232:233]\n"
    "v_pk_mul_f32 v[232:233], v[232:233], v[142:143]\n"
    "v_rcp_f32 v232, v232\n"
    "v_rcp_f32 v233, v233\n"
    "v_fma_f32 v230, v230, v232, v247\n"
    "v_fmac_f32 v230, v231, v233\n"
    "v_cvt_pk_bf16_f32 v230, v230, v230\n"
    "s_add_u32 s67, s67, 64\n"
    "s_waitcnt lgkmcnt(0)\n"
    "ds_write_b16 v245, v230\n"
    "ds_read_b64 v[228:229], v246\n"
    "s_lshl1_add_u32 s76, s72, s76\n"
    "buffer_load_dwordx4 v[236:237], s[40:43], 0 idxen offen offset:0 nt lds\n"
    "buffer_load_dwordx4 v[238:239], s[40:43], 0 idxen offen offset:1024 nt lds\n"
    "s_and_b32 s62, s58, 63\n"
    "s_mul_i32 s62, s62, 272\n"
    "s_mov_b32 s58, s59\n"
    "s_mov_b32 s59, s60\n"
    "s_mov_b32 s81, s65\n"
    "s_xor_b32 s65, s65, s64\n"
    "s_mov_b32 s80, s83\n"
    "s_add_u32 s82, s65, 256\n"
    "s_cmp_lt_i32 s59, s57\n"
    "s_cselect_b32 s76, s76, -1\n"
    "s_bitcmp1_b32 s58, 30\n"
    "s_cbranch_scc1 .Ldin_A3s\n"
    ".Ldin_A3:\n"
    "v_mfma_f32_16x16x32_bf16 v[128:131], v[0:3], v[160:163], v[112:115]\n"
    "v_exp_f32 v144, v144 clamp\n"
    "v_mfma_f32_16x16x32_bf16 v[128:131], v[4:7], v[164:167], v[128:131]\n"
    "v_exp_f32 v145, v145 clamp\n"
    "v_mfma_f32_16x16x32_bf16 v[132:135], v[8:11], v[160:163], v[116:119]\n"
    "v_exp_f32 v146, v146 clamp\n"
    "v_mfma_f32_16x16x32_bf16 v[132:135], v[12:15], v[164:167], v[132:135]\n"
    "v_exp_f32 v147, v147 clamp\n"
    "v_mfma_f32_16x16x32_bf16 v[136:139], v[16:19], v[160:163], v[120:123]\n"
    "v_exp_f32 v148, v148 clamp\n"
    "v_mfma_f32_16x16x32_bf16 v[136:139], v[20:23], v[164:167], v[136:139]\n"
    "v_exp_f32 v149, v149 clamp\n"
    "v_mfma_f32_16x16x32_bf16 v[140:143], v[24:27], v[160:163], v[124:127]\n"
    "v_exp_f32 v150, v150 clamp\n"
    "v_mfma_f32_16x16x32_bf16 v[140:143], v[28:31], v[164:167], v[140:143]\n"
    "v_exp_f32 v151, v151 clamp\n"
    "v_exp_f32 v152, v152 clamp\n"
    "s_mov_b32 m0, s62\n"
    "v_exp_f32 v153, v153 clamp\n"
    "v_add_u32 v253, s67, v244\n"
    "s_waitcnt lgkmcnt(0)\n"
    "v_mfma_f32_4x4x4_16b_bf16 v[224:227], v[228:229], v[168:169], v[224:227] cbsz:4 abid:0\n"
    "v_exp_f32 v154, v154 clamp\n"
    "v_exp_f32 v155, v155 clamp\n"
    "v_mfma_f32_4x4x4_16b_bf16 v[224:227], v[228:229], v[170:171], v[224:227] cbsz:4 abid:1\n"
    "v_exp_f32 v156, v156 clamp\n"
    "v_exp_f32 v157, v157 clamp\n"
    "v_mfma_f32_4x4x4_16b_bf16 v[224:227], v[228:229], v[172:173], v[224:227] cbsz:4 abid:2\n"
    "v_exp_f32 v158, v158 clamp\n"
    "v_exp_f32 v159, v159 clamp\n"
    "v_mfma_f32_4x4x4_16b_bf16 v[224:227], v[228:229], v[174:175], v[224:227] cbsz:4 abid:3\n"
    "s_waitcnt vmcnt(4)\n"
    "ds_read_b64_tr_b16 v[168:169], v240 offset:0\n"
    "ds_read_b64_tr_b16 v[170:171], v241 offset:256\n"
    "ds_read_b64_tr_b16 v[172:173], v242 offset:512\n"
    "ds_read_b64_tr_b16 v[174:175], v243 offset:768\n"
    "ds_read_b32 v236, v253 offset:0\n"
    "ds_read_b32 v238, v253 offset:4\n"
    "ds_read_b128 v[160:163], v234 offset:2048\n"
    "ds_read_b128 v[164:167], v235 offset:2048\n"
    "s_cbranch_vccz .Ldin_A3_np\n"
    "s_setprio 1\n"
    ".Ldin_A3_np:\n"
    "v_pk_fma_f32 v[144:145], v[144:145], s[70:71], v[96:97]\n"
    "v_pk_fma_f32 v[146:147], v[146:147], s[70:71], v[98:99]\n"
    "v_pk_add_f32 v[190:191], v[224:225], v[226:227]\n"
    "v_add_f32 v190, v190, v191\n"
    "ds_write_addtid_b32 v190 offset:32768\n"
    "s_add_u32 m0, s68, 0\n"
    "s_cmp_ge_i32 s59, s57\n"
    "s_cbranch_scc1 .Ldin_A3_nl\n"
    "s_bfe_u32 s94, s59, 0x180006\n"
    "s_mul_i32 s86, s94, s56\n"
    "v_mbcnt_lo_u32_b32 v184, s20, 0\n"
    "v_mbcnt_lo_u32_b32 v185, s22, 0\n"
    "v_mbcnt_lo_u32_b32 v186, s24, 0\n"
    "v_mbcnt_lo_u32_b32 v187, s26, 0\n"
    "s_bcnt1_i32_b64 s90, s[20:21]\n"
    "s_bcnt1_i32_b64 s91, s[22:23]\n"
    "s_bcnt1_i32_b64 s92, s[24:25]\n"
    "s_bcnt1_i32_b64 s93, s[26:27]\n"
    "v_mbcnt_hi_u32_b32 v184, s21, v184\n"
    "v_mbcnt_hi_u32_b32 v185, s23, v185\n"
    "v_mbcnt_hi_u32_b32 v186, s25, v186\n"
    "v_mbcnt_hi_u32_b32 v187, s27, v187\n"
    "s_lshl2_add_u32 s90, s90, s65\n"
    "s_lshl2_add_u32 s91, s91, s90\n"
    "s_lshl2_add_u32 s92, s92, s91\n"
    "s_lshl2_add_u32 s93, s93, s92\n"
    "v_add_u32 v188, s86, v251\n"
    "v_lshl_add_u32 v184, v184, 2, s65\n"
    "v_lshl_add_u32 v185, v185, 2, s90\n"
    "v_lshl_add_u32 v186, v186, 2, s91\n"
    "v_lshl_add_u32 v187, v187, 2, s92\n"
    "v_mov_b32 v191, -1\n"
    "v_lshl_add_u32 v190, v249, 1, s93\n"
    "ds_write2_b32 v190, v191, v191 offset1:64\n"
    "s_mov_b64 exec, s[20:21]\n"
    "ds_write_b32 v184, v188\n"
    "s_mov_b64 exec, s[22:23]\n"
    "v_add_u32 v189, 64, v188\n"
    "ds_write_b32 v185, v189\n"
    "s_mov_b64 exec, s[24:25]\n"
    "v_add_u32 v190, 128, v188\n"
    "ds_write_b32 v186, v190\n"
    "s_mov_b64 exec, s[26:27]\n"
    "v_add_u32 v189, 192, v188\n"
    "ds_write_b32 v187, v189\n"
    "s_mov_b64 exec, -1\n"
    "s_add_u32 s95, s93, 60\n"
    "s_add_u32 s94, s82, 128\n"
    "s_andn2_b32 s95, s95, 63\n"
    "s_max_u32 s83, s95, s94\n"
    ".Ldin_A3_nl:\n"
    "v_pk_add_f32 v[230:231], v[144:145], v[146:147]\n"
    "v_pk_mul_f32 v[232:233], v[144:145], v[146:147]\n"
    "v_pk_fma_f32 v[148:149], v[148:149], s[70:71], v[100:101]\n"
    "v_pk_fma_f32 v[230:231], v[230:231], v[148:149], v[232:233]\n"
    "v_pk_mul_f32 v[232:233], v[232:233], v[148:149]\n"
    "v_pk_fma_f32 v[150:151], v[150:151], s[70:71], v[102:103]\n"
    "v_pk_fma_f32 v[230:231], v[230:231], v[150:151], v[232:233]\n"
    "v_pk_mul_f32 v[232:233], v[232:233], v[150:151]\n"
    "v_pk_fma_f32 v[152:153], v[152:153], s[70:71], v[104:105]\n"
    "v_pk_fma_f32 v[230:231], v[230:231], v[152:153], v[232:233]\n"
    "v_pk_mul_f32 v[232:233], v[232:233], v[152:153]\n"
    "v_pk_fma_f32 v[154:155], v[154:155], s[70:71], v[106:107]\n"
    "v_pk_fma_f32 v[230:231], v[230:231], v[154:155], v[232:233]\n"
    "v_pk_mul_f32 v[232:233], v[232:233], v[154:155]\n"
    "v_pk_fma_f32 v[156:157], v[156:157], s[70:71], v[108:109]\n"
    "v_pk_fma_f32 v[230:231], v[230:231], v[156:157], v[232:233]\n"
    "v_pk_mul_f32 v[232:233], v[232:233], v[156:157]\n"
    "v_pk_fma_f32 v[158:159], v[158:159], s[70:71], v[110:111]\n"
    "v_pk_fma_f32 v[230:231], v[230:231], v[158:159], v[232:233]\n"
    "v_pk_mul_f32 v[232:233], v[232:233], v[158:159]\n"
    "v_rcp_f32 v232, v232\n"
    "v_rcp_f32 v233, v233\n"
    "v_fma_f32 v230, v230, v232, v247\n"
    "v_fmac_f32 v230, v231, v233\n"
    "v_cvt_pk_bf16_f32 v230, v230, v230\n"
    "s_add_u32 s67, s67, 64\n"
    "s_waitcnt lgkmcnt(0)\n"
    "ds_write_b16 v245, v230\n"
    "ds_read_b64 v[228:229], v246\n"
    "buffer_load_dwordx4 v[236:237], s[40:43], 0 idxen offen offset:0 nt lds\n"
    "buffer_load_dwordx4 v[238:239], s[40:43], 0 idxen offen offset:1024 nt lds\n"
    ".Ldin_B0:\n"
    "v_mfma_f32_16x16x32_bf16 v[144:147], v[0:3], v[160:163], v[112:115]\n"
    "v_exp_f32 v128, v128 clamp\n"
    "v_mfma_f32_16x16x32_bf16 v[144:147], v[4:7], v[164:167], v[144:147]\n"
    "v_exp_f32 v129, v129 clamp\n"
    "v_mfma_f32_16x16x32_bf16 v[148:151], v[8:11], v[160:163], v[116:119]\n"
    "v_exp_f32 v130, v130 clamp\n"
    "v_mfma_f32_16x16x32_bf16 v[148:151], v[12:15], v[164:167], v[148:151]\n"
    "v_exp_f32 v131, v131 clamp\n"
    "v_mfma_f32_16x16x32_bf16 v[152:155], v[16:19], v[160:163], v[120:123]\n"
    "v_exp_f32 v132, v132 clamp\n"
    "v_mfma_f32_16x16x32_bf16 v[152:155], v[20:23], v[164:167], v[152:155]\n"
    "v_exp_f32 v133, v133 clamp\n"
    "v_mfma_f32_16x16x32_bf16 v[156:159], v[24:27], v[160:163], v[124:127]\n"
    "v_exp_f32 v134, v134 clamp\n"
    "v_mfma_f32_16x16x32_bf16 v[156:159], v[28:31], v[164:167], v[156:159]\n"
    "v_exp_f32 v135, v135 clamp\n"
    "v_exp_f32 v136, v136 clamp\n"
    "s_cmp_eq_u32 s67, s80\n"
    "s_cselect_b32 s67, s65, s67\n"
    "s_and_b32 s94, s59, 63\n"
    "s_mul_i32 s94, s94, 272\n"
    "v_add_u32 v252, s94, v250\n"
    "s_add_u32 m0, s68, 2048\n"
    "v_exp_f32 v137, v137 clamp\n"
    "v_add_u32 v253, s67, v244\n"
    "s_waitcnt lgkmcnt(0)\n"
    "v_mfma_f32_4x4x4_16b_bf16 v[224:227], v[228:229], v[176:177], 0 cbsz:4 abid:0\n"
    "v_exp_f32 v138, v138 clamp\n"
    "v_exp_f32 v139, v139 clamp\n"
    "v_mfma_f32_4x4x4_16b_bf16 v[224:227], v[228:229], v[178:179], v[224:227] cbsz:4 abid:1\n"
    "v_exp_f32 v140, v140 clamp\n"
    "v_exp_f32 v141, v141 clamp\n"
    "v_mfma_f32_4x4x4_16b_bf16 v[224:227], v[228:229], v[180:181], v[224:227] cbsz:4 abid:2\n"
    "v_exp_f32 v142, v142 clamp\n"
    "v_exp_f32 v143, v143 clamp\n"
    "v_mfma_f32_4x4x4_16b_bf16 v[224:227], v[228:229], v[182:183], v[224:227] cbsz:4 abid:3\n"
    "s_waitcnt vmcnt(4)\n"
    "s_memrealtime s[34:35]\n"
    "ds_read_b64_tr_b16 v[176:177], v240 offset:2048\n"
    "ds_read_b64_tr_b16 v[178:179], v241 offset:2304\n"
    "ds_read_b64_tr_b16 v[180:181], v242 offset:2560\n"
    "ds_read_b64_tr_b16 v[182:183], v243 offset:2816\n"
    "ds_read_b32 v236, v253 offset:0\n"
    "ds_read_b32 v238, v253 offset:4\n"
    "ds_read_b128 v[160:163], v234 offset:4096\n"
    "ds_read_b128 v[164:167], v235 offset:4096\n"
    "v_add_u32_e64 v184, v248, v252\n"
    "ds_read_b64 v[184:185], v184\n"
    "v_pk_fma_f32 v[128:129], v[128:129], s[70:71], v[96:97]\n"
    "v_pk_fma_f32 v[130:131], v[130:131], s[70:71], v[98:99]\n"
    "v_pk_add_f32 v[230:231], v[128:129], v[130:131]\n"
    "v_pk_mul_f32 v[232:233], v[128:129], v[130:131]\n"
    "v_pk_fma_f32 v[132:133], v[132:133], s[70:71], v[100:101]\n"
    "v_pk_fma_f32 v[230:231], v[230:231], v[132:133], v[232:233]\n"
    "v_pk_mul_f32 v[232:233], v[232:233], v[132:133]\n"
    "v_pk_fma_f32 v[134:135], v[134:135], s[70:71], v[102:103]\n"
    "v_pk_fma_f32 v[230:231], v[230:231], v[134:135], v[232:233]\n"
    "v_pk_mul_f32 v[232:233], v[232:233], v[134:135]\n"
    "v_pk_fma_f32 v[136:137], v[136:137], s[70:71], v[104:105]\n"
    "v_pk_fma_f32 v[230:231], v[230:231], v[136:137], v[232:233]\n"
    "v_pk_mul_f32 v[232:233], v[232:233], v[136:137]\n"
    "v_pk_fma_f32 v[138:139], v[138:139], s[70:71], v[106:107]\n"
    "v_pk_fma_f32 v[230:231], v[230:231], v[138:139], v[232:233]\n"
    "v_pk_mul_f32 v[232:233], v[232:233], v[138:139]\n"
    "v_pk_fma_f32 v[140:141], v[140:141], s[70:71], v[108:109]\n"
    "v_pk_fma_f32 v[230:231], v[230:231], v[140:141], v[232:233]\n"
    "v_pk_mul_f32 v[232:233], v[232:233], v[140:141]\n"
    "v_pk_fma_f32 v[142:143], v[142:143], s[70:71], v[110:111]\n"
    "v_pk_fma_f32 v[230:231], v[230:231], v[142:143], v[232:233]\n"
    "v_pk_mul_f32 v[232:233], v[232:233], v[142:143]\n"
    "v_rcp_f32 v232, v232\n"
    "v_rcp_f32 v233, v233\n"
    "v_fma_f32 v230, v230, v232, v247\n"
    "v_fmac_f32 v230, v231, v233\n"
    "v_cvt_pk_bf16_f32 v230, v230, v230\n"
    "s_add_u32 s67, s67, 64\n"
    "s_waitcnt lgkmcnt(0)\n"
    "ds_write_b16 v245, v230\n"
    "ds_read_b64 v[228:229], v246\n"
    "v_and_b32 v184, v254, v184\n"
    "v_and_b32 v185, v255, v185\n"
    "s_cmp_le_u32 s34, s76\n"
    "s_cbranch_scc1 .Ldin_B0_wait\n"
    ".Ldin_B0_go:\n"
    "s_sub_u32 s94, s34, 60\n"
    "s_max_u32 s76, s76, s94\n"
    "s_add_u32 s76, s76, s72\n"
    ".Ldin_B0_dma:\n"
    "buffer_load_dwordx4 v[236:237], s[40:43], 0 idxen offen offset:0 nt lds\n"
    "buffer_load_dwordx4 v[238:239], s[40:43], 0 idxen offen offset:1024 nt lds\n"
    ".Ldin_B0_nd:\n"
    "s_branch .Ldin_G1\n"
    ".Ldin_L2_poll:\n"
    "s_sleep 1\n"
    "ds_read_b64 v[232:233], v253\n"
    "s_waitcnt lgkmcnt(0)\n"
    "v_readfirstlane_b32 s60, v232\n"
    "s_cmp_eq_u32 s60, -1\n"
    "s_cbranch_scc1 .Ldin_L2_poll\n"
    "s_branch .Ldin_L2_pr\n"
    ".Ldin_B0_wait:\n"
    "s_cmp_lt_i32 s59, s57\n"
    "s_cbranch_scc1 .Ldin_B0_w2\n"
    "s_sub_u32 s94, s67, s65\n"
    "s_cmp_gt_u32 s94, 256\n"
    "s_cbranch_scc1 .Ldin_B0_dma\n"
    "s_branch .Ldin_T1\n"
    ".Ldin_B0_slp:\n"
    "s_sleep 1\n"
    ".Ldin_B0_w2:\n"
    "s_memrealtime s[34:35]\n"
    "s_waitcnt lgkmcnt(0)\n"
    "s_sub_u32 s94, s34, s76\n"
    "s_cmp_lt_i32 s94, 0\n"
    "s_cbranch_scc1 .Ldin_B0_slp\n"
    "s_branch .Ldin_B0_go\n"
    ".p2align 5\n"
    ".Ldin_L1:\n"
    "s_setprio 0\n"
    "s_add_u32 s76, s76, s73\n"
    "v_mov_b32_e64 v253, s63\n"
    "ds_read_b64 v[232:233], v253\n"
    "v_mfma_f32_4x4x4_16b_bf16 v[128:131], v[184:185], v[32:33], v[64:67] cbsz:2 abid:0\n"
    "ds_read_b128 v[112:115], v252 offset:17408\n"
    "v_mfma_f32_4x4x4_16b_bf16 v[132:135], v[184:185], v[34:35], v[68:71] cbsz:2 abid:1\n"
    "ds_read_b128 v[116:119], v252 offset:17424\n"
    "v_mfma_f32_4x4x4_16b_bf16 v[136:139], v[184:185], v[36:37], v[72:75] cbsz:2 abid:2\n"
    "ds_read_b128 v[120:123], v252 offset:17536\n"
    "ds_read_b128 v[124:127], v252 offset:17552\n"
    "v_cvt_pk_bf16_f32 v0, v128, v129\n"
    "v_cvt_pk_bf16_f32 v1, v130, v131\n"
    "v_mfma_f32_4x4x4_16b_bf16 v[140:143], v[184:185], v[38:39], v[76:79] cbsz:2 abid:3\n"
    "v_cvt_pk_bf16_f32 v2, v132, v133\n"
    "v_cvt_pk_bf16_f32 v3, v134, v135\n"
    "v_mfma_f32_4x4x4_16b_bf16 v[128:131], v[184:185], v[40:41], v[80:83] cbsz:2 abid:0\n"
    "v_cvt_pk_bf16_f32 v4, v136, v137\n"
    "v_cvt_pk_bf16_f32 v5, v138, v139\n"
    "v_mfma_f32_4x4x4_16b_bf16 v[132:135], v[184:185], v[42:43], v[84:87] cbsz:2 abid:1\n"
    "v_cvt_pk_bf16_f32 v6, v140, v141\n"
    "v_cvt_pk_bf16_f32 v7, v142, v143\n"
    "v_mfma_f32_4x4x4_16b_bf16 v[136:139], v[184:185], v[44:45], v[88:91] cbsz:2 abid:2\n"
    "v_cvt_pk_bf16_f32 v8, v128, v129\n"
    "v_cvt_pk_bf16_f32 v9, v130, v131\n"
    "v_mfma_f32_4x4x4_16b_bf16 v[140:143], v[184:185], v[46:47], v[92:95] cbsz:2 abid:3\n"
    "v_cvt_pk_bf16_f32 v10, v132, v133\n"
    "v_cvt_pk_bf16_f32 v11, v134, v135\n"
    "v_mfma_f32_4x4x4_16b_bf16 v[128:131], v[184:185], v[48:49], v[192:195] cbsz:2 abid:0\n"
    "v_cvt_pk_bf16_f32 v12, v136, v137\n"
    "v_cvt_pk_bf16_f32 v13, v138, v139\n"
    "v_mfma_f32_4x4x4_16b_bf16 v[132:135], v[184:185], v[50:51], v[196:199] cbsz:2 abid:1\n"
    "v_cvt_pk_bf16_f32 v14, v140, v141\n"
    "v_cvt_pk_bf16_f32 v15, v142, v143\n"
    "v_mfma_f32_4x4x4_16b_bf16 v[136:139], v[184:185], v[52:53], v[200:203] cbsz:2 abid:2\n"
    "s_waitcnt lgkmcnt(0)\n"
    "v_readfirstlane_b32 s60, v232\n"
    "v_cvt_pk_bf16_f32 v16, v128, v129\n"
    "v_cvt_pk_bf16_f32 v17, v130, v131\n"
    "v_mfma_f32_4x4x4_16b_bf16 v[140:143], v[184:185], v[54:55], v[204:207] cbsz:2 abid:3\n"
    "s_cmp_eq_u32 s60, -1\n"
    "s_cbranch_scc1 .Ldin_L1_poll\n"
    ".Ldin_L1_pr:\n"
    "v_readfirstlane_b32 s95, v233\n"
    "s_and_b32 s94, s60, 63\n"
    "s_lshl_b32 s94, s94, 6\n"
    "s_add_u32 s94, s94, 68480\n"
    "s_add_u32 s63, s63, 8\n"
    "v_lshl_add_u32 v252, v249, 1, s94\n"
    "ds_read_b32 v252, v252\n"
    "s_sub_u32 s95, s95, s76\n"
    "s_ashr_i32 s95, s95, 4\n"
    "s_min_i32 s95, s95, s74\n"
    "s_max_i32 s72, s95, s75\n"
    "v_cvt_pk_bf16_f32 v18, v132, v133\n"
    "v_cvt_pk_bf16_f32 v19, v134, v135\n"
    "v_mfma_f32_4x4x4_16b_bf16 v[128:131], v[184:185], v[56:57], v[208:211] cbsz:2 abid:0\n"
    "v_cvt_pk_bf16_f32 v20, v136, v137\n"
    "v_cvt_pk_bf16_f32 v21, v138, v139\n"
    "v_mfma_f32_4x4x4_16b_bf16 v[132:135], v[184:185], v[58:59], v[212:215] cbsz:2 abid:1\n"
    "v_cvt_pk_bf16_f32 v22, v140, v141\n"
    "v_cvt_pk_bf16_f32 v23, v142, v143\n"
    "v_mfma_f32_4x4x4_16b_bf16 v[136:139], v[184:185], v[60:61], v[216:219] cbsz:2 abid:2\n"
    "v_cvt_pk_bf16_f32 v24, v128, v129\n"
    "v_cvt_pk_bf16_f32 v25, v130, v131\n"
    "v_mfma_f32_4x4x4_16b_bf16 v[140:143], v[184:185], v[62:63], v[220:223] cbsz:2 abid:3\n"
    "v_cvt_pk_bf16_f32 v26, v132, v133\n"
    "v_cvt_pk_bf16_f32 v27, v134, v135\n"
    "v_cvt_pk_bf16_f32 v28, v136, v137\n"
    "v_cvt_pk_bf16_f32 v29, v138, v139\n"
    "v_exp_f32 v152, v152 clamp\n"
    "v_cvt_pk_bf16_f32 v30, v140, v141\n"
    "v_cvt_pk_bf16_f32 v31, v142, v143\n"
    "v_mfma_f32_16x16x32_bf16 v[128:131], v[0:3], v[160:163], v[112:115]\n"
    "v_exp_f32 v144, v144 clamp\n"
    "v_mfma_f32_16x16x32_bf16 v[128:131], v[4:7], v[164:167], v[128:131]\n"
    "v_exp_f32 v145, v145 clamp\n"
    "v_mfma_f32_16x16x32_bf16 v[132:135], v[8:11], v[160:163], v[116:119]\n"
    "v_exp_f32 v146, v146 clamp\n"
    "v_mfma_f32_16x16x32_bf16 v[132:135], v[12:15], v[164:167], v[132:135]\n"
    "v_exp_f32 v147, v147 clamp\n"
    "v_mfma_f32_16x16x32_bf16 v[136:139], v[16:19], v[160:163], v[120:123]\n"
    "v_exp_f32 v148, v148 clamp\n"
    "v_mfma_f32_16x16x32_bf16 v[136:139], v[20:23], v[164:167], v[136:139]\n"
    "v_exp_f32 v149, v149 clamp\n"
    "v_mfma_f32_16x16x32_bf16 v[140:143], v[24:27], v[160:163], v[124:127]\n"
    "v_exp_f32 v150, v150 clamp\n"
    "v_mfma_f32_16x16x32_bf16 v[140:143], v[28:31], v[164:167], v[140:143]\n"
    "v_exp_f32 v151, v151 clamp\n"
    "s_add_u32 m0, s68, 4096\n"
    "v_exp_f32 v153, v153 clamp\n"
    "v_add_u32 v253, s67, v244\n"
    "v_mfma_f32_4x4x4_16b_bf16 v[224:227], v[228:229], v[168:169], v[224:227] cbsz:4 abid:0\n"
    "v_exp_f32 v154, v154 clamp\n"
    "v_exp_f32 v155, v155 clamp\n"
    "v_mfma_f32_4x4x4_16b_bf16 v[224:227], v[228:229], v[170:171], v[224:227] cbsz:4 abid:1\n"
    "v_exp_f32 v156, v156 clamp\n"
    "v_exp_f32 v157, v157 clamp\n"
    "v_mfma_f32_4x4x4_16b_bf16 v[224:227], v[228:229], v[172:173], v[224:227] cbsz:4 abid:2\n"
    "v_exp_f32 v158, v158 clamp\n"
    "v_exp_f32 v159, v159 clamp\n"
    "v_mfma_f32_4x4x4_16b_bf16 v[224:227], v[228:229], v[174:175], v[224:227] cbsz:4 abid:3\n"
    "s_waitcnt vmcnt(4)\n"
    "ds_read_b64_tr_b16 v[168:169], v240 offset:4096\n"
    "ds_read_b64_tr_b16 v[170:171], v241 offset:4352\n"
    "ds_read_b64_tr_b16 v[172:173], v242 offset:4608\n"
    "ds_read_b64_tr_b16 v[174:175], v243 offset:4864\n"
    "ds_read_b32 v236, v253 offset:0\n"
    "ds_read_b32 v238, v253 offset:4\n"
    "ds_read_b128 v[160:163], v234 offset:6144\n"
    "ds_read_b128 v[164:167], v235 offset:6144\n"
    "s_waitcnt lgkmcnt(8)\n"
    "s_nop 0\n"
    "v_readlane_b32 s20, v252, 0\n"
    "v_readlane_b32 s21, v252, 1\n"
    "v_readlane_b32 s22, v252, 2\n"
    "v_readlane_b32 s23, v252, 3\n"
    "v_readlane_b32 s24, v252, 4\n"
    "v_readlane_b32 s25, v252, 5\n"
    "v_readlane_b32 s26, v252, 6\n"
    "v_readlane_b32 s27, v252, 7\n"
    "v_pk_fma_f32 v[144:145], v[144:145], s[70:71], v[96:97]\n"
    "v_pk_fma_f32 v[146:147], v[146:147], s[70:71], v[98:99]\n"
    "v_pk_add_f32 v[230:231], v[144:145], v[146:147]\n"
    "v_pk_mul_f32 v[232:233], v[144:145], v[146:147]\n"
    "v_pk_fma_f32 v[148:149], v[148:149], s[70:71], v[100:101]\n"
    "v_pk_fma_f32 v[230:231], v[230:231], v[148:149], v[232:233]\n"
    "v_pk_mul_f32 v[232:233], v[232:233], v[148:149]\n"
    "v_pk_fma_f32 v[150:151], v[150:151], s[70:71], v[102:103]\n"
    "v_pk_fma_f32 v[230:231], v[230:231], v[150:151], v[232:233]\n"
    "v_pk_mul_f32 v[232:233], v[232:233], v[150:151]\n"
    "v_pk_fma_f32 v[152:153], v[152:153], s[70:71], v[104:105]\n"
    "v_pk_fma_f32 v[230:231], v[230:231], v[152:153], v[232:233]\n"
    "v_pk_mul_f32 v[232:233], v[232:233], v[152:153]\n"
    "v_pk_fma_f32 v[154:155], v[154:155], s[70:71], v[106:107]\n"
    "v_pk_fma_f32 v[230:231], v[230:231], v[154:155], v[232:233]\n"
    "v_pk_mul_f32 v[232:233], v[232:233], v[154:155]\n"
    "v_pk_fma_f32 v[156:157], v[156:157], s[70:71], v[108:109]\n"
    "v_pk_fma_f32 v[230:231], v[230:231], v[156:157], v[232:233]\n"
    "v_pk_mul_f32 v[232:233], v[232:233], v[156:157]\n"
    "v_pk_fma_f32 v[158:159], v[158:159], s[70:71], v[110:111]\n"
    "v_pk_fma_f32 v[230:231], v[230:231], v[158:159], v[232:233]\n"
    "v_pk_mul_f32 v[232:233], v[232:233], v[158:159]\n"
    "v_rcp_f32 v232, v232\n"
    "v_rcp_f32 v233, v233\n"
    "v_fma_f32 v230, v230, v232, v247\n"
    "v_fmac_f32 v230, v231, v233\n"
    "v_cvt_pk_bf16_f32 v230, v230, v230\n"
    "s_add_u32 s67, s67, 64\n"
    "s_waitcnt lgkmcnt(0)\n"
    "ds_write_b16 v245, v230\n"
    "ds_read_b64 v[228:229], v246\n"
    "s_lshl1_add_u32 s76, s72, s76\n"
    "buffer_load_dwordx4 v[236:237], s[40:43], 0 idxen offen offset:0 nt lds\n"
    "buffer_load_dwordx4 v[238:239], s[40:43], 0 idxen offen offset:1024 nt lds\n"
    "s_and_b32 s62, s58, 63\n"
    "s_mul_i32 s62, s62, 272\n"
    "s_mov_b32 s58, s59\n"
    "s_mov_b32 s59, s60\n"
    "s_mov_b32 s81, s65\n"
    "s_xor_b32 s65, s65, s64\n"
    "s_mov_b32 s80, s83\n"
    "s_add_u32 s82, s65, 256\n"
    "s_cmp_lt_i32 s59, s57\n"
    "s_cselect_b32 s76, s76, -1\n"
    "s_bitcmp1_b32 s58, 30\n"
    "s_cbranch_scc1 .Ldin_A2s\n"
    ".Ldin_A2:\n"
    "v_mfma_f32_16x16x32_bf16 v[144:147], v[0:3], v[160:163], v[112:115]\n"
    "v_exp_f32 v128, v128 clamp\n"
    "v_mfma_f32_16x16x32_bf16 v[144:147], v[4:7], v[164:167], v[144:147]\n"
    "v_exp_f32 v129, v129 clamp\n"
    "v_mfma_f32_16x16x32_bf16 v[148:151], v[8:11], v[160:163], v[116:119]\n"
    "v_exp_f32 v130, v130 clamp\n"
    "v_mfma_f32_16x16x32_bf16 v[148:151], v[12:15], v[164:167], v[148:151]\n"
    "v_exp_f32 v131, v131 clamp\n"
    "v_mfma_f32_16x16x32_bf16 v[152:155], v[16:19], v[160:163], v[120:123]\n"
    "v_exp_f32 v132, v132 clamp\n"
    "v_mfma_f32_16x16x32_bf16 v[152:155], v[20:23], v[164:167], v[152:155]\n"
    "v_exp_f32 v133, v133 clamp\n"
    "v_mfma_f32_16x16x32_bf16 v[156:159], v[24:27], v[160:163], v[124:127]\n"
    "v_exp_f32 v134, v134 clamp\n"
    "v_mfma_f32_16x16x32_bf16 v[156:159], v[28:31], v[164:167], v[156:159]\n"
    "v_exp_f32 v135, v135 clamp\n"
    "v_exp_f32 v136, v136 clamp\n"
    "s_mov_b32 m0, s62\n"
    "v_exp_f32 v137, v137 clamp\n"
    "v_add_u32 v253, s67, v244\n"
    "s_waitcnt lgkmcnt(0)\n"
    "v_mfma_f32_4x4x4_16b_bf16 v[224:227], v[228:229], v[176:177], v[224:227] cbsz:4 abid:0\n"
    "v_exp_f32 v138, v138 clamp\n"
    "v_exp_f32 v139, v139 clamp\n"
    "v_mfma_f32_4x4x4_16b_bf16 v[224:227], v[228:229], v[178:179], v[224:227] cbsz:4 abid:1\n"
    "v_exp_f32 v140, v140 clamp\n"
    "v_exp_f32 v141, v141 clamp\n"
    "v_mfma_f32_4x4x4_16b_bf16 v[224:227], v[228:229], v[180:181], v[224:227] cbsz:4 abid:2\n"
    "v_exp_f32 v142, v142 clamp\n"
    "v_exp_f32 v143, v143 clamp\n"
    "v_mfma_f32_4x4x4_16b_bf16 v[224:227], v[228:229], v[182:183], v[224:227] cbsz:4 abid:3\n"
    "s_waitcnt vmcnt(4)\n"
    "ds_read_b64_tr_b16 v[176:177], v240 offset:6144\n"
    "ds_read_b64_tr_b16 v[178:179], v241 offset:6400\n"
    "ds_read_b64_tr_b16 v[180:181], v242 offset:6656\n"
    "ds_read_b64_tr_b16 v[182:183], v243 offset:6912\n"
    "ds_read_b32 v236, v253 offset:0\n"
    "ds_read_b32 v238, v253 offset:4\n"
    "ds_read_b128 v[160:163], v234 offset:0\n"
    "ds_read_b128 v[164:167], v235 offset:0\n"
    "s_cbranch_vccz .Ldin_A2_np\n"
    "s_setprio 1\n"
    ".Ldin_A2_np:\n"
    "v_pk_fma_f32 v[128:129], v[128:129], s[70:71], v[96:97]\n"
    "v_pk_fma_f32 v[130:131], v[130:131], s[70:71], v[98:99]\n"
    "v_pk_add_f32 v[190:191], v[224:225], v[226:227]\n"
    "v_add_f32 v190, v190, v191\n"
    "ds_write_addtid_b32 v190 offset:32768\n"
    "s_add_u32 m0, s68, 6144\n"
    "s_cmp_ge_i32 s59, s57\n"
    "s_cbranch_scc1 .Ldin_A2_nl\n"
    "s_bfe_u32 s94, s59, 0x180006\n"
    "s_mul_i32 s86, s94, s56\n"
    "v_mbcnt_lo_u32_b32 v184, s20, 0\n"
    "v_mbcnt_lo_u32_b32 v185, s22, 0\n"
    "v_mbcnt_lo_u32_b32 v186, s24, 0\n"
    "v_mbcnt_lo_u32_b32 v187, s26, 0\n"
    "s_bcnt1_i32_b64 s90, s[20:21]\n"
    "s_bcnt1_i32_b64 s91, s[22:23]\n"
    "s_bcnt1_i32_b64 s92, s[24:25]\n"
    "s_bcnt1_i32_b64 s93, s[26:27]\n"
    "v_mbcnt_hi_u32_b32 v184, s21, v184\n"
    "v_mbcnt_hi_u32_b32 v185, s23, v185\n"
    "v_mbcnt_hi_u32_b32 v186, s25, v186\n"
    "v_mbcnt_hi_u32_b32 v187, s27, v187\n"
    "s_lshl2_add_u32 s90, s90, s65\n"
    "s_lshl2_add_u32 s91, s91, s90\n"
    "s_lshl2_add_u32 s92, s92, s91\n"
    "s_lshl2_add_u32 s93, s93, s92\n"
    "v_add_u32 v188, s86, v251\n"
    "v_lshl_add_u32 v184, v184, 2, s65\n"
    "v_lshl_add_u32 v185, v185, 2, s90\n"
    "v_lshl_add_u32 v186, v186, 2, s91\n"
    "v_lshl_add_u32 v187, v187, 2, s92\n"
    "v_mov_b32 v191, -1\n"
    "v_lshl_add_u32 v190, v249, 1, s93\n"
    "ds_write2_b32 v190, v191, v191 offset1:64\n"
    "s_mov_b64 exec, s[20:21]\n"
    "ds_write_b32 v184, v188\n"
    "s_mov_b64 exec, s[22:23]\n"
    "v_add_u32 v189, 64, v188\n"
    "ds_write_b32 v185, v189\n"
    "s_mov_b64 exec, s[24:25]\n"
    "v_add_u32 v190, 128, v188\n"
    "ds_write_b32 v186, v190\n"
    "s_mov_b64 exec, s[26:27]\n"
    "v_add_u32 v189, 192, v188\n"
    "ds_write_b32 v187, v189\n"
    "s_mov_b64 exec, -1\n"
    "s_sub_u32 s95, s93, -60\n"
    "s_add_u32 s94, s82, 128\n"
    "s_andn2_b32 s95, s95, 63\n"
    "s_max_u32 s83, s95, s94\n"
    ".Ldin_A2_nl:\n"
    "v_pk_add_f32 v[230:231], v[128:129], v[130:131]\n"
    "v_pk_mul_f32 v[232:233], v[128:129], v[130:131]\n"
    "v_pk_fma_f32 v[132:133], v[132:133], s[70:71], v[100:101]\n"
    "v_pk_fma_f32 v[230:231], v[230:231], v[132:133], v[232:233]\n"
    "v_pk_mul_f32 v[232:233], v[232:233], v[132:133]\n"
    "v_pk_fma_f32 v[134:135], v[134:135], s[70:71], v[102:103]\n"
    "v_pk_fma_f32 v[230:231], v[230:231], v[134:135], v[232:233]\n"
    "v_pk_mul_f32 v[232:233], v[232:233], v[134:135]\n"
    "v_pk_fma_f32 v[136:137], v[136:137], s[70:71], v[104:105]\n"
    "v_pk_fma_f32 v[230:231], v[230:231], v[136:137], v[232:233]\n"
    "v_pk_mul_f32 v[232:233], v[232:233], v[136:137]\n"
    "v_pk_fma_f32 v[138:139], v[138:139], s[70:71], v[106:107]\n"
    "v_pk_fma_f32 v[230:231], v[230:231], v[138:139], v[232:233]\n"
    "v_pk_mul_f32 v[232:233], v[232:233], v[138:139]\n"
    "v_pk_fma_f32 v[140:141], v[140:141], s[70:71], v[108:109]\n"
    "v_pk_fma_f32 v[230:231], v[230:231], v[140:141], v[232:233]\n"
    "v_pk_mul_f32 v[232:233], v[232:233], v[140:141]\n"
    "v_pk_fma_f32 v[142:143], v[142:143], s[70:71], v[110:111]\n"
    "v_pk_fma_f32 v[230:231], v[230:231], v[142:143], v[232:233]\n"
    "v_pk_mul_f32 v[232:233], v[232:233], v[142:143]\n"
    "v_rcp_f32 v232, v232\n"
    "v_rcp_f32 v233, v233\n"
    "v_fma_f32 v230, v230, v232, v247\n"
    "v_fmac_f32 v230, v231, v233\n"
    "v_cvt_pk_bf16_f32 v230, v230, v230\n"
    "s_add_u32 s67, s67, 64\n"
    "s_waitcnt lgkmcnt(0)\n"
    "ds_write_b16 v245, v230\n"
    "ds_read_b64 v[228:229], v246\n"
    "buffer_load_dwordx4 v[236:237], s[40:43], 0 idxen offen offset:0 nt lds\n"
    "buffer_load_dwordx4 v[238:239], s[40:43], 0 idxen offen offset:1024 nt lds\n"
    ".Ldin_B3:\n"
    "v_mfma_f32_16x16x32_bf16 v[128:131], v[0:3], v[160:163], v[112:115]\n"
    "v_exp_f32 v144, v144 clamp\n"
    "v_mfma_f32_16x16x32_bf16 v[128:131], v[4:7], v[164:167], v[128:131]\n"
    "v_exp_f32 v145, v145 clamp\n"
    "v_mfma_f32_16x16x32_bf16 v[132:135], v[8:11], v[160:163], v[116:119]\n"
    "v_exp_f32 v146, v146 clamp\n"
    "v_mfma_f32_16x16x32_bf16 v[132:135], v[12:15], v[164:167], v[132:135]\n"
    "v_exp_f32 v147, v147 clamp\n"
    "v_mfma_f32_16x16x32_bf16 v[136:139], v[16:19], v[160:163], v[120:123]\n"
    "v_exp_f32 v148, v148 clamp\n"
    "v_mfma_f32_16x16x32_bf16 v[136:139], v[20:23], v[164:167], v[136:139]\n"
    "v_exp_f32 v149, v149 clamp\n"
    "v_mfma_f32_16x16x32_bf16 v[140:143], v[24:27], v[160:163], v[124:127]\n"
    "v_exp_f32 v150, v150 clamp\n"
    "v_mfma_f32_16x16x32_bf16 v[140:143], v[28:31], v[164:167], v[140:143]\n"
    "v_exp_f32 v151, v151 clamp\n"
    "v_exp_f32 v152, v152 clamp\n"
    "s_cmp_eq_u32 s67, s80\n"
    "s_cselect_b32 s67, s65, s67\n"
    "s_and_b32 s94, s59, 63\n"
    "s_mul_i32 s94, s94, 272\n"
    "v_add_u32 v252, s94, v250\n"
    "s_add_u32 m0, s68, 0\n"
    "v_exp_f32 v153, v153 clamp\n"
    "v_add_u32_e64 v253, s67, v244\n"
    "s_waitcnt lgkmcnt(0)\n"
    "v_mfma_f32_4x4x4_16b_bf16 v[224:227], v[228:229], v[168:169], 0 cbsz:4 abid:0\n"
    "v_exp_f32 v154, v154 clamp\n"
    "v_exp_f32 v155, v155 clamp\n"
    "v_mfma_f32_4x4x4_16b_bf16 v[224:227], v[228:229], v[170:171], v[224:227] cbsz:4 abid:1\n"
    "v_exp_f32 v156, v156 clamp\n"
    "v_exp_f32 v157, v157 clamp\n"
    "v_mfma_f32_4x4x4_16b_bf16 v[224:227], v[228:229], v[172:173], v[224:227] cbsz:4 abid:2\n"
    "v_exp_f32 v158, v158 clamp\n"
    "v_exp_f32 v159, v159 clamp\n"
    "v_mfma_f32_4x4x4_16b_bf16 v[224:227], v[228:229], v[174:175], v[224:227] cbsz:4 abid:3\n"
    "s_waitcnt vmcnt(4)\n"
    "s_memrealtime s[34:35]\n"
    "ds_read_b64_tr_b16 v[168:169], v240 offset:0\n"
    "ds_read_b64_tr_b16 v[170:171], v241 offset:256\n"
    "ds_read_b64_tr_b16 v[172:173], v242 offset:512\n"
    "ds_read_b64_tr_b16 v[174:175], v243 offset:768\n"
    "ds_read_b32 v236, v253 offset:0\n"
    "ds_read_b32 v238, v253 offset:4\n"
    "ds_read_b128 v[160:163], v234 offset:2048\n"
    "ds_read_b128 v[164:167], v235 offset:2048\n"
    "v_add_u32_e64 v184, v248, v252\n"
    "ds_read_b64 v[184:185], v184\n"
    "v_pk_fma_f32 v[144:145], v[144:145], s[70:71], v[96:97]\n"
    "v_pk_fma_f32 v[146:147], v[146:147], s[70:71], v[98:99]\n"
    "v_pk_add_f32 v[230:231], v[144:145], v[146:147]\n"
    "v_pk_mul_f32 v[232:233], v[144:145], v[146:147]\n"
    "v_pk_fma_f32 v[148:149], v[148:149], s[70:71], v[100:101]\n"
    "v_pk_fma_f32 v[230:231], v[230:231], v[148:149], v[232:233]\n"
    "v_pk_mul_f32 v[232:233], v[232:233], v[148:149]\n"
    "v_pk_fma_f32 v[150:151], v[150:151], s[70:71], v[102:103]\n"
    "v_pk_fma_f32 v[230:231], v[230:231], v[150:151], v[232:233]\n"
    "v_pk_mul_f32 v[232:233], v[232:233], v[150:151]\n"
    "v_pk_fma_f32 v[152:153], v[152:153], s[70:71], v[104:105]\n"
    "v_pk_fma_f32 v[230:231], v[230:231], v[152:153], v[232:233]\n"
    "v_pk_mul_f32 v[232:233], v[232:233], v[152:153]\n"
    "v_pk_fma_f32 v[154:155], v[154:155], s[70:71], v[106:107]\n"
    "v_pk_fma_f32 v[230:231], v[230:231], v[154:155], v[232:233]\n"
    "v_pk_mul_f32 v[232:233], v[232:233], v[154:155]\n"
    "v_pk_fma_f32 v[156:157], v[156:157], s[70:71], v[108:109]\n"
    "v_pk_fma_f32 v[230:231], v[230:231], v[156:157], v[232:233]\n"
    "v_pk_mul_f32 v[232:233], v[232:233], v[156:157]\n"
    "v_pk_fma_f32 v[158:159], v[158:159], s[70:71], v[110:111]\n"
    "v_pk_fma_f32 v[230:231], v[230:231], v[158:159], v[232:233]\n"
    "v_pk_mul_f32 v[232:233], v[232:233], v[158:159]\n"
    "v_rcp_f32 v232, v232\n"
    "v_rcp_f32 v233, v233\n"
    "v_fma_f32 v230, v230, v232, v247\n"
    "v_fmac_f32 v230, v231, v233\n"
    "v_cvt_pk_bf16_f32 v230, v230, v230\n"
    "s_add_u32 s67, s67, 64\n"
    "s_waitcnt lgkmcnt(0)\n"
    "ds_write_b16 v245, v230\n"
    "ds_read_b64 v[228:229], v246\n"
    "v_and_b32 v184, v254, v184\n"
    "v_and_b32 v185, v255, v185\n"
    "s_cmp_le_u32 s34, s76\n"
    "s_cbranch_scc1 .Ldin_B3_wait\n"
    ".Ldin_B3_go:\n"
    "s_sub_u32 s94, s34, 60\n"
    "s_max_u32 s76, s76, s94\n"
    "s_add_u32 s76, s76, s72\n"
    ".Ldin_B3_dma:\n"
    "buffer_load_dwordx4 v[236:237], s[40:43], 0 idxen offen offset:0 nt lds\n"
    "buffer_load_dwordx4 v[238:239], s[40:43], 0 idxen offen offset:1024 nt lds\n"
    ".Ldin_B3_nd:\n"
    "s_branch .Ldin_G0\n"
    ".Ldin_L1_poll:\n"
    "s_sleep 1\n"
    "ds_read_b64 v[232:233], v253\n"
    "s_waitcnt lgkmcnt(0)\n"
    "v_readfirstlane_b32 s60, v232\n"
    "s_cmp_eq_u32 s60, -1\n"
    "s_cbranch_scc1 .Ldin_L1_poll\n"
    "s_branch .Ldin_L1_pr\n"
    ".Ldin_B3_wait:\n"
    "s_cmp_lt_i32 s59, s57\n"
    "s_cbranch_scc1 .Ldin_B3_w2\n"
    "s_sub_u32 s94, s67, s65\n"
    "s_cmp_gt_u32 s94, 256\n"
    "s_cbranch_scc1 .Ldin_B3_dma\n"
    "s_branch .Ldin_T0\n"
    ".Ldin_B3_slp:\n"
    "s_sleep 1\n"
    ".Ldin_B3_w2:\n"
    "s_memrealtime s[34:35]\n"
    "s_waitcnt lgkmcnt(0)\n"
    "s_sub_u32 s94, s34, s76\n"
    "s_cmp_lt_i32 s94, 0\n"
    "s_cbranch_scc1 .Ldin_B3_slp\n"
    "s_branch .Ldin_B3_go\n"
    ".p2align 5\n"
    ".Ldin_L0:\n"
    "s_setprio 0\n"
    "s_add_u32 s76, s76, s73\n"
    "v_mov_b32_e64 v253, s63\n"
    "ds_read_b64 v[232:233], v253\n"
    "v_mfma_f32_4x4x4_16b_bf16 v[144:147], v[184:185], v[32:33], v[64:67] cbsz:2 abid:0\n"
    "ds_read_b128 v[112:115], v252 offset:17408\n"
    "v_mfma_f32_4x4x4_16b_bf16 v[148:151], v[184:185], v[34:35], v[68:71] cbsz:2 abid:1\n"
    "ds_read_b128 v[116:119], v252 offset:17424\n"
    "v_mfma_f32_4x4x4_16b_bf16 v[152:155], v[184:185], v[36:37], v[72:75] cbsz:2 abid:2\n"
    "ds_read_b128 v[120:123], v252 offset:17536\n"
    "ds_read_b128 v[124:127], v252 offset:17552\n"
    "v_cvt_pk_bf16_f32 v0, v144, v145\n"
    "v_cvt_pk_bf16_f32 v1, v146, v147\n"
    "v_mfma_f32_4x4x4_16b_bf16 v[156:159], v[184:185], v[38:39], v[76:79] cbsz:2 abid:3\n"
    "v_cvt_pk_bf16_f32 v2, v148, v149\n"
    "v_cvt_pk_bf16_f32 v3, v150, v151\n"
    "v_mfma_f32_4x4x4_16b_bf16 v[144:147], v[184:185], v[40:41], v[80:83] cbsz:2 abid:0\n"
    "v_cvt_pk_bf16_f32 v4, v152, v153\n"
    "v_cvt_pk_bf16_f32 v5, v154, v155\n"
    "v_mfma_f32_4x4x4_16b_bf16 v[148:151], v[184:185], v[42:43], v[84:87] cbsz:2 abid:1\n"
    "v_cvt_pk_bf16_f32 v6, v156, v157\n"
    "v_cvt_pk_bf16_f32 v7, v158, v159\n"
    "v_mfma_f32_4x4x4_16b_bf16 v[152:155], v[184:185], v[44:45], v[88:91] cbsz:2 abid:2\n"
    "v_cvt_pk_bf16_f32 v8, v144, v145\n"
    "v_cvt_pk_bf16_f32 v9, v146, v147\n"
    "v_mfma_f32_4x4x4_16b_bf16 v[156:159], v[184:185], v[46:47], v[92:95] cbsz:2 abid:3\n"
    "v_cvt_pk_bf16_f32 v10, v148, v149\n"
    "v_cvt_pk_bf16_f32 v11, v150, v151\n"
    "v_mfma_f32_4x4x4_16b_bf16 v[144:147], v[184:185], v[48:49], v[192:195] cbsz:2 abid:0\n"
    "v_cvt_pk_bf16_f32 v12, v152, v153\n"
    "v_cvt_pk_bf16_f32 v13, v154, v155\n"
    "v_mfma_f32_4x4x4_16b_bf16 v[148:151], v[184:185], v[50:51], v[196:199] cbsz:2 abid:1\n"
    "v_cvt_pk_bf16_f32 v14, v156, v157\n"
    "v_cvt_pk_bf16_f32 v15, v158, v159\n"
    "v_mfma_f32_4x4x4_16b_bf16 v[152:155], v[184:185], v[52:53], v[200:203] cbsz:2 abid:2\n"
    "s_waitcnt lgkmcnt(0)\n"
    "v_readfirstlane_b32 s60, v232\n"
    "v_cvt_pk_bf16_f32 v16, v144, v145\n"
    "v_cvt_pk_bf16_f32 v17, v146, v147\n"
    "v_mfma_f32_4x4x4_16b_bf16 v[156:159], v[184:185], v[54:55], v[204:207] cbsz:2 abid:3\n"
    "s_cmp_eq_u32 s60, -1\n"
    "s_cbranch_scc1 .Ldin_L0_poll\n"
    ".Ldin_L0_pr:\n"
    "v_readfirstlane_b32 s95, v233\n"
    "s_and_b32 s94, s60, 63\n"
    "s_lshl_b32 s94, s94, 6\n"
    "s_add_u32 s94, s94, 68480\n"
    "s_add_u32 s63, s63, 8\n"
    "v_lshl_add_u32 v252, v249, 1, s94\n"
    "ds_read_b32 v252, v252\n"
    "s_sub_u32 s95, s95, s76\n"
    "s_ashr_i32 s95, s95, 4\n"
    "s_min_i32 s95, s95, s74\n"
    "s_max_i32 s72, s95, s75\n"
    "v_cvt_pk_bf16_f32 v18, v148, v149\n"
    "v_cvt_pk_bf16_f32 v19, v150, v151\n"
    "v_mfma_f32_4x4x4_16b_bf16 v[144:147], v[184:185], v[56:57], v[208:211] cbsz:2 abid:0\n"
    "v_cvt_pk_bf16_f32 v20, v152, v153\n"
    "v_cvt_pk_bf16_f32 v21, v154, v155\n"
    "v_mfma_f32_4x4x4_16b_bf16 v[148:151], v[184:185], v[58:59], v[212:215] cbsz:2 abid:1\n"
    "v_cvt_pk_bf16_f32 v22, v156, v157\n"
    "v_cvt_pk_bf16_f32 v23, v158, v159\n"
    "v_mfma_f32_4x4x4_16b_bf16 v[152:155], v[184:185], v[60:61], v[216:219] cbsz:2 abid:2\n"
    "v_cvt_pk_bf16_f32 v24, v144, v145\n"
    "v_cvt_pk_bf16_f32 v25, v146, v147\n"
    "v_mfma_f32_4x4x4_16b_bf16 v[156:159], v[184:185], v[62:63], v[220:223] cbsz:2 abid:3\n"
    "v_cvt_pk_bf16_f32 v26, v148, v149\n"
    "v_cvt_pk_bf16_f32 v27, v150, v151\n"
    "v_cvt_pk_bf16_f32 v28, v152, v153\n"
    "v_cvt_pk_bf16_f32 v29, v154, v155\n"
    "v_exp_f32 v136, v136 clamp\n"
    "v_cvt_pk_bf16_f32 v30, v156, v157\n"
    "v_cvt_pk_bf16_f32 v31, v158, v159\n"
    "v_mfma_f32_16x16x32_bf16 v[144:147], v[0:3], v[160:163], v[112:115]\n"
    "v_exp_f32 v128, v128 clamp\n"
    "v_mfma_f32_16x16x32_bf16 v[144:147], v[4:7], v[164:167], v[144:147]\n"
    "v_exp_f32 v129, v129 clamp\n"
    "v_mfma_f32_16x16x32_bf16 v[148:151], v[8:11], v[160:163], v[116:119]\n"
    "v_exp_f32 v130, v130 clamp\n"
    "v_mfma_f32_16x16x32_bf16 v[148:151], v[12:15], v[164:167], v[148:151]\n"
    "v_exp_f32 v131, v131 clamp\n"
    "v_mfma_f32_16x16x32_bf16 v[152:155], v[16:19], v[160:163], v[120:123]\n"
    "v_exp_f32 v132, v132 clamp\n"
    "v_mfma_f32_16x16x32_bf16 v[152:155], v[20:23], v[164:167], v[152:155]\n"
    "v_exp_f32 v133, v133 clamp\n"
    "v_mfma_f32_16x16x32_bf16 v[156:159], v[24:27], v[160:163], v[124:127]\n"
    "v_exp_f32 v134, v134 clamp\n"
    "v_mfma_f32_16x16x32_bf16 v[156:159], v[28:31], v[164:167], v[156:159]\n"
    "v_exp_f32 v135, v135 clamp\n"
    "s_add_u32 m0, s68, 2048\n"
    "v_exp_f32 v137, v137 clamp\n"
    "v_add_u32 v253, s67, v244\n"
    "v_mfma_f32_4x4x4_16b_bf16 v[224:227], v[228:229], v[176:177], v[224:227] cbsz:4 abid:0\n"
    "v_exp_f32 v138, v138 clamp\n"
    "v_exp_f32 v139, v139 clamp\n"
    "v_mfma_f32_4x4x4_16b_bf16 v[224:227], v[228:229], v[178:179], v[224:227] cbsz:4 abid:1\n"
    "v_exp_f32 v140, v140 clamp\n"
    "v_exp_f32 v141, v141 clamp\n"
    "v_mfma_f32_4x4x4_16b_bf16 v[224:227], v[228:229], v[180:181], v[224:227] cbsz:4 abid:2\n"
    "v_exp_f32 v142, v142 clamp\n"
    "v_exp_f32 v143, v143 clamp\n"
    "v_mfma_f32_4x4x4_16b_bf16 v[224:227], v[228:229], v[182:183], v[224:227] cbsz:4 abid:3\n"
    "s_waitcnt vmcnt(4)\n"
    "ds_read_b64_tr_b16 v[176:177], v240 offset:2048\n"
    "ds_read_b64_tr_b16 v[178:179], v241 offset:2304\n"
    "ds_read_b64_tr_b16 v[180:181], v242 offset:2560\n"
    "ds_read_b64_tr_b16 v[182:183], v243 offset:2816\n"
    "ds_read_b32 v236, v253 offset:0\n"
    "ds_read_b32 v238, v253 offset:4\n"
    "ds_read_b128 v[160:163], v234 offset:4096\n"
    "ds_read_b128 v[164:167], v235 offset:4096\n"
    "s_waitcnt lgkmcnt(8)\n"
    "s_nop 0\n"
    "v_readlane_b32 s20, v252, 0\n"
    "v_readlane_b32 s21, v252, 1\n"
    "v_readlane_b32 s22, v252, 2\n"
    "v_readlane_b32 s23, v252, 3\n"
    "v_readlane_b32 s24, v252, 4\n"
    "v_readlane_b32 s25, v252, 5\n"
    "v_readlane_b32 s26, v252, 6\n"
    "v_readlane_b32 s27, v252, 7\n"
    "v_pk_fma_f32 v[128:129], v[128:129], s[70:71], v[96:97]\n"
    "v_pk_fma_f32 v[130:131], v[130:131], s[70:71], v[98:99]\n"
    "v_pk_add_f32 v[230:231], v[128:129], v[130:131]\n"
    "v_pk_mul_f32 v[232:233], v[128:129], v[130:131]\n"
    "v_pk_fma_f32 v[132:133], v[132:133], s[70:71], v[100:101]\n"
    "v_pk_fma_f32 v[230:231], v[230:231], v[132:133], v[232:233]\n"
    "v_pk_mul_f32 v[232:233], v[232:233], v[132:133]\n"
    "v_pk_fma_f32 v[134:135], v[134:135], s[70:71], v[102:103]\n"
    "v_pk_fma_f32 v[230:231], v[230:231], v[134:135], v[232:233]\n"
    "v_pk_mul_f32 v[232:233], v[232:233], v[134:135]\n"
    "v_pk_fma_f32 v[136:137], v[136:137], s[70:71], v[104:105]\n"
    "v_pk_fma_f32 v[230:231], v[230:231], v[136:137], v[232:233]\n"
    "v_pk_mul_f32 v[232:233], v[232:233], v[136:137]\n"
    "v_pk_fma_f32 v[138:139], v[138:139], s[70:71], v[106:107]\n"
    "v_pk_fma_f32 v[230:231], v[230:231], v[138:139], v[232:233]\n"
    "v_pk_mul_f32 v[232:233], v[232:233], v[138:139]\n"
    "v_pk_fma_f32 v[140:141], v[140:141], s[70:71], v[108:109]\n"
    "v_pk_fma_f32 v[230:231], v[230:231], v[140:141], v[232:233]\n"
    "v_pk_mul_f32 v[232:233], v[232:233], v[140:141]\n"
    "v_pk_fma_f32 v[142:143], v[142:143], s[70:71], v[110:111]\n"
    "v_pk_fma_f32 v[230:231], v[230:231], v[142:143], v[232:233]\n"
    "v_pk_mul_f32 v[232:233], v[232:233], v[142:143]\n"
    "v_rcp_f32 v232, v232\n"
    "v_rcp_f32 v233, v233\n"
    "v_fma_f32 v230, v230, v232, v247\n"
    "v_fmac_f32 v230, v231, v233\n"
    "v_cvt_pk_bf16_f32 v230, v230, v230\n"
    "s_add_u32 s67, s67, 64\n"
    "s_waitcnt lgkmcnt(0)\n"
    "ds_write_b16 v245, v230\n"
    "ds_read_b64 v[228:229], v246\n"
    "s_lshl1_add_u32 s76, s72, s76\n"
    "buffer_load_dwordx4 v[236:237], s[40:43], 0 idxen offen offset:0 nt lds\n"
    "buffer_load_dwordx4 v[238:239], s[40:43], 0 idxen offen offset:1024 nt lds\n"
    "s_and_b32 s62, s58, 63\n"
    "s_mul_i32 s62, s62, 272\n"
    "s_mov_b32 s58, s59\n"
    "s_mov_b32 s59, s60\n"
    "s_mov_b32 s81, s65\n"
    "s_xor_b32 s65, s65, s64\n"
    "s_mov_b32 s80, s83\n"
    "s_add_u32 s82, s65, 256\n"
    "s_cmp_lt_i32 s59, s57\n"
    "s_cselect_b32 s76, s76, -1\n"
    "s_bitcmp1_b32 s58, 30\n"
    "s_cbranch_scc1 .Ldin_A1s\n"
    ".Ldin_A1:\n"
    "v_mfma_f32_16x16x32_bf16 v[128:131], v[0:3], v[160:163], v[112:115]\n"
    "v_exp_f32 v144, v144 clamp\n"
    "v_mfma_f32_16x16x32_bf16 v[128:131], v[4:7], v[164:167], v[128:131]\n"
    "v_exp_f32 v145, v145 clamp\n"
    "v_mfma_f32_16x16x32_bf16 v[132:135], v[8:11], v[160:163], v[116:119]\n"
    "v_exp_f32 v146, v146 clamp\n"
    "v_mfma_f32_16x16x32_bf16 v[132:135], v[12:15], v[164:167], v[132:135]\n"
    "v_exp_f32 v147, v147 clamp\n"
    "v_mfma_f32_16x16x32_bf16 v[136:139], v[16:19], v[160:163], v[120:123]\n"
    "v_exp_f32 v148, v148 clamp\n"
    "v_mfma_f32_16x16x32_bf16 v[136:139], v[20:23], v[164:167], v[136:139]\n"
    "v_exp_f32 v149, v149 clamp\n"
    "v_mfma_f32_16x16x32_bf16 v[140:143], v[24:27], v[160:163], v[124:127]\n"
    "v_exp_f32 v150, v150 clamp\n"
    "v_mfma_f32_16x16x32_bf16 v[140:143], v[28:31], v[164:167], v[140:143]\n"
    "v_exp_f32 v151, v151 clamp\n"
    "v_exp_f32 v152, v152 clamp\n"
    "s_mov_b32 m0, s62\n"
    "v_exp_f32 v153, v153 clamp\n"
    "v_add_u32 v253, s67, v244\n"
    "s_waitcnt lgkmcnt(0)\n"
    "v_mfma_f32_4x4x4_16b_bf16 v[224:227], v[228:229], v[168:169], v[224:227] cbsz:4 abid:0\n"
    "v_exp_f32 v154, v154 clamp\n"
    "v_exp_f32 v155, v155 clamp\n"
    "v_mfma_f32_4x4x4_16b_bf16 v[224:227], v[228:229], v[170:171], v[224:227] cbsz:4 abid:1\n"
    "v_exp_f32 v156, v156 clamp\n"
    "v_exp_f32 v157, v157 clamp\n"
    "v_mfma_f32_4x4x4_16b_bf16 v[224:227], v[228:229], v[172:173], v[224:227] cbsz:4 abid:2\n"
    "v_exp_f32 v158, v158 clamp\n"
    "v_exp_f32 v159, v159 clamp\n"
    "v_mfma_f32_4x4x4_16b_bf16 v[224:227], v[228:229], v[174:175], v[224:227] cbsz:4 abid:3\n"
    "s_waitcnt vmcnt(4)\n"
    "ds_read_b64_tr_b16 v[168:169], v240 offset:4096\n"
    "ds_read_b64_tr_b16 v[170:171], v241 offset:4352\n"
    "ds_read_b64_tr_b16 v[172:173], v242 offset:4608\n"
    "ds_read_b64_tr_b16 v[174:175], v243 offset:4864\n"
    "ds_read_b32 v236, v253 offset:0\n"
    "ds_read_b32 v238, v253 offset:4\n"
    "ds_read_b128 v[160:163], v234 offset:6144\n"
    "ds_read_b128 v[164:167], v235 offset:6144\n"
    "s_cbranch_vccz .Ldin_A1_np\n"
    "s_setprio 1\n"
    ".Ldin_A1_np:\n"
    "v_pk_fma_f32 v[144:145], v[144:145], s[70:71], v[96:97]\n"
    "v_pk_fma_f32 v[146:147], v[146:147], s[70:71], v[98:99]\n"
    "v_pk_add_f32 v[190:191], v[224:225], v[226:227]\n"
    "v_add_f32 v190, v190, v191\n"
    "ds_write_addtid_b32 v190 offset:32768\n"
    "s_add_u32 m0, s68, 4096\n"
    "s_cmp_ge_i32 s59, s57\n"
    "s_cbranch_scc1 .Ldin_A1_nl\n"
    "s_bfe_u32 s94, s59, 0x180006\n"
    "s_mul_i32 s86, s94, s56\n"
    "v_mbcnt_lo_u32_b32 v184, s20, 0\n"
    "v_mbcnt_lo_u32_b32 v185, s22, 0\n"
    "v_mbcnt_lo_u32_b32 v186, s24, 0\n"
    "v_mbcnt_lo_u32_b32 v187, s26, 0\n"
    "s_bcnt1_i32_b64 s90, s[20:21]\n"
    "s_bcnt1_i32_b64 s91, s[22:23]\n"
    "s_bcnt1_i32_b64 s92, s[24:25]\n"
    "s_bcnt1_i32_b64 s93, s[26:27]\n"
    "v_mbcnt_hi_u32_b32 v184, s21, v184\n"
    "v_mbcnt_hi_u32_b32 v185, s23, v185\n"
    "v_mbcnt_hi_u32_b32 v186, s25, v186\n"
    "v_mbcnt_hi_u32_b32 v187, s27, v187\n"
    "s_lshl2_add_u32 s90, s90, s65\n"
    "s_lshl2_add_u32 s91, s91, s90\n"
    "s_lshl2_add_u32 s92, s92, s91\n"
    "s_lshl2_add_u32 s93, s93, s92\n"
    "v_add_u32 v188, s86, v251\n"
    "v_lshl_add_u32 v184, v184, 2, s65\n"
    "v_lshl_add_u32 v185, v185, 2, s90\n"
    "v_lshl_add_u32 v186, v186, 2, s91\n"
    "v_lshl_add_u32 v187, v187, 2, s92\n"
    "v_mov_b32 v191, -1\n"
    "v_lshl_add_u32 v190, v249, 1, s93\n"
    "ds_write2_b32 v190, v191, v191 offset1:64\n"
    "s_mov_b64 exec, s[20:21]\n"
    "ds_write_b32 v184, v188\n"
    "s_mov_b64 exec, s[22:23]\n"
    "v_add_u32 v189, 64, v188\n"
    "ds_write_b32 v185, v189\n"
    "s_mov_b64 exec, s[24:25]\n"
    "v_add_u32 v190, 128, v188\n"
    "ds_write_b32 v186, v190\n"
    "s_mov_b64 exec, s[26:27]\n"
    "v_add_u32 v189, 192, v188\n"
    "ds_write_b32 v187, v189\n"
    "s_mov_b64 exec, -1\n"
    "s_sub_u32 s95, s93, -60\n"
    "s_add_u32 s94, s82, 128\n"
    "s_andn2_b32 s95, s95, 63\n"
    "s_max_u32 s83, s95, s94\n"
    ".Ldin_A1_nl:\n"
    "v_pk_add_f32 v[230:231], v[144:145], v[146:147]\n"
    "v_pk_mul_f32 v[232:233], v[144:145], v[146:147]\n"
    "v_pk_fma_f32 v[148:149], v[148:149], s[70:71], v[100:101]\n"
    "v_pk_fma_f32 v[230:231], v[230:231], v[148:149], v[232:233]\n"
    "v_pk_mul_f32 v[232:233], v[232:233], v[148:149]\n"
    "v_pk_fma_f32 v[150:151], v[150:151], s[70:71], v[102:103]\n"
    "v_pk_fma_f32 v[230:231], v[230:231], v[150:151], v[232:233]\n"
    "v_pk_mul_f32 v[232:233], v[232:233], v[150:151]\n"
    "v_pk_fma_f32 v[152:153], v[152:153], s[70:71], v[104:105]\n"
    "v_pk_fma_f32 v[230:231], v[230:231], v[152:153], v[232:233]\n"
    "v_pk_mul_f32 v[232:233], v[232:233], v[152:153]\n"
    "v_pk_fma_f32 v[154:155], v[154:155], s[70:71], v[106:107]\n"
    "v_pk_fma_f32 v[230:231], v[230:231], v[154:155], v[232:233]\n"
    "v_pk_mul_f32 v[232:233], v[232:233], v[154:155]\n"
    "v_pk_fma_f32 v[156:157], v[156:157], s[70:71], v[108:109]\n"
    "v_pk_fma_f32 v[230:231], v[230:231], v[156:157], v[232:233]\n"
    "v_pk_mul_f32 v[232:233], v[232:233], v[156:157]\n"
    "v_pk_fma_f32 v[158:159], v[158:159], s[70:71], v[110:111]\n"
    "v_pk_fma_f32 v[230:231], v[230:231], v[158:159], v[232:233]\n"
    "v_pk_mul_f32 v[232:233], v[232:233], v[158:159]\n"
    "v_rcp_f32 v232, v232\n"
    "v_rcp_f32 v233, v233\n"
    "v_fma_f32 v230, v230, v232, v247\n"
    "v_fmac_f32 v230, v231, v233\n"
    "v_cvt_pk_bf16_f32 v230, v230, v230\n"
    "s_add_u32 s67, s67, 64\n"
    "s_waitcnt lgkmcnt(0)\n"
    "ds_write_b16 v245, v230\n"
    "ds_read_b64 v[228:229], v246\n"
    "buffer_load_dwordx4 v[236:237], s[40:43], 0 idxen offen offset:0 nt lds\n"
    "buffer_load_dwordx4 v[238:239], s[40:43], 0 idxen offen offset:1024 nt lds\n"
    ".Ldin_B2:\n"
    "v_mfma_f32_16x16x32_bf16 v[144:147], v[0:3], v[160:163], v[112:115]\n"
    "v_exp_f32 v128, v128 clamp\n"
    "v_mfma_f32_16x16x32_bf16 v[144:147], v[4:7], v[164:167], v[144:147]\n"
    "v_exp_f32 v129, v129 clamp\n"
    "v_mfma_f32_16x16x32_bf16 v[148:151], v[8:11], v[160:163], v[116:119]\n"
    "v_exp_f32 v130, v130 clamp\n"
    "v_mfma_f32_16x16x32_bf16 v[148:151], v[12:15], v[164:167], v[148:151]\n"
    "v_exp_f32 v131, v131 clamp\n"
    "v_mfma_f32_16x16x32_bf16 v[152:155], v[16:19], v[160:163], v[120:123]\n"
    "v_exp_f32 v132, v132 clamp\n"
    "v_mfma_f32_16x16x32_bf16 v[152:155], v[20:23], v[164:167], v[152:155]\n"
    "v_exp_f32 v133, v133 clamp\n"
    "v_mfma_f32_16x16x32_bf16 v[156:159], v[24:27], v[160:163], v[124:127]\n"
    "v_exp_f32 v134, v134 clamp\n"
    "v_mfma_f32_16x16x32_bf16 v[156:159], v[28:31], v[164:167], v[156:159]\n"
    "v_exp_f32 v135, v135 clamp\n"
    "v_exp_f32 v136, v136 clamp\n"
    "s_cmp_eq_u32 s67, s80\n"
    "s_cselect_b32 s67, s65, s67\n"
    "s_and_b32 s94, s59, 63\n"
    "s_mul_i32 s94, s94, 272\n"
    "v_add_u32 v252, s94, v250\n"
    "s_add_u32 m0, s68, 6144\n"
    "v_exp_f32 v137, v137 clamp\n"
    "v_add_u32 v253, s67, v244\n"
    "s_waitcnt lgkmcnt(0)\n"
    "v_mfma_f32_4x4x4_16b_bf16 v[224:227], v[228:229], v[176:177], 0 cbsz:4 abid:0\n"
    "v_exp_f32 v138, v138 clamp\n"
    "v_exp_f32 v139, v139 clamp\n"
    "v_mfma_f32_4x4x4_16b_bf16 v[224:227], v[228:229], v[178:179], v[224:227] cbsz:4 abid:1\n"
    "v_exp_f32 v140, v140 clamp\n"
    "v_exp_f32 v141, v141 clamp\n"
    "v_mfma_f32_4x4x4_16b_bf16 v[224:227], v[228:229], v[180:181], v[224:227] cbsz:4 abid:2\n"
    "v_exp_f32 v142, v142 clamp\n"
    "v_exp_f32 v143, v143 clamp\n"
    "v_mfma_f32_4x4x4_16b_bf16 v[224:227], v[228:229], v[182:183], v[224:227] cbsz:4 abid:3\n"
    "s_waitcnt vmcnt(4)\n"
    "s_memrealtime s[34:35]\n"
    "ds_read_b64_tr_b16 v[176:177], v240 offset:6144\n"
    "ds_read_b64_tr_b16 v[178:179], v241 offset:6400\n"
    "ds_read_b64_tr_b16 v[180:181], v242 offset:6656\n"
    "ds_read_b64_tr_b16 v[182:183], v243 offset:6912\n"
    "ds_read_b32 v236, v253 offset:0\n"
    "ds_read_b32 v238, v253 offset:4\n"
    "ds_read_b128 v[160:163], v234 offset:0\n"
    "ds_read_b128 v[164:167], v235 offset:0\n"
    "v_add_u32_e64 v184, v248, v252\n"
    "ds_read_b64 v[184:185], v184\n"
    "v_pk_fma_f32 v[128:129], v[128:129], s[70:71], v[96:97]\n"
    "v_pk_fma_f32 v[130:131], v[130:131], s[70:71], v[98:99]\n"
    "v_pk_add_f32 v[230:231], v[128:129], v[130:131]\n"
    "v_pk_mul_f32 v[232:233], v[128:129], v[130:131]\n"
    "v_pk_fma_f32 v[132:133], v[132:133], s[70:71], v[100:101]\n"
    "v_pk_fma_f32 v[230:231], v[230:231], v[132:133], v[232:233]\n"
    "v_pk_mul_f32 v[232:233], v[232:233], v[132:133]\n"
    "v_pk_fma_f32 v[134:135], v[134:135], s[70:71], v[102:103]\n"
    "v_pk_fma_f32 v[230:231], v[230:231], v[134:135], v[232:233]\n"
    "v_pk_mul_f32 v[232:233], v[232:233], v[134:135]\n"
    "v_pk_fma_f32 v[136:137], v[136:137], s[70:71], v[104:105]\n"
    "v_pk_fma_f32 v[230:231], v[230:231], v[136:137], v[232:233]\n"
    "v_pk_mul_f32 v[232:233], v[232:233], v[136:137]\n"
    "v_pk_fma_f32 v[138:139], v[138:139], s[70:71], v[106:107]\n"
    "v_pk_fma_f32 v[230:231], v[230:231], v[138:139], v[232:233]\n"
    "v_pk_mul_f32 v[232:233], v[232:233], v[138:139]\n"
    "v_pk_fma_f32 v[140:141], v[140:141], s[70:71], v[108:109]\n"
    "v_pk_fma_f32 v[230:231], v[230:231], v[140:141], v[232:233]\n"
    "v_pk_mul_f32 v[232:233], v[232:233], v[140:141]\n"
    "v_pk_fma_f32 v[142:143], v[142:143], s[70:71], v[110:111]\n"
    "v_pk_fma_f32 v[230:231], v[230:231], v[142:143], v[232:233]\n"
    "v_pk_mul_f32 v[232:233], v[232:233], v[142:143]\n"
    "v_rcp_f32 v232, v232\n"
    "v_rcp_f32 v233, v233\n"
    "v_fma_f32 v230, v230, v232, v247\n"
    "v_fmac_f32 v230, v231, v233\n"
    "v_cvt_pk_bf16_f32 v230, v230, v230\n"
    "s_add_u32 s67, s67, 64\n"
    "s_waitcnt lgkmcnt(0)\n"
    "ds_write_b16 v245, v230\n"
    "ds_read_b64 v[228:229], v246\n"
    "v_and_b32 v184, v254, v184\n"
    "v_and_b32 v185, v255, v185\n"
    "s_cmp_le_u32 s34, s76\n"
    "s_cbranch_scc1 .Ldin_B2_wait\n"
    ".Ldin_B2_go:\n"
    "s_sub_u32 s94, s34, 60\n"
    "s_max_u32 s76, s76, s94\n"
    "s_add_u32 s76, s76, s72\n"
    ".Ldin_B2_dma:\n"
    "buffer_load_dwordx4 v[236:237], s[40:43], 0 idxen offen offset:0 nt lds\n"
    "buffer_load_dwordx4 v[238:239], s[40:43], 0 idxen offen offset:1024 nt lds\n"
    ".Ldin_B2_nd:\n"
    "s_branch .Ldin_G3\n"
    ".Ldin_L0_poll:\n"
    "s_sleep 1\n"
    "ds_read_b64 v[232:233], v253\n"
    "s_waitcnt lgkmcnt(0)\n"
    "v_readfirstlane_b32 s60, v232\n"
    "s_cmp_eq_u32 s60, -1\n"
    "s_cbranch_scc1 .Ldin_L0_poll\n"
    "s_branch .Ldin_L0_pr\n"
    ".Ldin_B2_wait:\n"
    "s_cmp_lt_i32 s59, s57\n"
    "s_cbranch_scc1 .Ldin_B2_w2\n"
    "s_sub_u32 s94, s67, s65\n"
    "s_cmp_gt_u32 s94, 256\n"
    "s_cbranch_scc1 .Ldin_B2_dma\n"
    "s_branch .Ldin_T3\n"
    ".Ldin_B2_slp:\n"
    "s_sleep 1\n"
    ".Ldin_B2_w2:\n"
    "s_memrealtime s[34:35]\n"
    "s_waitcnt lgkmcnt(0)\n"
    "s_sub_u32 s94, s34, s76\n"
    "s_cmp_lt_i32 s94, 0\n"
    "s_cbranch_scc1 .Ldin_B2_slp\n"
    "s_branch .Ldin_B2_go\n"
    ".p2align 5\n"
    ".Ldin_L3:\n"
    "s_setprio 0\n"
    "s_add_u32 s76, s76, s73\n"
    "v_mov_b32_e64 v253, s63\n"
    "ds_read_b64 v[232:233], v253\n"
    "v_mfma_f32_4x4x4_16b_bf16 v[128:131], v[184:185], v[32:33], v[64:67] cbsz:2 abid:0\n"
    "ds_read_b128 v[112:115], v252 offset:17408\n"
    "v_mfma_f32_4x4x4_16b_bf16 v[132:135], v[184:185], v[34:35], v[68:71] cbsz:2 abid:1\n"
    "ds_read_b128 v[116:119], v252 offset:17424\n"
    "v_mfma_f32_4x4x4_16b_bf16 v[136:139], v[184:185], v[36:37], v[72:75] cbsz:2 abid:2\n"
    "ds_read_b128 v[120:123], v252 offset:17536\n"
    "ds_read_b128 v[124:127], v252 offset:17552\n"
    "v_cvt_pk_bf16_f32 v0, v128, v129\n"
    "v_cvt_pk_bf16_f32 v1, v130, v131\n"
    "v_mfma_f32_4x4x4_16b_bf16 v[140:143], v[184:185], v[38:39], v[76:79] cbsz:2 abid:3\n"
    "v_cvt_pk_bf16_f32 v2, v132, v133\n"
    "v_cvt_pk_bf16_f32 v3, v134, v135\n"
    "v_mfma_f32_4x4x4_16b_bf16 v[128:131], v[184:185], v[40:41], v[80:83] cbsz:2 abid:0\n"
    "v_cvt_pk_bf16_f32 v4, v136, v137\n"
    "v_cvt_pk_bf16_f32 v5, v138, v139\n"
    "v_mfma_f32_4x4x4_16b_bf16 v[132:135], v[184:185], v[42:43], v[84:87] cbsz:2 abid:1\n"
    "v_cvt_pk_bf16_f32 v6, v140, v141\n"
    "v_cvt_pk_bf16_f32 v7, v142, v143\n"
    "v_mfma_f32_4x4x4_16b_bf16 v[136:139], v[184:185], v[44:45], v[88:91] cbsz:2 abid:2\n"
    "v_cvt_pk_bf16_f32 v8, v128, v129\n"
    "v_cvt_pk_bf16_f32 v9, v130, v131\n"
    "v_mfma_f32_4x4x4_16b_bf16 v[140:143], v[184:185], v[46:47], v[92:95] cbsz:2 abid:3\n"
    "v_cvt_pk_bf16_f32 v10, v132, v133\n"
    "v_cvt_pk_bf16_f32 v11, v134, v135\n"
    "v_mfma_f32_4x4x4_16b_bf16 v[128:131], v[184:185], v[48:49], v[192:195] cbsz:2 abid:0\n"
    "v_cvt_pk_bf16_f32 v12, v136, v137\n"
    "v_cvt_pk_bf16_f32 v13, v138, v139\n"
    "v_mfma_f32_4x4x4_16b_bf16 v[132:135], v[184:185], v[50:51], v[196:199] cbsz:2 abid:1\n"
    "v_cvt_pk_bf16_f32 v14, v140, v141\n"
    "v_cvt_pk_bf16_f32 v15, v142, v143\n"
    "v_mfma_f32_4x4x4_16b_bf16 v[136:139], v[184:185], v[52:53], v[200:203] cbsz:2 abid:2\n"
    "s_waitcnt lgkmcnt(0)\n"
    "v_readfirstlane_b32 s60, v232\n"
    "v_cvt_pk_bf16_f32 v16, v128, v129\n"
    "v_cvt_pk_bf16_f32 v17, v130, v131\n"
    "v_mfma_f32_4x4x4_16b_bf16 v[140:143], v[184:185], v[54:55], v[204:207] cbsz:2 abid:3\n"
    "s_cmp_eq_u32 s60, -1\n"
    "s_cbranch_scc1 .Ldin_L3_poll\n"
    ".Ldin_L3_pr:\n"
    "v_readfirstlane_b32 s95, v233\n"
    "s_and_b32 s94, s60, 63\n"
    "s_lshl_b32 s94, s94, 6\n"
    "s_add_u32 s94, s94, 68480\n"
    "s_add_u32 s63, s63, 8\n"
    "v_lshl_add_u32 v252, v249, 1, s94\n"
    "ds_read_b32 v252, v252\n"
    "s_sub_u32 s95, s95, s76\n"
    "s_ashr_i32 s95, s95, 4\n"
    "s_min_i32 s95, s95, s74\n"
    "s_max_i32 s72, s95, s75\n"
    "v_cvt_pk_bf16_f32 v18, v132, v133\n"
    "v_cvt_pk_bf16_f32 v19, v134, v135\n"
    "v_mfma_f32_4x4x4_16b_bf16 v[128:131], v[184:185], v[56:57], v[208:211] cbsz:2 abid:0\n"
    "v_cvt_pk_bf16_f32 v20, v136, v137\n"
    "v_cvt_pk_bf16_f32 v21, v138, v139\n"
    "v_mfma_f32_4x4x4_16b_bf16 v[132:135], v[184:185], v[58:59], v[212:215] cbsz:2 abid:1\n"
    "v_cvt_pk_bf16_f32 v22, v140, v141\n"
    "v_cvt_pk_bf16_f32 v23, v142, v143\n"
    "v_mfma_f32_4x4x4_16b_bf16 v[136:139], v[184:185], v[60:61], v[216:219] cbsz:2 abid:2\n"
    "v_cvt_pk_bf16_f32 v24, v128, v129\n"
    "v_cvt_pk_bf16_f32 v25, v130, v131\n"
    "v_mfma_f32_4x4x4_16b_bf16 v[140:143], v[184:185], v[62:63], v[220:223] cbsz:2 abid:3\n"
    "v_cvt_pk_bf16_f32 v26, v132, v133\n"
    "v_cvt_pk_bf16_f32 v27, v134, v135\n"
    "v_cvt_pk_bf16_f32 v28, v136, v137\n"
    "v_cvt_pk_bf16_f32 v29, v138, v139\n"
    "v_exp_f32 v152, v152 clamp\n"
    "v_cvt_pk_bf16_f32 v30, v140, v141\n"
    "v_cvt_pk_bf16_f32 v31, v142, v143\n"
    "v_mfma_f32_16x16x32_bf16 v[128:131], v[0:3], v[160:163], v[112:115]\n"
    "v_exp_f32 v144, v144 clamp\n"
    "v_mfma_f32_16x16x32_bf16 v[128:131], v[4:7], v[164:167], v[128:131]\n"
    "v_exp_f32 v145, v145 clamp\n"
    "v_mfma_f32_16x16x32_bf16 v[132:135], v[8:11], v[160:163], v[116:119]\n"
    "v_exp_f32 v146, v146 clamp\n"
    "v_mfma_f32_16x16x32_bf16 v[132:135], v[12:15], v[164:167], v[132:135]\n"
    "v_exp_f32 v147, v147 clamp\n"
    "v_mfma_f32_16x16x32_bf16 v[136:139], v[16:19], v[160:163], v[120:123]\n"
    "v_exp_f32 v148, v148 clamp\n"
    "v_mfma_f32_16x16x32_bf16 v[136:139], v[20:23], v[164:167], v[136:139]\n"
    "v_exp_f32 v149, v149 clamp\n"
    "v_mfma_f32_16x16x32_bf16 v[140:143], v[24:27], v[160:163], v[124:127]\n"
    "v_exp_f32 v150, v150 clamp\n"
    "v_mfma_f32_16x16x32_bf16 v[140:143], v[28:31], v[164:167], v[140:143]\n"
    "v_exp_f32 v151, v151 clamp\n"
    "s_add_u32 m0, s68, 0\n"
    "v_exp_f32 v153, v153 clamp\n"
    "v_add_u32_e64 v253, s67, v244\n"
    "v_mfma_f32_4x4x4_16b_bf16 v[224:227], v[228:229], v[168:169], v[224:227] cbsz:4 abid:0\n"
    "v_exp_f32 v154, v154 clamp\n"
    "v_exp_f32 v155, v155 clamp\n"
    "v_mfma_f32_4x4x4_16b_bf16 v[224:227], v[228:229], v[170:171], v[224:227] cbsz:4 abid:1\n"
    "v_exp_f32 v156, v156 clamp\n"
    "v_exp_f32 v157, v157 clamp\n"
    "v_mfma_f32_4x4x4_16b_bf16 v[224:227], v[228:229], v[172:173], v[224:227] cbsz:4 abid:2\n"
    "v_exp_f32 v158, v158 clamp\n"
    "v_exp_f32 v159, v159 clamp\n"
    "v_mfma_f32_4x4x4_16b_bf16 v[224:227], v[228:229], v[174:175], v[224:227] cbsz:4 abid:3\n"
    "s_waitcnt vmcnt(4)\n"
    "ds_read_b64_tr_b16 v[168:169], v240 offset:0\n"
    "ds_read_b64_tr_b16 v[170:171], v241 offset:256\n"
    "ds_read_b64_tr_b16 v[172:173], v242 offset:512\n"
    "ds_read_b64_tr_b16 v[174:175], v243 offset:768\n"
    "ds_read_b32 v236, v253 offset:0\n"
    "ds_read_b32 v238, v253 offset:4\n"
    "ds_read_b128 v[160:163], v234 offset:2048\n"
    "ds_read_b128 v[164:167], v235 offset:2048\n"
    "s_waitcnt lgkmcnt(8)\n"
    "s_nop 0\n"
    "v_readlane_b32 s20, v252, 0\n"
    "v_readlane_b32 s21, v252, 1\n"
    "v_readlane_b32 s22, v252, 2\n"
    "v_readlane_b32 s23, v252, 3\n"
    "v_readlane_b32 s24, v252, 4\n"
    "v_readlane_b32 s25, v252, 5\n"
    "v_readlane_b32 s26, v252, 6\n"
    "v_readlane_b32 s27, v252, 7\n"
    "v_pk_fma_f32 v[144:145], v[144:145], s[70:71], v[96:97]\n"
    "v_pk_fma_f32 v[146:147], v[146:147], s[70:71], v[98:99]\n"
    "v_pk_add_f32 v[230:231], v[144:145], v[146:147]\n"
    "v_pk_mul_f32 v[232:233], v[144:145], v[146:147]\n"
    "v_pk_fma_f32 v[148:149], v[148:149], s[70:71], v[100:101]\n"
    "v_pk_fma_f32 v[230:231], v[230:231], v[148:149], v[232:233]\n"
    "v_pk_mul_f32 v[232:233], v[232:233], v[148:149]\n"
    "v_pk_fma_f32 v[150:151], v[150:151], s[70:71], v[102:103]\n"
    "v_pk_fma_f32 v[230:231], v[230:231], v[150:151], v[232:233]\n"
    "v_pk_mul_f32 v[232:233], v[232:233], v[150:151]\n"
    "v_pk_fma_f32 v[152:153], v[152:153], s[70:71], v[104:105]\n"
    "v_pk_fma_f32 v[230:231], v[230:231], v[152:153], v[232:233]\n"
    "v_pk_mul_f32 v[232:233], v[232:233], v[152:153]\n"
    "v_pk_fma_f32 v[154:155], v[154:155], s[70:71], v[106:107]\n"
    "v_pk_fma_f32 v[230:231], v[230:231], v[154:155], v[232:233]\n"
    "v_pk_mul_f32 v[232:233], v[232:233], v[154:155]\n"
    "v_pk_fma_f32 v[156:157], v[156:157], s[70:71], v[108:109]\n"
    "v_pk_fma_f32 v[230:231], v[230:231], v[156:157], v[232:233]\n"
    "v_pk_mul_f32 v[232:233], v[232:233], v[156:157]\n"
    "v_pk_fma_f32 v[158:159], v[158:159], s[70:71], v[110:111]\n"
    "v_pk_fma_f32 v[230:231], v[230:231], v[158:159], v[232:233]\n"
    "v_pk_mul_f32 v[232:233], v[232:233], v[158:159]\n"
    "v_rcp_f32 v232, v232\n"
    "v_rcp_f32 v233, v233\n"
    "v_fma_f32 v230, v230, v232, v247\n"
    "v_fmac_f32 v230, v231, v233\n"
    "v_cvt_pk_bf16_f32 v230, v230, v230\n"
    "s_add_u32 s67, s67, 64\n"
    "s_waitcnt lgkmcnt(0)\n"
    "ds_write_b16 v245, v230\n"
    "ds_read_b64 v[228:229], v246\n"
    "s_lshl1_add_u32 s76, s72, s76\n"
    "buffer_load_dwordx4 v[236:237], s[40:43], 0 idxen offen offset:0 nt lds\n"
    "buffer_load_dwordx4 v[238:239], s[40:43], 0 idxen offen offset:1024 nt lds\n"
    "s_and_b32 s62, s58, 63\n"
    "s_mul_i32 s62, s62, 272\n"
    "s_mov_b32 s58, s59\n"
    "s_mov_b32 s59, s60\n"
    "s_mov_b32 s81, s65\n"
    "s_xor_b32 s65, s65, s64\n"
    "s_mov_b32 s80, s83\n"
    "s_add_u32 s82, s65, 256\n"
    "s_cmp_lt_i32 s59, s57\n"
    "s_cselect_b32 s76, s76, -1\n"
    "s_bitcmp1_b32 s58, 30\n"
    "s_cbranch_scc1 .Ldin_A0s\n"
    "s_branch .Ldin_A0\n"
    ".Ldin_L3_poll:\n"
    "s_sleep 1\n"
    "ds_read_b64 v[232:233], v253\n"
    "s_waitcnt lgkmcnt(0)\n"
    "v_readfirstlane_b32 s60, v232\n"
    "s_cmp_eq_u32 s60, -1\n"
    "s_cbranch_scc1 .Ldin_L3_poll\n"
    "s_branch .Ldin_L3_pr\n"
    ".p2align 5\n"
    ".Ldin_stub_8:\n"
    "s_cbranch_execz .Ldin_t2_ret\n"
    "s_nop 0\n"
    ".Ldin_T0:\n"
    "v_mfma_f32_16x16x32_bf16 v[144:147], v[0:3], v[160:163], v[112:115]\n"
    "v_exp_f32 v128, v128 clamp\n"
    "v_mfma_f32_16x16x32_bf16 v[144:147], v[4:7], v[164:167], v[144:147]\n"
    "v_exp_f32 v129, v129 clamp\n"
    "v_mfma_f32_16x16x32_bf16 v[148:151], v[8:11], v[160:163], v[116:119]\n"
    "v_exp_f32 v130, v130 clamp\n"
    "v_mfma_f32_16x16x32_bf16 v[148:151], v[12:15], v[164:167], v[148:151]\n"
    "v_exp_f32 v131, v131 clamp\n"
    "v_mfma_f32_16x16x32_bf16 v[152:155], v[16:19], v[160:163], v[120:123]\n"
    "v_exp_f32 v132, v132 clamp\n"
    "v_mfma_f32_16x16x32_bf16 v[152:155], v[20:23], v[164:167], v[152:155]\n"
    "v_exp_f32 v133, v133 clamp\n"
    "v_mfma_f32_16x16x32_bf16 v[156:159], v[24:27], v[160:163], v[124:127]\n"
    "v_exp_f32 v134, v134 clamp\n"
    "v_mfma_f32_16x16x32_bf16 v[156:159], v[28:31], v[164:167], v[156:159]\n"
    "v_exp_f32 v135, v135 clamp\n"
    "v_exp_f32 v136, v136 clamp\n"
    "v_exp_f32 v137, v137 clamp\n"
    "s_waitcnt lgkmcnt(0)\n"
    "v_mfma_f32_4x4x4_16b_bf16 v[224:227], v[228:229], v[176:177], v[224:227] cbsz:4 abid:0\n"
    "v_exp_f32 v138, v138 clamp\n"
    "v_exp_f32 v139, v139 clamp\n"
    "v_mfma_f32_4x4x4_16b_bf16 v[224:227], v[228:229], v[178:179], v[224:227] cbsz:4 abid:1\n"
    "v_exp_f32 v140, v140 clamp\n"
    "v_exp_f32 v141, v141 clamp\n"
    "v_mfma_f32_4x4x4_16b_bf16 v[224:227], v[228:229], v[180:181], v[224:227] cbsz:4 abid:2\n"
    "v_exp_f32 v142, v142 clamp\n"
    "v_exp_f32 v143, v143 clamp\n"
    "v_mfma_f32_4x4x4_16b_bf16 v[224:227], v[228:229], v[182:183], v[224:227] cbsz:4 abid:3\n"
    "ds_read_b64_tr_b16 v[176:177], v240 offset:2048\n"
    "ds_read_b64_tr_b16 v[178:179], v241 offset:2304\n"
    "ds_read_b64_tr_b16 v[180:181], v242 offset:2560\n"
    "ds_read_b64_tr_b16 v[182:183], v243 offset:2816\n"
    "v_add_u32 v253, s66, v244\n"
    "ds_read_b32 v253, v253\n"
    "v_lshlrev_b32_e64 v202, 3, v249\n"
    "v_and_b32 v202, 0x70, v202\n"
    "s_mov_b32 s86, s69\n"
    "s_mov_b32 s87, s69\n"
    "v_pk_fma_f32 v[128:129], v[128:129], s[70:71], v[96:97]\n"
    "v_pk_fma_f32 v[130:131], v[130:131], s[70:71], v[98:99]\n"
    "v_pk_add_f32 v[230:231], v[128:129], v[130:131]\n"
    "v_pk_mul_f32 v[232:233], v[128:129], v[130:131]\n"
    "v_pk_fma_f32 v[132:133], v[132:133], s[70:71], v[100:101]\n"
    "v_pk_fma_f32 v[230:231], v[230:231], v[132:133], v[232:233]\n"
    "v_pk_mul_f32 v[232:233], v[232:233], v[132:133]\n"
    "v_pk_fma_f32 v[134:135], v[134:135], s[70:71], v[102:103]\n"
    "s_waitcnt lgkmcnt(0)\n"
    "v_and_b32 v201, 63, v253\n"
    "v_and_b32 v200, 0x3fffffff, v253\n"
    "v_mul_u32_u24 v201, 272, v201\n"
    "v_cmp_gt_u32_e64 s[88:89], s58, v200\n"
    "v_lshl_add_u32 v201, v202, 1, v201\n"
    "v_and_b32 v200, 0x3fffffc0, v200\n"
    "ds_read_b128 v[208:211], v201 offset:32768\n"
    "ds_read_b128 v[212:215], v201 offset:32784\n"
    "v_lshl_add_u32 v200, v200, 1, v202\n"
    "v_bfrev_b32 v202, 1\n"
    "s_nop 0\n"
    "v_cndmask_b32_e64 v200, v202, v200, s[88:89]\n"
    "v_pk_fma_f32 v[230:231], v[230:231], v[134:135], v[232:233]\n"
    "v_pk_mul_f32 v[232:233], v[232:233], v[134:135]\n"
    "v_pk_fma_f32 v[136:137], v[136:137], s[70:71], v[104:105]\n"
    "v_pk_fma_f32 v[230:231], v[230:231], v[136:137], v[232:233]\n"
    "v_pk_mul_f32 v[232:233], v[232:233], v[136:137]\n"
    "v_pk_fma_f32 v[138:139], v[138:139], s[70:71], v[106:107]\n"
    "v_pk_fma_f32 v[230:231], v[230:231], v[138:139], v[232:233]\n"
    "v_pk_mul_f32 v[232:233], v[232:233], v[138:139]\n"
    "v_pk_fma_f32 v[140:141], v[140:141], s[70:71], v[108:109]\n"
    "v_pk_fma_f32 v[230:231], v[230:231], v[140:141], v[232:233]\n"
    "v_pk_mul_f32 v[232:233], v[232:233], v[140:141]\n"
    "v_pk_fma_f32 v[142:143], v[142:143], s[70:71], v[110:111]\n"
    "v_pk_fma_f32 v[230:231], v[230:231], v[142:143], v[232:233]\n"
    "v_pk_mul_f32 v[232:233], v[232:233], v[142:143]\n"
    "s_waitcnt lgkmcnt(0)\n"
    "s_nop 0\n"
    "v_pk_mul_f32 v[208:209], v[208:209], s[86:87]\n"
    "v_pk_mul_f32 v[210:211], v[210:211], s[86:87]\n"
    "v_pk_mul_f32 v[212:213], v[212:213], s[86:87]\n"
    "v_pk_mul_f32 v[214:215], v[214:215], s[86:87]\n"
    "v_cvt_pk_bf16_f32 v216, v208, v209\n"
    "v_cvt_pk_bf16_f32 v217, v210, v211\n"
    "v_cvt_pk_bf16_f32 v218, v212, v213\n"
    "v_cvt_pk_bf16_f32 v219, v214, v215\n"
    "buffer_store_dwordx4 v[216:219], v200, s[52:55], 0 offen sc0 sc1 nt\n"
    "s_waitcnt vmcnt(3)\n"
    "ds_read_b128 v[160:163], v234 offset:4096\n"
    "ds_read_b128 v[164:167], v235 offset:4096\n"
    "v_rcp_f32 v232, v232\n"
    "v_rcp_f32 v233, v233\n"
    "v_fma_f32 v230, v230, v232, v247\n"
    "v_fmac_f32 v230, v231, v233\n"
    "v_cvt_pk_bf16_f32 v230, v230, v230\n"
    "ds_write_b16 v245, v230\n"
    "ds_read_b64 v[228:229], v246\n"
    "v_exp_f32 v152, v152 clamp\n"
    "s_waitcnt lgkmcnt(2)\n"
    "v_mfma_f32_16x16x32_bf16 v[128:131], v[0:3], v[160:163], v[112:115]\n"
    "v_exp_f32 v144, v144 clamp\n"
    "v_mfma_f32_16x16x32_bf16 v[128:131], v[4:7], v[164:167], v[128:131]\n"
    "v_exp_f32 v145, v145 clamp\n"
    "v_mfma_f32_16x16x32_bf16 v[132:135], v[8:11], v[160:163], v[116:119]\n"
    "v_exp_f32 v146, v146 clamp\n"
    "v_mfma_f32_16x16x32_bf16 v[132:135], v[12:15], v[164:167], v[132:135]\n"
    "v_exp_f32 v147, v147 clamp\n"
    "v_mfma_f32_16x16x32_bf16 v[136:139], v[16:19], v[160:163], v[120:123]\n"
    "v_exp_f32 v148, v148 clamp\n"
    "v_mfma_f32_16x16x32_bf16 v[136:139], v[20:23], v[164:167], v[136:139]\n"
    "v_exp_f32 v149, v149 clamp\n"
    "v_mfma_f32_16x16x32_bf16 v[140:143], v[24:27], v[160:163], v[124:127]\n"
    "v_exp_f32 v150, v150 clamp\n"
    "v_mfma_f32_16x16x32_bf16 v[140:143], v[28:31], v[164:167], v[140:143]\n"
    "v_exp_f32 v151, v151 clamp\n"
    "v_exp_f32 v153, v153 clamp\n"
    "s_waitcnt lgkmcnt(0)\n"
    "v_mfma_f32_4x4x4_16b_bf16 v[224:227], v[228:229], v[168:169], v[224:227] cbsz:4 abid:0\n"
    "v_exp_f32 v154, v154 clamp\n"
    "v_exp_f32 v155, v155 clamp\n"
    "v_mfma_f32_4x4x4_16b_bf16 v[224:227], v[228:229], v[170:171], v[224:227] cbsz:4 abid:1\n"
    "v_exp_f32 v156, v156 clamp\n"
    "v_exp_f32 v157, v157 clamp\n"
    "v_mfma_f32_4x4x4_16b_bf16 v[224:227], v[228:229], v[172:173], v[224:227] cbsz:4 abid:2\n"
    "v_exp_f32 v158, v158 clamp\n"
    ".Ldin_stub_9:\n"
    "s_cbranch_execz .Ldin_t2_ret\n"
    "s_nop 0\n"
    "v_exp_f32 v159, v159 clamp\n"
    "v_mfma_f32_4x4x4_16b_bf16 v[224:227], v[228:229], v[174:175], v[224:227] cbsz:4 abid:3\n"
    "ds_read_b64_tr_b16 v[168:169], v240 offset:4096\n"
    "ds_read_b64_tr_b16 v[170:171], v241 offset:4352\n"
    "ds_read_b64_tr_b16 v[172:173], v242 offset:4608\n"
    "ds_read_b64_tr_b16 v[174:175], v243 offset:4864\n"
    "v_pk_fma_f32 v[144:145], v[144:145], s[70:71], v[96:97]\n"
    "v_pk_fma_f32 v[146:147], v[146:147], s[70:71], v[98:99]\n"
    "v_pk_add_f32 v[230:231], v[144:145], v[146:147]\n"
    "v_pk_mul_f32 v[232:233], v[144:145], v[146:147]\n"
    "v_pk_fma_f32 v[148:149], v[148:149], s[70:71], v[100:101]\n"
    "v_pk_fma_f32 v[230:231], v[230:231], v[148:149], v[232:233]\n"
    "v_pk_mul_f32 v[232:233], v[232:233], v[148:149]\n"
    "v_pk_fma_f32 v[150:151], v[150:151], s[70:71], v[102:103]\n"
    "v_pk_fma_f32 v[230:231], v[230:231], v[150:151], v[232:233]\n"
    "v_pk_mul_f32 v[232:233], v[232:233], v[150:151]\n"
    "v_pk_fma_f32 v[152:153], v[152:153], s[70:71], v[104:105]\n"
    "v_pk_fma_f32 v[230:231], v[230:231], v[152:153], v[232:233]\n"
    "v_pk_mul_f32 v[232:233], v[232:233], v[152:153]\n"
    "v_pk_fma_f32 v[154:155], v[154:155], s[70:71], v[106:107]\n"
    "v_pk_fma_f32 v[230:231], v[230:231], v[154:155], v[232:233]\n"
    "v_pk_mul_f32 v[232:233], v[232:233], v[154:155]\n"
    "v_pk_fma_f32 v[156:157], v[156:157], s[70:71], v[108:109]\n"
    "v_pk_fma_f32 v[230:231], v[230:231], v[156:157], v[232:233]\n"
    "v_pk_mul_f32 v[232:233], v[232:233], v[156:157]\n"
    "v_pk_fma_f32 v[158:159], v[158:159], s[70:71], v[110:111]\n"
    "v_pk_fma_f32 v[230:231], v[230:231], v[158:159], v[232:233]\n"
    "v_pk_mul_f32 v[232:233], v[232:233], v[158:159]\n"
    "s_waitcnt vmcnt(1)\n"
    "ds_read_b128 v[160:163], v234 offset:6144\n"
    "ds_read_b128 v[164:167], v235 offset:6144\n"
    "v_rcp_f32 v232, v232\n"
    "v_rcp_f32 v233, v233\n"
    "v_fma_f32 v230, v230, v232, v247\n"
    "v_fmac_f32 v230, v231, v233\n"
    "v_cvt_pk_bf16_f32 v230, v230, v230\n"
    "ds_write_b16 v245, v230\n"
    "ds_read_b64 v[228:229], v246\n"
    "v_exp_f32 v136, v136 clamp\n"
    "s_waitcnt lgkmcnt(2)\n"
    "v_mfma_f32_16x16x32_bf16 v[144:147], v[0:3], v[160:163], v[112:115]\n"
    "v_exp_f32 v128, v128 clamp\n"
    "v_mfma_f32_16x16x32_bf16 v[144:147], v[4:7], v[164:167], v[144:147]\n"
    "v_exp_f32 v129, v129 clamp\n"
    "v_mfma_f32_16x16x32_bf16 v[148:151], v[8:11], v[160:163], v[116:119]\n"
    "v_exp_f32 v130, v130 clamp\n"
    "v_mfma_f32_16x16x32_bf16 v[148:151], v[12:15], v[164:167], v[148:151]\n"
    "v_exp_f32 v131, v131 clamp\n"
    "v_mfma_f32_16x16x32_bf16 v[152:155], v[16:19], v[160:163], v[120:123]\n"
    "v_exp_f32 v132, v132 clamp\n"
    "v_mfma_f32_16x16x32_bf16 v[152:155], v[20:23], v[164:167], v[152:155]\n"
    "v_exp_f32 v133, v133 clamp\n"
    "v_mfma_f32_16x16x32_bf16 v[156:159], v[24:27], v[160:163], v[124:127]\n"
    "v_exp_f32 v134, v134 clamp\n"
    "v_mfma_f32_16x16x32_bf16 v[156:159], v[28:31], v[164:167], v[156:159]\n"
    "v_exp_f32 v135, v135 clamp\n"
    "v_exp_f32 v137, v137 clamp\n"
    "s_waitcnt lgkmcnt(0)\n"
    "v_mfma_f32_4x4x4_16b_bf16 v[224:227], v[228:229], v[176:177], v[224:227] cbsz:4 abid:0\n"
    "v_exp_f32 v138, v138 clamp\n"
    "v_exp_f32 v139, v139 clamp\n"
    "v_mfma_f32_4x4x4_16b_bf16 v[224:227], v[228:229], v[178:179], v[224:227] cbsz:4 abid:1\n"
    "v_exp_f32 v140, v140 clamp\n"
    "v_exp_f32 v141, v141 clamp\n"
    "v_mfma_f32_4x4x4_16b_bf16 v[224:227], v[228:229], v[180:181], v[224:227] cbsz:4 abid:2\n"
    "v_exp_f32 v142, v142 clamp\n"
    "v_exp_f32 v143, v143 clamp\n"
    "v_mfma_f32_4x4x4_16b_bf16 v[224:227], v[228:229], v[182:183], v[224:227] cbsz:4 abid:3\n"
    "ds_read_b64_tr_b16 v[176:177], v240 offset:6144\n"
    "ds_read_b64_tr_b16 v[178:179], v241 offset:6400\n"
    "ds_read_b64_tr_b16 v[180:181], v242 offset:6656\n"
    "ds_read_b64_tr_b16 v[182:183], v243 offset:6912\n"
    "v_pk_fma_f32 v[128:129], v[128:129], s[70:71], v[96:97]\n"
    "v_pk_fma_f32 v[130:131], v[130:131], s[70:71], v[98:99]\n"
    "v_pk_add_f32 v[230:231], v[128:129], v[130:131]\n"
    "v_pk_mul_f32 v[232:233], v[128:129], v[130:131]\n"
    "v_pk_fma_f32 v[132:133], v[132:133], s[70:71], v[100:101]\n"
    "v_pk_fma_f32 v[230:231], v[230:231], v[132:133], v[232:233]\n"
    "v_pk_mul_f32 v[232:233], v[232:233], v[132:133]\n"
    "v_pk_fma_f32 v[134:135], v[134:135], s[70:71], v[102:103]\n"
    "v_pk_fma_f32 v[230:231], v[230:231], v[134:135], v[232:233]\n"
    "v_pk_mul_f32 v[232:233], v[232:233], v[134:135]\n"
    "v_pk_fma_f32 v[136:137], v[136:137], s[70:71], v[104:105]\n"
    "v_pk_fma_f32 v[230:231], v[230:231], v[136:137], v[232:233]\n"
    "v_pk_mul_f32 v[232:233], v[232:233], v[136:137]\n"
    "v_pk_fma_f32 v[138:139], v[138:139], s[70:71], v[106:107]\n"
    "v_pk_fma_f32 v[230:231], v[230:231], v[138:139], v[232:233]\n"
    "v_pk_mul_f32 v[232:233], v[232:233], v[138:139]\n"
    "v_pk_fma_f32 v[140:141], v[140:141], s[70:71], v[108:109]\n"
    "v_pk_fma_f32 v[230:231], v[230:231], v[140:141], v[232:233]\n"
    "v_pk_mul_f32 v[232:233], v[232:233], v[140:141]\n"
    "v_pk_fma_f32 v[142:143], v[142:143], s[70:71], v[110:111]\n"
    "v_pk_fma_f32 v[230:231], v[230:231], v[142:143], v[232:233]\n"
    "v_pk_mul_f32 v[232:233], v[232:233], v[142:143]\n"
    "v_rcp_f32 v232, v232\n"
    "v_rcp_f32 v233, v233\n"
    "v_fma_f32 v230, v230, v232, v247\n"
    "v_fmac_f32 v230, v231, v233\n"
    "v_cvt_pk_bf16_f32 v230, v230, v230\n"
    "ds_write_b16 v245, v230\n"
    "ds_read_b64 v[228:229], v246\n"
    "s_setprio 0\n"
    "s_branch .Ldin_F1\n"
    ".p2align 5\n"
    ".Ldin_T1:\n"
    "v_mfma_f32_16x16x32_bf16 v[128:131], v[0:3], v[160:163], v[112:115]\n"
    "v_exp_f32 v144, v144 clamp\n"
    "v_mfma_f32_16x16x32_bf16 v[128:131], v[4:7], v[164:167], v[128:131]\n"
    "v_exp_f32 v145, v145 clamp\n"
    "v_mfma_f32_16x16x32_bf16 v[132:135], v[8:11], v[160:163], v[116:119]\n"
    "v_exp_f32 v146, v146 clamp\n"
    "v_mfma_f32_16x16x32_bf16 v[132:135], v[12:15], v[164:167], v[132:135]\n"
    "v_exp_f32 v147, v147 clamp\n"
    "v_mfma_f32_16x16x32_bf16 v[136:139], v[16:19], v[160:163], v[120:123]\n"
    "v_exp_f32 v148, v148 clamp\n"
    "v_mfma_f32_16x16x32_bf16 v[136:139], v[20:23], v[164:167], v[136:139]\n"
    "v_exp_f32 v149, v149 clamp\n"
    "v_mfma_f32_16x16x32_bf16 v[140:143], v[24:27], v[160:163], v[124:127]\n"
    "v_exp_f32 v150, v150 clamp\n"
    "v_mfma_f32_16x16x32_bf16 v[140:143], v[28:31], v[164:167], v[140:143]\n"
    "v_exp_f32 v151, v151 clamp\n"
    "v_exp_f32 v152, v152 clamp\n"
    "v_exp_f32 v153, v153 clamp\n"
    "s_waitcnt lgkmcnt(0)\n"
    "v_mfma_f32_4x4x4_16b_bf16 v[224:227], v[228:229], v[168:169], v[224:227] cbsz:4 abid:0\n"
    "v_exp_f32 v154, v154 clamp\n"
    ".Ldin_stub_10:\n"
    "s_cbranch_execz .Ldin_t2_ret\n"
    "s_nop 0\n"
    "v_exp_f32 v155, v155 clamp\n"
    "v_mfma_f32_4x4x4_16b_bf16 v[224:227], v[228:229], v[170:171], v[224:227] cbsz:4 abid:1\n"
    "v_exp_f32 v156, v156 clamp\n"
    "v_exp_f32 v157, v157 clamp\n"
    "v_mfma_f32_4x4x4_16b_bf16 v[224:227], v[228:229], v[172:173], v[224:227] cbsz:4 abid:2\n"
    "v_exp_f32 v158, v158 clamp\n"
    "v_exp_f32 v159, v159 clamp\n"
    "v_mfma_f32_4x4x4_16b_bf16 v[224:227], v[228:229], v[174:175], v[224:227] cbsz:4 abid:3\n"
    "ds_read_b64_tr_b16 v[168:169], v240 offset:4096\n"
    "ds_read_b64_tr_b16 v[170:171], v241 offset:4352\n"
    "ds_read_b64_tr_b16 v[172:173], v242 offset:4608\n"
    "ds_read_b64_tr_b16 v[174:175], v243 offset:4864\n"
    "v_add_u32 v253, s66, v244\n"
    "ds_read_b32 v253, v253\n"
    "v_lshlrev_b32_e64 v202, 3, v249\n"
    "v_and_b32 v202, 0x70, v202\n"
    "s_mov_b32 s86, s69\n"
    "s_mov_b32 s87, s69\n"
    "v_pk_fma_f32 v[144:145], v[144:145], s[70:71], v[96:97]\n"
    "v_pk_fma_f32 v[146:147], v[146:147], s[70:71], v[98:99]\n"
    "v_pk_add_f32 v[230:231], v[144:145], v[146:147]\n"
    "v_pk_mul_f32 v[232:233], v[144:145], v[146:147]\n"
    "v_pk_fma_f32 v[148:149], v[148:149], s[70:71], v[100:101]\n"
    "v_pk_fma_f32 v[230:231], v[230:231], v[148:149], v[232:233]\n"
    "v_pk_mul_f32 v[232:233], v[232:233], v[148:149]\n"
    "v_pk_fma_f32 v[150:151], v[150:151], s[70:71], v[102:103]\n"
    "s_waitcnt lgkmcnt(0)\n"
    "v_and_b32 v201, 63, v253\n"
    "v_and_b32 v200, 0x3fffffff, v253\n"
    "v_mul_u32_u24 v201, 272, v201\n"
    "v_cmp_gt_u32_e64 s[88:89], s58, v200\n"
    "v_lshl_add_u32 v201, v202, 1, v201\n"
    "v_and_b32 v200, 0x3fffffc0, v200\n"
    "ds_read_b128 v[208:211], v201 offset:32768\n"
    "ds_read_b128 v[212:215], v201 offset:32784\n"
    "v_lshl_add_u32 v200, v200, 1, v202\n"
    "v_bfrev_b32 v202, 1\n"
    "s_nop 0\n"
    "v_cndmask_b32_e64 v200, v202, v200, s[88:89]\n"
    "v_pk_fma_f32 v[230:231], v[230:231], v[150:151], v[232:233]\n"
    "v_pk_mul_f32 v[232:233], v[232:233], v[150:151]\n"
    "v_pk_fma_f32 v[152:153], v[152:153], s[70:71], v[104:105]\n"
    "v_pk_fma_f32 v[230:231], v[230:231], v[152:153], v[232:233]\n"
    "v_pk_mul_f32 v[232:233], v[232:233], v[152:153]\n"
    "v_pk_fma_f32 v[154:155], v[154:155], s[70:71], v[106:107]\n"
    "v_pk_fma_f32 v[230:231], v[230:231], v[154:155], v[232:233]\n"
    "v_pk_mul_f32 v[232:233], v[232:233], v[154:155]\n"
    "v_pk_fma_f32 v[156:157], v[156:157], s[70:71], v[108:109]\n"
    "v_pk_fma_f32 v[230:231], v[230:231], v[156:157], v[232:233]\n"
    "v_pk_mul_f32 v[232:233], v[232:233], v[156:157]\n"
    "v_pk_fma_f32 v[158:159], v[158:159], s[70:71], v[110:111]\n"
    "v_pk_fma_f32 v[230:231], v[230:231], v[158:159], v[232:233]\n"
    "v_pk_mul_f32 v[232:233], v[232:233], v[158:159]\n"
    "s_waitcnt lgkmcnt(0)\n"
    "s_nop 0\n"
    "v_pk_mul_f32 v[208:209], v[208:209], s[86:87]\n"
    "v_pk_mul_f32 v[210:211], v[210:211], s[86:87]\n"
    "v_pk_mul_f32 v[212:213], v[212:213], s[86:87]\n"
    "v_pk_mul_f32 v[214:215], v[214:215], s[86:87]\n"
    "v_cvt_pk_bf16_f32 v216, v208, v209\n"
    "v_cvt_pk_bf16_f32 v217, v210, v211\n"
    "v_cvt_pk_bf16_f32 v218, v212, v213\n"
    "v_cvt_pk_bf16_f32 v219, v214, v215\n"
    "buffer_store_dwordx4 v[216:219], v200, s[52:55], 0 offen sc0 sc1 nt\n"
    "s_waitcnt vmcnt(3)\n"
    "ds_read_b128 v[160:163], v234 offset:6144\n"
    "ds_read_b128 v[164:167], v235 offset:6144\n"
    "v_rcp_f32 v232, v232\n"
    "v_rcp_f32 v233, v233\n"
    "v_fma_f32 v230, v230, v232, v247\n"
    "v_fmac_f32 v230, v231, v233\n"
    "v_cvt_pk_bf16_f32 v230, v230, v230\n"
    "ds_write_b16 v245, v230\n"
    "ds_read_b64 v[228:229], v246\n"
    "v_exp_f32 v136, v136 clamp\n"
    "s_waitcnt lgkmcnt(2)\n"
    "v_mfma_f32_16x16x32_bf16 v[144:147], v[0:3], v[160:163], v[112:115]\n"
    "v_exp_f32 v128, v128 clamp\n"
    "v_mfma_f32_16x16x32_bf16 v[144:147], v[4:7], v[164:167], v[144:147]\n"
    "v_exp_f32 v129, v129 clamp\n"
    "v_mfma_f32_16x16x32_bf16 v[148:151], v[8:11], v[160:163], v[116:119]\n"
    "v_exp_f32 v130, v130 clamp\n"
    "v_mfma_f32_16x16x32_bf16 v[148:151], v[12:15], v[164:167], v[148:151]\n"
    "v_exp_f32 v131, v131 clamp\n"
    "v_mfma_f32_16x16x32_bf16 v[152:155], v[16:19], v[160:163], v[120:123]\n"
    "v_exp_f32 v132, v132 clamp\n"
    "v_mfma_f32_16x16x32_bf16 v[152:155], v[20:23], v[164:167], v[152:155]\n"
    "v_exp_f32 v133, v133 clamp\n"
    "v_mfma_f32_16x16x32_bf16 v[156:159], v[24:27], v[160:163], v[124:127]\n"
    "v_exp_f32 v134, v134 clamp\n"
    "v_mfma_f32_16x16x32_bf16 v[156:159], v[28:31], v[164:167], v[156:159]\n"
    "v_exp_f32 v135, v135 clamp\n"
    "v_exp_f32 v137, v137 clamp\n"
    "s_waitcnt lgkmcnt(0)\n"
    "v_mfma_f32_4x4x4_16b_bf16 v[224:227], v[228:229], v[176:177], v[224:227] cbsz:4 abid:0\n"
    "v_exp_f32 v138, v138 clamp\n"
    "v_exp_f32 v139, v139 clamp\n"
    "v_mfma_f32_4x4x4_16b_bf16 v[224:227], v[228:229], v[178:179], v[224:227] cbsz:4 abid:1\n"
    "v_exp_f32 v140, v140 clamp\n"
    "v_exp_f32 v141, v141 clamp\n"
    "v_mfma_f32_4x4x4_16b_bf16 v[224:227], v[228:229], v[180:181], v[224:227] cbsz:4 abid:2\n"
    "v_exp_f32 v142, v142 clamp\n"
    "v_exp_f32 v143, v143 clamp\n"
    "v_mfma_f32_4x4x4_16b_bf16 v[224:227], v[228:229], v[182:183], v[224:227] cbsz:4 abid:3\n"
    "ds_read_b64_tr_b16 v[176:177], v240 offset:6144\n"
    "ds_read_b64_tr_b16 v[178:179], v241 offset:6400\n"
    "ds_read_b64_tr_b16 v[180:181], v242 offset:6656\n"
    "ds_read_b64_tr_b16 v[182:183], v243 offset:6912\n"
    "v_pk_fma_f32 v[128:129], v[128:129], s[70:71], v[96:97]\n"
    "v_pk_fma_f32 v[130:131], v[130:131], s[70:71], v[98:99]\n"
    "v_pk_add_f32 v[230:231], v[128:129], v[130:131]\n"
    "v_pk_mul_f32 v[232:233], v[128:129], v[130:131]\n"
    "v_pk_fma_f32 v[132:133], v[132:133], s[70:71], v[100:101]\n"
    "v_pk_fma_f32 v[230:231], v[230:231], v[132:133], v[232:233]\n"
    "v_pk_mul_f32 v[232:233], v[232:233], v[132:133]\n"
    "v_pk_fma_f32 v[134:135], v[134:135], s[70:71], v[102:103]\n"
    "v_pk_fma_f32 v[230:231], v[230:231], v[134:135], v[232:233]\n"
    "v_pk_mul_f32 v[232:233], v[232:233], v[134:135]\n"
    "v_pk_fma_f32 v[136:137], v[136:137], s[70:71], v[104:105]\n"
    "v_pk_fma_f32 v[230:231], v[230:231], v[136:137], v[232:233]\n"
    "v_pk_mul_f32 v[232:233], v[232:233], v[136:137]\n"
    "v_pk_fma_f32 v[138:139], v[138:139], s[70:71], v[106:107]\n"
    "v_pk_fma_f32 v[230:231], v[230:231], v[138:139], v[232:233]\n"
    "v_pk_mul_f32 v[232:233], v[232:233], v[138:139]\n"
    "v_pk_fma_f32 v[140:141], v[140:141], s[70:71], v[108:109]\n"
    "v_pk_fma_f32 v[230:231], v[230:231], v[140:141], v[232:233]\n"
    "v_pk_mul_f32 v[232:233], v[232:233], v[140:141]\n"
    "v_pk_fma_f32 v[142:143], v[142:143], s[70:71], v[110:111]\n"
    "v_pk_fma_f32 v[230:231], v[230:231], v[142:143], v[232:233]\n"
    "v_pk_mul_f32 v[232:233], v[232:233], v[142:143]\n"
    "s_waitcnt vmcnt(1)\n"
    "ds_read_b128 v[160:163], v234 offset:0\n"
    "ds_read_b128 v[164:167], v235 offset:0\n"
    "v_rcp_f32 v232, v232\n"
    "v_rcp_f32 v233, v233\n"
    "v_fma_f32 v230, v230, v232, v247\n"
    ".Ldin_stub_11:\n"
    "s_cbranch_execz .Ldin_t2_ret\n"
    "s_nop 0\n"
    "v_fmac_f32 v230, v231, v233\n"
    "v_cvt_pk_bf16_f32 v230, v230, v230\n"
    "ds_write_b16 v245, v230\n"
    "ds_read_b64 v[228:229], v246\n"
    "v_exp_f32 v152, v152 clamp\n"
    "s_waitcnt lgkmcnt(2)\n"
    "v_mfma_f32_16x16x32_bf16 v[128:131], v[0:3], v[160:163], v[112:115]\n"
    "v_exp_f32 v144, v144 clamp\n"
    "v_mfma_f32_16x16x32_bf16 v[128:131], v[4:7], v[164:167], v[128:131]\n"
    "v_exp_f32 v145, v145 clamp\n"
    "v_mfma_f32_16x16x32_bf16 v[132:135], v[8:11], v[160:163], v[116:119]\n"
    "v_exp_f32 v146, v146 clamp\n"
    "v_mfma_f32_16x16x32_bf16 v[132:135], v[12:15], v[164:167], v[132:135]\n"
    "v_exp_f32 v147, v147 clamp\n"
    "v_mfma_f32_16x16x32_bf16 v[136:139], v[16:19], v[160:163], v[120:123]\n"
    "v_exp_f32 v148, v148 clamp\n"
    "v_mfma_f32_16x16x32_bf16 v[136:139], v[20:23], v[164:167], v[136:139]\n"
    "v_exp_f32 v149, v149 clamp\n"
    "v_mfma_f32_16x16x32_bf16 v[140:143], v[24:27], v[160:163], v[124:127]\n"
    "v_exp_f32 v150, v150 clamp\n"
    "v_mfma_f32_16x16x32_bf16 v[140:143], v[28:31], v[164:167], v[140:143]\n"
    "v_exp_f32 v151, v151 clamp\n"
    "v_exp_f32 v153, v153 clamp\n"
    "s_waitcnt lgkmcnt(0)\n"
    "v_mfma_f32_4x4x4_16b_bf16 v[224:227], v[228:229], v[168:169], v[224:227] cbsz:4 abid:0\n"
    "v_exp_f32 v154, v154 clamp\n"
    "v_exp_f32 v155, v155 clamp\n"
    "v_mfma_f32_4x4x4_16b_bf16 v[224:227], v[228:229], v[170:171], v[224:227] cbsz:4 abid:1\n"
    "v_exp_f32 v156, v156 clamp\n"
    "v_exp_f32 v157, v157 clamp\n"
    "v_mfma_f32_4x4x4_16b_bf16 v[224:227], v[228:229], v[172:173], v[224:227] cbsz:4 abid:2\n"
    "v_exp_f32 v158, v158 clamp\n"
    "v_exp_f32 v159, v159 clamp\n"
    "v_mfma_f32_4x4x4_16b_bf16 v[224:227], v[228:229], v[174:175], v[224:227] cbsz:4 abid:3\n"
    "ds_read_b64_tr_b16 v[168:169], v240 offset:0\n"
    "ds_read_b64_tr_b16 v[170:171], v241 offset:256\n"
    "ds_read_b64_tr_b16 v[172:173], v242 offset:512\n"
    "ds_read_b64_tr_b16 v[174:175], v243 offset:768\n"
    "v_pk_fma_f32 v[144:145], v[144:145], s[70:71], v[96:97]\n"
    "v_pk_fma_f32 v[146:147], v[146:147], s[70:71], v[98:99]\n"
    "v_pk_add_f32 v[230:231], v[144:145], v[146:147]\n"
    "v_pk_mul_f32 v[232:233], v[144:145], v[146:147]\n"
    "v_pk_fma_f32 v[148:149], v[148:149], s[70:71], v[100:101]\n"
    "v_pk_fma_f32 v[230:231], v[230:231], v[148:149], v[232:233]\n"
    "v_pk_mul_f32 v[232:233], v[232:233], v[148:149]\n"
    "v_pk_fma_f32 v[150:151], v[150:151], s[70:71], v[102:103]\n"
    "v_pk_fma_f32 v[230:231], v[230:231], v[150:151], v[232:233]\n"
    "v_pk_mul_f32 v[232:233], v[232:233], v[150:151]\n"
    "v_pk_fma_f32 v[152:153], v[152:153], s[70:71], v[104:105]\n"
    "v_pk_fma_f32 v[230:231], v[230:231], v[152:153], v[232:233]\n"
    "v_pk_mul_f32 v[232:233], v[232:233], v[152:153]\n"
    "v_pk_fma_f32 v[154:155], v[154:155], s[70:71], v[106:107]\n"
    "v_pk_fma_f32 v[230:231], v[230:231], v[154:155], v[232:233]\n"
    "v_pk_mul_f32 v[232:233], v[232:233], v[154:155]\n"
    "v_pk_fma_f32 v[156:157], v[156:157], s[70:71], v[108:109]\n"
    "v_pk_fma_f32 v[230:231], v[230:231], v[156:157], v[232:233]\n"
    "v_pk_mul_f32 v[232:233], v[232:233], v[156:157]\n"
    "v_pk_fma_f32 v[158:159], v[158:159], s[70:71], v[110:111]\n"
    "v_pk_fma_f32 v[230:231], v[230:231], v[158:159], v[232:233]\n"
    "v_pk_mul_f32 v[232:233], v[232:233], v[158:159]\n"
    "v_rcp_f32 v232, v232\n"
    "v_rcp_f32 v233, v233\n"
    "v_fma_f32 v230, v230, v232, v247\n"
    "v_fmac_f32 v230, v231, v233\n"
    "v_cvt_pk_bf16_f32 v230, v230, v230\n"
    "ds_write_b16 v245, v230\n"
    "ds_read_b64 v[228:229], v246\n"
    "s_setprio 0\n"
    "s_branch .Ldin_F0\n"
    ".p2align 5\n"
    ".Ldin_T2:\n"
    "v_mfma_f32_16x16x32_bf16 v[144:147], v[0:3], v[160:163], v[112:115]\n"
    "v_exp_f32 v128, v128 clamp\n"
    "v_mfma_f32_16x16x32_bf16 v[144:147], v[4:7], v[164:167], v[144:147]\n"
    "v_exp_f32 v129, v129 clamp\n"
    "v_mfma_f32_16x16x32_bf16 v[148:151], v[8:11], v[160:163], v[116:119]\n"
    "v_exp_f32 v130, v130 clamp\n"
    "v_mfma_f32_16x16x32_bf16 v[148:151], v[12:15], v[164:167], v[148:151]\n"
    "v_exp_f32 v131, v131 clamp\n"
    "v_mfma_f32_16x16x32_bf16 v[152:155], v[16:19], v[160:163], v[120:123]\n"
    "v_exp_f32 v132, v132 clamp\n"
    "v_mfma_f32_16x16x32_bf16 v[152:155], v[20:23], v[164:167], v[152:155]\n"
    "v_exp_f32 v133, v133 clamp\n"
    "v_mfma_f32_16x16x32_bf16 v[156:159], v[24:27], v[160:163], v[124:127]\n"
    "v_exp_f32 v134, v134 clamp\n"
    "v_mfma_f32_16x16x32_bf16 v[156:159], v[28:31], v[164:167], v[156:159]\n"
    "v_exp_f32 v135, v135 clamp\n"
    "v_exp_f32 v136, v136 clamp\n"
    "v_exp_f32 v137, v137 clamp\n"
    "s_waitcnt lgkmcnt(0)\n"
    "v_mfma_f32_4x4x4_16b_bf16 v[224:227], v[228:229], v[176:177], v[224:227] cbsz:4 abid:0\n"
    "v_exp_f32 v138, v138 clamp\n"
    "v_exp_f32 v139, v139 clamp\n"
    "v_mfma_f32_4x4x4_16b_bf16 v[224:227], v[228:229], v[178:179], v[224:227] cbsz:4 abid:1\n"
    "v_exp_f32 v140, v140 clamp\n"
    "v_exp_f32 v141, v141 clamp\n"
    "v_mfma_f32_4x4x4_16b_bf16 v[224:227], v[228:229], v[180:181], v[224:227] cbsz:4 abid:2\n"
    "v_exp_f32 v142, v142 clamp\n"
    "v_exp_f32 v143, v143 clamp\n"
    "v_mfma_f32_4x4x4_16b_bf16 v[224:227], v[228:229], v[182:183], v[224:227] cbsz:4 abid:3\n"
    "ds_read_b64_tr_b16 v[176:177], v240 offset:6144\n"
    "ds_read_b64_tr_b16 v[178:179], v241 offset:6400\n"
    "ds_read_b64_tr_b16 v[180:181], v242 offset:6656\n"
    "ds_read_b64_tr_b16 v[182:183], v243 offset:6912\n"
    "v_add_u32 v253, s66, v244\n"
    "ds_read_b32 v253, v253\n"
    "v_lshlrev_b32_e64 v202, 3, v249\n"
    "v_and_b32 v202, 0x70, v202\n"
    "s_mov_b32 s86, s69\n"
    "s_mov_b32 s87, s69\n"
    "v_pk_fma_f32 v[128:129], v[128:129], s[70:71], v[96:97]\n"
    "v_pk_fma_f32 v[130:131], v[130:131], s[70:71], v[98:99]\n"
    "v_pk_add_f32 v[230:231], v[128:129], v[130:131]\n"
    "v_pk_mul_f32 v[232:233], v[128:129], v[130:131]\n"
    "v_pk_fma_f32 v[132:133], v[132:133], s[70:71], v[100:101]\n"
    "v_pk_fma_f32 v[230:231], v[230:231], v[132:133], v[232:233]\n"
    "v_pk_mul_f32 v[232:233], v[232:233], v[132:133]\n"
    "v_pk_fma_f32 v[134:135], v[134:135], s[70:71], v[102:103]\n"
    "s_waitcnt lgkmcnt(0)\n"
    "v_and_b32 v201, 63, v253\n"
    "v_and_b32 v200, 0x3fffffff, v253\n"
    "v_mul_u32_u24 v201, 272, v201\n"
    "v_cmp_gt_u32_e64 s[88:89], s58, v200\n"
    "v_lshl_add_u32 v201, v202, 1, v201\n"
    "v_and_b32 v200, 0x3fffffc0, v200\n"
    "ds_read_b128 v[208:211], v201 offset:32768\n"
    "ds_read_b128 v[212:215], v201 offset:32784\n"
    "v_lshl_add_u32 v200, v200, 1, v202\n"
    "v_bfrev_b32 v202, 1\n"
    "s_nop 0\n"
    "v_cndmask_b32_e64 v200, v202, v200, s[88:89]\n"
    "v_pk_fma_f32 v[230:231], v[230:231], v[134:135], v[232:233]\n"
    "v_pk_mul_f32 v[232:233], v[232:233], v[134:135]\n"
    "v_pk_fma_f32 v[136:137], v[136:137], s[70:71], v[104:105]\n"
    "v_pk_fma_f32 v[230:231], v[230:231], v[136:137], v[232:233]\n"
    ".Ldin_stub_16:\n"
    "s_cbranch_execz .Ldin_t4_ret\n"
    "s_nop 0\n"
    "v_pk_mul_f32 v[232:233], v[232:233], v[136:137]\n"
    "v_pk_fma_f32 v[138:139], v[138:139], s[70:71], v[106:107]\n"
    "v_pk_fma_f32 v[230:231], v[230:231], v[138:139], v[232:233]\n"
    "v_pk_mul_f32 v[232:233], v[232:233], v[138:139]\n"
    "v_pk_fma_f32 v[140:141], v[140:141], s[70:71], v[108:109]\n"
    "v_pk_fma_f32 v[230:231], v[230:231], v[140:141], v[232:233]\n"
    "v_pk_mul_f32 v[232:233], v[232:233], v[140:141]\n"
    "v_pk_fma_f32 v[142:143], v[142:143], s[70:71], v[110:111]\n"
    "v_pk_fma_f32 v[230:231], v[230:231], v[142:143], v[232:233]\n"
    "v_pk_mul_f32 v[232:233], v[232:233], v[142:143]\n"
    "s_waitcnt lgkmcnt(0)\n"
    "s_nop 0\n"
    "v_pk_mul_f32 v[208:209], v[208:209], s[86:87]\n"
    "v_pk_mul_f32 v[210:211], v[210:211], s[86:87]\n"
    "v_pk_mul_f32 v[212:213], v[212:213], s[86:87]\n"
    "v_pk_mul_f32 v[214:215], v[214:215], s[86:87]\n"
    "v_cvt_pk_bf16_f32 v216, v208, v209\n"
    "v_cvt_pk_bf16_f32 v217, v210, v211\n"
    "v_cvt_pk_bf16_f32 v218, v212, v213\n"
    "v_cvt_pk_bf16_f32 v219, v214, v215\n"
    "buffer_store_dwordx4 v[216:219], v200, s[52:55], 0 offen sc0 sc1 nt\n"
    "s_waitcnt vmcnt(3)\n"
    "ds_read_b128 v[160:163], v234 offset:0\n"
    "ds_read_b128 v[164:167], v235 offset:0\n"
    "v_rcp_f32 v232, v232\n"
    "v_rcp_f32 v233, v233\n"
    "v_fma_f32 v230, v230, v232, v247\n"
    "v_fmac_f32 v230, v231, v233\n"
    "v_cvt_pk_bf16_f32 v230, v230, v230\n"
    "ds_write_b16 v245, v230\n"
    "ds_read_b64 v[228:229], v246\n"
    "v_exp_f32 v152, v152 clamp\n"
    "s_waitcnt lgkmcnt(2)\n"
    "v_mfma_f32_16x16x32_bf16 v[128:131], v[0:3], v[160:163], v[112:115]\n"
    "v_exp_f32 v144, v144 clamp\n"
    "v_mfma_f32_16x16x32_bf16 v[128:131], v[4:7], v[164:167], v[128:131]\n"
    "v_exp_f32 v145, v145 clamp\n"
    "v_mfma_f32_16x16x32_bf16 v[132:135], v[8:11], v[160:163], v[116:119]\n"
    "v_exp_f32 v146, v146 clamp\n"
    "v_mfma_f32_16x16x32_bf16 v[132:135], v[12:15], v[164:167], v[132:135]\n"
    "v_exp_f32 v147, v147 clamp\n"
    "v_mfma_f32_16x16x32_bf16 v[136:139], v[16:19], v[160:163], v[120:123]\n"
    "v_exp_f32 v148, v148 clamp\n"
    "v_mfma_f32_16x16x32_bf16 v[136:139], v[20:23], v[164:167], v[136:139]\n"
    "v_exp_f32 v149, v149 clamp\n"
    "v_mfma_f32_16x16x32_bf16 v[140:143], v[24:27], v[160:163], v[124:127]\n"
    "v_exp_f32 v150, v150 clamp\n"
    "v_mfma_f32_16x16x32_bf16 v[140:143], v[28:31], v[164:167], v[140:143]\n"
    "v_exp_f32 v151, v151 clamp\n"
    "v_exp_f32 v153, v153 clamp\n"
    "s_waitcnt lgkmcnt(0)\n"
    "v_mfma_f32_4x4x4_16b_bf16 v[224:227], v[228:229], v[168:169], v[224:227] cbsz:4 abid:0\n"
    "v_exp_f32 v154, v154 clamp\n"
    "v_exp_f32 v155, v155 clamp\n"
    "v_mfma_f32_4x4x4_16b_bf16 v[224:227], v[228:229], v[170:171], v[224:227] cbsz:4 abid:1\n"
    "v_exp_f32 v156, v156 clamp\n"
    "v_exp_f32 v157, v157 clamp\n"
    "v_mfma_f32_4x4x4_16b_bf16 v[224:227], v[228:229], v[172:173], v[224:227] cbsz:4 abid:2\n"
    "v_exp_f32 v158, v158 clamp\n"
    "v_exp_f32 v159, v159 clamp\n"
    "v_mfma_f32_4x4x4_16b_bf16 v[224:227], v[228:229], v[174:175], v[224:227] cbsz:4 abid:3\n"
    "ds_read_b64_tr_b16 v[168:169], v240 offset:0\n"
    "ds_read_b64_tr_b16 v[170:171], v241 offset:256\n"
    "ds_read_b64_tr_b16 v[172:173], v242 offset:512\n"
    "ds_read_b64_tr_b16 v[174:175], v243 offset:768\n"
    "v_pk_fma_f32 v[144:145], v[144:145], s[70:71], v[96:97]\n"
    "v_pk_fma_f32 v[146:147], v[146:147], s[70:71], v[98:99]\n"
    "v_pk_add_f32 v[230:231], v[144:145], v[146:147]\n"
    "v_pk_mul_f32 v[232:233], v[144:145], v[146:147]\n"
    "v_pk_fma_f32 v[148:149], v[148:149], s[70:71], v[100:101]\n"
    "v_pk_fma_f32 v[230:231], v[230:231], v[148:149], v[232:233]\n"
    "v_pk_mul_f32 v[232:233], v[232:233], v[148:149]\n"
    "v_pk_fma_f32 v[150:151], v[150:151], s[70:71], v[102:103]\n"
    "v_pk_fma_f32 v[230:231], v[230:231], v[150:151], v[232:233]\n"
    "v_pk_mul_f32 v[232:233], v[232:233], v[150:151]\n"
    "v_pk_fma_f32 v[152:153], v[152:153], s[70:71], v[104:105]\n"
    "v_pk_fma_f32 v[230:231], v[230:231], v[152:153], v[232:233]\n"
    "v_pk_mul_f32 v[232:233], v[232:233], v[152:153]\n"
    "v_pk_fma_f32 v[154:155], v[154:155], s[70:71], v[106:107]\n"
    "v_pk_fma_f32 v[230:231], v[230:231], v[154:155], v[232:233]\n"
    "v_pk_mul_f32 v[232:233], v[232:233], v[154:155]\n"
    "v_pk_fma_f32 v[156:157], v[156:157], s[70:71], v[108:109]\n"
    "v_pk_fma_f32 v[230:231], v[230:231], v[156:157], v[232:233]\n"
    "v_pk_mul_f32 v[232:233], v[232:233], v[156:157]\n"
    "v_pk_fma_f32 v[158:159], v[158:159], s[70:71], v[110:111]\n"
    "v_pk_fma_f32 v[230:231], v[230:231], v[158:159], v[232:233]\n"
    "v_pk_mul_f32 v[232:233], v[232:233], v[158:159]\n"
    "s_waitcnt vmcnt(1)\n"
    "ds_read_b128 v[160:163], v234 offset:2048\n"
    "ds_read_b128 v[164:167], v235 offset:2048\n"
    "v_rcp_f32 v232, v232\n"
    "v_rcp_f32 v233, v233\n"
    "v_fma_f32 v230, v230, v232, v247\n"
    "v_fmac_f32 v230, v231, v233\n"
    "v_cvt_pk_bf16_f32 v230, v230, v230\n"
    "ds_write_b16 v245, v230\n"
    "ds_read_b64 v[228:229], v246\n"
    "v_exp_f32 v136, v136 clamp\n"
    "s_waitcnt lgkmcnt(2)\n"
    "v_mfma_f32_16x16x32_bf16 v[144:147], v[0:3], v[160:163], v[112:115]\n"
    "v_exp_f32 v128, v128 clamp\n"
    "v_mfma_f32_16x16x32_bf16 v[144:147], v[4:7], v[164:167], v[144:147]\n"
    "v_exp_f32 v129, v129 clamp\n"
    "v_mfma_f32_16x16x32_bf16 v[148:151], v[8:11], v[160:163], v[116:119]\n"
    "v_exp_f32 v130, v130 clamp\n"
    "v_mfma_f32_16x16x32_bf16 v[148:151], v[12:15], v[164:167], v[148:151]\n"
    "v_exp_f32 v131, v131 clamp\n"
    "v_mfma_f32_16x16x32_bf16 v[152:155], v[16:19], v[160:163], v[120:123]\n"
    "v_exp_f32 v132, v132 clamp\n"
    "v_mfma_f32_16x16x32_bf16 v[152:155], v[20:23], v[164:167], v[152:155]\n"
    "v_exp_f32 v133, v133 clamp\n"
    "v_mfma_f32_16x16x32_bf16 v[156:159], v[24:27], v[160:163], v[124:127]\n"
    "v_exp_f32 v134, v134 clamp\n"
    "v_mfma_f32_16x16x32_bf16 v[156:159], v[28:31], v[164:167], v[156:159]\n"
    "v_exp_f32 v135, v135 clamp\n"
    "v_exp_f32 v137, v137 clamp\n"
    "s_waitcnt lgkmcnt(0)\n"
    "v_mfma_f32_4x4x4_16b_bf16 v[224:227], v[228:229], v[176:177], v[224:227] cbsz:4 abid:0\n"
    "v_exp_f32 v138, v138 clamp\n"
    "v_exp_f32 v139, v139 clamp\n"
    "v_mfma_f32_4x4x4_16b_bf16 v[224:227], v[228:229], v[178:179], v[224:227] cbsz:4 abid:1\n"
    "v_exp_f32 v140, v140 clamp\n"
    "v_exp_f32 v141, v141 clamp\n"
    "v_mfma_f32_4x4x4_16b_bf16 v[224:227], v[228:229], v[180:181], v[224:227] cbsz:4 abid:2\n"
    "v_exp_f32 v142, v142 clamp\n"
    "v_exp_f32 v143, v143 clamp\n"
    "v_mfma_f32_4x4x4_16b_bf16 v[224:227], v[228:229], v[182:183], v[224:227] cbsz:4 abid:3\n"
    "ds_read_b64_tr_b16 v[176:177], v240 offset:2048\n"
    "ds_read_b64_tr_b16 v[178:179], v241 offset:2304\n"
    "ds_read_b64_tr_b16 v[180:181], v242 offset:2560\n"
    "ds_read_b64_tr_b16 v[182:183], v243 offset:2816\n"
    "v_pk_fma_f32 v[128:129], v[128:129], s[70:71], v[96:97]\n"
    "v_pk_fma_f32 v[130:131], v[130:131], s[70:71], v[98:99]\n"
    "v_pk_add_f32 v[230:231], v[128:129], v[130:131]\n"
    ".Ldin_stub_17:\n"
    "s_cbranch_execz .Ldin_t4_ret\n"
    "s_nop 0\n"
    "v_pk_mul_f32 v[232:233], v[128:129], v[130:131]\n"
    "v_pk_fma_f32 v[132:133], v[132:133], s[70:71], v[100:101]\n"
    "v_pk_fma_f32 v[230:231], v[230:231], v[132:133], v[232:233]\n"
    "v_pk_mul_f32 v[232:233], v[232:233], v[132:133]\n"
    "v_pk_fma_f32 v[134:135], v[134:135], s[70:71], v[102:103]\n"
    "v_pk_fma_f32 v[230:231], v[230:231], v[134:135], v[232:233]\n"
    "v_pk_mul_f32 v[232:233], v[232:233], v[134:135]\n"
    "v_pk_fma_f32 v[136:137], v[136:137], s[70:71], v[104:105]\n"
    "v_pk_fma_f32 v[230:231], v[230:231], v[136:137], v[232:233]\n"
    "v_pk_mul_f32 v[232:233], v[232:233], v[136:137]\n"
    "v_pk_fma_f32 v[138:139], v[138:139], s[70:71], v[106:107]\n"
    "v_pk_fma_f32 v[230:231], v[230:231], v[138:139], v[232:233]\n"
    "v_pk_mul_f32 v[232:233], v[232:233], v[138:139]\n"
    "v_pk_fma_f32 v[140:141], v[140:141], s[70:71], v[108:109]\n"
    "v_pk_fma_f32 v[230:231], v[230:231], v[140:141], v[232:233]\n"
    "v_pk_mul_f32 v[232:233], v[232:233], v[140:141]\n"
    "v_pk_fma_f32 v[142:143], v[142:143], s[70:71], v[110:111]\n"
    "v_pk_fma_f32 v[230:231], v[230:231], v[142:143], v[232:233]\n"
    "v_pk_mul_f32 v[232:233], v[232:233], v[142:143]\n"
    "v_rcp_f32 v232, v232\n"
    "v_rcp_f32 v233, v233\n"
    "v_fma_f32 v230, v230, v232, v247\n"
    "v_fmac_f32 v230, v231, v233\n"
    "v_cvt_pk_bf16_f32 v230, v230, v230\n"
    "ds_write_b16 v245, v230\n"
    "ds_read_b64 v[228:229], v246\n"
    "s_setprio 0\n"
    "s_branch .Ldin_F1\n"
    ".p2align 5\n"
    ".Ldin_T3:\n"
    "v_mfma_f32_16x16x32_bf16 v[128:131], v[0:3], v[160:163], v[112:115]\n"
    "v_exp_f32 v144, v144 clamp\n"
    "v_mfma_f32_16x16x32_bf16 v[128:131], v[4:7], v[164:167], v[128:131]\n"
    "v_exp_f32 v145, v145 clamp\n"
    "v_mfma_f32_16x16x32_bf16 v[132:135], v[8:11], v[160:163], v[116:119]\n"
    "v_exp_f32 v146, v146 clamp\n"
    "v_mfma_f32_16x16x32_bf16 v[132:135], v[12:15], v[164:167], v[132:135]\n"
    "v_exp_f32 v147, v147 clamp\n"
    "v_mfma_f32_16x16x32_bf16 v[136:139], v[16:19], v[160:163], v[120:123]\n"
    "v_exp_f32 v148, v148 clamp\n"
    "v_mfma_f32_16x16x32_bf16 v[136:139], v[20:23], v[164:167], v[136:139]\n"
    "v_exp_f32 v149, v149 clamp\n"
    "v_mfma_f32_16x16x32_bf16 v[140:143], v[24:27], v[160:163], v[124:127]\n"
    "v_exp_f32 v150, v150 clamp\n"
    "v_mfma_f32_16x16x32_bf16 v[140:143], v[28:31], v[164:167], v[140:143]\n"
    "v_exp_f32 v151, v151 clamp\n"
    "v_exp_f32 v152, v152 clamp\n"
    "v_exp_f32 v153, v153 clamp\n"
    "s_waitcnt lgkmcnt(0)\n"
    "v_mfma_f32_4x4x4_16b_bf16 v[224:227], v[228:229], v[168:169], v[224:227] cbsz:4 abid:0\n"
    "v_exp_f32 v154, v154 clamp\n"
    "v_exp_f32 v155, v155 clamp\n"
    "v_mfma_f32_4x4x4_16b_bf16 v[224:227], v[228:229], v[170:171], v[224:227] cbsz:4 abid:1\n"
    "v_exp_f32 v156, v156 clamp\n"
    "v_exp_f32 v157, v157 clamp\n"
    "v_mfma_f32_4x4x4_16b_bf16 v[224:227], v[228:229], v[172:173], v[224:227] cbsz:4 abid:2\n"
    "v_exp_f32 v158, v158 clamp\n"
    "v_exp_f32 v159, v159 clamp\n"
    "v_mfma_f32_4x4x4_16b_bf16 v[224:227], v[228:229], v[174:175], v[224:227] cbsz:4 abid:3\n"
    "ds_read_b64_tr_b16 v[168:169], v240 offset:0\n"
    "ds_read_b64_tr_b16 v[170:171], v241 offset:256\n"
    "ds_read_b64_tr_b16 v[172:173], v242 offset:512\n"
    "ds_read_b64_tr_b16 v[174:175], v243 offset:768\n"
    "v_add_u32 v253, s66, v244\n"
    "ds_read_b32 v253, v253\n"
    "v_lshlrev_b32_e64 v202, 3, v249\n"
    "v_and_b32 v202, 0x70, v202\n"
    "s_mov_b32 s86, s69\n"
    "s_mov_b32 s87, s69\n"
    "v_pk_fma_f32 v[144:145], v[144:145], s[70:71], v[96:97]\n"
    "v_pk_fma_f32 v[146:147], v[146:147], s[70:71], v[98:99]\n"
    "v_pk_add_f32 v[230:231], v[144:145], v[146:147]\n"
    "v_pk_mul_f32 v[232:233], v[144:145], v[146:147]\n"
    "v_pk_fma_f32 v[148:149], v[148:149], s[70:71], v[100:101]\n"
    "v_pk_fma_f32 v[230:231], v[230:231], v[148:149], v[232:233]\n"
    "v_pk_mul_f32 v[232:233], v[232:233], v[148:149]\n"
    "v_pk_fma_f32 v[150:151], v[150:151], s[70:71], v[102:103]\n"
    "s_waitcnt lgkmcnt(0)\n"
    "v_and_b32 v201, 63, v253\n"
    "v_and_b32 v200, 0x3fffffff, v253\n"
    "v_mul_u32_u24 v201, 272, v201\n"
    "v_cmp_gt_u32_e64 s[88:89], s58, v200\n"
    "v_lshl_add_u32 v201, v202, 1, v201\n"
    "v_and_b32 v200, 0x3fffffc0, v200\n"
    "ds_read_b128 v[208:211], v201 offset:32768\n"
    "ds_read_b128 v[212:215], v201 offset:32784\n"
    "v_lshl_add_u32 v200, v200, 1, v202\n"
    "v_bfrev_b32 v202, 1\n"
    "s_nop 0\n"
    "v_cndmask_b32_e64 v200, v202, v200, s[88:89]\n"
    "v_pk_fma_f32 v[230:231], v[230:231], v[150:151], v[232:233]\n"
    "v_pk_mul_f32 v[232:233], v[232:233], v[150:151]\n"
    "v_pk_fma_f32 v[152:153], v[152:153], s[70:71], v[104:105]\n"
    "v_pk_fma_f32 v[230:231], v[230:231], v[152:153], v[232:233]\n"
    "v_pk_mul_f32 v[232:233], v[232:233], v[152:153]\n"
    "v_pk_fma_f32 v[154:155], v[154:155], s[70:71], v[106:107]\n"
    "v_pk_fma_f32 v[230:231], v[230:231], v[154:155], v[232:233]\n"
    "v_pk_mul_f32 v[232:233], v[232:233], v[154:155]\n"
    "v_pk_fma_f32 v[156:157], v[156:157], s[70:71], v[108:109]\n"
    "v_pk_fma_f32 v[230:231], v[230:231], v[156:157], v[232:233]\n"
    "v_pk_mul_f32 v[232:233], v[232:233], v[156:157]\n"
    "v_pk_fma_f32 v[158:159], v[158:159], s[70:71], v[110:111]\n"
    "v_pk_fma_f32 v[230:231], v[230:231], v[158:159], v[232:233]\n"
    "v_pk_mul_f32 v[232:233], v[232:233], v[158:159]\n"
    "s_waitcnt lgkmcnt(0)\n"
    "s_nop 0\n"
    "v_pk_mul_f32 v[208:209], v[208:209], s[86:87]\n"
    "v_pk_mul_f32 v[210:211], v[210:211], s[86:87]\n"
    "v_pk_mul_f32 v[212:213], v[212:213], s[86:87]\n"
    "v_pk_mul_f32 v[214:215], v[214:215], s[86:87]\n"
    "v_cvt_pk_bf16_f32 v216, v208, v209\n"
    "v_cvt_pk_bf16_f32 v217, v210, v211\n"
    "v_cvt_pk_bf16_f32 v218, v212, v213\n"
    "v_cvt_pk_bf16_f32 v219, v214, v215\n"
    "buffer_store_dwordx4 v[216:219], v200, s[52:55], 0 offen sc0 sc1 nt\n"
    "s_waitcnt vmcnt(3)\n"
    "ds_read_b128 v[160:163], v234 offset:2048\n"
    "ds_read_b128 v[164:167], v235 offset:2048\n"
    "v_rcp_f32 v232, v232\n"
    "v_rcp_f32 v233, v233\n"
    "v_fma_f32 v230, v230, v232, v247\n"
    "v_fmac_f32 v230, v231, v233\n"
    "v_cvt_pk_bf16_f32 v230, v230, v230\n"
    "ds_write_b16 v245, v230\n"
    "ds_read_b64 v[228:229], v246\n"
    "v_exp_f32 v136, v136 clamp\n"
    "s_waitcnt lgkmcnt(2)\n"
    "v_mfma_f32_16x16x32_bf16 v[144:147], v[0:3], v[160:163], v[112:115]\n"
    "v_exp_f32 v128, v128 clamp\n"
    "v_mfma_f32_16x16x32_bf16 v[144:147], v[4:7], v[164:167], v[144:147]\n"
    "v_exp_f32 v129, v129 clamp\n"
    "v_mfma_f32_16x16x32_bf16 v[148:151], v[8:11], v[160:163], v[116:119]\n"
    "v_exp_f32 v130, v130 clamp\n"
    "v_mfma_f32_16x16x32_bf16 v[148:151], v[12:15], v[164:167], v[148:151]\n"
    "v_exp_f32 v131, v131 clamp\n"
    "v_mfma_f32_16x16x32_bf16 v[152:155], v[16:19], v[160:163], v[120:123]\n"
    "v_exp_f32 v132, v132 clamp\n"
    "v_mfma_f32_16x16x32_bf16 v[152:155], v[20:23], v[164:167], v[152:155]\n"
    ".Ldin_stub_18:\n"
    "s_cbranch_execz .Ldin_t4_ret\n"
    "s_nop 0\n"
    "v_exp_f32 v133, v133 clamp\n"
    "v_mfma_f32_16x16x32_bf16 v[156:159], v[24:27], v[160:163], v[124:127]\n"
    "v_exp_f32 v134, v134 clamp\n"
    "v_mfma_f32_16x16x32_bf16 v[156:159], v[28:31], v[164:167], v[156:159]\n"
    "v_exp_f32 v135, v135 clamp\n"
    "v_exp_f32 v137, v137 clamp\n"
    "s_waitcnt lgkmcnt(0)\n"
    "v_mfma_f32_4x4x4_16b_bf16 v[224:227], v[228:229], v[176:177], v[224:227] cbsz:4 abid:0\n"
    "v_exp_f32 v138, v138 clamp\n"
    "v_exp_f32 v139, v139 clamp\n"
    "v_mfma_f32_4x4x4_16b_bf16 v[224:227], v[228:229], v[178:179], v[224:227] cbsz:4 abid:1\n"
    "v_exp_f32 v140, v140 clamp\n"
    "v_exp_f32 v141, v141 clamp\n"
    "v_mfma_f32_4x4x4_16b_bf16 v[224:227], v[228:229], v[180:181], v[224:227] cbsz:4 abid:2\n"
    "v_exp_f32 v142, v142 clamp\n"
    "v_exp_f32 v143, v143 clamp\n"
    "v_mfma_f32_4x4x4_16b_bf16 v[224:227], v[228:229], v[182:183], v[224:227] cbsz:4 abid:3\n"
    "ds_read_b64_tr_b16 v[176:177], v240 offset:2048\n"
    "ds_read_b64_tr_b16 v[178:179], v241 offset:2304\n"
    "ds_read_b64_tr_b16 v[180:181], v242 offset:2560\n"
    "ds_read_b64_tr_b16 v[182:183], v243 offset:2816\n"
    "v_pk_fma_f32 v[128:129], v[128:129], s[70:71], v[96:97]\n"
    "v_pk_fma_f32 v[130:131], v[130:131], s[70:71], v[98:99]\n"
    "v_pk_add_f32 v[230:231], v[128:129], v[130:131]\n"
    "v_pk_mul_f32 v[232:233], v[128:129], v[130:131]\n"
    "v_pk_fma_f32 v[132:133], v[132:133], s[70:71], v[100:101]\n"
    "v_pk_fma_f32 v[230:231], v[230:231], v[132:133], v[232:233]\n"
    "v_pk_mul_f32 v[232:233], v[232:233], v[132:133]\n"
    "v_pk_fma_f32 v[134:135], v[134:135], s[70:71], v[102:103]\n"
    "v_pk_fma_f32 v[230:231], v[230:231], v[134:135], v[232:233]\n"
    "v_pk_mul_f32 v[232:233], v[232:233], v[134:135]\n"
    "v_pk_fma_f32 v[136:137], v[136:137], s[70:71], v[104:105]\n"
    "v_pk_fma_f32 v[230:231], v[230:231], v[136:137], v[232:233]\n"
    "v_pk_mul_f32 v[232:233], v[232:233], v[136:137]\n"
    "v_pk_fma_f32 v[138:139], v[138:139], s[70:71], v[106:107]\n"
    "v_pk_fma_f32 v[230:231], v[230:231], v[138:139], v[232:233]\n"
    "v_pk_mul_f32 v[232:233], v[232:233], v[138:139]\n"
    "v_pk_fma_f32 v[140:141], v[140:141], s[70:71], v[108:109]\n"
    "v_pk_fma_f32 v[230:231], v[230:231], v[140:141], v[232:233]\n"
    "v_pk_mul_f32 v[232:233], v[232:233], v[140:141]\n"
    "v_pk_fma_f32 v[142:143], v[142:143], s[70:71], v[110:111]\n"
    "v_pk_fma_f32 v[230:231], v[230:231], v[142:143], v[232:233]\n"
    "v_pk_mul_f32 v[232:233], v[232:233], v[142:143]\n"
    "s_waitcnt vmcnt(1)\n"
    "ds_read_b128 v[160:163], v234 offset:4096\n"
    "ds_read_b128 v[164:167], v235 offset:4096\n"
    "v_rcp_f32 v232, v232\n"
    "v_rcp_f32 v233, v233\n"
    "v_fma_f32 v230, v230, v232, v247\n"
    "v_fmac_f32 v230, v231, v233\n"
    "v_cvt_pk_bf16_f32 v230, v230, v230\n"
    "ds_write_b16 v245, v230\n"
    "ds_read_b64 v[228:229], v246\n"
    "v_exp_f32 v152, v152 clamp\n"
    "s_waitcnt lgkmcnt(2)\n"
    "v_mfma_f32_16x16x32_bf16 v[128:131], v[0:3], v[160:163], v[112:115]\n"
    "v_exp_f32 v144, v144 clamp\n"
    "v_mfma_f32_16x16x32_bf16 v[128:131], v[4:7], v[164:167], v[128:131]\n"
    "v_exp_f32 v145, v145 clamp\n"
    "v_mfma_f32_16x16x32_bf16 v[132:135], v[8:11], v[160:163], v[116:119]\n"
    "v_exp_f32 v146, v146 clamp\n"
    "v_mfma_f32_16x16x32_bf16 v[132:135], v[12:15], v[164:167], v[132:135]\n"
    "v_exp_f32 v147, v147 clamp\n"
    "v_mfma_f32_16x16x32_bf16 v[136:139], v[16:19], v[160:163], v[120:123]\n"
    "v_exp_f32 v148, v148 clamp\n"
    "v_mfma_f32_16x16x32_bf16 v[136:139], v[20:23], v[164:167], v[136:139]\n"
    "v_exp_f32 v149, v149 clamp\n"
    "v_mfma_f32_16x16x32_bf16 v[140:143], v[24:27], v[160:163], v[124:127]\n"
    "v_exp_f32 v150, v150 clamp\n"
    "v_mfma_f32_16x16x32_bf16 v[140:143], v[28:31], v[164:167], v[140:143]\n"
    "v_exp_f32 v151, v151 clamp\n"
    "v_exp_f32 v153, v153 clamp\n"
    "s_waitcnt lgkmcnt(0)\n"
    "v_mfma_f32_4x4x4_16b_bf16 v[224:227], v[228:229], v[168:169], v[224:227] cbsz:4 abid:0\n"
    "v_exp_f32 v154, v154 clamp\n"
    "v_exp_f32 v155, v155 clamp\n"
    "v_mfma_f32_4x4x4_16b_bf16 v[224:227], v[228:229], v[170:171], v[224:227] cbsz:4 abid:1\n"
    "v_exp_f32 v156, v156 clamp\n"
    "v_exp_f32 v157, v157 clamp\n"
    "v_mfma_f32_4x4x4_16b_bf16 v[224:227], v[228:229], v[172:173], v[224:227] cbsz:4 abid:2\n"
    "v_exp_f32 v158, v158 clamp\n"
    "v_exp_f32 v159, v159 clamp\n"
    "v_mfma_f32_4x4x4_16b_bf16 v[224:227], v[228:229], v[174:175], v[224:227] cbsz:4 abid:3\n"
    "ds_read_b64_tr_b16 v[168:169], v240 offset:4096\n"
    "ds_read_b64_tr_b16 v[170:171], v241 offset:4352\n"
    "ds_read_b64_tr_b16 v[172:173], v242 offset:4608\n"
    "ds_read_b64_tr_b16 v[174:175], v243 offset:4864\n"
    "v_pk_fma_f32 v[144:145], v[144:145], s[70:71], v[96:97]\n"
    "v_pk_fma_f32 v[146:147], v[146:147], s[70:71], v[98:99]\n"
    "v_pk_add_f32 v[230:231], v[144:145], v[146:147]\n"
    "v_pk_mul_f32 v[232:233], v[144:145], v[146:147]\n"
    "v_pk_fma_f32 v[148:149], v[148:149], s[70:71], v[100:101]\n"
    "v_pk_fma_f32 v[230:231], v[230:231], v[148:149], v[232:233]\n"
    "v_pk_mul_f32 v[232:233], v[232:233], v[148:149]\n"
    "v_pk_fma_f32 v[150:151], v[150:151], s[70:71], v[102:103]\n"
    "v_pk_fma_f32 v[230:231], v[230:231], v[150:151], v[232:233]\n"
    "v_pk_mul_f32 v[232:233], v[232:233], v[150:151]\n"
    "v_pk_fma_f32 v[152:153], v[152:153], s[70:71], v[104:105]\n"
    "v_pk_fma_f32 v[230:231], v[230:231], v[152:153], v[232:233]\n"
    "v_pk_mul_f32 v[232:233], v[232:233], v[152:153]\n"
    "v_pk_fma_f32 v[154:155], v[154:155], s[70:71], v[106:107]\n"
    "v_pk_fma_f32 v[230:231], v[230:231], v[154:155], v[232:233]\n"
    "v_pk_mul_f32 v[232:233], v[232:233], v[154:155]\n"
    "v_pk_fma_f32 v[156:157], v[156:157], s[70:71], v[108:109]\n"
    "v_pk_fma_f32 v[230:231], v[230:231], v[156:157], v[232:233]\n"
    "v_pk_mul_f32 v[232:233], v[232:233], v[156:157]\n"
    "v_pk_fma_f32 v[158:159], v[158:159], s[70:71], v[110:111]\n"
    "v_pk_fma_f32 v[230:231], v[230:231], v[158:159], v[232:233]\n"
    "v_pk_mul_f32 v[232:233], v[232:233], v[158:159]\n"
    "v_rcp_f32 v232, v232\n"
    "v_rcp_f32 v233, v233\n"
    "v_fma_f32 v230, v230, v232, v247\n"
    "v_fmac_f32 v230, v231, v233\n"
    "v_cvt_pk_bf16_f32 v230, v230, v230\n"
    "ds_write_b16 v245, v230\n"
    "ds_read_b64 v[228:229], v246\n"
    "s_setprio 0\n"
    "s_branch .Ldin_F0\n"
    ".p2align 5\n"
    ".Ldin_F0:\n"
    "v_exp_f32 v128, v128 clamp\n"
    "v_exp_f32 v129, v129 clamp\n"
    "v_exp_f32 v130, v130 clamp\n"
    "v_exp_f32 v131, v131 clamp\n"
    "v_exp_f32 v132, v132 clamp\n"
    "v_exp_f32 v133, v133 clamp\n"
    "v_exp_f32 v134, v134 clamp\n"
    "v_exp_f32 v135, v135 clamp\n"
    "s_waitcnt lgkmcnt(0)\n"
    "v_mfma_f32_4x4x4_16b_bf16 v[224:227], v[228:229], v[176:177], v[224:227] cbsz:4 abid:0\n"
    "v_exp_f32 v136, v136 clamp\n"
    "v_exp_f32 v137, v137 clamp\n"
    "v_mfma_f32_4x4x4_16b_bf16 v[224:227], v[228:229], v[178:179], v[224:227] cbsz:4 abid:1\n"
    ".Ldin_stub_19:\n"
    "s_cbranch_execz .Ldin_t4_ret\n"
    "s_nop 0\n"
    "v_exp_f32 v138, v138 clamp\n"
    "v_exp_f32 v139, v139 clamp\n"
    "v_mfma_f32_4x4x4_16b_bf16 v[224:227], v[228:229], v[180:181], v[224:227] cbsz:4 abid:2\n"
    "v_exp_f32 v140, v140 clamp\n"
    "v_exp_f32 v141, v141 clamp\n"
    "v_mfma_f32_4x4x4_16b_bf16 v[224:227], v[228:229], v[182:183], v[224:227] cbsz:4 abid:3\n"
    "v_exp_f32 v142, v142 clamp\n"
    "v_exp_f32 v143, v143 clamp\n"
    "s_nop 0\n"
    "v_pk_fma_f32 v[128:129], v[128:129], s[70:71], v[96:97]\n"
    "v_pk_fma_f32 v[130:131], v[130:131], s[70:71], v[98:99]\n"
    "v_pk_add_f32 v[230:231], v[128:129], v[130:131]\n"
    "v_pk_mul_f32 v[232:233], v[128:129], v[130:131]\n"
    "v_pk_fma_f32 v[132:133], v[132:133], s[70:71], v[100:101]\n"
    "v_pk_fma_f32 v[230:231], v[230:231], v[132:133], v[232:233]\n"
    "v_pk_mul_f32 v[232:233], v[232:233], v[132:133]\n"
    "v_pk_fma_f32 v[134:135], v[134:135], s[70:71], v[102:103]\n"
    "v_pk_fma_f32 v[230:231], v[230:231], v[134:135], v[232:233]\n"
    "v_pk_mul_f32 v[232:233], v[232:233], v[134:135]\n"
    "v_pk_fma_f32 v[136:137], v[136:137], s[70:71], v[104:105]\n"
    "v_pk_fma_f32 v[230:231], v[230:231], v[136:137], v[232:233]\n"
    "v_pk_mul_f32 v[232:233], v[232:233], v[136:137]\n"
    "v_pk_fma_f32 v[138:139], v[138:139], s[70:71], v[106:107]\n"
    "v_pk_fma_f32 v[230:231], v[230:231], v[138:139], v[232:233]\n"
    "v_pk_mul_f32 v[232:233], v[232:233], v[138:139]\n"
    "v_pk_fma_f32 v[140:141], v[140:141], s[70:71], v[108:109]\n"
    "v_pk_fma_f32 v[230:231], v[230:231], v[140:141], v[232:233]\n"
    "v_pk_mul_f32 v[232:233], v[232:233], v[140:141]\n"
    "v_pk_fma_f32 v[142:143], v[142:143], s[70:71], v[110:111]\n"
    "v_pk_fma_f32 v[230:231], v[230:231], v[142:143], v[232:233]\n"
    "v_pk_mul_f32 v[232:233], v[232:233], v[142:143]\n"
    "v_rcp_f32 v232, v232\n"
    "v_rcp_f32 v233, v233\n"
    "v_fma_f32 v230, v230, v232, v247\n"
    "v_fmac_f32 v230, v231, v233\n"
    "v_cvt_pk_bf16_f32 v230, v230, v230\n"
    "ds_write_b16 v245, v230\n"
    "ds_read_b64 v[228:229], v246\n"
    "s_lshl_b32 s94, s58, 1\n"
    "s_andn2_b32 s94, s94, 127\n"
    "s_waitcnt lgkmcnt(0)\n"
    "v_mfma_f32_4x4x4_16b_bf16 v[224:227], v[228:229], v[168:169], v[224:227] cbsz:4 abid:0\n"
    "s_nop 1\n"
    "v_mfma_f32_4x4x4_16b_bf16 v[224:227], v[228:229], v[170:171], v[224:227] cbsz:4 abid:1\n"
    "s_nop 1\n"
    "v_mfma_f32_4x4x4_16b_bf16 v[224:227], v[228:229], v[172:173], v[224:227] cbsz:4 abid:2\n"
    "s_nop 1\n"
    "v_mfma_f32_4x4x4_16b_bf16 v[224:227], v[228:229], v[174:175], v[224:227] cbsz:4 abid:3\n"
    "s_nop 4\n"
    "v_pk_add_f32 v[232:233], v[224:225], v[226:227]\n"
    "v_add_f32 v232, v232, v233\n"
    "v_mul_f32 v232, s69, v232\n"
    "s_mov_b32 s86, 0x55555555\n"
    "s_mov_b32 s87, s86\n"
    "v_mov_b32_dpp v233, v232 quad_perm:[1,1,3,3] row_mask:0xf bank_mask:0xf\n"
    "s_mov_b64 exec, s[86:87]\n"
    "v_cvt_pk_bf16_f32 v232, v232, v233\n"
    "buffer_store_dword v232, v249, s[52:55], s94 offen sc0 sc1 nt\n"
    "s_endpgm\n"
    ".p2align 5\n"
    ".Ldin_F1:\n"
    "v_exp_f32 v144, v144 clamp\n"
    "v_exp_f32 v145, v145 clamp\n"
    "v_exp_f32 v146, v146 clamp\n"
    "v_exp_f32 v147, v147 clamp\n"
    "v_exp_f32 v148, v148 clamp\n"
    "v_exp_f32 v149, v149 clamp\n"
    "v_exp_f32 v150, v150 clamp\n"
    "v_exp_f32 v151, v151 clamp\n"
    "s_waitcnt lgkmcnt(0)\n"
    "v_mfma_f32_4x4x4_16b_bf16 v[224:227], v[228:229], v[168:169], v[224:227] cbsz:4 abid:0\n"
    "v_exp_f32 v152, v152 clamp\n"
    "v_exp_f32 v153, v153 clamp\n"
    "v_mfma_f32_4x4x4_16b_bf16 v[224:227], v[228:229], v[170:171], v[224:227] cbsz:4 abid:1\n"
    "v_exp_f32 v154, v154 clamp\n"
    "v_exp_f32 v155, v155 clamp\n"
    "v_mfma_f32_4x4x4_16b_bf16 v[224:227], v[228:229], v[172:173], v[224:227] cbsz:4 abid:2\n"
    "v_exp_f32 v156, v156 clamp\n"
    "v_exp_f32 v157, v157 clamp\n"
    "v_mfma_f32_4x4x4_16b_bf16 v[224:227], v[228:229], v[174:175], v[224:227] cbsz:4 abid:3\n"
    "v_exp_f32 v158, v158 clamp\n"
    "v_exp_f32 v159, v159 clamp\n"
    "s_nop 0\n"
    "v_pk_fma_f32 v[144:145], v[144:145], s[70:71], v[96:97]\n"
    "v_pk_fma_f32 v[146:147], v[146:147], s[70:71], v[98:99]\n"
    "v_pk_add_f32 v[230:231], v[144:145], v[146:147]\n"
    "v_pk_mul_f32 v[232:233], v[144:145], v[146:147]\n"
    "v_pk_fma_f32 v[148:149], v[148:149], s[70:71], v[100:101]\n"
    "v_pk_fma_f32 v[230:231], v[230:231], v[148:149], v[232:233]\n"
    "v_pk_mul_f32 v[232:233], v[232:233], v[148:149]\n"
    "v_pk_fma_f32 v[150:151], v[150:151], s[70:71], v[102:103]\n"
    "v_pk_fma_f32 v[230:231], v[230:231], v[150:151], v[232:233]\n"
    "v_pk_mul_f32 v[232:233], v[232:233], v[150:151]\n"
    "v_pk_fma_f32 v[152:153], v[152:153], s[70:71], v[104:105]\n"
    "v_pk_fma_f32 v[230:231], v[230:231], v[152:153], v[232:233]\n"
    "v_pk_mul_f32 v[232:233], v[232:233], v[152:153]\n"
    "v_pk_fma_f32 v[154:155], v[154:155], s[70:71], v[106:107]\n"
    "v_pk_fma_f32 v[230:231], v[230:231], v[154:155], v[232:233]\n"
    "v_pk_mul_f32 v[232:233], v[232:233], v[154:155]\n"
    "v_pk_fma_f32 v[156:157], v[156:157], s[70:71], v[108:109]\n"
    "v_pk_fma_f32 v[230:231], v[230:231], v[156:157], v[232:233]\n"
    "v_pk_mul_f32 v[232:233], v[232:233], v[156:157]\n"
    "v_pk_fma_f32 v[158:159], v[158:159], s[70:71], v[110:111]\n"
    "v_pk_fma_f32 v[230:231], v[230:231], v[158:159], v[232:233]\n"
    "v_pk_mul_f32 v[232:233], v[232:233], v[158:159]\n"
    "v_rcp_f32 v232, v232\n"
    "v_rcp_f32 v233, v233\n"
    "v_fma_f32 v230, v230, v232, v247\n"
    "v_fmac_f32 v230, v231, v233\n"
    "v_cvt_pk_bf16_f32 v230, v230, v230\n"
    "ds_write_b16 v245, v230\n"
    "ds_read_b64 v[228:229], v246\n"
    "s_lshl_b32 s94, s58, 1\n"
    "s_andn2_b32 s94, s94, 127\n"
    "s_waitcnt lgkmcnt(0)\n"
    "v_mfma_f32_4x4x4_16b_bf16 v[224:227], v[228:229], v[176:177], v[224:227] cbsz:4 abid:0\n"
    "s_nop 1\n"
    "v_mfma_f32_4x4x4_16b_bf16 v[224:227], v[228:229], v[178:179], v[224:227] cbsz:4 abid:1\n"
    "s_nop 1\n"
    "v_mfma_f32_4x4x4_16b_bf16 v[224:227], v[228:229], v[180:181], v[224:227] cbsz:4 abid:2\n"
    "s_nop 1\n"
    "v_mfma_f32_4x4x4_16b_bf16 v[224:227], v[228:229], v[182:183], v[224:227] cbsz:4 abid:3\n"
    "s_nop 4\n"
    "v_pk_add_f32 v[232:233], v[224:225], v[226:227]\n"
    "v_add_f32 v232, v232, v233\n"
    "v_mul_f32 v232, s69, v232\n"
    "s_mov_b32 s86, 0x55555555\n"
    "s_mov_b32 s87, s86\n"
    "v_mov_b32_dpp v233, v232 quad_perm:[1,1,3,3] row_mask:0xf bank_mask:0xf\n"
    "s_mov_b64 exec, s[86:87]\n"
    "v_cvt_pk_bf16_f32 v232, v232, v233\n"
    "buffer_store_dword v232, v249, s[52:55], s94 offen sc0 sc1 nt\n"
    "s_endpgm\n"
    ".Ldin_drain0:\n"
    "v_add_u32 v253, s66, v244\n"
    "ds_read_b32 v253, v253\n"
    "v_lshlrev_b32 v202, 3, v249\n"
    "s_mov_b32 m0, s62\n"
    "s_mov_b32 s70, s69\n"
    "s_mov_b32 s71, s69\n"
    "s_waitcnt lgkmcnt(0)\n"
    "v_mfma_f32_4x4x4_16b_bf16 v[224:227], v[228:229], v[168:169], v[224:227] cbsz:4 abid:0\n"
    "v_and_b32_e64 v201, 63, v253\n"
    "v_and_b32 v202, 0x70, v202\n"
    "v_mfma_f32_4x4x4_16b_bf16 v[224:227], v[228:229], v[170:171], v[224:227] cbsz:4 abid:1\n"
    "v_and_b32 v200, 0x3fffffc0, v253\n"
    "v_mul_u32_u24 v201, 272, v201\n"
    "v_mfma_f32_4x4x4_16b_bf16 v[224:227], v[228:229], v[172:173], v[224:227] cbsz:4 abid:2\n"
    "v_lshl_add_u32 v200, v200, 1, v202\n"
    "v_lshl_add_u32 v201, v202, 1, v201\n"
    "v_mfma_f32_4x4x4_16b_bf16 v[224:227], v[228:229], v[174:175], v[224:227] cbsz:4 abid:3\n"
    "v_cmp_gt_u32 vcc, s57, v253\n"
    "v_bfrev_b32 v202, 1\n"
    "s_nop 0\n"
    "v_cndmask_b32 v200, v202, v200, vcc\n"
    "s_nop 0\n"
    "v_pk_add_f32 v[232:233], v[224:225], v[226:227]\n"
    "v_add_f32 v232, v232, v233\n"
    "ds_write_addtid_b32 v232 offset:32768\n"
    "ds_read_b128 v[208:211], v201 offset:32768\n"
    "ds_read_b128 v[212:215], v201 offset:32784\n"
    "s_waitcnt lgkmcnt(1)\n"
    "v_pk_mul_f32 v[208:209], v[208:209], s[70:71]\n"
    "v_pk_mul_f32 v[210:211], v[210:211], s[70:71]\n"
    "v_cvt_pk_bf16_f32 v216, v208, v209\n"
    "v_cvt_pk_bf16_f32 v217, v210, v211\n"
    "s_waitcnt lgkmcnt(0)\n"
    "v_pk_mul_f32 v[212:213], v[212:213], s[70:71]\n"
    "v_pk_mul_f32 v[214:215], v[214:215], s[70:71]\n"
    "v_cvt_pk_bf16_f32 v218, v212, v213\n"
    "v_cvt_pk_bf16_f32 v219, v214, v215\n"
    "s_waitcnt vmcnt(0)\n"
    "buffer_store_dwordx4 v[216:219], v200, s[52:55], 0 offen sc0 sc1 nt\n"
    "s_endpgm\n"
    ".Ldin_drain1:\n"
    "v_add_u32 v253, s66, v244\n"
    "ds_read_b32 v253, v253\n"
    "v_lshlrev_b32 v202, 3, v249\n"
    "s_mov_b32 m0, s62\n"
    "s_mov_b32 s70, s69\n"
    "s_mov_b32 s71, s69\n"
    "s_waitcnt lgkmcnt(0)\n"
    "v_mfma_f32_4x4x4_16b_bf16 v[224:227], v[228:229], v[176:177], v[224:227] cbsz:4 abid:0\n"
    "v_and_b32_e64 v201, 63, v253\n"
    "v_and_b32 v202, 0x70, v202\n"
    "v_mfma_f32_4x4x4_16b_bf16 v[224:227], v[228:229], v[178:179], v[224:227] cbsz:4 abid:1\n"
    "v_and_b32 v200, 0x3fffffc0, v253\n"
    "v_mul_u32_u24 v201, 272, v201\n"
    "v_mfma_f32_4x4x4_16b_bf16 v[224:227], v[228:229], v[180:181], v[224:227] cbsz:4 abid:2\n"
    "v_lshl_add_u32 v200, v200, 1, v202\n"
    "v_lshl_add_u32 v201, v202, 1, v201\n"
    "v_mfma_f32_4x4x4_16b_bf16 v[224:227], v[228:229], v[182:183], v[224:227] cbsz:4 abid:3\n"
    "v_cmp_gt_u32 vcc, s57, v253\n"
    "v_bfrev_b32 v202, 1\n"
    "s_nop 0\n"
    "v_cndmask_b32 v200, v202, v200, vcc\n"
    "s_nop 0\n"
    "v_pk_add_f32 v[232:233], v[224:225], v[226:227]\n"
    "v_add_f32 v232, v232, v233\n"
    "ds_write_addtid_b32 v232 offset:32768\n"
    "ds_read_b128 v[208:211], v201 offset:32768\n"
    "ds_read_b128 v[212:215], v201 offset:32784\n"
    "s_waitcnt lgkmcnt(1)\n"
    "v_pk_mul_f32 v[208:209], v[208:209], s[70:71]\n"
    "v_pk_mul_f32 v[210:211], v[210:211], s[70:71]\n"
    "v_cvt_pk_bf16_f32 v216, v208, v209\n"
    "v_cvt_pk_bf16_f32 v217, v210, v211\n"
    "s_waitcnt lgkmcnt(0)\n"
    "v_pk_mul_f32 v[212:213], v[212:213], s[70:71]\n"
    "v_pk_mul_f32 v[214:215], v[214:215], s[70:71]\n"
    "v_cvt_pk_bf16_f32 v218, v212, v213\n"
    "v_cvt_pk_bf16_f32 v219, v214, v215\n"
    "s_waitcnt vmcnt(0)\n"
    "buffer_store_dwordx4 v[216:219], v200, s[52:55], 0 offen sc0 sc1 nt\n"
    "s_endpgm\n"
    ".Ldin_A0s:\n"
    "v_mfma_f32_16x16x32_bf16 v[144:147], v[0:3], v[160:163], v[112:115]\n"
    "v_exp_f32 v128, v128 clamp\n"
    "v_mfma_f32_16x16x32_bf16 v[144:147], v[4:7], v[164:167], v[144:147]\n"
    "v_exp_f32 v129, v129 clamp\n"
    "v_mfma_f32_16x16x32_bf16 v[148:151], v[8:11], v[160:163], v[116:119]\n"
    "v_exp_f32 v130, v130 clamp\n"
    "v_mfma_f32_16x16x32_bf16 v[148:151], v[12:15], v[164:167], v[148:151]\n"
    "v_exp_f32 v131, v131 clamp\n"
    "v_mfma_f32_16x16x32_bf16 v[152:155], v[16:19], v[160:163], v[120:123]\n"
    "v_exp_f32 v132, v132 clamp\n"
    "v_mfma_f32_16x16x32_bf16 v[152:155], v[20:23], v[164:167], v[152:155]\n"
    "v_exp_f32 v133, v133 clamp\n"
    "v_mfma_f32_16x16x32_bf16 v[156:159], v[24:27], v[160:163], v[124:127]\n"
    "v_exp_f32 v134, v134 clamp\n"
    "v_mfma_f32_16x16x32_bf16 v[156:159], v[28:31], v[164:167], v[156:159]\n"
    "v_exp_f32 v135, v135 clamp\n"
    "v_exp_f32 v136, v136 clamp\n"
    "s_mov_b32 m0, s62\n"
    "v_exp_f32 v137, v137 clamp\n"
    "v_add_u32 v253, s67, v244\n"
    "s_waitcnt lgkmcnt(0)\n"
    "v_mfma_f32_4x4x4_16b_bf16 v[224:227], v[228:229], v[176:177], v[224:227] cbsz:4 abid:0\n"
    "v_exp_f32 v138, v138 clamp\n"
    "v_exp_f32 v139, v139 clamp\n"
    "v_mfma_f32_4x4x4_16b_bf16 v[224:227], v[228:229], v[178:179], v[224:227] cbsz:4 abid:1\n"
    "v_exp_f32 v140, v140 clamp\n"
    "v_exp_f32 v141, v141 clamp\n"
    "v_mfma_f32_4x4x4_16b_bf16 v[224:227], v[228:229], v[180:181], v[224:227] cbsz:4 abid:2\n"
    "v_exp_f32 v142, v142 clamp\n"
    "v_exp_f32 v143, v143 clamp\n"
    "v_mfma_f32_4x4x4_16b_bf16 v[224:227], v[228:229], v[182:183], v[224:227] cbsz:4 abid:3\n"
    "s_waitcnt vmcnt(4)\n"
    "ds_read_b64_tr_b16 v[176:177], v240 offset:2048\n"
    "ds_read_b64_tr_b16 v[178:179], v241 offset:2304\n"
    "ds_read_b64_tr_b16 v[180:181], v242 offset:2560\n"
    "ds_read_b64_tr_b16 v[182:183], v243 offset:2816\n"
    "ds_read_b32 v236, v253 offset:0\n"
    "ds_read_b32 v238, v253 offset:4\n"
    "ds_read_b128 v[160:163], v234 offset:4096\n"
    "ds_read_b128 v[164:167], v235 offset:4096\n"
    "s_cbranch_vccz .Ldin_A0s_np\n"
    "s_setprio 1\n"
    ".Ldin_A0s_np:\n"
    "v_pk_fma_f32 v[128:129], v[128:129], s[70:71], v[96:97]\n"
    "v_pk_fma_f32 v[130:131], v[130:131], s[70:71], v[98:99]\n"
    "v_pk_add_f32 v[190:191], v[224:225], v[226:227]\n"
    "v_add_f32 v190, v190, v191\n"
    "ds_write_addtid_b32 v190 offset:32768\n"
    "s_add_u32 m0, s68, 2048\n"
    "s_cmp_ge_i32 s59, s57\n"
    "s_cbranch_scc1 .Ldin_A0s_nl\n"
    "s_bfe_u32 s94, s59, 0x180006\n"
    "s_mul_i32 s86, s94, s56\n"
    "v_mbcnt_lo_u32_b32 v184, s20, 0\n"
    "v_mbcnt_lo_u32_b32 v185, s22, 0\n"
    "v_mbcnt_lo_u32_b32 v186, s24, 0\n"
    "v_mbcnt_lo_u32_b32 v187, s26, 0\n"
    "s_bcnt1_i32_b64 s90, s[20:21]\n"
    "s_bcnt1_i32_b64 s91, s[22:23]\n"
    "s_bcnt1_i32_b64 s92, s[24:25]\n"
    "s_bcnt1_i32_b64 s93, s[26:27]\n"
    "v_mbcnt_hi_u32_b32 v184, s21, v184\n"
    "v_mbcnt_hi_u32_b32 v185, s23, v185\n"
    "v_mbcnt_hi_u32_b32 v186, s25, v186\n"
    "v_mbcnt_hi_u32_b32 v187, s27, v187\n"
    "s_lshl2_add_u32 s90, s90, s65\n"
    "s_lshl2_add_u32 s91, s91, s90\n"
    "s_lshl2_add_u32 s92, s92, s91\n"
    "s_lshl2_add_u32 s93, s93, s92\n"
    "v_add_u32 v188, s86, v251\n"
    "v_lshl_add_u32 v184, v184, 2, s65\n"
    "v_lshl_add_u32 v185, v185, 2, s90\n"
    "v_lshl_add_u32 v186, v186, 2, s91\n"
    "v_lshl_add_u32 v187, v187, 2, s92\n"
    "v_mov_b32 v191, -1\n"
    "v_lshl_add_u32 v190, v249, 1, s93\n"
    "ds_write2_b32 v190, v191, v191 offset1:64\n"
    "s_mov_b64 exec, s[20:21]\n"
    "ds_write_b32 v184, v188\n"
    "s_mov_b64 exec, s[22:23]\n"
    "v_add_u32 v189, 64, v188\n"
    "ds_write_b32 v185, v189\n"
    "s_mov_b64 exec, s[24:25]\n"
    "v_add_u32 v190, 128, v188\n"
    "ds_write_b32 v186, v190\n"
    "s_mov_b64 exec, s[26:27]\n"
    "v_add_u32 v189, 192, v188\n"
    "ds_write_b32 v187, v189\n"
    "s_mov_b64 exec, -1\n"
    "s_sub_u32 s95, s93, -60\n"
    "s_add_u32 s94, s82, 128\n"
    "s_andn2_b32 s95, s95, 63\n"
    "s_max_u32 s83, s95, s94\n"
    ".Ldin_A0s_nl:\n"
    "v_pk_add_f32 v[230:231], v[128:129], v[130:131]\n"
    "v_pk_mul_f32 v[232:233], v[128:129], v[130:131]\n"
    "v_pk_fma_f32 v[132:133], v[132:133], s[70:71], v[100:101]\n"
    "v_pk_fma_f32 v[230:231], v[230:231], v[132:133], v[232:233]\n"
    "v_pk_mul_f32 v[232:233], v[232:233], v[132:133]\n"
    "v_pk_fma_f32 v[134:135], v[134:135], s[70:71], v[102:103]\n"
    "v_pk_fma_f32 v[230:231], v[230:231], v[134:135], v[232:233]\n"
    "v_pk_mul_f32 v[232:233], v[232:233], v[134:135]\n"
    "v_pk_fma_f32 v[136:137], v[136:137], s[70:71], v[104:105]\n"
    "v_pk_fma_f32 v[230:231], v[230:231], v[136:137], v[232:233]\n"
    "v_pk_mul_f32 v[232:233], v[232:233], v[136:137]\n"
    "v_pk_fma_f32 v[138:139], v[138:139], s[70:71], v[106:107]\n"
    "v_pk_fma_f32 v[230:231], v[230:231], v[138:139], v[232:233]\n"
    "v_pk_mul_f32 v[232:233], v[232:233], v[138:139]\n"
    "v_pk_fma_f32 v[140:141], v[140:141], s[70:71], v[108:109]\n"
    "v_pk_fma_f32 v[230:231], v[230:231], v[140:141], v[232:233]\n"
    "v_pk_mul_f32 v[232:233], v[232:233], v[140:141]\n"
    "v_pk_fma_f32 v[142:143], v[142:143], s[70:71], v[110:111]\n"
    "v_pk_fma_f32 v[230:231], v[230:231], v[142:143], v[232:233]\n"
    "v_pk_mul_f32 v[232:233], v[232:233], v[142:143]\n"
    "v_rcp_f32 v232, v232\n"
    "v_rcp_f32 v233, v233\n"
    "v_fma_f32 v230, v230, v232, v247\n"
    "v_fmac_f32 v230, v231, v233\n"
    "v_lshlrev_b32 v232, 2, v249\n"
    "v_lshrrev_b32 v233, 2, v249\n"
    "v_and_b32 v232, 56, v232\n"
    "v_and_b32 v233, 4, v233\n"
    "v_add3_u32 v232, v232, v233, s81\n"
    "ds_read_b32 v232, v232\n"
    "s_add_u32 s81, s81, 64\n"
    "s_waitcnt lgkmcnt(0)\n"
    "v_lshlrev_b32 v232, 1, v232\n"
    "buffer_load_ushort v232, v232, s[44:47], 0 offen\n"
    "s_waitcnt vmcnt(0)\n"
    "v_lshlrev_b32 v232, 16, v232\n"
    "v_mul_f32 v230, v230, v232\n"
    "v_cvt_pk_bf16_f32 v230, v230, v230\n"
    "s_add_u32 s67, s67, 64\n"
    "s_waitcnt lgkmcnt(0)\n"
    "ds_write_b16 v245, v230\n"
    "ds_read_b64 v[228:229], v246\n"
    "buffer_load_dwordx4 v[236:237], s[40:43], 0 idxen offen offset:0 nt lds\n"
    "buffer_load_dwordx4 v[238:239], s[40:43], 0 idxen offen offset:1024 nt lds\n"
    ".Ldin_B1s:\n"
    "v_mfma_f32_16x16x32_bf16 v[128:131], v[0:3], v[160:163], v[112:115]\n"
    "v_exp_f32 v144, v144 clamp\n"
    "v_mfma_f32_16x16x32_bf16 v[128:131], v[4:7], v[164:167], v[128:131]\n"
    "v_exp_f32 v145, v145 clamp\n"
    "v_mfma_f32_16x16x32_bf16 v[132:135], v[8:11], v[160:163], v[116:119]\n"
    "v_exp_f32 v146, v146 clamp\n"
    "v_mfma_f32_16x16x32_bf16 v[132:135], v[12:15], v[164:167], v[132:135]\n"
    "v_exp_f32 v147, v147 clamp\n"
    "v_mfma_f32_16x16x32_bf16 v[136:139], v[16:19], v[160:163], v[120:123]\n"
    "v_exp_f32 v148, v148 clamp\n"
    "v_mfma_f32_16x16x32_bf16 v[136:139], v[20:23], v[164:167], v[136:139]\n"
    "v_exp_f32 v149, v149 clamp\n"
    "v_mfma_f32_16x16x32_bf16 v[140:143], v[24:27], v[160:163], v[124:127]\n"
    "v_exp_f32 v150, v150 clamp\n"
    "v_mfma_f32_16x16x32_bf16 v[140:143], v[28:31], v[164:167], v[140:143]\n"
    "v_exp_f32 v151, v151 clamp\n"
    "v_exp_f32 v152, v152 clamp\n"
    "s_cmp_eq_u32 s67, s80\n"
    "s_cselect_b32 s67, s65, s67\n"
    "s_and_b32 s94, s59, 63\n"
    "s_mul_i32 s94, s94, 272\n"
    "v_add_u32 v252, s94, v250\n"
    "s_add_u32 m0, s68, 4096\n"
    "v_exp_f32 v153, v153 clamp\n"
    "v_add_u32 v253, s67, v244\n"
    "s_waitcnt lgkmcnt(0)\n"
    "v_mfma_f32_4x4x4_16b_bf16 v[224:227], v[228:229], v[168:169], 0 cbsz:4 abid:0\n"
    "v_exp_f32 v154, v154 clamp\n"
    "v_exp_f32 v155, v155 clamp\n"
    "v_mfma_f32_4x4x4_16b_bf16 v[224:227], v[228:229], v[170:171], v[224:227] cbsz:4 abid:1\n"
    "v_exp_f32 v156, v156 clamp\n"
    "v_exp_f32 v157, v157 clamp\n"
    "v_mfma_f32_4x4x4_16b_bf16 v[224:227], v[228:229], v[172:173], v[224:227] cbsz:4 abid:2\n"
    "v_exp_f32 v158, v158 clamp\n"
    "v_exp_f32 v159, v159 clamp\n"
    "v_mfma_f32_4x4x4_16b_bf16 v[224:227], v[228:229], v[174:175], v[224:227] cbsz:4 abid:3\n"
    "s_waitcnt vmcnt(4)\n"
    "s_memrealtime s[34:35]\n"
    "ds_read_b64_tr_b16 v[168:169], v240 offset:4096\n"
    "ds_read_b64_tr_b16 v[170:171], v241 offset:4352\n"
    "ds_read_b64_tr_b16 v[172:173], v242 offset:4608\n"
    "ds_read_b64_tr_b16 v[174:175], v243 offset:4864\n"
    "ds_read_b32 v236, v253 offset:0\n"
    "ds_read_b32 v238, v253 offset:4\n"
    "ds_read_b128 v[160:163], v234 offset:6144\n"
    "ds_read_b128 v[164:167], v235 offset:6144\n"
    "v_add_u32_e64 v184, v248, v252\n"
    "ds_read_b64 v[184:185], v184\n"
    "v_pk_fma_f32 v[144:145], v[144:145], s[70:71], v[96:97]\n"
    "v_pk_fma_f32 v[146:147], v[146:147], s[70:71], v[98:99]\n"
    "v_pk_add_f32 v[230:231], v[144:145], v[146:147]\n"
    "v_pk_mul_f32 v[232:233], v[144:145], v[146:147]\n"
    "v_pk_fma_f32 v[148:149], v[148:149], s[70:71], v[100:101]\n"
    "v_pk_fma_f32 v[230:231], v[230:231], v[148:149], v[232:233]\n"
    "v_pk_mul_f32 v[232:233], v[232:233], v[148:149]\n"
    "v_pk_fma_f32 v[150:151], v[150:151], s[70:71], v[102:103]\n"
    "v_pk_fma_f32 v[230:231], v[230:231], v[150:151], v[232:233]\n"
    "v_pk_mul_f32 v[232:233], v[232:233], v[150:151]\n"
    "v_pk_fma_f32 v[152:153], v[152:153], s[70:71], v[104:105]\n"
    "v_pk_fma_f32 v[230:231], v[230:231], v[152:153], v[232:233]\n"
    "v_pk_mul_f32 v[232:233], v[232:233], v[152:153]\n"
    "v_pk_fma_f32 v[154:155], v[154:155], s[70:71], v[106:107]\n"
    "v_pk_fma_f32 v[230:231], v[230:231], v[154:155], v[232:233]\n"
    "v_pk_mul_f32 v[232:233], v[232:233], v[154:155]\n"
    "v_pk_fma_f32 v[156:157], v[156:157], s[70:71], v[108:109]\n"
    "v_pk_fma_f32 v[230:231], v[230:231], v[156:157], v[232:233]\n"
    "v_pk_mul_f32 v[232:233], v[232:233], v[156:157]\n"
    "v_pk_fma_f32 v[158:159], v[158:159], s[70:71], v[110:111]\n"
    "v_pk_fma_f32 v[230:231], v[230:231], v[158:159], v[232:233]\n"
    "v_pk_mul_f32 v[232:233], v[232:233], v[158:159]\n"
    "v_rcp_f32 v232, v232\n"
    "v_rcp_f32 v233, v233\n"
    "v_fma_f32 v230, v230, v232, v247\n"
    "v_fmac_f32 v230, v231, v233\n"
    "v_lshlrev_b32 v232, 2, v249\n"
    "v_lshrrev_b32 v233, 2, v249\n"
    "v_and_b32 v232, 56, v232\n"
    "v_and_b32 v233, 4, v233\n"
    "v_add3_u32 v232, v232, v233, s81\n"
    "ds_read_b32 v232, v232\n"
    "s_add_u32 s81, s81, 64\n"
    "s_waitcnt lgkmcnt(0)\n"
    "v_lshlrev_b32 v232, 1, v232\n"
    "buffer_load_ushort v232, v232, s[44:47], 0 offen\n"
    "s_waitcnt vmcnt(0)\n"
    "v_lshlrev_b32 v232, 16, v232\n"
    "v_mul_f32 v230, v230, v232\n"
    "v_cvt_pk_bf16_f32 v230, v230, v230\n"
    "s_add_u32 s67, s67, 64\n"
    "s_waitcnt lgkmcnt(0)\n"
    "ds_write_b16 v245, v230\n"
    "ds_read_b64 v[228:229], v246\n"
    "v_and_b32 v184, v254, v184\n"
    "v_and_b32 v185, v255, v185\n"
    "s_cmp_le_u32 s34, s76\n"
    "s_cbranch_scc1 .Ldin_B1s_wait\n"
    ".Ldin_B1s_go:\n"
    "s_sub_u32 s94, s34, 60\n"
    "s_max_u32 s76, s76, s94\n"
    "s_add_u32 s76, s76, s72\n"
    ".Ldin_B1s_dma:\n"
    "buffer_load_dwordx4 v[236:237], s[40:43], 0 idxen offen offset:0 nt lds\n"
    "buffer_load_dwordx4 v[238:239], s[40:43], 0 idxen offen offset:1024 nt lds\n"
    ".Ldin_B1s_nd:\n"
    ".Ldin_G2s:\n"
    "v_mfma_f32_16x16x32_bf16 v[144:147], v[0:3], v[160:163], v[112:115]\n"
    "v_exp_f32 v128, v128 clamp\n"
    "v_mfma_f32_16x16x32_bf16 v[144:147], v[4:7], v[164:167], v[144:147]\n"
    "v_exp_f32 v129, v129 clamp\n"
    "v_mfma_f32_16x16x32_bf16 v[148:151], v[8:11], v[160:163], v[116:119]\n"
    "v_exp_f32 v130, v130 clamp\n"
    "v_mfma_f32_16x16x32_bf16 v[148:151], v[12:15], v[164:167], v[148:151]\n"
    "v_exp_f32 v131, v131 clamp\n"
    "v_mfma_f32_16x16x32_bf16 v[152:155], v[16:19], v[160:163], v[120:123]\n"
    "v_exp_f32 v132, v132 clamp\n"
    "v_mfma_f32_16x16x32_bf16 v[152:155], v[20:23], v[164:167], v[152:155]\n"
    "v_exp_f32 v133, v133 clamp\n"
    "v_mfma_f32_16x16x32_bf16 v[156:159], v[24:27], v[160:163], v[124:127]\n"
    "v_exp_f32 v134, v134 clamp\n"
    "v_mfma_f32_16x16x32_bf16 v[156:159], v[28:31], v[164:167], v[156:159]\n"
    "v_exp_f32 v135, v135 clamp\n"
    "v_exp_f32 v136, v136 clamp\n"
    "s_cmp_eq_u32 s67, s80\n"
    "s_cselect_b32 s67, s65, s67\n"
    "s_add_u32 m0, s68, 6144\n"
    "v_exp_f32 v137, v137 clamp\n"
    "v_add_u32_e64 v253, s67, v244\n"
    "s_waitcnt lgkmcnt(0)\n"
    "v_mfma_f32_4x4x4_16b_bf16 v[224:227], v[228:229], v[176:177], v[224:227] cbsz:4 abid:0\n"
    "v_exp_f32 v138, v138 clamp\n"
    "v_exp_f32 v139, v139 clamp\n"
    "v_mfma_f32_4x4x4_16b_bf16 v[224:227], v[228:229], v[178:179], v[224:227] cbsz:4 abid:1\n"
    "v_exp_f32 v140, v140 clamp\n"
    "v_exp_f32 v141, v141 clamp\n"
    "v_mfma_f32_4x4x4_16b_bf16 v[224:227], v[228:229], v[180:181], v[224:227] cbsz:4 abid:2\n"
    "v_exp_f32 v142, v142 clamp\n"
    "v_exp_f32 v143, v143 clamp\n"
    "v_mfma_f32_4x4x4_16b_bf16 v[224:227], v[228:229], v[182:183], v[224:227] cbsz:4 abid:3\n"
    "s_waitcnt vmcnt(4)\n"
    "s_memrealtime s[34:35]\n"
    "ds_read_b64_tr_b16 v[176:177], v240 offset:6144\n"
    "ds_read_b64_tr_b16 v[178:179], v241 offset:6400\n"
    "ds_read_b64_tr_b16 v[180:181], v242 offset:6656\n"
    "ds_read_b64_tr_b16 v[182:183], v243 offset:6912\n"
    "ds_read_b32 v236, v253 offset:0\n"
    "ds_read_b32 v238, v253 offset:4\n"
    "ds_read_b128 v[160:163], v234 offset:0\n"
    "ds_read_b128 v[164:167], v235 offset:0\n"
    "v_pk_fma_f32 v[128:129], v[128:129], s[70:71], v[96:97]\n"
    "v_pk_fma_f32 v[130:131], v[130:131], s[70:71], v[98:99]\n"
    "v_pk_add_f32 v[230:231], v[128:129], v[130:131]\n"
    "v_pk_mul_f32 v[232:233], v[128:129], v[130:131]\n"
    "v_pk_fma_f32 v[132:133], v[132:133], s[70:71], v[100:101]\n"
    "v_pk_fma_f32 v[230:231], v[230:231], v[132:133], v[232:233]\n"
    "v_pk_mul_f32 v[232:233], v[232:233], v[132:133]\n"
    "v_pk_fma_f32 v[134:135], v[134:135], s[70:71], v[102:103]\n"
    "v_pk_fma_f32 v[230:231], v[230:231], v[134:135], v[232:233]\n"
    "v_pk_mul_f32 v[232:233], v[232:233], v[134:135]\n"
    "v_pk_fma_f32 v[136:137], v[136:137], s[70:71], v[104:105]\n"
    "v_pk_fma_f32 v[230:231], v[230:231], v[136:137], v[232:233]\n"
    "v_pk_mul_f32 v[232:233], v[232:233], v[136:137]\n"
    "v_pk_fma_f32 v[138:139], v[138:139], s[70:71], v[106:107]\n"
    "v_pk_fma_f32 v[230:231], v[230:231], v[138:139], v[232:233]\n"
    "v_pk_mul_f32 v[232:233], v[232:233], v[138:139]\n"
    "v_pk_fma_f32 v[140:141], v[140:141], s[70:71], v[108:109]\n"
    "v_pk_fma_f32 v[230:231], v[230:231], v[140:141], v[232:233]\n"
    "v_pk_mul_f32 v[232:233], v[232:233], v[140:141]\n"
    "v_pk_fma_f32 v[142:143], v[142:143], s[70:71], v[110:111]\n"
    "v_pk_fma_f32 v[230:231], v[230:231], v[142:143], v[232:233]\n"
    "v_pk_mul_f32 v[232:233], v[232:233], v[142:143]\n"
    "v_rcp_f32 v232, v232\n"
    "v_rcp_f32 v233, v233\n"
    "v_fma_f32 v230, v230, v232, v247\n"
    "v_fmac_f32 v230, v231, v233\n"
    "v_lshlrev_b32 v232, 2, v249\n"
    "v_lshrrev_b32 v233, 2, v249\n"
    "v_and_b32 v232, 56, v232\n"
    "v_and_b32 v233, 4, v233\n"
    "v_add3_u32 v232, v232, v233, s81\n"
    "ds_read_b32 v232, v232\n"
    "s_add_u32 s81, s81, 64\n"
    "s_waitcnt lgkmcnt(0)\n"
    "v_lshlrev_b32 v232, 1, v232\n"
    "buffer_load_ushort v232, v232, s[44:47], 0 offen\n"
    "s_waitcnt vmcnt(0)\n"
    "v_lshlrev_b32 v232, 16, v232\n"
    "v_mul_f32 v230, v230, v232\n"
    "v_cvt_pk_bf16_f32 v230, v230, v230\n"
    "s_add_u32 s67, s67, 64\n"
    "s_waitcnt lgkmcnt(0)\n"
    "ds_write_b16 v245, v230\n"
    "ds_read_b64 v[228:229], v246\n"
    "s_cmp_le_u32 s34, s76\n"
    "s_cbranch_scc1 .Ldin_G2s_wait\n"
    ".Ldin_G2s_go:\n"
    "s_sub_u32 s94, s34, 60\n"
    "s_max_u32 s76, s76, s94\n"
    "s_add_u32 s76, s76, s72\n"
    ".Ldin_G2s_dma:\n"
    "buffer_load_dwordx4 v[236:237], s[40:43], 0 idxen offen offset:0 nt lds\n"
    "buffer_load_dwordx4 v[238:239], s[40:43], 0 idxen offen offset:1024 nt lds\n"
    ".Ldin_G2s_nd:\n"
    "s_cmp_eq_u32 s67, s82\n"
    "s_cbranch_scc1 .Ldin_L3s\n"
    ".Ldin_G3s:\n"
    "v_mfma_f32_16x16x32_bf16 v[128:131], v[0:3], v[160:163], v[112:115]\n"
    "v_exp_f32 v144, v144 clamp\n"
    "v_mfma_f32_16x16x32_bf16 v[128:131], v[4:7], v[164:167], v[128:131]\n"
    "v_exp_f32 v145, v145 clamp\n"
    "v_mfma_f32_16x16x32_bf16 v[132:135], v[8:11], v[160:163], v[116:119]\n"
    "v_exp_f32 v146, v146 clamp\n"
    "v_mfma_f32_16x16x32_bf16 v[132:135], v[12:15], v[164:167], v[132:135]\n"
    "v_exp_f32 v147, v147 clamp\n"
    "v_mfma_f32_16x16x32_bf16 v[136:139], v[16:19], v[160:163], v[120:123]\n"
    "v_exp_f32 v148, v148 clamp\n"
    "v_mfma_f32_16x16x32_bf16 v[136:139], v[20:23], v[164:167], v[136:139]\n"
    "v_exp_f32 v149, v149 clamp\n"
    "v_mfma_f32_16x16x32_bf16 v[140:143], v[24:27], v[160:163], v[124:127]\n"
    "v_exp_f32 v150, v150 clamp\n"
    "v_mfma_f32_16x16x32_bf16 v[140:143], v[28:31], v[164:167], v[140:143]\n"
    "v_exp_f32 v151, v151 clamp\n"
    "v_exp_f32 v152, v152 clamp\n"
    "s_cmp_eq_u32 s67, s80\n"
    "s_cselect_b32 s67, s65, s67\n"
    "s_add_u32 m0, s68, 0\n"
    "v_exp_f32 v153, v153 clamp\n"
    "v_add_u32 v253, s67, v244\n"
    "s_waitcnt lgkmcnt(0)\n"
    "v_mfma_f32_4x4x4_16b_bf16 v[224:227], v[228:229], v[168:169], v[224:227] cbsz:4 abid:0\n"
    "v_exp_f32 v154, v154 clamp\n"
    "v_exp_f32 v155, v155 clamp\n"
    "v_mfma_f32_4x4x4_16b_bf16 v[224:227], v[228:229], v[170:171], v[224:227] cbsz:4 abid:1\n"
    "v_exp_f32 v156, v156 clamp\n"
    "v_exp_f32 v157, v157 clamp\n"
    "v_mfma_f32_4x4x4_16b_bf16 v[224:227], v[228:229], v[172:173], v[224:227] cbsz:4 abid:2\n"
    "v_exp_f32 v158, v158 clamp\n"
    "v_exp_f32 v159, v159 clamp\n"
    "v_mfma_f32_4x4x4_16b_bf16 v[224:227], v[228:229], v[174:175], v[224:227] cbsz:4 abid:3\n"
    "s_waitcnt vmcnt(4)\n"
    "s_memrealtime s[34:35]\n"
    "ds_read_b64_tr_b16 v[168:169], v240 offset:0\n"
    "ds_read_b64_tr_b16 v[170:171], v241 offset:256\n"
    "ds_read_b64_tr_b16 v[172:173], v242 offset:512\n"
    "ds_read_b64_tr_b16 v[174:175], v243 offset:768\n"
    "ds_read_b32 v236, v253 offset:0\n"
    "ds_read_b32 v238, v253 offset:4\n"
    "ds_read_b128 v[160:163], v234 offset:2048\n"
    "ds_read_b128 v[164:167], v235 offset:2048\n"
    "v_pk_fma_f32 v[144:145], v[144:145], s[70:71], v[96:97]\n"
    "v_pk_fma_f32 v[146:147], v[146:147], s[70:71], v[98:99]\n"
    "v_pk_add_f32 v[230:231], v[144:145], v[146:147]\n"
    "v_pk_mul_f32 v[232:233], v[144:145], v[146:147]\n"
    "v_pk_fma_f32 v[148:149], v[148:149], s[70:71], v[100:101]\n"
    "v_pk_fma_f32 v[230:231], v[230:231], v[148:149], v[232:233]\n"
    "v_pk_mul_f32 v[232:233], v[232:233], v[148:149]\n"
    "v_pk_fma_f32 v[150:151], v[150:151], s[70:71], v[102:103]\n"
    "v_pk_fma_f32 v[230:231], v[230:231], v[150:151], v[232:233]\n"
    "v_pk_mul_f32 v[232:233], v[232:233], v[150:151]\n"
    "v_pk_fma_f32 v[152:153], v[152:153], s[70:71], v[104:105]\n"
    "v_pk_fma_f32 v[230:231], v[230:231], v[152:153], v[232:233]\n"
    "v_pk_mul_f32 v[232:233], v[232:233], v[152:153]\n"
    "v_pk_fma_f32 v[154:155], v[154:155], s[70:71], v[106:107]\n"
    "v_pk_fma_f32 v[230:231], v[230:231], v[154:155], v[232:233]\n"
    "v_pk_mul_f32 v[232:233], v[232:233], v[154:155]\n"
    "v_pk_fma_f32 v[156:157], v[156:157], s[70:71], v[108:109]\n"
    "v_pk_fma_f32 v[230:231], v[230:231], v[156:157], v[232:233]\n"
    "v_pk_mul_f32 v[232:233], v[232:233], v[156:157]\n"
    "v_pk_fma_f32 v[158:159], v[158:159], s[70:71], v[110:111]\n"
    "v_pk_fma_f32 v[230:231], v[230:231], v[158:159], v[232:233]\n"
    "v_pk_mul_f32 v[232:233], v[232:233], v[158:159]\n"
    "v_rcp_f32 v232, v232\n"
    "v_rcp_f32 v233, v233\n"
    "v_fma_f32 v230, v230, v232, v247\n"
    "v_fmac_f32 v230, v231, v233\n"
    "v_lshlrev_b32 v232, 2, v249\n"
    "v_lshrrev_b32 v233, 2, v249\n"
    "v_and_b32 v232, 56, v232\n"
    "v_and_b32 v233, 4, v233\n"
    "v_add3_u32 v232, v232, v233, s81\n"
    "ds_read_b32 v232, v232\n"
    "s_add_u32 s81, s81, 64\n"
    "s_waitcnt lgkmcnt(0)\n"
    "v_lshlrev_b32 v232, 1, v232\n"
    "buffer_load_ushort v232, v232, s[44:47], 0 offen\n"
    "s_waitcnt vmcnt(0)\n"
    "v_lshlrev_b32 v232, 16, v232\n"
    "v_mul_f32 v230, v230, v232\n"
    "v_cvt_pk_bf16_f32 v230, v230, v230\n"
    "s_add_u32 s67, s67, 64\n"
    "s_waitcnt lgkmcnt(0)\n"
    "ds_write_b16 v245, v230\n"
    "ds_read_b64 v[228:229], v246\n"
    "s_cmp_le_u32 s34, s76\n"
    "s_cbranch_scc1 .Ldin_G3s_wait\n"
    ".Ldin_G3s_go:\n"
    "s_add_u32 s76, s76, s72\n"
    ".Ldin_G3s_dma:\n"
    "buffer_load_dwordx4 v[236:237], s[40:43], 0 idxen offen offset:0 nt lds\n"
    "buffer_load_dwordx4 v[238:239], s[40:43], 0 idxen offen offset:1024 nt lds\n"
    ".Ldin_G3s_nd:\n"
    "s_cmp_eq_u32 s67, s82\n"
    "s_cbranch_scc1 .Ldin_L0s\n"
    ".Ldin_G0s:\n"
    "v_mfma_f32_16x16x32_bf16 v[144:147], v[0:3], v[160:163], v[112:115]\n"
    "v_exp_f32 v128, v128 clamp\n"
    "v_mfma_f32_16x16x32_bf16 v[144:147], v[4:7], v[164:167], v[144:147]\n"
    "v_exp_f32 v129, v129 clamp\n"
    "v_mfma_f32_16x16x32_bf16 v[148:151], v[8:11], v[160:163], v[116:119]\n"
    "v_exp_f32 v130, v130 clamp\n"
    "v_mfma_f32_16x16x32_bf16 v[148:151], v[12:15], v[164:167], v[148:151]\n"
    "v_exp_f32 v131, v131 clamp\n"
    "v_mfma_f32_16x16x32_bf16 v[152:155], v[16:19], v[160:163], v[120:123]\n"
    "v_exp_f32 v132, v132 clamp\n"
    "v_mfma_f32_16x16x32_bf16 v[152:155], v[20:23], v[164:167], v[152:155]\n"
    "v_exp_f32 v133, v133 clamp\n"
    "v_mfma_f32_16x16x32_bf16 v[156:159], v[24:27], v[160:163], v[124:127]\n"
    "v_exp_f32 v134, v134 clamp\n"
    "v_mfma_f32_16x16x32_bf16 v[156:159], v[28:31], v[164:167], v[156:159]\n"
    "v_exp_f32 v135, v135 clamp\n"
    "v_exp_f32 v136, v136 clamp\n"
    "s_cmp_eq_u32 s67, s80\n"
    "s_cselect_b32 s67, s65, s67\n"
    "s_add_u32 m0, s68, 2048\n"
    "v_exp_f32 v137, v137 clamp\n"
    "v_add_u32_e64 v253, s67, v244\n"
    "s_waitcnt lgkmcnt(0)\n"
    "v_mfma_f32_4x4x4_16b_bf16 v[224:227], v[228:229], v[176:177], v[224:227] cbsz:4 abid:0\n"
    "v_exp_f32 v138, v138 clamp\n"
    "v_exp_f32 v139, v139 clamp\n"
    "v_mfma_f32_4x4x4_16b_bf16 v[224:227], v[228:229], v[178:179], v[224:227] cbsz:4 abid:1\n"
    "v_exp_f32 v140, v140 clamp\n"
    "v_exp_f32 v141, v141 clamp\n"
    "v_mfma_f32_4x4x4_16b_bf16 v[224:227], v[228:229], v[180:181], v[224:227] cbsz:4 abid:2\n"
    "v_exp_f32 v142, v142 clamp\n"
    "v_exp_f32 v143, v143 clamp\n"
    "v_mfma_f32_4x4x4_16b_bf16 v[224:227], v[228:229], v[182:183], v[224:227] cbsz:4 abid:3\n"
    "s_waitcnt vmcnt(4)\n"
    "s_memrealtime s[34:35]\n"
    "ds_read_b64_tr_b16 v[176:177], v240 offset:2048\n"
    "ds_read_b64_tr_b16 v[178:179], v241 offset:2304\n"
    "ds_read_b64_tr_b16 v[180:181], v242 offset:2560\n"
    "ds_read_b64_tr_b16 v[182:183], v243 offset:2816\n"
    "ds_read_b32 v236, v253 offset:0\n"
    "ds_read_b32 v238, v253 offset:4\n"
    "ds_read_b128 v[160:163], v234 offset:4096\n"
    "ds_read_b128 v[164:167], v235 offset:4096\n"
    "v_pk_fma_f32 v[128:129], v[128:129], s[70:71], v[96:97]\n"
    "v_pk_fma_f32 v[130:131], v[130:131], s[70:71], v[98:99]\n"
    "v_pk_add_f32 v[230:231], v[128:129], v[130:131]\n"
    "v_pk_mul_f32 v[232:233], v[128:129], v[130:131]\n"
    "v_pk_fma_f32 v[132:133], v[132:133], s[70:71], v[100:101]\n"
    "v_pk_fma_f32 v[230:231], v[230:231], v[132:133], v[232:233]\n"
    "v_pk_mul_f32 v[232:233], v[232:233], v[132:133]\n"
    "v_pk_fma_f32 v[134:135], v[134:135], s[70:71], v[102:103]\n"
    "v_pk_fma_f32 v[230:231], v[230:231], v[134:135], v[232:233]\n"
    "v_pk_mul_f32 v[232:233], v[232:233], v[134:135]\n"
    "v_pk_fma_f32 v[136:137], v[136:137], s[70:71], v[104:105]\n"
    "v_pk_fma_f32 v[230:231], v[230:231], v[136:137], v[232:233]\n"
    "v_pk_mul_f32 v[232:233], v[232:233], v[136:137]\n"
    "v_pk_fma_f32 v[138:139], v[138:139], s[70:71], v[106:107]\n"
    "v_pk_fma_f32 v[230:231], v[230:231], v[138:139], v[232:233]\n"
    "v_pk_mul_f32 v[232:233], v[232:233], v[138:139]\n"
    "v_pk_fma_f32 v[140:141], v[140:141], s[70:71], v[108:109]\n"
    "v_pk_fma_f32 v[230:231], v[230:231], v[140:141], v[232:233]\n"
    "v_pk_mul_f32 v[232:233], v[232:233], v[140:141]\n"
    "v_pk_fma_f32 v[142:143], v[142:143], s[70:71], v[110:111]\n"
    "v_pk_fma_f32 v[230:231], v[230:231], v[142:143], v[232:233]\n"
    "v_pk_mul_f32 v[232:233], v[232:233], v[142:143]\n"
    "v_rcp_f32 v232, v232\n"
    "v_rcp_f32 v233, v233\n"
    "v_fma_f32 v230, v230, v232, v247\n"
    "v_fmac_f32 v230, v231, v233\n"
    "v_lshlrev_b32 v232, 2, v249\n"
    "v_lshrrev_b32 v233, 2, v249\n"
    "v_and_b32 v232, 56, v232\n"
    "v_and_b32 v233, 4, v233\n"
    "v_add3_u32 v232, v232, v233, s81\n"
    "ds_read_b32 v232, v232\n"
    "s_add_u32 s81, s81, 64\n"
    "s_waitcnt lgkmcnt(0)\n"
    "v_lshlrev_b32 v232, 1, v232\n"
    "buffer_load_ushort v232, v232, s[44:47], 0 offen\n"
    "s_waitcnt vmcnt(0)\n"
    "v_lshlrev_b32 v232, 16, v232\n"
    "v_mul_f32 v230, v230, v232\n"
    "v_cvt_pk_bf16_f32 v230, v230, v230\n"
    "s_add_u32 s67, s67, 64\n"
    "s_waitcnt lgkmcnt(0)\n"
    "ds_write_b16 v245, v230\n"
    "ds_read_b64 v[228:229], v246\n"
    "s_cmp_le_u32 s34, s76\n"
    "s_cbranch_scc1 .Ldin_G0s_wait\n"
    ".Ldin_G0s_go:\n"
    "s_sub_u32 s94, s34, 60\n"
    "s_max_u32 s76, s76, s94\n"
    "s_add_u32 s76, s76, s72\n"
    ".Ldin_G0s_dma:\n"
    "buffer_load_dwordx4 v[236:237], s[40:43], 0 idxen offen offset:0 nt lds\n"
    "buffer_load_dwordx4 v[238:239], s[40:43], 0 idxen offen offset:1024 nt lds\n"
    ".Ldin_G0s_nd:\n"
    "s_cmp_eq_u32 s67, s82\n"
    "s_cbranch_scc1 .Ldin_L1s\n"
    ".Ldin_G1s:\n"
    "v_mfma_f32_16x16x32_bf16 v[128:131], v[0:3], v[160:163], v[112:115]\n"
    "v_exp_f32 v144, v144 clamp\n"
    "v_mfma_f32_16x16x32_bf16 v[128:131], v[4:7], v[164:167], v[128:131]\n"
    "v_exp_f32 v145, v145 clamp\n"
    "v_mfma_f32_16x16x32_bf16 v[132:135], v[8:11], v[160:163], v[116:119]\n"
    "v_exp_f32 v146, v146 clamp\n"
    "v_mfma_f32_16x16x32_bf16 v[132:135], v[12:15], v[164:167], v[132:135]\n"
    "v_exp_f32 v147, v147 clamp\n"
    "v_mfma_f32_16x16x32_bf16 v[136:139], v[16:19], v[160:163], v[120:123]\n"
    "v_exp_f32 v148, v148 clamp\n"
    "v_mfma_f32_16x16x32_bf16 v[136:139], v[20:23], v[164:167], v[136:139]\n"
    "v_exp_f32 v149, v149 clamp\n"
    "v_mfma_f32_16x16x32_bf16 v[140:143], v[24:27], v[160:163], v[124:127]\n"
    "v_exp_f32 v150, v150 clamp\n"
    "v_mfma_f32_16x16x32_bf16 v[140:143], v[28:31], v[164:167], v[140:143]\n"
    "v_exp_f32 v151, v151 clamp\n"
    "v_exp_f32 v152, v152 clamp\n"
    "s_cmp_eq_u32 s67, s80\n"
    "s_cselect_b32 s67, s65, s67\n"
    "s_add_u32 m0, s68, 4096\n"
    "v_exp_f32 v153, v153 clamp\n"
    "v_add_u32_e64 v253, s67, v244\n"
    "s_waitcnt lgkmcnt(0)\n"
    "v_mfma_f32_4x4x4_16b_bf16 v[224:227], v[228:229], v[168:169], v[224:227] cbsz:4 abid:0\n"
    "v_exp_f32 v154, v154 clamp\n"
    "v_exp_f32 v155, v155 clamp\n"
    "v_mfma_f32_4x4x4_16b_bf16 v[224:227], v[228:229], v[170:171], v[224:227] cbsz:4 abid:1\n"
    "v_exp_f32 v156, v156 clamp\n"
    "v_exp_f32 v157, v157 clamp\n"
    "v_mfma_f32_4x4x4_16b_bf16 v[224:227], v[228:229], v[172:173], v[224:227] cbsz:4 abid:2\n"
    "v_exp_f32 v158, v158 clamp\n"
    "v_exp_f32 v159, v159 clamp\n"
    "v_mfma_f32_4x4x4_16b_bf16 v[224:227], v[228:229], v[174:175], v[224:227] cbsz:4 abid:3\n"
    "s_waitcnt vmcnt(4)\n"
    "s_memrealtime s[34:35]\n"
    "ds_read_b64_tr_b16 v[168:169], v240 offset:4096\n"
    "ds_read_b64_tr_b16 v[170:171], v241 offset:4352\n"
    "ds_read_b64_tr_b16 v[172:173], v242 offset:4608\n"
    "ds_read_b64_tr_b16 v[174:175], v243 offset:4864\n"
    "ds_read_b32 v236, v253 offset:0\n"
    "ds_read_b32 v238, v253 offset:4\n"
    "ds_read_b128 v[160:163], v234 offset:6144\n"
    "ds_read_b128 v[164:167], v235 offset:6144\n"
    "v_pk_fma_f32 v[144:145], v[144:145], s[70:71], v[96:97]\n"
    "v_pk_fma_f32 v[146:147], v[146:147], s[70:71], v[98:99]\n"
    "v_pk_add_f32 v[230:231], v[144:145], v[146:147]\n"
    "v_pk_mul_f32 v[232:233], v[144:145], v[146:147]\n"
    "v_pk_fma_f32 v[148:149], v[148:149], s[70:71], v[100:101]\n"
    "v_pk_fma_f32 v[230:231], v[230:231], v[148:149], v[232:233]\n"
    "v_pk_mul_f32 v[232:233], v[232:233], v[148:149]\n"
    "v_pk_fma_f32 v[150:151], v[150:151], s[70:71], v[102:103]\n"
    "v_pk_fma_f32 v[230:231], v[230:231], v[150:151], v[232:233]\n"
    "v_pk_mul_f32 v[232:233], v[232:233], v[150:151]\n"
    "v_pk_fma_f32 v[152:153], v[152:153], s[70:71], v[104:105]\n"
    "v_pk_fma_f32 v[230:231], v[230:231], v[152:153], v[232:233]\n"
    "v_pk_mul_f32 v[232:233], v[232:233], v[152:153]\n"
    "v_pk_fma_f32 v[154:155], v[154:155], s[70:71], v[106:107]\n"
    "v_pk_fma_f32 v[230:231], v[230:231], v[154:155], v[232:233]\n"
    "v_pk_mul_f32 v[232:233], v[232:233], v[154:155]\n"
    "v_pk_fma_f32 v[156:157], v[156:157], s[70:71], v[108:109]\n"
    "v_pk_fma_f32 v[230:231], v[230:231], v[156:157], v[232:233]\n"
    "v_pk_mul_f32 v[232:233], v[232:233], v[156:157]\n"
    "v_pk_fma_f32 v[158:159], v[158:159], s[70:71], v[110:111]\n"
    "v_pk_fma_f32 v[230:231], v[230:231], v[158:159], v[232:233]\n"
    "v_pk_mul_f32 v[232:233], v[232:233], v[158:159]\n"
    "v_rcp_f32 v232, v232\n"
    "v_rcp_f32 v233, v233\n"
    "v_fma_f32 v230, v230, v232, v247\n"
    "v_fmac_f32 v230, v231, v233\n"
    "v_lshlrev_b32 v232, 2, v249\n"
    "v_lshrrev_b32 v233, 2, v249\n"
    "v_and_b32 v232, 56, v232\n"
    "v_and_b32 v233, 4, v233\n"
    "v_add3_u32 v232, v232, v233, s81\n"
    "ds_read_b32 v232, v232\n"
    "s_add_u32 s81, s81, 64\n"
    "s_waitcnt lgkmcnt(0)\n"
    "v_lshlrev_b32 v232, 1, v232\n"
    "buffer_load_ushort v232, v232, s[44:47], 0 offen\n"
    "s_waitcnt vmcnt(0)\n"
    "v_lshlrev_b32 v232, 16, v232\n"
    "v_mul_f32 v230, v230, v232\n"
    "v_cvt_pk_bf16_f32 v230, v230, v230\n"
    "s_add_u32 s67, s67, 64\n"
    "s_waitcnt lgkmcnt(0)\n"
    "ds_write_b16 v245, v230\n"
    "ds_read_b64 v[228:229], v246\n"
    "s_cmp_le_u32 s34, s76\n"
    "s_cbranch_scc1 .Ldin_G1s_wait\n"
    ".Ldin_G1s_go:\n"
    "s_add_u32 s76, s76, s72\n"
    ".Ldin_G1s_dma:\n"
    "buffer_load_dwordx4 v[236:237], s[40:43], 0 idxen offen offset:0 nt lds\n"
    "buffer_load_dwordx4 v[238:239], s[40:43], 0 idxen offen offset:1024 nt lds\n"
    ".Ldin_G1s_nd:\n"
    "s_cmp_eq_u32 s67, s82\n"
    "s_cbranch_scc1 .Ldin_L2s\n"
    "s_branch .Ldin_G2s\n"
    ".Ldin_B1s_wait:\n"
    "s_cmp_lt_i32 s59, s57\n"
    "s_cbranch_scc1 .Ldin_B1s_w2\n"
    "s_sub_u32 s94, s67, s65\n"
    "s_cmp_gt_u32 s94, 256\n"
    "s_cbranch_scc1 .Ldin_B1s_dma\n"
    "s_cmp_eq_u32 s94, 64\n"
    "s_waitcnt vmcnt(2)\n"
    "s_cbranch_scc1 .Ldin_B1s_nd\n"
    "s_waitcnt vmcnt(0)\n"
    "s_branch .Ldin_B1s_nd\n"
    ".Ldin_B1s_slp:\n"
    "s_sleep 1\n"
    ".Ldin_B1s_w2:\n"
    "s_memrealtime s[34:35]\n"
    "s_waitcnt lgkmcnt(0)\n"
    "s_sub_u32 s94, s34, s76\n"
    "s_cmp_lt_i32 s94, 0\n"
    "s_cbranch_scc1 .Ldin_B1s_slp\n"
    "s_branch .Ldin_B1s_go\n"
    ".Ldin_G2s_wait:\n"
    "s_cmp_lt_i32 s59, s57\n"
    "s_cbranch_scc1 .Ldin_G2s_w2\n"
    "s_sub_u32 s94, s67, s65\n"
    "s_cmp_gt_u32 s94, 256\n"
    "s_cbranch_scc1 .Ldin_G2s_dma\n"
    "s_cmp_eq_u32 s94, 64\n"
    "s_waitcnt vmcnt(2)\n"
    "s_cbranch_scc1 .Ldin_G2s_nd\n"
    "s_waitcnt vmcnt(0)\n"
    "s_branch .Ldin_G2s_nd\n"
    ".Ldin_G2s_slp:\n"
    "s_sleep 1\n"
    ".Ldin_G2s_w2:\n"
    "s_memrealtime s[34:35]\n"
    "s_waitcnt lgkmcnt(0)\n"
    "s_sub_u32 s94, s34, s76\n"
    "s_cmp_lt_i32 s94, 0\n"
    "s_cbranch_scc1 .Ldin_G2s_slp\n"
    "s_branch .Ldin_G2s_go\n"
    ".Ldin_G3s_wait:\n"
    "s_cmp_lt_i32 s59, s57\n"
    "s_cbranch_scc1 .Ldin_G3s_w2\n"
    "s_sub_u32 s94, s67, s65\n"
    "s_cmp_gt_u32 s94, 256\n"
    "s_cbranch_scc1 .Ldin_G3s_dma\n"
    "s_cmp_eq_u32 s94, 64\n"
    "s_waitcnt vmcnt(2)\n"
    "s_cbranch_scc1 .Ldin_G3s_nd\n"
    "s_waitcnt vmcnt(0)\n"
    "s_branch .Ldin_G3s_nd\n"
    ".Ldin_G3s_slp:\n"
    "s_sleep 1\n"
    ".Ldin_G3s_w2:\n"
    "s_memrealtime s[34:35]\n"
    "s_waitcnt lgkmcnt(0)\n"
    "s_sub_u32 s94, s34, s76\n"
    "s_cmp_lt_i32 s94, 0\n"
    "s_cbranch_scc1 .Ldin_G3s_slp\n"
    "s_branch .Ldin_G3s_go\n"
    ".Ldin_G0s_wait:\n"
    "s_cmp_lt_i32 s59, s57\n"
    "s_cbranch_scc1 .Ldin_G0s_w2\n"
    "s_sub_u32 s94, s67, s65\n"
    "s_cmp_gt_u32 s94, 256\n"
    "s_cbranch_scc1 .Ldin_G0s_dma\n"
    "s_cmp_eq_u32 s94, 64\n"
    "s_waitcnt vmcnt(2)\n"
    "s_cbranch_scc1 .Ldin_G0s_nd\n"
    "s_waitcnt vmcnt(0)\n"
    "s_branch .Ldin_G0s_nd\n"
    ".Ldin_G0s_slp:\n"
    "s_sleep 1\n"
    ".Ldin_G0s_w2:\n"
    "s_memrealtime s[34:35]\n"
    "s_waitcnt lgkmcnt(0)\n"
    "s_sub_u32 s94, s34, s76\n"
    "s_cmp_lt_i32 s94, 0\n"
    "s_cbranch_scc1 .Ldin_G0s_slp\n"
    "s_branch .Ldin_G0s_go\n"
    ".Ldin_G1s_wait:\n"
    "s_cmp_lt_i32 s59, s57\n"
    "s_cbranch_scc1 .Ldin_G1s_w2\n"
    "s_sub_u32 s94, s67, s65\n"
    "s_cmp_gt_u32 s94, 256\n"
    "s_cbranch_scc1 .Ldin_G1s_dma\n"
    "s_cmp_eq_u32 s94, 64\n"
    "s_waitcnt vmcnt(2)\n"
    "s_cbranch_scc1 .Ldin_G1s_nd\n"
    "s_waitcnt vmcnt(0)\n"
    "s_branch .Ldin_G1s_nd\n"
    ".Ldin_G1s_slp:\n"
    "s_sleep 1\n"
    ".Ldin_G1s_w2:\n"
    "s_memrealtime s[34:35]\n"
    "s_waitcnt lgkmcnt(0)\n"
    "s_sub_u32 s94, s34, s76\n"
    "s_cmp_lt_i32 s94, 0\n"
    "s_cbranch_scc1 .Ldin_G1s_slp\n"
    "s_branch .Ldin_G1s_go\n"
    ".p2align 5\n"
    ".Ldin_L2s:\n"
    "s_setprio 0\n"
    "s_cmp_ge_i32 s59, s57\n"
    "s_cbranch_scc1 .Ldin_E0s\n"
    "s_add_u32 s76, s76, s73\n"
    "v_mov_b32_e64 v253, s63\n"
    "ds_read_b64 v[232:233], v253\n"
    "v_mfma_f32_4x4x4_16b_bf16 v[144:147], v[184:185], v[32:33], v[64:67] cbsz:2 abid:0\n"
    "ds_read_b128 v[112:115], v252 offset:17408\n"
    "v_mfma_f32_4x4x4_16b_bf16 v[148:151], v[184:185], v[34:35], v[68:71] cbsz:2 abid:1\n"
    "ds_read_b128 v[116:119], v252 offset:17424\n"
    "v_mfma_f32_4x4x4_16b_bf16 v[152:155], v[184:185], v[36:37], v[72:75] cbsz:2 abid:2\n"
    "ds_read_b128 v[120:123], v252 offset:17536\n"
    "ds_read_b128 v[124:127], v252 offset:17552\n"
    "v_cvt_pk_bf16_f32 v0, v144, v145\n"
    "v_cvt_pk_bf16_f32 v1, v146, v147\n"
    "v_mfma_f32_4x4x4_16b_bf16 v[156:159], v[184:185], v[38:39], v[76:79] cbsz:2 abid:3\n"
    "v_cvt_pk_bf16_f32 v2, v148, v149\n"
    "v_cvt_pk_bf16_f32 v3, v150, v151\n"
    "v_mfma_f32_4x4x4_16b_bf16 v[144:147], v[184:185], v[40:41], v[80:83] cbsz:2 abid:0\n"
    "v_cvt_pk_bf16_f32 v4, v152, v153\n"
    "v_cvt_pk_bf16_f32 v5, v154, v155\n"
    "v_mfma_f32_4x4x4_16b_bf16 v[148:151], v[184:185], v[42:43], v[84:87] cbsz:2 abid:1\n"
    "v_cvt_pk_bf16_f32 v6, v156, v157\n"
    "v_cvt_pk_bf16_f32 v7, v158, v159\n"
    "v_mfma_f32_4x4x4_16b_bf16 v[152:155], v[184:185], v[44:45], v[88:91] cbsz:2 abid:2\n"
    "v_cvt_pk_bf16_f32 v8, v144, v145\n"
    "v_cvt_pk_bf16_f32 v9, v146, v147\n"
    "v_mfma_f32_4x4x4_16b_bf16 v[156:159], v[184:185], v[46:47], v[92:95] cbsz:2 abid:3\n"
    "v_cvt_pk_bf16_f32 v10, v148, v149\n"
    "v_cvt_pk_bf16_f32 v11, v150, v151\n"
    "v_mfma_f32_4x4x4_16b_bf16 v[144:147], v[184:185], v[48:49], v[192:195] cbsz:2 abid:0\n"
    "v_cvt_pk_bf16_f32 v12, v152, v153\n"
    "v_cvt_pk_bf16_f32 v13, v154, v155\n"
    "v_mfma_f32_4x4x4_16b_bf16 v[148:151], v[184:185], v[50:51], v[196:199] cbsz:2 abid:1\n"
    "v_cvt_pk_bf16_f32 v14, v156, v157\n"
    "v_cvt_pk_bf16_f32 v15, v158, v159\n"
    "v_mfma_f32_4x4x4_16b_bf16 v[152:155], v[184:185], v[52:53], v[200:203] cbsz:2 abid:2\n"
    "s_waitcnt lgkmcnt(0)\n"
    "v_readfirstlane_b32 s60, v232\n"
    "v_cvt_pk_bf16_f32 v16, v144, v145\n"
    "v_cvt_pk_bf16_f32 v17, v146, v147\n"
    "v_mfma_f32_4x4x4_16b_bf16 v[156:159], v[184:185], v[54:55], v[204:207] cbsz:2 abid:3\n"
    "s_cmp_eq_u32 s60, -1\n"
    "s_cbranch_scc1 .Ldin_L2s_poll\n"
    ".Ldin_L2s_pr:\n"
    "v_readfirstlane_b32 s95, v233\n"
    "s_and_b32 s94, s60, 63\n"
    "s_lshl_b32 s94, s94, 6\n"
    "s_add_u32 s94, s94, 68480\n"
    "s_add_u32 s63, s63, 8\n"
    "v_lshl_add_u32 v252, v249, 1, s94\n"
    "ds_read_b32 v252, v252\n"
    "s_sub_u32 s95, s95, s76\n"
    "s_ashr_i32 s95, s95, 4\n"
    "s_min_i32 s95, s95, s74\n"
    "s_max_i32 s72, s95, s75\n"
    "v_cvt_pk_bf16_f32 v18, v148, v149\n"
    "v_cvt_pk_bf16_f32 v19, v150, v151\n"
    "v_mfma_f32_4x4x4_16b_bf16 v[144:147], v[184:185], v[56:57], v[208:211] cbsz:2 abid:0\n"
    "v_cvt_pk_bf16_f32 v20, v152, v153\n"
    "v_cvt_pk_bf16_f32 v21, v154, v155\n"
    "v_mfma_f32_4x4x4_16b_bf16 v[148:151], v[184:185], v[58:59], v[212:215] cbsz:2 abid:1\n"
    "v_cvt_pk_bf16_f32 v22, v156, v157\n"
    "v_cvt_pk_bf16_f32 v23, v158, v159\n"
    "v_mfma_f32_4x4x4_16b_bf16 v[152:155], v[184:185], v[60:61], v[216:219] cbsz:2 abid:2\n"
    "v_cvt_pk_bf16_f32 v24, v144, v145\n"
    "v_cvt_pk_bf16_f32 v25, v146, v147\n"
    "v_mfma_f32_4x4x4_16b_bf16 v[156:159], v[184:185], v[62:63], v[220:223] cbsz:2 abid:3\n"
    "v_cvt_pk_bf16_f32 v26, v148, v149\n"
    "v_cvt_pk_bf16_f32 v27, v150, v151\n"
    "v_cvt_pk_bf16_f32 v28, v152, v153\n"
    "v_cvt_pk_bf16_f32 v29, v154, v155\n"
    "v_exp_f32 v136, v136 clamp\n"
    "v_cvt_pk_bf16_f32 v30, v156, v157\n"
    "v_cvt_pk_bf16_f32 v31, v158, v159\n"
    "v_mfma_f32_16x16x32_bf16 v[144:147], v[0:3], v[160:163], v[112:115]\n"
    "v_exp_f32 v128, v128 clamp\n"
    "v_mfma_f32_16x16x32_bf16 v[144:147], v[4:7], v[164:167], v[144:147]\n"
    "v_exp_f32 v129, v129 clamp\n"
    "v_mfma_f32_16x16x32_bf16 v[148:151], v[8:11], v[160:163], v[116:119]\n"
    "v_exp_f32 v130, v130 clamp\n"
    "v_mfma_f32_16x16x32_bf16 v[148:151], v[12:15], v[164:167], v[148:151]\n"
    "v_exp_f32 v131, v131 clamp\n"
    "v_mfma_f32_16x16x32_bf16 v[152:155], v[16:19], v[160:163], v[120:123]\n"
    "v_exp_f32 v132, v132 clamp\n"
    "v_mfma_f32_16x16x32_bf16 v[152:155], v[20:23], v[164:167], v[152:155]\n"
    "v_exp_f32 v133, v133 clamp\n"
    "v_mfma_f32_16x16x32_bf16 v[156:159], v[24:27], v[160:163], v[124:127]\n"
    "v_exp_f32 v134, v134 clamp\n"
    "v_mfma_f32_16x16x32_bf16 v[156:159], v[28:31], v[164:167], v[156:159]\n"
    "v_exp_f32 v135, v135 clamp\n"
    "s_add_u32 m0, s68, 6144\n"
    "v_exp_f32 v137, v137 clamp\n"
    "v_add_u32 v253, s67, v244\n"
    "v_mfma_f32_4x4x4_16b_bf16 v[224:227], v[228:229], v[176:177], v[224:227] cbsz:4 abid:0\n"
    "v_exp_f32 v138, v138 clamp\n"
    "v_exp_f32 v139, v139 clamp\n"
    "v_mfma_f32_4x4x4_16b_bf16 v[224:227], v[228:229], v[178:179], v[224:227] cbsz:4 abid:1\n"
    "v_exp_f32 v140, v140 clamp\n"
    "v_exp_f32 v141, v141 clamp\n"
    "v_mfma_f32_4x4x4_16b_bf16 v[224:227], v[228:229], v[180:181], v[224:227] cbsz:4 abid:2\n"
    "v_exp_f32 v142, v142 clamp\n"
    "v_exp_f32 v143, v143 clamp\n"
    "v_mfma_f32_4x4x4_16b_bf16 v[224:227], v[228:229], v[182:183], v[224:227] cbsz:4 abid:3\n"
    "s_waitcnt vmcnt(4)\n"
    "ds_read_b64_tr_b16 v[176:177], v240 offset:6144\n"
    "ds_read_b64_tr_b16 v[178:179], v241 offset:6400\n"
    "ds_read_b64_tr_b16 v[180:181], v242 offset:6656\n"
    "ds_read_b64_tr_b16 v[182:183], v243 offset:6912\n"
    "ds_read_b32 v236, v253 offset:0\n"
    "ds_read_b32 v238, v253 offset:4\n"
    "ds_read_b128 v[160:163], v234 offset:0\n"
    "ds_read_b128 v[164:167], v235 offset:0\n"
    "s_waitcnt lgkmcnt(8)\n"
    "s_nop 0\n"
    "v_readlane_b32 s20, v252, 0\n"
    "v_readlane_b32 s21, v252, 1\n"
    "v_readlane_b32 s22, v252, 2\n"
    "v_readlane_b32 s23, v252, 3\n"
    "v_readlane_b32 s24, v252, 4\n"
    "v_readlane_b32 s25, v252, 5\n"
    "v_readlane_b32 s26, v252, 6\n"
    "v_readlane_b32 s27, v252, 7\n"
    "v_pk_fma_f32 v[128:129], v[128:129], s[70:71], v[96:97]\n"
    "v_pk_fma_f32 v[130:131], v[130:131], s[70:71], v[98:99]\n"
    "v_pk_add_f32 v[230:231], v[128:129], v[130:131]\n"
    "v_pk_mul_f32 v[232:233], v[128:129], v[130:131]\n"
    "v_pk_fma_f32 v[132:133], v[132:133], s[70:71], v[100:101]\n"
    "v_pk_fma_f32 v[230:231], v[230:231], v[132:133], v[232:233]\n"
    "v_pk_mul_f32 v[232:233], v[232:233], v[132:133]\n"
    "v_pk_fma_f32 v[134:135], v[134:135], s[70:71], v[102:103]\n"
    "v_pk_fma_f32 v[230:231], v[230:231], v[134:135], v[232:233]\n"
    "v_pk_mul_f32 v[232:233], v[232:233], v[134:135]\n"
    "v_pk_fma_f32 v[136:137], v[136:137], s[70:71], v[104:105]\n"
    "v_pk_fma_f32 v[230:231], v[230:231], v[136:137], v[232:233]\n"
    "v_pk_mul_f32 v[232:233], v[232:233], v[136:137]\n"
    "v_pk_fma_f32 v[138:139], v[138:139], s[70:71], v[106:107]\n"
    "v_pk_fma_f32 v[230:231], v[230:231], v[138:139], v[232:233]\n"
    "v_pk_mul_f32 v[232:233], v[232:233], v[138:139]\n"
    "v_pk_fma_f32 v[140:141], v[140:141], s[70:71], v[108:109]\n"
    "v_pk_fma_f32 v[230:231], v[230:231], v[140:141], v[232:233]\n"
    "v_pk_mul_f32 v[232:233], v[232:233], v[140:141]\n"
    "v_pk_fma_f32 v[142:143], v[142:143], s[70:71], v[110:111]\n"
    "v_pk_fma_f32 v[230:231], v[230:231], v[142:143], v[232:233]\n"
    "v_pk_mul_f32 v[232:233], v[232:233], v[142:143]\n"
    "v_rcp_f32 v232, v232\n"
    "v_rcp_f32 v233, v233\n"
    "v_fma_f32 v230, v230, v232, v247\n"
    "v_fmac_f32 v230, v231, v233\n"
    "v_lshlrev_b32 v232, 2, v249\n"
    "v_lshrrev_b32 v233, 2, v249\n"
    "v_and_b32 v232, 56, v232\n"
    "v_and_b32 v233, 4, v233\n"
    "v_add3_u32 v232, v232, v233, s81\n"
    "ds_read_b32 v232, v232\n"
    "s_add_u32 s81, s81, 64\n"
    "s_waitcnt lgkmcnt(0)\n"
    "v_lshlrev_b32 v232, 1, v232\n"
    "buffer_load_ushort v232, v232, s[44:47], 0 offen\n"
    "s_waitcnt vmcnt(0)\n"
    "v_lshlrev_b32 v232, 16, v232\n"
    "v_mul_f32 v230, v230, v232\n"
    "v_cvt_pk_bf16_f32 v230, v230, v230\n"
    "s_add_u32 s67, s67, 64\n"
    "s_waitcnt lgkmcnt(0)\n"
    "ds_write_b16 v245, v230\n"
    "ds_read_b64 v[228:229], v246\n"
    "s_lshl1_add_u32 s76, s72, s76\n"
    "buffer_load_dwordx4 v[236:237], s[40:43], 0 idxen offen offset:0 nt lds\n"
    "buffer_load_dwordx4 v[238:239], s[40:43], 0 idxen offen offset:1024 nt lds\n"
    "s_and_b32 s62, s58, 63\n"
    "s_mul_i32 s62, s62, 272\n"
    "s_mov_b32 s58, s59\n"
    "s_mov_b32 s59, s60\n"
    "s_mov_b32 s81, s65\n"
    "s_xor_b32 s65, s65, s64\n"
    "s_mov_b32 s80, s83\n"
    "s_add_u32 s82, s65, 256\n"
    "s_cmp_lt_i32 s59, s57\n"
    "s_cselect_b32 s76, s76, -1\n"
    "s_bitcmp1_b32 s58, 30\n"
    "s_cbranch_scc0 .Ldin_A3\n"
    ".Ldin_A3s:\n"
    "v_mfma_f32_16x16x32_bf16 v[128:131], v[0:3], v[160:163], v[112:115]\n"
    "v_exp_f32 v144, v144 clamp\n"
    "v_mfma_f32_16x16x32_bf16 v[128:131], v[4:7], v[164:167], v[128:131]\n"
    "v_exp_f32 v145, v145 clamp\n"
    "v_mfma_f32_16x16x32_bf16 v[132:135], v[8:11], v[160:163], v[116:119]\n"
    "v_exp_f32 v146, v146 clamp\n"
    "v_mfma_f32_16x16x32_bf16 v[132:135], v[12:15], v[164:167], v[132:135]\n"
    "v_exp_f32 v147, v147 clamp\n"
    "v_mfma_f32_16x16x32_bf16 v[136:139], v[16:19], v[160:163], v[120:123]\n"
    "v_exp_f32 v148, v148 clamp\n"
    "v_mfma_f32_16x16x32_bf16 v[136:139], v[20:23], v[164:167], v[136:139]\n"
    "v_exp_f32 v149, v149 clamp\n"
    "v_mfma_f32_16x16x32_bf16 v[140:143], v[24:27], v[160:163], v[124:127]\n"
    "v_exp_f32 v150, v150 clamp\n"
    "v_mfma_f32_16x16x32_bf16 v[140:143], v[28:31], v[164:167], v[140:143]\n"
    "v_exp_f32 v151, v151 clamp\n"
    "v_exp_f32 v152, v152 clamp\n"
    "s_mov_b32 m0, s62\n"
    "v_exp_f32 v153, v153 clamp\n"
    "v_add_u32 v253, s67, v244\n"
    "s_waitcnt lgkmcnt(0)\n"
    "v_mfma_f32_4x4x4_16b_bf16 v[224:227], v[228:229], v[168:169], v[224:227] cbsz:4 abid:0\n"
    "v_exp_f32 v154, v154 clamp\n"
    "v_exp_f32 v155, v155 clamp\n"
    "v_mfma_f32_4x4x4_16b_bf16 v[224:227], v[228:229], v[170:171], v[224:227] cbsz:4 abid:1\n"
    "v_exp_f32 v156, v156 clamp\n"
    "v_exp_f32 v157, v157 clamp\n"
    "v_mfma_f32_4x4x4_16b_bf16 v[224:227], v[228:229], v[172:173], v[224:227] cbsz:4 abid:2\n"
    "v_exp_f32 v158, v158 clamp\n"
    "v_exp_f32 v159, v159 clamp\n"
    "v_mfma_f32_4x4x4_16b_bf16 v[224:227], v[228:229], v[174:175], v[224:227] cbsz:4 abid:3\n"
    "s_waitcnt vmcnt(4)\n"
    "ds_read_b64_tr_b16 v[168:169], v240 offset:0\n"
    "ds_read_b64_tr_b16 v[170:171], v241 offset:256\n"
    "ds_read_b64_tr_b16 v[172:173], v242 offset:512\n"
    "ds_read_b64_tr_b16 v[174:175], v243 offset:768\n"
    "ds_read_b32 v236, v253 offset:0\n"
    "ds_read_b32 v238, v253 offset:4\n"
    "ds_read_b128 v[160:163], v234 offset:2048\n"
    "ds_read_b128 v[164:167], v235 offset:2048\n"
    "s_cbranch_vccz .Ldin_A3s_np\n"
    "s_setprio 1\n"
    ".Ldin_A3s_np:\n"
    "v_pk_fma_f32 v[144:145], v[144:145], s[70:71], v[96:97]\n"
    "v_pk_fma_f32 v[146:147], v[146:147], s[70:71], v[98:99]\n"
    "v_pk_add_f32 v[190:191], v[224:225], v[226:227]\n"
    "v_add_f32 v190, v190, v191\n"
    "ds_write_addtid_b32 v190 offset:32768\n"
    "s_add_u32 m0, s68, 0\n"
    "s_cmp_ge_i32 s59, s57\n"
    "s_cbranch_scc1 .Ldin_A3s_nl\n"
    "s_bfe_u32 s94, s59, 0x180006\n"
    "s_mul_i32 s86, s94, s56\n"
    "v_mbcnt_lo_u32_b32 v184, s20, 0\n"
    "v_mbcnt_lo_u32_b32 v185, s22, 0\n"
    "v_mbcnt_lo_u32_b32 v186, s24, 0\n"
    "v_mbcnt_lo_u32_b32 v187, s26, 0\n"
    "s_bcnt1_i32_b64 s90, s[20:21]\n"
    "s_bcnt1_i32_b64 s91, s[22:23]\n"
    "s_bcnt1_i32_b64 s92, s[24:25]\n"
    "s_bcnt1_i32_b64 s93, s[26:27]\n"
    "v_mbcnt_hi_u32_b32 v184, s21, v184\n"
    "v_mbcnt_hi_u32_b32 v185, s23, v185\n"
    "v_mbcnt_hi_u32_b32 v186, s25, v186\n"
    "v_mbcnt_hi_u32_b32 v187, s27, v187\n"
    "s_lshl2_add_u32 s90, s90, s65\n"
    "s_lshl2_add_u32 s91, s91, s90\n"
    "s_lshl2_add_u32 s92, s92, s91\n"
    "s_lshl2_add_u32 s93, s93, s92\n"
    "v_add_u32 v188, s86, v251\n"
    "v_lshl_add_u32 v184, v184, 2, s65\n"
    "v_lshl_add_u32 v185, v185, 2, s90\n"
    "v_lshl_add_u32 v186, v186, 2, s91\n"
    "v_lshl_add_u32 v187, v187, 2, s92\n"
    "v_mov_b32 v191, -1\n"
    "v_lshl_add_u32 v190, v249, 1, s93\n"
    "ds_write2_b32 v190, v191, v191 offset1:64\n"
    "s_mov_b64 exec, s[20:21]\n"
    "ds_write_b32 v184, v188\n"
    "s_mov_b64 exec, s[22:23]\n"
    "v_add_u32 v189, 64, v188\n"
    "ds_write_b32 v185, v189\n"
    "s_mov_b64 exec, s[24:25]\n"
    "v_add_u32 v190, 128, v188\n"
    "ds_write_b32 v186, v190\n"
    "s_mov_b64 exec, s[26:27]\n"
    "v_add_u32 v189, 192, v188\n"
    "ds_write_b32 v187, v189\n"
    "s_mov_b64 exec, -1\n"
    "s_add_u32 s95, s93, 60\n"
    "s_add_u32 s94, s82, 128\n"
    "s_andn2_b32 s95, s95, 63\n"
    "s_max_u32 s83, s95, s94\n"
    ".Ldin_A3s_nl:\n"
    "v_pk_add_f32 v[230:231], v[144:145], v[146:147]\n"
    "v_pk_mul_f32 v[232:233], v[144:145], v[146:147]\n"
    "v_pk_fma_f32 v[148:149], v[148:149], s[70:71], v[100:101]\n"
    "v_pk_fma_f32 v[230:231], v[230:231], v[148:149], v[232:233]\n"
    "v_pk_mul_f32 v[232:233], v[232:233], v[148:149]\n"
    "v_pk_fma_f32 v[150:151], v[150:151], s[70:71], v[102:103]\n"
    "v_pk_fma_f32 v[230:231], v[230:231], v[150:151], v[232:233]\n"
    "v_pk_mul_f32 v[232:233], v[232:233], v[150:151]\n"
    "v_pk_fma_f32 v[152:153], v[152:153], s[70:71], v[104:105]\n"
    "v_pk_fma_f32 v[230:231], v[230:231], v[152:153], v[232:233]\n"
    "v_pk_mul_f32 v[232:233], v[232:233], v[152:153]\n"
    "v_pk_fma_f32 v[154:155], v[154:155], s[70:71], v[106:107]\n"
    "v_pk_fma_f32 v[230:231], v[230:231], v[154:155], v[232:233]\n"
    "v_pk_mul_f32 v[232:233], v[232:233], v[154:155]\n"
    "v_pk_fma_f32 v[156:157], v[156:157], s[70:71], v[108:109]\n"
    "v_pk_fma_f32 v[230:231], v[230:231], v[156:157], v[232:233]\n"
    "v_pk_mul_f32 v[232:233], v[232:233], v[156:157]\n"
    "v_pk_fma_f32 v[158:159], v[158:159], s[70:71], v[110:111]\n"
    "v_pk_fma_f32 v[230:231], v[230:231], v[158:159], v[232:233]\n"
    "v_pk_mul_f32 v[232:233], v[232:233], v[158:159]\n"
    "v_rcp_f32 v232, v232\n"
    "v_rcp_f32 v233, v233\n"
    "v_fma_f32 v230, v230, v232, v247\n"
    "v_fmac_f32 v230, v231, v233\n"
    "v_lshlrev_b32 v232, 2, v249\n"
    "v_lshrrev_b32 v233, 2, v249\n"
    "v_and_b32 v232, 56, v232\n"
    "v_and_b32 v233, 4, v233\n"
    "v_add3_u32 v232, v232, v233, s81\n"
    "ds_read_b32 v232, v232\n"
    "s_add_u32 s81, s81, 64\n"
    "s_waitcnt lgkmcnt(0)\n"
    "v_lshlrev_b32 v232, 1, v232\n"
    "buffer_load_ushort v232, v232, s[44:47], 0 offen\n"
    "s_waitcnt vmcnt(0)\n"
    "v_lshlrev_b32 v232, 16, v232\n"
    "v_mul_f32 v230, v230, v232\n"
    "v_cvt_pk_bf16_f32 v230, v230, v230\n"
    "s_add_u32 s67, s67, 64\n"
    "s_waitcnt lgkmcnt(0)\n"
    "ds_write_b16 v245, v230\n"
    "ds_read_b64 v[228:229], v246\n"
    "buffer_load_dwordx4 v[236:237], s[40:43], 0 idxen offen offset:0 nt lds\n"
    "buffer_load_dwordx4 v[238:239], s[40:43], 0 idxen offen offset:1024 nt lds\n"
    ".Ldin_B0s:\n"
    "v_mfma_f32_16x16x32_bf16 v[144:147], v[0:3], v[160:163], v[112:115]\n"
    "v_exp_f32 v128, v128 clamp\n"
    "v_mfma_f32_16x16x32_bf16 v[144:147], v[4:7], v[164:167], v[144:147]\n"
    "v_exp_f32 v129, v129 clamp\n"
    "v_mfma_f32_16x16x32_bf16 v[148:151], v[8:11], v[160:163], v[116:119]\n"
    "v_exp_f32 v130, v130 clamp\n"
    "v_mfma_f32_16x16x32_bf16 v[148:151], v[12:15], v[164:167], v[148:151]\n"
    "v_exp_f32 v131, v131 clamp\n"
    "v_mfma_f32_16x16x32_bf16 v[152:155], v[16:19], v[160:163], v[120:123]\n"
    "v_exp_f32 v132, v132 clamp\n"
    "v_mfma_f32_16x16x32_bf16 v[152:155], v[20:23], v[164:167], v[152:155]\n"
    "v_exp_f32 v133, v133 clamp\n"
    "v_mfma_f32_16x16x32_bf16 v[156:159], v[24:27], v[160:163], v[124:127]\n"
    "v_exp_f32 v134, v134 clamp\n"
    "v_mfma_f32_16x16x32_bf16 v[156:159], v[28:31], v[164:167], v[156:159]\n"
    "v_exp_f32 v135, v135 clamp\n"
    "v_exp_f32 v136, v136 clamp\n"
    "s_cmp_eq_u32 s67, s80\n"
    "s_cselect_b32 s67, s65, s67\n"
    "s_and_b32 s94, s59, 63\n"
    "s_mul_i32 s94, s94, 272\n"
    "v_add_u32 v252, s94, v250\n"
    "s_add_u32 m0, s68, 2048\n"
    "v_exp_f32 v137, v137 clamp\n"
    "v_add_u32 v253, s67, v244\n"
    "s_waitcnt lgkmcnt(0)\n"
    "v_mfma_f32_4x4x4_16b_bf16 v[224:227], v[228:229], v[176:177], 0 cbsz:4 abid:0\n"
    "v_exp_f32 v138, v138 clamp\n"
    "v_exp_f32 v139, v139 clamp\n"
    "v_mfma_f32_4x4x4_16b_bf16 v[224:227], v[228:229], v[178:179], v[224:227] cbsz:4 abid:1\n"
    "v_exp_f32 v140, v140 clamp\n"
    "v_exp_f32 v141, v141 clamp\n"
    "v_mfma_f32_4x4x4_16b_bf16 v[224:227], v[228:229], v[180:181], v[224:227] cbsz:4 abid:2\n"
    "v_exp_f32 v142, v142 clamp\n"
    "v_exp_f32 v143, v143 clamp\n"
    "v_mfma_f32_4x4x4_16b_bf16 v[224:227], v[228:229], v[182:183], v[224:227] cbsz:4 abid:3\n"
    "s_waitcnt vmcnt(4)\n"
    "s_memrealtime s[34:35]\n"
    "ds_read_b64_tr_b16 v[176:177], v240 offset:2048\n"
    "ds_read_b64_tr_b16 v[178:179], v241 offset:2304\n"
    "ds_read_b64_tr_b16 v[180:181], v242 offset:2560\n"
    "ds_read_b64_tr_b16 v[182:183], v243 offset:2816\n"
    "ds_read_b32 v236, v253 offset:0\n"
    "ds_read_b32 v238, v253 offset:4\n"
    "ds_read_b128 v[160:163], v234 offset:4096\n"
    "ds_read_b128 v[164:167], v235 offset:4096\n"
    "v_add_u32_e64 v184, v248, v252\n"
    "ds_read_b64 v[184:185], v184\n"
    "v_pk_fma_f32 v[128:129], v[128:129], s[70:71], v[96:97]\n"
    "v_pk_fma_f32 v[130:131], v[130:131], s[70:71], v[98:99]\n"
    "v_pk_add_f32 v[230:231], v[128:129], v[130:131]\n"
    "v_pk_mul_f32 v[232:233], v[128:129], v[130:131]\n"
    "v_pk_fma_f32 v[132:133], v[132:133], s[70:71], v[100:101]\n"
    "v_pk_fma_f32 v[230:231], v[230:231], v[132:133], v[232:233]\n"
    "v_pk_mul_f32 v[232:233], v[232:233], v[132:133]\n"
    "v_pk_fma_f32 v[134:135], v[134:135], s[70:71], v[102:103]\n"
    "v_pk_fma_f32 v[230:231], v[230:231], v[134:135], v[232:233]\n"
    "v_pk_mul_f32 v[232:233], v[232:233], v[134:135]\n"
    "v_pk_fma_f32 v[136:137], v[136:137], s[70:71], v[104:105]\n"
    "v_pk_fma_f32 v[230:231], v[230:231], v[136:137], v[232:233]\n"
    "v_pk_mul_f32 v[232:233], v[232:233], v[136:137]\n"
    "v_pk_fma_f32 v[138:139], v[138:139], s[70:71], v[106:107]\n"
    "v_pk_fma_f32 v[230:231], v[230:231], v[138:139], v[232:233]\n"
    "v_pk_mul_f32 v[232:233], v[232:233], v[138:139]\n"
    "v_pk_fma_f32 v[140:141], v[140:141], s[70:71], v[108:109]\n"
    "v_pk_fma_f32 v[230:231], v[230:231], v[140:141], v[232:233]\n"
    "v_pk_mul_f32 v[232:233], v[232:233], v[140:141]\n"
    "v_pk_fma_f32 v[142:143], v[142:143], s[70:71], v[110:111]\n"
    "v_pk_fma_f32 v[230:231], v[230:231], v[142:143], v[232:233]\n"
    "v_pk_mul_f32 v[232:233], v[232:233], v[142:143]\n"
    "v_rcp_f32 v232, v232\n"
    "v_rcp_f32 v233, v233\n"
    "v_fma_f32 v230, v230, v232, v247\n"
    "v_fmac_f32 v230, v231, v233\n"
    "v_lshlrev_b32 v232, 2, v249\n"
    "v_lshrrev_b32 v233, 2, v249\n"
    "v_and_b32 v232, 56, v232\n"
    "v_and_b32 v233, 4, v233\n"
    "v_add3_u32 v232, v232, v233, s81\n"
    "ds_read_b32 v232, v232\n"
    "s_add_u32 s81, s81, 64\n"
    "s_waitcnt lgkmcnt(0)\n"
    "v_lshlrev_b32 v232, 1, v232\n"
    "buffer_load_ushort v232, v232, s[44:47], 0 offen\n"
    "s_waitcnt vmcnt(0)\n"
    "v_lshlrev_b32 v232, 16, v232\n"
    "v_mul_f32 v230, v230, v232\n"
    "v_cvt_pk_bf16_f32 v230, v230, v230\n"
    "s_add_u32 s67, s67, 64\n"
    "s_waitcnt lgkmcnt(0)\n"
    "ds_write_b16 v245, v230\n"
    "ds_read_b64 v[228:229], v246\n"
    "v_and_b32 v184, v254, v184\n"
    "v_and_b32 v185, v255, v185\n"
    "s_cmp_le_u32 s34, s76\n"
    "s_cbranch_scc1 .Ldin_B0s_wait\n"
    ".Ldin_B0s_go:\n"
    "s_sub_u32 s94, s34, 60\n"
    "s_max_u32 s76, s76, s94\n"
    "s_add_u32 s76, s76, s72\n"
    ".Ldin_B0s_dma:\n"
    "buffer_load_dwordx4 v[236:237], s[40:43], 0 idxen offen offset:0 nt lds\n"
    "buffer_load_dwordx4 v[238:239], s[40:43], 0 idxen offen offset:1024 nt lds\n"
    ".Ldin_B0s_nd:\n"
    "s_branch .Ldin_G1s\n"
    ".Ldin_L2s_poll:\n"
    "s_sleep 1\n"
    "ds_read_b64 v[232:233], v253\n"
    "s_waitcnt lgkmcnt(0)\n"
    "v_readfirstlane_b32 s60, v232\n"
    "s_cmp_eq_u32 s60, -1\n"
    "s_cbranch_scc1 .Ldin_L2s_poll\n"
    "s_branch .Ldin_L2s_pr\n"
    ".Ldin_B0s_wait:\n"
    "s_cmp_lt_i32 s59, s57\n"
    "s_cbranch_scc1 .Ldin_B0s_w2\n"
    "s_sub_u32 s94, s67, s65\n"
    "s_cmp_gt_u32 s94, 256\n"
    "s_cbranch_scc1 .Ldin_B0s_dma\n"
    "s_cmp_eq_u32 s94, 64\n"
    "s_waitcnt vmcnt(2)\n"
    "s_cbranch_scc1 .Ldin_B0s_nd\n"
    "s_waitcnt vmcnt(0)\n"
    "s_branch .Ldin_B0s_nd\n"
    ".Ldin_B0s_slp:\n"
    "s_sleep 1\n"
    ".Ldin_B0s_w2:\n"
    "s_memrealtime s[34:35]\n"
    "s_waitcnt lgkmcnt(0)\n"
    "s_sub_u32 s94, s34, s76\n"
    "s_cmp_lt_i32 s94, 0\n"
    "s_cbranch_scc1 .Ldin_B0s_slp\n"
    "s_branch .Ldin_B0s_go\n"
    ".p2align 5\n"
    ".Ldin_L1s:\n"
    "s_setprio 0\n"
    "s_cmp_ge_i32 s59, s57\n"
    "s_cbranch_scc1 .Ldin_E1s\n"
    "s_add_u32 s76, s76, s73\n"
    "v_mov_b32_e64 v253, s63\n"
    "ds_read_b64 v[232:233], v253\n"
    "v_mfma_f32_4x4x4_16b_bf16 v[128:131], v[184:185], v[32:33], v[64:67] cbsz:2 abid:0\n"
    "ds_read_b128 v[112:115], v252 offset:17408\n"
    "v_mfma_f32_4x4x4_16b_bf16 v[132:135], v[184:185], v[34:35], v[68:71] cbsz:2 abid:1\n"
    "ds_read_b128 v[116:119], v252 offset:17424\n"
    "v_mfma_f32_4x4x4_16b_bf16 v[136:139], v[184:185], v[36:37], v[72:75] cbsz:2 abid:2\n"
    "ds_read_b128 v[120:123], v252 offset:17536\n"
    "ds_read_b128 v[124:127], v252 offset:17552\n"
    "v_cvt_pk_bf16_f32 v0, v128, v129\n"
    "v_cvt_pk_bf16_f32 v1, v130, v131\n"
    "v_mfma_f32_4x4x4_16b_bf16 v[140:143], v[184:185], v[38:39], v[76:79] cbsz:2 abid:3\n"
    "v_cvt_pk_bf16_f32 v2, v132, v133\n"
    "v_cvt_pk_bf16_f32 v3, v134, v135\n"
    "v_mfma_f32_4x4x4_16b_bf16 v[128:131], v[184:185], v[40:41], v[80:83] cbsz:2 abid:0\n"
    "v_cvt_pk_bf16_f32 v4, v136, v137\n"
    "v_cvt_pk_bf16_f32 v5, v138, v139\n"
    "v_mfma_f32_4x4x4_16b_bf16 v[132:135], v[184:185], v[42:43], v[84:87] cbsz:2 abid:1\n"
    "v_cvt_pk_bf16_f32 v6, v140, v141\n"
    "v_cvt_pk_bf16_f32 v7, v142, v143\n"
    "v_mfma_f32_4x4x4_16b_bf16 v[136:139], v[184:185], v[44:45], v[88:91] cbsz:2 abid:2\n"
    "v_cvt_pk_bf16_f32 v8, v128, v129\n"
    "v_cvt_pk_bf16_f32 v9, v130, v131\n"
    "v_mfma_f32_4x4x4_16b_bf16 v[140:143], v[184:185], v[46:47], v[92:95] cbsz:2 abid:3\n"
    "v_cvt_pk_bf16_f32 v10, v132, v133\n"
    "v_cvt_pk_bf16_f32 v11, v134, v135\n"
    "v_mfma_f32_4x4x4_16b_bf16 v[128:131], v[184:185], v[48:49], v[192:195] cbsz:2 abid:0\n"
    "v_cvt_pk_bf16_f32 v12, v136, v137\n"
    "v_cvt_pk_bf16_f32 v13, v138, v139\n"
    "v_mfma_f32_4x4x4_16b_bf16 v[132:135], v[184:185], v[50:51], v[196:199] cbsz:2 abid:1\n"
    "v_cvt_pk_bf16_f32 v14, v140, v141\n"
    "v_cvt_pk_bf16_f32 v15, v142, v143\n"
    "v_mfma_f32_4x4x4_16b_bf16 v[136:139], v[184:185], v[52:53], v[200:203] cbsz:2 abid:2\n"
    "s_waitcnt lgkmcnt(0)\n"
    "v_readfirstlane_b32 s60, v232\n"
    "v_cvt_pk_bf16_f32 v16, v128, v129\n"
    "v_cvt_pk_bf16_f32 v17, v130, v131\n"
    "v_mfma_f32_4x4x4_16b_bf16 v[140:143], v[184:185], v[54:55], v[204:207] cbsz:2 abid:3\n"
    "s_cmp_eq_u32 s60, -1\n"
    "s_cbranch_scc1 .Ldin_L1s_poll\n"
    ".Ldin_L1s_pr:\n"
    "v_readfirstlane_b32 s95, v233\n"
    "s_and_b32 s94, s60, 63\n"
    "s_lshl_b32 s94, s94, 6\n"
    "s_add_u32 s94, s94, 68480\n"
    "s_add_u32 s63, s63, 8\n"
    "v_lshl_add_u32 v252, v249, 1, s94\n"
    "ds_read_b32 v252, v252\n"
    "s_sub_u32 s95, s95, s76\n"
    "s_ashr_i32 s95, s95, 4\n"
    "s_min_i32 s95, s95, s74\n"
    "s_max_i32 s72, s95, s75\n"
    "v_cvt_pk_bf16_f32 v18, v132, v133\n"
    "v_cvt_pk_bf16_f32 v19, v134, v135\n"
    "v_mfma_f32_4x4x4_16b_bf16 v[128:131], v[184:185], v[56:57], v[208:211] cbsz:2 abid:0\n"
    "v_cvt_pk_bf16_f32 v20, v136, v137\n"
    "v_cvt_pk_bf16_f32 v21, v138, v139\n"
    "v_mfma_f32_4x4x4_16b_bf16 v[132:135], v[184:185], v[58:59], v[212:215] cbsz:2 abid:1\n"
    "v_cvt_pk_bf16_f32 v22, v140, v141\n"
    "v_cvt_pk_bf16_f32 v23, v142, v143\n"
    "v_mfma_f32_4x4x4_16b_bf16 v[136:139], v[184:185], v[60:61], v[216:219] cbsz:2 abid:2\n"
    "v_cvt_pk_bf16_f32 v24, v128, v129\n"
    "v_cvt_pk_bf16_f32 v25, v130, v131\n"
    "v_mfma_f32_4x4x4_16b_bf16 v[140:143], v[184:185], v[62:63], v[220:223] cbsz:2 abid:3\n"
    "v_cvt_pk_bf16_f32 v26, v132, v133\n"
    "v_cvt_pk_bf16_f32 v27, v134, v135\n"
    "v_cvt_pk_bf16_f32 v28, v136, v137\n"
    "v_cvt_pk_bf16_f32 v29, v138, v139\n"
    "v_exp_f32 v152, v152 clamp\n"
    "v_cvt_pk_bf16_f32 v30, v140, v141\n"
    "v_cvt_pk_bf16_f32 v31, v142, v143\n"
    "v_mfma_f32_16x16x32_bf16 v[128:131], v[0:3], v[160:163], v[112:115]\n"
    "v_exp_f32 v144, v144 clamp\n"
    "v_mfma_f32_16x16x32_bf16 v[128:131], v[4:7], v[164:167], v[128:131]\n"
    "v_exp_f32 v145, v145 clamp\n"
    "v_mfma_f32_16x16x32_bf16 v[132:135], v[8:11], v[160:163], v[116:119]\n"
    "v_exp_f32 v146, v146 clamp\n"
    "v_mfma_f32_16x16x32_bf16 v[132:135], v[12:15], v[164:167], v[132:135]\n"
    "v_exp_f32 v147, v147 clamp\n"
    "v_mfma_f32_16x16x32_bf16 v[136:139], v[16:19], v[160:163], v[120:123]\n"
    "v_exp_f32 v148, v148 clamp\n"
    "v_mfma_f32_16x16x32_bf16 v[136:139], v[20:23], v[164:167], v[136:139]\n"
    "v_exp_f32 v149, v149 clamp\n"
    "v_mfma_f32_16x16x32_bf16 v[140:143], v[24:27], v[160:163], v[124:127]\n"
    "v_exp_f32 v150, v150 clamp\n"
    "v_mfma_f32_16x16x32_bf16 v[140:143], v[28:31], v[164:167], v[140:143]\n"
    "v_exp_f32 v151, v151 clamp\n"
    "s_add_u32 m0, s68, 4096\n"
    "v_exp_f32 v153, v153 clamp\n"
    "v_add_u32 v253, s67, v244\n"
    "v_mfma_f32_4x4x4_16b_bf16 v[224:227], v[228:229], v[168:169], v[224:227] cbsz:4 abid:0\n"
    "v_exp_f32 v154, v154 clamp\n"
    "v_exp_f32 v155, v155 clamp\n"
    "v_mfma_f32_4x4x4_16b_bf16 v[224:227], v[228:229], v[170:171], v[224:227] cbsz:4 abid:1\n"
    "v_exp_f32 v156, v156 clamp\n"
    "v_exp_f32 v157, v157 clamp\n"
    "v_mfma_f32_4x4x4_16b_bf16 v[224:227], v[228:229], v[172:173], v[224:227] cbsz:4 abid:2\n"
    "v_exp_f32 v158, v158 clamp\n"
    "v_exp_f32 v159, v159 clamp\n"
    "v_mfma_f32_4x4x4_16b_bf16 v[224:227], v[228:229], v[174:175], v[224:227] cbsz:4 abid:3\n"
    "s_waitcnt vmcnt(4)\n"
    "ds_read_b64_tr_b16 v[168:169], v240 offset:4096\n"
    "ds_read_b64_tr_b16 v[170:171], v241 offset:4352\n"
    "ds_read_b64_tr_b16 v[172:173], v242 offset:4608\n"
    "ds_read_b64_tr_b16 v[174:175], v243 offset:4864\n"
    "ds_read_b32 v236, v253 offset:0\n"
    "ds_read_b32 v238, v253 offset:4\n"
    "ds_read_b128 v[160:163], v234 offset:6144\n"
    "ds_read_b128 v[164:167], v235 offset:6144\n"
    "s_waitcnt lgkmcnt(8)\n"
    "s_nop 0\n"
    "v_readlane_b32 s20, v252, 0\n"
    "v_readlane_b32 s21, v252, 1\n"
    "v_readlane_b32 s22, v252, 2\n"
    "v_readlane_b32 s23, v252, 3\n"
    "v_readlane_b32 s24, v252, 4\n"
    "v_readlane_b32 s25, v252, 5\n"
    "v_readlane_b32 s26, v252, 6\n"
    "v_readlane_b32 s27, v252, 7\n"
    "v_pk_fma_f32 v[144:145], v[144:145], s[70:71], v[96:97]\n"
    "v_pk_fma_f32 v[146:147], v[146:147], s[70:71], v[98:99]\n"
    "v_pk_add_f32 v[230:231], v[144:145], v[146:147]\n"
    "v_pk_mul_f32 v[232:233], v[144:145], v[146:147]\n"
    "v_pk_fma_f32 v[148:149], v[148:149], s[70:71], v[100:101]\n"
    "v_pk_fma_f32 v[230:231], v[230:231], v[148:149], v[232:233]\n"
    "v_pk_mul_f32 v[232:233], v[232:233], v[148:149]\n"
    "v_pk_fma_f32 v[150:151], v[150:151], s[70:71], v[102:103]\n"
    "v_pk_fma_f32 v[230:231], v[230:231], v[150:151], v[232:233]\n"
    "v_pk_mul_f32 v[232:233], v[232:233], v[150:151]\n"
    "v_pk_fma_f32 v[152:153], v[152:153], s[70:71], v[104:105]\n"
    "v_pk_fma_f32 v[230:231], v[230:231], v[152:153], v[232:233]\n"
    "v_pk_mul_f32 v[232:233], v[232:233], v[152:153]\n"
    "v_pk_fma_f32 v[154:155], v[154:155], s[70:71], v[106:107]\n"
    "v_pk_fma_f32 v[230:231], v[230:231], v[154:155], v[232:233]\n"
    "v_pk_mul_f32 v[232:233], v[232:233], v[154:155]\n"
    "v_pk_fma_f32 v[156:157], v[156:157], s[70:71], v[108:109]\n"
    "v_pk_fma_f32 v[230:231], v[230:231], v[156:157], v[232:233]\n"
    "v_pk_mul_f32 v[232:233], v[232:233], v[156:157]\n"
    "v_pk_fma_f32 v[158:159], v[158:159], s[70:71], v[110:111]\n"
    "v_pk_fma_f32 v[230:231], v[230:231], v[158:159], v[232:233]\n"
    "v_pk_mul_f32 v[232:233], v[232:233], v[158:159]\n"
    "v_rcp_f32 v232, v232\n"
    "v_rcp_f32 v233, v233\n"
    "v_fma_f32 v230, v230, v232, v247\n"
    "v_fmac_f32 v230, v231, v233\n"
    "v_lshlrev_b32 v232, 2, v249\n"
    "v_lshrrev_b32 v233, 2, v249\n"
    "v_and_b32 v232, 56, v232\n"
    "v_and_b32 v233, 4, v233\n"
    "v_add3_u32 v232, v232, v233, s81\n"
    "ds_read_b32 v232, v232\n"
    "s_add_u32 s81, s81, 64\n"
    "s_waitcnt lgkmcnt(0)\n"
    "v_lshlrev_b32 v232, 1, v232\n"
    "buffer_load_ushort v232, v232, s[44:47], 0 offen\n"
    "s_waitcnt vmcnt(0)\n"
    "v_lshlrev_b32 v232, 16, v232\n"
    "v_mul_f32 v230, v230, v232\n"
    "v_cvt_pk_bf16_f32 v230, v230, v230\n"
    "s_add_u32 s67, s67, 64\n"
    "s_waitcnt lgkmcnt(0)\n"
    "ds_write_b16 v245, v230\n"
    "ds_read_b64 v[228:229], v246\n"
    "s_lshl1_add_u32 s76, s72, s76\n"
    "buffer_load_dwordx4 v[236:237], s[40:43], 0 idxen offen offset:0 nt lds\n"
    "buffer_load_dwordx4 v[238:239], s[40:43], 0 idxen offen offset:1024 nt lds\n"
    "s_and_b32 s62, s58, 63\n"
    "s_mul_i32 s62, s62, 272\n"
    "s_mov_b32 s58, s59\n"
    "s_mov_b32 s59, s60\n"
    "s_mov_b32 s81, s65\n"
    "s_xor_b32 s65, s65, s64\n"
    "s_mov_b32 s80, s83\n"
    "s_add_u32 s82, s65, 256\n"
    "s_cmp_lt_i32 s59, s57\n"
    "s_cselect_b32 s76, s76, -1\n"
    "s_bitcmp1_b32 s58, 30\n"
    "s_cbranch_scc0 .Ldin_A2\n"
    ".Ldin_A2s:\n"
    "v_mfma_f32_16x16x32_bf16 v[144:147], v[0:3], v[160:163], v[112:115]\n"
    "v_exp_f32 v128, v128 clamp\n"
    "v_mfma_f32_16x16x32_bf16 v[144:147], v[4:7], v[164:167], v[144:147]\n"
    "v_exp_f32 v129, v129 clamp\n"
    "v_mfma_f32_16x16x32_bf16 v[148:151], v[8:11], v[160:163], v[116:119]\n"
    "v_exp_f32 v130, v130 clamp\n"
    "v_mfma_f32_16x16x32_bf16 v[148:151], v[12:15], v[164:167], v[148:151]\n"
    "v_exp_f32 v131, v131 clamp\n"
    "v_mfma_f32_16x16x32_bf16 v[152:155], v[16:19], v[160:163], v[120:123]\n"
    "v_exp_f32 v132, v132 clamp\n"
    "v_mfma_f32_16x16x32_bf16 v[152:155], v[20:23], v[164:167], v[152:155]\n"
    "v_exp_f32 v133, v133 clamp\n"
    "v_mfma_f32_16x16x32_bf16 v[156:159], v[24:27], v[160:163], v[124:127]\n"
    "v_exp_f32 v134, v134 clamp\n"
    "v_mfma_f32_16x16x32_bf16 v[156:159], v[28:31], v[164:167], v[156:159]\n"
    "v_exp_f32 v135, v135 clamp\n"
    "v_exp_f32 v136, v136 clamp\n"
    "s_mov_b32 m0, s62\n"
    "v_exp_f32 v137, v137 clamp\n"
    "v_add_u32 v253, s67, v244\n"
    "s_waitcnt lgkmcnt(0)\n"
    "v_mfma_f32_4x4x4_16b_bf16 v[224:227], v[228:229], v[176:177], v[224:227] cbsz:4 abid:0\n"
    "v_exp_f32 v138, v138 clamp\n"
    "v_exp_f32 v139, v139 clamp\n"
    "v_mfma_f32_4x4x4_16b_bf16 v[224:227], v[228:229], v[178:179], v[224:227] cbsz:4 abid:1\n"
    "v_exp_f32 v140, v140 clamp\n"
    "v_exp_f32 v141, v141 clamp\n"
    "v_mfma_f32_4x4x4_16b_bf16 v[224:227], v[228:229], v[180:181], v[224:227] cbsz:4 abid:2\n"
    "v_exp_f32 v142, v142 clamp\n"
    "v_exp_f32 v143, v143 clamp\n"
    "v_mfma_f32_4x4x4_16b_bf16 v[224:227], v[228:229], v[182:183], v[224:227] cbsz:4 abid:3\n"
    "s_waitcnt vmcnt(4)\n"
    "ds_read_b64_tr_b16 v[176:177], v240 offset:6144\n"
    "ds_read_b64_tr_b16 v[178:179], v241 offset:6400\n"
    "ds_read_b64_tr_b16 v[180:181], v242 offset:6656\n"
    "ds_read_b64_tr_b16 v[182:183], v243 offset:6912\n"
    "ds_read_b32 v236, v253 offset:0\n"
    "ds_read_b32 v238, v253 offset:4\n"
    "ds_read_b128 v[160:163], v234 offset:0\n"
    "ds_read_b128 v[164:167], v235 offset:0\n"
    "s_cbranch_vccz .Ldin_A2s_np\n"
    "s_setprio 1\n"
    ".Ldin_A2s_np:\n"
    "v_pk_fma_f32 v[128:129], v[128:129], s[70:71], v[96:97]\n"
    "v_pk_fma_f32 v[130:131], v[130:131], s[70:71], v[98:99]\n"
    "v_pk_add_f32 v[190:191], v[224:225], v[226:227]\n"
    "v_add_f32 v190, v190, v191\n"
    "ds_write_addtid_b32 v190 offset:32768\n"
    "s_add_u32 m0, s68, 6144\n"
    "s_cmp_ge_i32 s59, s57\n"
    "s_cbranch_scc1 .Ldin_A2s_nl\n"
    "s_bfe_u32 s94, s59, 0x180006\n"
    "s_mul_i32 s86, s94, s56\n"
    "v_mbcnt_lo_u32_b32 v184, s20, 0\n"
    "v_mbcnt_lo_u32_b32 v185, s22, 0\n"
    "v_mbcnt_lo_u32_b32 v186, s24, 0\n"
    "v_mbcnt_lo_u32_b32 v187, s26, 0\n"
    "s_bcnt1_i32_b64 s90, s[20:21]\n"
    "s_bcnt1_i32_b64 s91, s[22:23]\n"
    "s_bcnt1_i32_b64 s92, s[24:25]\n"
    "s_bcnt1_i32_b64 s93, s[26:27]\n"
    "v_mbcnt_hi_u32_b32 v184, s21, v184\n"
    "v_mbcnt_hi_u32_b32 v185, s23, v185\n"
    "v_mbcnt_hi_u32_b32 v186, s25, v186\n"
    "v_mbcnt_hi_u32_b32 v187, s27, v187\n"
    "s_lshl2_add_u32 s90, s90, s65\n"
    "s_lshl2_add_u32 s91, s91, s90\n"
    "s_lshl2_add_u32 s92, s92, s91\n"
    "s_lshl2_add_u32 s93, s93, s92\n"
    "v_add_u32 v188, s86, v251\n"
    "v_lshl_add_u32 v184, v184, 2, s65\n"
    "v_lshl_add_u32 v185, v185, 2, s90\n"
    "v_lshl_add_u32 v186, v186, 2, s91\n"
    "v_lshl_add_u32 v187, v187, 2, s92\n"
    "v_mov_b32 v191, -1\n"
    "v_lshl_add_u32 v190, v249, 1, s93\n"
    "ds_write2_b32 v190, v191, v191 offset1:64\n"
    "s_mov_b64 exec, s[20:21]\n"
    "ds_write_b32 v184, v188\n"
    "s_mov_b64 exec, s[22:23]\n"
    "v_add_u32 v189, 64, v188\n"
    "ds_write_b32 v185, v189\n"
    "s_mov_b64 exec, s[24:25]\n"
    "v_add_u32 v190, 128, v188\n"
    "ds_write_b32 v186, v190\n"
    "s_mov_b64 exec, s[26:27]\n"
    "v_add_u32 v189, 192, v188\n"
    "ds_write_b32 v187, v189\n"
    "s_mov_b64 exec, -1\n"
    "s_sub_u32 s95, s93, -60\n"
    "s_add_u32 s94, s82, 128\n"
    "s_andn2_b32 s95, s95, 63\n"
    "s_max_u32 s83, s95, s94\n"
    ".Ldin_A2s_nl:\n"
    "v_pk_add_f32 v[230:231], v[128:129], v[130:131]\n"
    "v_pk_mul_f32 v[232:233], v[128:129], v[130:131]\n"
    "v_pk_fma_f32 v[132:133], v[132:133], s[70:71], v[100:101]\n"
    "v_pk_fma_f32 v[230:231], v[230:231], v[132:133], v[232:233]\n"
    "v_pk_mul_f32 v[232:233], v[232:233], v[132:133]\n"
    "v_pk_fma_f32 v[134:135], v[134:135], s[70:71], v[102:103]\n"
    "v_pk_fma_f32 v[230:231], v[230:231], v[134:135], v[232:233]\n"
    "v_pk_mul_f32 v[232:233], v[232:233], v[134:135]\n"
    "v_pk_fma_f32 v[136:137], v[136:137], s[70:71], v[104:105]\n"
    "v_pk_fma_f32 v[230:231], v[230:231], v[136:137], v[232:233]\n"
    "v_pk_mul_f32 v[232:233], v[232:233], v[136:137]\n"
    "v_pk_fma_f32 v[138:139], v[138:139], s[70:71], v[106:107]\n"
    "v_pk_fma_f32 v[230:231], v[230:231], v[138:139], v[232:233]\n"
    "v_pk_mul_f32 v[232:233], v[232:233], v[138:139]\n"
    "v_pk_fma_f32 v[140:141], v[140:141], s[70:71], v[108:109]\n"
    "v_pk_fma_f32 v[230:231], v[230:231], v[140:141], v[232:233]\n"
    "v_pk_mul_f32 v[232:233], v[232:233], v[140:141]\n"
    "v_pk_fma_f32 v[142:143], v[142:143], s[70:71], v[110:111]\n"
    "v_pk_fma_f32 v[230:231], v[230:231], v[142:143], v[232:233]\n"
    "v_pk_mul_f32 v[232:233], v[232:233], v[142:143]\n"
    "v_rcp_f32 v232, v232\n"
    "v_rcp_f32 v233, v233\n"
    "v_fma_f32 v230, v230, v232, v247\n"
    "v_fmac_f32 v230, v231, v233\n"
    "v_lshlrev_b32 v232, 2, v249\n"
    "v_lshrrev_b32 v233, 2, v249\n"
    "v_and_b32 v232, 56, v232\n"
    "v_and_b32 v233, 4, v233\n"
    "v_add3_u32 v232, v232, v233, s81\n"
    "ds_read_b32 v232, v232\n"
    "s_add_u32 s81, s81, 64\n"
    "s_waitcnt lgkmcnt(0)\n"
    "v_lshlrev_b32 v232, 1, v232\n"
    "buffer_load_ushort v232, v232, s[44:47], 0 offen\n"
    "s_waitcnt vmcnt(0)\n"
    "v_lshlrev_b32 v232, 16, v232\n"
    "v_mul_f32 v230, v230, v232\n"
    "v_cvt_pk_bf16_f32 v230, v230, v230\n"
    "s_add_u32 s67, s67, 64\n"
    "s_waitcnt lgkmcnt(0)\n"
    "ds_write_b16 v245, v230\n"
    "ds_read_b64 v[228:229], v246\n"
    "buffer_load_dwordx4 v[236:237], s[40:43], 0 idxen offen offset:0 nt lds\n"
    "buffer_load_dwordx4 v[238:239], s[40:43], 0 idxen offen offset:1024 nt lds\n"
    ".Ldin_B3s:\n"
    "v_mfma_f32_16x16x32_bf16 v[128:131], v[0:3], v[160:163], v[112:115]\n"
    "v_exp_f32 v144, v144 clamp\n"
    "v_mfma_f32_16x16x32_bf16 v[128:131], v[4:7], v[164:167], v[128:131]\n"
    "v_exp_f32 v145, v145 clamp\n"
    "v_mfma_f32_16x16x32_bf16 v[132:135], v[8:11], v[160:163], v[116:119]\n"
    "v_exp_f32 v146, v146 clamp\n"
    "v_mfma_f32_16x16x32_bf16 v[132:135], v[12:15], v[164:167], v[132:135]\n"
    "v_exp_f32 v147, v147 clamp\n"
    "v_mfma_f32_16x16x32_bf16 v[136:139], v[16:19], v[160:163], v[120:123]\n"
    "v_exp_f32 v148, v148 clamp\n"
    "v_mfma_f32_16x16x32_bf16 v[136:139], v[20:23], v[164:167], v[136:139]\n"
    "v_exp_f32 v149, v149 clamp\n"
    "v_mfma_f32_16x16x32_bf16 v[140:143], v[24:27], v[160:163], v[124:127]\n"
    "v_exp_f32 v150, v150 clamp\n"
    "v_mfma_f32_16x16x32_bf16 v[140:143], v[28:31], v[164:167], v[140:143]\n"
    "v_exp_f32 v151, v151 clamp\n"
    "v_exp_f32 v152, v152 clamp\n"
    "s_cmp_eq_u32 s67, s80\n"
    "s_cselect_b32 s67, s65, s67\n"
    "s_and_b32 s94, s59, 63\n"
    "s_mul_i32 s94, s94, 272\n"
    "v_add_u32 v252, s94, v250\n"
    "s_add_u32 m0, s68, 0\n"
    "v_exp_f32 v153, v153 clamp\n"
    "v_add_u32_e64 v253, s67, v244\n"
    "s_waitcnt lgkmcnt(0)\n"
    "v_mfma_f32_4x4x4_16b_bf16 v[224:227], v[228:229], v[168:169], 0 cbsz:4 abid:0\n"
    "v_exp_f32 v154, v154 clamp\n"
    "v_exp_f32 v155, v155 clamp\n"
    "v_mfma_f32_4x4x4_16b_bf16 v[224:227], v[228:229], v[170:171], v[224:227] cbsz:4 abid:1\n"
    "v_exp_f32 v156, v156 clamp\n"
    "v_exp_f32 v157, v157 clamp\n"
    "v_mfma_f32_4x4x4_16b_bf16 v[224:227], v[228:229], v[172:173], v[224:227] cbsz:4 abid:2\n"
    "v_exp_f32 v158, v158 clamp\n"
    "v_exp_f32 v159, v159 clamp\n"
    "v_mfma_f32_4x4x4_16b_bf16 v[224:227], v[228:229], v[174:175], v[224:227] cbsz:4 abid:3\n"
    "s_waitcnt vmcnt(4)\n"
    "s_memrealtime s[34:35]\n"
    "ds_read_b64_tr_b16 v[168:169], v240 offset:0\n"
    "ds_read_b64_tr_b16 v[170:171], v241 offset:256\n"
    "ds_read_b64_tr_b16 v[172:173], v242 offset:512\n"
    "ds_read_b64_tr_b16 v[174:175], v243 offset:768\n"
    "ds_read_b32 v236, v253 offset:0\n"
    "ds_read_b32 v238, v253 offset:4\n"
    "ds_read_b128 v[160:163], v234 offset:2048\n"
    "ds_read_b128 v[164:167], v235 offset:2048\n"
    "v_add_u32_e64 v184, v248, v252\n"
    "ds_read_b64 v[184:185], v184\n"
    "v_pk_fma_f32 v[144:145], v[144:145], s[70:71], v[96:97]\n"
    "v_pk_fma_f32 v[146:147], v[146:147], s[70:71], v[98:99]\n"
    "v_pk_add_f32 v[230:231], v[144:145], v[146:147]\n"
    "v_pk_mul_f32 v[232:233], v[144:145], v[146:147]\n"
    "v_pk_fma_f32 v[148:149], v[148:149], s[70:71], v[100:101]\n"
    "v_pk_fma_f32 v[230:231], v[230:231], v[148:149], v[232:233]\n"
    "v_pk_mul_f32 v[232:233], v[232:233], v[148:149]\n"
    "v_pk_fma_f32 v[150:151], v[150:151], s[70:71], v[102:103]\n"
    "v_pk_fma_f32 v[230:231], v[230:231], v[150:151], v[232:233]\n"
    "v_pk_mul_f32 v[232:233], v[232:233], v[150:151]\n"
    "v_pk_fma_f32 v[152:153], v[152:153], s[70:71], v[104:105]\n"
    "v_pk_fma_f32 v[230:231], v[230:231], v[152:153], v[232:233]\n"
    "v_pk_mul_f32 v[232:233], v[232:233], v[152:153]\n"
    "v_pk_fma_f32 v[154:155], v[154:155], s[70:71], v[106:107]\n"
    "v_pk_fma_f32 v[230:231], v[230:231], v[154:155], v[232:233]\n"
    "v_pk_mul_f32 v[232:233], v[232:233], v[154:155]\n"
    "v_pk_fma_f32 v[156:157], v[156:157], s[70:71], v[108:109]\n"
    "v_pk_fma_f32 v[230:231], v[230:231], v[156:157], v[232:233]\n"
    "v_pk_mul_f32 v[232:233], v[232:233], v[156:157]\n"
    "v_pk_fma_f32 v[158:159], v[158:159], s[70:71], v[110:111]\n"
    "v_pk_fma_f32 v[230:231], v[230:231], v[158:159], v[232:233]\n"
    "v_pk_mul_f32 v[232:233], v[232:233], v[158:159]\n"
    "v_rcp_f32 v232, v232\n"
    "v_rcp_f32 v233, v233\n"
    "v_fma_f32 v230, v230, v232, v247\n"
    "v_fmac_f32 v230, v231, v233\n"
    "v_lshlrev_b32 v232, 2, v249\n"
    "v_lshrrev_b32 v233, 2, v249\n"
    "v_and_b32 v232, 56, v232\n"
    "v_and_b32 v233, 4, v233\n"
    "v_add3_u32 v232, v232, v233, s81\n"
    "ds_read_b32 v232, v232\n"
    "s_add_u32 s81, s81, 64\n"
    "s_waitcnt lgkmcnt(0)\n"
    "v_lshlrev_b32 v232, 1, v232\n"
    "buffer_load_ushort v232, v232, s[44:47], 0 offen\n"
    "s_waitcnt vmcnt(0)\n"
    "v_lshlrev_b32 v232, 16, v232\n"
    "v_mul_f32 v230, v230, v232\n"
    "v_cvt_pk_bf16_f32 v230, v230, v230\n"
    "s_add_u32 s67, s67, 64\n"
    "s_waitcnt lgkmcnt(0)\n"
    "ds_write_b16 v245, v230\n"
    "ds_read_b64 v[228:229], v246\n"
    "v_and_b32 v184, v254, v184\n"
    "v_and_b32 v185, v255, v185\n"
    "s_cmp_le_u32 s34, s76\n"
    "s_cbranch_scc1 .Ldin_B3s_wait\n"
    ".Ldin_B3s_go:\n"
    "s_sub_u32 s94, s34, 60\n"
    "s_max_u32 s76, s76, s94\n"
    "s_add_u32 s76, s76, s72\n"
    ".Ldin_B3s_dma:\n"
    "buffer_load_dwordx4 v[236:237], s[40:43], 0 idxen offen offset:0 nt lds\n"
    "buffer_load_dwordx4 v[238:239], s[40:43], 0 idxen offen offset:1024 nt lds\n"
    ".Ldin_B3s_nd:\n"
    "s_branch .Ldin_G0s\n"
    ".Ldin_L1s_poll:\n"
    "s_sleep 1\n"
    "ds_read_b64 v[232:233], v253\n"
    "s_waitcnt lgkmcnt(0)\n"
    "v_readfirstlane_b32 s60, v232\n"
    "s_cmp_eq_u32 s60, -1\n"
    "s_cbranch_scc1 .Ldin_L1s_poll\n"
    "s_branch .Ldin_L1s_pr\n"
    ".Ldin_B3s_wait:\n"
    "s_cmp_lt_i32 s59, s57\n"
    "s_cbranch_scc1 .Ldin_B3s_w2\n"
    "s_sub_u32 s94, s67, s65\n"
    "s_cmp_gt_u32 s94, 256\n"
    "s_cbranch_scc1 .Ldin_B3s_dma\n"
    "s_cmp_eq_u32 s94, 64\n"
    "s_waitcnt vmcnt(2)\n"
    "s_cbranch_scc1 .Ldin_B3s_nd\n"
    "s_waitcnt vmcnt(0)\n"
    "s_branch .Ldin_B3s_nd\n"
    ".Ldin_B3s_slp:\n"
    "s_sleep 1\n"
    ".Ldin_B3s_w2:\n"
    "s_memrealtime s[34:35]\n"
    "s_waitcnt lgkmcnt(0)\n"
    "s_sub_u32 s94, s34, s76\n"
    "s_cmp_lt_i32 s94, 0\n"
    "s_cbranch_scc1 .Ldin_B3s_slp\n"
    "s_branch .Ldin_B3s_go\n"
    ".p2align 5\n"
    ".Ldin_L0s:\n"
    "s_setprio 0\n"
    "s_cmp_ge_i32 s59, s57\n"
    "s_cbranch_scc1 .Ldin_E0s\n"
    "s_add_u32 s76, s76, s73\n"
    "v_mov_b32_e64 v253, s63\n"
    "ds_read_b64 v[232:233], v253\n"
    "v_mfma_f32_4x4x4_16b_bf16 v[144:147], v[184:185], v[32:33], v[64:67] cbsz:2 abid:0\n"
    "ds_read_b128 v[112:115], v252 offset:17408\n"
    "v_mfma_f32_4x4x4_16b_bf16 v[148:151], v[184:185], v[34:35], v[68:71] cbsz:2 abid:1\n"
    "ds_read_b128 v[116:119], v252 offset:17424\n"
    "v_mfma_f32_4x4x4_16b_bf16 v[152:155], v[184:185], v[36:37], v[72:75] cbsz:2 abid:2\n"
    "ds_read_b128 v[120:123], v252 offset:17536\n"
    "ds_read_b128 v[124:127], v252 offset:17552\n"
    "v_cvt_pk_bf16_f32 v0, v144, v145\n"
    "v_cvt_pk_bf16_f32 v1, v146, v147\n"
    "v_mfma_f32_4x4x4_16b_bf16 v[156:159], v[184:185], v[38:39], v[76:79] cbsz:2 abid:3\n"
    "v_cvt_pk_bf16_f32 v2, v148, v149\n"
    "v_cvt_pk_bf16_f32 v3, v150, v151\n"
    "v_mfma_f32_4x4x4_16b_bf16 v[144:147], v[184:185], v[40:41], v[80:83] cbsz:2 abid:0\n"
    "v_cvt_pk_bf16_f32 v4, v152, v153\n"
    "v_cvt_pk_bf16_f32 v5, v154, v155\n"
    "v_mfma_f32_4x4x4_16b_bf16 v[148:151], v[184:185], v[42:43], v[84:87] cbsz:2 abid:1\n"
    "v_cvt_pk_bf16_f32 v6, v156, v157\n"
    "v_cvt_pk_bf16_f32 v7, v158, v159\n"
    "v_mfma_f32_4x4x4_16b_bf16 v[152:155], v[184:185], v[44:45], v[88:91] cbsz:2 abid:2\n"
    "v_cvt_pk_bf16_f32 v8, v144, v145\n"
    "v_cvt_pk_bf16_f32 v9, v146, v147\n"
    "v_mfma_f32_4x4x4_16b_bf16 v[156:159], v[184:185], v[46:47], v[92:95] cbsz:2 abid:3\n"
    "v_cvt_pk_bf16_f32 v10, v148, v149\n"
    "v_cvt_pk_bf16_f32 v11, v150, v151\n"
    "v_mfma_f32_4x4x4_16b_bf16 v[144:147], v[184:185], v[48:49], v[192:195] cbsz:2 abid:0\n"
    "v_cvt_pk_bf16_f32 v12, v152, v153\n"
    "v_cvt_pk_bf16_f32 v13, v154, v155\n"
    "v_mfma_f32_4x4x4_16b_bf16 v[148:151], v[184:185], v[50:51], v[196:199] cbsz:2 abid:1\n"
    "v_cvt_pk_bf16_f32 v14, v156, v157\n"
    "v_cvt_pk_bf16_f32 v15, v158, v159\n"
    "v_mfma_f32_4x4x4_16b_bf16 v[152:155], v[184:185], v[52:53], v[200:203] cbsz:2 abid:2\n"
    "s_waitcnt lgkmcnt(0)\n"
    "v_readfirstlane_b32 s60, v232\n"
    "v_cvt_pk_bf16_f32 v16, v144, v145\n"
    "v_cvt_pk_bf16_f32 v17, v146, v147\n"
    "v_mfma_f32_4x4x4_16b_bf16 v[156:159], v[184:185], v[54:55], v[204:207] cbsz:2 abid:3\n"
    "s_cmp_eq_u32 s60, -1\n"
    "s_cbranch_scc1 .Ldin_L0s_poll\n"
    ".Ldin_L0s_pr:\n"
    "v_readfirstlane_b32 s95, v233\n"
    "s_and_b32 s94, s60, 63\n"
    "s_lshl_b32 s94, s94, 6\n"
    "s_add_u32 s94, s94, 68480\n"
    "s_add_u32 s63, s63, 8\n"
    "v_lshl_add_u32 v252, v249, 1, s94\n"
    "ds_read_b32 v252, v252\n"
    "s_sub_u32 s95, s95, s76\n"
    "s_ashr_i32 s95, s95, 4\n"
    "s_min_i32 s95, s95, s74\n"
    "s_max_i32 s72, s95, s75\n"
    "v_cvt_pk_bf16_f32 v18, v148, v149\n"
    "v_cvt_pk_bf16_f32 v19, v150, v151\n"
    "v_mfma_f32_4x4x4_16b_bf16 v[144:147], v[184:185], v[56:57], v[208:211] cbsz:2 abid:0\n"
    "v_cvt_pk_bf16_f32 v20, v152, v153\n"
    "v_cvt_pk_bf16_f32 v21, v154, v155\n"
    "v_mfma_f32_4x4x4_16b_bf16 v[148:151], v[184:185], v[58:59], v[212:215] cbsz:2 abid:1\n"
    "v_cvt_pk_bf16_f32 v22, v156, v157\n"
    "v_cvt_pk_bf16_f32 v23, v158, v159\n"
    "v_mfma_f32_4x4x4_16b_bf16 v[152:155], v[184:185], v[60:61], v[216:219] cbsz:2 abid:2\n"
    "v_cvt_pk_bf16_f32 v24, v144, v145\n"
    "v_cvt_pk_bf16_f32 v25, v146, v147\n"
    "v_mfma_f32_4x4x4_16b_bf16 v[156:159], v[184:185], v[62:63], v[220:223] cbsz:2 abid:3\n"
    "v_cvt_pk_bf16_f32 v26, v148, v149\n"
    "v_cvt_pk_bf16_f32 v27, v150, v151\n"
    "v_cvt_pk_bf16_f32 v28, v152, v153\n"
    "v_cvt_pk_bf16_f32 v29, v154, v155\n"
    "v_exp_f32 v136, v136 clamp\n"
    "v_cvt_pk_bf16_f32 v30, v156, v157\n"
    "v_cvt_pk_bf16_f32 v31, v158, v159\n"
    "v_mfma_f32_16x16x32_bf16 v[144:147], v[0:3], v[160:163], v[112:115]\n"
    "v_exp_f32 v128, v128 clamp\n"
    "v_mfma_f32_16x16x32_bf16 v[144:147], v[4:7], v[164:167], v[144:147]\n"
    "v_exp_f32 v129, v129 clamp\n"
    "v_mfma_f32_16x16x32_bf16 v[148:151], v[8:11], v[160:163], v[116:119]\n"
    "v_exp_f32 v130, v130 clamp\n"
    "v_mfma_f32_16x16x32_bf16 v[148:151], v[12:15], v[164:167], v[148:151]\n"
    "v_exp_f32 v131, v131 clamp\n"
    "v_mfma_f32_16x16x32_bf16 v[152:155], v[16:19], v[160:163], v[120:123]\n"
    "v_exp_f32 v132, v132 clamp\n"
    "v_mfma_f32_16x16x32_bf16 v[152:155], v[20:23], v[164:167], v[152:155]\n"
    "v_exp_f32 v133, v133 clamp\n"
    "v_mfma_f32_16x16x32_bf16 v[156:159], v[24:27], v[160:163], v[124:127]\n"
    "v_exp_f32 v134, v134 clamp\n"
    "v_mfma_f32_16x16x32_bf16 v[156:159], v[28:31], v[164:167], v[156:159]\n"
    "v_exp_f32 v135, v135 clamp\n"
    "s_add_u32 m0, s68, 2048\n"
    "v_exp_f32 v137, v137 clamp\n"
    "v_add_u32 v253, s67, v244\n"
    "v_mfma_f32_4x4x4_16b_bf16 v[224:227], v[228:229], v[176:177], v[224:227] cbsz:4 abid:0\n"
    "v_exp_f32 v138, v138 clamp\n"
    "v_exp_f32 v139, v139 clamp\n"
    "v_mfma_f32_4x4x4_16b_bf16 v[224:227], v[228:229], v[178:179], v[224:227] cbsz:4 abid:1\n"
    "v_exp_f32 v140, v140 clamp\n"
    "v_exp_f32 v141, v141 clamp\n"
    "v_mfma_f32_4x4x4_16b_bf16 v[224:227], v[228:229], v[180:181], v[224:227] cbsz:4 abid:2\n"
    "v_exp_f32 v142, v142 clamp\n"
    "v_exp_f32 v143, v143 clamp\n"
    "v_mfma_f32_4x4x4_16b_bf16 v[224:227], v[228:229], v[182:183], v[224:227] cbsz:4 abid:3\n"
    "s_waitcnt vmcnt(4)\n"
    "ds_read_b64_tr_b16 v[176:177], v240 offset:2048\n"
    "ds_read_b64_tr_b16 v[178:179], v241 offset:2304\n"
    "ds_read_b64_tr_b16 v[180:181], v242 offset:2560\n"
    "ds_read_b64_tr_b16 v[182:183], v243 offset:2816\n"
    "ds_read_b32 v236, v253 offset:0\n"
    "ds_read_b32 v238, v253 offset:4\n"
    "ds_read_b128 v[160:163], v234 offset:4096\n"
    "ds_read_b128 v[164:167], v235 offset:4096\n"
    "s_waitcnt lgkmcnt(8)\n"
    "s_nop 0\n"
    "v_readlane_b32 s20, v252, 0\n"
    "v_readlane_b32 s21, v252, 1\n"
    "v_readlane_b32 s22, v252, 2\n"
    "v_readlane_b32 s23, v252, 3\n"
    "v_readlane_b32 s24, v252, 4\n"
    "v_readlane_b32 s25, v252, 5\n"
    "v_readlane_b32 s26, v252, 6\n"
    "v_readlane_b32 s27, v252, 7\n"
    "v_pk_fma_f32 v[128:129], v[128:129], s[70:71], v[96:97]\n"
    "v_pk_fma_f32 v[130:131], v[130:131], s[70:71], v[98:99]\n"
    "v_pk_add_f32 v[230:231], v[128:129], v[130:131]\n"
    "v_pk_mul_f32 v[232:233], v[128:129], v[130:131]\n"
    "v_pk_fma_f32 v[132:133], v[132:133], s[70:71], v[100:101]\n"
    "v_pk_fma_f32 v[230:231], v[230:231], v[132:133], v[232:233]\n"
    "v_pk_mul_f32 v[232:233], v[232:233], v[132:133]\n"
    "v_pk_fma_f32 v[134:135], v[134:135], s[70:71], v[102:103]\n"
    "v_pk_fma_f32 v[230:231], v[230:231], v[134:135], v[232:233]\n"
    "v_pk_mul_f32 v[232:233], v[232:233], v[134:135]\n"
    "v_pk_fma_f32 v[136:137], v[136:137], s[70:71], v[104:105]\n"
    "v_pk_fma_f32 v[230:231], v[230:231], v[136:137], v[232:233]\n"
    "v_pk_mul_f32 v[232:233], v[232:233], v[136:137]\n"
    "v_pk_fma_f32 v[138:139], v[138:139], s[70:71], v[106:107]\n"
    "v_pk_fma_f32 v[230:231], v[230:231], v[138:139], v[232:233]\n"
    "v_pk_mul_f32 v[232:233], v[232:233], v[138:139]\n"
    "v_pk_fma_f32 v[140:141], v[140:141], s[70:71], v[108:109]\n"
    "v_pk_fma_f32 v[230:231], v[230:231], v[140:141], v[232:233]\n"
    "v_pk_mul_f32 v[232:233], v[232:233], v[140:141]\n"
    "v_pk_fma_f32 v[142:143], v[142:143], s[70:71], v[110:111]\n"
    "v_pk_fma_f32 v[230:231], v[230:231], v[142:143], v[232:233]\n"
    "v_pk_mul_f32 v[232:233], v[232:233], v[142:143]\n"
    "v_rcp_f32 v232, v232\n"
    "v_rcp_f32 v233, v233\n"
    "v_fma_f32 v230, v230, v232, v247\n"
    "v_fmac_f32 v230, v231, v233\n"
    "v_lshlrev_b32 v232, 2, v249\n"
    "v_lshrrev_b32 v233, 2, v249\n"
    "v_and_b32 v232, 56, v232\n"
    "v_and_b32 v233, 4, v233\n"
    "v_add3_u32 v232, v232, v233, s81\n"
    "ds_read_b32 v232, v232\n"
    "s_add_u32 s81, s81, 64\n"
    "s_waitcnt lgkmcnt(0)\n"
    "v_lshlrev_b32 v232, 1, v232\n"
    "buffer_load_ushort v232, v232, s[44:47], 0 offen\n"
    "s_waitcnt vmcnt(0)\n"
    "v_lshlrev_b32 v232, 16, v232\n"
    "v_mul_f32 v230, v230, v232\n"
    "v_cvt_pk_bf16_f32 v230, v230, v230\n"
    "s_add_u32 s67, s67, 64\n"
    "s_waitcnt lgkmcnt(0)\n"
    "ds_write_b16 v245, v230\n"
    "ds_read_b64 v[228:229], v246\n"
    "s_lshl1_add_u32 s76, s72, s76\n"
    "buffer_load_dwordx4 v[236:237], s[40:43], 0 idxen offen offset:0 nt lds\n"
    "buffer_load_dwordx4 v[238:239], s[40:43], 0 idxen offen offset:1024 nt lds\n"
    "s_and_b32 s62, s58, 63\n"
    "s_mul_i32 s62, s62, 272\n"
    "s_mov_b32 s58, s59\n"
    "s_mov_b32 s59, s60\n"
    "s_mov_b32 s81, s65\n"
    "s_xor_b32 s65, s65, s64\n"
    "s_mov_b32 s80, s83\n"
    "s_add_u32 s82, s65, 256\n"
    "s_cmp_lt_i32 s59, s57\n"
    "s_cselect_b32 s76, s76, -1\n"
    "s_bitcmp1_b32 s58, 30\n"
    "s_cbranch_scc0 .Ldin_A1\n"
    ".Ldin_A1s:\n"
    "v_mfma_f32_16x16x32_bf16 v[128:131], v[0:3], v[160:163], v[112:115]\n"
    "v_exp_f32 v144, v144 clamp\n"
    "v_mfma_f32_16x16x32_bf16 v[128:131], v[4:7], v[164:167], v[128:131]\n"
    "v_exp_f32 v145, v145 clamp\n"
    "v_mfma_f32_16x16x32_bf16 v[132:135], v[8:11], v[160:163], v[116:119]\n"
    "v_exp_f32 v146, v146 clamp\n"
    "v_mfma_f32_16x16x32_bf16 v[132:135], v[12:15], v[164:167], v[132:135]\n"
    "v_exp_f32 v147, v147 clamp\n"
    "v_mfma_f32_16x16x32_bf16 v[136:139], v[16:19], v[160:163], v[120:123]\n"
    "v_exp_f32 v148, v148 clamp\n"
    "v_mfma_f32_16x16x32_bf16 v[136:139], v[20:23], v[164:167], v[136:139]\n"
    "v_exp_f32 v149, v149 clamp\n"
    "v_mfma_f32_16x16x32_bf16 v[140:143], v[24:27], v[160:163], v[124:127]\n"
    "v_exp_f32 v150, v150 clamp\n"
    "v_mfma_f32_16x16x32_bf16 v[140:143], v[28:31], v[164:167], v[140:143]\n"
    "v_exp_f32 v151, v151 clamp\n"
    "v_exp_f32 v152, v152 clamp\n"
    "s_mov_b32 m0, s62\n"
    "v_exp_f32 v153, v153 clamp\n"
    "v_add_u32 v253, s67, v244\n"
    "s_waitcnt lgkmcnt(0)\n"
    "v_mfma_f32_4x4x4_16b_bf16 v[224:227], v[228:229], v[168:169], v[224:227] cbsz:4 abid:0\n"
    "v_exp_f32 v154, v154 clamp\n"
    "v_exp_f32 v155, v155 clamp\n"
    "v_mfma_f32_4x4x4_16b_bf16 v[224:227], v[228:229], v[170:171], v[224:227] cbsz:4 abid:1\n"
    "v_exp_f32 v156, v156 clamp\n"
    "v_exp_f32 v157, v157 clamp\n"
    "v_mfma_f32_4x4x4_16b_bf16 v[224:227], v[228:229], v[172:173], v[224:227] cbsz:4 abid:2\n"
    "v_exp_f32 v158, v158 clamp\n"
    "v_exp_f32 v159, v159 clamp\n"
    "v_mfma_f32_4x4x4_16b_bf16 v[224:227], v[228:229], v[174:175], v[224:227] cbsz:4 abid:3\n"
    "s_waitcnt vmcnt(4)\n"
    "ds_read_b64_tr_b16 v[168:169], v240 offset:4096\n"
    "ds_read_b64_tr_b16 v[170:171], v241 offset:4352\n"
    "ds_read_b64_tr_b16 v[172:173], v242 offset:4608\n"
    "ds_read_b64_tr_b16 v[174:175], v243 offset:4864\n"
    "ds_read_b32 v236, v253 offset:0\n"
    "ds_read_b32 v238, v253 offset:4\n"
    "ds_read_b128 v[160:163], v234 offset:6144\n"
    "ds_read_b128 v[164:167], v235 offset:6144\n"
    "s_cbranch_vccz .Ldin_A1s_np\n"
    "s_setprio 1\n"
    ".Ldin_A1s_np:\n"
    "v_pk_fma_f32 v[144:145], v[144:145], s[70:71], v[96:97]\n"
    "v_pk_fma_f32 v[146:147], v[146:147], s[70:71], v[98:99]\n"
    "v_pk_add_f32 v[190:191], v[224:225], v[226:227]\n"
    "v_add_f32 v190, v190, v191\n"
    "ds_write_addtid_b32 v190 offset:32768\n"
    "s_add_u32 m0, s68, 4096\n"
    "s_cmp_ge_i32 s59, s57\n"
    "s_cbranch_scc1 .Ldin_A1s_nl\n"
    "s_bfe_u32 s94, s59, 0x180006\n"
    "s_mul_i32 s86, s94, s56\n"
    "v_mbcnt_lo_u32_b32 v184, s20, 0\n"
    "v_mbcnt_lo_u32_b32 v185, s22, 0\n"
    "v_mbcnt_lo_u32_b32 v186, s24, 0\n"
    "v_mbcnt_lo_u32_b32 v187, s26, 0\n"
    "s_bcnt1_i32_b64 s90, s[20:21]\n"
    "s_bcnt1_i32_b64 s91, s[22:23]\n"
    "s_bcnt1_i32_b64 s92, s[24:25]\n"
    "s_bcnt1_i32_b64 s93, s[26:27]\n"
    "v_mbcnt_hi_u32_b32 v184, s21, v184\n"
    "v_mbcnt_hi_u32_b32 v185, s23, v185\n"
    "v_mbcnt_hi_u32_b32 v186, s25, v186\n"
    "v_mbcnt_hi_u32_b32 v187, s27, v187\n"
    "s_lshl2_add_u32 s90, s90, s65\n"
    "s_lshl2_add_u32 s91, s91, s90\n"
    "s_lshl2_add_u32 s92, s92, s91\n"
    "s_lshl2_add_u32 s93, s93, s92\n"
    "v_add_u32 v188, s86, v251\n"
    "v_lshl_add_u32 v184, v184, 2, s65\n"
    "v_lshl_add_u32 v185, v185, 2, s90\n"
    "v_lshl_add_u32 v186, v186, 2, s91\n"
    "v_lshl_add_u32 v187, v187, 2, s92\n"
    "v_mov_b32 v191, -1\n"
    "v_lshl_add_u32 v190, v249, 1, s93\n"
    "ds_write2_b32 v190, v191, v191 offset1:64\n"
    "s_mov_b64 exec, s[20:21]\n"
    "ds_write_b32 v184, v188\n"
    "s_mov_b64 exec, s[22:23]\n"
    "v_add_u32 v189, 64, v188\n"
    "ds_write_b32 v185, v189\n"
    "s_mov_b64 exec, s[24:25]\n"
    "v_add_u32 v190, 128, v188\n"
    "ds_write_b32 v186, v190\n"
    "s_mov_b64 exec, s[26:27]\n"
    "v_add_u32 v189, 192, v188\n"
    "ds_write_b32 v187, v189\n"
    "s_mov_b64 exec, -1\n"
    "s_sub_u32 s95, s93, -60\n"
    "s_add_u32 s94, s82, 128\n"
    "s_andn2_b32 s95, s95, 63\n"
    "s_max_u32 s83, s95, s94\n"
    ".Ldin_A1s_nl:\n"
    "v_pk_add_f32 v[230:231], v[144:145], v[146:147]\n"
    "v_pk_mul_f32 v[232:233], v[144:145], v[146:147]\n"
    "v_pk_fma_f32 v[148:149], v[148:149], s[70:71], v[100:101]\n"
    "v_pk_fma_f32 v[230:231], v[230:231], v[148:149], v[232:233]\n"
    "v_pk_mul_f32 v[232:233], v[232:233], v[148:149]\n"
    "v_pk_fma_f32 v[150:151], v[150:151], s[70:71], v[102:103]\n"
    "v_pk_fma_f32 v[230:231], v[230:231], v[150:151], v[232:233]\n"
    "v_pk_mul_f32 v[232:233], v[232:233], v[150:151]\n"
    "v_pk_fma_f32 v[152:153], v[152:153], s[70:71], v[104:105]\n"
    "v_pk_fma_f32 v[230:231], v[230:231], v[152:153], v[232:233]\n"
    "v_pk_mul_f32 v[232:233], v[232:233], v[152:153]\n"
    "v_pk_fma_f32 v[154:155], v[154:155], s[70:71], v[106:107]\n"
    "v_pk_fma_f32 v[230:231], v[230:231], v[154:155], v[232:233]\n"
    "v_pk_mul_f32 v[232:233], v[232:233], v[154:155]\n"
    "v_pk_fma_f32 v[156:157], v[156:157], s[70:71], v[108:109]\n"
    "v_pk_fma_f32 v[230:231], v[230:231], v[156:157], v[232:233]\n"
    "v_pk_mul_f32 v[232:233], v[232:233], v[156:157]\n"
    "v_pk_fma_f32 v[158:159], v[158:159], s[70:71], v[110:111]\n"
    "v_pk_fma_f32 v[230:231], v[230:231], v[158:159], v[232:233]\n"
    "v_pk_mul_f32 v[232:233], v[232:233], v[158:159]\n"
    "v_rcp_f32 v232, v232\n"
    "v_rcp_f32 v233, v233\n"
    "v_fma_f32 v230, v230, v232, v247\n"
    "v_fmac_f32 v230, v231, v233\n"
    "v_lshlrev_b32 v232, 2, v249\n"
    "v_lshrrev_b32 v233, 2, v249\n"
    "v_and_b32 v232, 56, v232\n"
    "v_and_b32 v233, 4, v233\n"
    "v_add3_u32 v232, v232, v233, s81\n"
    "ds_read_b32 v232, v232\n"
    "s_add_u32 s81, s81, 64\n"
    "s_waitcnt lgkmcnt(0)\n"
    "v_lshlrev_b32 v232, 1, v232\n"
    "buffer_load_ushort v232, v232, s[44:47], 0 offen\n"
    "s_waitcnt vmcnt(0)\n"
    "v_lshlrev_b32 v232, 16, v232\n"
    "v_mul_f32 v230, v230, v232\n"
    "v_cvt_pk_bf16_f32 v230, v230, v230\n"
    "s_add_u32 s67, s67, 64\n"
    "s_waitcnt lgkmcnt(0)\n"
    "ds_write_b16 v245, v230\n"
    "ds_read_b64 v[228:229], v246\n"
    "buffer_load_dwordx4 v[236:237], s[40:43], 0 idxen offen offset:0 nt lds\n"
    "buffer_load_dwordx4 v[238:239], s[40:43], 0 idxen offen offset:1024 nt lds\n"
    ".Ldin_B2s:\n"
    "v_mfma_f32_16x16x32_bf16 v[144:147], v[0:3], v[160:163], v[112:115]\n"
    "v_exp_f32 v128, v128 clamp\n"
    "v_mfma_f32_16x16x32_bf16 v[144:147], v[4:7], v[164:167], v[144:147]\n"
    "v_exp_f32 v129, v129 clamp\n"
    "v_mfma_f32_16x16x32_bf16 v[148:151], v[8:11], v[160:163], v[116:119]\n"
    "v_exp_f32 v130, v130 clamp\n"
    "v_mfma_f32_16x16x32_bf16 v[148:151], v[12:15], v[164:167], v[148:151]\n"
    "v_exp_f32 v131, v131 clamp\n"
    "v_mfma_f32_16x16x32_bf16 v[152:155], v[16:19], v[160:163], v[120:123]\n"
    "v_exp_f32 v132, v132 clamp\n"
    "v_mfma_f32_16x16x32_bf16 v[152:155], v[20:23], v[164:167], v[152:155]\n"
    "v_exp_f32 v133, v133 clamp\n"
    "v_mfma_f32_16x16x32_bf16 v[156:159], v[24:27], v[160:163], v[124:127]\n"
    "v_exp_f32 v134, v134 clamp\n"
    "v_mfma_f32_16x16x32_bf16 v[156:159], v[28:31], v[164:167], v[156:159]\n"
    "v_exp_f32 v135, v135 clamp\n"
    "v_exp_f32 v136, v136 clamp\n"
    "s_cmp_eq_u32 s67, s80\n"
    "s_cselect_b32 s67, s65, s67\n"
    "s_and_b32 s94, s59, 63\n"
    "s_mul_i32 s94, s94, 272\n"
    "v_add_u32 v252, s94, v250\n"
    "s_add_u32 m0, s68, 6144\n"
    "v_exp_f32 v137, v137 clamp\n"
    "v_add_u32 v253, s67, v244\n"
    "s_waitcnt lgkmcnt(0)\n"
    "v_mfma_f32_4x4x4_16b_bf16 v[224:227], v[228:229], v[176:177], 0 cbsz:4 abid:0\n"
    "v_exp_f32 v138, v138 clamp\n"
    "v_exp_f32 v139, v139 clamp\n"
    "v_mfma_f32_4x4x4_16b_bf16 v[224:227], v[228:229], v[178:179], v[224:227] cbsz:4 abid:1\n"
    "v_exp_f32 v140, v140 clamp\n"
    "v_exp_f32 v141, v141 clamp\n"
    "v_mfma_f32_4x4x4_16b_bf16 v[224:227], v[228:229], v[180:181], v[224:227] cbsz:4 abid:2\n"
    "v_exp_f32 v142, v142 clamp\n"
    "v_exp_f32 v143, v143 clamp\n"
    "v_mfma_f32_4x4x4_16b_bf16 v[224:227], v[228:229], v[182:183], v[224:227] cbsz:4 abid:3\n"
    "s_waitcnt vmcnt(4)\n"
    "s_memrealtime s[34:35]\n"
    "ds_read_b64_tr_b16 v[176:177], v240 offset:6144\n"
    "ds_read_b64_tr_b16 v[178:179], v241 offset:6400\n"
    "ds_read_b64_tr_b16 v[180:181], v242 offset:6656\n"
    "ds_read_b64_tr_b16 v[182:183], v243 offset:6912\n"
    "ds_read_b32 v236, v253 offset:0\n"
    "ds_read_b32 v238, v253 offset:4\n"
    "ds_read_b128 v[160:163], v234 offset:0\n"
    "ds_read_b128 v[164:167], v235 offset:0\n"
    "v_add_u32_e64 v184, v248, v252\n"
    "ds_read_b64 v[184:185], v184\n"
    "v_pk_fma_f32 v[128:129], v[128:129], s[70:71], v[96:97]\n"
    "v_pk_fma_f32 v[130:131], v[130:131], s[70:71], v[98:99]\n"
    "v_pk_add_f32 v[230:231], v[128:129], v[130:131]\n"
    "v_pk_mul_f32 v[232:233], v[128:129], v[130:131]\n"
    "v_pk_fma_f32 v[132:133], v[132:133], s[70:71], v[100:101]\n"
    "v_pk_fma_f32 v[230:231], v[230:231], v[132:133], v[232:233]\n"
    "v_pk_mul_f32 v[232:233], v[232:233], v[132:133]\n"
    "v_pk_fma_f32 v[134:135], v[134:135], s[70:71], v[102:103]\n"
    "v_pk_fma_f32 v[230:231], v[230:231], v[134:135], v[232:233]\n"
    "v_pk_mul_f32 v[232:233], v[232:233], v[134:135]\n"
    "v_pk_fma_f32 v[136:137], v[136:137], s[70:71], v[104:105]\n"
    "v_pk_fma_f32 v[230:231], v[230:231], v[136:137], v[232:233]\n"
    "v_pk_mul_f32 v[232:233], v[232:233], v[136:137]\n"
    "v_pk_fma_f32 v[138:139], v[138:139], s[70:71], v[106:107]\n"
    "v_pk_fma_f32 v[230:231], v[230:231], v[138:139], v[232:233]\n"
    "v_pk_mul_f32 v[232:233], v[232:233], v[138:139]\n"
    "v_pk_fma_f32 v[140:141], v[140:141], s[70:71], v[108:109]\n"
    "v_pk_fma_f32 v[230:231], v[230:231], v[140:141], v[232:233]\n"
    "v_pk_mul_f32 v[232:233], v[232:233], v[140:141]\n"
    "v_pk_fma_f32 v[142:143], v[142:143], s[70:71], v[110:111]\n"
    "v_pk_fma_f32 v[230:231], v[230:231], v[142:143], v[232:233]\n"
    "v_pk_mul_f32 v[232:233], v[232:233], v[142:143]\n"
    "v_rcp_f32 v232, v232\n"
    "v_rcp_f32 v233, v233\n"
    "v_fma_f32 v230, v230, v232, v247\n"
    "v_fmac_f32 v230, v231, v233\n"
    "v_lshlrev_b32 v232, 2, v249\n"
    "v_lshrrev_b32 v233, 2, v249\n"
    "v_and_b32 v232, 56, v232\n"
    "v_and_b32 v233, 4, v233\n"
    "v_add3_u32 v232, v232, v233, s81\n"
    "ds_read_b32 v232, v232\n"
    "s_add_u32 s81, s81, 64\n"
    "s_waitcnt lgkmcnt(0)\n"
    "v_lshlrev_b32 v232, 1, v232\n"
    "buffer_load_ushort v232, v232, s[44:47], 0 offen\n"
    "s_waitcnt vmcnt(0)\n"
    "v_lshlrev_b32 v232, 16, v232\n"
    "v_mul_f32 v230, v230, v232\n"
    "v_cvt_pk_bf16_f32 v230, v230, v230\n"
    "s_add_u32 s67, s67, 64\n"
    "s_waitcnt lgkmcnt(0)\n"
    "ds_write_b16 v245, v230\n"
    "ds_read_b64 v[228:229], v246\n"
    "v_and_b32 v184, v254, v184\n"
    "v_and_b32 v185, v255, v185\n"
    "s_cmp_le_u32 s34, s76\n"
    "s_cbranch_scc1 .Ldin_B2s_wait\n"
    ".Ldin_B2s_go:\n"
    "s_sub_u32 s94, s34, 60\n"
    "s_max_u32 s76, s76, s94\n"
    "s_add_u32 s76, s76, s72\n"
    ".Ldin_B2s_dma:\n"
    "buffer_load_dwordx4 v[236:237], s[40:43], 0 idxen offen offset:0 nt lds\n"
    "buffer_load_dwordx4 v[238:239], s[40:43], 0 idxen offen offset:1024 nt lds\n"
    ".Ldin_B2s_nd:\n"
    "s_branch .Ldin_G3s\n"
    ".Ldin_L0s_poll:\n"
    "s_sleep 1\n"
    "ds_read_b64 v[232:233], v253\n"
    "s_waitcnt lgkmcnt(0)\n"
    "v_readfirstlane_b32 s60, v232\n"
    "s_cmp_eq_u32 s60, -1\n"
    "s_cbranch_scc1 .Ldin_L0s_poll\n"
    "s_branch .Ldin_L0s_pr\n"
    ".Ldin_B2s_wait:\n"
    "s_cmp_lt_i32 s59, s57\n"
    "s_cbranch_scc1 .Ldin_B2s_w2\n"
    "s_sub_u32 s94, s67, s65\n"
    "s_cmp_gt_u32 s94, 256\n"
    "s_cbranch_scc1 .Ldin_B2s_dma\n"
    "s_cmp_eq_u32 s94, 64\n"
    "s_waitcnt vmcnt(2)\n"
    "s_cbranch_scc1 .Ldin_B2s_nd\n"
    "s_waitcnt vmcnt(0)\n"
    "s_branch .Ldin_B2s_nd\n"
    ".Ldin_B2s_slp:\n"
    "s_sleep 1\n"
    ".Ldin_B2s_w2:\n"
    "s_memrealtime s[34:35]\n"
    "s_waitcnt lgkmcnt(0)\n"
    "s_sub_u32 s94, s34, s76\n"
    "s_cmp_lt_i32 s94, 0\n"
    "s_cbranch_scc1 .Ldin_B2s_slp\n"
    "s_branch .Ldin_B2s_go\n"
    ".p2align 5\n"
    ".Ldin_L3s:\n"
    "s_setprio 0\n"
    "s_cmp_ge_i32 s59, s57\n"
    "s_cbranch_scc1 .Ldin_E1s\n"
    "s_add_u32 s76, s76, s73\n"
    "v_mov_b32_e64 v253, s63\n"
    "ds_read_b64 v[232:233], v253\n"
    "v_mfma_f32_4x4x4_16b_bf16 v[128:131], v[184:185], v[32:33], v[64:67] cbsz:2 abid:0\n"
    "ds_read_b128 v[112:115], v252 offset:17408\n"
    "v_mfma_f32_4x4x4_16b_bf16 v[132:135], v[184:185], v[34:35], v[68:71] cbsz:2 abid:1\n"
    "ds_read_b128 v[116:119], v252 offset:17424\n"
    "v_mfma_f32_4x4x4_16b_bf16 v[136:139], v[184:185], v[36:37], v[72:75] cbsz:2 abid:2\n"
    "ds_read_b128 v[120:123], v252 offset:17536\n"
    "ds_read_b128 v[124:127], v252 offset:17552\n"
    "v_cvt_pk_bf16_f32 v0, v128, v129\n"
    "v_cvt_pk_bf16_f32 v1, v130, v131\n"
    "v_mfma_f32_4x4x4_16b_bf16 v[140:143], v[184:185], v[38:39], v[76:79] cbsz:2 abid:3\n"
    "v_cvt_pk_bf16_f32 v2, v132, v133\n"
    "v_cvt_pk_bf16_f32 v3, v134, v135\n"
    "v_mfma_f32_4x4x4_16b_bf16 v[128:131], v[184:185], v[40:41], v[80:83] cbsz:2 abid:0\n"
    "v_cvt_pk_bf16_f32 v4, v136, v137\n"
    "v_cvt_pk_bf16_f32 v5, v138, v139\n"
    "v_mfma_f32_4x4x4_16b_bf16 v[132:135], v[184:185], v[42:43], v[84:87] cbsz:2 abid:1\n"
    "v_cvt_pk_bf16_f32 v6, v140, v141\n"
    "v_cvt_pk_bf16_f32 v7, v142, v143\n"
    "v_mfma_f32_4x4x4_16b_bf16 v[136:139], v[184:185], v[44:45], v[88:91] cbsz:2 abid:2\n"
    "v_cvt_pk_bf16_f32 v8, v128, v129\n"
    "v_cvt_pk_bf16_f32 v9, v130, v131\n"
    "v_mfma_f32_4x4x4_16b_bf16 v[140:143], v[184:185], v[46:47], v[92:95] cbsz:2 abid:3\n"
    "v_cvt_pk_bf16_f32 v10, v132, v133\n"
    "v_cvt_pk_bf16_f32 v11, v134, v135\n"
    "v_mfma_f32_4x4x4_16b_bf16 v[128:131], v[184:185], v[48:49], v[192:195] cbsz:2 abid:0\n"
    "v_cvt_pk_bf16_f32 v12, v136, v137\n"
    "v_cvt_pk_bf16_f32 v13, v138, v139\n"
    "v_mfma_f32_4x4x4_16b_bf16 v[132:135], v[184:185], v[50:51], v[196:199] cbsz:2 abid:1\n"
    "v_cvt_pk_bf16_f32 v14, v140, v141\n"
    "v_cvt_pk_bf16_f32 v15, v142, v143\n"
    "v_mfma_f32_4x4x4_16b_bf16 v[136:139], v[184:185], v[52:53], v[200:203] cbsz:2 abid:2\n"
    "s_waitcnt lgkmcnt(0)\n"
    "v_readfirstlane_b32 s60, v232\n"
    "v_cvt_pk_bf16_f32 v16, v128, v129\n"
    "v_cvt_pk_bf16_f32 v17, v130, v131\n"
    "v_mfma_f32_4x4x4_16b_bf16 v[140:143], v[184:185], v[54:55], v[204:207] cbsz:2 abid:3\n"
    "s_cmp_eq_u32 s60, -1\n"
    "s_cbranch_scc1 .Ldin_L3s_poll\n"
    ".Ldin_L3s_pr:\n"
    "v_readfirstlane_b32 s95, v233\n"
    "s_and_b32 s94, s60, 63\n"
    "s_lshl_b32 s94, s94, 6\n"
    "s_add_u32 s94, s94, 68480\n"
    "s_add_u32 s63, s63, 8\n"
    "v_lshl_add_u32 v252, v249, 1, s94\n"
    "ds_read_b32 v252, v252\n"
    "s_sub_u32 s95, s95, s76\n"
    "s_ashr_i32 s95, s95, 4\n"
    "s_min_i32 s95, s95, s74\n"
    "s_max_i32 s72, s95, s75\n"
    "v_cvt_pk_bf16_f32 v18, v132, v133\n"
    "v_cvt_pk_bf16_f32 v19, v134, v135\n"
    "v_mfma_f32_4x4x4_16b_bf16 v[128:131], v[184:185], v[56:57], v[208:211] cbsz:2 abid:0\n"
    "v_cvt_pk_bf16_f32 v20, v136, v137\n"
    "v_cvt_pk_bf16_f32 v21, v138, v139\n"
    "v_mfma_f32_4x4x4_16b_bf16 v[132:135], v[184:185], v[58:59], v[212:215] cbsz:2 abid:1\n"
    "v_cvt_pk_bf16_f32 v22, v140, v141\n"
    "v_cvt_pk_bf16_f32 v23, v142, v143\n"
    "v_mfma_f32_4x4x4_16b_bf16 v[136:139], v[184:185], v[60:61], v[216:219] cbsz:2 abid:2\n"
    "v_cvt_pk_bf16_f32 v24, v128, v129\n"
    "v_cvt_pk_bf16_f32 v25, v130, v131\n"
    "v_mfma_f32_4x4x4_16b_bf16 v[140:143], v[184:185], v[62:63], v[220:223] cbsz:2 abid:3\n"
    "v_cvt_pk_bf16_f32 v26, v132, v133\n"
    "v_cvt_pk_bf16_f32 v27, v134, v135\n"
    "v_cvt_pk_bf16_f32 v28, v136, v137\n"
    "v_cvt_pk_bf16_f32 v29, v138, v139\n"
    "v_exp_f32 v152, v152 clamp\n"
    "v_cvt_pk_bf16_f32 v30, v140, v141\n"
    "v_cvt_pk_bf16_f32 v31, v142, v143\n"
    "v_mfma_f32_16x16x32_bf16 v[128:131], v[0:3], v[160:163], v[112:115]\n"
    "v_exp_f32 v144, v144 clamp\n"
    "v_mfma_f32_16x16x32_bf16 v[128:131], v[4:7], v[164:167], v[128:131]\n"
    "v_exp_f32 v145, v145 clamp\n"
    "v_mfma_f32_16x16x32_bf16 v[132:135], v[8:11], v[160:163], v[116:119]\n"
    "v_exp_f32 v146, v146 clamp\n"
    "v_mfma_f32_16x16x32_bf16 v[132:135], v[12:15], v[164:167], v[132:135]\n"
    "v_exp_f32 v147, v147 clamp\n"
    "v_mfma_f32_16x16x32_bf16 v[136:139], v[16:19], v[160:163], v[120:123]\n"
    "v_exp_f32 v148, v148 clamp\n"
    "v_mfma_f32_16x16x32_bf16 v[136:139], v[20:23], v[164:167], v[136:139]\n"
    "v_exp_f32 v149, v149 clamp\n"
    "v_mfma_f32_16x16x32_bf16 v[140:143], v[24:27], v[160:163], v[124:127]\n"
    "v_exp_f32 v150, v150 clamp\n"
    "v_mfma_f32_16x16x32_bf16 v[140:143], v[28:31], v[164:167], v[140:143]\n"
    "v_exp_f32 v151, v151 clamp\n"
    "s_add_u32 m0, s68, 0\n"
    "v_exp_f32 v153, v153 clamp\n"
    "v_add_u32_e64 v253, s67, v244\n"
    "v_mfma_f32_4x4x4_16b_bf16 v[224:227], v[228:229], v[168:169], v[224:227] cbsz:4 abid:0\n"
    "v_exp_f32 v154, v154 clamp\n"
    "v_exp_f32 v155, v155 clamp\n"
    "v_mfma_f32_4x4x4_16b_bf16 v[224:227], v[228:229], v[170:171], v[224:227] cbsz:4 abid:1\n"
    "v_exp_f32 v156, v156 clamp\n"
    "v_exp_f32 v157, v157 clamp\n"
    "v_mfma_f32_4x4x4_16b_bf16 v[224:227], v[228:229], v[172:173], v[224:227] cbsz:4 abid:2\n"
    "v_exp_f32 v158, v158 clamp\n"
    "v_exp_f32 v159, v159 clamp\n"
    "v_mfma_f32_4x4x4_16b_bf16 v[224:227], v[228:229], v[174:175], v[224:227] cbsz:4 abid:3\n"
    "s_waitcnt vmcnt(4)\n"
    "ds_read_b64_tr_b16 v[168:169], v240 offset:0\n"
    "ds_read_b64_tr_b16 v[170:171], v241 offset:256\n"
    "ds_read_b64_tr_b16 v[172:173], v242 offset:512\n"
    "ds_read_b64_tr_b16 v[174:175], v243 offset:768\n"
    "ds_read_b32 v236, v253 offset:0\n"
    "ds_read_b32 v238, v253 offset:4\n"
    "ds_read_b128 v[160:163], v234 offset:2048\n"
    "ds_read_b128 v[164:167], v235 offset:2048\n"
    "s_waitcnt lgkmcnt(8)\n"
    "s_nop 0\n"
    "v_readlane_b32 s20, v252, 0\n"
    "v_readlane_b32 s21, v252, 1\n"
    "v_readlane_b32 s22, v252, 2\n"
    "v_readlane_b32 s23, v252, 3\n"
    "v_readlane_b32 s24, v252, 4\n"
    "v_readlane_b32 s25, v252, 5\n"
    "v_readlane_b32 s26, v252, 6\n"
    "v_readlane_b32 s27, v252, 7\n"
    "v_pk_fma_f32 v[144:145], v[144:145], s[70:71], v[96:97]\n"
    "v_pk_fma_f32 v[146:147], v[146:147], s[70:71], v[98:99]\n"
    "v_pk_add_f32 v[230:231], v[144:145], v[146:147]\n"
    "v_pk_mul_f32 v[232:233], v[144:145], v[146:147]\n"
    "v_pk_fma_f32 v[148:149], v[148:149], s[70:71], v[100:101]\n"
    "v_pk_fma_f32 v[230:231], v[230:231], v[148:149], v[232:233]\n"
    "v_pk_mul_f32 v[232:233], v[232:233], v[148:149]\n"
    "v_pk_fma_f32 v[150:151], v[150:151], s[70:71], v[102:103]\n"
    "v_pk_fma_f32 v[230:231], v[230:231], v[150:151], v[232:233]\n"
    "v_pk_mul_f32 v[232:233], v[232:233], v[150:151]\n"
    "v_pk_fma_f32 v[152:153], v[152:153], s[70:71], v[104:105]\n"
    "v_pk_fma_f32 v[230:231], v[230:231], v[152:153], v[232:233]\n"
    "v_pk_mul_f32 v[232:233], v[232:233], v[152:153]\n"
    "v_pk_fma_f32 v[154:155], v[154:155], s[70:71], v[106:107]\n"
    "v_pk_fma_f32 v[230:231], v[230:231], v[154:155], v[232:233]\n"
    "v_pk_mul_f32 v[232:233], v[232:233], v[154:155]\n"
    "v_pk_fma_f32 v[156:157], v[156:157], s[70:71], v[108:109]\n"
    "v_pk_fma_f32 v[230:231], v[230:231], v[156:157], v[232:233]\n"
    "v_pk_mul_f32 v[232:233], v[232:233], v[156:157]\n"
    "v_pk_fma_f32 v[158:159], v[158:159], s[70:71], v[110:111]\n"
    "v_pk_fma_f32 v[230:231], v[230:231], v[158:159], v[232:233]\n"
    "v_pk_mul_f32 v[232:233], v[232:233], v[158:159]\n"
    "v_rcp_f32 v232, v232\n"
    "v_rcp_f32 v233, v233\n"
    "v_fma_f32 v230, v230, v232, v247\n"
    "v_fmac_f32 v230, v231, v233\n"
    "v_lshlrev_b32 v232, 2, v249\n"
    "v_lshrrev_b32 v233, 2, v249\n"
    "v_and_b32 v232, 56, v232\n"
    "v_and_b32 v233, 4, v233\n"
    "v_add3_u32 v232, v232, v233, s81\n"
    "ds_read_b32 v232, v232\n"
    "s_add_u32 s81, s81, 64\n"
    "s_waitcnt lgkmcnt(0)\n"
    "v_lshlrev_b32 v232, 1, v232\n"
    "buffer_load_ushort v232, v232, s[44:47], 0 offen\n"
    "s_waitcnt vmcnt(0)\n"
    "v_lshlrev_b32 v232, 16, v232\n"
    "v_mul_f32 v230, v230, v232\n"
    "v_cvt_pk_bf16_f32 v230, v230, v230\n"
    "s_sub_u32 s67, s67, -64\n"
    "s_waitcnt lgkmcnt(0)\n"
    "ds_write_b16 v245, v230\n"
    "ds_read_b64 v[228:229], v246\n"
    "s_lshl1_add_u32 s76, s72, s76\n"
    "buffer_load_dwordx4 v[236:237], s[40:43], 0 idxen offen offset:0 nt lds\n"
    "buffer_load_dwordx4 v[238:239], s[40:43], 0 idxen offen offset:1024 nt lds\n"
    "s_and_b32 s62, s58, 63\n"
    "s_mul_i32 s62, s62, 272\n"
    "s_mov_b32 s58, s59\n"
    "s_mov_b32 s59, s60\n"
    "s_mov_b32 s81, s65\n"
    "s_xor_b32 s65, s65, s64\n"
    "s_mov_b32 s80, s83\n"
    "s_add_u32 s82, s65, 256\n"
    "s_cmp_lt_i32 s59, s57\n"
    "s_cselect_b32 s76, s76, -1\n"
    "s_bitcmp1_b32 s58, 30\n"
    "s_cbranch_scc0 .Ldin_A0\n"
    "s_branch .Ldin_A0s\n"
    ".Ldin_L3s_poll:\n"
    "s_sleep 1\n"
    "ds_read_b64 v[232:233], v253\n"
    "s_waitcnt lgkmcnt(0)\n"
    "v_readfirstlane_b32 s60, v232\n"
    "s_cmp_eq_u32 s60, -1\n"
    "s_cbranch_scc1 .Ldin_L3s_poll\n"
    "s_branch .Ldin_L3s_pr\n"
    ".Ldin_E0s:\n"
    "v_exp_f32 v128, v128 clamp\n"
    "v_exp_f32 v129, v129 clamp\n"
    "v_exp_f32 v130, v130 clamp\n"
    "v_exp_f32 v131, v131 clamp\n"
    "v_exp_f32 v132, v132 clamp\n"
    "v_exp_f32 v133, v133 clamp\n"
    "v_exp_f32 v134, v134 clamp\n"
    "v_exp_f32 v135, v135 clamp\n"
    "s_and_b32 s62, s58, 63\n"
    "s_mul_i32 s62, s62, 272\n"
    "s_waitcnt lgkmcnt(0)\n"
    "v_mfma_f32_4x4x4_16b_bf16 v[224:227], v[228:229], v[176:177], v[224:227] cbsz:4 abid:0\n"
    "v_exp_f32 v136, v136 clamp\n"
    "v_exp_f32 v137, v137 clamp\n"
    "v_mfma_f32_4x4x4_16b_bf16 v[224:227], v[228:229], v[178:179], v[224:227] cbsz:4 abid:1\n"
    "v_exp_f32 v138, v138 clamp\n"
    "v_exp_f32 v139, v139 clamp\n"
    "v_mfma_f32_4x4x4_16b_bf16 v[224:227], v[228:229], v[180:181], v[224:227] cbsz:4 abid:2\n"
    "v_exp_f32 v140, v140 clamp\n"
    "v_exp_f32 v141, v141 clamp\n"
    "v_mfma_f32_4x4x4_16b_bf16 v[224:227], v[228:229], v[182:183], v[224:227] cbsz:4 abid:3\n"
    "v_exp_f32 v142, v142 clamp\n"
    "v_exp_f32 v143, v143 clamp\n"
    "v_pk_fma_f32 v[128:129], v[128:129], s[70:71], v[96:97]\n"
    "v_pk_fma_f32 v[130:131], v[130:131], s[70:71], v[98:99]\n"
    "v_pk_add_f32 v[230:231], v[128:129], v[130:131]\n"
    "v_pk_mul_f32 v[232:233], v[128:129], v[130:131]\n"
    "v_pk_fma_f32 v[132:133], v[132:133], s[70:71], v[100:101]\n"
    "v_pk_fma_f32 v[230:231], v[230:231], v[132:133], v[232:233]\n"
    "v_pk_mul_f32 v[232:233], v[232:233], v[132:133]\n"
    "v_pk_fma_f32 v[134:135], v[134:135], s[70:71], v[102:103]\n"
    "v_pk_fma_f32 v[230:231], v[230:231], v[134:135], v[232:233]\n"
    "v_pk_mul_f32 v[232:233], v[232:233], v[134:135]\n"
    "v_pk_fma_f32 v[136:137], v[136:137], s[70:71], v[104:105]\n"
    "v_pk_fma_f32 v[230:231], v[230:231], v[136:137], v[232:233]\n"
    "v_pk_mul_f32 v[232:233], v[232:233], v[136:137]\n"
    "v_pk_fma_f32 v[138:139], v[138:139], s[70:71], v[106:107]\n"
    "v_pk_fma_f32 v[230:231], v[230:231], v[138:139], v[232:233]\n"
    "v_pk_mul_f32 v[232:233], v[232:233], v[138:139]\n"
    "v_pk_fma_f32 v[140:141], v[140:141], s[70:71], v[108:109]\n"
    "v_pk_fma_f32 v[230:231], v[230:231], v[140:141], v[232:233]\n"
    "v_pk_mul_f32 v[232:233], v[232:233], v[140:141]\n"
    "v_pk_fma_f32 v[142:143], v[142:143], s[70:71], v[110:111]\n"
    "v_pk_fma_f32 v[230:231], v[230:231], v[142:143], v[232:233]\n"
    "v_pk_mul_f32 v[232:233], v[232:233], v[142:143]\n"
    "v_rcp_f32 v232, v232\n"
    "v_rcp_f32 v233, v233\n"
    "v_fma_f32 v230, v230, v232, v247\n"
    "v_fmac_f32 v230, v231, v233\n"
    "v_lshlrev_b32 v232, 2, v249\n"
    "v_lshrrev_b32 v233, 2, v249\n"
    "v_and_b32 v232, 56, v232\n"
    "v_and_b32 v233, 4, v233\n"
    "v_add3_u32 v232, v232, v233, s81\n"
    "ds_read_b32 v232, v232\n"
    "s_add_u32 s81, s81, 64\n"
    "s_waitcnt lgkmcnt(0)\n"
    "v_lshlrev_b32 v232, 1, v232\n"
    "buffer_load_ushort v232, v232, s[44:47], 0 offen\n"
    "s_waitcnt vmcnt(0)\n"
    "v_lshlrev_b32 v232, 16, v232\n"
    "v_mul_f32 v230, v230, v232\n"
    "v_cvt_pk_bf16_f32 v230, v230, v230\n"
    "ds_write_b16 v245, v230\n"
    "ds_read_b64 v[228:229], v246\n"
    "s_branch .Ldin_drain0\n"
    ".Ldin_E1s:\n"
    "v_exp_f32 v144, v144 clamp\n"
    "v_exp_f32 v145, v145 clamp\n"
    "v_exp_f32 v146, v146 clamp\n"
    "v_exp_f32 v147, v147 clamp\n"
    "v_exp_f32 v148, v148 clamp\n"
    "v_exp_f32 v149, v149 clamp\n"
    "v_exp_f32 v150, v150 clamp\n"
    "v_exp_f32 v151, v151 clamp\n"
    "s_and_b32 s62, s58, 63\n"
    "s_mul_i32 s62, s62, 272\n"
    "s_waitcnt lgkmcnt(0)\n"
    "v_mfma_f32_4x4x4_16b_bf16 v[224:227], v[228:229], v[168:169], v[224:227] cbsz:4 abid:0\n"
    "v_exp_f32 v152, v152 clamp\n"
    "v_exp_f32 v153, v153 clamp\n"
    "v_mfma_f32_4x4x4_16b_bf16 v[224:227], v[228:229], v[170:171], v[224:227] cbsz:4 abid:1\n"
    "v_exp_f32 v154, v154 clamp\n"
    "v_exp_f32 v155, v155 clamp\n"
    "v_mfma_f32_4x4x4_16b_bf16 v[224:227], v[228:229], v[172:173], v[224:227] cbsz:4 abid:2\n"
    "v_exp_f32 v156, v156 clamp\n"
    "v_exp_f32 v157, v157 clamp\n"
    "v_mfma_f32_4x4x4_16b_bf16 v[224:227], v[228:229], v[174:175], v[224:227] cbsz:4 abid:3\n"
    "v_exp_f32 v158, v158 clamp\n"
    "v_exp_f32 v159, v159 clamp\n"
    "v_pk_fma_f32 v[144:145], v[144:145], s[70:71], v[96:97]\n"
    "v_pk_fma_f32 v[146:147], v[146:147], s[70:71], v[98:99]\n"
    "v_pk_add_f32 v[230:231], v[144:145], v[146:147]\n"
    "v_pk_mul_f32 v[232:233], v[144:145], v[146:147]\n"
    "v_pk_fma_f32 v[148:149], v[148:149], s[70:71], v[100:101]\n"
    "v_pk_fma_f32 v[230:231], v[230:231], v[148:149], v[232:233]\n"
    "v_pk_mul_f32 v[232:233], v[232:233], v[148:149]\n"
    "v_pk_fma_f32 v[150:151], v[150:151], s[70:71], v[102:103]\n"
    "v_pk_fma_f32 v[230:231], v[230:231], v[150:151], v[232:233]\n"
    "v_pk_mul_f32 v[232:233], v[232:233], v[150:151]\n"
    "v_pk_fma_f32 v[152:153], v[152:153], s[70:71], v[104:105]\n"
    "v_pk_fma_f32 v[230:231], v[230:231], v[152:153], v[232:233]\n"
    "v_pk_mul_f32 v[232:233], v[232:233], v[152:153]\n"
    "v_pk_fma_f32 v[154:155], v[154:155], s[70:71], v[106:107]\n"
    "v_pk_fma_f32 v[230:231], v[230:231], v[154:155], v[232:233]\n"
    "v_pk_mul_f32 v[232:233], v[232:233], v[154:155]\n"
    "v_pk_fma_f32 v[156:157], v[156:157], s[70:71], v[108:109]\n"
    "v_pk_fma_f32 v[230:231], v[230:231], v[156:157], v[232:233]\n"
    "v_pk_mul_f32 v[232:233], v[232:233], v[156:157]\n"
    "v_pk_fma_f32 v[158:159], v[158:159], s[70:71], v[110:111]\n"
    "v_pk_fma_f32 v[230:231], v[230:231], v[158:159], v[232:233]\n"
    "v_pk_mul_f32 v[232:233], v[232:233], v[158:159]\n"
    "v_rcp_f32 v232, v232\n"
    "v_rcp_f32 v233, v233\n"
    "v_fma_f32 v230, v230, v232, v247\n"
    "v_fmac_f32 v230, v231, v233\n"
    "v_lshlrev_b32 v232, 2, v249\n"
    "v_lshrrev_b32 v233, 2, v249\n"
    "v_and_b32 v232, 56, v232\n"
    "v_and_b32 v233, 4, v233\n"
    "v_add3_u32 v232, v232, v233, s81\n"
    "ds_read_b32 v232, v232\n"
    "s_add_u32 s81, s81, 64\n"
    "s_waitcnt lgkmcnt(0)\n"
    "v_lshlrev_b32 v232, 1, v232\n"
    "buffer_load_ushort v232, v232, s[44:47], 0 offen\n"
    "s_waitcnt vmcnt(0)\n"
    "v_lshlrev_b32 v232, 16, v232\n"
    "v_mul_f32 v230, v230, v232\n"
    "v_cvt_pk_bf16_f32 v230, v230, v230\n"
    "ds_write_b16 v245, v230\n"
    "ds_read_b64 v[228:229], v246\n"
    "s_branch .Ldin_drain1\n"
    ".Ldin_exit:\n"

      : "+{s[0:1]}"(kp), "+{v0}"(tid)
      : "{s2}"(blockIdx.x)
      : "memory", "vcc", "scc", "v1", "v2", "v3", "v4", "v5", "v6", "v7", "v8", "v9", "v10", "v11", "v12", "v13", "v14", "v15", "v16", "v17", "v18", "v19", "v20", "v21", "v22", "v23", "v24", "v25", "v26", "v27", "v28", "v29", "v30", "v31", "v32", "v33", "v34", "v35", "v36", "v37", "v38", "v39", "v40", "v41", "v42", "v43", "v44", "v45", "v46", "v47", "v48", "v49", "v50", "v51", "v52", "v53", "v54", "v55", "v56", "v57", "v58", "v59", "v60", "v61", "v62", "v63", "v64", "v65", "v66", "v67", "v68", "v69", "v70", "v71", "v72", "v73", "v74", "v75", "v76", "v77", "v78", "v79", "v80", "v81", "v82", "v83", "v84", "v85", "v86", "v87", "v88", "v89", "v90", "v91", "v92", "v93", "v94", "v95", "v96", "v97", "v98", "v99", "v100", "v101", "v102", "v103", "v104", "v105", "v106", "v107", "v108", "v109", "v110", "v111", "v112", "v113", "v114", "v115", "v116", "v117", "v118", "v119", "v120", "v121", "v122", "v123", "v124", "v125", "v126", "v127", "v128", "v129", "v130", "v131", "v132", "v133", "v134", "v135", "v136", "v137", "v138", "v139", "v140", "v141", "v142", "v143", "v144", "v145", "v146", "v147", "v148", "v149", "v150", "v151", "v152", "v153", "v154", "v155", "v156", "v157", "v158", "v159", "v160", "v161", "v162", "v163", "v164", "v165", "v166", "v167", "v168", "v169", "v170", "v171", "v172", "v173", "v174", "v175", "v176", "v177", "v178", "v179", "v180", "v181", "v182", "v183", "v184", "v185", "v186", "v187", "v188", "v189", "v190", "v191", "v192", "v193", "v194", "v195", "v196", "v197", "v198", "v199", "v200", "v201", "v202", "v203", "v204", "v205", "v206", "v207", "v208", "v209", "v210", "v211", "v212", "v213", "v214", "v215", "v216", "v217", "v218", "v219", "v220", "v221", "v222", "v223", "v224", "v225", "v226", "v227", "v228", "v229", "v230", "v231", "v232", "v233", "v234", "v235", "v236", "v237", "v238", "v239", "v240", "v241", "v242", "v243", "v244", "v245", "v246", "v247", "v248", "v249", "v250", "v251", "v252", "v253", "v254", "v255", "s3", "s4", "s5", "s6", "s7", "s8", "s9", "s10", "s11", "s12", "s13", "s14", "s15", "s16", "s17", "s18", "s19", "s20", "s21", "s22", "s23", "s24", "s25", "s26", "s27", "s28", "s29", "s30", "s31", "s33", "s34", "s35", "s36", "s37", "s38", "s39", "s40", "s41", "s42", "s43", "s44", "s45", "s46", "s47", "s48", "s49", "s50", "s51", "s52", "s53", "s54", "s55", "s56", "s57", "s58", "s59", "s60", "s61", "s62", "s63", "s64", "s65", "s66", "s67", "s68", "s69", "s70", "s71", "s72", "s73", "s74", "s75", "s76", "s77", "s78", "s79", "s80", "s81", "s82", "s83", "s84", "s85", "s86", "s87", "s88", "s89", "s90", "s91", "s92", "s93", "s94", "s95", "s96");
  asm volatile("" ::"s"((uint32_t)(uintptr_t)smem));
}

__global__ __launch_bounds__(64) void din_fallback_kernel(
    const unsigned short* __restrict__ target, const unsigned short* __restrict__ hist,
    const unsigned short* __restrict__ mask, const unsigned short* __restrict__ w1,
    const unsigned short* __restrict__ b1, const unsigned short* __restrict__ w2,
    const unsigned short* __restrict__ b2, unsigned short* __restrict__ out, int B, int L) {
  __shared__ float s_w[64 * 64];
  __shared__ float s_x[64];
  const int b = blockIdx.x, h = threadIdx.x;
  float bias = bf2f(b1[h]);
  for (int d = 0; d < 64; ++d) {
    const float t = bf2f(target[(size_t)b * 64 + d]);
    const float wc = bf2f(w1[(128 + d) * 64 + h]);
    bias += t * (bf2f(w1[(64 + d) * 64 + h]) - wc);
    s_w[d * 64 + h] = bf2f(w1[d * 64 + h]) + wc + t * bf2f(w1[(192 + d) * 64 + h]);
  }
  const float w2h = bf2f(w2[h]), b2v = bf2f(b2[0]);
  float acc = 0.f;
  for (int l = 0; l < L; ++l) {
    const float m = bf2f(mask[(size_t)b * L + l]);
    if (m == 0.f) continue;
    const float x = bf2f(hist[((size_t)b * L + l) * 64 + h]);
    __syncthreads();
    s_x[h] = x;
    __syncthreads();
    float z = bias;
    for (int d = 0; d < 64; ++d) z += s_x[d] * s_w[d * 64 + h];
    float s = w2h / (1.0f + __expf(-z));
    for (int o = 32; o > 0; o >>= 1) s += __shfl_xor(s, o);
    acc += (s + b2v) * m * x;
  }
  const uint32_t u = __float_as_uint(acc);
  out[(size_t)b * 64 + h] = (unsigned short)((u + 0x7fffu + ((u >> 16) & 1u)) >> 16);
}

extern "C" void kernel_launch(void* const* d_in, const int* in_sizes, int n_in, void* d_out,
                              int out_size, void* d_ws, size_t ws_size, hipStream_t stream) {
  const unsigned short* target = (const unsigned short*)d_in[0];
  const unsigned short* hist = (const unsigned short*)d_in[1];
  const unsigned short* mask = (const unsigned short*)d_in[2];
  const unsigned short* w1 = (const unsigned short*)d_in[3];
  const unsigned short* b1 = (const unsigned short*)d_in[4];
  const unsigned short* w2 = (const unsigned short*)d_in[5];
  const unsigned short* b2 = (const unsigned short*)d_in[6];
  const int B = in_sizes[0] / 64;
  const int L = in_sizes[2] / B;
  if ((L & 1) || L > 256 || B >= (1 << 24) - 64) {
    din_fallback_kernel<<<B, 64, 0, stream>>>(target, hist, mask, w1, b1, w2, b2, (unsigned short*)d_out, B, L);
    return;
  }
  const uint32_t grid = (B + 63) / 64, fl = 0x20000u;
  const bool paced = grid >= 224;
  din_kernel<<<grid, 512, 0, stream>>>(hist, (uint32_t)B * L, fl, mask, (uint32_t)B * L * 2u, fl, target, (uint32_t)B * 128u, fl,
                                       (unsigned short*)d_out, (uint32_t)B * 128u, fl, w1, w2, b1, b2, (uint32_t)L, paced ? 53u | 8u << 16 : 0u, (uint32_t)B,
                                       grid * 8u, 2u * L, grid * 16u * L, 0u, 0u);
}
```
